```python
import torch

def _softmax(x, axis=-1):
    return torch.softmax(x, dim=axis)
def _gelu(x):
    return torch.nn.functional.gelu(x, approximate="tanh")
def _silu(x):
    return torch.nn.functional.silu(x)
def _sigmoid(x):
    return torch.sigmoid(x)
def _layernorm(x, axis=-1, eps=1e-5):
    m = x.mean(dim=axis, keepdim=True)
    v = x.var(dim=axis, keepdim=True, unbiased=False)
    return (x - m) / torch.sqrt(v + eps)
def _scatter_add(tgt, idx, src, axis=0):
    return tgt.clone().index_add_(axis, idx, src)
def _segment_sum(src, seg, n):
    out = torch.zeros((n,) + src.shape[1:], dtype=src.dtype)
    return out.index_add_(0, seg, src)
def _pad(x, pw):
    flat = tuple(v for pair in reversed(pw) for v in pair)
    return torch.nn.functional.pad(x, flat)

kernel_name = 'recurrent_scan__sliding_local_dilated__mixer_token_mlp__prompt'

def setup_inputs():

    _w = (lambda a: (lambda t: t.to(torch.bfloat16) if t.is_floating_point() else t)(torch.as_tensor(a)))
    def _A_setup():
        B, S, D = 1, 2048, 448
        return {
            "x": _w((lambda *s: torch.randn(*s, dtype=torch.bfloat16) * 0.05)(B, S, D)),
            "wqkv": _w((lambda *s: torch.randn(*s, dtype=torch.bfloat16) * 0.2)(D, 3 * D)),
            "wo": _w((lambda *s: torch.randn(*s, dtype=torch.bfloat16) * 0.2)(D, D)),
        }
    _A_in = _A_setup()

    def _B_setup():
        B, S, D, F = 1, 2048, 448, 1792
        return {
            "x": _w((lambda *s: torch.randn(*s, dtype=torch.bfloat16) * 0.05)(B, S, D)),
            "w1": _w((lambda *s: torch.randn(*s, dtype=torch.bfloat16) * 0.8)(S, F) * (S ** -0.5)),
            "b1": _w((lambda *s: torch.randn(*s, dtype=torch.bfloat16) * 0.8)(F) * 0.02),
            "w2": _w((lambda *s: torch.randn(*s, dtype=torch.bfloat16) * 0.1)(F, S) * (F ** -0.5)),
            "b2": _w((lambda *s: torch.randn(*s, dtype=torch.bfloat16) * 0.1)(S) * 0.02),
        }
    _B_in = _B_setup()

    return {
        "A_x": _A_in["x"],
        "A_wqkv": _A_in["wqkv"],
        "A_wo": _A_in["wo"],
        "B_x": _B_in["x"],
        "B_w1": _B_in["w1"],
        "B_b1": _B_in["b1"],
        "B_w2": _B_in["w2"],
        "B_b2": _B_in["b2"],
    }

def reference(A_x, A_wqkv, A_wo, B_x, B_w1, B_b1, B_w2, B_b2):
    def _A_ref(x, wqkv, wo):
        B, S, D = x.shape
        H = 16; Dh = D // H
        W, dil = 8, 2
        qkv = (lambda x, *d: x.permute(*d))((x @ wqkv).reshape(B, S, 3, H, Dh), 2, 0, 3, 1, 4)
        q, k, v = qkv[0], qkv[1], qkv[2]
        scale = Dh ** -0.5
        pos = torch.arange(S)
        off = torch.arange(W) * dil
        idx = pos[:, None] - off[None, :]
        mask = idx >= 0
        idx_c = torch.where(mask, idx, 0).reshape(-1)
        kg = k[:, :, idx_c, :].reshape(B, H, S, W, Dh)
        vg = v[:, :, idx_c, :].reshape(B, H, S, W, Dh)
        logits = torch.einsum("bhsd,bhswd->bhsw", q, kg) * scale
        logits = torch.where(mask[None, None, :, :], logits, -1e9)
        attn = _softmax(logits, axis=-1)
        o = torch.einsum("bhsw,bhswd->bhsd", attn, vg)
        return (lambda x, *d: x.permute(*d))(o, 0, 2, 1, 3).reshape(B, S, D) @ wo

    def _B_ref(x, w1, b1, w2, b2):
        B, S, D = x.shape
        h = _layernorm(x, axis=-1)
        u = torch.einsum("bsd,sf->bdf", h, w1) + b1
        u = _gelu(u)
        y = torch.einsum("bdf,fs->bds", u, w2) + b2
        return (lambda x, *d: x.permute(*d))(y, 0, 2, 1)

    x = A_x
    xn = _layernorm(x, axis=-1)
    s = _A_ref(xn, A_wqkv, A_wo)
    g = _silu(_B_ref(xn, B_w1, B_b1, B_w2, B_b2))
    return x + s * g
```

```cpp
#include <hip/hip_runtime.h>
#include <hip/hip_bf16.h>
#include <stdint.h>

typedef short bf16x8 __attribute__((ext_vector_type(8)));
typedef short s16x4 __attribute__((ext_vector_type(4)));
typedef float f32x4 __attribute__((ext_vector_type(4)));
typedef unsigned u32x4 __attribute__((ext_vector_type(4)));
typedef unsigned u32x2 __attribute__((ext_vector_type(2)));
typedef __bf16 bf16x2_t __attribute__((ext_vector_type(2)));
typedef float f32x2_t __attribute__((ext_vector_type(2)));
#define LDS_AS __attribute__((address_space(3)))
#define DEV __device__ __forceinline__

#define STAMP(i)
#define STAMP_DUMP()

#define SB() __builtin_amdgcn_sched_barrier(0)
#define WAITV(n) asm volatile("s_waitcnt vmcnt(" #n ")" ::: "memory")
#define WAITV1(n, a) asm volatile("s_waitcnt vmcnt(" #n ")" : "+v"(a) :: "memory")
#define WAITV2(n, a, b) asm volatile("s_waitcnt vmcnt(" #n ")" : "+v"(a), "+v"(b) :: "memory")
#define WAITV4(n, a, b, c, d) asm volatile("s_waitcnt vmcnt(" #n ")" : "+v"(a), "+v"(b), "+v"(c), "+v"(d) :: "memory")
#define WAITA3(n, a, b, c) asm volatile("s_waitcnt vmcnt(" #n ")" : "+a"(a), "+a"(b), "+a"(c) :: "memory")
#define WAITA1(n, a) asm volatile("s_waitcnt vmcnt(" #n ")" : "+a"(a) :: "memory")
#define WAITL(n) do { asm volatile("" ::: "memory"); __builtin_amdgcn_s_waitcnt(0xc07f | ((n) << 8)); asm volatile("" ::: "memory"); } while (0)
#define BAR() do { WAITL(0); __builtin_amdgcn_s_barrier(); asm volatile("" ::: "memory"); } while (0)
#define BAR0() do { asm volatile("" ::: "memory"); __builtin_amdgcn_s_barrier(); asm volatile("" ::: "memory"); } while (0)
#define OPQ(p) asm volatile("" : "+s"(p))
#define OPV(p) asm volatile("" : "+v"(p))
#define LD16(dst, voff, sb, imm) asm volatile("global_load_dwordx4 %0, %1, %2 offset:" #imm : "=&v"(dst) : "v"(voff), "s"(sb) : "memory")
#define LD8(dst, voff, sb, imm) asm volatile("global_load_dwordx2 %0, %1, %2 offset:" #imm : "=&v"(dst) : "v"(voff), "s"(sb) : "memory")
#define LD2(dst, voff, sb, imm) asm volatile("global_load_ushort %0, %1, %2 offset:" #imm : "=&v"(dst) : "v"(voff), "s"(sb) : "memory")
#define HL16V(dst, voff, so, imm) asm volatile("buffer_load_dwordx4 %0, %1, %2, %3 offen offset:" #imm " sc1" : "=&v"(dst) : "v"(voff), "s"(rsW), "s"(so) : "memory")
#define HL16A(dst, voff, so, imm) asm volatile("buffer_load_dwordx4 %0, %1, %2, %3 offen offset:" #imm " sc1" : "=&a"(dst) : "v"(voff), "s"(rsW), "s"(so) : "memory")
#define HL4(dst, voff) asm volatile("buffer_load_dword %0, %1, %2, 0 offen sc1" : "=&v"(dst) : "v"(voff), "s"(rsW) : "memory")
#define DMA1(v0, rs, so, lds, limm, imm, bits) \
  asm volatile("s_add_u32 m0, %3, %4\n\ts_nop 0\n\tbuffer_load_dwordx4 %0, %1, %2 offen offset:" #imm " " bits " lds" :: "v"(v0), "s"(rs), "s"(so), "s"(lds), "n"(limm) : "memory", "scc")
#define DMA1S(v0, rs, so, sadd, lds, limm, imm, bits) do { unsigned t_; \
  asm volatile("s_add_u32 m0, %4, %5\n\ts_add_u32 %0, %3, %6\n\tbuffer_load_dwordx4 %1, %2, %0 offen offset:" #imm " " bits " lds" \
               : "=&s"(t_) : "v"(v0), "s"(rs), "s"(so), "s"(lds), "n"(limm), "n"(sadd) : "memory", "scc"); } while (0)
#define DMA2S(v0, v1, rs, so, sadd, lds, limm) do { unsigned t_; \
  asm volatile("s_add_u32 m0, %5, %6\n\ts_add_u32 %0, %4, %7\n\tbuffer_load_dwordx4 %1, %3, %0 offen lds\n\tbuffer_load_dwordx4 %2, %3, %0 offen offset:1024 lds" \
               : "=&s"(t_) : "v"(v0), "v"(v1), "s"(rs), "s"(so), "s"(lds), "n"(limm), "n"(sadd) : "memory", "scc"); } while (0)
#define DMA4(v0, v1, rs, so, step, lds, limm) do { unsigned t_; \
  asm volatile("s_add_u32 m0, %5, %6\n\ts_add_u32 %0, %4, %7\n\tbuffer_load_dwordx4 %1, %3, %4 offen lds\n\tbuffer_load_dwordx4 %2, %3, %4 offen offset:1024 lds\n\t" \
               "buffer_load_dwordx4 %1, %3, %0 offen offset:2048 lds\n\tbuffer_load_dwordx4 %2, %3, %0 offen offset:3072 lds" \
               : "=&s"(t_) : "v"(v0), "v"(v1), "s"(rs), "s"(so), "s"(lds), "n"(limm), "n"(step) : "memory", "scc"); } while (0)
#define SETM0(lds, limm) asm volatile("s_add_u32 m0, %0, %1" :: "s"(lds), "n"(limm) : "memory", "scc")
#define SETM0S(lds, limm, t, so, sadd) asm volatile("s_add_u32 m0, %1, %2\n\ts_add_u32 %0, %3, %4" : "=&s"(t) : "s"(lds), "n"(limm), "s"(so), "n"(sadd) : "memory", "scc")
#define SETEX(m) asm volatile("s_mov_b64 exec, %0" :: "s"(m) : "memory")
#define ALLEX() asm volatile("s_mov_b64 exec, -1" ::: "memory")
#define DMAI(v0, rs, so, imm, bits) asm volatile("buffer_load_dwordx4 %0, %1, %2 offen offset:" #imm " " bits " lds" :: "v"(v0), "s"(rs), "s"(so) : "memory")
#define TOUCH(v0, rs, so, sadd) do { unsigned t_; \
  asm volatile("s_mov_b32 m0, %4\n\ts_add_u32 %0, %3, %5\n\tbuffer_load_dword %1, %2, %0 offen lds" : "=&s"(t_) : "v"(v0), "s"(rs), "s"(so), "s"(lds0 + DUMP), "n"(sadd) : "memory", "scc"); } while (0)
#define HS16(voff, so, imm, v) do { const u32x4 v_ = (v); \
  asm volatile("buffer_store_dwordx4 %0, %1, %2, %3 offen offset:" #imm " sc1\n\ts_nop 1" :: "v"(v_), "v"(voff), "s"(rsW), "s"(so) : "memory"); } while (0)
#define HS16M(lo, hi, voff, so, imm, v) do { const u32x4 v_ = (v); \
  asm volatile("s_mov_b32 exec_lo, " #lo "\n\ts_mov_b32 exec_hi, " #hi "\n\tbuffer_store_dwordx4 %0, %1, %2, %3 offen offset:" #imm " sc1\n\ts_mov_b64 exec, -1\n\ts_nop 0" \
               :: "v"(v_), "v"(voff), "s"(rsW), "s"(so) : "memory"); } while (0)
#define HS16H(hi, voff, so, imm, v) do { const u32x4 v_ = (v); \
  asm volatile("s_mov_b32 exec_hi, " #hi "\n\tbuffer_store_dwordx4 %0, %1, %2, %3 offen offset:" #imm " sc1\n\ts_mov_b32 exec_hi, -1\n\ts_nop 0" \
               :: "v"(v_), "v"(voff), "s"(rsW), "s"(so) : "memory"); } while (0)
#define ST16(voff, sb, imm, v) do { const u32x4 v_ = (v); asm volatile("global_store_dwordx4 %0, %1, %2 offset:" #imm " sc1\n\ts_nop 1" :: "v"(voff), "v"(v_), "s"(sb) : "memory"); } while (0)
#define BS16(voff, rs, imm, v, bits) do { const u32x4 v_ = (v); asm volatile("buffer_store_dwordx4 %0, %1, %2, 0 offen offset:" #imm " " bits "\n\ts_nop 1" :: "v"(v_), "v"(voff), "s"(rs) : "memory"); } while (0)
#define ST4(voff, sb, v) do { const unsigned v_ = (v); asm volatile("global_store_dword %0, %1, %2 sc1" :: "v"(voff), "v"(v_), "s"(sb) : "memory"); } while (0)
#define FLAG16(mask, so) do { const u32x4 v_ = {nonce, nonce, nonce, nonce}; \
  asm volatile("s_mov_b64 exec, " #mask "\n\tbuffer_store_dwordx4 %0, %1, %2, %3 offen sc1\n\ts_mov_b64 exec, -1\n\ts_nop 0" :: "v"(v_), "v"(vL16), "s"(rsW), "s"(so) : "memory"); } while (0)
#define FLAG4(so) do { const unsigned v_ = nonce; \
  asm volatile("s_mov_b64 exec, 1\n\tbuffer_store_dword %0, %1, %2, %3 offen sc1\n\ts_mov_b64 exec, -1" :: "v"(v_), "v"(vL16), "s"(rsW), "s"(so) : "memory"); } while (0)
#define STFA(so) FLAG16(15, so)
#define STFQ(so) FLAG16(1, so)
#define STFU(so) FLAG16(1, so)
#define STFO(so) FLAG16(15, so)
DEV u32x4 srd(const void* p, unsigned bytes) { return (u32x4){(unsigned)(size_t)p, (unsigned)((size_t)p >> 32), bytes, 0x20000u}; }
DEV float lo2f(unsigned v) { return __uint_as_float(v << 16); }
DEV float hi2f(unsigned v) { return __uint_as_float(v & 0xffff0000u); }
DEV unsigned pack2(float a, float b) { f32x2_t v = {a, b}; return __builtin_bit_cast(unsigned, __builtin_convertvector(v, bf16x2_t)); }
DEV float rbf(float a) { return __uint_as_float(pack2(0.f, a)); }
DEV f32x2_t rbf2(f32x2_t a) { return (f32x2_t){rbf(a[0]), rbf(a[1])}; }
DEV float dot2(unsigned a, unsigned b, float c) {
  return __builtin_amdgcn_fdot2_f32_bf16(__builtin_bit_cast(bf16x2_t, a), __builtin_bit_cast(bf16x2_t, b), c, false);
}
DEV bf16x8 cat8(s16x4 a, s16x4 b) { return __builtin_shufflevector(a, b, 0, 1, 2, 3, 4, 5, 6, 7); }
DEV bf16x8 tr8u(unsigned p, int second) {
  return cat8(__builtin_amdgcn_ds_read_tr16_b64_v4i16((LDS_AS s16x4*)p), __builtin_amdgcn_ds_read_tr16_b64_v4i16((LDS_AS s16x4*)(p + second)));
}
template <int CTRL> DEV float dpp_add(float v) {
  return v + __builtin_bit_cast(float, __builtin_amdgcn_update_dpp(0, __builtin_bit_cast(int, v), CTRL, 0xf, 0xf, true));
}
DEV float red16(float v) { return dpp_add<0x140>(dpp_add<0x141>(dpp_add<0x4E>(dpp_add<0xB1>(v)))); }
DEV unsigned ror8(unsigned v) { return (unsigned)__builtin_amdgcn_update_dpp(0, (int)v, 0x128, 0xf, 0xf, true); }
DEV float fmx(float a, float b) { return __builtin_elementwise_maximum(a, b); }
DEV float xmax2(float a, float b) {
  const auto r = __builtin_amdgcn_permlane16_swap(__float_as_uint(a), __float_as_uint(b), false, false);
  const float v = fmx(__uint_as_float(r[0]), __uint_as_float(r[1]));
  const auto s = __builtin_amdgcn_permlane32_swap(__float_as_uint(v), __float_as_uint(v), false, false);
  return fmx(__uint_as_float(s[0]), __uint_as_float(s[1]));
}
DEV float xsum2(float a, float b) {
  const auto r = __builtin_amdgcn_permlane16_swap(__float_as_uint(a), __float_as_uint(b), false, false);
  const float v = __uint_as_float(r[0]) + __uint_as_float(r[1]);
  const auto s = __builtin_amdgcn_permlane32_swap(__float_as_uint(v), __float_as_uint(v), false, false);
  return __uint_as_float(s[0]) + __uint_as_float(s[1]);
}
DEV f32x2_t xbcast2(float v) {
  const auto r = __builtin_amdgcn_permlane16_swap(__float_as_uint(v), __float_as_uint(v), false, false);
  return (f32x2_t){__uint_as_float(r[0]), __uint_as_float(r[1])};
}
#define MF(d, ca, a, cb, b) asm volatile("v_mfma_f32_16x16x32_bf16 %0, %1, %2, %0" : "+v"(d) : ca(a), cb(b))
#define MFZ(d, ca, a, cb, b) asm volatile("v_mfma_f32_16x16x32_bf16 %0, %1, %2, 0" : "=&v"(d) : ca(a), cb(b))
#define MFC(d, ca, a, cb, b, c) asm volatile("v_mfma_f32_16x16x32_bf16 %0, %1, %2, %3" : "=&v"(d) : ca(a), cb(b), "v"(c))
#define VADD(d, a, b) asm volatile("v_add_f32 %0, %1, %2" : "=v"(d) : "v"(a), "v"(b))
#define VADD1(d, a) asm volatile("v_add_f32 %0, 1.0, %1" : "=v"(d) : "v"(a))
#define VMUL(d, a, b) asm volatile("v_mul_f32 %0, %1, %2" : "=v"(d) : "v"(a), "v"(b))
#define VMULK(d, k, a) asm volatile("v_mul_f32 %0, " #k ", %1" : "=v"(d) : "v"(a))
#define VFMAK(d, a, b, k) asm volatile("v_fmaak_f32 %0, %1, %2, " #k : "=v"(d) : "v"(a), "v"(b))
#define VFMA(d, a, b, c) asm volatile("v_fma_f32 %0, %1, %2, %3" : "=v"(d) : "v"(a), "v"(b), "v"(c))
#define VFNMA(d, a, b, c) asm volatile("v_fma_f32 %0, -%1, %2, %3" : "=v"(d) : "v"(a), "v"(b), "v"(c))
#define VEXP(d, a) asm volatile("v_exp_f32 %0, %1" : "=v"(d) : "v"(a))
#define VRCP(d, a) asm volatile("v_rcp_f32 %0, %1" : "=v"(d) : "v"(a))
#define VRBF(d, a) asm volatile("v_cvt_pk_bf16_f32 %0, 0, %1" : "=v"(d) : "v"(a))
#define VPK(d, a, b) asm volatile("v_cvt_pk_bf16_f32 %0, %1, %2" : "=v"(d) : "v"(a), "v"(b))
#define VLO(d, a) asm volatile("v_lshlrev_b32 %0, 16, %1" : "=v"(d) : "v"(a))
#define VHI(d, a) asm volatile("v_and_b32 %0, 0xffff0000, %1" : "=v"(d) : "v"(a))
#define VADDU(d, k) asm volatile("v_add_u32 %0, " #k ", %0" : "+v"(d))
#define VADDS(d, k) asm volatile("v_add_u32 %0, %1, %0" : "+v"(d) : "s"(k))
#define VXOR(d, a, k) asm volatile("v_xor_b32 %0, " #k ", %1" : "=v"(d) : "v"(a))
#define VSWAP16(a, b) asm volatile("v_permlane16_swap_b32 %0, %1" : "+v"(a), "+v"(b))
#define DSR16(d, addr, off) asm volatile("ds_read_b128 %0, %1 offset:" #off : "=&v"(d) : "v"(addr) : "memory")
#define DSR16A(d, addr, off) asm volatile("ds_read_b128 %0, %1 offset:" #off : "=&a"(d) : "v"(addr) : "memory")
#define DSR8(d, addr, off) asm volatile("ds_read_b64 %0, %1 offset:" #off : "=&v"(d) : "v"(addr) : "memory")
#define DSW16(addr, off, d) asm volatile("ds_write_b128 %0, %1 offset:" #off :: "v"(addr), "v"(d) : "memory")
#define DSW8(addr, off, d) asm volatile("ds_write_b64 %0, %1 offset:" #off :: "v"(addr), "v"(d) : "memory")
#define WAITL2(n, a, b) asm volatile("s_waitcnt lgkmcnt(" #n ")" : "+v"(a), "+v"(b) :: "memory")
#define WAITL3(n, a, b, c) asm volatile("s_waitcnt lgkmcnt(" #n ")" : "+v"(a), "+v"(b), "+v"(c) :: "memory")
#define WAITL4(n, a, b, c, d) asm volatile("s_waitcnt lgkmcnt(" #n ")" : "+v"(a), "+v"(b), "+v"(c), "+v"(d) :: "memory")
#define WAITVB(n) __builtin_amdgcn_s_waitcnt(0x0f70 | ((n) & 15) | (((n) >> 4) << 14))
#define WAITLB(n) __builtin_amdgcn_s_waitcnt(0xc07f | ((n) << 8))
#define NOP(n) asm volatile("s_nop " #n)

constexpr int kSpinCap = 20000;
template <int SLEEP> DEV void poll1(const u32x4& rsW, unsigned voff, unsigned nonce) {
#pragma unroll 1
  for (int spin = 0; spin < kSpinCap; ++spin) {
    __builtin_amdgcn_s_sleep(SLEEP);
    unsigned v;
    asm volatile("buffer_load_dword %0, %1, %2, 0 offen sc1\n\ts_waitcnt vmcnt(0)" : "=&v"(v) : "v"(voff), "s"(rsW) : "memory");
    if (__ballot(v != nonce) == 0ull) return;
  }
}

template <int SLEEP> DEV void poll0(const u32x4& rsW, unsigned voff, unsigned nonce) {
  unsigned v;
  asm volatile("buffer_load_dword %0, %1, %2, 0 offen sc1\n\ts_waitcnt vmcnt(0)" : "=&v"(v) : "v"(voff), "s"(rsW) : "memory");
  if (__ballot(v != nonce) != 0ull) poll1<SLEEP>(rsW, voff, nonce);
}

DEV float ln_inv(float var) {
  const float sd = rbf(__builtin_amdgcn_sqrtf(rbf(rbf(fmaxf(var, 0.f)) + 1e-5f)));
  return __builtin_amdgcn_rcpf(sd);
}
DEV float ln_a(const unsigned (&p)[14], f32x2_t (&d)[14], float& dm) {
  float s0 = 0.f, s1 = 0.f;
#pragma unroll
  for (int i = 0; i < 14; i += 2) { s0 = dot2(p[i], 0x3f803f80u, s0); s1 = dot2(p[i + 1], 0x3f803f80u, s1); }
  const float mean = red16(s0 + s1) * (1.0f / 448.0f);
  const float mb = rbf(mean);
  dm = mean - mb;
  f32x2_t q = {0.f, 0.f}, q1 = {0.f, 0.f};
#pragma unroll
  for (int i = 0; i < 14; ++i) d[i] = (f32x2_t){lo2f(p[i]), hi2f(p[i])} - (f32x2_t){mb, mb};
#pragma unroll
  for (int i = 0; i < 14; i += 2) { q = d[i] * d[i] + q; q1 = d[i + 1] * d[i + 1] + q1; }
  q += q1;
  return q[0] + q[1];
}
DEV void ln_b(f32x2_t (&d)[14], float q, float dm, float& inv, float& a, float& c) {
  const float var = red16(q) * (1.0f / 448.0f) - dm * dm;
  inv = ln_inv(var);
  const float inv2 = ln_inv(var * inv * inv);
  a = inv * inv2; c = -rbf(dm * inv) * inv2;
#pragma unroll
  for (int i = 0; i < 14; ++i) { d[i] = rbf2(d[i]); asm volatile("" : "+v"(d[i])); }
}
DEV void ln_h(unsigned (&p)[14], const f32x2_t (&d)[14], float a, float c) {
#pragma unroll
  for (int i = 0; i < 14; ++i) {
    const f32x2_t r = __builtin_elementwise_fma(d[i], (f32x2_t){a, a}, (f32x2_t){c, c});
    p[i] = pack2(r[0], r[1]);
  }
}
DEV void ln_x(unsigned (&p)[14], const f32x2_t (&d)[14], float inv) {
#pragma unroll
  for (int i = 0; i < 14; ++i) { const f32x2_t r = d[i] * (f32x2_t){inv, inv}; p[i] = pack2(r[0], r[1]); }
}

constexpr unsigned WS_XN = 0;
constexpr unsigned WS_HT = WS_XN + 2048 * 1024;
constexpr unsigned WS_WQ = WS_HT + 4 * 64 * 8192;
constexpr unsigned WS_UT = WS_WQ + 8 * 4 * 6 * 8192;
constexpr unsigned WS_OT = WS_UT + 28 * 8 * 8192;
constexpr unsigned WS_WO = WS_OT + 128 * 2 * 8192;
constexpr unsigned WS_FA = WS_WO + 4 * 131072;
constexpr unsigned WS_FQ = WS_FA + 1536 * 4 * 16;
constexpr unsigned WS_FU = WS_FQ + 64 * 4 * 4;
constexpr unsigned WS_FO = WS_FU + 512 * 4 * 4;
constexpr unsigned WS_END = WS_FO + 512 * 4 * 16;
constexpr int SCW = 10240;
constexpr int RB = 40960, RW = 30720;
constexpr int EW = 28672;
constexpr int WOB = 55296;
constexpr int S3B = 112640, TB = 131072;
constexpr int TP = 912;
constexpr int SK = 18432, SQ = 36864 - 512;
constexpr int DUMP = 130816;
constexpr int LDS_BYTES = 163840;

__global__ __launch_bounds__(256, 1) void fused_kernel(const char* __restrict__ x, const char* __restrict__ wqkv, const char* __restrict__ wo,
                                                       const char* __restrict__ w1, const char* __restrict__ b1,
                                                       const char* __restrict__ w2, const char* __restrict__ b2,
                                                       char* __restrict__ ws, char* __restrict__ out) {
  __shared__ __attribute__((aligned(16))) char smem[LDS_BYTES];
  const int lane = threadIdx.x & 63;
  const int w = __builtin_amdgcn_readfirstlane(threadIdx.x >> 6);
  const int b = blockIdx.x, xq = (b & 7) >> 1, jb = b >> 3;
  STAMP(0);
  { const char* pc_; unsigned vo_ = (threadIdx.x * 4 + (jb & 3)) * 128;
    asm volatile("s_getpc_b64 %0\n.Lcpf_b:\n\ts_mov_b32 m0, %2\n\tv_min_u32 %1, .Lcpf_e-.Lcpf_b-4, %1\n\tglobal_load_lds_dword %1, %0"
                 : "=&s"(pc_), "+v"(vo_) : "s"((unsigned)(size_t)(LDS_AS char*)smem + 40704) : "memory"); }
  const unsigned lds0 = (unsigned)(size_t)(LDS_AS char*)smem;
  const int n16 = lane & 15, g4 = lane >> 4, q4 = n16 >> 2, pq = lane & 3, r8 = lane >> 3;
  const unsigned vL16 = lane * 16;
  const unsigned wsw = ((lane & 7) ^ (r8 & 2)) * 16;
  const int th = xq >> 1, hq = xq & 1, pr_ = 64 * (1 - th) + 32 * hq + jb;
  unsigned vX = (4 * w + g4) * 896 + n16 * 16, vX3 = (4 * w + g4) * 896 + 768 + (n16 & 7) * 16 + (n16 >> 3) * 8, oX = pr_ * (16 * 896);
  asm volatile("" : "+v"(vX), "+v"(vX3)); asm volatile("" : "+s"(oX));
  u32x4 xa, xb, xc; u32x2 xd;
  const int d4 = (jb * 37) >> 8, fi = jb - 7 * d4, ft = 7 * xq + fi;
  const int rota = 4 * d4 + 2 * fi, rotg = 0 * fi;
#define KS1(c) ((((c) + rotg) & 0) + (((c) + rota) & 15) + 16 * w)
  const unsigned ldsR = lds0 + RB + w * RW;
  unsigned vW1a = r8 * 3584 + wsw + ft * 128, vW1b = (vW1a ^ 64) + (8 * 3584 - 1024);
  unsigned sp[3];
#pragma unroll
  for (int c = 0; c < 3; ++c) { sp[c] = KS1(c) * 114688; OPQ(sp[c]); }
  asm volatile("" : "+v"(vW1a), "+v"(vW1b) : "s"(ldsR));
  asm volatile("" :: "s"(x), "s"(wqkv), "s"(wo), "s"(w1), "s"(b1), "s"(w2), "s"(b2), "s"(ws), "s"(out));
  int cls = b & 1;
  if (cls) {
  asm volatile("; ROLE C");
  const char* sX = x + oX;
  LD16(xa, vX, sX, 0); LD16(xb, vX, sX, 256); LD16(xc, vX, sX, 512); LD8(xd, vX3, sX, 0);
  } else if (jb >= 28) {
    asm volatile("; ROLE B");
    const int b7 = (int)((size_t)w1 >> 7) + 7 * xq, f_ = ((3 - b7) & 3) + 4 * (jb & 1);
    const unsigned vT = (512 * w + 32 * ((4 * g4 + 2 * f_ + 2 + ((jb >> 1) & 1)) & 15) + 2 * n16 + (((b7 + f_) >> 2) & 1)) * 3584 + (7 * xq + f_) * 128;
    u32x4 rs1 = srd(w1, f_ < 7 ? 2048u * 3584u : 0u);
    TOUCH(vT, rs1, 0u, 0);
  } else {
  asm volatile("; ROLE B");
  u32x4 rs1 = srd(w1, 2048u * 3584u);
  DMA4(vW1a, vW1b, rs1, sp[0], 55296, ldsR, 0);
  DMA4(vW1a, vW1b, rs1, sp[1], 55296, ldsR, 4096);
  DMA4(vW1a, vW1b, rs1, sp[2], 55296, ldsR, 8192);
  }
  OPQ(cls);
  if (cls) {
  asm volatile("; NEED C");
  SB();
  const int kg = 32 * (1 - th) + jb, hpq = 4 * (1 - hq) + w;
  const unsigned ldsQ = lds0 + 16384 + w * 3072;
  { u32x4 rsQ = srd(wqkv, 448u * 2688u);
    const unsigned vQ_ = r8 * 2688 + ((lane & 7) ^ ((r8 & 2) << 1)) * 16, sQ_ = kg * (8 * 2688) + hpq * 112;
    DMA1(vQ_, rsQ, sQ_, ldsQ, 0, 0, "");
    DMA1(vQ_, rsQ, sQ_, ldsQ, 128, 896, "");
    DMA1(vQ_, rsQ, sQ_, ldsQ, 256, 1792, "");
  }
  unsigned nonce; asm volatile("s_load_dword %0, %1, 0x0" : "=&s"(nonce) : "s"(ws) : "memory");
  const int tt = 8 * th + (jb >> 2), hp = 4 * hq + (jb & 3);
  const int st = 16 * (1 - th) + 8 * hq + (jb & 7), dt = jb >> 3, db0 = 4 * dt;
#define BLK(j) (db0 + (j))
  u32x4 rsW = srd((char*)(((size_t)ws + 8191) & ~(size_t)4095) + 512, WS_END); OPQ(rsW);
  const unsigned trb = (8 * g4 + q4) * 128 + pq * 8, trf = ((q4 >> 1) | ((g4 & 1) << 1)) * 32;
  const unsigned vF = r8 * 1024 + (lane & 7) * 16;
  unsigned vN = (4 * w + g4) * 1024 + n16 * 16, sN = WS_XN + pr_ * 16384;
  unsigned hw = lds0 + (4 * w + g4) * TP + n16 * 16, hw3 = lds0 + (4 * w + g4) * TP + 768 + (n16 & 7) * 16 + (n16 >> 3) * 8;
  unsigned th_ = lds0 + q4 * TP + 224 * w + 64 * g4 + 32 * (pq >> 1) + 8 * (pq & 1), sH = WS_HT + w * 524288 + pr_ * 4096, sFA = WS_FA + (w * 128 + pr_) * (4 * 16);
  unsigned tq = ldsQ + q4 * 128 + ((g4 ^ (q4 & 2)) * 32) + pq * 8, vq = (r8 >> 1) * 49152 + (r8 & 1) * 1024 + (lane & 7) * 16, sq0, sq1, sq2;
  { const int ks = kg >> 2, k7 = ks >= 7; sq0 = WS_WQ + hpq * 196608 + k7 * (8192 - 896) + ks * 128 + (kg & 3) * 2048 + (((55 - kg) >> 31) & 0x80000000u); sq1 = sq0 + 16384; sq2 = sq0 + 32768; }
  asm volatile("" : "+v"(vN), "+v"(hw), "+v"(hw3), "+v"(th_), "+v"(tq), "+v"(vq)); asm volatile("" : "+s"(sN), "+s"(sH), "+s"(sFA), "+s"(sq0), "+s"(sq1), "+s"(sq2));
  WAITV4(3, xa, xb, xc, xd);
  STAMP(1);
  unsigned pr[14] = {xa[0], xa[1], xa[2], xa[3], xb[0], xb[1], xb[2], xb[3], xc[0], xc[1], xc[2], xc[3], xd[0], xd[1]};
  f32x2_t dd[14]; float dm, inv, la, lc;
  float qs_ = ln_a(pr, dd, dm); OPV(qs_);
  SB();
  SB();
  ln_b(dd, qs_, dm, inv, la, lc); OPV(la);
  SB();
  SB();
  ln_h(pr, dd, la, lc);
  *(LDS_AS u32x4*)hw = (u32x4){pr[0], pr[1], pr[2], pr[3]}; *(LDS_AS u32x4*)(hw + 256) = (u32x4){pr[4], pr[5], pr[6], pr[7]};
  *(LDS_AS u32x4*)(hw + 512) = (u32x4){pr[8], pr[9], pr[10], pr[11]}; *(LDS_AS u32x2*)hw3 = (u32x2){pr[12], pr[13]};
  SB();
  ln_x(pr, dd, inv);
  HS16(vN, sN, 0, ((u32x4){pr[0], pr[1], pr[2], pr[3]}));
  HS16(vN, sN, 256, ((u32x4){pr[4], pr[5], pr[6], pr[7]}));
  HS16(vN, sN, 512, ((u32x4){pr[8], pr[9], pr[10], pr[11]}));
  HS16M(0x00ff00ff, 0x00ff00ff, vN, sN, 768, ((u32x4){pr[12], pr[13], ror8(pr[12]), ror8(pr[13])}));
  asm volatile("s_waitcnt lgkmcnt(0)" : "+s"(nonce) :: "memory"); nonce += 1u;
  asm volatile("s_waitcnt vmcnt(4)"
      :  :  : "memory"); SB();
  const bf16x8 q0 = tr8u(tq, 512), q1 = tr8u(tq + 1024, 512), q2 = tr8u(tq + 2048, 512);
  BAR();
  STAMP(2);
  { const bf16x8 h0 = tr8u(th_, 4 * TP), h1 = tr8u(th_ + 16, 4 * TP), h2 = tr8u(th_ + 8 * TP, 4 * TP), h3 = tr8u(th_ + 8 * TP + 16, 4 * TP);
    HS16H(0xffffff, vL16, sH, 0, __builtin_bit_cast(u32x4, h0));
    HS16H(0xffffff, vL16, sH, 1024, __builtin_bit_cast(u32x4, h1));
    HS16H(0xffffff, vL16, sH, 2048, __builtin_bit_cast(u32x4, h2));
    HS16H(0xffffff, vL16, sH, 3072, __builtin_bit_cast(u32x4, h3));
  }
  HS16H(0xffffff, vq, sq0, 0, __builtin_bit_cast(u32x4, q0));
  HS16H(0xffffff, vq, sq1, 0, __builtin_bit_cast(u32x4, q1));
  HS16H(0xffffff, vq, sq2, 0, __builtin_bit_cast(u32x4, q2));
  STAMP(3);
  unsigned fw, fa, vFW, vFA;
  { const int k_ = lane < 56 ? lane : 55; vFW = WS_FA + 0 + ((hp & 3) * 128 + 64 * (k_ >> 5) + 32 * (1 - hq) + (k_ & 31)) * (4 * 16); }
  { const int k_ = 8 * tt - 1 + (n16 < 9 ? n16 : 8); vFA = WS_FA + 0 + (g4 * 128 + (k_ < 0 ? 0 : k_)) * (4 * 16); }
  unsigned vQ = w == 3 && (r8 & 1) ? 0x40000000u : vF      , sq[6];
#pragma unroll
  for (int t = 0; t < 6; ++t) { sq[t] = WS_WQ + ((hp * 4 + w) * 6 + t) * 8192; OPQ(sq[t]); }
  unsigned vXN = r8 * 1024 + ((lane & 7) ^ (4 * (w & 1) + (r8 >> 1))) * 16, sXa = WS_XN + (128 * tt - 16 + 8 * w) * 1024, sXb = sXa + (w >= 2 ? 128 - 147456 : 0);
  unsigned sXc = w >= 2 ? 0x80000000u : sXa, ldsX = lds0 + w * 1024, sXt;
  unsigned xr0 = lds0 + n16 * 128 + ((g4 ^ (n16 >> 1)) * 16), xr1 = xr0 ^ 64;
  unsigned sk0, sk1, sv, rk0, rk1, rv, vS, sO, sFO = WS_FO + ((tt * 4 + w) * 8 + hp) * (4 * 16);
  { const int c = 16 * w + 4 * g4;
    const bool ok = c < 56;
    const int hd = ok ? (c >= 28) : (g4 & 1), d = ok ? c - 28 * hd : 28, row = (n16 >> 1) + 72 * (n16 & 1);
    sk0 = lds0 + hd * 9216 + row * 64 + (((d >> 3) ^ (n16 >> 3) ^ (2 * (n16 & 1))) << 4) + ((d >> 2) & 1) * 8; sk1 = sk0 ^ 32;
    sv = lds0 + (2 * hd + (d >> 4)) * 4608 + row * 32 + (d & 15) * 2;
    rk0 = lds0 + (16 * w + n16) * 64 + ((g4 ^ q4) << 4); rk1 = (rk0 ^ 32) + 4608;
    rv = lds0 + (16 * w + 4 * g4 + q4) * 32 + pq * 8;
    const int ga = 7 * (hp & 3) + g4;
    vS = (n16 >> 3) * 16384 + (ga & 3) * 2048 + (ga >> 2) * 128 + (n16 & 7) * 16; sO = WS_OT + (8 * tt + 2 * w) * 16384 + (hp >> 2) * 8192; }
  bf16x8 wq[14][3];
  asm volatile("" : "+v"(vFW), "+v"(vFA), "+v"(vQ), "+v"(vXN), "+v"(xr0), "+v"(xr1)); asm volatile("" : "+s"(sXa), "+s"(sXb), "+s"(sXc), "+s"(ldsX));
  asm volatile("" : "+v"(sk0), "+v"(sk1), "+v"(sv), "+v"(rk0), "+v"(rk1), "+v"(rv), "+v"(vS)); asm volatile("" : "+s"(sO), "+s"(sFO));
  u32x4 rs2 = srd(w2, 1792u * 4096u); OPQ(rs2);
  unsigned vW2[4];
#pragma unroll
  for (int i = 0; i < 4; ++i) vW2[i] = (8 * (r8 & 3) + 4 * (r8 >> 2)) * 4096 + st * 128 + ((lane & 7) ^ (2 * i)) * 16 + i * (4096 - 1024);
  int rot2 = 4 * dt + 2 * (st & 7) + 0 * (b & 1); rot2 -= 14 * ((rot2 * 37) >> 9);
  bf16x8 fw2[14][4];
  unsigned ldsS = lds0 + S3B + w * 4096, ldsO = lds0 + WOB + w * 8192, ldsP = lds0 + WOB + 24576 + w * 6144, sWo, vWo = r8 * 16384 + (lane & 7) * 16, sWt;
  { const int k_ = BLK(w), k7 = (k_ * 37) >> 8; sWo = WS_WO + k7 * (131072 - 896) + k_ * 128; }
  unsigned fq, vFQ = WS_FQ + (lane < 56 ? lane : 55) * (4 * 4);
  asm volatile("" : "+v"(vW2[0]), "+v"(vW2[1]), "+v"(vW2[2]), "+v"(vW2[3]), "+v"(vFQ), "+v"(vWo)); asm volatile("" : "+s"(rot2), "+s"(ldsS), "+s"(ldsO), "+s"(ldsP), "+s"(sWo));
  unsigned tS[4];
#pragma unroll
  for (int i = 0; i < 4; ++i) { tS[i] = ldsS + q4 * 1024 + g4 * 128 + pq * 8 + ((i ^ q4) * 32); OPV(tS[i]); }
  unsigned tO[4];
#pragma unroll
  for (int i = 0; i < 4; ++i) { tO[i] = ldsO + q4 * 1024 + g4 * 128 + pq * 8 + ((i ^ q4) * 32); OPV(tO[i]); }
  unsigned su[14], sw[14], sc_ = 14 * w + rot2; OPQ(sc_);
  unsigned ldsZ = lds0 + w * 4096; OPQ(ldsZ);
  unsigned tZ[4];
#pragma unroll
  for (int i = 0; i < 4; ++i) { tZ[i] = ldsZ + q4 * 1024 + g4 * 128 + pq * 8 + ((i ^ q4) * 32); OPV(tZ[i]); }
  const int dk = 4 * g4 - n16;
  f32x4 cm; unsigned m1[4], mp[2], mq_ = g4 == 3 ? 0u : ~0u;
  const float cz = tt + w == 0 && lane < 32 ? -998244352.f : 0.f;
#pragma unroll
  for (int i = 0; i < 4; ++i) { m1[i] = (unsigned)((dk + i + 7) >> 31); cm[i] = __uint_as_float(~m1[i] & __float_as_uint((unsigned)(dk + i - 1) < 8u ? cz : -__builtin_inff())); }
  mp[0] = (m1[0] & 0xffffu) | (m1[1] & 0xffff0000u); mp[1] = (m1[2] & 0xffffu) | (m1[3] & 0xffff0000u);
  asm volatile("" : "+v"(cm), "+v"(m1[0]), "+v"(m1[1]), "+v"(m1[2]), "+v"(m1[3]), "+v"(mp[0]), "+v"(mp[1]), "+v"(mq_));
  asm volatile("s_waitcnt vmcnt(0)"
      :  :  : "memory"); SB();
  STFA(sFA + 0);
  STAMP(4);
  unsigned vFU, vFO;
  { const int da = (db0 * 37) >> 8, db = ((db0 + 3) * 37) >> 8, l_ = lane < 56 ? lane : 55;
    vFU = WS_FU + ((l_ < 28 ? da * 128 + l_ : db * 128 + l_ - 28) + w * 28) * (4 * 4);
    vFO = WS_FO + ((2 * st + (w >> 1)) * 8 + (lane & 7)) * (4 * 16);
    asm volatile("" : "+v"(vFU), "+v"(vFO)); }
  unsigned vFj[4], so0 = WS_OT + (4 * st + w) * 16384, so1 = so0 + 8192;
#pragma unroll
  for (int j = 0; j < 4; ++j) { const int k_ = BLK(j), k7 = (k_ * 37) >> 8; vFj[j] = vF + (WS_UT + k7 * (56 * 8192 - 896) + k_ * 128); OPV(vFj[j]); }
  asm volatile("" : "+s"(so0), "+s"(so1));
  const int tau = 2 * (n16 & 7) + (n16 >> 3);
  unsigned rwd = lds0 + w * SCW + vL16, rr = lds0 + w * 1024 + (16 * g4 + tau) * 16, ro = lds0 + WOB + vL16;
  unsigned xo = ldsS + 34816 + vL16, braw, xt = ldsS + 34816 + tau * 128 + (((g4 >> 1) ^ (n16 & 7)) * 16) + (g4 & 1) * 8, vB2 = (64 * st + 16 * w + tau) * 2;
  asm volatile("" : "+v"(rwd), "+v"(rr), "+v"(ro), "+v"(xt), "+v"(xo), "+v"(vB2));
  unsigned vXa = (64 * st + 16 * w + r8) * 896 + db0 * 32 + ((lane & 7) ^ (r8 >> 1)) * 16, vXb = (vXa ^ 64) + (8 * 896 - 1024); OPV(vXa); OPV(vXb);
  { unsigned k_ = sc_ + (rot2 >= 14 ? -14 : 0); OPQ(k_); su[0] = k_ << 13; sw[0] = k_ << 17; OPQ(su[0]); OPQ(sw[0]); }
  { unsigned k_ = sc_ + (rot2 >= 13 ? -13 : 1); OPQ(k_); su[1] = k_ << 13; sw[1] = k_ << 17; OPQ(su[1]); OPQ(sw[1]); }
  { unsigned k_ = sc_ + (rot2 >= 12 ? -12 : 2); OPQ(k_); su[2] = k_ << 13; sw[2] = k_ << 17; OPQ(su[2]); OPQ(sw[2]); }
  { unsigned k_ = sc_ + (rot2 >= 11 ? -11 : 3); OPQ(k_); su[3] = k_ << 13; sw[3] = k_ << 17; OPQ(su[3]); OPQ(sw[3]); }
  { unsigned k_ = sc_ + (rot2 >= 10 ? -10 : 4); OPQ(k_); su[4] = k_ << 13; sw[4] = k_ << 17; OPQ(su[4]); OPQ(sw[4]); }
  { unsigned k_ = sc_ + (rot2 >= 9 ? -9 : 5); OPQ(k_); su[5] = k_ << 13; sw[5] = k_ << 17; OPQ(su[5]); OPQ(sw[5]); }
  { unsigned k_ = sc_ + (rot2 >= 8 ? -8 : 6); OPQ(k_); su[6] = k_ << 13; sw[6] = k_ << 17; OPQ(su[6]); OPQ(sw[6]); }
  { unsigned k_ = sc_ + (rot2 >= 7 ? -7 : 7); OPQ(k_); su[7] = k_ << 13; sw[7] = k_ << 17; OPQ(su[7]); OPQ(sw[7]); }
  { unsigned k_ = sc_ + (rot2 >= 6 ? -6 : 8); OPQ(k_); su[8] = k_ << 13; sw[8] = k_ << 17; OPQ(su[8]); OPQ(sw[8]); }
  { unsigned k_ = sc_ + (rot2 >= 5 ? -5 : 9); OPQ(k_); su[9] = k_ << 13; sw[9] = k_ << 17; OPQ(su[9]); OPQ(sw[9]); }
  { unsigned k_ = sc_ + (rot2 >= 4 ? -4 : 10); OPQ(k_); su[10] = k_ << 13; sw[10] = k_ << 17; OPQ(su[10]); OPQ(sw[10]); }
  { unsigned k_ = sc_ + (rot2 >= 3 ? -3 : 11); OPQ(k_); su[11] = k_ << 13; sw[11] = k_ << 17; OPQ(su[11]); OPQ(sw[11]); }
  { unsigned k_ = sc_ + (rot2 >= 2 ? -2 : 12); OPQ(k_); su[12] = k_ << 13; sw[12] = k_ << 17; OPQ(su[12]); OPQ(sw[12]); }
  { unsigned k_ = sc_ + (rot2 >= 1 ? -1 : 13); OPQ(k_); su[13] = k_ << 13; sw[13] = k_ << 17; OPQ(su[13]); OPQ(sw[13]); }
  STAMP(5);
  __builtin_amdgcn_s_sleep(2);
  __builtin_amdgcn_s_sleep(2);
  __builtin_amdgcn_s_sleep(2);
  __builtin_amdgcn_s_sleep(2);
  __builtin_amdgcn_s_sleep(2);
  __builtin_amdgcn_s_sleep(2);
  asm volatile("buffer_load_dword %0, %2, %3, 0 offen sc1\n\tbuffer_load_dword %1, %4, %3, 0 offen sc1"
      : "=&v"(fw), "=&v"(fa) : "v"(vFW), "s"(rsW), "v"(vFA) : "memory"); SB();
  BAR();
  WAITV2(0, fw, fa);
  if (__ballot(fa != nonce) != 0ull) poll1<4>(rsW, vFA, nonce);
  STAMP(6);
  asm volatile("s_add_u32 m0, %1, 0\n\ts_add_u32 %0, %2, 0\n\tbuffer_load_dwordx4 %3, %4, %0 offen offset:0 sc1 lds\n\ts_add_u32 m0, %1, 4096\n\ts_add_u32 %0, %2, 32768\n\tbuffer_load_dwordx4 %3, %4, %0 offen offset:0 sc1 lds\n\ts_add_u32 m0, %1, 8192\n\ts_add_u32 %0, %2, 65536\n\tbuffer_load_dwordx4 %3, %4, %0 offen offset:0 sc1 lds\n\ts_add_u32 m0, %1, 12288\n\ts_add_u32 %0, %2, 98304\n\tbuffer_load_dwordx4 %3, %4, %0 offen offset:0 sc1 lds\n\ts_add_u32 m0, %1, 16384\n\ts_add_u32 %0, %5, 131072\n\tbuffer_load_dwordx4 %3, %4, %0 offen offset:0 sc1 lds"
      : "=&s"(sXt) : "s"(ldsX), "s"(sXa), "v"(vXN), "s"(rsW), "s"(sXb) : "memory", "scc"); SB();
  if (__ballot(fw != nonce) != 0ull) poll1<4>(rsW, vFW, nonce);
  STAMP(7);
  asm volatile("buffer_load_dwordx4 %0, %10, %11, %12 offen offset:0 sc1\n\tbuffer_load_dwordx4 %1, %10, %11, %13 offen offset:0 sc1\n\tbuffer_load_dwordx4 %2, %10, %11, %14 offen offset:0 sc1\n\tbuffer_load_dwordx4 %3, %10, %11, %12 offen offset:128 sc1\n\tbuffer_load_dwordx4 %4, %10, %11, %13 offen offset:128 sc1\n\tbuffer_load_dwordx4 %5, %10, %11, %14 offen offset:128 sc1\n\ts_add_u32 m0, %15, 20480\n\ts_add_u32 %6, %16, 16512\n\tbuffer_load_dwordx4 %17, %11, %6 offen offset:0 sc1 lds\n\ts_add_u32 m0, %15, 24576\n\ts_add_u32 %6, %16, 49280\n\tbuffer_load_dwordx4 %17, %11, %6 offen offset:0 sc1 lds\n\ts_add_u32 m0, %15, 28672\n\ts_add_u32 %6, %16, 82048\n\tbuffer_load_dwordx4 %17, %11, %6 offen offset:0 sc1 lds\n\ts_add_u32 m0, %15, 32768\n\ts_add_u32 %6, %16, 114816\n\tbuffer_load_dwordx4 %17, %11, %6 offen offset:0 sc1 lds\n\tbuffer_load_dwordx4 %7, %10, %11, %12 offen offset:256 sc1\n\tbuffer_load_dwordx4 %8, %10, %11, %13 offen offset:256 sc1\n\tbuffer_load_dwordx4 %9, %10, %11, %14 offen offset:256 sc1"
      : "=&a"(wq[0][0]), "=&a"(wq[0][1]), "=&a"(wq[0][2]), "=&a"(wq[1][0]), "=&a"(wq[1][1]), "=&a"(wq[1][2]), "=&s"(sXt), "=&a"(wq[2][0]), "=&a"(wq[2][1]), "=&a"(wq[2][2]) : "v"(vQ), "s"(rsW), "s"(sq[0]), "s"(sq[2]), "s"(sq[4]), "s"(ldsX), "s"(sXa), "v"(vXN) : "memory", "scc"); SB();
  f32x4 aq[8], ak[9], av[9]; bf16x8 xf[14][9]; u32x2 sg[27];
  asm volatile("s_waitcnt vmcnt(13)"
      :  :  : "memory"); SB();
  BAR0();
  STAMP(8);
  asm volatile("ds_read_b128 %0, %9 offset:0\n\tds_read_b128 %1, %9 offset:2048\n\tds_read_b128 %2, %9 offset:4096\n\tds_read_b128 %3, %9 offset:6144\n\tds_read_b128 %4, %9 offset:8192\n\tds_read_b128 %5, %9 offset:10240\n\tds_read_b128 %6, %9 offset:12288\n\tds_read_b128 %7, %9 offset:14336\n\tds_read_b128 %8, %9 offset:16384"
      : "=&v"(xf[0][0]), "=&v"(xf[0][1]), "=&v"(xf[0][2]), "=&v"(xf[0][3]), "=&v"(xf[0][4]), "=&v"(xf[0][5]), "=&v"(xf[0][6]), "=&v"(xf[0][7]), "=&v"(xf[0][8]) : "v"(xr0) : "memory"); SB();
  WAITVB(10);
  WAITL(8);
  SB();
  asm volatile("v_mfma_f32_16x16x32_bf16 %0, %4, %5, 0\n\ts_add_u32 m0, %6, 36864\n\ts_add_u32 %1, %7, 256\n\tv_mfma_f32_16x16x32_bf16 %2, %8, %5, 0\n\tds_read_b128 %3, %9 offset:0"
      : "=&v"(ak[0]), "=&s"(sXt), "=&v"(av[0]), "=&v"(xf[1][0]) : "a"(wq[0][1]), "v"(xf[0][0]), "s"(ldsX), "s"(sXa), "a"(wq[0][2]), "v"(xr1) : "memory", "scc"); SB();
  WAITL(8);
  SB();
  asm volatile("v_mfma_f32_16x16x32_bf16 %0, %4, %5, 0\n\tbuffer_load_dwordx4 %6, %7, %8 offen offset:0 sc1 lds\n\tv_mfma_f32_16x16x32_bf16 %1, %9, %5, 0\n\tds_read_b128 %2, %10 offset:2048\n\tv_mfma_f32_16x16x32_bf16 %3, %11, %5, 0"
      : "=&v"(ak[1]), "=&v"(av[1]), "=&v"(xf[1][1]), "=&v"(aq[0]) : "a"(wq[0][1]), "v"(xf[0][1]), "v"(vXN), "s"(rsW), "s"(sXt), "a"(wq[0][2]), "v"(xr1), "a"(wq[0][0]) : "memory"); SB();
  WAITL(8);
  SB();
  asm volatile("v_mfma_f32_16x16x32_bf16 %0, %6, %7, 0\n\tds_read_b128 %1, %8 offset:4096\n\tv_mfma_f32_16x16x32_bf16 %2, %9, %7, 0\n\tbuffer_load_dwordx4 %3, %10, %11, %12 offen offset:384 sc1\n\tv_mfma_f32_16x16x32_bf16 %4, %13, %7, 0\n\tds_read_b128 %5, %8 offset:6144"
      : "=&v"(ak[2]), "=&v"(xf[1][2]), "=&v"(av[2]), "=&a"(wq[3][0]), "=&v"(aq[1]), "=&v"(xf[1][3]) : "a"(wq[0][1]), "v"(xf[0][2]), "v"(xr1), "a"(wq[0][2]), "v"(vQ), "s"(rsW), "s"(sq[0]), "a"(wq[0][0]) : "memory"); SB();
  WAITL(9);
  SB();
  asm volatile("v_mfma_f32_16x16x32_bf16 %0, %5, %6, 0\n\ts_add_u32 m0, %7, 40960\n\ts_add_u32 %1, %8, 33024\n\tv_mfma_f32_16x16x32_bf16 %2, %9, %6, 0\n\tds_read_b128 %3, %10 offset:8192\n\tv_mfma_f32_16x16x32_bf16 %4, %11, %6, 0"
      : "=&v"(ak[3]), "=&s"(sXt), "=&v"(av[3]), "=&v"(xf[1][4]), "=&v"(aq[2]) : "a"(wq[0][1]), "v"(xf[0][3]), "s"(ldsX), "s"(sXa), "a"(wq[0][2]), "v"(xr1), "a"(wq[0][0]) : "memory", "scc"); SB();
  WAITL(9);
  SB();
  asm volatile("v_mfma_f32_16x16x32_bf16 %0, %5, %6, 0\n\tds_read_b128 %1, %7 offset:10240\n\tv_mfma_f32_16x16x32_bf16 %2, %8, %6, 0\n\tbuffer_load_dwordx4 %9, %10, %11 offen offset:0 sc1 lds\n\tv_mfma_f32_16x16x32_bf16 %3, %12, %6, 0\n\tds_read_b128 %4, %7 offset:12288"
      : "=&v"(ak[4]), "=&v"(xf[1][5]), "=&v"(av[4]), "=&v"(aq[3]), "=&v"(xf[1][6]) : "a"(wq[0][1]), "v"(xf[0][4]), "v"(xr1), "a"(wq[0][2]), "v"(vXN), "s"(rsW), "s"(sXt), "a"(wq[0][0]) : "memory"); SB();
  WAITL(10);
  SB();
  asm volatile("v_mfma_f32_16x16x32_bf16 %0, %5, %6, 0\n\tbuffer_load_dwordx4 %1, %7, %8, %9 offen offset:384 sc1\n\tv_mfma_f32_16x16x32_bf16 %2, %10, %6, 0\n\tds_read_b128 %3, %11 offset:14336\n\tv_mfma_f32_16x16x32_bf16 %4, %12, %6, 0"
      : "=&v"(ak[5]), "=&a"(wq[3][1]), "=&v"(av[5]), "=&v"(xf[1][7]), "=&v"(aq[4]) : "a"(wq[0][1]), "v"(xf[0][5]), "v"(vQ), "s"(rsW), "s"(sq[2]), "a"(wq[0][2]), "v"(xr1), "a"(wq[0][0]) : "memory"); SB();
  WAITL(10);
  SB();
  asm volatile("v_mfma_f32_16x16x32_bf16 %0, %5, %6, 0\n\tds_read_b128 %1, %7 offset:16384\n\tv_mfma_f32_16x16x32_bf16 %2, %8, %6, 0\n\ts_add_u32 m0, %9, 45056\n\ts_add_u32 %3, %10, 65792\n\tv_mfma_f32_16x16x32_bf16 %4, %11, %6, 0"
      : "=&v"(ak[6]), "=&v"(xf[1][8]), "=&v"(av[6]), "=&s"(sXt), "=&v"(aq[5]) : "a"(wq[0][1]), "v"(xf[0][6]), "v"(xr1), "a"(wq[0][2]), "s"(ldsX), "s"(sXa), "a"(wq[0][0]) : "memory", "scc"); SB();
  WAITL(10);
  SB();
  asm volatile("v_mfma_f32_16x16x32_bf16 %0, %4, %5, 0\n\tbuffer_load_dwordx4 %6, %7, %8 offen offset:0 sc1 lds\n\tv_mfma_f32_16x16x32_bf16 %1, %9, %5, 0\n\tbuffer_load_dwordx4 %2, %10, %7, %11 offen offset:384 sc1\n\tv_mfma_f32_16x16x32_bf16 %3, %12, %5, 0"
      : "=&v"(ak[7]), "=&v"(av[7]), "=&a"(wq[3][2]), "=&v"(aq[6]) : "a"(wq[0][1]), "v"(xf[0][7]), "v"(vXN), "s"(rsW), "s"(sXt), "a"(wq[0][2]), "v"(vQ), "s"(sq[4]), "a"(wq[0][0]) : "memory"); SB();
  WAITL(9);
  SB();
  asm volatile("v_mfma_f32_16x16x32_bf16 %0, %4, %5, 0\n\tbuffer_load_dwordx4 %1, %6, %7, %8 offen offset:512 sc1\n\tv_mfma_f32_16x16x32_bf16 %2, %9, %5, 0\n\tv_mfma_f32_16x16x32_bf16 %3, %10, %5, 0"
      : "=&v"(ak[8]), "=&a"(wq[4][0]), "=&v"(av[8]), "=&v"(aq[7]) : "a"(wq[0][1]), "v"(xf[0][8]), "v"(vQ), "s"(rsW), "s"(sq[0]), "a"(wq[0][2]), "a"(wq[0][0]) : "memory"); SB();
  WAITVB(14);
  WAITL(3);
  SB();
  asm volatile("v_mfma_f32_16x16x32_bf16 %0, %1, %2, %0\n\ts_waitcnt vmcnt(10)"
      : "+v"(ak[0]) : "a"(wq[1][1]), "v"(xf[1][0]) : "memory"); SB();
  BAR0();
  SB();
  asm volatile("v_mfma_f32_16x16x32_bf16 %0, %11, %12, %0\n\tds_read_b128 %1, %13 offset:18432\n\tv_mfma_f32_16x16x32_bf16 %2, %14, %15, %2\n\ts_add_u32 m0, %16, 49152\n\ts_add_u32 %3, %17, 98560\n\tv_mfma_f32_16x16x32_bf16 %4, %11, %15, %4\n\tds_read_b128 %5, %13 offset:20480\n\tv_mfma_f32_16x16x32_bf16 %6, %18, %15, %6\n\tbuffer_load_dwordx4 %19, %20, %3 offen offset:0 sc1 lds\n\tv_mfma_f32_16x16x32_bf16 %7, %14, %21, %7\n\tds_read_b128 %8, %13 offset:22528\n\tv_mfma_f32_16x16x32_bf16 %9, %11, %21, %9\n\tbuffer_load_dwordx4 %10, %22, %20, %23 offen offset:512 sc1"
      : "+v"(av[0]), "=&v"(xf[2][0]), "+v"(ak[1]), "=&s"(sXt), "+v"(av[1]), "=&v"(xf[2][1]), "+v"(aq[0]), "+v"(ak[2]), "=&v"(xf[2][2]), "+v"(av[2]), "=&a"(wq[4][1]) : "a"(wq[1][2]), "v"(xf[1][0]), "v"(xr0), "a"(wq[1][1]), "v"(xf[1][1]), "s"(ldsX), "s"(sXa), "a"(wq[1][0]), "v"(vXN), "s"(rsW), "v"(xf[1][2]), "v"(vQ), "s"(sq[2]) : "memory", "scc"); SB();
  asm volatile("v_mfma_f32_16x16x32_bf16 %0, %11, %12, %0\n\tds_read_b128 %1, %13 offset:24576\n\tv_mfma_f32_16x16x32_bf16 %2, %14, %15, %2\n\ts_add_u32 m0, %16, 53248\n\ts_add_u32 %3, %17, 131328\n\tv_mfma_f32_16x16x32_bf16 %4, %18, %15, %4\n\tds_read_b128 %5, %13 offset:26624\n\tv_mfma_f32_16x16x32_bf16 %6, %11, %15, %6\n\tbuffer_load_dwordx4 %19, %20, %3 offen offset:0 sc1 lds\n\tv_mfma_f32_16x16x32_bf16 %7, %14, %21, %7\n\tds_read_b128 %8, %13 offset:28672\n\tv_mfma_f32_16x16x32_bf16 %9, %18, %21, %9\n\tbuffer_load_dwordx4 %10, %22, %20, %23 offen offset:512 sc1"
      : "+v"(aq[1]), "=&v"(xf[2][3]), "+v"(ak[3]), "=&s"(sXt), "+v"(av[3]), "=&v"(xf[2][4]), "+v"(aq[2]), "+v"(ak[4]), "=&v"(xf[2][5]), "+v"(av[4]), "=&a"(wq[4][2]) : "a"(wq[1][0]), "v"(xf[1][2]), "v"(xr0), "a"(wq[1][1]), "v"(xf[1][3]), "s"(ldsX), "s"(sXb), "a"(wq[1][2]), "v"(vXN), "s"(rsW), "v"(xf[1][4]), "v"(vQ), "s"(sq[4]) : "memory", "scc"); SB();
  asm volatile("v_mfma_f32_16x16x32_bf16 %0, %7, %8, %0\n\tds_read_b128 %1, %9 offset:30720\n\tv_mfma_f32_16x16x32_bf16 %2, %10, %11, %2\n\ts_add_u32 m0, %12, 57344\n\ts_add_u32 %3, %13, 16768\n\tv_mfma_f32_16x16x32_bf16 %4, %14, %11, %4\n\tds_read_b128 %5, %9 offset:32768\n\tv_mfma_f32_16x16x32_bf16 %6, %7, %11, %6"
      : "+v"(aq[3]), "=&v"(xf[2][6]), "+v"(ak[5]), "=&s"(sXt), "+v"(av[5]), "=&v"(xf[2][7]), "+v"(aq[4]) : "a"(wq[1][0]), "v"(xf[1][4]), "v"(xr0), "a"(wq[1][1]), "v"(xf[1][5]), "s"(ldsX), "s"(sXa), "a"(wq[1][2]) : "memory", "scc"); SB();
  WAITL(8);
  SB();
  asm volatile("v_mfma_f32_16x16x32_bf16 %0, %10, %11, %0\n\tds_read_b128 %1, %12 offset:34816\n\tv_mfma_f32_16x16x32_bf16 %2, %13, %11, %2\n\tbuffer_load_dwordx4 %14, %15, %16 offen offset:0 sc1 lds\n\tv_mfma_f32_16x16x32_bf16 %3, %17, %11, %3\n\tbuffer_load_dwordx4 %4, %18, %15, %19 offen offset:640 sc1\n\tv_mfma_f32_16x16x32_bf16 %5, %10, %20, %5\n\tbuffer_load_dwordx4 %6, %18, %15, %21 offen offset:640 sc1\n\tv_mfma_f32_16x16x32_bf16 %7, %13, %20, %7\n\tv_mfma_f32_16x16x32_bf16 %8, %17, %20, %8\n\tv_mfma_f32_16x16x32_bf16 %9, %10, %22, %9"
      : "+v"(ak[6]), "=&v"(xf[2][8]), "+v"(av[6]), "+v"(aq[5]), "=&a"(wq[5][0]), "+v"(ak[7]), "=&a"(wq[5][1]), "+v"(av[7]), "+v"(aq[6]), "+v"(ak[8]) : "a"(wq[1][1]), "v"(xf[1][6]), "v"(xr0), "a"(wq[1][2]), "v"(vXN), "s"(rsW), "s"(sXt), "a"(wq[1][0]), "v"(vQ), "s"(sq[0]), "v"(xf[1][7]), "s"(sq[2]), "v"(xf[1][8]) : "memory"); SB();
  asm volatile("v_mfma_f32_16x16x32_bf16 %0, %2, %3, %0\n\tv_mfma_f32_16x16x32_bf16 %1, %4, %3, %1"
      : "+v"(av[8]), "+v"(aq[7]) : "a"(wq[1][2]), "v"(xf[1][8]), "a"(wq[1][0])); SB();
  STAMP(30);
  WAITVB(14);
  WAITL(3);
  SB();
  asm volatile("v_mfma_f32_16x16x32_bf16 %0, %11, %12, %0\n\ts_add_u32 m0, %13, 61440\n\ts_add_u32 %1, %14, 49536\n\tv_mfma_f32_16x16x32_bf16 %2, %15, %12, %2\n\tds_read_b128 %3, %16 offset:18432\n\tv_mfma_f32_16x16x32_bf16 %4, %11, %17, %4\n\tbuffer_load_dwordx4 %18, %19, %1 offen offset:0 sc1 lds\n\tv_mfma_f32_16x16x32_bf16 %5, %15, %17, %5\n\tds_read_b128 %6, %16 offset:20480\n\tv_mfma_f32_16x16x32_bf16 %7, %20, %17, %7\n\tbuffer_load_dwordx4 %8, %21, %19, %22 offen offset:640 sc1\n\tv_mfma_f32_16x16x32_bf16 %9, %11, %23, %9\n\tds_read_b128 %10, %16 offset:22528"
      : "+v"(ak[0]), "=&s"(sXt), "+v"(av[0]), "=&v"(xf[3][0]), "+v"(ak[1]), "+v"(av[1]), "=&v"(xf[3][1]), "+v"(aq[0]), "=&a"(wq[5][2]), "+v"(ak[2]), "=&v"(xf[3][2]) : "a"(wq[2][1]), "v"(xf[2][0]), "s"(ldsX), "s"(sXa), "a"(wq[2][2]), "v"(xr1), "v"(xf[2][1]), "v"(vXN), "s"(rsW), "a"(wq[2][0]), "v"(vQ), "s"(sq[4]), "v"(xf[2][2]) : "memory", "scc"); SB();
  asm volatile("v_mfma_f32_16x16x32_bf16 %0, %11, %12, %0\n\ts_add_u32 m0, %13, 65536\n\ts_add_u32 %1, %14, 82304\n\tv_mfma_f32_16x16x32_bf16 %2, %15, %12, %2\n\tds_read_b128 %3, %16 offset:24576\n\tv_mfma_f32_16x16x32_bf16 %4, %17, %18, %4\n\tbuffer_load_dwordx4 %19, %20, %1 offen offset:0 sc1 lds\n\tv_mfma_f32_16x16x32_bf16 %5, %11, %18, %5\n\tds_read_b128 %6, %16 offset:26624\n\tv_mfma_f32_16x16x32_bf16 %7, %15, %18, %7\n\tbuffer_load_dwordx4 %8, %21, %20, %22 offen offset:768 sc1\n\tv_mfma_f32_16x16x32_bf16 %9, %17, %23, %9\n\tds_read_b128 %10, %16 offset:28672"
      : "+v"(av[2]), "=&s"(sXt), "+v"(aq[1]), "=&v"(xf[3][3]), "+v"(ak[3]), "+v"(av[3]), "=&v"(xf[3][4]), "+v"(aq[2]), "=&a"(wq[6][0]), "+v"(ak[4]), "=&v"(xf[3][5]) : "a"(wq[2][2]), "v"(xf[2][2]), "s"(ldsX), "s"(sXa), "a"(wq[2][0]), "v"(xr1), "a"(wq[2][1]), "v"(xf[2][3]), "v"(vXN), "s"(rsW), "v"(vQ), "s"(sq[0]), "v"(xf[2][4]) : "memory", "scc"); SB();
  asm volatile("v_mfma_f32_16x16x32_bf16 %0, %8, %9, %0\n\tbuffer_load_dwordx4 %1, %10, %11, %12 offen offset:768 sc1\n\tv_mfma_f32_16x16x32_bf16 %2, %13, %9, %2\n\tds_read_b128 %3, %14 offset:30720\n\tv_mfma_f32_16x16x32_bf16 %4, %15, %16, %4\n\tv_mfma_f32_16x16x32_bf16 %5, %8, %16, %5\n\tds_read_b128 %6, %14 offset:32768\n\tv_mfma_f32_16x16x32_bf16 %7, %13, %16, %7"
      : "+v"(av[4]), "=&a"(wq[6][1]), "+v"(aq[3]), "=&v"(xf[3][6]), "+v"(ak[5]), "+v"(av[5]), "=&v"(xf[3][7]), "+v"(aq[4]) : "a"(wq[2][2]), "v"(xf[2][4]), "v"(vQ), "s"(rsW), "s"(sq[2]), "a"(wq[2][0]), "v"(xr1), "a"(wq[2][1]), "v"(xf[2][5]) : "memory"); SB();
  WAITL(8);
  SB();
  asm volatile("v_mfma_f32_16x16x32_bf16 %0, %10, %11, %0\n\tds_read_b128 %1, %12 offset:34816\n\tv_mfma_f32_16x16x32_bf16 %2, %13, %11, %2\n\tv_mfma_f32_16x16x32_bf16 %3, %14, %11, %3\n\tv_mfma_f32_16x16x32_bf16 %4, %10, %15, %4\n\tv_mfma_f32_16x16x32_bf16 %5, %13, %15, %5\n\tv_mfma_f32_16x16x32_bf16 %6, %14, %15, %6\n\tv_mfma_f32_16x16x32_bf16 %7, %10, %16, %7\n\tv_mfma_f32_16x16x32_bf16 %8, %13, %16, %8\n\tv_mfma_f32_16x16x32_bf16 %9, %14, %16, %9"
      : "+v"(ak[6]), "=&v"(xf[3][8]), "+v"(av[6]), "+v"(aq[5]), "+v"(ak[7]), "+v"(av[7]), "+v"(aq[6]), "+v"(ak[8]), "+v"(av[8]), "+v"(aq[7]) : "a"(wq[2][1]), "v"(xf[2][6]), "v"(xr1), "a"(wq[2][2]), "a"(wq[2][0]), "v"(xf[2][7]), "v"(xf[2][8]) : "memory"); SB();
  WAITVB(13);
  WAITL(3);
  SB();
  asm volatile("v_mfma_f32_16x16x32_bf16 %0, %1, %2, %0\n\ts_waitcnt vmcnt(9)"
      : "+v"(ak[0]) : "a"(wq[3][1]), "v"(xf[3][0]) : "memory"); SB();
  BAR0();
  SB();
  asm volatile("v_mfma_f32_16x16x32_bf16 %0, %11, %12, %0\n\tds_read_b128 %1, %13 offset:36864\n\tv_mfma_f32_16x16x32_bf16 %2, %14, %15, %2\n\ts_add_u32 m0, %16, 69632\n\ts_add_u32 %3, %17, 115072\n\tv_mfma_f32_16x16x32_bf16 %4, %11, %15, %4\n\tds_read_b128 %5, %13 offset:38912\n\tv_mfma_f32_16x16x32_bf16 %6, %18, %15, %6\n\tbuffer_load_dwordx4 %19, %20, %3 offen offset:0 sc1 lds\n\tv_mfma_f32_16x16x32_bf16 %7, %14, %21, %7\n\tds_read_b128 %8, %13 offset:40960\n\tv_mfma_f32_16x16x32_bf16 %9, %11, %21, %9\n\tbuffer_load_dwordx4 %10, %22, %20, %23 offen offset:768 sc1"
      : "+v"(av[0]), "=&v"(xf[4][0]), "+v"(ak[1]), "=&s"(sXt), "+v"(av[1]), "=&v"(xf[4][1]), "+v"(aq[0]), "+v"(ak[2]), "=&v"(xf[4][2]), "+v"(av[2]), "=&a"(wq[6][2]) : "a"(wq[3][2]), "v"(xf[3][0]), "v"(xr0), "a"(wq[3][1]), "v"(xf[3][1]), "s"(ldsX), "s"(sXa), "a"(wq[3][0]), "v"(vXN), "s"(rsW), "v"(xf[3][2]), "v"(vQ), "s"(sq[4]) : "memory", "scc"); SB();
  asm volatile("v_mfma_f32_16x16x32_bf16 %0, %11, %12, %0\n\tds_read_b128 %1, %13 offset:43008\n\tv_mfma_f32_16x16x32_bf16 %2, %14, %15, %2\n\ts_add_u32 m0, %16, 73728\n\ts_add_u32 %3, %17, 512\n\tv_mfma_f32_16x16x32_bf16 %4, %18, %15, %4\n\tds_read_b128 %5, %13 offset:45056\n\tv_mfma_f32_16x16x32_bf16 %6, %11, %15, %6\n\tbuffer_load_dwordx4 %19, %20, %3 offen offset:0 sc1 lds\n\tv_mfma_f32_16x16x32_bf16 %7, %14, %21, %7\n\tds_read_b128 %8, %13 offset:47104\n\tv_mfma_f32_16x16x32_bf16 %9, %18, %21, %9\n\tbuffer_load_dwordx4 %10, %22, %20, %23 offen offset:0 sc1"
      : "+v"(aq[1]), "=&v"(xf[4][3]), "+v"(ak[3]), "=&s"(sXt), "+v"(av[3]), "=&v"(xf[4][4]), "+v"(aq[2]), "+v"(ak[4]), "=&v"(xf[4][5]), "+v"(av[4]), "=&a"(wq[7][0]) : "a"(wq[3][0]), "v"(xf[3][2]), "v"(xr0), "a"(wq[3][1]), "v"(xf[3][3]), "s"(ldsX), "s"(sXa), "a"(wq[3][2]), "v"(vXN), "s"(rsW), "v"(xf[3][4]), "v"(vQ), "s"(sq[1]) : "memory", "scc"); SB();
  asm volatile("v_mfma_f32_16x16x32_bf16 %0, %7, %8, %0\n\tds_read_b128 %1, %9 offset:49152\n\tv_mfma_f32_16x16x32_bf16 %2, %10, %11, %2\n\ts_add_u32 m0, %12, 77824\n\ts_add_u32 %3, %13, 33280\n\tv_mfma_f32_16x16x32_bf16 %4, %14, %11, %4\n\tds_read_b128 %5, %9 offset:51200\n\tv_mfma_f32_16x16x32_bf16 %6, %7, %11, %6"
      : "+v"(aq[3]), "=&v"(xf[4][6]), "+v"(ak[5]), "=&s"(sXt), "+v"(av[5]), "=&v"(xf[4][7]), "+v"(aq[4]) : "a"(wq[3][0]), "v"(xf[3][4]), "v"(xr0), "a"(wq[3][1]), "v"(xf[3][5]), "s"(ldsX), "s"(sXa), "a"(wq[3][2]) : "memory", "scc"); SB();
  WAITL(8);
  SB();
  asm volatile("v_mfma_f32_16x16x32_bf16 %0, %10, %11, %0\n\tds_read_b128 %1, %12 offset:53248\n\tv_mfma_f32_16x16x32_bf16 %2, %13, %11, %2\n\tbuffer_load_dwordx4 %14, %15, %16 offen offset:0 sc1 lds\n\tv_mfma_f32_16x16x32_bf16 %3, %17, %11, %3\n\tbuffer_load_dwordx4 %4, %18, %15, %19 offen offset:0 sc1\n\tv_mfma_f32_16x16x32_bf16 %5, %10, %20, %5\n\tbuffer_load_dwordx4 %6, %18, %15, %21 offen offset:0 sc1\n\tv_mfma_f32_16x16x32_bf16 %7, %13, %20, %7\n\tv_mfma_f32_16x16x32_bf16 %8, %17, %20, %8\n\tv_mfma_f32_16x16x32_bf16 %9, %10, %22, %9"
      : "+v"(ak[6]), "=&v"(xf[4][8]), "+v"(av[6]), "+v"(aq[5]), "=&a"(wq[7][1]), "+v"(ak[7]), "=&a"(wq[7][2]), "+v"(av[7]), "+v"(aq[6]), "+v"(ak[8]) : "a"(wq[3][1]), "v"(xf[3][6]), "v"(xr0), "a"(wq[3][2]), "v"(vXN), "s"(rsW), "s"(sXt), "a"(wq[3][0]), "v"(vQ), "s"(sq[3]), "v"(xf[3][7]), "s"(sq[5]), "v"(xf[3][8]) : "memory"); SB();
  asm volatile("v_mfma_f32_16x16x32_bf16 %0, %2, %3, %0\n\tv_mfma_f32_16x16x32_bf16 %1, %4, %3, %1"
      : "+v"(av[8]), "+v"(aq[7]) : "a"(wq[3][2]), "v"(xf[3][8]), "a"(wq[3][0])); SB();
  WAITVB(15);
  WAITL(3);
  SB();
  asm volatile("v_mfma_f32_16x16x32_bf16 %0, %11, %12, %0\n\ts_add_u32 m0, %13, 81920\n\ts_add_u32 %1, %14, 66048\n\tv_mfma_f32_16x16x32_bf16 %2, %15, %12, %2\n\tds_read_b128 %3, %16 offset:36864\n\tv_mfma_f32_16x16x32_bf16 %4, %11, %17, %4\n\tbuffer_load_dwordx4 %18, %19, %1 offen offset:0 sc1 lds\n\tv_mfma_f32_16x16x32_bf16 %5, %15, %17, %5\n\tds_read_b128 %6, %16 offset:38912\n\tv_mfma_f32_16x16x32_bf16 %7, %20, %17, %7\n\tbuffer_load_dwordx4 %8, %21, %19, %22 offen offset:128 sc1\n\tv_mfma_f32_16x16x32_bf16 %9, %11, %23, %9\n\tds_read_b128 %10, %16 offset:40960"
      : "+v"(ak[0]), "=&s"(sXt), "+v"(av[0]), "=&v"(xf[5][0]), "+v"(ak[1]), "+v"(av[1]), "=&v"(xf[5][1]), "+v"(aq[0]), "=&a"(wq[8][0]), "+v"(ak[2]), "=&v"(xf[5][2]) : "a"(wq[4][1]), "v"(xf[4][0]), "s"(ldsX), "s"(sXa), "a"(wq[4][2]), "v"(xr1), "v"(xf[4][1]), "v"(vXN), "s"(rsW), "a"(wq[4][0]), "v"(vQ), "s"(sq[1]), "v"(xf[4][2]) : "memory", "scc"); SB();
  asm volatile("v_mfma_f32_16x16x32_bf16 %0, %11, %12, %0\n\ts_add_u32 m0, %13, 86016\n\ts_add_u32 %1, %14, 98816\n\tv_mfma_f32_16x16x32_bf16 %2, %15, %12, %2\n\tds_read_b128 %3, %16 offset:43008\n\tv_mfma_f32_16x16x32_bf16 %4, %17, %18, %4\n\tbuffer_load_dwordx4 %19, %20, %1 offen offset:0 sc1 lds\n\tv_mfma_f32_16x16x32_bf16 %5, %11, %18, %5\n\tds_read_b128 %6, %16 offset:45056\n\tv_mfma_f32_16x16x32_bf16 %7, %15, %18, %7\n\tbuffer_load_dwordx4 %8, %21, %20, %22 offen offset:128 sc1\n\tv_mfma_f32_16x16x32_bf16 %9, %17, %23, %9\n\tds_read_b128 %10, %16 offset:47104"
      : "+v"(av[2]), "=&s"(sXt), "+v"(aq[1]), "=&v"(xf[5][3]), "+v"(ak[3]), "+v"(av[3]), "=&v"(xf[5][4]), "+v"(aq[2]), "=&a"(wq[8][1]), "+v"(ak[4]), "=&v"(xf[5][5]) : "a"(wq[4][2]), "v"(xf[4][2]), "s"(ldsX), "s"(sXa), "a"(wq[4][0]), "v"(xr1), "a"(wq[4][1]), "v"(xf[4][3]), "v"(vXN), "s"(rsW), "v"(vQ), "s"(sq[3]), "v"(xf[4][4]) : "memory", "scc"); SB();
  asm volatile("v_mfma_f32_16x16x32_bf16 %0, %9, %10, %0\n\tbuffer_load_dwordx4 %1, %11, %12, %13 offen offset:128 sc1\n\tv_mfma_f32_16x16x32_bf16 %2, %14, %10, %2\n\tds_read_b128 %3, %15 offset:49152\n\tv_mfma_f32_16x16x32_bf16 %4, %16, %17, %4\n\tbuffer_load_dwordx4 %5, %11, %12, %18 offen offset:256 sc1\n\tv_mfma_f32_16x16x32_bf16 %6, %9, %17, %6\n\tds_read_b128 %7, %15 offset:51200\n\tv_mfma_f32_16x16x32_bf16 %8, %14, %17, %8"
      : "+v"(av[4]), "=&a"(wq[8][2]), "+v"(aq[3]), "=&v"(xf[5][6]), "+v"(ak[5]), "=&a"(wq[9][0]), "+v"(av[5]), "=&v"(xf[5][7]), "+v"(aq[4]) : "a"(wq[4][2]), "v"(xf[4][4]), "v"(vQ), "s"(rsW), "s"(sq[5]), "a"(wq[4][0]), "v"(xr1), "a"(wq[4][1]), "v"(xf[4][5]), "s"(sq[1]) : "memory"); SB();
  WAITL(8);
  SB();
  asm volatile("v_mfma_f32_16x16x32_bf16 %0, %10, %11, %0\n\tds_read_b128 %1, %12 offset:53248\n\tv_mfma_f32_16x16x32_bf16 %2, %13, %11, %2\n\tv_mfma_f32_16x16x32_bf16 %3, %14, %11, %3\n\tv_mfma_f32_16x16x32_bf16 %4, %10, %15, %4\n\tv_mfma_f32_16x16x32_bf16 %5, %13, %15, %5\n\tv_mfma_f32_16x16x32_bf16 %6, %14, %15, %6\n\tv_mfma_f32_16x16x32_bf16 %7, %10, %16, %7\n\tv_mfma_f32_16x16x32_bf16 %8, %13, %16, %8\n\tv_mfma_f32_16x16x32_bf16 %9, %14, %16, %9"
      : "+v"(ak[6]), "=&v"(xf[5][8]), "+v"(av[6]), "+v"(aq[5]), "+v"(ak[7]), "+v"(av[7]), "+v"(aq[6]), "+v"(ak[8]), "+v"(av[8]), "+v"(aq[7]) : "a"(wq[4][1]), "v"(xf[4][6]), "v"(xr1), "a"(wq[4][2]), "a"(wq[4][0]), "v"(xf[4][7]), "v"(xf[4][8]) : "memory"); SB();
  WAITVB(16);
  WAITL(3);
  SB();
  asm volatile("v_mfma_f32_16x16x32_bf16 %0, %3, %4, %0\n\tv_add_u32 %1, 55296, %1\n\tv_mfma_f32_16x16x32_bf16 %2, %5, %4, %2\n\ts_waitcnt vmcnt(12)"
      : "+v"(ak[0]), "+v"(xr0), "+v"(av[0]) : "a"(wq[5][1]), "v"(xf[5][0]), "a"(wq[5][2]) : "memory"); SB();
  BAR0();
  SB();
  asm volatile("v_mfma_f32_16x16x32_bf16 %0, %12, %13, %0\n\tds_read_b128 %1, %14 offset:0\n\tv_mfma_f32_16x16x32_bf16 %2, %15, %13, %2\n\tds_read_b128 %3, %14 offset:2048\n\tv_mfma_f32_16x16x32_bf16 %4, %16, %13, %4\n\ts_add_u32 m0, %17, 90112\n\ts_add_u32 %5, %18, 131584\n\tv_mfma_f32_16x16x32_bf16 %6, %12, %19, %6\n\tds_read_b128 %7, %14 offset:4096\n\tv_mfma_f32_16x16x32_bf16 %8, %15, %19, %8\n\tbuffer_load_dwordx4 %20, %21, %5 offen offset:0 sc1 lds\n\tv_mfma_f32_16x16x32_bf16 %9, %16, %19, %9\n\tds_read_b128 %10, %14 offset:6144\n\tv_mfma_f32_16x16x32_bf16 %11, %12, %22, %11"
      : "+v"(ak[1]), "=&v"(xf[6][0]), "+v"(av[1]), "=&v"(xf[6][1]), "+v"(aq[0]), "=&s"(sXt), "+v"(ak[2]), "=&v"(xf[6][2]), "+v"(av[2]), "+v"(aq[1]), "=&v"(xf[6][3]), "+v"(ak[3]) : "a"(wq[5][1]), "v"(xf[5][1]), "v"(xr0), "a"(wq[5][2]), "a"(wq[5][0]), "s"(ldsX), "s"(sXb), "v"(xf[5][2]), "v"(vXN), "s"(rsW), "v"(xf[5][3]) : "memory", "scc"); SB();
  asm volatile("buffer_load_dwordx4 %0, %11, %12, %13 offen offset:256 sc1\n\tv_mfma_f32_16x16x32_bf16 %1, %14, %15, %1\n\tds_read_b128 %2, %16 offset:8192\n\tv_mfma_f32_16x16x32_bf16 %3, %17, %15, %3\n\ts_add_u32 m0, %18, 94208\n\ts_add_u32 %4, %19, 17024\n\tv_mfma_f32_16x16x32_bf16 %5, %20, %21, %5\n\tds_read_b128 %6, %16 offset:10240\n\tv_mfma_f32_16x16x32_bf16 %7, %14, %21, %7\n\tbuffer_load_dwordx4 %22, %12, %4 offen offset:0 sc1 lds\n\tv_mfma_f32_16x16x32_bf16 %8, %17, %21, %8\n\tds_read_b128 %9, %16 offset:12288\n\tv_mfma_f32_16x16x32_bf16 %10, %20, %23, %10"
      : "=&a"(wq[9][1]), "+v"(av[3]), "=&v"(xf[6][4]), "+v"(aq[2]), "=&s"(sXt), "+v"(ak[4]), "=&v"(xf[6][5]), "+v"(av[4]), "+v"(aq[3]), "=&v"(xf[6][6]), "+v"(ak[5]) : "v"(vQ), "s"(rsW), "s"(sq[3]), "a"(wq[5][2]), "v"(xf[5][3]), "v"(xr0), "a"(wq[5][0]), "s"(ldsX), "s"(sXa), "a"(wq[5][1]), "v"(xf[5][4]), "v"(vXN), "v"(xf[5][5]) : "memory", "scc"); SB();
  asm volatile("buffer_load_dwordx4 %0, %4, %5, %6 offen offset:256 sc1\n\tv_mfma_f32_16x16x32_bf16 %1, %7, %8, %1\n\tds_read_b128 %2, %9 offset:14336\n\tv_mfma_f32_16x16x32_bf16 %3, %10, %8, %3"
      : "=&a"(wq[9][2]), "+v"(av[5]), "=&v"(xf[6][7]), "+v"(aq[4]) : "v"(vQ), "s"(rsW), "s"(sq[5]), "a"(wq[5][2]), "v"(xf[5][5]), "v"(xr0), "a"(wq[5][0]) : "memory"); SB();
  WAITL(8);
  SB();
  asm volatile("v_mfma_f32_16x16x32_bf16 %0, %10, %11, %0\n\tds_read_b128 %1, %12 offset:16384\n\tv_mfma_f32_16x16x32_bf16 %2, %13, %11, %2\n\ts_add_u32 m0, %14, 98304\n\ts_add_u32 %3, %15, 49792\n\tv_mfma_f32_16x16x32_bf16 %4, %16, %11, %4\n\tbuffer_load_dwordx4 %17, %18, %3 offen offset:0 sc1 lds\n\tv_mfma_f32_16x16x32_bf16 %5, %10, %19, %5\n\tbuffer_load_dwordx4 %6, %20, %18, %21 offen offset:384 sc1\n\tv_mfma_f32_16x16x32_bf16 %7, %13, %19, %7\n\tv_mfma_f32_16x16x32_bf16 %8, %16, %19, %8\n\tv_mfma_f32_16x16x32_bf16 %9, %10, %22, %9"
      : "+v"(ak[6]), "=&v"(xf[6][8]), "+v"(av[6]), "=&s"(sXt), "+v"(aq[5]), "+v"(ak[7]), "=&a"(wq[10][0]), "+v"(av[7]), "+v"(aq[6]), "+v"(ak[8]) : "a"(wq[5][1]), "v"(xf[5][6]), "v"(xr0), "a"(wq[5][2]), "s"(ldsX), "s"(sXa), "a"(wq[5][0]), "v"(vXN), "s"(rsW), "v"(xf[5][7]), "v"(vQ), "s"(sq[1]), "v"(xf[5][8]) : "memory", "scc"); SB();
  asm volatile("v_mfma_f32_16x16x32_bf16 %0, %2, %3, %0\n\tv_mfma_f32_16x16x32_bf16 %1, %4, %3, %1"
      : "+v"(av[8]), "+v"(aq[7]) : "a"(wq[5][2]), "v"(xf[5][8]), "a"(wq[5][0])); SB();
  STAMP(31);
  WAITVB(17);
  WAITL(3);
  SB();
  asm volatile("v_mfma_f32_16x16x32_bf16 %0, %12, %13, %0\n\tv_add_u32 %1, 55296, %1\n\tv_mfma_f32_16x16x32_bf16 %2, %14, %13, %2\n\tds_read_b128 %3, %1 offset:0\n\tv_mfma_f32_16x16x32_bf16 %4, %12, %15, %4\n\ts_add_u32 m0, %16, 102400\n\ts_add_u32 %5, %17, 82560\n\tv_mfma_f32_16x16x32_bf16 %6, %14, %15, %6\n\tds_read_b128 %7, %1 offset:2048\n\tv_mfma_f32_16x16x32_bf16 %8, %18, %15, %8\n\tbuffer_load_dwordx4 %19, %20, %5 offen offset:0 sc1 lds\n\tv_mfma_f32_16x16x32_bf16 %9, %12, %21, %9\n\tds_read_b128 %10, %1 offset:4096\n\tv_mfma_f32_16x16x32_bf16 %11, %14, %21, %11"
      : "+v"(ak[0]), "+v"(xr1), "+v"(av[0]), "=&v"(xf[7][0]), "+v"(ak[1]), "=&s"(sXt), "+v"(av[1]), "=&v"(xf[7][1]), "+v"(aq[0]), "+v"(ak[2]), "=&v"(xf[7][2]), "+v"(av[2]) : "a"(wq[6][1]), "v"(xf[6][0]), "a"(wq[6][2]), "v"(xf[6][1]), "s"(ldsX), "s"(sXa), "a"(wq[6][0]), "v"(vXN), "s"(rsW), "v"(xf[6][2]) : "memory", "scc"); SB();
  asm volatile("buffer_load_dwordx4 %0, %11, %12, %13 offen offset:384 sc1\n\tv_mfma_f32_16x16x32_bf16 %1, %14, %15, %1\n\tds_read_b128 %2, %16 offset:6144\n\tv_mfma_f32_16x16x32_bf16 %3, %17, %18, %3\n\ts_add_u32 m0, %19, 106496\n\ts_add_u32 %4, %20, 115328\n\tv_mfma_f32_16x16x32_bf16 %5, %21, %18, %5\n\tds_read_b128 %6, %16 offset:8192\n\tv_mfma_f32_16x16x32_bf16 %7, %14, %18, %7\n\tbuffer_load_dwordx4 %22, %12, %4 offen offset:0 sc1 lds\n\tv_mfma_f32_16x16x32_bf16 %8, %17, %23, %8\n\tds_read_b128 %9, %16 offset:10240\n\tv_mfma_f32_16x16x32_bf16 %10, %21, %23, %10"
      : "=&a"(wq[10][1]), "+v"(aq[1]), "=&v"(xf[7][3]), "+v"(ak[3]), "=&s"(sXt), "+v"(av[3]), "=&v"(xf[7][4]), "+v"(aq[2]), "+v"(ak[4]), "=&v"(xf[7][5]), "+v"(av[4]) : "v"(vQ), "s"(rsW), "s"(sq[3]), "a"(wq[6][0]), "v"(xf[6][2]), "v"(xr1), "a"(wq[6][1]), "v"(xf[6][3]), "s"(ldsX), "s"(sXa), "a"(wq[6][2]), "v"(vXN), "v"(xf[6][4]) : "memory", "scc"); SB();
  asm volatile("buffer_load_dwordx4 %0, %8, %9, %10 offen offset:384 sc1\n\tv_mfma_f32_16x16x32_bf16 %1, %11, %12, %1\n\tds_read_b128 %2, %13 offset:12288\n\tv_mfma_f32_16x16x32_bf16 %3, %14, %15, %3\n\tbuffer_load_dwordx4 %4, %8, %9, %16 offen offset:512 sc1\n\tv_mfma_f32_16x16x32_bf16 %5, %17, %15, %5\n\tds_read_b128 %6, %13 offset:14336\n\tv_mfma_f32_16x16x32_bf16 %7, %11, %15, %7"
      : "=&a"(wq[10][2]), "+v"(aq[3]), "=&v"(xf[7][6]), "+v"(ak[5]), "=&a"(wq[11][0]), "+v"(av[5]), "=&v"(xf[7][7]), "+v"(aq[4]) : "v"(vQ), "s"(rsW), "s"(sq[5]), "a"(wq[6][0]), "v"(xf[6][4]), "v"(xr1), "a"(wq[6][1]), "v"(xf[6][5]), "s"(sq[1]), "a"(wq[6][2]) : "memory"); SB();
  WAITL(8);
  SB();
  asm volatile("v_mfma_f32_16x16x32_bf16 %0, %11, %12, %0\n\tds_read_b128 %1, %13 offset:16384\n\tv_mfma_f32_16x16x32_bf16 %2, %14, %12, %2\n\tbuffer_load_dwordx4 %3, %15, %16, %17 offen offset:512 sc1\n\tv_mfma_f32_16x16x32_bf16 %4, %18, %12, %4\n\tv_mfma_f32_16x16x32_bf16 %5, %11, %19, %5\n\tv_mfma_f32_16x16x32_bf16 %6, %14, %19, %6\n\tv_mfma_f32_16x16x32_bf16 %7, %18, %19, %7\n\tv_mfma_f32_16x16x32_bf16 %8, %11, %20, %8\n\tv_mfma_f32_16x16x32_bf16 %9, %14, %20, %9\n\tv_mfma_f32_16x16x32_bf16 %10, %18, %20, %10"
      : "+v"(ak[6]), "=&v"(xf[7][8]), "+v"(av[6]), "=&a"(wq[11][1]), "+v"(aq[5]), "+v"(ak[7]), "+v"(av[7]), "+v"(aq[6]), "+v"(ak[8]), "+v"(av[8]), "+v"(aq[7]) : "a"(wq[6][1]), "v"(xf[6][6]), "v"(xr1), "a"(wq[6][2]), "v"(vQ), "s"(rsW), "s"(sq[3]), "a"(wq[6][0]), "v"(xf[6][7]), "v"(xf[6][8]) : "memory"); SB();
  WAITVB(18);
  WAITL(3);
  SB();
  asm volatile("v_mfma_f32_16x16x32_bf16 %0, %1, %2, %0\n\ts_waitcnt vmcnt(11)"
      : "+v"(ak[0]) : "a"(wq[7][1]), "v"(xf[7][0]) : "memory"); SB();
  BAR0();
  SB();
  asm volatile("v_mfma_f32_16x16x32_bf16 %0, %11, %12, %0\n\tds_read_b128 %1, %13 offset:18432\n\tv_mfma_f32_16x16x32_bf16 %2, %14, %15, %2\n\ts_add_u32 m0, %16, 110592\n\ts_add_u32 %3, %17, 768\n\tv_mfma_f32_16x16x32_bf16 %4, %11, %15, %4\n\tds_read_b128 %5, %13 offset:20480\n\tv_mfma_f32_16x16x32_bf16 %6, %18, %15, %6\n\tbuffer_load_dwordx4 %19, %20, %3 offen offset:0 sc1 lds\n\tv_mfma_f32_16x16x32_bf16 %7, %14, %21, %7\n\tds_read_b128 %8, %13 offset:22528\n\tv_mfma_f32_16x16x32_bf16 %9, %11, %21, %9\n\tbuffer_load_dwordx4 %10, %22, %20, %23 offen offset:512 sc1"
      : "+v"(av[0]), "=&v"(xf[8][0]), "+v"(ak[1]), "=&s"(sXt), "+v"(av[1]), "=&v"(xf[8][1]), "+v"(aq[0]), "+v"(ak[2]), "=&v"(xf[8][2]), "+v"(av[2]), "=&a"(wq[11][2]) : "a"(wq[7][2]), "v"(xf[7][0]), "v"(xr0), "a"(wq[7][1]), "v"(xf[7][1]), "s"(ldsX), "s"(sXa), "a"(wq[7][0]), "v"(vXN), "s"(rsW), "v"(xf[7][2]), "v"(vQ), "s"(sq[5]) : "memory", "scc"); SB();
  asm volatile("v_mfma_f32_16x16x32_bf16 %0, %11, %12, %0\n\tds_read_b128 %1, %13 offset:24576\n\tv_mfma_f32_16x16x32_bf16 %2, %14, %15, %2\n\ts_add_u32 m0, %16, 114688\n\ts_add_u32 %3, %17, 33536\n\tv_mfma_f32_16x16x32_bf16 %4, %18, %15, %4\n\tds_read_b128 %5, %13 offset:26624\n\tv_mfma_f32_16x16x32_bf16 %6, %11, %15, %6\n\tbuffer_load_dwordx4 %19, %20, %3 offen offset:0 sc1 lds\n\tv_mfma_f32_16x16x32_bf16 %7, %14, %21, %7\n\tds_read_b128 %8, %13 offset:28672\n\tv_mfma_f32_16x16x32_bf16 %9, %18, %21, %9\n\tbuffer_load_dwordx4 %10, %22, %20, %23 offen offset:640 sc1"
      : "+v"(aq[1]), "=&v"(xf[8][3]), "+v"(ak[3]), "=&s"(sXt), "+v"(av[3]), "=&v"(xf[8][4]), "+v"(aq[2]), "+v"(ak[4]), "=&v"(xf[8][5]), "+v"(av[4]), "=&a"(wq[12][0]) : "a"(wq[7][0]), "v"(xf[7][2]), "v"(xr0), "a"(wq[7][1]), "v"(xf[7][3]), "s"(ldsX), "s"(sXa), "a"(wq[7][2]), "v"(vXN), "s"(rsW), "v"(xf[7][4]), "v"(vQ), "s"(sq[1]) : "memory", "scc"); SB();
  asm volatile("v_mfma_f32_16x16x32_bf16 %0, %7, %8, %0\n\tds_read_b128 %1, %9 offset:30720\n\tv_mfma_f32_16x16x32_bf16 %2, %10, %11, %2\n\ts_add_u32 m0, %12, 118784\n\ts_add_u32 %3, %13, 66304\n\tv_mfma_f32_16x16x32_bf16 %4, %14, %11, %4\n\tds_read_b128 %5, %9 offset:32768\n\tv_mfma_f32_16x16x32_bf16 %6, %7, %11, %6"
      : "+v"(aq[3]), "=&v"(xf[8][6]), "+v"(ak[5]), "=&s"(sXt), "+v"(av[5]), "=&v"(xf[8][7]), "+v"(aq[4]) : "a"(wq[7][0]), "v"(xf[7][4]), "v"(xr0), "a"(wq[7][1]), "v"(xf[7][5]), "s"(ldsX), "s"(sXa), "a"(wq[7][2]) : "memory", "scc"); SB();
  WAITL(8);
  SB();
  asm volatile("v_mfma_f32_16x16x32_bf16 %0, %10, %11, %0\n\tds_read_b128 %1, %12 offset:34816\n\tv_mfma_f32_16x16x32_bf16 %2, %13, %11, %2\n\tbuffer_load_dwordx4 %14, %15, %16 offen offset:0 sc1 lds\n\tv_mfma_f32_16x16x32_bf16 %3, %17, %11, %3\n\tbuffer_load_dwordx4 %4, %18, %15, %19 offen offset:640 sc1\n\tv_mfma_f32_16x16x32_bf16 %5, %10, %20, %5\n\tbuffer_load_dwordx4 %6, %18, %15, %21 offen offset:640 sc1\n\tv_mfma_f32_16x16x32_bf16 %7, %13, %20, %7\n\tbuffer_load_dword %8, %22, %15, 0 offen sc1\n\tv_mfma_f32_16x16x32_bf16 %9, %17, %20, %9"
      : "+v"(ak[6]), "=&v"(xf[8][8]), "+v"(av[6]), "+v"(aq[5]), "=&a"(wq[12][1]), "+v"(ak[7]), "=&a"(wq[12][2]), "+v"(av[7]), "=&v"(fq), "+v"(aq[6]) : "a"(wq[7][1]), "v"(xf[7][6]), "v"(xr0), "a"(wq[7][2]), "v"(vXN), "s"(rsW), "s"(sXt), "a"(wq[7][0]), "v"(vQ), "s"(sq[3]), "v"(xf[7][7]), "s"(sq[5]), "v"(vFQ) : "memory"); SB();
  asm volatile("v_mfma_f32_16x16x32_bf16 %0, %3, %4, %0\n\tv_mfma_f32_16x16x32_bf16 %1, %5, %4, %1\n\tv_mfma_f32_16x16x32_bf16 %2, %6, %4, %2"
      : "+v"(ak[8]), "+v"(av[8]), "+v"(aq[7]) : "a"(wq[7][1]), "v"(xf[7][8]), "a"(wq[7][2]), "a"(wq[7][0])); SB();
  WAITVB(21);
  WAITL(3);
  SB();
  asm volatile("v_mfma_f32_16x16x32_bf16 %0, %11, %12, %0\n\ts_add_u32 m0, %13, 122880\n\ts_add_u32 %1, %14, 99072\n\tv_mfma_f32_16x16x32_bf16 %2, %15, %12, %2\n\tds_read_b128 %3, %16 offset:18432\n\tv_mfma_f32_16x16x32_bf16 %4, %11, %17, %4\n\tbuffer_load_dwordx4 %18, %19, %1 offen offset:0 sc1 lds\n\tv_mfma_f32_16x16x32_bf16 %5, %15, %17, %5\n\tds_read_b128 %6, %16 offset:20480\n\tv_mfma_f32_16x16x32_bf16 %7, %20, %17, %7\n\tbuffer_load_dwordx4 %8, %21, %19, %22 offen offset:768 sc1\n\tv_mfma_f32_16x16x32_bf16 %9, %11, %23, %9\n\tds_read_b128 %10, %16 offset:22528"
      : "+v"(ak[0]), "=&s"(sXt), "+v"(av[0]), "=&v"(xf[9][0]), "+v"(ak[1]), "+v"(av[1]), "=&v"(xf[9][1]), "+v"(aq[0]), "=&a"(wq[13][0]), "+v"(ak[2]), "=&v"(xf[9][2]) : "a"(wq[8][1]), "v"(xf[8][0]), "s"(ldsX), "s"(sXa), "a"(wq[8][2]), "v"(xr1), "v"(xf[8][1]), "v"(vXN), "s"(rsW), "a"(wq[8][0]), "v"(vQ), "s"(sq[1]), "v"(xf[8][2]) : "memory", "scc"); SB();
  asm volatile("v_mfma_f32_16x16x32_bf16 %0, %11, %12, %0\n\ts_add_u32 m0, %13, 126976\n\ts_add_u32 %1, %14, 131840\n\tv_mfma_f32_16x16x32_bf16 %2, %15, %12, %2\n\tds_read_b128 %3, %16 offset:24576\n\tv_mfma_f32_16x16x32_bf16 %4, %17, %18, %4\n\tbuffer_load_dwordx4 %19, %20, %1 offen offset:0 sc1 lds\n\tv_mfma_f32_16x16x32_bf16 %5, %11, %18, %5\n\tds_read_b128 %6, %16 offset:26624\n\tv_mfma_f32_16x16x32_bf16 %7, %15, %18, %7\n\tbuffer_load_dwordx4 %8, %21, %20, %22 offen offset:768 sc1\n\tv_mfma_f32_16x16x32_bf16 %9, %17, %23, %9\n\tds_read_b128 %10, %16 offset:28672"
      : "+v"(av[2]), "=&s"(sXt), "+v"(aq[1]), "=&v"(xf[9][3]), "+v"(ak[3]), "+v"(av[3]), "=&v"(xf[9][4]), "+v"(aq[2]), "=&a"(wq[13][1]), "+v"(ak[4]), "=&v"(xf[9][5]) : "a"(wq[8][2]), "v"(xf[8][2]), "s"(ldsX), "s"(sXc), "a"(wq[8][0]), "v"(xr1), "a"(wq[8][1]), "v"(xf[8][3]), "v"(vXN), "s"(rsW), "v"(vQ), "s"(sq[3]), "v"(xf[8][4]) : "memory", "scc"); SB();
  asm volatile("v_mfma_f32_16x16x32_bf16 %0, %5, %6, %0\n\tbuffer_load_dwordx4 %1, %7, %8, %9 offen offset:768 sc1\n\tv_mfma_f32_16x16x32_bf16 %2, %10, %6, %2\n\tds_read_b128 %3, %11 offset:30720\n\tv_mfma_f32_16x16x32_bf16 %4, %12, %13, %4"
      : "+v"(av[4]), "=&a"(wq[13][2]), "+v"(aq[3]), "=&v"(xf[9][6]), "+v"(ak[5]) : "a"(wq[8][2]), "v"(xf[8][4]), "v"(vQ), "s"(rsW), "s"(sq[5]), "a"(wq[8][0]), "v"(xr1), "a"(wq[8][1]), "v"(xf[8][5]) : "memory"); SB();
  { const unsigned vT = lane < 14 ? ((jb * 4 + w) * 14 + lane) * 4096 + ((st & ~7) + ((3 - (int)((size_t)w2 >> 7)) & 7)) * 128 : 0x40000000u; TOUCH(vT, rs2, 0u, 0); }
  SB();
  asm volatile("v_mfma_f32_16x16x32_bf16 %0, %3, %4, %0\n\tds_read_b128 %1, %5 offset:32768\n\tv_mfma_f32_16x16x32_bf16 %2, %6, %4, %2"
      : "+v"(av[5]), "=&v"(xf[9][7]), "+v"(aq[4]) : "a"(wq[8][2]), "v"(xf[8][5]), "v"(xr1), "a"(wq[8][0]) : "memory"); SB();
  WAITL(8);
  SB();
  asm volatile("v_mfma_f32_16x16x32_bf16 %0, %10, %11, %0\n\tds_read_b128 %1, %12 offset:34816\n\tv_mfma_f32_16x16x32_bf16 %2, %13, %11, %2\n\tv_mfma_f32_16x16x32_bf16 %3, %14, %11, %3\n\tv_mfma_f32_16x16x32_bf16 %4, %10, %15, %4\n\tv_mfma_f32_16x16x32_bf16 %5, %13, %15, %5\n\tv_mfma_f32_16x16x32_bf16 %6, %14, %15, %6\n\tv_mfma_f32_16x16x32_bf16 %7, %10, %16, %7\n\tv_mfma_f32_16x16x32_bf16 %8, %13, %16, %8\n\tv_mfma_f32_16x16x32_bf16 %9, %14, %16, %9"
      : "+v"(ak[6]), "=&v"(xf[9][8]), "+v"(av[6]), "+v"(aq[5]), "+v"(ak[7]), "+v"(av[7]), "+v"(aq[6]), "+v"(ak[8]), "+v"(av[8]), "+v"(aq[7]) : "a"(wq[8][1]), "v"(xf[8][6]), "v"(xr1), "a"(wq[8][2]), "a"(wq[8][0]), "v"(xf[8][7]), "v"(xf[8][8]) : "memory"); SB();
  WAITVB(22);
  WAITL(3);
  SB();
  asm volatile("v_mfma_f32_16x16x32_bf16 %0, %3, %4, %0\n\tv_add_u32 %1, 36864, %1\n\ts_add_u32 m0, %5, 18432\n\tv_mfma_f32_16x16x32_bf16 %2, %6, %4, %2\n\ts_waitcnt vmcnt(17)"
      : "+v"(ak[0]), "+v"(xr0), "+v"(av[0]) : "a"(wq[9][1]), "v"(xf[9][0]), "s"(ldsS), "a"(wq[9][2]) : "memory", "scc"); SB();
  BAR0();
  SB();
  asm volatile("v_mfma_f32_16x16x32_bf16 %0, %11, %12, %0\n\tds_read_b128 %1, %13 offset:0\n\tv_mfma_f32_16x16x32_bf16 %2, %14, %12, %2\n\tds_read_b128 %3, %13 offset:2048\n\tv_mfma_f32_16x16x32_bf16 %4, %15, %12, %4\n\tbuffer_load_dwordx4 %16, %17, %18 offen offset:0  lds\n\tv_mfma_f32_16x16x32_bf16 %5, %11, %19, %5\n\tds_read_b128 %6, %13 offset:4096\n\tv_mfma_f32_16x16x32_bf16 %7, %14, %19, %7\n\tbuffer_load_dwordx4 %20, %17, %18 offen offset:1024  lds\n\tv_mfma_f32_16x16x32_bf16 %8, %15, %19, %8\n\tds_read_b128 %9, %13 offset:6144\n\tv_mfma_f32_16x16x32_bf16 %10, %11, %21, %10\n\tbuffer_load_dwordx4 %22, %17, %18 offen offset:2048  lds"
      : "+v"(ak[1]), "=&v"(xf[10][0]), "+v"(av[1]), "=&v"(xf[10][1]), "+v"(aq[0]), "+v"(ak[2]), "=&v"(xf[10][2]), "+v"(av[2]), "+v"(aq[1]), "=&v"(xf[10][3]), "+v"(ak[3]) : "a"(wq[9][1]), "v"(xf[9][1]), "v"(xr0), "a"(wq[9][2]), "a"(wq[9][0]), "v"(vW2[0]), "s"(rs2), "s"(sw[0]), "v"(xf[9][2]), "v"(vW2[1]), "v"(xf[9][3]), "v"(vW2[2]) : "memory"); SB();
  asm volatile("v_mfma_f32_16x16x32_bf16 %0, %12, %13, %0\n\tds_read_b128 %1, %14 offset:8192\n\tv_mfma_f32_16x16x32_bf16 %2, %15, %13, %2\n\tbuffer_load_dwordx4 %16, %17, %18 offen offset:3072  lds\n\tv_mfma_f32_16x16x32_bf16 %3, %19, %20, %3\n\tds_read_b128 %4, %14 offset:10240\n\tv_mfma_f32_16x16x32_bf16 %5, %12, %20, %5\n\tv_mfma_f32_16x16x32_bf16 %6, %15, %20, %6\n\tds_read_b128 %7, %14 offset:12288\n\tv_mfma_f32_16x16x32_bf16 %8, %19, %21, %8\n\tv_mfma_f32_16x16x32_bf16 %9, %12, %21, %9\n\tds_read_b128 %10, %14 offset:14336\n\tv_mfma_f32_16x16x32_bf16 %11, %15, %21, %11"
      : "+v"(av[3]), "=&v"(xf[10][4]), "+v"(aq[2]), "+v"(ak[4]), "=&v"(xf[10][5]), "+v"(av[4]), "+v"(aq[3]), "=&v"(xf[10][6]), "+v"(ak[5]), "+v"(av[5]), "=&v"(xf[10][7]), "+v"(aq[4]) : "a"(wq[9][2]), "v"(xf[9][3]), "v"(xr0), "a"(wq[9][0]), "v"(vW2[3]), "s"(rs2), "s"(sw[0]), "a"(wq[9][1]), "v"(xf[9][4]), "v"(xf[9][5]) : "memory"); SB();
  WAITL(8);
  SB();
  asm volatile("v_mfma_f32_16x16x32_bf16 %0, %10, %11, %0\n\tds_read_b128 %1, %12 offset:16384\n\tv_mfma_f32_16x16x32_bf16 %2, %13, %11, %2\n\tv_mfma_f32_16x16x32_bf16 %3, %14, %11, %3\n\tv_mfma_f32_16x16x32_bf16 %4, %10, %15, %4\n\tv_mfma_f32_16x16x32_bf16 %5, %13, %15, %5\n\tv_mfma_f32_16x16x32_bf16 %6, %14, %15, %6\n\tv_mfma_f32_16x16x32_bf16 %7, %10, %16, %7\n\tv_mfma_f32_16x16x32_bf16 %8, %13, %16, %8\n\tv_mfma_f32_16x16x32_bf16 %9, %14, %16, %9"
      : "+v"(ak[6]), "=&v"(xf[10][8]), "+v"(av[6]), "+v"(aq[5]), "+v"(ak[7]), "+v"(av[7]), "+v"(aq[6]), "+v"(ak[8]), "+v"(av[8]), "+v"(aq[7]) : "a"(wq[9][1]), "v"(xf[9][6]), "v"(xr0), "a"(wq[9][2]), "a"(wq[9][0]), "v"(xf[9][7]), "v"(xf[9][8]) : "memory"); SB();
  STAMP(32);
  WAITVB(20);
  WAITL(3);
  SB();
  asm volatile("v_mfma_f32_16x16x32_bf16 %0, %11, %12, %0\n\tv_add_u32 %1, 36864, %1\n\ts_add_u32 m0, %13, 34816\n\tv_mfma_f32_16x16x32_bf16 %2, %14, %12, %2\n\tds_read_b128 %3, %1 offset:0\n\tv_mfma_f32_16x16x32_bf16 %4, %11, %15, %4\n\tbuffer_load_dwordx4 %16, %17, %18 offen offset:0  lds\n\tv_mfma_f32_16x16x32_bf16 %5, %14, %15, %5\n\tds_read_b128 %6, %1 offset:2048\n\tv_mfma_f32_16x16x32_bf16 %7, %19, %15, %7\n\tbuffer_load_dwordx4 %20, %17, %18 offen offset:1024  lds\n\tv_mfma_f32_16x16x32_bf16 %8, %11, %21, %8\n\tds_read_b128 %9, %1 offset:4096\n\tv_mfma_f32_16x16x32_bf16 %10, %14, %21, %10"
      : "+v"(ak[0]), "+v"(xr1), "+v"(av[0]), "=&v"(xf[11][0]), "+v"(ak[1]), "+v"(av[1]), "=&v"(xf[11][1]), "+v"(aq[0]), "+v"(ak[2]), "=&v"(xf[11][2]), "+v"(av[2]) : "a"(wq[10][1]), "v"(xf[10][0]), "s"(ldsS), "a"(wq[10][2]), "v"(xf[10][1]), "v"(vW2[0]), "s"(rs2), "s"(sw[1]), "a"(wq[10][0]), "v"(vW2[1]), "v"(xf[10][2]) : "memory", "scc"); SB();
  asm volatile("buffer_load_dwordx4 %11, %12, %13 offen offset:2048  lds\n\tv_mfma_f32_16x16x32_bf16 %0, %14, %15, %0\n\tds_read_b128 %1, %16 offset:6144\n\tv_mfma_f32_16x16x32_bf16 %2, %17, %18, %2\n\tbuffer_load_dwordx4 %19, %12, %13 offen offset:3072  lds\n\tv_mfma_f32_16x16x32_bf16 %3, %20, %18, %3\n\tds_read_b128 %4, %16 offset:8192\n\tv_mfma_f32_16x16x32_bf16 %5, %14, %18, %5\n\tv_mfma_f32_16x16x32_bf16 %6, %17, %21, %6\n\tds_read_b128 %7, %16 offset:10240\n\tv_mfma_f32_16x16x32_bf16 %8, %20, %21, %8\n\tv_mfma_f32_16x16x32_bf16 %9, %14, %21, %9\n\tds_read_b128 %10, %16 offset:12288"
      : "+v"(aq[1]), "=&v"(xf[11][3]), "+v"(ak[3]), "+v"(av[3]), "=&v"(xf[11][4]), "+v"(aq[2]), "+v"(ak[4]), "=&v"(xf[11][5]), "+v"(av[4]), "+v"(aq[3]), "=&v"(xf[11][6]) : "v"(vW2[2]), "s"(rs2), "s"(sw[1]), "a"(wq[10][0]), "v"(xf[10][2]), "v"(xr1), "a"(wq[10][1]), "v"(xf[10][3]), "v"(vW2[3]), "a"(wq[10][2]), "v"(xf[10][4]) : "memory"); SB();
  asm volatile("v_mfma_f32_16x16x32_bf16 %0, %4, %5, %0\n\tv_mfma_f32_16x16x32_bf16 %1, %6, %5, %1\n\tds_read_b128 %2, %7 offset:14336\n\tv_mfma_f32_16x16x32_bf16 %3, %8, %5, %3"
      : "+v"(ak[5]), "+v"(av[5]), "=&v"(xf[11][7]), "+v"(aq[4]) : "a"(wq[10][1]), "v"(xf[10][5]), "a"(wq[10][2]), "v"(xr1), "a"(wq[10][0]) : "memory"); SB();
  WAITL(8);
  SB();
  asm volatile("v_mfma_f32_16x16x32_bf16 %0, %3, %4, %0\n\tds_read_b128 %1, %5 offset:16384\n\tv_mfma_f32_16x16x32_bf16 %2, %6, %4, %2\n\ts_waitcnt vmcnt(11)"
      : "+v"(ak[6]), "=&v"(xf[11][8]), "+v"(av[6]) : "a"(wq[10][1]), "v"(xf[10][6]), "v"(xr1), "a"(wq[10][2]) : "memory"); SB();
  BAR0();
  SB();
  asm volatile("v_mfma_f32_16x16x32_bf16 %0, %9, %10, %0\n\tds_read_b128 %1, %11 offset:18432\n\tds_read_b128 %2, %12 offset:18432\n\tv_mfma_f32_16x16x32_bf16 %3, %13, %14, %3\n\tv_mfma_f32_16x16x32_bf16 %4, %15, %14, %4\n\tv_mfma_f32_16x16x32_bf16 %5, %9, %14, %5\n\tv_mfma_f32_16x16x32_bf16 %6, %13, %16, %6\n\tv_mfma_f32_16x16x32_bf16 %7, %15, %16, %7\n\tv_mfma_f32_16x16x32_bf16 %8, %9, %16, %8"
      : "+v"(aq[5]), "=&v"(xf[12][0]), "=&v"(xf[13][0]), "+v"(ak[7]), "+v"(av[7]), "+v"(aq[6]), "+v"(ak[8]), "+v"(av[8]), "+v"(aq[7]) : "a"(wq[10][0]), "v"(xf[10][6]), "v"(xr0), "v"(xr1), "a"(wq[10][1]), "v"(xf[10][7]), "a"(wq[10][2]), "v"(xf[10][8]) : "memory"); SB();
  STAMP(13);
  WAITVB(20);
  WAITL(0);
  SB();
  asm volatile("v_mfma_f32_16x16x32_bf16 %0, %4, %5, %0\n\tds_read_b128 %1, %6 offset:20480\n\tds_read_b128 %2, %7 offset:20480\n\tv_mfma_f32_16x16x32_bf16 %3, %8, %5, %3"
      : "+v"(ak[0]), "=&v"(xf[12][1]), "=&v"(xf[13][1]), "+v"(av[0]) : "a"(wq[11][1]), "v"(xf[11][0]), "v"(xr0), "v"(xr1), "a"(wq[11][2]) : "memory"); SB();
  WAITVB(15);
  SB();
  asm volatile("v_mfma_f32_16x16x32_bf16 %0, %2, %3, %0\n\tv_mfma_f32_16x16x32_bf16 %1, %4, %3, %1"
      : "+v"(ak[0]), "+v"(av[0]) : "a"(wq[12][1]), "v"(xf[12][0]), "a"(wq[12][2])); SB();
  WAITVB(9);
  SB();
  asm volatile("v_mfma_f32_16x16x32_bf16 %0, %2, %3, %0\n\tv_mfma_f32_16x16x32_bf16 %1, %4, %3, %1"
      : "+v"(ak[0]), "+v"(av[0]) : "a"(wq[13][1]), "v"(xf[13][0]), "a"(wq[13][2])); SB();
  WAITL(0);
  SB();
  asm volatile("v_mfma_f32_16x16x32_bf16 %0, %9, %10, %0\n\tds_read_b128 %1, %11 offset:22528\n\tds_read_b128 %2, %12 offset:22528\n\tv_mfma_f32_16x16x32_bf16 %3, %13, %10, %3\n\tv_mfma_f32_16x16x32_bf16 %4, %14, %10, %4\n\tv_mfma_f32_16x16x32_bf16 %0, %15, %16, %0\n\tv_mfma_f32_16x16x32_bf16 %3, %17, %16, %3\n\tv_cvt_pk_bf16_f32 %5, %18, %19\n\tv_cvt_pk_bf16_f32 %6, %20, %21\n\tv_mfma_f32_16x16x32_bf16 %4, %22, %16, %4\n\tv_cvt_pk_bf16_f32 %7, %23, %24\n\tv_cvt_pk_bf16_f32 %8, %25, %26"
      : "+v"(ak[1]), "=&v"(xf[12][2]), "=&v"(xf[13][2]), "+v"(av[1]), "+v"(aq[0]), "=&v"(sg[0][0]), "=&v"(sg[0][1]), "=&v"(sg[1][0]), "=&v"(sg[1][1]) : "a"(wq[11][1]), "v"(xf[11][1]), "v"(xr0), "v"(xr1), "a"(wq[11][2]), "a"(wq[11][0]), "a"(wq[12][1]), "v"(xf[12][1]), "a"(wq[12][2]), "v"(ak[0][0]), "v"(ak[0][1]), "v"(ak[0][2]), "v"(ak[0][3]), "a"(wq[12][0]), "v"(av[0][0]), "v"(av[0][1]), "v"(av[0][2]), "v"(av[0][3]) : "memory"); SB();
  asm volatile("v_mfma_f32_16x16x32_bf16 %0, %3, %4, %0\n\tv_mfma_f32_16x16x32_bf16 %1, %5, %4, %1\n\tv_mfma_f32_16x16x32_bf16 %2, %6, %4, %2"
      : "+v"(ak[1]), "+v"(av[1]), "+v"(aq[0]) : "a"(wq[13][1]), "v"(xf[13][1]), "a"(wq[13][2]), "a"(wq[13][0])); SB();
  WAITL(0);
  SB();
  asm volatile("v_mfma_f32_16x16x32_bf16 %0, %7, %8, %0\n\tds_read_b128 %1, %9 offset:24576\n\tds_read_b128 %2, %10 offset:24576\n\tv_mfma_f32_16x16x32_bf16 %3, %11, %8, %3\n\tds_write_b64 %12, %13 offset:18432\n\tv_mfma_f32_16x16x32_bf16 %4, %14, %8, %4\n\tds_write_b64 %15, %16 offset:0\n\tv_mfma_f32_16x16x32_bf16 %0, %17, %18, %0\n\tv_mfma_f32_16x16x32_bf16 %3, %19, %18, %3\n\tv_cvt_pk_bf16_f32 %5, %20, %21\n\tv_cvt_pk_bf16_f32 %6, %22, %23\n\tv_mfma_f32_16x16x32_bf16 %4, %24, %18, %4"
      : "+v"(ak[2]), "=&v"(xf[12][3]), "=&v"(xf[13][3]), "+v"(av[2]), "+v"(aq[1]), "=&v"(sg[3][0]), "=&v"(sg[3][1]) : "a"(wq[11][1]), "v"(xf[11][2]), "v"(xr0), "v"(xr1), "a"(wq[11][2]), "v"(sk0), "v"(sg[0]), "a"(wq[11][0]), "v"(sv), "v"(sg[1]), "a"(wq[12][1]), "v"(xf[12][2]), "a"(wq[12][2]), "v"(ak[1][0]), "v"(ak[1][1]), "v"(ak[1][2]), "v"(ak[1][3]), "a"(wq[12][0]) : "memory"); SB();
  asm volatile("v_cvt_pk_bf16_f32 %0, %7, %8\n\tv_cvt_pk_bf16_f32 %1, %9, %10\n\tv_mfma_f32_16x16x32_bf16 %2, %11, %12, %2\n\tv_cvt_pk_bf16_f32 %3, %13, %14\n\tv_cvt_pk_bf16_f32 %4, %15, %16\n\tv_mfma_f32_16x16x32_bf16 %5, %17, %12, %5\n\tv_mfma_f32_16x16x32_bf16 %6, %18, %12, %6"
      : "=&v"(sg[4][0]), "=&v"(sg[4][1]), "+v"(ak[2]), "=&v"(sg[5][0]), "=&v"(sg[5][1]), "+v"(av[2]), "+v"(aq[1]) : "v"(av[1][0]), "v"(av[1][1]), "v"(av[1][2]), "v"(av[1][3]), "a"(wq[13][1]), "v"(xf[13][2]), "v"(aq[0][0]), "v"(aq[0][1]), "v"(aq[0][2]), "v"(aq[0][3]), "a"(wq[13][2]), "a"(wq[13][0])); SB();
  WAITL(2);
  SB();
  asm volatile("v_mfma_f32_16x16x32_bf16 %0, %7, %8, %0\n\tds_read_b128 %1, %9 offset:26624\n\tds_read_b128 %2, %10 offset:26624\n\tv_mfma_f32_16x16x32_bf16 %3, %11, %8, %3\n\tds_write_b64 %12, %13 offset:18944\n\tv_mfma_f32_16x16x32_bf16 %4, %14, %8, %4\n\tds_write_b64 %15, %16 offset:256\n\tv_mfma_f32_16x16x32_bf16 %0, %17, %18, %0\n\tds_write_b64 %19, %20 offset:36864\n\tv_mfma_f32_16x16x32_bf16 %3, %21, %18, %3\n\tv_cvt_pk_bf16_f32 %5, %22, %23\n\tv_cvt_pk_bf16_f32 %6, %24, %25\n\tv_mfma_f32_16x16x32_bf16 %4, %26, %18, %4"
      : "+v"(ak[3]), "=&v"(xf[12][4]), "=&v"(xf[13][4]), "+v"(av[3]), "+v"(aq[2]), "=&v"(sg[6][0]), "=&v"(sg[6][1]) : "a"(wq[11][1]), "v"(xf[11][3]), "v"(xr0), "v"(xr1), "a"(wq[11][2]), "v"(sk1), "v"(sg[3]), "a"(wq[11][0]), "v"(sv), "v"(sg[4]), "a"(wq[12][1]), "v"(xf[12][3]), "v"(sk0), "v"(sg[5]), "a"(wq[12][2]), "v"(ak[2][0]), "v"(ak[2][1]), "v"(ak[2][2]), "v"(ak[2][3]), "a"(wq[12][0]) : "memory"); SB();
  asm volatile("v_cvt_pk_bf16_f32 %0, %7, %8\n\tv_cvt_pk_bf16_f32 %1, %9, %10\n\tv_mfma_f32_16x16x32_bf16 %2, %11, %12, %2\n\tv_cvt_pk_bf16_f32 %3, %13, %14\n\tv_cvt_pk_bf16_f32 %4, %15, %16\n\tv_mfma_f32_16x16x32_bf16 %5, %17, %12, %5\n\tv_mfma_f32_16x16x32_bf16 %6, %18, %12, %6"
      : "=&v"(sg[7][0]), "=&v"(sg[7][1]), "+v"(ak[3]), "=&v"(sg[8][0]), "=&v"(sg[8][1]), "+v"(av[3]), "+v"(aq[2]) : "v"(av[2][0]), "v"(av[2][1]), "v"(av[2][2]), "v"(av[2][3]), "a"(wq[13][1]), "v"(xf[13][3]), "v"(aq[1][0]), "v"(aq[1][1]), "v"(aq[1][2]), "v"(aq[1][3]), "a"(wq[13][2]), "a"(wq[13][0])); SB();
  WAITL(3);
  SB();
  asm volatile("v_mfma_f32_16x16x32_bf16 %0, %7, %8, %0\n\tds_read_b128 %1, %9 offset:28672\n\tds_read_b128 %2, %10 offset:28672\n\tv_mfma_f32_16x16x32_bf16 %3, %11, %8, %3\n\tds_write_b64 %12, %13 offset:19456\n\tv_mfma_f32_16x16x32_bf16 %4, %14, %8, %4\n\tds_write_b64 %15, %16 offset:512\n\tv_mfma_f32_16x16x32_bf16 %0, %17, %18, %0\n\tds_write_b64 %19, %20 offset:37376\n\tv_mfma_f32_16x16x32_bf16 %3, %21, %18, %3\n\tv_cvt_pk_bf16_f32 %5, %22, %23\n\tv_cvt_pk_bf16_f32 %6, %24, %25\n\tv_mfma_f32_16x16x32_bf16 %4, %26, %18, %4"
      : "+v"(ak[4]), "=&v"(xf[12][5]), "=&v"(xf[13][5]), "+v"(av[4]), "+v"(aq[3]), "=&v"(sg[9][0]), "=&v"(sg[9][1]) : "a"(wq[11][1]), "v"(xf[11][4]), "v"(xr0), "v"(xr1), "a"(wq[11][2]), "v"(sk0), "v"(sg[6]), "a"(wq[11][0]), "v"(sv), "v"(sg[7]), "a"(wq[12][1]), "v"(xf[12][4]), "v"(sk1), "v"(sg[8]), "a"(wq[12][2]), "v"(ak[3][0]), "v"(ak[3][1]), "v"(ak[3][2]), "v"(ak[3][3]), "a"(wq[12][0]) : "memory"); SB();
  asm volatile("v_cvt_pk_bf16_f32 %0, %7, %8\n\tv_cvt_pk_bf16_f32 %1, %9, %10\n\tv_mfma_f32_16x16x32_bf16 %2, %11, %12, %2\n\tv_cvt_pk_bf16_f32 %3, %13, %14\n\tv_cvt_pk_bf16_f32 %4, %15, %16\n\tv_mfma_f32_16x16x32_bf16 %5, %17, %12, %5\n\ts_add_u32 m0, %18, 0\n\ts_nop 0\n\tbuffer_load_dwordx4 %19, %20, %21 offen offset:0  lds\n\tv_mfma_f32_16x16x32_bf16 %6, %22, %12, %6"
      : "=&v"(sg[10][0]), "=&v"(sg[10][1]), "+v"(ak[4]), "=&v"(sg[11][0]), "=&v"(sg[11][1]), "+v"(av[4]), "+v"(aq[3]) : "v"(av[3][0]), "v"(av[3][1]), "v"(av[3][2]), "v"(av[3][3]), "a"(wq[13][1]), "v"(xf[13][4]), "v"(aq[2][0]), "v"(aq[2][1]), "v"(aq[2][2]), "v"(aq[2][3]), "a"(wq[13][2]), "s"(ldsO), "v"(vW2[0]), "s"(rs2), "s"(sw[2]), "a"(wq[13][0]) : "memory", "scc"); SB();
  WAITL(3);
  SB();
  asm volatile("v_mfma_f32_16x16x32_bf16 %0, %6, %7, %0\n\tds_read_b128 %1, %8 offset:30720\n\tds_read_b128 %2, %9 offset:30720\n\tv_mfma_f32_16x16x32_bf16 %3, %10, %7, %3\n\tbuffer_load_dwordx4 %11, %12, %13 offen offset:1024  lds\n\tds_write_b64 %14, %15 offset:19968\n\tv_mfma_f32_16x16x32_bf16 %4, %16, %7, %4\n\tds_write_b64 %17, %18 offset:768\n\tv_mfma_f32_16x16x32_bf16 %0, %19, %20, %0\n\tds_write_b64 %21, %22 offset:37888\n\tv_mfma_f32_16x16x32_bf16 %3, %23, %20, %3\n\tbuffer_load_dwordx4 %24, %12, %13 offen offset:2048  lds\n\tv_cvt_pk_bf16_f32 %5, %25, %26"
      : "+v"(ak[5]), "=&v"(xf[12][6]), "=&v"(xf[13][6]), "+v"(av[5]), "+v"(aq[4]), "=&v"(sg[12][0]) : "a"(wq[11][1]), "v"(xf[11][5]), "v"(xr0), "v"(xr1), "a"(wq[11][2]), "v"(vW2[1]), "s"(rs2), "s"(sw[2]), "v"(sk1), "v"(sg[9]), "a"(wq[11][0]), "v"(sv), "v"(sg[10]), "a"(wq[12][1]), "v"(xf[12][5]), "v"(sk0), "v"(sg[11]), "a"(wq[12][2]), "v"(vW2[2]), "v"(ak[4][0]), "v"(ak[4][1]) : "memory"); SB();
  asm volatile("v_cvt_pk_bf16_f32 %0, %8, %9\n\tv_mfma_f32_16x16x32_bf16 %1, %10, %11, %1\n\tv_cvt_pk_bf16_f32 %2, %12, %13\n\tv_cvt_pk_bf16_f32 %3, %14, %15\n\tv_mfma_f32_16x16x32_bf16 %4, %16, %17, %4\n\tv_cvt_pk_bf16_f32 %5, %18, %19\n\tv_cvt_pk_bf16_f32 %6, %20, %21\n\tv_mfma_f32_16x16x32_bf16 %7, %22, %17, %7\n\tbuffer_load_dwordx4 %23, %24, %25 offen offset:3072  lds\n\tv_mfma_f32_16x16x32_bf16 %1, %26, %17, %1"
      : "=&v"(sg[12][1]), "+v"(aq[4]), "=&v"(sg[13][0]), "=&v"(sg[13][1]), "+v"(ak[5]), "=&v"(sg[14][0]), "=&v"(sg[14][1]), "+v"(av[5]) : "v"(ak[4][2]), "v"(ak[4][3]), "a"(wq[12][0]), "v"(xf[12][5]), "v"(av[4][0]), "v"(av[4][1]), "v"(av[4][2]), "v"(av[4][3]), "a"(wq[13][1]), "v"(xf[13][5]), "v"(aq[3][0]), "v"(aq[3][1]), "v"(aq[3][2]), "v"(aq[3][3]), "a"(wq[13][2]), "v"(vW2[3]), "s"(rs2), "s"(sw[2]), "a"(wq[13][0]) : "memory"); SB();
  WAITL(3);
  SB();
  asm volatile("v_mfma_f32_16x16x32_bf16 %0, %5, %6, %0\n\tds_read_b128 %1, %7 offset:32768\n\tds_read_b128 %2, %8 offset:32768\n\tv_mfma_f32_16x16x32_bf16 %3, %9, %6, %3\n\ts_add_u32 m0, %10, 4096\n\ts_nop 0\n\tbuffer_load_dwordx4 %11, %12, %13 offen offset:0  lds\n\tds_write_b64 %14, %15 offset:20480\n\tv_mfma_f32_16x16x32_bf16 %4, %16, %6, %4\n\tds_write_b64 %17, %18 offset:1024\n\tv_mfma_f32_16x16x32_bf16 %0, %19, %20, %0\n\tds_write_b64 %21, %22 offset:38400\n\tv_mfma_f32_16x16x32_bf16 %3, %23, %20, %3\n\tbuffer_load_dwordx4 %24, %12, %13 offen offset:1024  lds"
      : "+v"(ak[6]), "=&v"(xf[12][7]), "=&v"(xf[13][7]), "+v"(av[6]), "+v"(aq[5]) : "a"(wq[11][1]), "v"(xf[11][6]), "v"(xr0), "v"(xr1), "a"(wq[11][2]), "s"(ldsO), "v"(vW2[0]), "s"(rs2), "s"(sw[3]), "v"(sk0), "v"(sg[12]), "a"(wq[11][0]), "v"(sv), "v"(sg[13]), "a"(wq[12][1]), "v"(xf[12][6]), "v"(sk1), "v"(sg[14]), "a"(wq[12][2]), "v"(vW2[1]) : "memory", "scc"); SB();
  asm volatile("v_cvt_pk_bf16_f32 %0, %9, %10\n\tv_cvt_pk_bf16_f32 %1, %11, %12\n\tv_mfma_f32_16x16x32_bf16 %2, %13, %14, %2\n\tv_cvt_pk_bf16_f32 %3, %15, %16\n\tv_cvt_pk_bf16_f32 %4, %17, %18\n\tv_mfma_f32_16x16x32_bf16 %5, %19, %20, %5\n\tv_cvt_pk_bf16_f32 %6, %21, %22\n\tv_cvt_pk_bf16_f32 %7, %23, %24\n\tv_mfma_f32_16x16x32_bf16 %8, %25, %20, %8"
      : "=&v"(sg[15][0]), "=&v"(sg[15][1]), "+v"(aq[5]), "=&v"(sg[16][0]), "=&v"(sg[16][1]), "+v"(ak[6]), "=&v"(sg[17][0]), "=&v"(sg[17][1]), "+v"(av[6]) : "v"(ak[5][0]), "v"(ak[5][1]), "v"(ak[5][2]), "v"(ak[5][3]), "a"(wq[12][0]), "v"(xf[12][6]), "v"(av[5][0]), "v"(av[5][1]), "v"(av[5][2]), "v"(av[5][3]), "a"(wq[13][1]), "v"(xf[13][6]), "v"(aq[4][0]), "v"(aq[4][1]), "v"(aq[4][2]), "v"(aq[4][3]), "a"(wq[13][2])); SB();
  asm volatile("buffer_load_dwordx4 %1, %2, %3 offen offset:2048  lds\n\tv_mfma_f32_16x16x32_bf16 %0, %4, %5, %0"
      : "+v"(aq[5]) : "v"(vW2[2]), "s"(rs2), "s"(sw[3]), "a"(wq[13][0]), "v"(xf[13][6]) : "memory"); SB();
  WAITL(3);
  SB();
  asm volatile("v_mfma_f32_16x16x32_bf16 %0, %5, %6, %0\n\tds_read_b128 %1, %7 offset:34816\n\tds_read_b128 %2, %8 offset:34816\n\tv_mfma_f32_16x16x32_bf16 %3, %9, %6, %3\n\tbuffer_load_dwordx4 %10, %11, %12 offen offset:3072  lds\n\tds_write_b64 %13, %14 offset:20992\n\tv_mfma_f32_16x16x32_bf16 %4, %15, %6, %4\n\tds_write_b64 %16, %17 offset:1280\n\tv_mfma_f32_16x16x32_bf16 %0, %18, %19, %0\n\tds_write_b64 %20, %21 offset:38912\n\tv_mfma_f32_16x16x32_bf16 %3, %22, %19, %3\n\ts_waitcnt vmcnt(12)"
      : "+v"(ak[7]), "=&v"(xf[12][8]), "=&v"(xf[13][8]), "+v"(av[7]), "+v"(aq[6]) : "a"(wq[11][1]), "v"(xf[11][7]), "v"(xr0), "v"(xr1), "a"(wq[11][2]), "v"(vW2[3]), "s"(rs2), "s"(sw[3]), "v"(sk1), "v"(sg[15]), "a"(wq[11][0]), "v"(sv), "v"(sg[16]), "a"(wq[12][1]), "v"(xf[12][7]), "v"(sk0), "v"(sg[17]), "a"(wq[12][2]) : "memory"); SB();
  fw2[0][0] = tr8u(tS[0] + 18432, 512);
  asm volatile("v_cvt_pk_bf16_f32 %0, %9, %10\n\tv_cvt_pk_bf16_f32 %1, %11, %12\n\tv_mfma_f32_16x16x32_bf16 %2, %13, %14, %2\n\tv_cvt_pk_bf16_f32 %3, %15, %16\n\tv_cvt_pk_bf16_f32 %4, %17, %18\n\tv_mfma_f32_16x16x32_bf16 %5, %19, %20, %5\n\tv_cvt_pk_bf16_f32 %6, %21, %22\n\tv_cvt_pk_bf16_f32 %7, %23, %24\n\tv_mfma_f32_16x16x32_bf16 %8, %25, %20, %8"
      : "=&v"(sg[18][0]), "=&v"(sg[18][1]), "+v"(aq[6]), "=&v"(sg[19][0]), "=&v"(sg[19][1]), "+v"(ak[7]), "=&v"(sg[20][0]), "=&v"(sg[20][1]), "+v"(av[7]) : "v"(ak[6][0]), "v"(ak[6][1]), "v"(ak[6][2]), "v"(ak[6][3]), "a"(wq[12][0]), "v"(xf[12][7]), "v"(av[6][0]), "v"(av[6][1]), "v"(av[6][2]), "v"(av[6][3]), "a"(wq[13][1]), "v"(xf[13][7]), "v"(aq[5][0]), "v"(aq[5][1]), "v"(aq[5][2]), "v"(aq[5][3]), "a"(wq[13][2])); SB();
  fw2[0][1] = tr8u(tS[1] + 18432, 512);
  SB();
  asm volatile("v_mfma_f32_16x16x32_bf16 %0, %1, %2, %0"
      : "+v"(aq[6]) : "a"(wq[13][0]), "v"(xf[13][7])); SB();
  WAITL(7);
  SB();
  asm volatile("v_mfma_f32_16x16x32_bf16 %0, %2, %3, %0\n\tds_write_b64 %4, %5 offset:21504\n\tv_mfma_f32_16x16x32_bf16 %1, %6, %3, %1"
      : "+v"(ak[8]), "+v"(av[8]) : "a"(wq[11][1]), "v"(xf[11][8]), "v"(sk0), "v"(sg[18]), "a"(wq[11][2]) : "memory"); SB();
  fw2[0][2] = tr8u(tS[2] + 18432, 512);
  asm volatile("ds_write_b64 %3, %4 offset:1536\n\tv_mfma_f32_16x16x32_bf16 %0, %5, %6, %0\n\tds_write_b64 %7, %8 offset:39424\n\tv_mfma_f32_16x16x32_bf16 %1, %9, %10, %1\n\tv_mfma_f32_16x16x32_bf16 %2, %11, %10, %2"
      : "+v"(aq[7]), "+v"(ak[8]), "+v"(av[8]) : "v"(sv), "v"(sg[19]), "a"(wq[11][0]), "v"(xf[11][8]), "v"(sk1), "v"(sg[20]), "a"(wq[12][1]), "v"(xf[12][8]), "a"(wq[12][2]) : "memory"); SB();
  fw2[0][3] = tr8u(tS[3] + 18432, 512);
  asm volatile("v_cvt_pk_bf16_f32 %0, %9, %10\n\tv_cvt_pk_bf16_f32 %1, %11, %12\n\tv_mfma_f32_16x16x32_bf16 %2, %13, %14, %2\n\tv_cvt_pk_bf16_f32 %3, %15, %16\n\tv_cvt_pk_bf16_f32 %4, %17, %18\n\tv_mfma_f32_16x16x32_bf16 %5, %19, %20, %5\n\tv_cvt_pk_bf16_f32 %6, %21, %22\n\tv_cvt_pk_bf16_f32 %7, %23, %24\n\tv_mfma_f32_16x16x32_bf16 %8, %25, %20, %8"
      : "=&v"(sg[21][0]), "=&v"(sg[21][1]), "+v"(aq[7]), "=&v"(sg[22][0]), "=&v"(sg[22][1]), "+v"(ak[8]), "=&v"(sg[23][0]), "=&v"(sg[23][1]), "+v"(av[8]) : "v"(ak[7][0]), "v"(ak[7][1]), "v"(ak[7][2]), "v"(ak[7][3]), "a"(wq[12][0]), "v"(xf[12][8]), "v"(av[7][0]), "v"(av[7][1]), "v"(av[7][2]), "v"(av[7][3]), "a"(wq[13][1]), "v"(xf[13][8]), "v"(aq[6][0]), "v"(aq[6][1]), "v"(aq[6][2]), "v"(aq[6][3]), "a"(wq[13][2])); SB();
  WAITL(0);
  asm volatile("s_add_u32 m0, %5, 18432\n\ts_nop 0\n\tbuffer_load_dwordx4 %6, %7, %8 offen offset:0  lds\n\tv_mfma_f32_16x16x32_bf16 %0, %9, %10, %0\n\ts_nop 5\n\tds_write_b64 %11, %12 offset:22016\n\tds_write_b64 %13, %14 offset:1792\n\tds_write_b64 %15, %16 offset:39936\n\tv_cvt_pk_bf16_f32 %1, %17, %18\n\tv_cvt_pk_bf16_f32 %2, %19, %20\n\tv_cvt_pk_bf16_f32 %3, %21, %22\n\tv_cvt_pk_bf16_f32 %4, %23, %24"
      : "+v"(aq[7]), "=&v"(sg[24][0]), "=&v"(sg[24][1]), "=&v"(sg[25][0]), "=&v"(sg[25][1]) : "s"(ldsS), "v"(vW2[0]), "s"(rs2), "s"(sw[4]), "a"(wq[13][0]), "v"(xf[13][8]), "v"(sk1), "v"(sg[21]), "v"(sv), "v"(sg[22]), "v"(sk0), "v"(sg[23]), "v"(ak[8][0]), "v"(ak[8][1]), "v"(ak[8][2]), "v"(ak[8][3]), "v"(av[8][0]), "v"(av[8][1]), "v"(av[8][2]), "v"(av[8][3]) : "memory", "scc"); SB();
  asm volatile("v_cvt_pk_bf16_f32 %0, %2, %3\n\tv_cvt_pk_bf16_f32 %1, %4, %5\n\tds_write_b64 %6, %7 offset:22528\n\tds_write_b64 %8, %9 offset:2048"
      : "=&v"(sg[26][0]), "=&v"(sg[26][1]) : "v"(aq[7][0]), "v"(aq[7][1]), "v"(aq[7][2]), "v"(aq[7][3]), "v"(sk0), "v"(sg[24]), "v"(sv), "v"(sg[25]) : "memory"); SB();
  asm volatile("ds_write_b64 %0, %1 offset:40448"
      :  : "v"(sk1), "v"(sg[26]) : "memory"); SB();
  WAITV1(23, fq);
  if (__ballot(fq != nonce) != 0ull) poll1<4>(rsW, vFQ, nonce);
  BAR();
  STAMP(14);
  unsigned fu; u32x4 qf[2][2]; bf16x8 kf[2][2][2], vf[2][2][2];
  qf[0][0] = *(LDS_AS const u32x4*)(rk0 + SQ + 512 + 0); kf[0][0][0] = *(LDS_AS const bf16x8*)(rk0 + SK + 0); kf[0][0][1] = *(LDS_AS const bf16x8*)(rk0 + SK + 1024);
  qf[0][1] = *(LDS_AS const u32x4*)(rk0 + SQ + 512 + 9216); kf[0][1][0] = *(LDS_AS const bf16x8*)(rk0 + SK + 9216); kf[0][1][1] = *(LDS_AS const bf16x8*)(rk0 + SK + 10240);
  qf[1][0] = *(LDS_AS const u32x4*)(rk1 + SQ + 512 + 0); kf[1][0][0] = *(LDS_AS const bf16x8*)(rk1 + SK + 0); kf[1][0][1] = *(LDS_AS const bf16x8*)(rk1 + SK + 1024);
  qf[1][1] = *(LDS_AS const u32x4*)(rk1 + SQ + 512 + 9216); kf[1][1][0] = *(LDS_AS const bf16x8*)(rk1 + SK + 9216); kf[1][1][1] = *(LDS_AS const bf16x8*)(rk1 + SK + 10240);
  SB();
  asm volatile("buffer_load_dwordx4 %0, %1, %2 offen offset:1024  lds\n\tbuffer_load_dwordx4 %3, %1, %2 offen offset:2048  lds"
      :  : "v"(vW2[1]), "s"(rs2), "s"(sw[4]), "v"(vW2[2]) : "memory"); SB();
  f32x4 sc[2][2][2];
  asm volatile("v_mfma_f32_16x16x32_bf16 %0, %2, %3, 0\n\tv_mfma_f32_16x16x32_bf16 %1, %4, %3, 0"
      : "=&v"(sc[0][0][0]), "=&v"(sc[0][0][1]) : "v"(kf[0][0][0]), "v"(qf[0][0]), "v"(kf[0][0][1])); SB();
  vf[0][0][0] = tr8u(rv + 0, 512);
  vf[0][0][1] = tr8u(rv + 4608, 512);
  SB();
  asm volatile("v_mfma_f32_16x16x32_bf16 %0, %2, %3, 0\n\tv_mfma_f32_16x16x32_bf16 %1, %4, %3, 0"
      : "=&v"(sc[0][1][0]), "=&v"(sc[0][1][1]) : "v"(kf[0][1][0]), "v"(qf[0][1]), "v"(kf[0][1][1])); SB();
  vf[0][1][0] = tr8u(rv + 9216, 512);
  vf[0][1][1] = tr8u(rv + 13824, 512);
  SB();
  asm volatile("v_mfma_f32_16x16x32_bf16 %0, %2, %3, 0\n\tv_mfma_f32_16x16x32_bf16 %1, %4, %3, 0"
      : "=&v"(sc[1][0][0]), "=&v"(sc[1][0][1]) : "v"(kf[1][0][0]), "v"(qf[1][0]), "v"(kf[1][0][1])); SB();
  vf[1][0][0] = tr8u(rv + 2304, 512);
  vf[1][0][1] = tr8u(rv + 6912, 512);
  SB();
  asm volatile("v_mfma_f32_16x16x32_bf16 %0, %2, %3, 0\n\tv_mfma_f32_16x16x32_bf16 %1, %4, %3, 0"
      : "=&v"(sc[1][1][0]), "=&v"(sc[1][1][1]) : "v"(kf[1][1][0]), "v"(qf[1][1]), "v"(kf[1][1][1])); SB();
  vf[1][1][0] = tr8u(rv + 11520, 512);
  vf[1][1][1] = tr8u(rv + 16128, 512);
  SB();
  SB();
  asm volatile("buffer_load_dwordx4 %0, %1, %2 offen offset:3072  lds\n\ts_add_u32 m0, %3, 0\n\ts_nop 0\n\tbuffer_load_dwordx4 %4, %1, %5 offen offset:0  lds"
      :  : "v"(vW2[3]), "s"(rs2), "s"(sw[4]), "s"(ldsS), "v"(vW2[0]), "s"(sw[5]) : "memory", "scc"); SB();
  f32x2_t lg[2][2][2], mx[2], rs[2]; u32x4 pf[2][2]; f32x4 oo[2][2][2];
  const f32x2_t scale = {0.18898223650461362f, 0.18898223650461362f};
#define SEL(p, h, i) __uint_as_float((m1[i] & __float_as_uint(sc[p][h][1][i])) | (~m1[i] & __float_as_uint(sc[p][h][0][i])))
  lg[0][0][0] = rbf2(__builtin_elementwise_fma(rbf2((f32x2_t){SEL(0, 0, 0), SEL(0, 0, 1)}), scale, (f32x2_t){cm[0], cm[1]}));
  lg[0][0][1] = rbf2(__builtin_elementwise_fma(rbf2((f32x2_t){SEL(0, 0, 2), SEL(0, 0, 3)}), scale, (f32x2_t){cm[2], cm[3]}));
  OPV(lg[0][0][0]); OPV(lg[0][0][1]);
  SB();
  asm volatile("buffer_load_dwordx4 %0, %1, %2 offen offset:1024  lds\n\tbuffer_load_dwordx4 %3, %1, %2 offen offset:2048  lds"
      :  : "v"(vW2[1]), "s"(rs2), "s"(sw[5]), "v"(vW2[2]) : "memory"); SB();
  lg[0][1][0] = rbf2(__builtin_elementwise_fma(rbf2((f32x2_t){SEL(0, 1, 0), SEL(0, 1, 1)}), scale, (f32x2_t){cm[0], cm[1]}));
  lg[0][1][1] = rbf2(__builtin_elementwise_fma(rbf2((f32x2_t){SEL(0, 1, 2), SEL(0, 1, 3)}), scale, (f32x2_t){cm[2], cm[3]}));
  OPV(lg[0][1][0]); OPV(lg[0][1][1]);
  SB();
  asm volatile("buffer_load_dwordx4 %0, %1, %2 offen offset:3072  lds\n\ts_waitcnt vmcnt(16)"
      :  : "v"(vW2[3]), "s"(rs2), "s"(sw[5]) : "memory"); SB();
  fw2[1][0] = tr8u(tS[0] + 34816, 512);
  SB();
  lg[1][0][0] = rbf2(__builtin_elementwise_fma(rbf2((f32x2_t){SEL(1, 0, 0), SEL(1, 0, 1)}), scale, (f32x2_t){cm[0], cm[1]}));
  lg[1][0][1] = rbf2(__builtin_elementwise_fma(rbf2((f32x2_t){SEL(1, 0, 2), SEL(1, 0, 3)}), scale, (f32x2_t){cm[2], cm[3]}));
  OPV(lg[1][0][0]); OPV(lg[1][0][1]);
  SB();
  fw2[1][1] = tr8u(tS[1] + 34816, 512);
  fw2[1][2] = tr8u(tS[2] + 34816, 512);
  SB();
  lg[1][1][0] = rbf2(__builtin_elementwise_fma(rbf2((f32x2_t){SEL(1, 1, 0), SEL(1, 1, 1)}), scale, (f32x2_t){cm[0], cm[1]}));
  lg[1][1][1] = rbf2(__builtin_elementwise_fma(rbf2((f32x2_t){SEL(1, 1, 2), SEL(1, 1, 3)}), scale, (f32x2_t){cm[2], cm[3]}));
  OPV(lg[1][1][0]); OPV(lg[1][1][1]);
  SB();
  fw2[1][3] = tr8u(tS[3] + 34816, 512);
  WAITL(0);
  asm volatile("s_add_u32 m0, %0, 34816\n\ts_nop 0\n\tbuffer_load_dwordx4 %1, %2, %3 offen offset:0  lds"
      :  : "s"(ldsS), "v"(vW2[0]), "s"(rs2), "s"(sw[6]) : "memory", "scc"); SB();
  #pragma unroll
  for (int p = 0; p < 2; ++p)
    mx[p] = xbcast2(xmax2(fmx(fmx(lg[p][0][0][0], lg[p][0][0][1]), fmx(lg[p][0][1][0], lg[p][0][1][1])), fmx(fmx(lg[p][1][0][0], lg[p][1][0][1]), fmx(lg[p][1][1][0], lg[p][1][1][1]))));
  OPV(mx[0]); OPV(mx[1]);
  SB();
  asm volatile("buffer_load_dwordx4 %0, %1, %2 offen offset:1024  lds\n\tbuffer_load_dwordx4 %3, %1, %2 offen offset:2048  lds\n\tbuffer_load_dwordx4 %4, %1, %2 offen offset:3072  lds\n\ts_waitcnt vmcnt(16)"
      :  : "v"(vW2[1]), "s"(rs2), "s"(sw[6]), "v"(vW2[2]), "v"(vW2[3]) : "memory"); SB();
  fw2[2][0] = tr8u(tO[0] + 0, 512);
  SB();
  #pragma unroll
  for (int j = 0; j < 2; ++j) {
    const f32x2_t z = (lg[0][0][j] - (f32x2_t){mx[0][0], mx[0][0]}) * (f32x2_t){1.442695041f, 1.442695041f};
    lg[0][0][j] = (f32x2_t){__builtin_amdgcn_exp2f(z[0]), __builtin_amdgcn_exp2f(z[1])}; OPV(lg[0][0][j]);
  }
  SB();
  fw2[2][1] = tr8u(tO[1] + 0, 512);
  fw2[2][2] = tr8u(tO[2] + 0, 512);
  SB();
  #pragma unroll
  for (int j = 0; j < 2; ++j) {
    const f32x2_t z = (lg[0][1][j] - (f32x2_t){mx[0][1], mx[0][1]}) * (f32x2_t){1.442695041f, 1.442695041f};
    lg[0][1][j] = (f32x2_t){__builtin_amdgcn_exp2f(z[0]), __builtin_amdgcn_exp2f(z[1])}; OPV(lg[0][1][j]);
  }
  SB();
  fw2[2][3] = tr8u(tO[3] + 0, 512);
  WAITL(0);
  asm volatile("s_add_u32 m0, %0, 0\n\ts_nop 0\n\tbuffer_load_dwordx4 %1, %2, %3 offen offset:0  lds"
      :  : "s"(ldsO), "v"(vW2[0]), "s"(rs2), "s"(sw[7]) : "memory", "scc"); SB();
  #pragma unroll
  for (int j = 0; j < 2; ++j) {
    const f32x2_t z = (lg[1][0][j] - (f32x2_t){mx[1][0], mx[1][0]}) * (f32x2_t){1.442695041f, 1.442695041f};
    lg[1][0][j] = (f32x2_t){__builtin_amdgcn_exp2f(z[0]), __builtin_amdgcn_exp2f(z[1])}; OPV(lg[1][0][j]);
  }
  SB();
  asm volatile("buffer_load_dwordx4 %0, %1, %2 offen offset:1024  lds\n\tbuffer_load_dwordx4 %3, %1, %2 offen offset:2048  lds"
      :  : "v"(vW2[1]), "s"(rs2), "s"(sw[7]), "v"(vW2[2]) : "memory"); SB();
  #pragma unroll
  for (int j = 0; j < 2; ++j) {
    const f32x2_t z = (lg[1][1][j] - (f32x2_t){mx[1][1], mx[1][1]}) * (f32x2_t){1.442695041f, 1.442695041f};
    lg[1][1][j] = (f32x2_t){__builtin_amdgcn_exp2f(z[0]), __builtin_amdgcn_exp2f(z[1])}; OPV(lg[1][1][j]);
  }
  SB();
  asm volatile("buffer_load_dwordx4 %0, %1, %2 offen offset:3072  lds\n\ts_waitcnt vmcnt(16)"
      :  : "v"(vW2[3]), "s"(rs2), "s"(sw[7]) : "memory"); SB();
  fw2[3][0] = tr8u(tO[0] + 4096, 512);
  SB();
  #pragma unroll
  for (int p = 0; p < 2; ++p) {
    const f32x2_t e0 = lg[p][0][0] + lg[p][0][1], e1 = lg[p][1][0] + lg[p][1][1];
    rs[p] = xbcast2(__builtin_amdgcn_rcpf(xsum2(e0[0] + e0[1], e1[0] + e1[1])));
  }
  OPV(rs[0]); OPV(rs[1]);
  SB();
  fw2[3][1] = tr8u(tO[1] + 4096, 512);
  fw2[3][2] = tr8u(tO[2] + 4096, 512);
  fw2[3][3] = tr8u(tO[3] + 4096, 512);
  WAITL(0);
  asm volatile("s_add_u32 m0, %0, 4096\n\ts_nop 0\n\tbuffer_load_dwordx4 %1, %2, %3 offen offset:0  lds"
      :  : "s"(ldsO), "v"(vW2[0]), "s"(rs2), "s"(sw[8]) : "memory", "scc"); SB();
  { const f32x2_t pa = lg[0][0][0] * (f32x2_t){rs[0][0], rs[0][0]}, pb = lg[0][0][1] * (f32x2_t){rs[0][0], rs[0][0]};
    const unsigned wa = pack2(pa[0], pa[1]), wb = pack2(pb[0], pb[1]);
    pf[0][0] = (u32x4){wa & ~mp[0], wb & ~mp[1], wa & mp[0], wb & mp[1]}; OPV(pf[0][0]); }
  { const f32x2_t pa = lg[0][1][0] * (f32x2_t){rs[0][1], rs[0][1]}, pb = lg[0][1][1] * (f32x2_t){rs[0][1], rs[0][1]};
    const unsigned wa = pack2(pa[0], pa[1]), wb = pack2(pb[0], pb[1]);
    pf[0][1] = (u32x4){wa & ~mp[0], wb & ~mp[1], wa & mp[0], wb & mp[1]}; OPV(pf[0][1]); }
  asm volatile("s_nop 1\n\tv_mfma_f32_16x16x32_bf16 %0, %4, %5, 0\n\tv_mfma_f32_16x16x32_bf16 %1, %6, %7, 0\n\tv_mfma_f32_16x16x32_bf16 %2, %8, %5, 0\n\tv_mfma_f32_16x16x32_bf16 %3, %9, %7, 0"
      : "=&v"(oo[0][0][0]), "=&v"(oo[0][1][0]), "=&v"(oo[0][0][1]), "=&v"(oo[0][1][1]) : "v"(vf[0][0][0]), "v"(pf[0][0]), "v"(vf[0][1][0]), "v"(pf[0][1]), "v"(vf[0][0][1]), "v"(vf[0][1][1])); SB();
  { const f32x2_t pa = lg[1][0][0] * (f32x2_t){rs[1][0], rs[1][0]}, pb = lg[1][0][1] * (f32x2_t){rs[1][0], rs[1][0]};
    const unsigned wa = pack2(pa[0], pa[1]), wb = pack2(pb[0], pb[1]);
    pf[1][0] = (u32x4){wa & ~mp[0], wb & ~mp[1], wa & mp[0], wb & mp[1]}; OPV(pf[1][0]); }
  { const f32x2_t pa = lg[1][1][0] * (f32x2_t){rs[1][1], rs[1][1]}, pb = lg[1][1][1] * (f32x2_t){rs[1][1], rs[1][1]};
    const unsigned wa = pack2(pa[0], pa[1]), wb = pack2(pb[0], pb[1]);
    pf[1][1] = (u32x4){wa & ~mp[0], wb & ~mp[1], wa & mp[0], wb & mp[1]}; OPV(pf[1][1]); }
  asm volatile("s_nop 1\n\tv_mfma_f32_16x16x32_bf16 %0, %4, %5, 0\n\tv_mfma_f32_16x16x32_bf16 %1, %6, %7, 0\n\tv_mfma_f32_16x16x32_bf16 %2, %8, %5, 0\n\tv_mfma_f32_16x16x32_bf16 %3, %9, %7, 0"
      : "=&v"(oo[1][0][0]), "=&v"(oo[1][1][0]), "=&v"(oo[1][0][1]), "=&v"(oo[1][1][1]) : "v"(vf[1][0][0]), "v"(pf[1][0]), "v"(vf[1][1][0]), "v"(pf[1][1]), "v"(vf[1][0][1]), "v"(vf[1][1][1])); SB();
  HS16(vS, sO, 0, ((u32x4){pack2(oo[0][0][0][0], oo[0][0][0][1]), pack2(oo[0][0][0][2], oo[0][0][0][3]), pack2(oo[0][1][0][0], oo[0][1][0][1]), pack2(oo[0][1][0][2], oo[0][1][0][3])})); SB();
  HS16H(0xffff, vS, sO, 128, ((u32x4){pack2(oo[0][0][1][0], oo[0][0][1][1]), pack2(oo[0][0][1][2], oo[0][0][1][3]), pack2(oo[0][1][1][0], oo[0][1][1][1]), pack2(oo[0][1][1][2], oo[0][1][1][3])})); SB();
  HS16(vS, sO, 1024, ((u32x4){pack2(oo[1][0][0][0], oo[1][0][0][1]), pack2(oo[1][0][0][2], oo[1][0][0][3]), pack2(oo[1][1][0][0], oo[1][1][0][1]), pack2(oo[1][1][0][2], oo[1][1][0][3])})); SB();
  HS16H(0xffff, vS, sO, 1152, ((u32x4){pack2(oo[1][0][1][0], oo[1][0][1][1]), pack2(oo[1][0][1][2], oo[1][0][1][3]), pack2(oo[1][1][1][0], oo[1][1][1][1]), pack2(oo[1][1][1][2], oo[1][1][1][3])})); SB();
  STAMP(15);
  asm volatile("buffer_load_dwordx4 %0, %1, %2 offen offset:1024  lds\n\tbuffer_load_dwordx4 %3, %1, %2 offen offset:2048  lds\n\tbuffer_load_dwordx4 %4, %1, %2 offen offset:3072  lds\n\ts_waitcnt vmcnt(20)"
      :  : "v"(vW2[1]), "s"(rs2), "s"(sw[8]), "v"(vW2[2]), "v"(vW2[3]) : "memory"); SB();
  fw2[4][0] = tr8u(tS[0] + 18432, 512);
  fw2[4][1] = tr8u(tS[1] + 18432, 512);
  fw2[4][2] = tr8u(tS[2] + 18432, 512);
  fw2[4][3] = tr8u(tS[3] + 18432, 512);
  WAITL(0);
  asm volatile("s_add_u32 m0, %0, 18432\n\ts_nop 0\n\tbuffer_load_dwordx4 %1, %2, %3 offen offset:0  lds\n\tbuffer_load_dwordx4 %4, %2, %3 offen offset:1024  lds\n\tbuffer_load_dwordx4 %5, %2, %3 offen offset:2048  lds\n\tbuffer_load_dwordx4 %6, %2, %3 offen offset:3072  lds\n\ts_waitcnt vmcnt(20)"
      :  : "s"(ldsS), "v"(vW2[0]), "s"(rs2), "s"(sw[9]), "v"(vW2[1]), "v"(vW2[2]), "v"(vW2[3]) : "memory", "scc"); SB();
  fw2[5][0] = tr8u(tS[0] + 0, 512);
  fw2[5][1] = tr8u(tS[1] + 0, 512);
  fw2[5][2] = tr8u(tS[2] + 0, 512);
  fw2[5][3] = tr8u(tS[3] + 0, 512);
  WAITL(0);
  asm volatile("s_add_u32 m0, %0, 0\n\ts_nop 0\n\tbuffer_load_dwordx4 %1, %2, %3 offen offset:0  lds\n\tbuffer_load_dwordx4 %4, %2, %3 offen offset:1024  lds\n\tbuffer_load_dwordx4 %5, %2, %3 offen offset:2048  lds\n\tbuffer_load_dwordx4 %6, %2, %3 offen offset:3072  lds\n\ts_waitcnt vmcnt(20)"
      :  : "s"(ldsS), "v"(vW2[0]), "s"(rs2), "s"(sw[10]), "v"(vW2[1]), "v"(vW2[2]), "v"(vW2[3]) : "memory", "scc"); SB();
  fw2[6][0] = tr8u(tS[0] + 34816, 512);
  fw2[6][1] = tr8u(tS[1] + 34816, 512);
  fw2[6][2] = tr8u(tS[2] + 34816, 512);
  fw2[6][3] = tr8u(tS[3] + 34816, 512);
  asm volatile("s_waitcnt vmcnt(16)"
      :  :  : "memory"); SB();
  fw2[7][0] = tr8u(tO[0] + 0, 512);
  fw2[7][1] = tr8u(tO[1] + 0, 512);
  fw2[7][2] = tr8u(tO[2] + 0, 512);
  fw2[7][3] = tr8u(tO[3] + 0, 512);
  asm volatile("s_waitcnt vmcnt(11)"
      :  :  : "memory"); SB();
  STFO(sFO);
  STAMP(24);
  asm volatile("s_waitcnt vmcnt(9)"
      :  :  : "memory"); SB();
  fw2[8][0] = tr8u(tO[0] + 4096, 512);
  fw2[8][1] = tr8u(tO[1] + 4096, 512);
  fw2[8][2] = tr8u(tO[2] + 4096, 512);
  fw2[8][3] = tr8u(tO[3] + 4096, 512);
  WAITL(14);
  asm volatile("s_add_u32 m0, %0, 34816\n\ts_nop 0\n\tbuffer_load_dwordx4 %1, %2, %3 offen offset:0  lds\n\tbuffer_load_dwordx4 %4, %2, %3 offen offset:1024  lds\n\tbuffer_load_dwordx4 %5, %2, %3 offen offset:2048  lds\n\tbuffer_load_dwordx4 %6, %2, %3 offen offset:3072  lds\n\ts_waitcnt vmcnt(9)"
      :  : "s"(ldsS), "v"(vW2[0]), "s"(rs2), "s"(sw[11]), "v"(vW2[1]), "v"(vW2[2]), "v"(vW2[3]) : "memory", "scc"); SB();
  fw2[9][0] = tr8u(tS[0] + 18432, 512);
  fw2[9][1] = tr8u(tS[1] + 18432, 512);
  fw2[9][2] = tr8u(tS[2] + 18432, 512);
  fw2[9][3] = tr8u(tS[3] + 18432, 512);
  WAITL(0);
  asm volatile("s_add_u32 m0, %0, 18432\n\ts_nop 0\n\tbuffer_load_dwordx4 %1, %2, %3 offen offset:0  lds\n\tbuffer_load_dwordx4 %4, %2, %3 offen offset:1024  lds\n\tbuffer_load_dwordx4 %5, %2, %3 offen offset:2048  lds\n\tbuffer_load_dwordx4 %6, %2, %3 offen offset:3072  lds\n\ts_waitcnt vmcnt(9)"
      :  : "s"(ldsS), "v"(vW2[0]), "s"(rs2), "s"(sw[12]), "v"(vW2[1]), "v"(vW2[2]), "v"(vW2[3]) : "memory", "scc"); SB();
  fw2[10][0] = tr8u(tS[0] + 0, 512);
  fw2[10][1] = tr8u(tS[1] + 0, 512);
  fw2[10][2] = tr8u(tS[2] + 0, 512);
  fw2[10][3] = tr8u(tS[3] + 0, 512);
  WAITL(0);
  asm volatile("s_add_u32 m0, %1, 0\n\ts_nop 0\n\tbuffer_load_dwordx4 %2, %3, %4 offen offset:0  lds\n\tbuffer_load_dwordx4 %5, %3, %4 offen offset:1024  lds\n\tbuffer_load_dwordx4 %6, %3, %4 offen offset:2048  lds\n\tbuffer_load_dwordx4 %7, %3, %4 offen offset:3072  lds\n\ts_add_u32 m0, %8, 8192\n\ts_add_u32 %0, %9, 8192\n\tbuffer_load_dwordx4 %10, %11, %0 offen offset:0 sc1 lds\n\tbuffer_load_dwordx4 %10, %11, %0 offen offset:1024 sc1 lds\n\tbuffer_load_dwordx4 %10, %11, %0 offen offset:2048 sc1 lds\n\tbuffer_load_dwordx4 %10, %11, %0 offen offset:3072 sc1 lds\n\ts_waitcnt vmcnt(12)"
      : "=&s"(sWt) : "s"(ldsS), "v"(vW2[0]), "s"(rs2), "s"(sw[13]), "v"(vW2[1]), "v"(vW2[2]), "v"(vW2[3]), "s"(ldsP), "s"(sWo), "v"(vWo), "s"(rsW) : "memory", "scc"); SB();
  fw2[11][0] = tr8u(tS[0] + 34816, 512);
  fw2[11][1] = tr8u(tS[1] + 34816, 512);
  fw2[11][2] = tr8u(tS[2] + 34816, 512);
  fw2[11][3] = tr8u(tS[3] + 34816, 512);
  asm volatile("buffer_load_dword %0, %2, %3, 0 offen sc1\n\ts_add_u32 m0, %4, 12288\n\ts_add_u32 %1, %5, 12288\n\tbuffer_load_dwordx4 %6, %3, %1 offen offset:0 sc1 lds\n\tbuffer_load_dwordx4 %6, %3, %1 offen offset:1024 sc1 lds"
      : "=&v"(fu), "=&s"(sWt) : "v"(vFU), "s"(rsW), "s"(ldsP), "s"(sWo), "v"(vWo) : "memory", "scc"); SB();
  u32x4 rsX = srd(x, 2048u * 896u); OPQ(rsX);
  WAITL(0);
  asm volatile("s_add_u32 m0, %2, 34816\n\ts_nop 0\n\tbuffer_load_dwordx4 %3, %4, %5 offen offset:0  lds\n\tbuffer_load_dwordx4 %6, %4, %5 offen offset:1024  lds\n\tglobal_load_ushort %0, %7, %8 offset:0\n\ts_add_u32 m0, %9, 0\n\ts_add_u32 %1, %10, 0\n\tbuffer_load_dwordx4 %11, %12, %1 offen offset:0 sc1 lds\n\tbuffer_load_dwordx4 %11, %12, %1 offen offset:1024 sc1 lds\n\tbuffer_load_dwordx4 %11, %12, %1 offen offset:2048 sc1 lds\n\tbuffer_load_dwordx4 %11, %12, %1 offen offset:3072 sc1 lds\n\ts_add_u32 m0, %9, 4096\n\ts_add_u32 %1, %10, 4096\n\tbuffer_load_dwordx4 %11, %12, %1 offen offset:0 sc1 lds\n\tbuffer_load_dwordx4 %11, %12, %1 offen offset:1024 sc1 lds\n\tbuffer_load_dwordx4 %11, %12, %1 offen offset:2048 sc1 lds\n\tbuffer_load_dwordx4 %11, %12, %1 offen offset:3072 sc1 lds"
      : "=&v"(braw), "=&s"(sWt) : "s"(ldsS), "v"(vXa), "s"(rsX), "s"(0u), "v"(vXb), "v"(vB2), "s"(b2), "s"(ldsO), "s"(sWo), "v"(vWo), "s"(rsW) : "memory", "scc"); SB();
  STAMP(16);
  {
  bf16x8 pu[12], fwo[56];
  f32x4 ac[2][4], as[4], py[4][4];
  float ye[4], ze[4], ge[16], xe[16], bias;
  u32x2 xv[4], ov[4]; unsigned fo, xta[4];
  WAITV1(13, fu);
  if (__ballot(fu != nonce) != 0ull) poll1<4>(rsW, vFU, nonce);
  STAMP(17);
  asm volatile("buffer_load_dwordx4 %0, %10, %11, %12 offen offset:0 sc1\n\tbuffer_load_dwordx4 %1, %10, %11, %13 offen offset:0 sc1\n\tbuffer_load_dwordx4 %2, %10, %11, %14 offen offset:0 sc1\n\tbuffer_load_dwordx4 %3, %10, %11, %15 offen offset:0 sc1\n\tbuffer_load_dwordx4 %4, %10, %11, %16 offen offset:0 sc1\n\tbuffer_load_dwordx4 %5, %10, %11, %17 offen offset:0 sc1\n\tbuffer_load_dwordx4 %6, %10, %11, %18 offen offset:0 sc1\n\tbuffer_load_dwordx4 %7, %10, %11, %19 offen offset:0 sc1\n\tbuffer_load_dwordx4 %8, %10, %11, %20 offen offset:0 sc1\n\tbuffer_load_dwordx4 %9, %10, %11, %21 offen offset:0 sc1"
      : "=&v"(pu[0]), "=&v"(pu[1]), "=&v"(pu[2]), "=&v"(pu[3]), "=&v"(pu[4]), "=&v"(pu[5]), "=&v"(pu[6]), "=&v"(pu[7]), "=&v"(pu[8]), "=&v"(pu[9]) : "v"(vFj[0]), "s"(rsW), "s"(su[0]), "s"(su[1]), "s"(su[2]), "s"(su[3]), "s"(su[4]), "s"(su[5]), "s"(su[6]), "s"(su[7]), "s"(su[8]), "s"(su[9]) : "memory"); SB();
  STAMP(39);
  WAITVB(9);
  SB();
  asm volatile("v_mfma_f32_16x16x32_bf16 %0, %3, %4, 0\n\tbuffer_load_dwordx4 %1, %5, %6, %7 offen offset:0 sc1\n\tv_mfma_f32_16x16x32_bf16 %2, %3, %8, 0\n\ts_waitcnt vmcnt(33)"
      : "=&v"(ac[0][0]), "=&v"(pu[10]), "=&v"(ac[0][1]) : "v"(pu[0]), "a"(fw2[0][0]), "v"(vFj[0]), "s"(rsW), "s"(su[10]), "a"(fw2[0][1]) : "memory"); SB();
  WAITVB(19);
  SB();
  asm volatile("v_mfma_f32_16x16x32_bf16 %0, %4, %5, 0\n\tbuffer_load_dwordx4 %1, %6, %7, %8 offen offset:0 sc1\n\tv_lshlrev_b32 %2, 16, %9\n\tv_mfma_f32_16x16x32_bf16 %3, %4, %10, 0"
      : "=&v"(ac[0][2]), "=&v"(pu[11]), "=&v"(bias), "=&v"(ac[0][3]) : "v"(pu[0]), "a"(fw2[0][2]), "v"(vFj[0]), "s"(rsW), "s"(su[11]), "v"(braw), "a"(fw2[0][3]) : "memory"); SB();
  WAITVB(10);
  SB();
  asm volatile("v_mfma_f32_16x16x32_bf16 %0, %3, %4, %0\n\tbuffer_load_dwordx4 %1, %5, %6, %7 offen offset:0 sc1\n\tv_mfma_f32_16x16x32_bf16 %2, %3, %8, %2"
      : "+v"(ac[0][0]), "=&v"(pu[0]), "+v"(ac[0][1]) : "v"(pu[1]), "a"(fw2[1][0]), "v"(vFj[0]), "s"(rsW), "s"(su[12]), "a"(fw2[1][1]) : "memory"); SB();
  fw2[12][0] = tr8u(tS[0] + 18432, 512);
  SB();
  asm volatile("v_mfma_f32_16x16x32_bf16 %0, %1, %2, %0"
      : "+v"(ac[0][2]) : "v"(pu[1]), "a"(fw2[1][2])); SB();
  fw2[12][1] = tr8u(tS[1] + 18432, 512);
  SB();
  asm volatile("v_mfma_f32_16x16x32_bf16 %0, %1, %2, %0"
      : "+v"(ac[0][3]) : "v"(pu[1]), "a"(fw2[1][3])); SB();
  WAITVB(10);
  SB();
  asm volatile("v_mfma_f32_16x16x32_bf16 %0, %3, %4, %0\n\tbuffer_load_dwordx4 %1, %5, %6, %7 offen offset:0 sc1\n\tv_mfma_f32_16x16x32_bf16 %2, %3, %8, %2"
      : "+v"(ac[0][0]), "=&v"(pu[1]), "+v"(ac[0][1]) : "v"(pu[2]), "a"(fw2[2][0]), "v"(vFj[0]), "s"(rsW), "s"(su[13]), "a"(fw2[2][1]) : "memory"); SB();
  fw2[12][2] = tr8u(tS[2] + 18432, 512);
  SB();
  asm volatile("v_mfma_f32_16x16x32_bf16 %0, %1, %2, %0"
      : "+v"(ac[0][2]) : "v"(pu[2]), "a"(fw2[2][2])); SB();
  fw2[12][3] = tr8u(tS[3] + 18432, 512);
  asm volatile("s_waitcnt vmcnt(32)\n\tv_mfma_f32_16x16x32_bf16 %0, %1, %2, %0"
      : "+v"(ac[0][3]) : "v"(pu[2]), "a"(fw2[2][3]) : "memory"); SB();
  WAITVB(10);
  SB();
  asm volatile("v_mfma_f32_16x16x32_bf16 %0, %3, %4, %0\n\tbuffer_load_dwordx4 %1, %5, %6, %7 offen offset:0 sc1\n\tv_mfma_f32_16x16x32_bf16 %2, %3, %8, %2\n\ts_waitcnt vmcnt(15)"
      : "+v"(ac[0][0]), "=&v"(pu[2]), "+v"(ac[0][1]) : "v"(pu[3]), "a"(fw2[3][0]), "v"(vFj[1]), "s"(rsW), "s"(su[0]), "a"(fw2[3][1]) : "memory"); SB();
  BAR();
  SB();
  asm volatile("v_mfma_f32_16x16x32_bf16 %0, %1, %2, %0"
      : "+v"(ac[0][2]) : "v"(pu[3]), "a"(fw2[3][2])); SB();
  fw2[13][0] = tr8u(tS[0] + 0, 512);
  asm volatile("ds_read_b128 %0, %3 offset:0\n\tv_mfma_f32_16x16x32_bf16 %1, %4, %5, %1\n\tds_read_b128 %2, %3 offset:8192"
      : "=&a"(fwo[0]), "+v"(ac[0][3]), "=&a"(fwo[1]) : "v"(ro), "v"(pu[3]), "a"(fw2[3][3]) : "memory"); SB();
  WAITVB(10);
  SB();
  asm volatile("v_mfma_f32_16x16x32_bf16 %0, %3, %4, %0\n\tbuffer_load_dwordx4 %1, %5, %6, %7 offen offset:0 sc1\n\tv_mfma_f32_16x16x32_bf16 %2, %3, %8, %2"
      : "+v"(ac[0][0]), "=&v"(pu[3]), "+v"(ac[0][1]) : "v"(pu[4]), "a"(fw2[4][0]), "v"(vFj[1]), "s"(rsW), "s"(su[1]), "a"(fw2[4][1]) : "memory"); SB();
  fw2[13][1] = tr8u(tS[1] + 0, 512);
  SB();
  asm volatile("v_mfma_f32_16x16x32_bf16 %0, %1, %2, %0"
      : "+v"(ac[0][2]) : "v"(pu[4]), "a"(fw2[4][2])); SB();
  fw2[13][2] = tr8u(tS[2] + 0, 512);
  asm volatile("ds_read_b128 %0, %3 offset:16384\n\tv_mfma_f32_16x16x32_bf16 %1, %4, %5, %1\n\tds_read_b128 %2, %3 offset:24576"
      : "=&a"(fwo[2]), "+v"(ac[0][3]), "=&a"(fwo[3]) : "v"(ro), "v"(pu[4]), "a"(fw2[4][3]) : "memory"); SB();
  WAITVB(10);
  SB();
  asm volatile("v_mfma_f32_16x16x32_bf16 %0, %3, %4, %0\n\tbuffer_load_dwordx4 %1, %5, %6, %7 offen offset:0 sc1\n\tv_mfma_f32_16x16x32_bf16 %2, %3, %8, %2"
      : "+v"(ac[0][0]), "=&v"(pu[4]), "+v"(ac[0][1]) : "v"(pu[5]), "a"(fw2[5][0]), "v"(vFj[1]), "s"(rsW), "s"(su[2]), "a"(fw2[5][1]) : "memory"); SB();
  fw2[13][3] = tr8u(tS[3] + 0, 512);
  SB();
  asm volatile("v_mfma_f32_16x16x32_bf16 %0, %6, %7, %0\n\tv_xor_b32 %1, 0, %8\n\tds_read_b64 %2, %1 offset:0\n\tds_read_b128 %3, %9 offset:1024\n\tv_mfma_f32_16x16x32_bf16 %4, %6, %10, %4\n\tds_read_b128 %5, %9 offset:9216"
      : "+v"(ac[0][2]), "=&v"(xta[0]), "=&v"(xv[0]), "=&a"(fwo[4]), "+v"(ac[0][3]), "=&a"(fwo[5]) : "v"(pu[5]), "a"(fw2[5][2]), "v"(xt), "v"(ro), "a"(fw2[5][3]) : "memory"); SB();
  WAITVB(10);
  SB();
  asm volatile("v_mfma_f32_16x16x32_bf16 %0, %11, %12, %0\n\tbuffer_load_dwordx4 %1, %13, %14, %15 offen offset:0 sc1\n\tv_mfma_f32_16x16x32_bf16 %2, %11, %16, %2\n\tv_xor_b32 %3, 32, %17\n\tds_read_b64 %4, %3 offset:0\n\tv_mfma_f32_16x16x32_bf16 %5, %11, %18, %5\n\tv_xor_b32 %6, 64, %17\n\tds_read_b64 %7, %6 offset:0\n\tds_read_b128 %8, %19 offset:17408\n\tv_mfma_f32_16x16x32_bf16 %9, %11, %20, %9\n\tds_read_b128 %10, %19 offset:25600"
      : "+v"(ac[0][0]), "=&v"(pu[5]), "+v"(ac[0][1]), "=&v"(xta[1]), "=&v"(xv[1]), "+v"(ac[0][2]), "=&v"(xta[2]), "=&v"(xv[2]), "=&a"(fwo[6]), "+v"(ac[0][3]), "=&a"(fwo[7]) : "v"(pu[6]), "a"(fw2[6][0]), "v"(vFj[1]), "s"(rsW), "s"(su[3]), "a"(fw2[6][1]), "v"(xt), "a"(fw2[6][2]), "v"(ro), "a"(fw2[6][3]) : "memory"); SB();
  WAITVB(10);
  SB();
  asm volatile("v_mfma_f32_16x16x32_bf16 %0, %11, %12, %0\n\tbuffer_load_dwordx4 %1, %13, %14, %15 offen offset:0 sc1\n\tv_mfma_f32_16x16x32_bf16 %2, %11, %16, %2\n\tv_xor_b32 %3, 96, %17\n\tds_read_b64 %4, %3 offset:0\n\tv_mfma_f32_16x16x32_bf16 %5, %11, %18, %5\n\tds_read_b128 %6, %19 offset:2048\n\tds_read_b128 %7, %19 offset:10240\n\tds_read_b128 %8, %19 offset:18432\n\tv_mfma_f32_16x16x32_bf16 %9, %11, %20, %9\n\tds_read_b128 %10, %19 offset:26624"
      : "+v"(ac[0][0]), "=&v"(pu[6]), "+v"(ac[0][1]), "=&v"(xta[3]), "=&v"(xv[3]), "+v"(ac[0][2]), "=&a"(fwo[8]), "=&a"(fwo[9]), "=&a"(fwo[10]), "+v"(ac[0][3]), "=&a"(fwo[11]) : "v"(pu[7]), "a"(fw2[7][0]), "v"(vFj[1]), "s"(rsW), "s"(su[4]), "a"(fw2[7][1]), "v"(xt), "a"(fw2[7][2]), "v"(ro), "a"(fw2[7][3]) : "memory"); SB();
  WAITVB(10);
  SB();
  asm volatile("v_mfma_f32_16x16x32_bf16 %0, %6, %7, %0\n\tbuffer_load_dwordx4 %1, %8, %9, %10 offen offset:0 sc1\n\tv_mfma_f32_16x16x32_bf16 %2, %6, %11, %2\n\tds_read_b128 %3, %12 offset:3072\n\tds_read_b128 %4, %12 offset:11264\n\tv_mfma_f32_16x16x32_bf16 %5, %6, %13, %5"
      : "+v"(ac[0][0]), "=&v"(pu[7]), "+v"(ac[0][1]), "=&a"(fwo[12]), "=&a"(fwo[13]), "+v"(ac[0][2]) : "v"(pu[8]), "a"(fw2[8][0]), "v"(vFj[1]), "s"(rsW), "s"(su[5]), "a"(fw2[8][1]), "v"(ro), "a"(fw2[8][2]) : "memory"); SB();
  WAITL(6);
  asm volatile("v_lshlrev_b32 %0, 16, %4\n\tv_and_b32 %1, 0xffff0000, %4\n\tv_mfma_f32_16x16x32_bf16 %2, %5, %6, %2\n\tv_lshlrev_b32 %3, 16, %7"
      : "=&v"(xe[0]), "=&v"(xe[1]), "+v"(ac[0][3]), "=&v"(xe[2]) : "v"(xv[0][0]), "v"(pu[8]), "a"(fw2[8][3]), "v"(xv[0][1])); SB();
  WAITVB(10);
  SB();
  asm volatile("v_mfma_f32_16x16x32_bf16 %0, %11, %12, %0\n\tbuffer_load_dwordx4 %1, %13, %14, %15 offen offset:0 sc1\n\tv_mfma_f32_16x16x32_bf16 %2, %11, %16, %2\n\tv_and_b32 %3, 0xffff0000, %17\n\tv_lshlrev_b32 %4, 16, %18\n\tv_mfma_f32_16x16x32_bf16 %5, %11, %19, %5\n\tv_and_b32 %6, 0xffff0000, %18\n\tv_lshlrev_b32 %7, 16, %20\n\tv_and_b32 %8, 0xffff0000, %20\n\tv_mfma_f32_16x16x32_bf16 %9, %11, %21, %9\n\tv_lshlrev_b32 %10, 16, %22"
      : "+v"(ac[0][0]), "=&v"(pu[8]), "+v"(ac[0][1]), "=&v"(xe[3]), "=&v"(xe[4]), "+v"(ac[0][2]), "=&v"(xe[5]), "=&v"(xe[6]), "=&v"(xe[7]), "+v"(ac[0][3]), "=&v"(xe[8]) : "v"(pu[9]), "v"(fw2[9][0]), "v"(vFj[1]), "s"(rsW), "s"(su[6]), "v"(fw2[9][1]), "v"(xv[0][1]), "v"(xv[1][0]), "v"(fw2[9][2]), "v"(xv[1][1]), "v"(fw2[9][3]), "v"(xv[2][0]) : "memory"); SB();
  WAITVB(10);
  SB();
  asm volatile("v_mfma_f32_16x16x32_bf16 %0, %11, %12, %0\n\tbuffer_load_dwordx4 %1, %13, %14, %15 offen offset:0 sc1\n\tv_mfma_f32_16x16x32_bf16 %2, %11, %16, %2\n\tv_and_b32 %3, 0xffff0000, %17\n\tv_lshlrev_b32 %4, 16, %18\n\tv_mfma_f32_16x16x32_bf16 %5, %11, %19, %5\n\tv_and_b32 %6, 0xffff0000, %18\n\tv_lshlrev_b32 %7, 16, %20\n\tv_and_b32 %8, 0xffff0000, %20\n\tv_mfma_f32_16x16x32_bf16 %9, %11, %21, %9\n\tv_lshlrev_b32 %10, 16, %22"
      : "+v"(ac[0][0]), "=&v"(pu[9]), "+v"(ac[0][1]), "=&v"(xe[9]), "=&v"(xe[10]), "+v"(ac[0][2]), "=&v"(xe[11]), "=&v"(xe[12]), "=&v"(xe[13]), "+v"(ac[0][3]), "=&v"(xe[14]) : "v"(pu[10]), "v"(fw2[10][0]), "v"(vFj[1]), "s"(rsW), "s"(su[7]), "v"(fw2[10][1]), "v"(xv[2][0]), "v"(xv[2][1]), "v"(fw2[10][2]), "v"(xv[3][0]), "v"(fw2[10][3]), "v"(xv[3][1]) : "memory"); SB();
  WAITVB(10);
  SB();
  asm volatile("v_mfma_f32_16x16x32_bf16 %0, %11, %12, %0\n\tbuffer_load_dwordx4 %1, %13, %14, %15 offen offset:0 sc1\n\tv_mfma_f32_16x16x32_bf16 %2, %11, %16, %2\n\tv_and_b32 %3, 0xffff0000, %17\n\tds_read_b128 %4, %18 offset:19456\n\tv_mfma_f32_16x16x32_bf16 %5, %11, %19, %5\n\tds_read_b128 %6, %18 offset:27648\n\tds_read_b128 %7, %18 offset:4096\n\tds_read_b128 %8, %18 offset:12288\n\tv_mfma_f32_16x16x32_bf16 %9, %11, %20, %9\n\tds_read_b128 %10, %18 offset:20480"
      : "+v"(ac[0][0]), "=&v"(pu[10]), "+v"(ac[0][1]), "=&v"(xe[15]), "=&a"(fwo[14]), "+v"(ac[0][2]), "=&a"(fwo[15]), "=&a"(fwo[16]), "=&a"(fwo[17]), "+v"(ac[0][3]), "=&a"(fwo[18]) : "v"(pu[11]), "v"(fw2[11][0]), "v"(vFj[1]), "s"(rsW), "s"(su[8]), "v"(fw2[11][1]), "v"(xv[3][1]), "v"(ro), "v"(fw2[11][2]), "v"(fw2[11][3]) : "memory"); SB();
  WAITVB(10);
  SB();
  asm volatile("v_mfma_f32_16x16x32_bf16 %0, %8, %9, %0\n\tbuffer_load_dwordx4 %1, %10, %11, %12 offen offset:0 sc1\n\tv_mfma_f32_16x16x32_bf16 %2, %8, %13, %2\n\tds_read_b128 %3, %14 offset:28672\n\tds_read_b128 %4, %14 offset:5120\n\tv_mfma_f32_16x16x32_bf16 %5, %8, %15, %5\n\tds_read_b128 %6, %14 offset:13312\n\tv_mfma_f32_16x16x32_bf16 %7, %8, %16, %7"
      : "+v"(ac[0][0]), "=&v"(pu[11]), "+v"(ac[0][1]), "=&a"(fwo[19]), "=&a"(fwo[20]), "+v"(ac[0][2]), "=&a"(fwo[21]), "+v"(ac[0][3]) : "v"(pu[0]), "v"(fw2[12][0]), "v"(vFj[1]), "s"(rsW), "s"(su[9]), "v"(fw2[12][1]), "v"(ro), "v"(fw2[12][2]), "v"(fw2[12][3]) : "memory"); SB();
  WAITVB(10);
  SB();
  asm volatile("v_mfma_f32_16x16x32_bf16 %0, %5, %6, %0\n\tbuffer_load_dwordx4 %1, %7, %8, %9 offen offset:0 sc1\n\tv_mfma_f32_16x16x32_bf16 %2, %5, %10, %2\n\tv_mfma_f32_16x16x32_bf16 %3, %5, %11, %3\n\tv_mfma_f32_16x16x32_bf16 %4, %5, %12, %4"
      : "+v"(ac[0][0]), "=&v"(pu[0]), "+v"(ac[0][1]), "+v"(ac[0][2]), "+v"(ac[0][3]) : "v"(pu[1]), "v"(fw2[13][0]), "v"(vFj[1]), "s"(rsW), "s"(su[10]), "v"(fw2[13][1]), "v"(fw2[13][2]), "v"(fw2[13][3]) : "memory"); SB();
  STAMP(40);
  WAITVB(10);
  SB();
  asm volatile("v_mfma_f32_16x16x32_bf16 %0, %5, %6, 0\n\tbuffer_load_dwordx4 %1, %7, %8, %9 offen offset:0 sc1\n\tv_mfma_f32_16x16x32_bf16 %2, %5, %10, 0\n\tv_mfma_f32_16x16x32_bf16 %3, %5, %11, 0\n\tv_mfma_f32_16x16x32_bf16 %4, %5, %12, 0"
      : "=&v"(ac[1][0]), "=&v"(pu[1]), "=&v"(ac[1][1]), "=&v"(ac[1][2]), "=&v"(ac[1][3]) : "v"(pu[2]), "a"(fw2[0][0]), "v"(vFj[1]), "s"(rsW), "s"(su[11]), "a"(fw2[0][1]), "a"(fw2[0][2]), "a"(fw2[0][3]) : "memory"); SB();
  WAITVB(10);
  SB();
  asm volatile("v_mfma_f32_16x16x32_bf16 %0, %5, %6, %0\n\tbuffer_load_dwordx4 %1, %7, %8, %9 offen offset:0 sc1\n\tv_mfma_f32_16x16x32_bf16 %2, %5, %10, %2\n\tds_write_b128 %11, %12 offset:0\n\tv_mfma_f32_16x16x32_bf16 %3, %5, %13, %3\n\tv_mfma_f32_16x16x32_bf16 %4, %5, %14, %4"
      : "+v"(ac[1][0]), "=&v"(pu[2]), "+v"(ac[1][1]), "+v"(ac[1][2]), "+v"(ac[1][3]) : "v"(pu[3]), "a"(fw2[1][0]), "v"(vFj[1]), "s"(rsW), "s"(su[12]), "a"(fw2[1][1]), "v"(rwd), "v"(ac[0][0]), "a"(fw2[1][2]), "a"(fw2[1][3]) : "memory"); SB();
  WAITVB(10);
  SB();
  asm volatile("v_mfma_f32_16x16x32_bf16 %0, %5, %6, %0\n\tbuffer_load_dwordx4 %1, %7, %8, %9 offen offset:0 sc1\n\tv_mfma_f32_16x16x32_bf16 %2, %5, %10, %2\n\tds_write_b128 %11, %12 offset:1024\n\tv_mfma_f32_16x16x32_bf16 %3, %5, %13, %3\n\tv_mfma_f32_16x16x32_bf16 %4, %5, %14, %4"
      : "+v"(ac[1][0]), "=&v"(pu[3]), "+v"(ac[1][1]), "+v"(ac[1][2]), "+v"(ac[1][3]) : "v"(pu[4]), "a"(fw2[2][0]), "v"(vFj[1]), "s"(rsW), "s"(su[13]), "a"(fw2[2][1]), "v"(rwd), "v"(ac[0][1]), "a"(fw2[2][2]), "a"(fw2[2][3]) : "memory"); SB();
  WAITVB(10);
  SB();
  asm volatile("v_mfma_f32_16x16x32_bf16 %0, %5, %6, %0\n\tbuffer_load_dwordx4 %1, %7, %8, %9 offen offset:0 sc1\n\tv_mfma_f32_16x16x32_bf16 %2, %5, %10, %2\n\tds_write_b128 %11, %12 offset:2048\n\tv_mfma_f32_16x16x32_bf16 %3, %5, %13, %3\n\tv_mfma_f32_16x16x32_bf16 %4, %5, %14, %4"
      : "+v"(ac[1][0]), "=&v"(pu[4]), "+v"(ac[1][1]), "+v"(ac[1][2]), "+v"(ac[1][3]) : "v"(pu[5]), "a"(fw2[3][0]), "v"(vFj[2]), "s"(rsW), "s"(su[0]), "a"(fw2[3][1]), "v"(rwd), "v"(ac[0][2]), "a"(fw2[3][2]), "a"(fw2[3][3]) : "memory"); SB();
  WAITVB(10);
  SB();
  asm volatile("v_mfma_f32_16x16x32_bf16 %0, %5, %6, %0\n\tbuffer_load_dwordx4 %1, %7, %8, %9 offen offset:0 sc1\n\tv_mfma_f32_16x16x32_bf16 %2, %5, %10, %2\n\tds_write_b128 %11, %12 offset:3072\n\tv_mfma_f32_16x16x32_bf16 %3, %5, %13, %3\n\tv_mfma_f32_16x16x32_bf16 %4, %5, %14, %4"
      : "+v"(ac[1][0]), "=&v"(pu[5]), "+v"(ac[1][1]), "+v"(ac[1][2]), "+v"(ac[1][3]) : "v"(pu[6]), "a"(fw2[4][0]), "v"(vFj[2]), "s"(rsW), "s"(su[1]), "a"(fw2[4][1]), "v"(rwd), "v"(ac[0][3]), "a"(fw2[4][2]), "a"(fw2[4][3]) : "memory"); SB();
  WAITVB(10);
  SB();
  asm volatile("v_mfma_f32_16x16x32_bf16 %0, %3, %4, %0\n\tbuffer_load_dwordx4 %1, %5, %6, %7 offen offset:0 sc1\n\tv_mfma_f32_16x16x32_bf16 %2, %3, %8, %2"
      : "+v"(ac[1][0]), "=&v"(pu[6]), "+v"(ac[1][1]) : "v"(pu[7]), "a"(fw2[5][0]), "v"(vFj[2]), "s"(rsW), "s"(su[2]), "a"(fw2[5][1]) : "memory"); SB();
  BAR();
  SB();
  asm volatile("v_mfma_f32_16x16x32_bf16 %0, %6, %7, %0\n\tds_read_b128 %1, %8 offset:0\n\tds_read_b128 %2, %8 offset:10240\n\tds_read_b128 %3, %8 offset:20480\n\tv_mfma_f32_16x16x32_bf16 %4, %6, %9, %4\n\tds_read_b128 %5, %8 offset:30720"
      : "+v"(ac[1][2]), "=&v"(py[0][0]), "=&v"(py[0][1]), "=&v"(py[0][2]), "+v"(ac[1][3]), "=&v"(py[0][3]) : "v"(pu[7]), "a"(fw2[5][2]), "v"(rr), "a"(fw2[5][3]) : "memory"); SB();
  WAITVB(10);
  SB();
  asm volatile("v_mfma_f32_16x16x32_bf16 %0, %5, %6, %0\n\tbuffer_load_dwordx4 %1, %7, %8, %9 offen offset:0 sc1\n\tv_mfma_f32_16x16x32_bf16 %2, %5, %10, %2\n\tv_mfma_f32_16x16x32_bf16 %3, %5, %11, %3\n\tv_mfma_f32_16x16x32_bf16 %4, %5, %12, %4"
      : "+v"(ac[1][0]), "=&v"(pu[7]), "+v"(ac[1][1]), "+v"(ac[1][2]), "+v"(ac[1][3]) : "v"(pu[8]), "a"(fw2[6][0]), "v"(vFj[2]), "s"(rsW), "s"(su[3]), "a"(fw2[6][1]), "a"(fw2[6][2]), "a"(fw2[6][3]) : "memory"); SB();
  WAITVB(10);
  SB();
  asm volatile("v_mfma_f32_16x16x32_bf16 %0, %3, %4, %0\n\tbuffer_load_dwordx4 %1, %5, %6, %7 offen offset:0 sc1\n\tv_mfma_f32_16x16x32_bf16 %2, %3, %8, %2"
      : "+v"(ac[1][0]), "=&v"(pu[8]), "+v"(ac[1][1]) : "v"(pu[9]), "a"(fw2[7][0]), "v"(vFj[2]), "s"(rsW), "s"(su[4]), "a"(fw2[7][1]) : "memory"); SB();
  WAITL(0);
  asm volatile("v_add_f32 %0, %7, %8\n\tv_mfma_f32_16x16x32_bf16 %1, %9, %10, %1\n\tv_add_f32 %2, %11, %8\n\tv_add_f32 %3, %12, %8\n\tv_add_f32 %4, %13, %8\n\tv_mfma_f32_16x16x32_bf16 %5, %9, %14, %5\n\tv_add_f32 %6, %15, %16"
      : "=&v"(ye[0]), "+v"(ac[1][2]), "=&v"(ye[1]), "=&v"(ye[2]), "=&v"(ye[3]), "+v"(ac[1][3]), "=&v"(ze[0]) : "v"(py[0][0][0]), "v"(bias), "v"(pu[9]), "a"(fw2[7][2]), "v"(py[0][0][1]), "v"(py[0][0][2]), "v"(py[0][0][3]), "a"(fw2[7][3]), "v"(py[0][1][0]), "v"(py[0][2][0])); SB();
  WAITVB(10);
  SB();
  asm volatile("v_mfma_f32_16x16x32_bf16 %0, %9, %10, %0\n\tbuffer_load_dwordx4 %1, %11, %12, %13 offen offset:0 sc1\n\tv_mfma_f32_16x16x32_bf16 %2, %9, %14, %2\n\tv_add_f32 %3, %15, %16\n\tv_add_f32 %4, %17, %18\n\tv_mfma_f32_16x16x32_bf16 %5, %9, %19, %5\n\tv_add_f32 %6, %20, %21\n\tv_add_f32 %7, %7, %22\n\tv_add_f32 %8, %8, %23"
      : "+v"(ac[1][0]), "=&v"(pu[9]), "+v"(ac[1][1]), "=&v"(ze[1]), "=&v"(ze[2]), "+v"(ac[1][2]), "=&v"(ze[3]), "+v"(ye[0]), "+v"(ye[1]) : "v"(pu[10]), "a"(fw2[8][0]), "v"(vFj[2]), "s"(rsW), "s"(su[5]), "a"(fw2[8][1]), "v"(py[0][1][1]), "v"(py[0][2][1]), "v"(py[0][1][2]), "v"(py[0][2][2]), "a"(fw2[8][2]), "v"(py[0][1][3]), "v"(py[0][2][3]), "v"(py[0][3][0]), "v"(py[0][3][1]) : "memory"); SB();
  asm volatile("v_mfma_f32_16x16x32_bf16 %0, %2, %3, %0\n\tv_add_f32 %1, %1, %4"
      : "+v"(ac[1][3]), "+v"(ye[2]) : "v"(pu[10]), "a"(fw2[8][3]), "v"(py[0][3][2])); SB();
  WAITVB(10);
  SB();
  asm volatile("v_mfma_f32_16x16x32_bf16 %0, %9, %10, %0\n\tbuffer_load_dwordx4 %1, %11, %12, %13 offen offset:0 sc1\n\tv_mfma_f32_16x16x32_bf16 %2, %9, %14, %2\n\tv_add_f32 %3, %3, %15\n\tv_add_f32 %4, %4, %16\n\tv_mfma_f32_16x16x32_bf16 %5, %9, %17, %5\n\tv_add_f32 %6, %6, %18\n\tv_add_f32 %7, %7, %19\n\tv_add_f32 %3, %3, %20\n\tv_mfma_f32_16x16x32_bf16 %8, %9, %21, %8"
      : "+v"(ac[1][0]), "=&v"(pu[10]), "+v"(ac[1][1]), "+v"(ye[3]), "+v"(ye[0]), "+v"(ac[1][2]), "+v"(ye[1]), "+v"(ye[2]), "+v"(ac[1][3]) : "v"(pu[11]), "v"(fw2[9][0]), "v"(vFj[2]), "s"(rsW), "s"(su[6]), "v"(fw2[9][1]), "v"(py[0][3][3]), "v"(ze[0]), "v"(fw2[9][2]), "v"(ze[1]), "v"(ze[2]), "v"(ze[3]), "v"(fw2[9][3]) : "memory"); SB();
  asm volatile("v_mul_f32 %0, 0xbfb8aa3b, %1"
      : "=&v"(ze[0]) : "v"(ye[0])); SB();
  WAITVB(10);
  SB();
  asm volatile("v_mfma_f32_16x16x32_bf16 %0, %9, %10, %0\n\tbuffer_load_dwordx4 %1, %11, %12, %13 offen offset:0 sc1\n\tv_mfma_f32_16x16x32_bf16 %2, %9, %14, %2\n\tv_mul_f32 %3, 0xbfb8aa3b, %15\n\tv_mul_f32 %4, 0xbfb8aa3b, %16\n\tv_mfma_f32_16x16x32_bf16 %5, %9, %17, %5\n\tv_mul_f32 %6, 0xbfb8aa3b, %18\n\tv_exp_f32 %7, %7\n\tv_mfma_f32_16x16x32_bf16 %8, %9, %19, %8"
      : "+v"(ac[1][0]), "=&v"(pu[11]), "+v"(ac[1][1]), "=&v"(ze[1]), "=&v"(ze[2]), "+v"(ac[1][2]), "=&v"(ze[3]), "+v"(ze[0]), "+v"(ac[1][3]) : "v"(pu[0]), "v"(fw2[10][0]), "v"(vFj[2]), "s"(rsW), "s"(su[7]), "v"(fw2[10][1]), "v"(ye[1]), "v"(ye[2]), "v"(fw2[10][2]), "v"(ye[3]), "v"(fw2[10][3]) : "memory"); SB();
  WAITVB(10);
  SB();
  asm volatile("v_mfma_f32_16x16x32_bf16 %0, %7, %8, %0\n\tbuffer_load_dwordx4 %1, %9, %10, %11 offen offset:0 sc1\n\tv_mfma_f32_16x16x32_bf16 %2, %7, %12, %2\n\tv_exp_f32 %3, %3\n\tv_mfma_f32_16x16x32_bf16 %4, %7, %13, %4\n\tv_exp_f32 %5, %5\n\tv_mfma_f32_16x16x32_bf16 %6, %7, %14, %6"
      : "+v"(ac[1][0]), "=&v"(pu[0]), "+v"(ac[1][1]), "+v"(ze[1]), "+v"(ac[1][2]), "+v"(ze[2]), "+v"(ac[1][3]) : "v"(pu[1]), "v"(fw2[11][0]), "v"(vFj[2]), "s"(rsW), "s"(su[8]), "v"(fw2[11][1]), "v"(fw2[11][2]), "v"(fw2[11][3]) : "memory"); SB();
  WAITVB(10);
  SB();
  asm volatile("v_mfma_f32_16x16x32_bf16 %0, %9, %10, %0\n\tbuffer_load_dwordx4 %1, %11, %12, %13 offen offset:0 sc1\n\tv_mfma_f32_16x16x32_bf16 %2, %9, %14, %2\n\tv_exp_f32 %3, %3\n\tv_mfma_f32_16x16x32_bf16 %4, %9, %15, %4\n\tv_add_f32 %5, 1.0, %5\n\tv_add_f32 %6, 1.0, %6\n\tv_add_f32 %7, 1.0, %7\n\tv_mfma_f32_16x16x32_bf16 %8, %9, %16, %8\n\tv_add_f32 %3, 1.0, %3"
      : "+v"(ac[1][0]), "=&v"(pu[1]), "+v"(ac[1][1]), "+v"(ze[3]), "+v"(ac[1][2]), "+v"(ze[0]), "+v"(ze[1]), "+v"(ze[2]), "+v"(ac[1][3]) : "v"(pu[2]), "v"(fw2[12][0]), "v"(vFj[2]), "s"(rsW), "s"(su[9]), "v"(fw2[12][1]), "v"(fw2[12][2]), "v"(fw2[12][3]) : "memory"); SB();
  WAITVB(10);
  SB();
  asm volatile("v_mfma_f32_16x16x32_bf16 %0, %8, %9, %0\n\tbuffer_load_dwordx4 %1, %10, %11, %12 offen offset:0 sc1\n\tv_mfma_f32_16x16x32_bf16 %2, %8, %13, %2\n\tv_rcp_f32 %3, %3\n\tv_mfma_f32_16x16x32_bf16 %4, %8, %14, %4\n\tv_rcp_f32 %5, %5\n\tv_mfma_f32_16x16x32_bf16 %6, %8, %15, %6\n\tv_rcp_f32 %7, %7"
      : "+v"(ac[1][0]), "=&v"(pu[2]), "+v"(ac[1][1]), "+v"(ze[0]), "+v"(ac[1][2]), "+v"(ze[1]), "+v"(ac[1][3]), "+v"(ze[2]) : "v"(pu[3]), "v"(fw2[13][0]), "v"(vFj[2]), "s"(rsW), "s"(su[10]), "v"(fw2[13][1]), "v"(fw2[13][2]), "v"(fw2[13][3]) : "memory"); SB();
  STAMP(41);
  WAITVB(10);
  SB();
  asm volatile("v_mfma_f32_16x16x32_bf16 %0, %10, %11, 0\n\tbuffer_load_dwordx4 %1, %12, %13, %14 offen offset:0 sc1\n\tv_mfma_f32_16x16x32_bf16 %2, %10, %15, 0\n\tv_rcp_f32 %3, %3\n\tv_mfma_f32_16x16x32_bf16 %4, %10, %16, 0\n\tv_mul_f32 %5, %17, %18\n\tv_mul_f32 %6, %19, %20\n\tv_mul_f32 %7, %21, %22\n\tv_mfma_f32_16x16x32_bf16 %8, %10, %23, 0\n\tv_mul_f32 %9, %24, %3"
      : "=&v"(ac[0][0]), "=&v"(pu[3]), "=&v"(ac[0][1]), "+v"(ze[3]), "=&v"(ac[0][2]), "=&v"(ge[0]), "=&v"(ge[1]), "=&v"(ge[2]), "=&v"(ac[0][3]), "=&v"(ge[3]) : "v"(pu[4]), "a"(fw2[0][0]), "v"(vFj[2]), "s"(rsW), "s"(su[11]), "a"(fw2[0][1]), "a"(fw2[0][2]), "v"(ye[0]), "v"(ze[0]), "v"(ye[1]), "v"(ze[1]), "v"(ye[2]), "v"(ze[2]), "a"(fw2[0][3]), "v"(ye[3]) : "memory"); SB();
  WAITVB(10);
  SB();
  asm volatile("v_mfma_f32_16x16x32_bf16 %0, %5, %6, %0\n\tbuffer_load_dwordx4 %1, %7, %8, %9 offen offset:0 sc1\n\tv_mfma_f32_16x16x32_bf16 %2, %5, %10, %2\n\tds_write_b128 %11, %12 offset:4096\n\tv_mfma_f32_16x16x32_bf16 %3, %5, %13, %3\n\tv_mfma_f32_16x16x32_bf16 %4, %5, %14, %4"
      : "+v"(ac[0][0]), "=&v"(pu[4]), "+v"(ac[0][1]), "+v"(ac[0][2]), "+v"(ac[0][3]) : "v"(pu[5]), "a"(fw2[1][0]), "v"(vFj[2]), "s"(rsW), "s"(su[12]), "a"(fw2[1][1]), "v"(rwd), "v"(ac[1][0]), "a"(fw2[1][2]), "a"(fw2[1][3]) : "memory"); SB();
  WAITVB(10);
  SB();
  asm volatile("v_mfma_f32_16x16x32_bf16 %0, %6, %7, %0\n\tbuffer_load_dwordx4 %1, %8, %9, %10 offen offset:0 sc1\n\tv_mfma_f32_16x16x32_bf16 %2, %6, %11, %2\n\tbuffer_load_dword %3, %12, %9, 0 offen sc1\n\tv_mfma_f32_16x16x32_bf16 %4, %6, %13, %4\n\tds_write_b128 %14, %15 offset:5120\n\tv_mfma_f32_16x16x32_bf16 %5, %6, %16, %5"
      : "+v"(ac[0][0]), "=&v"(pu[5]), "+v"(ac[0][1]), "=&v"(fo), "+v"(ac[0][2]), "+v"(ac[0][3]) : "v"(pu[6]), "a"(fw2[2][0]), "v"(vFj[2]), "s"(rsW), "s"(su[13]), "a"(fw2[2][1]), "v"(vFO), "a"(fw2[2][2]), "v"(rwd), "v"(ac[1][1]), "a"(fw2[2][3]) : "memory"); SB();
  WAITVB(11);
  SB();
  asm volatile("v_mfma_f32_16x16x32_bf16 %0, %5, %6, %0\n\tbuffer_load_dwordx4 %1, %7, %8, %9 offen offset:0 sc1\n\tv_mfma_f32_16x16x32_bf16 %2, %5, %10, %2\n\tv_mfma_f32_16x16x32_bf16 %3, %5, %11, %3\n\tds_write_b128 %12, %13 offset:6144\n\tv_mfma_f32_16x16x32_bf16 %4, %5, %14, %4"
      : "+v"(ac[0][0]), "=&v"(pu[6]), "+v"(ac[0][1]), "+v"(ac[0][2]), "+v"(ac[0][3]) : "v"(pu[7]), "a"(fw2[3][0]), "v"(vFj[3]), "s"(rsW), "s"(su[0]), "a"(fw2[3][1]), "a"(fw2[3][2]), "v"(rwd), "v"(ac[1][2]), "a"(fw2[3][3]) : "memory"); SB();
  WAITVB(11);
  SB();
  asm volatile("v_mfma_f32_16x16x32_bf16 %0, %5, %6, %0\n\tbuffer_load_dwordx4 %1, %7, %8, %9 offen offset:0 sc1\n\tv_mfma_f32_16x16x32_bf16 %2, %5, %10, %2\n\tv_mfma_f32_16x16x32_bf16 %3, %5, %11, %3\n\tds_write_b128 %12, %13 offset:7168\n\tv_mfma_f32_16x16x32_bf16 %4, %5, %14, %4"
      : "+v"(ac[0][0]), "=&v"(pu[7]), "+v"(ac[0][1]), "+v"(ac[0][2]), "+v"(ac[0][3]) : "v"(pu[8]), "a"(fw2[4][0]), "v"(vFj[3]), "s"(rsW), "s"(su[1]), "a"(fw2[4][1]), "a"(fw2[4][2]), "v"(rwd), "v"(ac[1][3]), "a"(fw2[4][3]) : "memory"); SB();
  WAITVB(11);
  SB();
  asm volatile("v_mfma_f32_16x16x32_bf16 %0, %4, %5, %0\n\tbuffer_load_dwordx4 %1, %6, %7, %8 offen offset:0 sc1\n\tv_mfma_f32_16x16x32_bf16 %2, %4, %9, %2\n\tv_mfma_f32_16x16x32_bf16 %3, %4, %10, %3"
      : "+v"(ac[0][0]), "=&v"(pu[8]), "+v"(ac[0][1]), "+v"(ac[0][2]) : "v"(pu[9]), "a"(fw2[5][0]), "v"(vFj[3]), "s"(rsW), "s"(su[2]), "a"(fw2[5][1]), "a"(fw2[5][2]) : "memory"); SB();
  BAR();
  asm volatile("ds_read_b128 %0, %3 offset:4096\n\tv_mfma_f32_16x16x32_bf16 %1, %4, %5, %1\n\tds_read_b128 %2, %3 offset:14336"
      : "=&v"(py[1][0]), "+v"(ac[0][3]), "=&v"(py[1][1]) : "v"(rr), "v"(pu[9]), "a"(fw2[5][3]) : "memory"); SB();
  WAITVB(11);
  SB();
  asm volatile("v_mfma_f32_16x16x32_bf16 %0, %7, %8, %0\n\tbuffer_load_dwordx4 %1, %9, %10, %11 offen offset:0 sc1\n\tv_mfma_f32_16x16x32_bf16 %2, %7, %12, %2\n\tds_read_b128 %3, %13 offset:24576\n\tds_read_b128 %4, %13 offset:34816\n\tv_mfma_f32_16x16x32_bf16 %5, %7, %14, %5\n\tv_mfma_f32_16x16x32_bf16 %6, %7, %15, %6"
      : "+v"(ac[0][0]), "=&v"(pu[9]), "+v"(ac[0][1]), "=&v"(py[1][2]), "=&v"(py[1][3]), "+v"(ac[0][2]), "+v"(ac[0][3]) : "v"(pu[10]), "a"(fw2[6][0]), "v"(vFj[3]), "s"(rsW), "s"(su[3]), "a"(fw2[6][1]), "v"(rr), "a"(fw2[6][2]), "a"(fw2[6][3]) : "memory"); SB();
  WAITVB(11);
  SB();
  asm volatile("v_mfma_f32_16x16x32_bf16 %0, %4, %5, %0\n\tbuffer_load_dwordx4 %1, %6, %7, %8 offen offset:0 sc1\n\tv_mfma_f32_16x16x32_bf16 %2, %4, %9, %2\n\tv_mfma_f32_16x16x32_bf16 %3, %4, %10, %3"
      : "+v"(ac[0][0]), "=&v"(pu[10]), "+v"(ac[0][1]), "+v"(ac[0][2]) : "v"(pu[11]), "a"(fw2[7][0]), "v"(vFj[3]), "s"(rsW), "s"(su[4]), "a"(fw2[7][1]), "a"(fw2[7][2]) : "memory"); SB();
  WAITL(0);
  asm volatile("v_add_f32 %0, %4, %5\n\tv_add_f32 %1, %6, %5\n\tv_mfma_f32_16x16x32_bf16 %2, %7, %8, %2\n\tv_add_f32 %3, %9, %5"
      : "=&v"(ye[0]), "=&v"(ye[1]), "+v"(ac[0][3]), "=&v"(ye[2]) : "v"(py[1][0][0]), "v"(bias), "v"(py[1][0][1]), "v"(pu[11]), "a"(fw2[7][3]), "v"(py[1][0][2])); SB();
  WAITVB(11);
  SB();
  asm volatile("v_mfma_f32_16x16x32_bf16 %0, %9, %10, %0\n\tbuffer_load_dwordx4 %1, %11, %12, %13 offen offset:0 sc1\n\tv_mfma_f32_16x16x32_bf16 %2, %9, %14, %2\n\tv_add_f32 %3, %15, %16\n\tv_add_f32 %4, %17, %18\n\tv_mfma_f32_16x16x32_bf16 %5, %9, %19, %5\n\tv_add_f32 %6, %20, %21\n\tv_add_f32 %7, %22, %23\n\tv_add_f32 %8, %24, %25"
      : "+v"(ac[0][0]), "=&v"(pu[11]), "+v"(ac[0][1]), "=&v"(ye[3]), "=&v"(ze[0]), "+v"(ac[0][2]), "=&v"(ze[1]), "=&v"(ze[2]), "=&v"(ze[3]) : "v"(pu[0]), "a"(fw2[8][0]), "v"(vFj[3]), "s"(rsW), "s"(su[5]), "a"(fw2[8][1]), "v"(py[1][0][3]), "v"(bias), "v"(py[1][1][0]), "v"(py[1][2][0]), "a"(fw2[8][2]), "v"(py[1][1][1]), "v"(py[1][2][1]), "v"(py[1][1][2]), "v"(py[1][2][2]), "v"(py[1][1][3]), "v"(py[1][2][3]) : "memory"); SB();
  asm volatile("v_mfma_f32_16x16x32_bf16 %0, %2, %3, %0\n\tv_add_f32 %1, %1, %4"
      : "+v"(ac[0][3]), "+v"(ye[0]) : "v"(pu[0]), "a"(fw2[8][3]), "v"(py[1][3][0])); SB();
  WAITVB(11);
  SB();
  asm volatile("v_mfma_f32_16x16x32_bf16 %0, %9, %10, %0\n\tbuffer_load_dwordx4 %1, %11, %12, %13 offen offset:0 sc1\n\tv_mfma_f32_16x16x32_bf16 %2, %9, %14, %2\n\tv_add_f32 %3, %3, %15\n\tv_add_f32 %4, %4, %16\n\tv_mfma_f32_16x16x32_bf16 %5, %9, %17, %5\n\tv_add_f32 %6, %6, %18\n\tv_add_f32 %7, %7, %19\n\tv_add_f32 %3, %3, %20\n\tv_mfma_f32_16x16x32_bf16 %8, %9, %21, %8"
      : "+v"(ac[0][0]), "=&v"(pu[0]), "+v"(ac[0][1]), "+v"(ye[1]), "+v"(ye[2]), "+v"(ac[0][2]), "+v"(ye[3]), "+v"(ye[0]), "+v"(ac[0][3]) : "v"(pu[1]), "v"(fw2[9][0]), "v"(vFj[3]), "s"(rsW), "s"(su[6]), "v"(fw2[9][1]), "v"(py[1][3][1]), "v"(py[1][3][2]), "v"(fw2[9][2]), "v"(py[1][3][3]), "v"(ze[0]), "v"(ze[1]), "v"(fw2[9][3]) : "memory"); SB();
  asm volatile("v_add_f32 %0, %0, %1"
      : "+v"(ye[2]) : "v"(ze[2])); SB();
  WAITVB(11);
  SB();
  asm volatile("v_mfma_f32_16x16x32_bf16 %0, %10, %11, %0\n\tbuffer_load_dwordx4 %1, %12, %13, %14 offen offset:0 sc1\n\tv_mfma_f32_16x16x32_bf16 %2, %10, %15, %2\n\tv_add_f32 %3, %3, %4\n\tv_mul_f32 %5, 0xbfb8aa3b, %16\n\tv_mfma_f32_16x16x32_bf16 %6, %10, %17, %6\n\tv_mul_f32 %7, 0xbfb8aa3b, %18\n\tv_mul_f32 %8, 0xbfb8aa3b, %19\n\tv_mul_f32 %4, 0xbfb8aa3b, %3\n\tv_mfma_f32_16x16x32_bf16 %9, %10, %20, %9"
      : "+v"(ac[0][0]), "=&v"(pu[1]), "+v"(ac[0][1]), "+v"(ye[3]), "+v"(ze[3]), "=&v"(ze[0]), "+v"(ac[0][2]), "=&v"(ze[1]), "=&v"(ze[2]), "+v"(ac[0][3]) : "v"(pu[2]), "v"(fw2[10][0]), "v"(vFj[3]), "s"(rsW), "s"(su[7]), "v"(fw2[10][1]), "v"(ye[0]), "v"(fw2[10][2]), "v"(ye[1]), "v"(ye[2]), "v"(fw2[10][3]) : "memory"); SB();
  WAITVB(11);
  SB();
  asm volatile("v_mfma_f32_16x16x32_bf16 %0, %7, %8, %0\n\tbuffer_load_dwordx4 %1, %9, %10, %11 offen offset:0 sc1\n\tv_mfma_f32_16x16x32_bf16 %2, %7, %12, %2\n\tv_exp_f32 %3, %3\n\tv_mfma_f32_16x16x32_bf16 %4, %7, %13, %4\n\tv_exp_f32 %5, %5\n\tv_mfma_f32_16x16x32_bf16 %6, %7, %14, %6"
      : "+v"(ac[0][0]), "=&v"(pu[2]), "+v"(ac[0][1]), "+v"(ze[0]), "+v"(ac[0][2]), "+v"(ze[1]), "+v"(ac[0][3]) : "v"(pu[3]), "v"(fw2[11][0]), "v"(vFj[3]), "s"(rsW), "s"(su[8]), "v"(fw2[11][1]), "v"(fw2[11][2]), "v"(fw2[11][3]) : "memory"); SB();
  WAITVB(11);
  SB();
  asm volatile("v_mfma_f32_16x16x32_bf16 %0, %9, %10, %0\n\tbuffer_load_dwordx4 %1, %11, %12, %13 offen offset:0 sc1\n\tv_mfma_f32_16x16x32_bf16 %2, %9, %14, %2\n\tv_exp_f32 %3, %3\n\tv_mfma_f32_16x16x32_bf16 %4, %9, %15, %4\n\tv_exp_f32 %5, %5\n\tv_add_f32 %6, 1.0, %6\n\tv_mfma_f32_16x16x32_bf16 %7, %9, %16, %7\n\tv_add_f32 %8, 1.0, %8"
      : "+v"(ac[0][0]), "=&v"(pu[3]), "+v"(ac[0][1]), "+v"(ze[2]), "+v"(ac[0][2]), "+v"(ze[3]), "+v"(ze[0]), "+v"(ac[0][3]), "+v"(ze[1]) : "v"(pu[4]), "v"(fw2[12][0]), "v"(vFj[3]), "s"(rsW), "s"(su[9]), "v"(fw2[12][1]), "v"(fw2[12][2]), "v"(fw2[12][3]) : "memory"); SB();
  WAITVB(11);
  SB();
  asm volatile("v_mfma_f32_16x16x32_bf16 %0, %9, %10, %0\n\tbuffer_load_dwordx4 %1, %11, %12, %13 offen offset:0 sc1\n\tv_mfma_f32_16x16x32_bf16 %2, %9, %14, %2\n\tv_add_f32 %3, 1.0, %3\n\tv_add_f32 %4, 1.0, %4\n\tv_mfma_f32_16x16x32_bf16 %5, %9, %15, %5\n\tv_rcp_f32 %6, %6\n\tv_mfma_f32_16x16x32_bf16 %7, %9, %16, %7\n\tv_rcp_f32 %8, %8"
      : "+v"(ac[0][0]), "=&v"(pu[4]), "+v"(ac[0][1]), "+v"(ze[2]), "+v"(ze[3]), "+v"(ac[0][2]), "+v"(ze[0]), "+v"(ac[0][3]), "+v"(ze[1]) : "v"(pu[5]), "v"(fw2[13][0]), "v"(vFj[3]), "s"(rsW), "s"(su[10]), "v"(fw2[13][1]), "v"(fw2[13][2]), "v"(fw2[13][3]) : "memory"); SB();
  STAMP(42);
  WAITVB(10);
  SB();
  asm volatile("v_mfma_f32_16x16x32_bf16 %0, %9, %10, 0\n\tbuffer_load_dwordx4 %1, %11, %12, %13 offen offset:0 sc1\n\tv_mfma_f32_16x16x32_bf16 %2, %9, %14, 0\n\tv_rcp_f32 %3, %3\n\tv_mfma_f32_16x16x32_bf16 %4, %9, %15, 0\n\tv_rcp_f32 %5, %5\n\tv_mul_f32 %6, %16, %17\n\tv_mfma_f32_16x16x32_bf16 %7, %9, %18, 0\n\tv_mul_f32 %8, %19, %20"
      : "=&v"(ac[1][0]), "=&v"(pu[5]), "=&v"(ac[1][1]), "+v"(ze[2]), "=&v"(ac[1][2]), "+v"(ze[3]), "=&v"(ge[4]), "=&v"(ac[1][3]), "=&v"(ge[5]) : "v"(pu[6]), "a"(fw2[0][0]), "v"(vFj[3]), "s"(rsW), "s"(su[11]), "a"(fw2[0][1]), "a"(fw2[0][2]), "v"(ye[0]), "v"(ze[0]), "a"(fw2[0][3]), "v"(ye[1]), "v"(ze[1]) : "memory"); SB();
  WAITVB(10);
  SB();
  asm volatile("v_mfma_f32_16x16x32_bf16 %0, %9, %10, %0\n\tbuffer_load_dwordx4 %1, %11, %12, %13 offen offset:0 sc1\n\tv_mfma_f32_16x16x32_bf16 %2, %9, %14, %2\n\tds_write_b128 %15, %16 offset:0\n\tv_mfma_f32_16x16x32_bf16 %3, %9, %17, %3\n\tv_mul_f32 %4, %18, %19\n\tv_mul_f32 %5, %20, %21\n\tds_read_b128 %6, %22 offset:21504\n\tv_mfma_f32_16x16x32_bf16 %7, %9, %23, %7\n\tds_read_b128 %8, %22 offset:29696"
      : "+v"(ac[1][0]), "=&v"(pu[6]), "+v"(ac[1][1]), "+v"(ac[1][2]), "=&v"(ge[6]), "=&v"(ge[7]), "=&a"(fwo[22]), "+v"(ac[1][3]), "=&a"(fwo[23]) : "v"(pu[7]), "a"(fw2[1][0]), "v"(vFj[3]), "s"(rsW), "s"(su[12]), "a"(fw2[1][1]), "v"(rwd), "v"(ac[0][0]), "a"(fw2[1][2]), "v"(ye[2]), "v"(ze[2]), "v"(ye[3]), "v"(ze[3]), "v"(ro), "a"(fw2[1][3]) : "memory"); SB();
  WAITVB(10);
  SB();
  asm volatile("v_mfma_f32_16x16x32_bf16 %0, %9, %10, %0\n\tbuffer_load_dwordx4 %1, %11, %12, %13 offen offset:0 sc1\n\tv_mfma_f32_16x16x32_bf16 %2, %9, %14, %2\n\tds_write_b128 %15, %16 offset:1024\n\tv_mfma_f32_16x16x32_bf16 %3, %9, %17, %3\n\tds_read_b128 %4, %18 offset:6144\n\tds_read_b128 %5, %18 offset:14336\n\tds_read_b128 %6, %18 offset:22528\n\tv_mfma_f32_16x16x32_bf16 %7, %9, %19, %7\n\tds_read_b128 %8, %18 offset:30720"
      : "+v"(ac[1][0]), "=&v"(pu[7]), "+v"(ac[1][1]), "+v"(ac[1][2]), "=&a"(fwo[24]), "=&a"(fwo[25]), "=&a"(fwo[26]), "+v"(ac[1][3]), "=&a"(fwo[27]) : "v"(pu[8]), "a"(fw2[2][0]), "v"(vFj[3]), "s"(rsW), "s"(su[13]), "a"(fw2[2][1]), "v"(rwd), "v"(ac[0][1]), "a"(fw2[2][2]), "v"(ro), "a"(fw2[2][3]) : "memory"); SB();
  WAITVB(10);
  SB();
  asm volatile("v_mfma_f32_16x16x32_bf16 %0, %1, %2, %0"
      : "+v"(ac[1][0]) : "v"(pu[9]), "a"(fw2[3][0])); SB();
  WAITV1(14, fo);
  if (__ballot(fo != nonce) != 0ull) poll1<4>(rsW, vFO, nonce);
  STAMP(18);
  asm volatile("buffer_load_dwordx4 %0, %8, %9, %10 offen offset:0 sc1\n\tv_mfma_f32_16x16x32_bf16 %1, %11, %12, %1\n\tds_write_b128 %13, %14 offset:2048\n\tv_mfma_f32_16x16x32_bf16 %2, %11, %15, %2\n\tds_read_b128 %3, %16 offset:7168\n\tds_read_b128 %4, %16 offset:15360\n\tds_read_b128 %5, %16 offset:23552\n\tv_mfma_f32_16x16x32_bf16 %6, %11, %17, %6\n\tds_read_b128 %7, %16 offset:31744"
      : "=&v"(pu[8]), "+v"(ac[1][1]), "+v"(ac[1][2]), "=&a"(fwo[28]), "=&a"(fwo[29]), "=&a"(fwo[30]), "+v"(ac[1][3]), "=&a"(fwo[31]) : "v"(vF), "s"(rsW), "s"(so0), "v"(pu[9]), "a"(fw2[3][1]), "v"(rwd), "v"(ac[0][2]), "a"(fw2[3][2]), "v"(ro), "a"(fw2[3][3]) : "memory"); SB();
  WAITVB(10);
  SB();
  asm volatile("v_mfma_f32_16x16x32_bf16 %0, %9, %10, %0\n\tbuffer_load_dwordx4 %1, %11, %12, %13 offen offset:128 sc1\n\tv_mfma_f32_16x16x32_bf16 %2, %9, %14, %2\n\tds_write_b128 %15, %16 offset:3072\n\tv_mfma_f32_16x16x32_bf16 %3, %9, %17, %3\n\tds_read_b128 %4, %18 offset:32768\n\tds_read_b128 %5, %18 offset:38912\n\tds_read_b128 %6, %18 offset:45056\n\tv_mfma_f32_16x16x32_bf16 %7, %9, %19, %7\n\tds_read_b128 %8, %18 offset:51200"
      : "+v"(ac[1][0]), "=&v"(pu[9]), "+v"(ac[1][1]), "+v"(ac[1][2]), "=&a"(fwo[32]), "=&a"(fwo[33]), "=&a"(fwo[34]), "+v"(ac[1][3]), "=&a"(fwo[35]) : "v"(pu[10]), "a"(fw2[4][0]), "v"(vF), "s"(rsW), "s"(so0), "a"(fw2[4][1]), "v"(rwd), "v"(ac[0][3]), "a"(fw2[4][2]), "v"(ro), "a"(fw2[4][3]) : "memory"); SB();
  WAITVB(10);
  SB();
  asm volatile("v_mfma_f32_16x16x32_bf16 %0, %3, %4, %0\n\tbuffer_load_dwordx4 %1, %5, %6, %7 offen offset:256 sc1\n\tv_mfma_f32_16x16x32_bf16 %2, %3, %8, %2"
      : "+v"(ac[1][0]), "=&v"(pu[10]), "+v"(ac[1][1]) : "v"(pu[11]), "a"(fw2[5][0]), "v"(vF), "s"(rsW), "s"(so0), "a"(fw2[5][1]) : "memory"); SB();
  BAR();
  SB();
  asm volatile("v_mfma_f32_16x16x32_bf16 %0, %6, %7, %0\n\tds_read_b128 %1, %8 offset:0\n\tds_read_b128 %2, %8 offset:10240\n\tds_read_b128 %3, %8 offset:20480\n\tv_mfma_f32_16x16x32_bf16 %4, %6, %9, %4\n\tds_read_b128 %5, %8 offset:30720"
      : "+v"(ac[1][2]), "=&v"(py[2][0]), "=&v"(py[2][1]), "=&v"(py[2][2]), "+v"(ac[1][3]), "=&v"(py[2][3]) : "v"(pu[11]), "a"(fw2[5][2]), "v"(rr), "a"(fw2[5][3]) : "memory"); SB();
  WAITVB(10);
  SB();
  asm volatile("v_mfma_f32_16x16x32_bf16 %0, %11, %12, %0\n\tbuffer_load_dwordx4 %1, %13, %14, %15 offen offset:384 sc1\n\tv_mfma_f32_16x16x32_bf16 %2, %11, %16, %2\n\tds_read_b128 %3, %17 offset:33792\n\tds_read_b128 %4, %17 offset:39936\n\tv_mfma_f32_16x16x32_bf16 %5, %11, %18, %5\n\tds_read_b128 %6, %17 offset:46080\n\tds_read_b128 %7, %17 offset:52224\n\tds_read_b128 %8, %17 offset:34816\n\tv_mfma_f32_16x16x32_bf16 %9, %11, %19, %9\n\tds_read_b128 %10, %17 offset:40960"
      : "+v"(ac[1][0]), "=&v"(pu[11]), "+v"(ac[1][1]), "=&a"(fwo[36]), "=&a"(fwo[37]), "+v"(ac[1][2]), "=&a"(fwo[38]), "=&a"(fwo[39]), "=&a"(fwo[40]), "+v"(ac[1][3]), "=&a"(fwo[41]) : "v"(pu[0]), "a"(fw2[6][0]), "v"(vF), "s"(rsW), "s"(so0), "a"(fw2[6][1]), "v"(ro), "a"(fw2[6][2]), "a"(fw2[6][3]) : "memory"); SB();
  WAITVB(10);
  SB();
  asm volatile("v_mfma_f32_16x16x32_bf16 %0, %3, %4, %0\n\tbuffer_load_dwordx4 %1, %5, %6, %7 offen offset:512 sc1\n\tv_mfma_f32_16x16x32_bf16 %2, %3, %8, %2"
      : "+v"(ac[1][0]), "=&v"(pu[0]), "+v"(ac[1][1]) : "v"(pu[1]), "a"(fw2[7][0]), "v"(vF), "s"(rsW), "s"(so0), "a"(fw2[7][1]) : "memory"); SB();
  WAITL(6);
  asm volatile("v_add_f32 %0, %7, %8\n\tv_mfma_f32_16x16x32_bf16 %1, %9, %10, %1\n\tv_add_f32 %2, %11, %8\n\tv_add_f32 %3, %12, %8\n\tv_add_f32 %4, %13, %8\n\tv_mfma_f32_16x16x32_bf16 %5, %9, %14, %5\n\tv_add_f32 %6, %15, %16"
      : "=&v"(ye[0]), "+v"(ac[1][2]), "=&v"(ye[1]), "=&v"(ye[2]), "=&v"(ye[3]), "+v"(ac[1][3]), "=&v"(ze[0]) : "v"(py[2][0][0]), "v"(bias), "v"(pu[1]), "a"(fw2[7][2]), "v"(py[2][0][1]), "v"(py[2][0][2]), "v"(py[2][0][3]), "a"(fw2[7][3]), "v"(py[2][1][0]), "v"(py[2][2][0])); SB();
  WAITVB(10);
  SB();
  asm volatile("v_mfma_f32_16x16x32_bf16 %0, %9, %10, %0\n\tbuffer_load_dwordx4 %1, %11, %12, %13 offen offset:640 sc1\n\tv_mfma_f32_16x16x32_bf16 %2, %9, %14, %2\n\tv_add_f32 %3, %15, %16\n\tv_add_f32 %4, %17, %18\n\tv_mfma_f32_16x16x32_bf16 %5, %9, %19, %5\n\tv_add_f32 %6, %20, %21\n\tv_add_f32 %7, %7, %22\n\tv_add_f32 %8, %8, %23"
      : "+v"(ac[1][0]), "=&v"(pu[1]), "+v"(ac[1][1]), "=&v"(ze[1]), "=&v"(ze[2]), "+v"(ac[1][2]), "=&v"(ze[3]), "+v"(ye[0]), "+v"(ye[1]) : "v"(pu[2]), "a"(fw2[8][0]), "v"(vF), "s"(rsW), "s"(so0), "a"(fw2[8][1]), "v"(py[2][1][1]), "v"(py[2][2][1]), "v"(py[2][1][2]), "v"(py[2][2][2]), "a"(fw2[8][2]), "v"(py[2][1][3]), "v"(py[2][2][3]), "v"(py[2][3][0]), "v"(py[2][3][1]) : "memory"); SB();
  asm volatile("v_mfma_f32_16x16x32_bf16 %0, %2, %3, %0\n\tv_add_f32 %1, %1, %4"
      : "+v"(ac[1][3]), "+v"(ye[2]) : "v"(pu[2]), "a"(fw2[8][3]), "v"(py[2][3][2])); SB();
  WAITVB(10);
  SB();
  asm volatile("v_mfma_f32_16x16x32_bf16 %0, %9, %10, %0\n\tbuffer_load_dwordx4 %1, %11, %12, %13 offen offset:768 sc1\n\tv_mfma_f32_16x16x32_bf16 %2, %9, %14, %2\n\tv_add_f32 %3, %3, %15\n\tv_add_f32 %4, %4, %16\n\tv_mfma_f32_16x16x32_bf16 %5, %9, %17, %5\n\tv_add_f32 %6, %6, %18\n\tv_add_f32 %7, %7, %19\n\tv_add_f32 %3, %3, %20\n\tv_mfma_f32_16x16x32_bf16 %8, %9, %21, %8"
      : "+v"(ac[1][0]), "=&v"(pu[2]), "+v"(ac[1][1]), "+v"(ye[3]), "+v"(ye[0]), "+v"(ac[1][2]), "+v"(ye[1]), "+v"(ye[2]), "+v"(ac[1][3]) : "v"(pu[3]), "v"(fw2[9][0]), "v"(vF), "s"(rsW), "s"(so0), "v"(fw2[9][1]), "v"(py[2][3][3]), "v"(ze[0]), "v"(fw2[9][2]), "v"(ze[1]), "v"(ze[2]), "v"(ze[3]), "v"(fw2[9][3]) : "memory"); SB();
  asm volatile("v_mul_f32 %0, 0xbfb8aa3b, %1"
      : "=&v"(ze[0]) : "v"(ye[0])); SB();
  WAITVB(10);
  SB();
  asm volatile("v_mfma_f32_16x16x32_bf16 %0, %10, %11, %0\n\tbuffer_load_dwordx4 %1, %12, %13, %14 offen offset:0 sc1\n\tv_mfma_f32_16x16x32_bf16 %2, %10, %15, %2\n\tv_mul_f32 %3, 0xbfb8aa3b, %16\n\tv_mul_f32 %4, 0xbfb8aa3b, %17\n\tv_mfma_f32_16x16x32_bf16 %5, %10, %18, %5\n\tv_mul_f32 %6, 0xbfb8aa3b, %19\n\tv_exp_f32 %7, %7\n\tv_mfma_f32_16x16x32_bf16 %8, %10, %20, %8\n\tds_read_b128 %9, %21 offset:47104"
      : "+v"(ac[1][0]), "=&v"(pu[3]), "+v"(ac[1][1]), "=&v"(ze[1]), "=&v"(ze[2]), "+v"(ac[1][2]), "=&v"(ze[3]), "+v"(ze[0]), "+v"(ac[1][3]), "=&a"(fwo[42]) : "v"(pu[4]), "v"(fw2[10][0]), "v"(vF), "s"(rsW), "s"(so1), "v"(fw2[10][1]), "v"(ye[1]), "v"(ye[2]), "v"(fw2[10][2]), "v"(ye[3]), "v"(fw2[10][3]), "v"(ro) : "memory"); SB();
  WAITVB(10);
  SB();
  asm volatile("v_mfma_f32_16x16x32_bf16 %0, %9, %10, %0\n\tbuffer_load_dwordx4 %1, %11, %12, %13 offen offset:128 sc1\n\tv_mfma_f32_16x16x32_bf16 %2, %9, %14, %2\n\tv_exp_f32 %3, %3\n\tv_mfma_f32_16x16x32_bf16 %4, %9, %15, %4\n\tv_exp_f32 %5, %5\n\tds_read_b128 %6, %16 offset:53248\n\tv_mfma_f32_16x16x32_bf16 %7, %9, %17, %7\n\tds_read_b128 %8, %16 offset:35840"
      : "+v"(ac[1][0]), "=&v"(pu[4]), "+v"(ac[1][1]), "+v"(ze[1]), "+v"(ac[1][2]), "+v"(ze[2]), "=&a"(fwo[43]), "+v"(ac[1][3]), "=&a"(fwo[44]) : "v"(pu[5]), "v"(fw2[11][0]), "v"(vF), "s"(rsW), "s"(so1), "v"(fw2[11][1]), "v"(fw2[11][2]), "v"(ro), "v"(fw2[11][3]) : "memory"); SB();
  WAITVB(10);
  SB();
  asm volatile("v_mfma_f32_16x16x32_bf16 %0, %9, %10, %0\n\tbuffer_load_dwordx4 %1, %11, %12, %13 offen offset:256 sc1\n\tv_mfma_f32_16x16x32_bf16 %2, %9, %14, %2\n\tv_exp_f32 %3, %3\n\tv_mfma_f32_16x16x32_bf16 %4, %9, %15, %4\n\tv_add_f32 %5, 1.0, %5\n\tv_add_f32 %6, 1.0, %6\n\tv_add_f32 %7, 1.0, %7\n\tv_mfma_f32_16x16x32_bf16 %8, %9, %16, %8\n\tv_add_f32 %3, 1.0, %3"
      : "+v"(ac[1][0]), "=&v"(pu[5]), "+v"(ac[1][1]), "+v"(ze[3]), "+v"(ac[1][2]), "+v"(ze[0]), "+v"(ze[1]), "+v"(ze[2]), "+v"(ac[1][3]) : "v"(pu[6]), "v"(fw2[12][0]), "v"(vF), "s"(rsW), "s"(so1), "v"(fw2[12][1]), "v"(fw2[12][2]), "v"(fw2[12][3]) : "memory"); SB();
  WAITVB(10);
  SB();
  asm volatile("v_mfma_f32_16x16x32_bf16 %0, %9, %10, %0\n\tbuffer_load_dwordx4 %1, %11, %12, %13 offen offset:384 sc1\n\tv_mfma_f32_16x16x32_bf16 %2, %9, %14, %2\n\tv_rcp_f32 %3, %3\n\tv_mfma_f32_16x16x32_bf16 %4, %9, %15, %4\n\tv_rcp_f32 %5, %5\n\tds_read_b128 %6, %16 offset:41984\n\tv_mfma_f32_16x16x32_bf16 %7, %9, %17, %7\n\tv_rcp_f32 %8, %8"
      : "+v"(ac[1][0]), "=&v"(pu[6]), "+v"(ac[1][1]), "+v"(ze[0]), "+v"(ac[1][2]), "+v"(ze[1]), "=&a"(fwo[45]), "+v"(ac[1][3]), "+v"(ze[2]) : "v"(pu[7]), "v"(fw2[13][0]), "v"(vF), "s"(rsW), "s"(so1), "v"(fw2[13][1]), "v"(fw2[13][2]), "v"(ro), "v"(fw2[13][3]) : "memory"); SB();
  STAMP(43);
  WAITVB(10);
  SB();
  asm volatile("v_mfma_f32_16x16x32_bf16 %0, %10, %11, 0\n\tbuffer_load_dwordx4 %1, %12, %13, %14 offen offset:512 sc1\n\tv_mfma_f32_16x16x32_bf16 %2, %15, %11, 0\n\tv_rcp_f32 %3, %3\n\tv_mfma_f32_16x16x32_bf16 %4, %16, %11, 0\n\tv_mul_f32 %5, %17, %18\n\tv_mul_f32 %6, %19, %20\n\tv_mul_f32 %7, %21, %22\n\tv_mfma_f32_16x16x32_bf16 %8, %23, %11, 0\n\tv_mul_f32 %9, %24, %3"
      : "=&v"(as[0]), "=&v"(pu[7]), "=&v"(as[1]), "+v"(ze[3]), "=&v"(as[2]), "=&v"(ge[8]), "=&v"(ge[9]), "=&v"(ge[10]), "=&v"(as[3]), "=&v"(ge[11]) : "a"(fwo[0]), "v"(pu[8]), "v"(vF), "s"(rsW), "s"(so1), "a"(fwo[1]), "a"(fwo[2]), "v"(ye[0]), "v"(ze[0]), "v"(ye[1]), "v"(ze[1]), "v"(ye[2]), "v"(ze[2]), "a"(fwo[3]), "v"(ye[3]) : "memory"); SB();
  WAITVB(10);
  SB();
  asm volatile("v_mfma_f32_16x16x32_bf16 %0, %9, %10, %0\n\tbuffer_load_dwordx4 %1, %11, %12, %13 offen offset:640 sc1\n\tv_mfma_f32_16x16x32_bf16 %2, %14, %10, %2\n\tds_write_b128 %15, %16 offset:4096\n\tv_mfma_f32_16x16x32_bf16 %3, %17, %10, %3\n\tds_read_b128 %4, %18 offset:48128\n\tds_read_b128 %5, %18 offset:54272\n\tds_read_b128 %6, %18 offset:36864\n\tv_mfma_f32_16x16x32_bf16 %7, %19, %10, %7\n\tds_read_b128 %8, %18 offset:43008"
      : "+v"(as[0]), "=&v"(pu[8]), "+v"(as[1]), "+v"(as[2]), "=&a"(fwo[46]), "=&a"(fwo[47]), "=&a"(fwo[48]), "+v"(as[3]), "=&a"(fwo[49]) : "a"(fwo[4]), "v"(pu[9]), "v"(vF), "s"(rsW), "s"(so1), "a"(fwo[5]), "v"(rwd), "v"(ac[1][0]), "a"(fwo[6]), "v"(ro), "a"(fwo[7]) : "memory"); SB();
  WAITVB(10);
  SB();
  asm volatile("v_mfma_f32_16x16x32_bf16 %0, %9, %10, %0\n\tbuffer_load_dwordx4 %1, %11, %12, %13 offen offset:768 sc1\n\tv_mfma_f32_16x16x32_bf16 %2, %14, %10, %2\n\tds_write_b128 %15, %16 offset:5120\n\tv_mfma_f32_16x16x32_bf16 %3, %17, %10, %3\n\tds_read_b128 %4, %18 offset:49152\n\tds_read_b128 %5, %18 offset:55296\n\tds_read_b128 %6, %18 offset:37888\n\tv_mfma_f32_16x16x32_bf16 %7, %19, %10, %7\n\tds_read_b128 %8, %18 offset:44032"
      : "+v"(as[0]), "=&v"(pu[9]), "+v"(as[1]), "+v"(as[2]), "=&a"(fwo[50]), "=&a"(fwo[51]), "=&a"(fwo[52]), "+v"(as[3]), "=&a"(fwo[53]) : "a"(fwo[8]), "v"(pu[10]), "v"(vF), "s"(rsW), "s"(so1), "a"(fwo[9]), "v"(rwd), "v"(ac[1][1]), "a"(fwo[10]), "v"(ro), "a"(fwo[11]) : "memory"); SB();
  WAITVB(10);
  SB();
  asm volatile("v_mfma_f32_16x16x32_bf16 %0, %6, %7, %0\n\tds_read_b128 %1, %8 offset:50176\n\tds_read_b128 %2, %8 offset:56320\n\tv_mfma_f32_16x16x32_bf16 %3, %9, %7, %3\n\tds_write_b128 %10, %11 offset:6144\n\tv_mfma_f32_16x16x32_bf16 %4, %12, %7, %4\n\tv_mfma_f32_16x16x32_bf16 %5, %13, %7, %5"
      : "+v"(as[0]), "=&a"(fwo[54]), "=&a"(fwo[55]), "+v"(as[1]), "+v"(as[2]), "+v"(as[3]) : "a"(fwo[12]), "v"(pu[11]), "v"(ro), "a"(fwo[13]), "v"(rwd), "v"(ac[1][2]), "a"(fwo[14]), "a"(fwo[15]) : "memory"); SB();
  WAITVB(9);
  SB();
  asm volatile("v_mfma_f32_16x16x32_bf16 %0, %4, %5, %0\n\tv_mfma_f32_16x16x32_bf16 %1, %6, %5, %1\n\tds_write_b128 %7, %8 offset:7168\n\tv_mfma_f32_16x16x32_bf16 %2, %9, %5, %2\n\tv_mfma_f32_16x16x32_bf16 %3, %10, %5, %3"
      : "+v"(as[0]), "+v"(as[1]), "+v"(as[2]), "+v"(as[3]) : "a"(fwo[16]), "v"(pu[0]), "a"(fwo[17]), "v"(rwd), "v"(ac[1][3]), "a"(fwo[18]), "a"(fwo[19]) : "memory"); SB();
  WAITVB(8);
  SB();
  asm volatile("v_mfma_f32_16x16x32_bf16 %0, %2, %3, %0\n\tv_mfma_f32_16x16x32_bf16 %1, %4, %3, %1"
      : "+v"(as[0]), "+v"(as[1]) : "a"(fwo[20]), "v"(pu[1]), "a"(fwo[21])); SB();
  BAR();
  SB();
  asm volatile("v_mfma_f32_16x16x32_bf16 %0, %5, %6, %0\n\tds_read_b128 %1, %7 offset:4096\n\tds_read_b128 %2, %7 offset:14336\n\tds_read_b128 %3, %7 offset:24576\n\tv_mfma_f32_16x16x32_bf16 %4, %8, %6, %4"
      : "+v"(as[2]), "=&v"(py[3][0]), "=&v"(py[3][1]), "=&v"(py[3][2]), "+v"(as[3]) : "a"(fwo[22]), "v"(pu[1]), "v"(rr), "a"(fwo[23]) : "memory"); SB();
  WAITVB(7);
  SB();
  asm volatile("v_mfma_f32_16x16x32_bf16 %0, %5, %6, %0\n\tds_read_b128 %1, %7 offset:34816\n\tv_mfma_f32_16x16x32_bf16 %2, %8, %6, %2\n\tv_mfma_f32_16x16x32_bf16 %3, %9, %6, %3\n\tv_mfma_f32_16x16x32_bf16 %4, %10, %6, %4"
      : "+v"(as[0]), "=&v"(py[3][3]), "+v"(as[1]), "+v"(as[2]), "+v"(as[3]) : "a"(fwo[24]), "v"(pu[2]), "v"(rr), "a"(fwo[25]), "a"(fwo[26]), "a"(fwo[27]) : "memory"); SB();
  WAITVB(6);
  SB();
  asm volatile("v_mfma_f32_16x16x32_bf16 %0, %2, %3, %0\n\tv_mfma_f32_16x16x32_bf16 %1, %4, %3, %1"
      : "+v"(as[0]), "+v"(as[1]) : "a"(fwo[28]), "v"(pu[3]), "a"(fwo[29])); SB();
  WAITL(0);
  asm volatile("v_add_f32 %0, %6, %7\n\tv_mfma_f32_16x16x32_bf16 %1, %8, %9, %1\n\tv_add_f32 %2, %10, %7\n\tv_add_f32 %3, %11, %7\n\tv_add_f32 %4, %12, %7\n\tv_mfma_f32_16x16x32_bf16 %5, %13, %9, %5"
      : "=&v"(ye[0]), "+v"(as[2]), "=&v"(ye[1]), "=&v"(ye[2]), "=&v"(ye[3]), "+v"(as[3]) : "v"(py[3][0][0]), "v"(bias), "a"(fwo[30]), "v"(pu[3]), "v"(py[3][0][1]), "v"(py[3][0][2]), "v"(py[3][0][3]), "a"(fwo[31])); SB();
  WAITVB(5);
  SB();
  asm volatile("v_mfma_f32_16x16x32_bf16 %0, %9, %10, %0\n\tv_add_f32 %1, %11, %12\n\tv_add_f32 %2, %13, %14\n\tv_mfma_f32_16x16x32_bf16 %3, %15, %10, %3\n\tv_add_f32 %4, %16, %17\n\tv_add_f32 %5, %18, %19\n\tv_mfma_f32_16x16x32_bf16 %6, %20, %10, %6\n\tv_add_f32 %7, %7, %21\n\tv_add_f32 %8, %8, %22"
      : "+v"(as[0]), "=&v"(ze[0]), "=&v"(ze[1]), "+v"(as[1]), "=&v"(ze[2]), "=&v"(ze[3]), "+v"(as[2]), "+v"(ye[0]), "+v"(ye[1]) : "a"(fwo[32]), "v"(pu[4]), "v"(py[3][1][0]), "v"(py[3][2][0]), "v"(py[3][1][1]), "v"(py[3][2][1]), "a"(fwo[33]), "v"(py[3][1][2]), "v"(py[3][2][2]), "v"(py[3][1][3]), "v"(py[3][2][3]), "a"(fwo[34]), "v"(py[3][3][0]), "v"(py[3][3][1])); SB();
  asm volatile("v_add_f32 %0, %0, %3\n\tv_mfma_f32_16x16x32_bf16 %1, %4, %5, %1\n\tv_add_f32 %2, %2, %6"
      : "+v"(ye[2]), "+v"(as[3]), "+v"(ye[3]) : "v"(py[3][3][2]), "a"(fwo[35]), "v"(pu[4]), "v"(py[3][3][3])); SB();
  WAITVB(4);
  SB();
  asm volatile("v_mfma_f32_16x16x32_bf16 %0, %11, %12, %0\n\tv_add_f32 %1, %1, %2\n\tv_add_f32 %3, %3, %4\n\tv_mfma_f32_16x16x32_bf16 %5, %13, %12, %5\n\tv_add_f32 %6, %6, %7\n\tv_add_f32 %8, %8, %14\n\tv_mfma_f32_16x16x32_bf16 %9, %15, %12, %9\n\tv_mul_f32 %2, 0xbfb8aa3b, %1\n\tv_mul_f32 %4, 0xbfb8aa3b, %3\n\tv_mul_f32 %7, 0xbfb8aa3b, %6\n\tv_mfma_f32_16x16x32_bf16 %10, %16, %12, %10"
      : "+v"(as[0]), "+v"(ye[0]), "+v"(ze[0]), "+v"(ye[1]), "+v"(ze[1]), "+v"(as[1]), "+v"(ye[2]), "+v"(ze[2]), "+v"(ye[3]), "+v"(as[2]), "+v"(as[3]) : "a"(fwo[36]), "v"(pu[5]), "a"(fwo[37]), "v"(ze[3]), "a"(fwo[38]), "a"(fwo[39])); SB();
  WAITVB(3);
  SB();
  asm volatile("v_mfma_f32_16x16x32_bf16 %0, %7, %8, %0\n\tv_mul_f32 %1, 0xbfb8aa3b, %9\n\tv_mfma_f32_16x16x32_bf16 %2, %10, %8, %2\n\tv_exp_f32 %3, %3\n\tv_mfma_f32_16x16x32_bf16 %4, %11, %8, %4\n\tv_exp_f32 %5, %5\n\tv_mfma_f32_16x16x32_bf16 %6, %12, %8, %6"
      : "+v"(as[0]), "=&v"(ze[3]), "+v"(as[1]), "+v"(ze[0]), "+v"(as[2]), "+v"(ze[1]), "+v"(as[3]) : "a"(fwo[40]), "v"(pu[6]), "v"(ye[3]), "a"(fwo[41]), "a"(fwo[42]), "a"(fwo[43])); SB();
  WAITVB(2);
  SB();
  asm volatile("v_mfma_f32_16x16x32_bf16 %0, %8, %9, %0\n\tv_exp_f32 %1, %1\n\tv_mfma_f32_16x16x32_bf16 %2, %10, %9, %2\n\tv_exp_f32 %3, %3\n\tv_mfma_f32_16x16x32_bf16 %4, %11, %9, %4\n\tv_add_f32 %5, 1.0, %5\n\tv_add_f32 %6, 1.0, %6\n\tv_add_f32 %1, 1.0, %1\n\tv_mfma_f32_16x16x32_bf16 %7, %12, %9, %7"
      : "+v"(as[0]), "+v"(ze[2]), "+v"(as[1]), "+v"(ze[3]), "+v"(as[2]), "+v"(ze[0]), "+v"(ze[1]), "+v"(as[3]) : "a"(fwo[44]), "v"(pu[7]), "a"(fwo[45]), "a"(fwo[46]), "a"(fwo[47])); SB();
  WAITVB(1);
  SB();
  asm volatile("v_mfma_f32_16x16x32_bf16 %0, %7, %8, %0\n\tv_add_f32 %1, 1.0, %1\n\tv_mfma_f32_16x16x32_bf16 %2, %9, %8, %2\n\tv_rcp_f32 %3, %3\n\tv_mfma_f32_16x16x32_bf16 %4, %10, %8, %4\n\tv_rcp_f32 %5, %5\n\tv_mfma_f32_16x16x32_bf16 %6, %11, %8, %6"
      : "+v"(as[0]), "+v"(ze[3]), "+v"(as[1]), "+v"(ze[0]), "+v"(as[2]), "+v"(ze[1]), "+v"(as[3]) : "a"(fwo[48]), "v"(pu[8]), "a"(fwo[49]), "a"(fwo[50]), "a"(fwo[51])); SB();
  WAITVB(0);
  SB();
  asm volatile("v_mfma_f32_16x16x32_bf16 %0, %10, %11, %0\n\tv_rcp_f32 %1, %1\n\tv_mfma_f32_16x16x32_bf16 %2, %12, %11, %2\n\tv_rcp_f32 %3, %3\n\tv_mfma_f32_16x16x32_bf16 %4, %13, %11, %4\n\tv_mul_f32 %5, %14, %15\n\tv_mul_f32 %6, %16, %17\n\tv_mul_f32 %7, %18, %1\n\tv_mfma_f32_16x16x32_bf16 %8, %19, %11, %8\n\tv_mul_f32 %9, %20, %3"
      : "+v"(as[0]), "+v"(ze[2]), "+v"(as[1]), "+v"(ze[3]), "+v"(as[2]), "=&v"(ge[12]), "=&v"(ge[13]), "=&v"(ge[14]), "+v"(as[3]), "=&v"(ge[15]) : "a"(fwo[52]), "v"(pu[9]), "a"(fwo[53]), "a"(fwo[54]), "v"(ye[0]), "v"(ze[0]), "v"(ye[1]), "v"(ze[1]), "v"(ye[2]), "a"(fwo[55]), "v"(ye[3])); SB();
  STAMP(44);
  asm volatile("s_nop 4"
      :  : ); SB();
  u32x4 rsO_ = srd(out, 2048u * 896u); OPQ(rsO_);
  asm volatile("v_fma_f32 %0, %9, %10, %0\n\tv_fma_f32 %1, %11, %12, %1\n\tv_fma_f32 %2, %13, %14, %2\n\tv_fma_f32 %3, %15, %16, %3\n\tv_cvt_pk_bf16_f32 %4, %0, %1\n\tv_cvt_pk_bf16_f32 %5, %2, %3\n\tv_fma_f32 %6, %17, %18, %6\n\tv_fma_f32 %7, %19, %20, %7\n\tv_fma_f32 %8, %21, %22, %8"
      : "+v"(xe[0]), "+v"(xe[1]), "+v"(xe[2]), "+v"(xe[3]), "=&v"(ov[0][0]), "=&v"(ov[0][1]), "+v"(xe[4]), "+v"(xe[5]), "+v"(xe[6]) : "v"(as[0][0]), "v"(ge[0]), "v"(as[0][1]), "v"(ge[1]), "v"(as[0][2]), "v"(ge[2]), "v"(as[0][3]), "v"(ge[3]), "v"(as[1][0]), "v"(ge[4]), "v"(as[1][1]), "v"(ge[5]), "v"(as[1][2]), "v"(ge[6])); SB();
  asm volatile("v_fma_f32 %0, %9, %10, %0\n\tv_cvt_pk_bf16_f32 %1, %11, %12\n\tv_cvt_pk_bf16_f32 %2, %13, %0\n\tds_write_b64 %14, %15 offset:0\n\tv_fma_f32 %3, %16, %17, %3\n\tv_fma_f32 %4, %18, %19, %4\n\tv_fma_f32 %5, %20, %21, %5\n\tv_fma_f32 %6, %22, %23, %6\n\tv_cvt_pk_bf16_f32 %7, %3, %4\n\tv_cvt_pk_bf16_f32 %8, %5, %6"
      : "+v"(xe[7]), "=&v"(ov[1][0]), "=&v"(ov[1][1]), "+v"(xe[8]), "+v"(xe[9]), "+v"(xe[10]), "+v"(xe[11]), "=&v"(ov[2][0]), "=&v"(ov[2][1]) : "v"(as[1][3]), "v"(ge[7]), "v"(xe[4]), "v"(xe[5]), "v"(xe[6]), "v"(xta[0]), "v"(ov[0]), "v"(as[2][0]), "v"(ge[8]), "v"(as[2][1]), "v"(ge[9]), "v"(as[2][2]), "v"(ge[10]), "v"(as[2][3]), "v"(ge[11]) : "memory"); SB();
  asm volatile("ds_write_b64 %6, %7 offset:0\n\tv_fma_f32 %0, %8, %9, %0\n\tv_fma_f32 %1, %10, %11, %1\n\tv_fma_f32 %2, %12, %13, %2\n\tv_fma_f32 %3, %14, %15, %3\n\tv_cvt_pk_bf16_f32 %4, %0, %1\n\tv_cvt_pk_bf16_f32 %5, %2, %3\n\tds_write_b64 %16, %17 offset:0"
      : "+v"(xe[12]), "+v"(xe[13]), "+v"(xe[14]), "+v"(xe[15]), "=&v"(ov[3][0]), "=&v"(ov[3][1]) : "v"(xta[1]), "v"(ov[1]), "v"(as[3][0]), "v"(ge[12]), "v"(as[3][1]), "v"(ge[13]), "v"(as[3][2]), "v"(ge[14]), "v"(as[3][3]), "v"(ge[15]), "v"(xta[2]), "v"(ov[2]) : "memory"); SB();
  asm volatile("ds_write_b64 %0, %1 offset:0"
      :  : "v"(xta[3]), "v"(ov[3]) : "memory"); SB();
  { u32x4 t0_, t1_; DSR16(t0_, xo, 0); DSR16(t1_, xo, 1024); WAITLB(1);
    BS16(vXa, rsO_, 0, t0_, "sc1"); WAITLB(0); BS16(vXb, rsO_, 1024, t1_, "sc1"); }
  STAMP(23);

  STAMP(12);
  STAMP_DUMP();
  }
  } else if (jb < 28) {
  asm volatile("; NEED B");
  u32x4 rs1 = srd(w1, 2048u * 3584u);
  STAMP(1);
  unsigned nonce; asm volatile("s_load_dword %0, %1, 0x0" : "=&s"(nonce) : "s"(ws) : "memory");
  const unsigned trb = (8 * g4 + q4) * 128 + pq * 8, trf = ((q4 >> 1) | ((g4 & 1) << 1)) * 32;
  const unsigned vF = r8 * 1024 + (lane & 7) * 16;
  unsigned tX[4];
#pragma unroll
  for (int i = 0; i < 4; ++i) { tX[i] = ldsR + trb + ((i * 32 ^ w * 32) ^ trf); OPV(tX[i]); }
  bf16x8 fw1[16][4], p[14]; u32x2 bv; unsigned sh[16], sh0 = WS_HT + d4 * 524288; OPQ(sh0);
  asm volatile("s_waitcnt lgkmcnt(0)" : "+s"(nonce) :: "memory"); nonce += 1u;
  asm volatile("s_waitcnt vmcnt(8)"
      :  :  : "memory"); SB();
  fw1[0][0] = tr8u(tX[0] + 0, 512);
  fw1[0][1] = tr8u(tX[1] + 0, 512);
  fw1[0][2] = tr8u(tX[2] + 0, 512);
  fw1[0][3] = tr8u(tX[3] + 0, 512);
  STAMP(50);
  { int k_ = KS1(3); OPQ(k_); DMA4(vW1a, vW1b, rs1, k_ * 114688, 55296, ldsR, 12288); sh[3] = sh0 + k_ * 8192; OPQ(sh[3]); }
  SB();
  { const unsigned vB = (ft * 64 + w * 16 + g4 * 4) * 2;
    LD8(bv, vB, b1, 0); }
  u32x4 rsW = srd((char*)(((size_t)ws + 8191) & ~(size_t)4095) + 512, WS_END); OPQ(rsW);
  #pragma unroll
  for (int c = 0; c < 3; ++c) { sh[c] = sh0 + KS1(c) * 8192; OPQ(sh[c]); }
  unsigned fh, vFH = WS_FA + (d4 * 128 + w * 32 + (lane & 31)) * (4 * 16);
  unsigned rw = lds0 + w * SCW + vL16, rr1 = lds0 + (w ^ 1) * SCW + vL16, rr2 = lds0 + (w ^ 2) * SCW + vL16, rr3 = lds0 + (w ^ 3) * SCW + vL16;
  unsigned vU = (g4 & 1) * 128 + ((g4 >> 1) * 2 + (n16 >> 3)) * 1024 + (n16 & 7) * 16, sU, sFU = WS_FU + (d4 * 128 + ft * 4 + w) * (4 * 4);
  sU = WS_UT + (d4 * 56 + 2 * ft) * 8192 + w * 4096;
  float gc = 0.10294324f;
  asm volatile("" : "+v"(vFH), "+v"(rw), "+v"(rr1), "+v"(rr2), "+v"(rr3), "+v"(vU), "+v"(gc)); asm volatile("" : "+s"(sU), "+s"(sFU));
  const int r2 = (3 - (int)((size_t)w2 >> 7)) & 7;
  const int st = 8 * xq + ((r2 + 1 + fi) & 7), dt = d4, db0 = 16 + 3 * dt;
#undef BLK
#define BLK(j) (db0 + ((j) == 2 ? 2 - 2 * (dt & 1) : (j) + (dt & 1)))
  SB();
  asm volatile("s_waitcnt vmcnt(9)"
      :  :  : "memory"); SB();
  fw1[1][0] = tr8u(tX[0] + 4096, 512);
  fw1[1][1] = tr8u(tX[1] + 4096, 512);
  fw1[1][2] = tr8u(tX[2] + 4096, 512);
  fw1[1][3] = tr8u(tX[3] + 4096, 512);
  { int k_ = KS1(4); OPQ(k_); DMA4(vW1a, vW1b, rs1, k_ * 114688, 55296, ldsR, 16384); sh[4] = sh0 + k_ * 8192; OPQ(sh[4]); }
  SB();
  u32x4 rs2 = srd(w2, 1792u * 4096u); OPQ(rs2);
  unsigned vW2[4];
#pragma unroll
  for (int i = 0; i < 4; ++i) vW2[i] = (8 * (r8 & 3) + 4 * (r8 >> 2)) * 4096 + st * 128 + ((lane & 7) ^ (2 * i)) * 16 + i * (4096 - 1024);
  int rot2 = 4 * dt + 2 * (st & 7) + 2; rot2 -= 14 * ((rot2 * 37) >> 9);
  bf16x8 fw2[14][4];
  unsigned ldsS = lds0 + S3B + w * 4096, ldsO = lds0 + WOB + w * 8192, ldsP = lds0 + WOB + 24576 + w * 6144, sWo, vWo = r8 * 16384 + (lane & 7) * 16, sWt;
  { const int k_ = BLK(w), k7 = (k_ * 37) >> 8; sWo = WS_WO + k7 * (131072 - 896) + k_ * 128; }
  unsigned fq, vFQ = WS_FQ + (lane < 56 ? lane : 55) * (4 * 4);
  asm volatile("" : "+v"(vW2[0]), "+v"(vW2[1]), "+v"(vW2[2]), "+v"(vW2[3]), "+v"(vFQ), "+v"(vWo)); asm volatile("" : "+s"(rot2), "+s"(ldsS), "+s"(ldsO), "+s"(ldsP), "+s"(sWo));
  unsigned long long mWo = w < 3 ? ~0ull : 1ull; OPQ(mWo);
  unsigned tS[4];
#pragma unroll
  for (int i = 0; i < 4; ++i) { tS[i] = ldsS + q4 * 1024 + g4 * 128 + pq * 8 + ((i ^ q4) * 32); OPV(tS[i]); }
  unsigned tO[4];
#pragma unroll
  for (int i = 0; i < 4; ++i) { tO[i] = ldsO + q4 * 1024 + g4 * 128 + pq * 8 + ((i ^ q4) * 32); OPV(tO[i]); }
  unsigned su[14], sw[14], sc_ = 14 * w + rot2; OPQ(sc_);
  SB();
  asm volatile("s_waitcnt vmcnt(9)"
      :  :  : "memory"); SB();
  fw1[2][0] = tr8u(tX[0] + 8192, 512);
  fw1[2][1] = tr8u(tX[1] + 8192, 512);
  fw1[2][2] = tr8u(tX[2] + 8192, 512);
  fw1[2][3] = tr8u(tX[3] + 8192, 512);
  { int k_ = KS1(5); OPQ(k_); DMA4(vW1a, vW1b, rs1, k_ * 114688, 55296, ldsR, 20480); sh[5] = sh0 + k_ * 8192; OPQ(sh[5]); }
  SB();
  unsigned vFU, vFO;
  { const int da = (db0 * 37) >> 8, db = ((db0 + 2) * 37) >> 8, l_ = lane < 56 ? lane : 55;
    vFU = WS_FU + ((l_ < 28 ? da * 128 + l_ : db * 128 + l_ - 28) + w * 28) * (4 * 4);
    vFO = WS_FO + ((2 * st + (w >> 1)) * 8 + (lane & 7)) * (4 * 16);
    asm volatile("" : "+v"(vFU), "+v"(vFO)); }
  unsigned vFj[4], so0 = WS_OT + (4 * st + w) * 16384, so1 = so0 + 8192;
#pragma unroll
  for (int j = 0; j < 3; ++j) { const int k_ = BLK(j), k7 = (k_ * 37) >> 8; vFj[j] = vF + (WS_UT + k7 * (56 * 8192 - 896) + k_ * 128); OPV(vFj[j]); }
  asm volatile("" : "+s"(so0), "+s"(so1));
  SB();
  asm volatile("s_waitcnt vmcnt(9)"
      :  :  : "memory"); SB();
  fw1[3][0] = tr8u(tX[0] + 12288, 512);
  fw1[3][1] = tr8u(tX[1] + 12288, 512);
  fw1[3][2] = tr8u(tX[2] + 12288, 512);
  fw1[3][3] = tr8u(tX[3] + 12288, 512);
  STAMP(51);
  { int k_ = KS1(6); OPQ(k_); DMA4(vW1a, vW1b, rs1, k_ * 114688, 55296, ldsR, 24576); sh[6] = sh0 + k_ * 8192; OPQ(sh[6]); }
  SB();
  const int tau = 2 * (n16 & 7) + (n16 >> 3);
  unsigned rwd = lds0 + w * SCW + vL16, rr = lds0 + w * 1024 + (16 * g4 + tau) * 16, ro = lds0 + WOB + vL16;
  unsigned xo = ldsS + 34816 + vL16, braw, xt = ldsS + 34816 + tau * 128 + (((g4 >> 1) ^ (n16 & 7)) * 16) + (g4 & 1) * 8, vB2 = (64 * st + 16 * w + tau) * 2;
  asm volatile("" : "+v"(rwd), "+v"(rr), "+v"(ro), "+v"(xt), "+v"(xo), "+v"(vB2));
  SB();
  asm volatile("s_waitcnt vmcnt(8)"
      :  :  : "memory"); SB();
  fw1[4][0] = tr8u(tX[0] + 16384, 512);
  fw1[4][1] = tr8u(tX[1] + 16384, 512);
  fw1[4][2] = tr8u(tX[2] + 16384, 512);
  fw1[4][3] = tr8u(tX[3] + 16384, 512);
  WAITL(8);
  { int k_ = KS1(7); OPQ(k_); DMA4(vW1a, vW1b, rs1, k_ * 114688, 55296, ldsR, 0); sh[7] = sh0 + k_ * 8192; OPQ(sh[7]); }
  SB();
  unsigned vXa, vXb;
  { const int ga = (lane & 7) ^ (r8 >> 1), gb = ga ^ 4, r_ = (64 * st + 16 * w + r8) * 896;
    vXa = ga < 4 ? r_ + BLK(0) * 32 + ga * 16 : ga < 6 ? r_ + BLK(2) * 32 + (ga - 4) * 16 : 0x40000000;
    vXb = (gb < 4 ? r_ + BLK(0) * 32 + gb * 16 : gb < 6 ? r_ + BLK(2) * 32 + (gb - 4) * 16 : 0x40000000) + (8 * 896 - 1024); OPV(vXa); OPV(vXb); }
  SB();
  asm volatile("s_waitcnt vmcnt(8)"
      :  :  : "memory"); SB();
  fw1[5][0] = tr8u(tX[0] + 20480, 512);
  fw1[5][1] = tr8u(tX[1] + 20480, 512);
  fw1[5][2] = tr8u(tX[2] + 20480, 512);
  fw1[5][3] = tr8u(tX[3] + 20480, 512);
  WAITL(8);
  { int k_ = KS1(8); OPQ(k_); DMA4(vW1a, vW1b, rs1, k_ * 114688, 55296, ldsR, 4096); sh[8] = sh0 + k_ * 8192; OPQ(sh[8]); }
  SB();
  { unsigned k_ = sc_ + (rot2 >= 14 ? -14 : 0); OPQ(k_); su[0] = k_ << 13; sw[0] = k_ << 17; OPQ(su[0]); OPQ(sw[0]); }
  { unsigned k_ = sc_ + (rot2 >= 13 ? -13 : 1); OPQ(k_); su[1] = k_ << 13; sw[1] = k_ << 17; OPQ(su[1]); OPQ(sw[1]); }
  { unsigned k_ = sc_ + (rot2 >= 12 ? -12 : 2); OPQ(k_); su[2] = k_ << 13; sw[2] = k_ << 17; OPQ(su[2]); OPQ(sw[2]); }
  SB();
  asm volatile("s_waitcnt vmcnt(8)"
      :  :  : "memory"); SB();
  fw1[6][0] = tr8u(tX[0] + 24576, 512);
  fw1[6][1] = tr8u(tX[1] + 24576, 512);
  fw1[6][2] = tr8u(tX[2] + 24576, 512);
  fw1[6][3] = tr8u(tX[3] + 24576, 512);
  WAITV1(20, bv);
  f32x4 bb = {lo2f(bv[0]), hi2f(bv[0]), lo2f(bv[1]), hi2f(bv[1])}; OPV(bb);
  WAITL(8);
  { int k_ = KS1(9); OPQ(k_); DMA4(vW1a, vW1b, rs1, k_ * 114688, 55296, ldsR, 8192); sh[9] = sh0 + k_ * 8192; OPQ(sh[9]); }
  SB();
  { unsigned k_ = sc_ + (rot2 >= 11 ? -11 : 3); OPQ(k_); su[3] = k_ << 13; sw[3] = k_ << 17; OPQ(su[3]); OPQ(sw[3]); }
  { unsigned k_ = sc_ + (rot2 >= 10 ? -10 : 4); OPQ(k_); su[4] = k_ << 13; sw[4] = k_ << 17; OPQ(su[4]); OPQ(sw[4]); }
  { unsigned k_ = sc_ + (rot2 >= 9 ? -9 : 5); OPQ(k_); su[5] = k_ << 13; sw[5] = k_ << 17; OPQ(su[5]); OPQ(sw[5]); }
  SB();
  asm volatile("s_waitcnt vmcnt(8)"
      :  :  : "memory"); SB();
  fw1[7][0] = tr8u(tX[0] + 0, 512);
  fw1[7][1] = tr8u(tX[1] + 0, 512);
  fw1[7][2] = tr8u(tX[2] + 0, 512);
  fw1[7][3] = tr8u(tX[3] + 0, 512);
  STAMP(52);
  WAITL(8);
  { int k_ = KS1(10); OPQ(k_); DMA4(vW1a, vW1b, rs1, k_ * 114688, 55296, ldsR, 12288); sh[10] = sh0 + k_ * 8192; OPQ(sh[10]); }
  SB();
  { unsigned k_ = sc_ + (rot2 >= 8 ? -8 : 6); OPQ(k_); su[6] = k_ << 13; sw[6] = k_ << 17; OPQ(su[6]); OPQ(sw[6]); }
  { unsigned k_ = sc_ + (rot2 >= 7 ? -7 : 7); OPQ(k_); su[7] = k_ << 13; sw[7] = k_ << 17; OPQ(su[7]); OPQ(sw[7]); }
  { unsigned k_ = sc_ + (rot2 >= 6 ? -6 : 8); OPQ(k_); su[8] = k_ << 13; sw[8] = k_ << 17; OPQ(su[8]); OPQ(sw[8]); }
  SB();
  asm volatile("s_waitcnt vmcnt(8)"
      :  :  : "memory"); SB();
  fw1[8][0] = tr8u(tX[0] + 4096, 512);
  fw1[8][1] = tr8u(tX[1] + 4096, 512);
  fw1[8][2] = tr8u(tX[2] + 4096, 512);
  fw1[8][3] = tr8u(tX[3] + 4096, 512);
  WAITL(8);
  { int k_ = KS1(11); OPQ(k_); DMA4(vW1a, vW1b, rs1, k_ * 114688, 55296, ldsR, 16384); sh[11] = sh0 + k_ * 8192; OPQ(sh[11]); }
  SB();
  { unsigned k_ = sc_ + (rot2 >= 5 ? -5 : 9); OPQ(k_); su[9] = k_ << 13; sw[9] = k_ << 17; OPQ(su[9]); OPQ(sw[9]); }
  { unsigned k_ = sc_ + (rot2 >= 4 ? -4 : 10); OPQ(k_); su[10] = k_ << 13; sw[10] = k_ << 17; OPQ(su[10]); OPQ(sw[10]); }
  { unsigned k_ = sc_ + (rot2 >= 3 ? -3 : 11); OPQ(k_); su[11] = k_ << 13; sw[11] = k_ << 17; OPQ(su[11]); OPQ(sw[11]); }
  SB();
  asm volatile("s_waitcnt vmcnt(8)"
      :  :  : "memory"); SB();
  fw1[9][0] = tr8u(tX[0] + 8192, 512);
  fw1[9][1] = tr8u(tX[1] + 8192, 512);
  fw1[9][2] = tr8u(tX[2] + 8192, 512);
  fw1[9][3] = tr8u(tX[3] + 8192, 512);
  WAITL(8);
  { int k_ = KS1(12); OPQ(k_); DMA4(vW1a, vW1b, rs1, k_ * 114688, 55296, ldsR, 20480); sh[12] = sh0 + k_ * 8192; OPQ(sh[12]); }
  SB();
  { unsigned k_ = sc_ + (rot2 >= 2 ? -2 : 12); OPQ(k_); su[12] = k_ << 13; sw[12] = k_ << 17; OPQ(su[12]); OPQ(sw[12]); }
  { unsigned k_ = sc_ + (rot2 >= 1 ? -1 : 13); OPQ(k_); su[13] = k_ << 13; sw[13] = k_ << 17; OPQ(su[13]); OPQ(sw[13]); }
  SB();
  asm volatile("s_waitcnt vmcnt(8)"
      :  :  : "memory"); SB();
  fw1[10][0] = tr8u(tX[0] + 12288, 512);
  fw1[10][1] = tr8u(tX[1] + 12288, 512);
  fw1[10][2] = tr8u(tX[2] + 12288, 512);
  fw1[10][3] = tr8u(tX[3] + 12288, 512);
  WAITL(8);
  { int k_ = KS1(13); OPQ(k_); DMA4(vW1a, vW1b, rs1, k_ * 114688, 55296, ldsR, 24576); sh[13] = sh0 + k_ * 8192; OPQ(sh[13]); }
  asm volatile("s_waitcnt vmcnt(8)"
      :  :  : "memory"); SB();
  fw1[11][0] = tr8u(tX[0] + 16384, 512);
  fw1[11][1] = tr8u(tX[1] + 16384, 512);
  fw1[11][2] = tr8u(tX[2] + 16384, 512);
  fw1[11][3] = tr8u(tX[3] + 16384, 512);
  STAMP(53);
  WAITL(8);
  { int k_ = KS1(14); OPQ(k_); DMA4(vW1a, vW1b, rs1, k_ * 114688, 55296, ldsR, 0); sh[14] = sh0 + k_ * 8192; OPQ(sh[14]); }
  asm volatile("s_waitcnt vmcnt(8)"
      :  :  : "memory"); SB();
  fw1[12][0] = tr8u(tX[0] + 20480, 512);
  fw1[12][1] = tr8u(tX[1] + 20480, 512);
  fw1[12][2] = tr8u(tX[2] + 20480, 512);
  fw1[12][3] = tr8u(tX[3] + 20480, 512);
  WAITL(8);
  { int k_ = KS1(15); OPQ(k_); DMA4(vW1a, vW1b, rs1, k_ * 114688, 55296, ldsR, 4096); sh[15] = sh0 + k_ * 8192; OPQ(sh[15]); }
  asm volatile("buffer_load_dword %0, %1, %2, 0 offen sc1\n\ts_waitcnt vmcnt(9)"
      : "=&v"(fh) : "v"(vFH), "s"(rsW) : "memory"); SB();
  fw1[13][0] = tr8u(tX[0] + 24576, 512);
  fw1[13][1] = tr8u(tX[1] + 24576, 512);
  fw1[13][2] = tr8u(tX[2] + 24576, 512);
  fw1[13][3] = tr8u(tX[3] + 24576, 512);
  WAITV1(0, fh);
  if (__ballot(fh != nonce) != 0ull) poll1<4>(rsW, vFH, nonce);
  STAMP(6);
  asm volatile("buffer_load_dwordx4 %0, %4, %5, %6 offen offset:0 sc1\n\tbuffer_load_dwordx4 %1, %4, %5, %7 offen offset:0 sc1\n\tbuffer_load_dwordx4 %2, %4, %5, %8 offen offset:0 sc1\n\tbuffer_load_dwordx4 %3, %4, %5, %9 offen offset:0 sc1\n\ts_waitcnt vmcnt(9)"
      : "=&v"(p[0]), "=&v"(p[1]), "=&v"(p[2]), "=&v"(p[3]) : "v"(vF), "s"(rsW), "s"(sh[0]), "s"(sh[1]), "s"(sh[2]), "s"(sh[3]) : "memory"); SB();
  fw1[14][0] = tr8u(tX[0] + 0, 512);
  fw1[14][1] = tr8u(tX[1] + 0, 512);
  fw1[14][2] = tr8u(tX[2] + 0, 512);
  fw1[14][3] = tr8u(tX[3] + 0, 512);
  asm volatile("buffer_load_dwordx4 %0, %4, %5, %6 offen offset:0 sc1\n\tbuffer_load_dwordx4 %1, %4, %5, %7 offen offset:0 sc1\n\tbuffer_load_dwordx4 %2, %4, %5, %8 offen offset:0 sc1\n\tbuffer_load_dwordx4 %3, %4, %5, %9 offen offset:0 sc1\n\ts_waitcnt vmcnt(9)"
      : "=&v"(p[4]), "=&v"(p[5]), "=&v"(p[6]), "=&v"(p[7]) : "v"(vF), "s"(rsW), "s"(sh[4]), "s"(sh[5]), "s"(sh[6]), "s"(sh[7]) : "memory"); SB();
  fw1[15][0] = tr8u(tX[0] + 4096, 512);
  fw1[15][1] = tr8u(tX[1] + 4096, 512);
  fw1[15][2] = tr8u(tX[2] + 4096, 512);
  fw1[15][3] = tr8u(tX[3] + 4096, 512);
  STAMP(5);
  asm volatile("buffer_load_dwordx4 %0, %4, %5, %6 offen offset:0 sc1\n\tbuffer_load_dwordx4 %1, %4, %5, %7 offen offset:0 sc1\n\tbuffer_load_dwordx4 %2, %4, %5, %8 offen offset:0 sc1\n\tbuffer_load_dwordx4 %3, %4, %5, %9 offen offset:0 sc1"
      : "=&v"(p[8]), "=&v"(p[9]), "=&v"(p[10]), "=&v"(p[11]) : "v"(vF), "s"(rsW), "s"(sh[8]), "s"(sh[9]), "s"(sh[10]), "s"(sh[11]) : "memory"); SB();
  f32x4 acc[7][4], pm[7][3]; float gm[4], gz[4]; unsigned uv[8][2];
  WAITVB(11);
  SB();
  asm volatile("v_mfma_f32_16x16x32_bf16 %0, %6, %7, 0\n\tbuffer_load_dwordx4 %1, %8, %9, %10 offen offset:0 sc1\n\tv_mfma_f32_16x16x32_bf16 %2, %11, %7, 0\n\tv_mfma_f32_16x16x32_bf16 %3, %12, %7, 0\n\tbuffer_load_dwordx4 %4, %8, %9, %13 offen offset:0 sc1\n\tv_mfma_f32_16x16x32_bf16 %5, %14, %7, %15"
      : "=&v"(acc[0][1]), "=&v"(p[12]), "=&v"(acc[0][2]), "=&v"(acc[0][3]), "=&v"(p[13]), "=&v"(acc[0][0]) : "a"(fw1[0][1]), "v"(p[0]), "v"(vF), "s"(rsW), "s"(sh[12]), "a"(fw1[0][2]), "a"(fw1[0][3]), "s"(sh[13]), "a"(fw1[0][0]), "v"(bb) : "memory"); SB();
  WAITVB(12);
  SB();
  asm volatile("v_mfma_f32_16x16x32_bf16 %0, %5, %6, %0\n\tbuffer_load_dwordx4 %1, %7, %8, %9 offen offset:0 sc1\n\tv_mfma_f32_16x16x32_bf16 %2, %10, %6, %2\n\tv_mfma_f32_16x16x32_bf16 %3, %11, %6, %3\n\tv_mfma_f32_16x16x32_bf16 %4, %12, %6, %4"
      : "+v"(acc[0][1]), "=&v"(p[0]), "+v"(acc[0][2]), "+v"(acc[0][3]), "+v"(acc[0][0]) : "a"(fw1[1][1]), "v"(p[1]), "v"(vF), "s"(rsW), "s"(sh[14]), "a"(fw1[1][2]), "a"(fw1[1][3]), "a"(fw1[1][0]) : "memory"); SB();
  WAITVB(12);
  SB();
  asm volatile("v_mfma_f32_16x16x32_bf16 %0, %5, %6, %0\n\tbuffer_load_dwordx4 %1, %7, %8, %9 offen offset:0 sc1\n\tv_mfma_f32_16x16x32_bf16 %2, %10, %6, %2\n\tv_mfma_f32_16x16x32_bf16 %3, %11, %6, %3\n\tv_mfma_f32_16x16x32_bf16 %4, %12, %6, %4"
      : "+v"(acc[0][1]), "=&v"(p[1]), "+v"(acc[0][2]), "+v"(acc[0][3]), "+v"(acc[0][0]) : "a"(fw1[2][1]), "v"(p[2]), "v"(vF), "s"(rsW), "s"(sh[15]), "a"(fw1[2][2]), "a"(fw1[2][3]), "a"(fw1[2][0]) : "memory"); SB();
  WAITVB(12);
  SB();
  asm volatile("v_mfma_f32_16x16x32_bf16 %0, %5, %6, %0\n\tbuffer_load_dwordx4 %1, %7, %8, %9 offen offset:128 sc1\n\tv_mfma_f32_16x16x32_bf16 %2, %10, %6, %2\n\tv_mfma_f32_16x16x32_bf16 %3, %11, %6, %3\n\tv_mfma_f32_16x16x32_bf16 %4, %12, %6, %4"
      : "+v"(acc[0][1]), "=&v"(p[2]), "+v"(acc[0][2]), "+v"(acc[0][3]), "+v"(acc[0][0]) : "a"(fw1[3][1]), "v"(p[3]), "v"(vF), "s"(rsW), "s"(sh[0]), "a"(fw1[3][2]), "a"(fw1[3][3]), "a"(fw1[3][0]) : "memory"); SB();
  WAITVB(12);
  SB();
  asm volatile("v_mfma_f32_16x16x32_bf16 %0, %5, %6, %0\n\tbuffer_load_dwordx4 %1, %7, %8, %9 offen offset:128 sc1\n\tv_mfma_f32_16x16x32_bf16 %2, %10, %6, %2\n\tv_mfma_f32_16x16x32_bf16 %3, %11, %6, %3\n\tv_mfma_f32_16x16x32_bf16 %4, %12, %6, %4"
      : "+v"(acc[0][1]), "=&v"(p[3]), "+v"(acc[0][2]), "+v"(acc[0][3]), "+v"(acc[0][0]) : "a"(fw1[4][1]), "v"(p[4]), "v"(vF), "s"(rsW), "s"(sh[1]), "a"(fw1[4][2]), "a"(fw1[4][3]), "a"(fw1[4][0]) : "memory"); SB();
  WAITVB(12);
  SB();
  asm volatile("v_mfma_f32_16x16x32_bf16 %0, %5, %6, %0\n\tbuffer_load_dwordx4 %1, %7, %8, %9 offen offset:128 sc1\n\tv_mfma_f32_16x16x32_bf16 %2, %10, %6, %2\n\tv_mfma_f32_16x16x32_bf16 %3, %11, %6, %3\n\tv_mfma_f32_16x16x32_bf16 %4, %12, %6, %4"
      : "+v"(acc[0][1]), "=&v"(p[4]), "+v"(acc[0][2]), "+v"(acc[0][3]), "+v"(acc[0][0]) : "a"(fw1[5][1]), "v"(p[5]), "v"(vF), "s"(rsW), "s"(sh[2]), "a"(fw1[5][2]), "a"(fw1[5][3]), "a"(fw1[5][0]) : "memory"); SB();
  WAITVB(12);
  SB();
  asm volatile("v_mfma_f32_16x16x32_bf16 %0, %5, %6, %0\n\tbuffer_load_dwordx4 %1, %7, %8, %9 offen offset:128 sc1\n\tv_mfma_f32_16x16x32_bf16 %2, %10, %6, %2\n\tv_mfma_f32_16x16x32_bf16 %3, %11, %6, %3\n\tv_mfma_f32_16x16x32_bf16 %4, %12, %6, %4"
      : "+v"(acc[0][1]), "=&v"(p[5]), "+v"(acc[0][2]), "+v"(acc[0][3]), "+v"(acc[0][0]) : "a"(fw1[6][1]), "v"(p[6]), "v"(vF), "s"(rsW), "s"(sh[3]), "a"(fw1[6][2]), "a"(fw1[6][3]), "a"(fw1[6][0]) : "memory"); SB();
  WAITVB(12);
  SB();
  asm volatile("v_mfma_f32_16x16x32_bf16 %0, %5, %6, %0\n\tbuffer_load_dwordx4 %1, %7, %8, %9 offen offset:128 sc1\n\tv_mfma_f32_16x16x32_bf16 %2, %10, %6, %2\n\tv_mfma_f32_16x16x32_bf16 %3, %11, %6, %3\n\tv_mfma_f32_16x16x32_bf16 %4, %12, %6, %4"
      : "+v"(acc[0][1]), "=&v"(p[6]), "+v"(acc[0][2]), "+v"(acc[0][3]), "+v"(acc[0][0]) : "a"(fw1[7][1]), "v"(p[7]), "v"(vF), "s"(rsW), "s"(sh[4]), "a"(fw1[7][2]), "a"(fw1[7][3]), "a"(fw1[7][0]) : "memory"); SB();
  WAITVB(12);
  SB();
  asm volatile("v_mfma_f32_16x16x32_bf16 %0, %5, %6, %0\n\tbuffer_load_dwordx4 %1, %7, %8, %9 offen offset:128 sc1\n\tv_mfma_f32_16x16x32_bf16 %2, %10, %6, %2\n\tv_mfma_f32_16x16x32_bf16 %3, %11, %6, %3\n\tv_mfma_f32_16x16x32_bf16 %4, %12, %6, %4"
      : "+v"(acc[0][1]), "=&v"(p[7]), "+v"(acc[0][2]), "+v"(acc[0][3]), "+v"(acc[0][0]) : "a"(fw1[8][1]), "v"(p[8]), "v"(vF), "s"(rsW), "s"(sh[5]), "a"(fw1[8][2]), "a"(fw1[8][3]), "a"(fw1[8][0]) : "memory"); SB();
  WAITVB(12);
  SB();
  asm volatile("v_mfma_f32_16x16x32_bf16 %0, %5, %6, %0\n\tbuffer_load_dwordx4 %1, %7, %8, %9 offen offset:128 sc1\n\tv_mfma_f32_16x16x32_bf16 %2, %10, %6, %2\n\tv_mfma_f32_16x16x32_bf16 %3, %11, %6, %3\n\tv_mfma_f32_16x16x32_bf16 %4, %12, %6, %4"
      : "+v"(acc[0][1]), "=&v"(p[8]), "+v"(acc[0][2]), "+v"(acc[0][3]), "+v"(acc[0][0]) : "a"(fw1[9][1]), "v"(p[9]), "v"(vF), "s"(rsW), "s"(sh[6]), "a"(fw1[9][2]), "a"(fw1[9][3]), "a"(fw1[9][0]) : "memory"); SB();
  WAITVB(12);
  SB();
  asm volatile("v_mfma_f32_16x16x32_bf16 %0, %5, %6, %0\n\tbuffer_load_dwordx4 %1, %7, %8, %9 offen offset:128 sc1\n\tv_mfma_f32_16x16x32_bf16 %2, %10, %6, %2\n\tv_mfma_f32_16x16x32_bf16 %3, %11, %6, %3\n\tv_mfma_f32_16x16x32_bf16 %4, %12, %6, %4"
      : "+v"(acc[0][1]), "=&v"(p[9]), "+v"(acc[0][2]), "+v"(acc[0][3]), "+v"(acc[0][0]) : "a"(fw1[10][1]), "v"(p[10]), "v"(vF), "s"(rsW), "s"(sh[7]), "a"(fw1[10][2]), "a"(fw1[10][3]), "a"(fw1[10][0]) : "memory"); SB();
  WAITVB(12);
  SB();
  asm volatile("v_mfma_f32_16x16x32_bf16 %0, %5, %6, %0\n\tbuffer_load_dwordx4 %1, %7, %8, %9 offen offset:128 sc1\n\tv_mfma_f32_16x16x32_bf16 %2, %10, %6, %2\n\tv_mfma_f32_16x16x32_bf16 %3, %11, %6, %3\n\tv_mfma_f32_16x16x32_bf16 %4, %12, %6, %4"
      : "+v"(acc[0][1]), "=&v"(p[10]), "+v"(acc[0][2]), "+v"(acc[0][3]), "+v"(acc[0][0]) : "a"(fw1[11][1]), "v"(p[11]), "v"(vF), "s"(rsW), "s"(sh[8]), "a"(fw1[11][2]), "a"(fw1[11][3]), "a"(fw1[11][0]) : "memory"); SB();
  WAITVB(12);
  SB();
  asm volatile("v_mfma_f32_16x16x32_bf16 %0, %5, %6, %0\n\tbuffer_load_dwordx4 %1, %7, %8, %9 offen offset:128 sc1\n\tv_mfma_f32_16x16x32_bf16 %2, %10, %6, %2\n\tv_mfma_f32_16x16x32_bf16 %3, %11, %6, %3\n\tv_mfma_f32_16x16x32_bf16 %4, %12, %6, %4"
      : "+v"(acc[0][1]), "=&v"(p[11]), "+v"(acc[0][2]), "+v"(acc[0][3]), "+v"(acc[0][0]) : "a"(fw1[12][1]), "v"(p[12]), "v"(vF), "s"(rsW), "s"(sh[9]), "a"(fw1[12][2]), "a"(fw1[12][3]), "a"(fw1[12][0]) : "memory"); SB();
  WAITVB(12);
  SB();
  asm volatile("v_mfma_f32_16x16x32_bf16 %0, %5, %6, %0\n\tbuffer_load_dwordx4 %1, %7, %8, %9 offen offset:128 sc1\n\tv_mfma_f32_16x16x32_bf16 %2, %10, %6, %2\n\tv_mfma_f32_16x16x32_bf16 %3, %11, %6, %3\n\tv_mfma_f32_16x16x32_bf16 %4, %12, %6, %4"
      : "+v"(acc[0][1]), "=&v"(p[12]), "+v"(acc[0][2]), "+v"(acc[0][3]), "+v"(acc[0][0]) : "a"(fw1[13][1]), "v"(p[13]), "v"(vF), "s"(rsW), "s"(sh[10]), "a"(fw1[13][2]), "a"(fw1[13][3]), "a"(fw1[13][0]) : "memory"); SB();
  WAITVB(12);
  SB();
  asm volatile("v_mfma_f32_16x16x32_bf16 %0, %5, %6, %0\n\tbuffer_load_dwordx4 %1, %7, %8, %9 offen offset:128 sc1\n\tv_mfma_f32_16x16x32_bf16 %2, %10, %6, %2\n\tv_mfma_f32_16x16x32_bf16 %3, %11, %6, %3\n\tv_mfma_f32_16x16x32_bf16 %4, %12, %6, %4"
      : "+v"(acc[0][1]), "=&v"(p[13]), "+v"(acc[0][2]), "+v"(acc[0][3]), "+v"(acc[0][0]) : "a"(fw1[14][1]), "v"(p[0]), "v"(vF), "s"(rsW), "s"(sh[11]), "a"(fw1[14][2]), "a"(fw1[14][3]), "a"(fw1[14][0]) : "memory"); SB();
  WAITVB(12);
  SB();
  asm volatile("v_mfma_f32_16x16x32_bf16 %0, %5, %6, %0\n\tbuffer_load_dwordx4 %1, %7, %8, %9 offen offset:128 sc1\n\tv_mfma_f32_16x16x32_bf16 %2, %10, %6, %2\n\tv_mfma_f32_16x16x32_bf16 %3, %11, %6, %3\n\tv_mfma_f32_16x16x32_bf16 %4, %12, %6, %4"
      : "+v"(acc[0][1]), "=&v"(p[0]), "+v"(acc[0][2]), "+v"(acc[0][3]), "+v"(acc[0][0]) : "v"(fw1[15][1]), "v"(p[1]), "v"(vF), "s"(rsW), "s"(sh[12]), "v"(fw1[15][2]), "v"(fw1[15][3]), "v"(fw1[15][0]) : "memory"); SB();
  STAMP(7);
  WAITVB(12);
  SB();
  asm volatile("v_mfma_f32_16x16x32_bf16 %0, %5, %6, 0\n\tbuffer_load_dwordx4 %1, %7, %8, %9 offen offset:128 sc1\n\tv_mfma_f32_16x16x32_bf16 %2, %10, %6, 0\n\tv_mfma_f32_16x16x32_bf16 %3, %11, %6, 0\n\tv_mfma_f32_16x16x32_bf16 %4, %12, %6, %13"
      : "=&v"(acc[1][1]), "=&v"(p[1]), "=&v"(acc[1][2]), "=&v"(acc[1][3]), "=&v"(acc[1][0]) : "a"(fw1[0][1]), "v"(p[2]), "v"(vF), "s"(rsW), "s"(sh[13]), "a"(fw1[0][2]), "a"(fw1[0][3]), "a"(fw1[0][0]), "v"(bb) : "memory"); SB();
  WAITVB(12);
  SB();
  asm volatile("v_mfma_f32_16x16x32_bf16 %0, %5, %6, %0\n\tbuffer_load_dwordx4 %1, %7, %8, %9 offen offset:128 sc1\n\tv_mfma_f32_16x16x32_bf16 %2, %10, %6, %2\n\tds_write_b128 %11, %12 offset:1024\n\tv_mfma_f32_16x16x32_bf16 %3, %13, %6, %3\n\tv_mfma_f32_16x16x32_bf16 %4, %14, %6, %4"
      : "+v"(acc[1][1]), "=&v"(p[2]), "+v"(acc[1][2]), "+v"(acc[1][3]), "+v"(acc[1][0]) : "a"(fw1[1][1]), "v"(p[3]), "v"(vF), "s"(rsW), "s"(sh[14]), "a"(fw1[1][2]), "v"(rw), "v"(acc[0][1]), "a"(fw1[1][3]), "a"(fw1[1][0]) : "memory"); SB();
  WAITVB(12);
  SB();
  asm volatile("v_mfma_f32_16x16x32_bf16 %0, %5, %6, %0\n\tbuffer_load_dwordx4 %1, %7, %8, %9 offen offset:128 sc1\n\tv_mfma_f32_16x16x32_bf16 %2, %10, %6, %2\n\tds_write_b128 %11, %12 offset:2048\n\tv_mfma_f32_16x16x32_bf16 %3, %13, %6, %3\n\tv_mfma_f32_16x16x32_bf16 %4, %14, %6, %4"
      : "+v"(acc[1][1]), "=&v"(p[3]), "+v"(acc[1][2]), "+v"(acc[1][3]), "+v"(acc[1][0]) : "a"(fw1[2][1]), "v"(p[4]), "v"(vF), "s"(rsW), "s"(sh[15]), "a"(fw1[2][2]), "v"(rw), "v"(acc[0][2]), "a"(fw1[2][3]), "a"(fw1[2][0]) : "memory"); SB();
  WAITVB(12);
  SB();
  asm volatile("v_mfma_f32_16x16x32_bf16 %0, %5, %6, %0\n\tbuffer_load_dwordx4 %1, %7, %8, %9 offen offset:256 sc1\n\tv_mfma_f32_16x16x32_bf16 %2, %10, %6, %2\n\tds_write_b128 %11, %12 offset:3072\n\tv_mfma_f32_16x16x32_bf16 %3, %13, %6, %3\n\tv_mfma_f32_16x16x32_bf16 %4, %14, %6, %4"
      : "+v"(acc[1][1]), "=&v"(p[4]), "+v"(acc[1][2]), "+v"(acc[1][3]), "+v"(acc[1][0]) : "a"(fw1[3][1]), "v"(p[5]), "v"(vF), "s"(rsW), "s"(sh[0]), "a"(fw1[3][2]), "v"(rw), "v"(acc[0][3]), "a"(fw1[3][3]), "a"(fw1[3][0]) : "memory"); SB();
  WAITVB(12);
  SB();
  asm volatile("v_mfma_f32_16x16x32_bf16 %0, %3, %4, %0\n\tbuffer_load_dwordx4 %1, %5, %6, %7 offen offset:256 sc1\n\tv_mfma_f32_16x16x32_bf16 %2, %8, %4, %2"
      : "+v"(acc[1][1]), "=&v"(p[5]), "+v"(acc[1][2]) : "a"(fw1[4][1]), "v"(p[6]), "v"(vF), "s"(rsW), "s"(sh[1]), "a"(fw1[4][2]) : "memory"); SB();
  BAR();
  SB();
  asm volatile("v_mfma_f32_16x16x32_bf16 %0, %5, %6, %0\n\tds_read_b128 %1, %7 offset:1024\n\tds_read_b128 %2, %8 offset:2048\n\tv_mfma_f32_16x16x32_bf16 %3, %9, %6, %3\n\tds_read_b128 %4, %10 offset:3072"
      : "+v"(acc[1][3]), "=&v"(pm[0][0]), "=&v"(pm[0][1]), "+v"(acc[1][0]), "=&v"(pm[0][2]) : "a"(fw1[4][3]), "v"(p[6]), "v"(rr1), "v"(rr2), "a"(fw1[4][0]), "v"(rr3) : "memory"); SB();
  WAITVB(12);
  SB();
  asm volatile("v_mfma_f32_16x16x32_bf16 %0, %5, %6, %0\n\tbuffer_load_dwordx4 %1, %7, %8, %9 offen offset:256 sc1\n\tv_mfma_f32_16x16x32_bf16 %2, %10, %6, %2\n\tv_mfma_f32_16x16x32_bf16 %3, %11, %6, %3\n\tv_mfma_f32_16x16x32_bf16 %4, %12, %6, %4"
      : "+v"(acc[1][1]), "=&v"(p[6]), "+v"(acc[1][2]), "+v"(acc[1][3]), "+v"(acc[1][0]) : "a"(fw1[5][1]), "v"(p[7]), "v"(vF), "s"(rsW), "s"(sh[2]), "a"(fw1[5][2]), "a"(fw1[5][3]), "a"(fw1[5][0]) : "memory"); SB();
  WAITVB(12);
  SB();
  asm volatile("v_mfma_f32_16x16x32_bf16 %0, %3, %4, %0\n\tbuffer_load_dwordx4 %1, %5, %6, %7 offen offset:256 sc1\n\tv_mfma_f32_16x16x32_bf16 %2, %8, %4, %2"
      : "+v"(acc[1][1]), "=&v"(p[7]), "+v"(acc[1][2]) : "a"(fw1[6][1]), "v"(p[8]), "v"(vF), "s"(rsW), "s"(sh[3]), "a"(fw1[6][2]) : "memory"); SB();
  WAITL(0);
  asm volatile("v_add_f32 %0, %6, %7\n\tv_mfma_f32_16x16x32_bf16 %1, %8, %9, %1\n\tv_add_f32 %2, %10, %11\n\tv_add_f32 %3, %12, %13\n\tv_mfma_f32_16x16x32_bf16 %4, %14, %9, %4\n\tv_add_f32 %5, %15, %16"
      : "=&v"(gm[0]), "+v"(acc[1][3]), "=&v"(gm[1]), "=&v"(gm[2]), "+v"(acc[1][0]), "=&v"(gm[3]) : "v"(acc[0][0][0]), "v"(pm[0][0][0]), "a"(fw1[6][3]), "v"(p[8]), "v"(acc[0][0][1]), "v"(pm[0][0][1]), "v"(acc[0][0][2]), "v"(pm[0][0][2]), "a"(fw1[6][0]), "v"(acc[0][0][3]), "v"(pm[0][0][3])); SB();
  WAITVB(12);
  SB();
  asm volatile("v_mfma_f32_16x16x32_bf16 %0, %9, %10, %0\n\tbuffer_load_dwordx4 %1, %11, %12, %13 offen offset:256 sc1\n\tv_mfma_f32_16x16x32_bf16 %2, %14, %10, %2\n\tv_add_f32 %3, %15, %16\n\tv_add_f32 %4, %17, %18\n\tv_mfma_f32_16x16x32_bf16 %5, %19, %10, %5\n\tv_add_f32 %6, %20, %21\n\tv_add_f32 %7, %22, %23\n\tv_mfma_f32_16x16x32_bf16 %8, %24, %10, %8"
      : "+v"(acc[1][1]), "=&v"(p[8]), "+v"(acc[1][2]), "=&v"(gz[0]), "=&v"(gz[1]), "+v"(acc[1][3]), "=&v"(gz[2]), "=&v"(gz[3]), "+v"(acc[1][0]) : "a"(fw1[7][1]), "v"(p[9]), "v"(vF), "s"(rsW), "s"(sh[4]), "a"(fw1[7][2]), "v"(pm[0][1][0]), "v"(pm[0][2][0]), "v"(pm[0][1][1]), "v"(pm[0][2][1]), "a"(fw1[7][3]), "v"(pm[0][1][2]), "v"(pm[0][2][2]), "v"(pm[0][1][3]), "v"(pm[0][2][3]), "a"(fw1[7][0]) : "memory"); SB();
  asm volatile("v_add_f32 %0, %0, %1"
      : "+v"(gm[0]) : "v"(gz[0])); SB();
  WAITVB(12);
  SB();
  asm volatile("v_mfma_f32_16x16x32_bf16 %0, %10, %11, %0\n\tbuffer_load_dwordx4 %1, %12, %13, %14 offen offset:256 sc1\n\tv_mfma_f32_16x16x32_bf16 %2, %15, %11, %2\n\tv_add_f32 %3, %3, %4\n\tv_add_f32 %5, %5, %16\n\tv_mfma_f32_16x16x32_bf16 %6, %17, %11, %6\n\tv_add_f32 %7, %7, %18\n\tv_mul_f32 %8, %19, %19\n\tv_mfma_f32_16x16x32_bf16 %9, %20, %11, %9\n\tv_mul_f32 %4, %3, %3"
      : "+v"(acc[1][1]), "=&v"(p[9]), "+v"(acc[1][2]), "+v"(gm[1]), "+v"(gz[1]), "+v"(gm[2]), "+v"(acc[1][3]), "+v"(gm[3]), "=&v"(gz[0]), "+v"(acc[1][0]) : "a"(fw1[8][1]), "v"(p[10]), "v"(vF), "s"(rsW), "s"(sh[5]), "a"(fw1[8][2]), "v"(gz[2]), "a"(fw1[8][3]), "v"(gz[3]), "v"(gm[0]), "a"(fw1[8][0]) : "memory"); SB();
  WAITVB(12);
  SB();
  asm volatile("v_mfma_f32_16x16x32_bf16 %0, %9, %10, %0\n\tbuffer_load_dwordx4 %1, %11, %12, %13 offen offset:256 sc1\n\tv_mfma_f32_16x16x32_bf16 %2, %14, %10, %2\n\tv_mul_f32 %3, %15, %15\n\tv_mul_f32 %4, %16, %16\n\tv_mfma_f32_16x16x32_bf16 %5, %17, %10, %5\n\tv_fmaak_f32 %6, %18, %6, 0x40135761\n\tv_fmaak_f32 %7, %18, %7, 0x40135761\n\tv_mfma_f32_16x16x32_bf16 %8, %19, %10, %8\n\tv_fmaak_f32 %3, %18, %3, 0x40135761"
      : "+v"(acc[1][1]), "=&v"(p[10]), "+v"(acc[1][2]), "=&v"(gz[2]), "=&v"(gz[3]), "+v"(acc[1][3]), "+v"(gz[0]), "+v"(gz[1]), "+v"(acc[1][0]) : "a"(fw1[9][1]), "v"(p[11]), "v"(vF), "s"(rsW), "s"(sh[6]), "a"(fw1[9][2]), "v"(gm[2]), "v"(gm[3]), "a"(fw1[9][3]), "v"(gc), "a"(fw1[9][0]) : "memory"); SB();
  WAITVB(12);
  SB();
  asm volatile("v_mfma_f32_16x16x32_bf16 %0, %9, %10, %0\n\tbuffer_load_dwordx4 %1, %11, %12, %13 offen offset:256 sc1\n\tv_mfma_f32_16x16x32_bf16 %2, %14, %10, %2\n\tv_fmaak_f32 %3, %15, %3, 0x40135761\n\tv_mul_f32 %4, %4, %16\n\tv_mfma_f32_16x16x32_bf16 %5, %17, %10, %5\n\tv_mul_f32 %6, %6, %18\n\tv_mul_f32 %7, %7, %19\n\tv_mfma_f32_16x16x32_bf16 %8, %20, %10, %8\n\tv_mul_f32 %3, %3, %21"
      : "+v"(acc[1][1]), "=&v"(p[11]), "+v"(acc[1][2]), "+v"(gz[3]), "+v"(gz[0]), "+v"(acc[1][3]), "+v"(gz[1]), "+v"(gz[2]), "+v"(acc[1][0]) : "a"(fw1[10][1]), "v"(p[12]), "v"(vF), "s"(rsW), "s"(sh[7]), "a"(fw1[10][2]), "v"(gc), "v"(gm[0]), "a"(fw1[10][3]), "v"(gm[1]), "v"(gm[2]), "a"(fw1[10][0]), "v"(gm[3]) : "memory"); SB();
  WAITVB(12);
  SB();
  asm volatile("v_mfma_f32_16x16x32_bf16 %0, %7, %8, %0\n\tbuffer_load_dwordx4 %1, %9, %10, %11 offen offset:256 sc1\n\tv_mfma_f32_16x16x32_bf16 %2, %12, %8, %2\n\tv_exp_f32 %3, %3\n\tv_mfma_f32_16x16x32_bf16 %4, %13, %8, %4\n\tv_exp_f32 %5, %5\n\tv_mfma_f32_16x16x32_bf16 %6, %14, %8, %6"
      : "+v"(acc[1][1]), "=&v"(p[12]), "+v"(acc[1][2]), "+v"(gz[0]), "+v"(acc[1][3]), "+v"(gz[1]), "+v"(acc[1][0]) : "a"(fw1[11][1]), "v"(p[13]), "v"(vF), "s"(rsW), "s"(sh[8]), "a"(fw1[11][2]), "a"(fw1[11][3]), "a"(fw1[11][0]) : "memory"); SB();
  WAITVB(12);
  SB();
  asm volatile("v_mfma_f32_16x16x32_bf16 %0, %8, %9, %0\n\tbuffer_load_dwordx4 %1, %10, %11, %12 offen offset:256 sc1\n\tv_mfma_f32_16x16x32_bf16 %2, %13, %9, %2\n\tv_exp_f32 %3, %3\n\tv_mfma_f32_16x16x32_bf16 %4, %14, %9, %4\n\tv_exp_f32 %5, %5\n\tv_mfma_f32_16x16x32_bf16 %6, %15, %9, %6\n\tv_add_f32 %7, 1.0, %7"
      : "+v"(acc[1][1]), "=&v"(p[13]), "+v"(acc[1][2]), "+v"(gz[2]), "+v"(acc[1][3]), "+v"(gz[3]), "+v"(acc[1][0]), "+v"(gz[0]) : "a"(fw1[12][1]), "v"(p[0]), "v"(vF), "s"(rsW), "s"(sh[9]), "a"(fw1[12][2]), "a"(fw1[12][3]), "a"(fw1[12][0]) : "memory"); SB();
  WAITVB(12);
  SB();
  asm volatile("v_mfma_f32_16x16x32_bf16 %0, %8, %9, %0\n\tbuffer_load_dwordx4 %1, %10, %11, %12 offen offset:256 sc1\n\tv_mfma_f32_16x16x32_bf16 %2, %13, %9, %2\n\tv_add_f32 %3, 1.0, %3\n\tv_add_f32 %4, 1.0, %4\n\tv_mfma_f32_16x16x32_bf16 %5, %14, %9, %5\n\tv_add_f32 %6, 1.0, %6\n\tv_mfma_f32_16x16x32_bf16 %7, %15, %9, %7"
      : "+v"(acc[1][1]), "=&v"(p[0]), "+v"(acc[1][2]), "+v"(gz[1]), "+v"(gz[2]), "+v"(acc[1][3]), "+v"(gz[3]), "+v"(acc[1][0]) : "a"(fw1[13][1]), "v"(p[1]), "v"(vF), "s"(rsW), "s"(sh[10]), "a"(fw1[13][2]), "a"(fw1[13][3]), "a"(fw1[13][0]) : "memory"); SB();
  WAITVB(12);
  SB();
  asm volatile("v_mfma_f32_16x16x32_bf16 %0, %7, %8, %0\n\tbuffer_load_dwordx4 %1, %9, %10, %11 offen offset:256 sc1\n\tv_mfma_f32_16x16x32_bf16 %2, %12, %8, %2\n\tv_rcp_f32 %3, %3\n\tv_mfma_f32_16x16x32_bf16 %4, %13, %8, %4\n\tv_rcp_f32 %5, %5\n\tv_mfma_f32_16x16x32_bf16 %6, %14, %8, %6"
      : "+v"(acc[1][1]), "=&v"(p[1]), "+v"(acc[1][2]), "+v"(gz[0]), "+v"(acc[1][3]), "+v"(gz[1]), "+v"(acc[1][0]) : "a"(fw1[14][1]), "v"(p[2]), "v"(vF), "s"(rsW), "s"(sh[11]), "a"(fw1[14][2]), "a"(fw1[14][3]), "a"(fw1[14][0]) : "memory"); SB();
  WAITVB(12);
  SB();
  asm volatile("v_mfma_f32_16x16x32_bf16 %0, %9, %10, %0\n\tbuffer_load_dwordx4 %1, %11, %12, %13 offen offset:256 sc1\n\tv_mfma_f32_16x16x32_bf16 %2, %14, %10, %2\n\tv_rcp_f32 %3, %3\n\tv_mfma_f32_16x16x32_bf16 %4, %15, %10, %4\n\tv_rcp_f32 %5, %5\n\tv_mfma_f32_16x16x32_bf16 %6, %16, %10, %6\n\tv_fma_f32 %7, -%17, %7, %17\n\tv_fma_f32 %8, -%18, %8, %18"
      : "+v"(acc[1][1]), "=&v"(p[2]), "+v"(acc[1][2]), "+v"(gz[2]), "+v"(acc[1][3]), "+v"(gz[3]), "+v"(acc[1][0]), "+v"(gz[0]), "+v"(gz[1]) : "v"(fw1[15][1]), "v"(p[3]), "v"(vF), "s"(rsW), "s"(sh[12]), "v"(fw1[15][2]), "v"(fw1[15][3]), "v"(fw1[15][0]), "v"(gm[0]), "v"(gm[1]) : "memory"); SB();
  WAITVB(12);
  SB();
  asm volatile("v_mfma_f32_16x16x32_bf16 %0, %9, %10, 0\n\tbuffer_load_dwordx4 %1, %11, %12, %13 offen offset:256 sc1\n\tv_mfma_f32_16x16x32_bf16 %2, %14, %10, 0\n\tv_fma_f32 %3, -%15, %3, %15\n\tv_fma_f32 %4, -%16, %4, %16\n\tv_mfma_f32_16x16x32_bf16 %5, %17, %10, 0\n\tv_cvt_pk_bf16_f32 %6, %18, %19\n\tv_cvt_pk_bf16_f32 %7, %3, %4\n\tv_mfma_f32_16x16x32_bf16 %8, %20, %10, %21"
      : "=&v"(acc[2][1]), "=&v"(p[3]), "=&v"(acc[2][2]), "+v"(gz[2]), "+v"(gz[3]), "=&v"(acc[2][3]), "=&v"(uv[0][0]), "=&v"(uv[0][1]), "=&v"(acc[2][0]) : "a"(fw1[0][1]), "v"(p[4]), "v"(vF), "s"(rsW), "s"(sh[13]), "a"(fw1[0][2]), "v"(gm[2]), "v"(gm[3]), "a"(fw1[0][3]), "v"(gz[0]), "v"(gz[1]), "a"(fw1[0][0]), "v"(bb) : "memory"); SB();
  WAITVB(12);
  SB();
  asm volatile("v_mfma_f32_16x16x32_bf16 %0, %5, %6, %0\n\tbuffer_load_dwordx4 %1, %7, %8, %9 offen offset:256 sc1\n\tv_mfma_f32_16x16x32_bf16 %2, %10, %6, %2\n\tds_write_b128 %11, %12 offset:5120\n\tv_mfma_f32_16x16x32_bf16 %3, %13, %6, %3\n\tv_mfma_f32_16x16x32_bf16 %4, %14, %6, %4"
      : "+v"(acc[2][1]), "=&v"(p[4]), "+v"(acc[2][2]), "+v"(acc[2][3]), "+v"(acc[2][0]) : "a"(fw1[1][1]), "v"(p[5]), "v"(vF), "s"(rsW), "s"(sh[14]), "a"(fw1[1][2]), "v"(rw), "v"(acc[1][1]), "a"(fw1[1][3]), "a"(fw1[1][0]) : "memory"); SB();
  WAITVB(12);
  SB();
  asm volatile("v_mfma_f32_16x16x32_bf16 %0, %5, %6, %0\n\tbuffer_load_dwordx4 %1, %7, %8, %9 offen offset:256 sc1\n\tv_mfma_f32_16x16x32_bf16 %2, %10, %6, %2\n\tds_write_b128 %11, %12 offset:6144\n\tv_mfma_f32_16x16x32_bf16 %3, %13, %6, %3\n\tv_mfma_f32_16x16x32_bf16 %4, %14, %6, %4"
      : "+v"(acc[2][1]), "=&v"(p[5]), "+v"(acc[2][2]), "+v"(acc[2][3]), "+v"(acc[2][0]) : "a"(fw1[2][1]), "v"(p[6]), "v"(vF), "s"(rsW), "s"(sh[15]), "a"(fw1[2][2]), "v"(rw), "v"(acc[1][2]), "a"(fw1[2][3]), "a"(fw1[2][0]) : "memory"); SB();
  WAITVB(12);
  SB();
  asm volatile("v_mfma_f32_16x16x32_bf16 %0, %5, %6, %0\n\tbuffer_load_dwordx4 %1, %7, %8, %9 offen offset:384 sc1\n\tv_mfma_f32_16x16x32_bf16 %2, %10, %6, %2\n\tds_write_b128 %11, %12 offset:7168\n\tv_mfma_f32_16x16x32_bf16 %3, %13, %6, %3\n\tv_mfma_f32_16x16x32_bf16 %4, %14, %6, %4"
      : "+v"(acc[2][1]), "=&v"(p[6]), "+v"(acc[2][2]), "+v"(acc[2][3]), "+v"(acc[2][0]) : "a"(fw1[3][1]), "v"(p[7]), "v"(vF), "s"(rsW), "s"(sh[0]), "a"(fw1[3][2]), "v"(rw), "v"(acc[1][3]), "a"(fw1[3][3]), "a"(fw1[3][0]) : "memory"); SB();
  WAITVB(12);
  SB();
  asm volatile("v_mfma_f32_16x16x32_bf16 %0, %3, %4, %0\n\tbuffer_load_dwordx4 %1, %5, %6, %7 offen offset:384 sc1\n\tv_mfma_f32_16x16x32_bf16 %2, %8, %4, %2"
      : "+v"(acc[2][1]), "=&v"(p[7]), "+v"(acc[2][2]) : "a"(fw1[4][1]), "v"(p[8]), "v"(vF), "s"(rsW), "s"(sh[1]), "a"(fw1[4][2]) : "memory"); SB();
  BAR();
  SB();
  asm volatile("v_mfma_f32_16x16x32_bf16 %0, %5, %6, %0\n\tds_read_b128 %1, %7 offset:5120\n\tds_read_b128 %2, %8 offset:6144\n\tv_mfma_f32_16x16x32_bf16 %3, %9, %6, %3\n\tds_read_b128 %4, %10 offset:7168"
      : "+v"(acc[2][3]), "=&v"(pm[1][0]), "=&v"(pm[1][1]), "+v"(acc[2][0]), "=&v"(pm[1][2]) : "a"(fw1[4][3]), "v"(p[8]), "v"(rr1), "v"(rr2), "a"(fw1[4][0]), "v"(rr3) : "memory"); SB();
  WAITVB(12);
  SB();
  asm volatile("v_mfma_f32_16x16x32_bf16 %0, %5, %6, %0\n\tbuffer_load_dwordx4 %1, %7, %8, %9 offen offset:384 sc1\n\tv_mfma_f32_16x16x32_bf16 %2, %10, %6, %2\n\tv_mfma_f32_16x16x32_bf16 %3, %11, %6, %3\n\tv_mfma_f32_16x16x32_bf16 %4, %12, %6, %4"
      : "+v"(acc[2][1]), "=&v"(p[8]), "+v"(acc[2][2]), "+v"(acc[2][3]), "+v"(acc[2][0]) : "a"(fw1[5][1]), "v"(p[9]), "v"(vF), "s"(rsW), "s"(sh[2]), "a"(fw1[5][2]), "a"(fw1[5][3]), "a"(fw1[5][0]) : "memory"); SB();
  WAITVB(12);
  SB();
  asm volatile("v_mfma_f32_16x16x32_bf16 %0, %3, %4, %0\n\tbuffer_load_dwordx4 %1, %5, %6, %7 offen offset:384 sc1\n\tv_mfma_f32_16x16x32_bf16 %2, %8, %4, %2"
      : "+v"(acc[2][1]), "=&v"(p[9]), "+v"(acc[2][2]) : "a"(fw1[6][1]), "v"(p[10]), "v"(vF), "s"(rsW), "s"(sh[3]), "a"(fw1[6][2]) : "memory"); SB();
  WAITL(0);
  asm volatile("v_add_f32 %0, %6, %7\n\tv_mfma_f32_16x16x32_bf16 %1, %8, %9, %1\n\tv_add_f32 %2, %10, %11\n\tv_add_f32 %3, %12, %13\n\tv_mfma_f32_16x16x32_bf16 %4, %14, %9, %4\n\tv_add_f32 %5, %15, %16"
      : "=&v"(gm[0]), "+v"(acc[2][3]), "=&v"(gm[1]), "=&v"(gm[2]), "+v"(acc[2][0]), "=&v"(gm[3]) : "v"(acc[1][0][0]), "v"(pm[1][0][0]), "a"(fw1[6][3]), "v"(p[10]), "v"(acc[1][0][1]), "v"(pm[1][0][1]), "v"(acc[1][0][2]), "v"(pm[1][0][2]), "a"(fw1[6][0]), "v"(acc[1][0][3]), "v"(pm[1][0][3])); SB();
  WAITVB(12);
  SB();
  asm volatile("v_mfma_f32_16x16x32_bf16 %0, %9, %10, %0\n\tbuffer_load_dwordx4 %1, %11, %12, %13 offen offset:384 sc1\n\tv_mfma_f32_16x16x32_bf16 %2, %14, %10, %2\n\tv_add_f32 %3, %15, %16\n\tv_add_f32 %4, %17, %18\n\tv_mfma_f32_16x16x32_bf16 %5, %19, %10, %5\n\tv_add_f32 %6, %20, %21\n\tv_add_f32 %7, %22, %23\n\tv_mfma_f32_16x16x32_bf16 %8, %24, %10, %8"
      : "+v"(acc[2][1]), "=&v"(p[10]), "+v"(acc[2][2]), "=&v"(gz[0]), "=&v"(gz[1]), "+v"(acc[2][3]), "=&v"(gz[2]), "=&v"(gz[3]), "+v"(acc[2][0]) : "a"(fw1[7][1]), "v"(p[11]), "v"(vF), "s"(rsW), "s"(sh[4]), "a"(fw1[7][2]), "v"(pm[1][1][0]), "v"(pm[1][2][0]), "v"(pm[1][1][1]), "v"(pm[1][2][1]), "a"(fw1[7][3]), "v"(pm[1][1][2]), "v"(pm[1][2][2]), "v"(pm[1][1][3]), "v"(pm[1][2][3]), "a"(fw1[7][0]) : "memory"); SB();
  asm volatile("v_add_f32 %0, %0, %1"
      : "+v"(gm[0]) : "v"(gz[0])); SB();
  WAITVB(12);
  SB();
  asm volatile("v_mfma_f32_16x16x32_bf16 %0, %10, %11, %0\n\tbuffer_load_dwordx4 %1, %12, %13, %14 offen offset:384 sc1\n\tv_mfma_f32_16x16x32_bf16 %2, %15, %11, %2\n\tv_add_f32 %3, %3, %4\n\tv_add_f32 %5, %5, %16\n\tv_mfma_f32_16x16x32_bf16 %6, %17, %11, %6\n\tv_add_f32 %7, %7, %18\n\tv_mul_f32 %8, %19, %19\n\tv_mfma_f32_16x16x32_bf16 %9, %20, %11, %9\n\tv_mul_f32 %4, %3, %3"
      : "+v"(acc[2][1]), "=&v"(p[11]), "+v"(acc[2][2]), "+v"(gm[1]), "+v"(gz[1]), "+v"(gm[2]), "+v"(acc[2][3]), "+v"(gm[3]), "=&v"(gz[0]), "+v"(acc[2][0]) : "a"(fw1[8][1]), "v"(p[12]), "v"(vF), "s"(rsW), "s"(sh[5]), "a"(fw1[8][2]), "v"(gz[2]), "a"(fw1[8][3]), "v"(gz[3]), "v"(gm[0]), "a"(fw1[8][0]) : "memory"); SB();
  WAITVB(12);
  SB();
  asm volatile("v_mfma_f32_16x16x32_bf16 %0, %9, %10, %0\n\tbuffer_load_dwordx4 %1, %11, %12, %13 offen offset:384 sc1\n\tv_mfma_f32_16x16x32_bf16 %2, %14, %10, %2\n\tv_mul_f32 %3, %15, %15\n\tv_mul_f32 %4, %16, %16\n\tv_mfma_f32_16x16x32_bf16 %5, %17, %10, %5\n\tv_fmaak_f32 %6, %18, %6, 0x40135761\n\tv_fmaak_f32 %7, %18, %7, 0x40135761\n\tv_mfma_f32_16x16x32_bf16 %8, %19, %10, %8\n\tv_fmaak_f32 %3, %18, %3, 0x40135761"
      : "+v"(acc[2][1]), "=&v"(p[12]), "+v"(acc[2][2]), "=&v"(gz[2]), "=&v"(gz[3]), "+v"(acc[2][3]), "+v"(gz[0]), "+v"(gz[1]), "+v"(acc[2][0]) : "a"(fw1[9][1]), "v"(p[13]), "v"(vF), "s"(rsW), "s"(sh[6]), "a"(fw1[9][2]), "v"(gm[2]), "v"(gm[3]), "a"(fw1[9][3]), "v"(gc), "a"(fw1[9][0]) : "memory"); SB();
  WAITVB(12);
  SB();
  asm volatile("v_mfma_f32_16x16x32_bf16 %0, %9, %10, %0\n\tbuffer_load_dwordx4 %1, %11, %12, %13 offen offset:384 sc1\n\tv_mfma_f32_16x16x32_bf16 %2, %14, %10, %2\n\tv_fmaak_f32 %3, %15, %3, 0x40135761\n\tv_mul_f32 %4, %4, %16\n\tv_mfma_f32_16x16x32_bf16 %5, %17, %10, %5\n\tv_mul_f32 %6, %6, %18\n\tv_mul_f32 %7, %7, %19\n\tv_mfma_f32_16x16x32_bf16 %8, %20, %10, %8\n\tv_mul_f32 %3, %3, %21"
      : "+v"(acc[2][1]), "=&v"(p[13]), "+v"(acc[2][2]), "+v"(gz[3]), "+v"(gz[0]), "+v"(acc[2][3]), "+v"(gz[1]), "+v"(gz[2]), "+v"(acc[2][0]) : "a"(fw1[10][1]), "v"(p[0]), "v"(vF), "s"(rsW), "s"(sh[7]), "a"(fw1[10][2]), "v"(gc), "v"(gm[0]), "a"(fw1[10][3]), "v"(gm[1]), "v"(gm[2]), "a"(fw1[10][0]), "v"(gm[3]) : "memory"); SB();
  WAITVB(12);
  SB();
  asm volatile("v_mfma_f32_16x16x32_bf16 %0, %7, %8, %0\n\tbuffer_load_dwordx4 %1, %9, %10, %11 offen offset:384 sc1\n\tv_mfma_f32_16x16x32_bf16 %2, %12, %8, %2\n\tv_exp_f32 %3, %3\n\tv_mfma_f32_16x16x32_bf16 %4, %13, %8, %4\n\tv_exp_f32 %5, %5\n\tv_mfma_f32_16x16x32_bf16 %6, %14, %8, %6"
      : "+v"(acc[2][1]), "=&v"(p[0]), "+v"(acc[2][2]), "+v"(gz[0]), "+v"(acc[2][3]), "+v"(gz[1]), "+v"(acc[2][0]) : "a"(fw1[11][1]), "v"(p[1]), "v"(vF), "s"(rsW), "s"(sh[8]), "a"(fw1[11][2]), "a"(fw1[11][3]), "a"(fw1[11][0]) : "memory"); SB();
  WAITVB(12);
  SB();
  asm volatile("v_mfma_f32_16x16x32_bf16 %0, %8, %9, %0\n\tbuffer_load_dwordx4 %1, %10, %11, %12 offen offset:384 sc1\n\tv_mfma_f32_16x16x32_bf16 %2, %13, %9, %2\n\tv_exp_f32 %3, %3\n\tv_mfma_f32_16x16x32_bf16 %4, %14, %9, %4\n\tv_exp_f32 %5, %5\n\tv_mfma_f32_16x16x32_bf16 %6, %15, %9, %6\n\tv_add_f32 %7, 1.0, %7"
      : "+v"(acc[2][1]), "=&v"(p[1]), "+v"(acc[2][2]), "+v"(gz[2]), "+v"(acc[2][3]), "+v"(gz[3]), "+v"(acc[2][0]), "+v"(gz[0]) : "a"(fw1[12][1]), "v"(p[2]), "v"(vF), "s"(rsW), "s"(sh[9]), "a"(fw1[12][2]), "a"(fw1[12][3]), "a"(fw1[12][0]) : "memory"); SB();
  WAITVB(12);
  SB();
  asm volatile("v_mfma_f32_16x16x32_bf16 %0, %8, %9, %0\n\tbuffer_load_dwordx4 %1, %10, %11, %12 offen offset:384 sc1\n\tv_mfma_f32_16x16x32_bf16 %2, %13, %9, %2\n\tv_add_f32 %3, 1.0, %3\n\tv_add_f32 %4, 1.0, %4\n\tv_mfma_f32_16x16x32_bf16 %5, %14, %9, %5\n\tv_add_f32 %6, 1.0, %6\n\tv_mfma_f32_16x16x32_bf16 %7, %15, %9, %7"
      : "+v"(acc[2][1]), "=&v"(p[2]), "+v"(acc[2][2]), "+v"(gz[1]), "+v"(gz[2]), "+v"(acc[2][3]), "+v"(gz[3]), "+v"(acc[2][0]) : "a"(fw1[13][1]), "v"(p[3]), "v"(vF), "s"(rsW), "s"(sh[10]), "a"(fw1[13][2]), "a"(fw1[13][3]), "a"(fw1[13][0]) : "memory"); SB();
  WAITVB(12);
  SB();
  asm volatile("v_mfma_f32_16x16x32_bf16 %0, %7, %8, %0\n\tbuffer_load_dwordx4 %1, %9, %10, %11 offen offset:384 sc1\n\tv_mfma_f32_16x16x32_bf16 %2, %12, %8, %2\n\tv_rcp_f32 %3, %3\n\tv_mfma_f32_16x16x32_bf16 %4, %13, %8, %4\n\tv_rcp_f32 %5, %5\n\tv_mfma_f32_16x16x32_bf16 %6, %14, %8, %6"
      : "+v"(acc[2][1]), "=&v"(p[3]), "+v"(acc[2][2]), "+v"(gz[0]), "+v"(acc[2][3]), "+v"(gz[1]), "+v"(acc[2][0]) : "a"(fw1[14][1]), "v"(p[4]), "v"(vF), "s"(rsW), "s"(sh[11]), "a"(fw1[14][2]), "a"(fw1[14][3]), "a"(fw1[14][0]) : "memory"); SB();
  WAITVB(12);
  SB();
  asm volatile("v_mfma_f32_16x16x32_bf16 %0, %9, %10, %0\n\tbuffer_load_dwordx4 %1, %11, %12, %13 offen offset:384 sc1\n\tv_mfma_f32_16x16x32_bf16 %2, %14, %10, %2\n\tv_rcp_f32 %3, %3\n\tv_mfma_f32_16x16x32_bf16 %4, %15, %10, %4\n\tv_rcp_f32 %5, %5\n\tv_mfma_f32_16x16x32_bf16 %6, %16, %10, %6\n\tv_fma_f32 %7, -%17, %7, %17\n\tv_fma_f32 %8, -%18, %8, %18"
      : "+v"(acc[2][1]), "=&v"(p[4]), "+v"(acc[2][2]), "+v"(gz[2]), "+v"(acc[2][3]), "+v"(gz[3]), "+v"(acc[2][0]), "+v"(gz[0]), "+v"(gz[1]) : "v"(fw1[15][1]), "v"(p[5]), "v"(vF), "s"(rsW), "s"(sh[12]), "v"(fw1[15][2]), "v"(fw1[15][3]), "v"(fw1[15][0]), "v"(gm[0]), "v"(gm[1]) : "memory"); SB();
  WAITVB(12);
  SB();
  asm volatile("v_mfma_f32_16x16x32_bf16 %0, %9, %10, 0\n\tbuffer_load_dwordx4 %1, %11, %12, %13 offen offset:384 sc1\n\tv_mfma_f32_16x16x32_bf16 %2, %14, %10, 0\n\tv_fma_f32 %3, -%15, %3, %15\n\tv_fma_f32 %4, -%16, %4, %16\n\tv_mfma_f32_16x16x32_bf16 %5, %17, %10, 0\n\tv_cvt_pk_bf16_f32 %6, %18, %19\n\tv_cvt_pk_bf16_f32 %7, %3, %4\n\tv_mfma_f32_16x16x32_bf16 %8, %20, %10, %21"
      : "=&v"(acc[3][1]), "=&v"(p[5]), "=&v"(acc[3][2]), "+v"(gz[2]), "+v"(gz[3]), "=&v"(acc[3][3]), "=&v"(uv[1][0]), "=&v"(uv[1][1]), "=&v"(acc[3][0]) : "a"(fw1[0][1]), "v"(p[6]), "v"(vF), "s"(rsW), "s"(sh[13]), "a"(fw1[0][2]), "v"(gm[2]), "v"(gm[3]), "a"(fw1[0][3]), "v"(gz[0]), "v"(gz[1]), "a"(fw1[0][0]), "v"(bb) : "memory"); SB();
  WAITVB(12);
  SB();
  asm volatile("v_mfma_f32_16x16x32_bf16 %0, %6, %7, %0\n\tbuffer_load_dwordx4 %1, %8, %9, %10 offen offset:384 sc1\n\tv_mfma_f32_16x16x32_bf16 %2, %11, %7, %2\n\tds_write_b128 %12, %13 offset:1024\n\tv_mfma_f32_16x16x32_bf16 %3, %14, %7, %3\n\tbuffer_load_dword %4, %15, %9, 0 offen sc1\n\tv_mfma_f32_16x16x32_bf16 %5, %16, %7, %5"
      : "+v"(acc[3][1]), "=&v"(p[6]), "+v"(acc[3][2]), "+v"(acc[3][3]), "=&v"(fq), "+v"(acc[3][0]) : "a"(fw1[1][1]), "v"(p[7]), "v"(vF), "s"(rsW), "s"(sh[14]), "a"(fw1[1][2]), "v"(rw), "v"(acc[2][1]), "a"(fw1[1][3]), "v"(vFQ), "a"(fw1[1][0]) : "memory"); SB();
  WAITVB(13);
  SB();
  asm volatile("v_mfma_f32_16x16x32_bf16 %0, %5, %6, %0\n\tbuffer_load_dwordx4 %1, %7, %8, %9 offen offset:384 sc1\n\tv_mfma_f32_16x16x32_bf16 %2, %10, %6, %2\n\tds_write_b128 %11, %12 offset:2048\n\tv_mfma_f32_16x16x32_bf16 %3, %13, %6, %3\n\tv_mfma_f32_16x16x32_bf16 %4, %14, %6, %4"
      : "+v"(acc[3][1]), "=&v"(p[7]), "+v"(acc[3][2]), "+v"(acc[3][3]), "+v"(acc[3][0]) : "a"(fw1[2][1]), "v"(p[8]), "v"(vF), "s"(rsW), "s"(sh[15]), "a"(fw1[2][2]), "v"(rw), "v"(acc[2][2]), "a"(fw1[2][3]), "a"(fw1[2][0]) : "memory"); SB();
  WAITVB(13);
  SB();
  asm volatile("v_mfma_f32_16x16x32_bf16 %0, %5, %6, %0\n\tbuffer_load_dwordx4 %1, %7, %8, %9 offen offset:512 sc1\n\tv_mfma_f32_16x16x32_bf16 %2, %10, %6, %2\n\tds_write_b128 %11, %12 offset:3072\n\tv_mfma_f32_16x16x32_bf16 %3, %13, %6, %3\n\tv_mfma_f32_16x16x32_bf16 %4, %14, %6, %4"
      : "+v"(acc[3][1]), "=&v"(p[8]), "+v"(acc[3][2]), "+v"(acc[3][3]), "+v"(acc[3][0]) : "a"(fw1[3][1]), "v"(p[9]), "v"(vF), "s"(rsW), "s"(sh[0]), "a"(fw1[3][2]), "v"(rw), "v"(acc[2][3]), "a"(fw1[3][3]), "a"(fw1[3][0]) : "memory"); SB();
  WAITVB(13);
  SB();
  asm volatile("v_mfma_f32_16x16x32_bf16 %0, %3, %4, %0\n\tbuffer_load_dwordx4 %1, %5, %6, %7 offen offset:512 sc1\n\tv_mfma_f32_16x16x32_bf16 %2, %8, %4, %2"
      : "+v"(acc[3][1]), "=&v"(p[9]), "+v"(acc[3][2]) : "a"(fw1[4][1]), "v"(p[10]), "v"(vF), "s"(rsW), "s"(sh[1]), "a"(fw1[4][2]) : "memory"); SB();
  BAR();
  SB();
  asm volatile("v_mfma_f32_16x16x32_bf16 %0, %5, %6, %0\n\tds_read_b128 %1, %7 offset:1024\n\tds_read_b128 %2, %8 offset:2048\n\tv_mfma_f32_16x16x32_bf16 %3, %9, %6, %3\n\tds_read_b128 %4, %10 offset:3072"
      : "+v"(acc[3][3]), "=&v"(pm[2][0]), "=&v"(pm[2][1]), "+v"(acc[3][0]), "=&v"(pm[2][2]) : "a"(fw1[4][3]), "v"(p[10]), "v"(rr1), "v"(rr2), "a"(fw1[4][0]), "v"(rr3) : "memory"); SB();
  WAITVB(13);
  SB();
  asm volatile("v_mfma_f32_16x16x32_bf16 %0, %5, %6, %0\n\tbuffer_load_dwordx4 %1, %7, %8, %9 offen offset:512 sc1\n\tv_mfma_f32_16x16x32_bf16 %2, %10, %6, %2\n\tv_mfma_f32_16x16x32_bf16 %3, %11, %6, %3\n\tv_mfma_f32_16x16x32_bf16 %4, %12, %6, %4"
      : "+v"(acc[3][1]), "=&v"(p[10]), "+v"(acc[3][2]), "+v"(acc[3][3]), "+v"(acc[3][0]) : "a"(fw1[5][1]), "v"(p[11]), "v"(vF), "s"(rsW), "s"(sh[2]), "a"(fw1[5][2]), "a"(fw1[5][3]), "a"(fw1[5][0]) : "memory"); SB();
  WAITVB(13);
  SB();
  asm volatile("v_mfma_f32_16x16x32_bf16 %0, %3, %4, %0\n\tbuffer_load_dwordx4 %1, %5, %6, %7 offen offset:512 sc1\n\tv_mfma_f32_16x16x32_bf16 %2, %8, %4, %2"
      : "+v"(acc[3][1]), "=&v"(p[11]), "+v"(acc[3][2]) : "a"(fw1[6][1]), "v"(p[12]), "v"(vF), "s"(rsW), "s"(sh[3]), "a"(fw1[6][2]) : "memory"); SB();
  WAITL(0);
  asm volatile("v_add_f32 %0, %6, %7\n\tv_mfma_f32_16x16x32_bf16 %1, %8, %9, %1\n\tv_add_f32 %2, %10, %11\n\tv_add_f32 %3, %12, %13\n\tv_mfma_f32_16x16x32_bf16 %4, %14, %9, %4\n\tv_add_f32 %5, %15, %16"
      : "=&v"(gm[0]), "+v"(acc[3][3]), "=&v"(gm[1]), "=&v"(gm[2]), "+v"(acc[3][0]), "=&v"(gm[3]) : "v"(acc[2][0][0]), "v"(pm[2][0][0]), "a"(fw1[6][3]), "v"(p[12]), "v"(acc[2][0][1]), "v"(pm[2][0][1]), "v"(acc[2][0][2]), "v"(pm[2][0][2]), "a"(fw1[6][0]), "v"(acc[2][0][3]), "v"(pm[2][0][3])); SB();
  WAITVB(13);
  SB();
  asm volatile("v_mfma_f32_16x16x32_bf16 %0, %9, %10, %0\n\tbuffer_load_dwordx4 %1, %11, %12, %13 offen offset:512 sc1\n\tv_mfma_f32_16x16x32_bf16 %2, %14, %10, %2\n\tv_add_f32 %3, %15, %16\n\tv_add_f32 %4, %17, %18\n\tv_mfma_f32_16x16x32_bf16 %5, %19, %10, %5\n\tv_add_f32 %6, %20, %21\n\tv_add_f32 %7, %22, %23\n\tv_mfma_f32_16x16x32_bf16 %8, %24, %10, %8"
      : "+v"(acc[3][1]), "=&v"(p[12]), "+v"(acc[3][2]), "=&v"(gz[0]), "=&v"(gz[1]), "+v"(acc[3][3]), "=&v"(gz[2]), "=&v"(gz[3]), "+v"(acc[3][0]) : "a"(fw1[7][1]), "v"(p[13]), "v"(vF), "s"(rsW), "s"(sh[4]), "a"(fw1[7][2]), "v"(pm[2][1][0]), "v"(pm[2][2][0]), "v"(pm[2][1][1]), "v"(pm[2][2][1]), "a"(fw1[7][3]), "v"(pm[2][1][2]), "v"(pm[2][2][2]), "v"(pm[2][1][3]), "v"(pm[2][2][3]), "a"(fw1[7][0]) : "memory"); SB();
  asm volatile("v_add_f32 %0, %0, %1"
      : "+v"(gm[0]) : "v"(gz[0])); SB();
  WAITVB(13);
  SB();
  asm volatile("v_mfma_f32_16x16x32_bf16 %0, %10, %11, %0\n\tbuffer_load_dwordx4 %1, %12, %13, %14 offen offset:512 sc1\n\tv_mfma_f32_16x16x32_bf16 %2, %15, %11, %2\n\tv_add_f32 %3, %3, %4\n\tv_add_f32 %5, %5, %16\n\tv_mfma_f32_16x16x32_bf16 %6, %17, %11, %6\n\tv_add_f32 %7, %7, %18\n\tv_mul_f32 %8, %19, %19\n\tv_mfma_f32_16x16x32_bf16 %9, %20, %11, %9\n\tv_mul_f32 %4, %3, %3"
      : "+v"(acc[3][1]), "=&v"(p[13]), "+v"(acc[3][2]), "+v"(gm[1]), "+v"(gz[1]), "+v"(gm[2]), "+v"(acc[3][3]), "+v"(gm[3]), "=&v"(gz[0]), "+v"(acc[3][0]) : "a"(fw1[8][1]), "v"(p[0]), "v"(vF), "s"(rsW), "s"(sh[5]), "a"(fw1[8][2]), "v"(gz[2]), "a"(fw1[8][3]), "v"(gz[3]), "v"(gm[0]), "a"(fw1[8][0]) : "memory"); SB();
  WAITVB(13);
  SB();
  asm volatile("v_mfma_f32_16x16x32_bf16 %0, %9, %10, %0\n\tbuffer_load_dwordx4 %1, %11, %12, %13 offen offset:512 sc1\n\tv_mfma_f32_16x16x32_bf16 %2, %14, %10, %2\n\tv_mul_f32 %3, %15, %15\n\tv_mul_f32 %4, %16, %16\n\tv_mfma_f32_16x16x32_bf16 %5, %17, %10, %5\n\tv_fmaak_f32 %6, %18, %6, 0x40135761\n\tv_fmaak_f32 %7, %18, %7, 0x40135761\n\tv_mfma_f32_16x16x32_bf16 %8, %19, %10, %8\n\tv_fmaak_f32 %3, %18, %3, 0x40135761"
      : "+v"(acc[3][1]), "=&v"(p[0]), "+v"(acc[3][2]), "=&v"(gz[2]), "=&v"(gz[3]), "+v"(acc[3][3]), "+v"(gz[0]), "+v"(gz[1]), "+v"(acc[3][0]) : "a"(fw1[9][1]), "v"(p[1]), "v"(vF), "s"(rsW), "s"(sh[6]), "a"(fw1[9][2]), "v"(gm[2]), "v"(gm[3]), "a"(fw1[9][3]), "v"(gc), "a"(fw1[9][0]) : "memory"); SB();
  WAITVB(13);
  SB();
  asm volatile("v_mfma_f32_16x16x32_bf16 %0, %9, %10, %0\n\tbuffer_load_dwordx4 %1, %11, %12, %13 offen offset:512 sc1\n\tv_mfma_f32_16x16x32_bf16 %2, %14, %10, %2\n\tv_fmaak_f32 %3, %15, %3, 0x40135761\n\tv_mul_f32 %4, %4, %16\n\tv_mfma_f32_16x16x32_bf16 %5, %17, %10, %5\n\tv_mul_f32 %6, %6, %18\n\tv_mul_f32 %7, %7, %19\n\tv_mfma_f32_16x16x32_bf16 %8, %20, %10, %8\n\tv_mul_f32 %3, %3, %21"
      : "+v"(acc[3][1]), "=&v"(p[1]), "+v"(acc[3][2]), "+v"(gz[3]), "+v"(gz[0]), "+v"(acc[3][3]), "+v"(gz[1]), "+v"(gz[2]), "+v"(acc[3][0]) : "a"(fw1[10][1]), "v"(p[2]), "v"(vF), "s"(rsW), "s"(sh[7]), "a"(fw1[10][2]), "v"(gc), "v"(gm[0]), "a"(fw1[10][3]), "v"(gm[1]), "v"(gm[2]), "a"(fw1[10][0]), "v"(gm[3]) : "memory"); SB();
  WAITVB(13);
  SB();
  asm volatile("v_mfma_f32_16x16x32_bf16 %0, %7, %8, %0\n\tbuffer_load_dwordx4 %1, %9, %10, %11 offen offset:512 sc1\n\tv_mfma_f32_16x16x32_bf16 %2, %12, %8, %2\n\tv_exp_f32 %3, %3\n\tv_mfma_f32_16x16x32_bf16 %4, %13, %8, %4\n\tv_exp_f32 %5, %5\n\tv_mfma_f32_16x16x32_bf16 %6, %14, %8, %6"
      : "+v"(acc[3][1]), "=&v"(p[2]), "+v"(acc[3][2]), "+v"(gz[0]), "+v"(acc[3][3]), "+v"(gz[1]), "+v"(acc[3][0]) : "a"(fw1[11][1]), "v"(p[3]), "v"(vF), "s"(rsW), "s"(sh[8]), "a"(fw1[11][2]), "a"(fw1[11][3]), "a"(fw1[11][0]) : "memory"); SB();
  WAITVB(13);
  SB();
  asm volatile("v_mfma_f32_16x16x32_bf16 %0, %8, %9, %0\n\tbuffer_load_dwordx4 %1, %10, %11, %12 offen offset:512 sc1\n\tv_mfma_f32_16x16x32_bf16 %2, %13, %9, %2\n\tv_exp_f32 %3, %3\n\tv_mfma_f32_16x16x32_bf16 %4, %14, %9, %4\n\tv_exp_f32 %5, %5\n\tv_mfma_f32_16x16x32_bf16 %6, %15, %9, %6\n\tv_add_f32 %7, 1.0, %7"
      : "+v"(acc[3][1]), "=&v"(p[3]), "+v"(acc[3][2]), "+v"(gz[2]), "+v"(acc[3][3]), "+v"(gz[3]), "+v"(acc[3][0]), "+v"(gz[0]) : "a"(fw1[12][1]), "v"(p[4]), "v"(vF), "s"(rsW), "s"(sh[9]), "a"(fw1[12][2]), "a"(fw1[12][3]), "a"(fw1[12][0]) : "memory"); SB();
  WAITVB(13);
  SB();
  asm volatile("v_mfma_f32_16x16x32_bf16 %0, %8, %9, %0\n\tbuffer_load_dwordx4 %1, %10, %11, %12 offen offset:512 sc1\n\tv_mfma_f32_16x16x32_bf16 %2, %13, %9, %2\n\tv_add_f32 %3, 1.0, %3\n\tv_add_f32 %4, 1.0, %4\n\tv_mfma_f32_16x16x32_bf16 %5, %14, %9, %5\n\tv_add_f32 %6, 1.0, %6\n\tv_mfma_f32_16x16x32_bf16 %7, %15, %9, %7"
      : "+v"(acc[3][1]), "=&v"(p[4]), "+v"(acc[3][2]), "+v"(gz[1]), "+v"(gz[2]), "+v"(acc[3][3]), "+v"(gz[3]), "+v"(acc[3][0]) : "a"(fw1[13][1]), "v"(p[5]), "v"(vF), "s"(rsW), "s"(sh[10]), "a"(fw1[13][2]), "a"(fw1[13][3]), "a"(fw1[13][0]) : "memory"); SB();
  WAITVB(13);
  SB();
  asm volatile("v_mfma_f32_16x16x32_bf16 %0, %7, %8, %0\n\tbuffer_load_dwordx4 %1, %9, %10, %11 offen offset:512 sc1\n\tv_mfma_f32_16x16x32_bf16 %2, %12, %8, %2\n\tv_rcp_f32 %3, %3\n\tv_mfma_f32_16x16x32_bf16 %4, %13, %8, %4\n\tv_rcp_f32 %5, %5\n\tv_mfma_f32_16x16x32_bf16 %6, %14, %8, %6"
      : "+v"(acc[3][1]), "=&v"(p[5]), "+v"(acc[3][2]), "+v"(gz[0]), "+v"(acc[3][3]), "+v"(gz[1]), "+v"(acc[3][0]) : "a"(fw1[14][1]), "v"(p[6]), "v"(vF), "s"(rsW), "s"(sh[11]), "a"(fw1[14][2]), "a"(fw1[14][3]), "a"(fw1[14][0]) : "memory"); SB();
  WAITVB(12);
  SB();
  asm volatile("v_mfma_f32_16x16x32_bf16 %0, %9, %10, %0\n\tbuffer_load_dwordx4 %1, %11, %12, %13 offen offset:512 sc1\n\tv_mfma_f32_16x16x32_bf16 %2, %14, %10, %2\n\tv_rcp_f32 %3, %3\n\tv_mfma_f32_16x16x32_bf16 %4, %15, %10, %4\n\tv_rcp_f32 %5, %5\n\tv_mfma_f32_16x16x32_bf16 %6, %16, %10, %6\n\tv_fma_f32 %7, -%17, %7, %17\n\tv_fma_f32 %8, -%18, %8, %18"
      : "+v"(acc[3][1]), "=&v"(p[6]), "+v"(acc[3][2]), "+v"(gz[2]), "+v"(acc[3][3]), "+v"(gz[3]), "+v"(acc[3][0]), "+v"(gz[0]), "+v"(gz[1]) : "v"(fw1[15][1]), "v"(p[7]), "v"(vF), "s"(rsW), "s"(sh[12]), "v"(fw1[15][2]), "v"(fw1[15][3]), "v"(fw1[15][0]), "v"(gm[0]), "v"(gm[1]) : "memory"); SB();
  STAMP(9);
  WAITVB(12);
  SB();
  asm volatile("v_mfma_f32_16x16x32_bf16 %0, %9, %10, 0\n\tbuffer_load_dwordx4 %1, %11, %12, %13 offen offset:512 sc1\n\tv_mfma_f32_16x16x32_bf16 %2, %14, %10, 0\n\tv_fma_f32 %3, -%15, %3, %15\n\tv_fma_f32 %4, -%16, %4, %16\n\tv_mfma_f32_16x16x32_bf16 %5, %17, %10, 0\n\tv_cvt_pk_bf16_f32 %6, %18, %19\n\tv_cvt_pk_bf16_f32 %7, %3, %4\n\tv_mfma_f32_16x16x32_bf16 %8, %20, %10, %21"
      : "=&v"(acc[4][1]), "=&v"(p[7]), "=&v"(acc[4][2]), "+v"(gz[2]), "+v"(gz[3]), "=&v"(acc[4][3]), "=&v"(uv[2][0]), "=&v"(uv[2][1]), "=&v"(acc[4][0]) : "a"(fw1[0][1]), "v"(p[8]), "v"(vF), "s"(rsW), "s"(sh[13]), "a"(fw1[0][2]), "v"(gm[2]), "v"(gm[3]), "a"(fw1[0][3]), "v"(gz[0]), "v"(gz[1]), "a"(fw1[0][0]), "v"(bb) : "memory"); SB();
  WAITVB(12);
  SB();
  asm volatile("v_mfma_f32_16x16x32_bf16 %0, %5, %6, %0\n\tbuffer_load_dwordx4 %1, %7, %8, %9 offen offset:512 sc1\n\tv_mfma_f32_16x16x32_bf16 %2, %10, %6, %2\n\tds_write_b128 %11, %12 offset:5120\n\tv_mfma_f32_16x16x32_bf16 %3, %13, %6, %3\n\tv_mfma_f32_16x16x32_bf16 %4, %14, %6, %4"
      : "+v"(acc[4][1]), "=&v"(p[8]), "+v"(acc[4][2]), "+v"(acc[4][3]), "+v"(acc[4][0]) : "a"(fw1[1][1]), "v"(p[9]), "v"(vF), "s"(rsW), "s"(sh[14]), "a"(fw1[1][2]), "v"(rw), "v"(acc[3][1]), "a"(fw1[1][3]), "a"(fw1[1][0]) : "memory"); SB();
  WAITVB(12);
  SB();
  asm volatile("v_mfma_f32_16x16x32_bf16 %0, %5, %6, %0\n\tbuffer_load_dwordx4 %1, %7, %8, %9 offen offset:512 sc1\n\tv_mfma_f32_16x16x32_bf16 %2, %10, %6, %2\n\tds_write_b128 %11, %12 offset:6144\n\tv_mfma_f32_16x16x32_bf16 %3, %13, %6, %3\n\tv_mfma_f32_16x16x32_bf16 %4, %14, %6, %4"
      : "+v"(acc[4][1]), "=&v"(p[9]), "+v"(acc[4][2]), "+v"(acc[4][3]), "+v"(acc[4][0]) : "a"(fw1[2][1]), "v"(p[10]), "v"(vF), "s"(rsW), "s"(sh[15]), "a"(fw1[2][2]), "v"(rw), "v"(acc[3][2]), "a"(fw1[2][3]), "a"(fw1[2][0]) : "memory"); SB();
  WAITVB(12);
  SB();
  asm volatile("v_mfma_f32_16x16x32_bf16 %0, %5, %6, %0\n\tbuffer_load_dwordx4 %1, %7, %8, %9 offen offset:640 sc1\n\tv_mfma_f32_16x16x32_bf16 %2, %10, %6, %2\n\tds_write_b128 %11, %12 offset:7168\n\tv_mfma_f32_16x16x32_bf16 %3, %13, %6, %3\n\tv_mfma_f32_16x16x32_bf16 %4, %14, %6, %4"
      : "+v"(acc[4][1]), "=&v"(p[10]), "+v"(acc[4][2]), "+v"(acc[4][3]), "+v"(acc[4][0]) : "a"(fw1[3][1]), "v"(p[11]), "v"(vF), "s"(rsW), "s"(sh[0]), "a"(fw1[3][2]), "v"(rw), "v"(acc[3][3]), "a"(fw1[3][3]), "a"(fw1[3][0]) : "memory"); SB();
  WAITVB(12);
  SB();
  asm volatile("v_mfma_f32_16x16x32_bf16 %0, %3, %4, %0\n\tbuffer_load_dwordx4 %1, %5, %6, %7 offen offset:640 sc1\n\tv_mfma_f32_16x16x32_bf16 %2, %8, %4, %2"
      : "+v"(acc[4][1]), "=&v"(p[11]), "+v"(acc[4][2]) : "a"(fw1[4][1]), "v"(p[12]), "v"(vF), "s"(rsW), "s"(sh[1]), "a"(fw1[4][2]) : "memory"); SB();
  BAR();
  SB();
  asm volatile("v_mfma_f32_16x16x32_bf16 %0, %5, %6, %0\n\tds_read_b128 %1, %7 offset:5120\n\tds_read_b128 %2, %8 offset:6144\n\tv_mfma_f32_16x16x32_bf16 %3, %9, %6, %3\n\tds_read_b128 %4, %10 offset:7168"
      : "+v"(acc[4][3]), "=&v"(pm[3][0]), "=&v"(pm[3][1]), "+v"(acc[4][0]), "=&v"(pm[3][2]) : "a"(fw1[4][3]), "v"(p[12]), "v"(rr1), "v"(rr2), "a"(fw1[4][0]), "v"(rr3) : "memory"); SB();
  WAITVB(12);
  SB();
  asm volatile("v_mfma_f32_16x16x32_bf16 %0, %5, %6, %0\n\tbuffer_load_dwordx4 %1, %7, %8, %9 offen offset:640 sc1\n\tv_mfma_f32_16x16x32_bf16 %2, %10, %6, %2\n\tv_mfma_f32_16x16x32_bf16 %3, %11, %6, %3\n\tv_mfma_f32_16x16x32_bf16 %4, %12, %6, %4"
      : "+v"(acc[4][1]), "=&v"(p[12]), "+v"(acc[4][2]), "+v"(acc[4][3]), "+v"(acc[4][0]) : "a"(fw1[5][1]), "v"(p[13]), "v"(vF), "s"(rsW), "s"(sh[2]), "a"(fw1[5][2]), "a"(fw1[5][3]), "a"(fw1[5][0]) : "memory"); SB();
  WAITVB(12);
  SB();
  asm volatile("v_mfma_f32_16x16x32_bf16 %0, %3, %4, %0\n\tbuffer_load_dwordx4 %1, %5, %6, %7 offen offset:640 sc1\n\tv_mfma_f32_16x16x32_bf16 %2, %8, %4, %2"
      : "+v"(acc[4][1]), "=&v"(p[13]), "+v"(acc[4][2]) : "a"(fw1[6][1]), "v"(p[0]), "v"(vF), "s"(rsW), "s"(sh[3]), "a"(fw1[6][2]) : "memory"); SB();
  WAITL(0);
  asm volatile("v_add_f32 %0, %6, %7\n\tv_mfma_f32_16x16x32_bf16 %1, %8, %9, %1\n\tv_add_f32 %2, %10, %11\n\tv_add_f32 %3, %12, %13\n\tv_mfma_f32_16x16x32_bf16 %4, %14, %9, %4\n\tv_add_f32 %5, %15, %16"
      : "=&v"(gm[0]), "+v"(acc[4][3]), "=&v"(gm[1]), "=&v"(gm[2]), "+v"(acc[4][0]), "=&v"(gm[3]) : "v"(acc[3][0][0]), "v"(pm[3][0][0]), "a"(fw1[6][3]), "v"(p[0]), "v"(acc[3][0][1]), "v"(pm[3][0][1]), "v"(acc[3][0][2]), "v"(pm[3][0][2]), "a"(fw1[6][0]), "v"(acc[3][0][3]), "v"(pm[3][0][3])); SB();
  WAITVB(12);
  SB();
  asm volatile("v_mfma_f32_16x16x32_bf16 %0, %9, %10, %0\n\tbuffer_load_dwordx4 %1, %11, %12, %13 offen offset:640 sc1\n\tv_mfma_f32_16x16x32_bf16 %2, %14, %10, %2\n\tv_add_f32 %3, %15, %16\n\tv_add_f32 %4, %17, %18\n\tv_mfma_f32_16x16x32_bf16 %5, %19, %10, %5\n\tv_add_f32 %6, %20, %21\n\tv_add_f32 %7, %22, %23\n\tv_mfma_f32_16x16x32_bf16 %8, %24, %10, %8"
      : "+v"(acc[4][1]), "=&v"(p[0]), "+v"(acc[4][2]), "=&v"(gz[0]), "=&v"(gz[1]), "+v"(acc[4][3]), "=&v"(gz[2]), "=&v"(gz[3]), "+v"(acc[4][0]) : "a"(fw1[7][1]), "v"(p[1]), "v"(vF), "s"(rsW), "s"(sh[4]), "a"(fw1[7][2]), "v"(pm[3][1][0]), "v"(pm[3][2][0]), "v"(pm[3][1][1]), "v"(pm[3][2][1]), "a"(fw1[7][3]), "v"(pm[3][1][2]), "v"(pm[3][2][2]), "v"(pm[3][1][3]), "v"(pm[3][2][3]), "a"(fw1[7][0]) : "memory"); SB();
  asm volatile("v_add_f32 %0, %0, %1"
      : "+v"(gm[0]) : "v"(gz[0])); SB();
  WAITVB(12);
  SB();
  asm volatile("v_mfma_f32_16x16x32_bf16 %0, %10, %11, %0\n\tbuffer_load_dwordx4 %1, %12, %13, %14 offen offset:640 sc1\n\tv_mfma_f32_16x16x32_bf16 %2, %15, %11, %2\n\tv_add_f32 %3, %3, %4\n\tv_add_f32 %5, %5, %16\n\tv_mfma_f32_16x16x32_bf16 %6, %17, %11, %6\n\tv_add_f32 %7, %7, %18\n\tv_mul_f32 %8, %19, %19\n\tv_mfma_f32_16x16x32_bf16 %9, %20, %11, %9\n\tv_mul_f32 %4, %3, %3"
      : "+v"(acc[4][1]), "=&v"(p[1]), "+v"(acc[4][2]), "+v"(gm[1]), "+v"(gz[1]), "+v"(gm[2]), "+v"(acc[4][3]), "+v"(gm[3]), "=&v"(gz[0]), "+v"(acc[4][0]) : "a"(fw1[8][1]), "v"(p[2]), "v"(vF), "s"(rsW), "s"(sh[5]), "a"(fw1[8][2]), "v"(gz[2]), "a"(fw1[8][3]), "v"(gz[3]), "v"(gm[0]), "a"(fw1[8][0]) : "memory"); SB();
  WAITVB(12);
  SB();
  asm volatile("v_mfma_f32_16x16x32_bf16 %0, %9, %10, %0\n\tbuffer_load_dwordx4 %1, %11, %12, %13 offen offset:640 sc1\n\tv_mfma_f32_16x16x32_bf16 %2, %14, %10, %2\n\tv_mul_f32 %3, %15, %15\n\tv_mul_f32 %4, %16, %16\n\tv_mfma_f32_16x16x32_bf16 %5, %17, %10, %5\n\tv_fmaak_f32 %6, %18, %6, 0x40135761\n\tv_fmaak_f32 %7, %18, %7, 0x40135761\n\tv_mfma_f32_16x16x32_bf16 %8, %19, %10, %8\n\tv_fmaak_f32 %3, %18, %3, 0x40135761"
      : "+v"(acc[4][1]), "=&v"(p[2]), "+v"(acc[4][2]), "=&v"(gz[2]), "=&v"(gz[3]), "+v"(acc[4][3]), "+v"(gz[0]), "+v"(gz[1]), "+v"(acc[4][0]) : "a"(fw1[9][1]), "v"(p[3]), "v"(vF), "s"(rsW), "s"(sh[6]), "a"(fw1[9][2]), "v"(gm[2]), "v"(gm[3]), "a"(fw1[9][3]), "v"(gc), "a"(fw1[9][0]) : "memory"); SB();
  WAITVB(12);
  SB();
  asm volatile("v_mfma_f32_16x16x32_bf16 %0, %9, %10, %0\n\tbuffer_load_dwordx4 %1, %11, %12, %13 offen offset:640 sc1\n\tv_mfma_f32_16x16x32_bf16 %2, %14, %10, %2\n\tv_fmaak_f32 %3, %15, %3, 0x40135761\n\tv_mul_f32 %4, %4, %16\n\tv_mfma_f32_16x16x32_bf16 %5, %17, %10, %5\n\tv_mul_f32 %6, %6, %18\n\tv_mul_f32 %7, %7, %19\n\tv_mfma_f32_16x16x32_bf16 %8, %20, %10, %8\n\tv_mul_f32 %3, %3, %21"
      : "+v"(acc[4][1]), "=&v"(p[3]), "+v"(acc[4][2]), "+v"(gz[3]), "+v"(gz[0]), "+v"(acc[4][3]), "+v"(gz[1]), "+v"(gz[2]), "+v"(acc[4][0]) : "a"(fw1[10][1]), "v"(p[4]), "v"(vF), "s"(rsW), "s"(sh[7]), "a"(fw1[10][2]), "v"(gc), "v"(gm[0]), "a"(fw1[10][3]), "v"(gm[1]), "v"(gm[2]), "a"(fw1[10][0]), "v"(gm[3]) : "memory"); SB();
  WAITVB(12);
  SB();
  asm volatile("v_mfma_f32_16x16x32_bf16 %0, %7, %8, %0\n\tbuffer_load_dwordx4 %1, %9, %10, %11 offen offset:640 sc1\n\tv_mfma_f32_16x16x32_bf16 %2, %12, %8, %2\n\tv_exp_f32 %3, %3\n\tv_mfma_f32_16x16x32_bf16 %4, %13, %8, %4\n\tv_exp_f32 %5, %5\n\tv_mfma_f32_16x16x32_bf16 %6, %14, %8, %6"
      : "+v"(acc[4][1]), "=&v"(p[4]), "+v"(acc[4][2]), "+v"(gz[0]), "+v"(acc[4][3]), "+v"(gz[1]), "+v"(acc[4][0]) : "a"(fw1[11][1]), "v"(p[5]), "v"(vF), "s"(rsW), "s"(sh[8]), "a"(fw1[11][2]), "a"(fw1[11][3]), "a"(fw1[11][0]) : "memory"); SB();
  WAITVB(12);
  SB();
  asm volatile("v_mfma_f32_16x16x32_bf16 %0, %8, %9, %0\n\tbuffer_load_dwordx4 %1, %10, %11, %12 offen offset:640 sc1\n\tv_mfma_f32_16x16x32_bf16 %2, %13, %9, %2\n\tv_exp_f32 %3, %3\n\tv_mfma_f32_16x16x32_bf16 %4, %14, %9, %4\n\tv_exp_f32 %5, %5\n\tv_mfma_f32_16x16x32_bf16 %6, %15, %9, %6\n\tv_add_f32 %7, 1.0, %7"
      : "+v"(acc[4][1]), "=&v"(p[5]), "+v"(acc[4][2]), "+v"(gz[2]), "+v"(acc[4][3]), "+v"(gz[3]), "+v"(acc[4][0]), "+v"(gz[0]) : "a"(fw1[12][1]), "v"(p[6]), "v"(vF), "s"(rsW), "s"(sh[9]), "a"(fw1[12][2]), "a"(fw1[12][3]), "a"(fw1[12][0]) : "memory"); SB();
  WAITVB(12);
  SB();
  asm volatile("v_mfma_f32_16x16x32_bf16 %0, %8, %9, %0\n\tbuffer_load_dwordx4 %1, %10, %11, %12 offen offset:640 sc1\n\tv_mfma_f32_16x16x32_bf16 %2, %13, %9, %2\n\tv_add_f32 %3, 1.0, %3\n\tv_add_f32 %4, 1.0, %4\n\tv_mfma_f32_16x16x32_bf16 %5, %14, %9, %5\n\tv_add_f32 %6, 1.0, %6\n\tv_mfma_f32_16x16x32_bf16 %7, %15, %9, %7"
      : "+v"(acc[4][1]), "=&v"(p[6]), "+v"(acc[4][2]), "+v"(gz[1]), "+v"(gz[2]), "+v"(acc[4][3]), "+v"(gz[3]), "+v"(acc[4][0]) : "a"(fw1[13][1]), "v"(p[7]), "v"(vF), "s"(rsW), "s"(sh[10]), "a"(fw1[13][2]), "a"(fw1[13][3]), "a"(fw1[13][0]) : "memory"); SB();
  WAITVB(12);
  SB();
  asm volatile("v_mfma_f32_16x16x32_bf16 %0, %7, %8, %0\n\tbuffer_load_dwordx4 %1, %9, %10, %11 offen offset:640 sc1\n\tv_mfma_f32_16x16x32_bf16 %2, %12, %8, %2\n\tv_rcp_f32 %3, %3\n\tv_mfma_f32_16x16x32_bf16 %4, %13, %8, %4\n\tv_rcp_f32 %5, %5\n\tv_mfma_f32_16x16x32_bf16 %6, %14, %8, %6"
      : "+v"(acc[4][1]), "=&v"(p[7]), "+v"(acc[4][2]), "+v"(gz[0]), "+v"(acc[4][3]), "+v"(gz[1]), "+v"(acc[4][0]) : "a"(fw1[14][1]), "v"(p[8]), "v"(vF), "s"(rsW), "s"(sh[11]), "a"(fw1[14][2]), "a"(fw1[14][3]), "a"(fw1[14][0]) : "memory"); SB();
  WAITVB(12);
  SB();
  asm volatile("v_mfma_f32_16x16x32_bf16 %0, %9, %10, %0\n\tbuffer_load_dwordx4 %1, %11, %12, %13 offen offset:640 sc1\n\tv_mfma_f32_16x16x32_bf16 %2, %14, %10, %2\n\tv_rcp_f32 %3, %3\n\tv_mfma_f32_16x16x32_bf16 %4, %15, %10, %4\n\tv_rcp_f32 %5, %5\n\tv_mfma_f32_16x16x32_bf16 %6, %16, %10, %6\n\tv_fma_f32 %7, -%17, %7, %17\n\tv_fma_f32 %8, -%18, %8, %18"
      : "+v"(acc[4][1]), "=&v"(p[8]), "+v"(acc[4][2]), "+v"(gz[2]), "+v"(acc[4][3]), "+v"(gz[3]), "+v"(acc[4][0]), "+v"(gz[0]), "+v"(gz[1]) : "v"(fw1[15][1]), "v"(p[9]), "v"(vF), "s"(rsW), "s"(sh[12]), "v"(fw1[15][2]), "v"(fw1[15][3]), "v"(fw1[15][0]), "v"(gm[0]), "v"(gm[1]) : "memory"); SB();
  WAITVB(12);
  SB();
  asm volatile("v_mfma_f32_16x16x32_bf16 %0, %9, %10, 0\n\tbuffer_load_dwordx4 %1, %11, %12, %13 offen offset:640 sc1\n\tv_mfma_f32_16x16x32_bf16 %2, %14, %10, 0\n\tv_fma_f32 %3, -%15, %3, %15\n\tv_fma_f32 %4, -%16, %4, %16\n\tv_mfma_f32_16x16x32_bf16 %5, %17, %10, 0\n\tv_cvt_pk_bf16_f32 %6, %18, %19\n\tv_cvt_pk_bf16_f32 %7, %3, %4\n\tv_mfma_f32_16x16x32_bf16 %8, %20, %10, %21"
      : "=&v"(acc[5][1]), "=&v"(p[9]), "=&v"(acc[5][2]), "+v"(gz[2]), "+v"(gz[3]), "=&v"(acc[5][3]), "=&v"(uv[3][0]), "=&v"(uv[3][1]), "=&v"(acc[5][0]) : "a"(fw1[0][1]), "v"(p[10]), "v"(vF), "s"(rsW), "s"(sh[13]), "a"(fw1[0][2]), "v"(gm[2]), "v"(gm[3]), "a"(fw1[0][3]), "v"(gz[0]), "v"(gz[1]), "a"(fw1[0][0]), "v"(bb) : "memory"); SB();
  WAITVB(12);
  SB();
  asm volatile("v_mfma_f32_16x16x32_bf16 %0, %5, %6, %0\n\tbuffer_load_dwordx4 %1, %7, %8, %9 offen offset:640 sc1\n\tv_mfma_f32_16x16x32_bf16 %2, %10, %6, %2\n\tds_write_b128 %11, %12 offset:1024\n\tv_mfma_f32_16x16x32_bf16 %3, %13, %6, %3\n\tv_mfma_f32_16x16x32_bf16 %4, %14, %6, %4"
      : "+v"(acc[5][1]), "=&v"(p[10]), "+v"(acc[5][2]), "+v"(acc[5][3]), "+v"(acc[5][0]) : "a"(fw1[1][1]), "v"(p[11]), "v"(vF), "s"(rsW), "s"(sh[14]), "a"(fw1[1][2]), "v"(rw), "v"(acc[4][1]), "a"(fw1[1][3]), "a"(fw1[1][0]) : "memory"); SB();
  WAITVB(12);
  SB();
  asm volatile("v_mfma_f32_16x16x32_bf16 %0, %5, %6, %0\n\tbuffer_load_dwordx4 %1, %7, %8, %9 offen offset:640 sc1\n\tv_mfma_f32_16x16x32_bf16 %2, %10, %6, %2\n\tds_write_b128 %11, %12 offset:2048\n\tv_mfma_f32_16x16x32_bf16 %3, %13, %6, %3\n\tv_mfma_f32_16x16x32_bf16 %4, %14, %6, %4"
      : "+v"(acc[5][1]), "=&v"(p[11]), "+v"(acc[5][2]), "+v"(acc[5][3]), "+v"(acc[5][0]) : "a"(fw1[2][1]), "v"(p[12]), "v"(vF), "s"(rsW), "s"(sh[15]), "a"(fw1[2][2]), "v"(rw), "v"(acc[4][2]), "a"(fw1[2][3]), "a"(fw1[2][0]) : "memory"); SB();
  WAITVB(12);
  SB();
  asm volatile("v_mfma_f32_16x16x32_bf16 %0, %5, %6, %0\n\tbuffer_load_dwordx4 %1, %7, %8, %9 offen offset:768 sc1\n\tv_mfma_f32_16x16x32_bf16 %2, %10, %6, %2\n\tds_write_b128 %11, %12 offset:3072\n\tv_mfma_f32_16x16x32_bf16 %3, %13, %6, %3\n\tv_mfma_f32_16x16x32_bf16 %4, %14, %6, %4"
      : "+v"(acc[5][1]), "=&v"(p[12]), "+v"(acc[5][2]), "+v"(acc[5][3]), "+v"(acc[5][0]) : "a"(fw1[3][1]), "v"(p[13]), "v"(vF), "s"(rsW), "s"(sh[0]), "a"(fw1[3][2]), "v"(rw), "v"(acc[4][3]), "a"(fw1[3][3]), "a"(fw1[3][0]) : "memory"); SB();
  WAITVB(12);
  SB();
  asm volatile("v_mfma_f32_16x16x32_bf16 %0, %3, %4, %0\n\tbuffer_load_dwordx4 %1, %5, %6, %7 offen offset:768 sc1\n\tv_mfma_f32_16x16x32_bf16 %2, %8, %4, %2"
      : "+v"(acc[5][1]), "=&v"(p[13]), "+v"(acc[5][2]) : "a"(fw1[4][1]), "v"(p[0]), "v"(vF), "s"(rsW), "s"(sh[1]), "a"(fw1[4][2]) : "memory"); SB();
  BAR();
  SB();
  asm volatile("v_mfma_f32_16x16x32_bf16 %0, %5, %6, %0\n\tds_read_b128 %1, %7 offset:1024\n\tds_read_b128 %2, %8 offset:2048\n\tv_mfma_f32_16x16x32_bf16 %3, %9, %6, %3\n\tds_read_b128 %4, %10 offset:3072"
      : "+v"(acc[5][3]), "=&v"(pm[4][0]), "=&v"(pm[4][1]), "+v"(acc[5][0]), "=&v"(pm[4][2]) : "a"(fw1[4][3]), "v"(p[0]), "v"(rr1), "v"(rr2), "a"(fw1[4][0]), "v"(rr3) : "memory"); SB();
  WAITVB(12);
  SB();
  asm volatile("v_mfma_f32_16x16x32_bf16 %0, %5, %6, %0\n\tbuffer_load_dwordx4 %1, %7, %8, %9 offen offset:768 sc1\n\tv_mfma_f32_16x16x32_bf16 %2, %10, %6, %2\n\tv_mfma_f32_16x16x32_bf16 %3, %11, %6, %3\n\tv_mfma_f32_16x16x32_bf16 %4, %12, %6, %4"
      : "+v"(acc[5][1]), "=&v"(p[0]), "+v"(acc[5][2]), "+v"(acc[5][3]), "+v"(acc[5][0]) : "a"(fw1[5][1]), "v"(p[1]), "v"(vF), "s"(rsW), "s"(sh[2]), "a"(fw1[5][2]), "a"(fw1[5][3]), "a"(fw1[5][0]) : "memory"); SB();
  WAITVB(12);
  SB();
  asm volatile("v_mfma_f32_16x16x32_bf16 %0, %3, %4, %0\n\tbuffer_load_dwordx4 %1, %5, %6, %7 offen offset:768 sc1\n\tv_mfma_f32_16x16x32_bf16 %2, %8, %4, %2"
      : "+v"(acc[5][1]), "=&v"(p[1]), "+v"(acc[5][2]) : "a"(fw1[6][1]), "v"(p[2]), "v"(vF), "s"(rsW), "s"(sh[3]), "a"(fw1[6][2]) : "memory"); SB();
  WAITL(0);
  asm volatile("v_add_f32 %0, %6, %7\n\tv_mfma_f32_16x16x32_bf16 %1, %8, %9, %1\n\tv_add_f32 %2, %10, %11\n\tv_add_f32 %3, %12, %13\n\tv_mfma_f32_16x16x32_bf16 %4, %14, %9, %4\n\tv_add_f32 %5, %15, %16"
      : "=&v"(gm[0]), "+v"(acc[5][3]), "=&v"(gm[1]), "=&v"(gm[2]), "+v"(acc[5][0]), "=&v"(gm[3]) : "v"(acc[4][0][0]), "v"(pm[4][0][0]), "a"(fw1[6][3]), "v"(p[2]), "v"(acc[4][0][1]), "v"(pm[4][0][1]), "v"(acc[4][0][2]), "v"(pm[4][0][2]), "a"(fw1[6][0]), "v"(acc[4][0][3]), "v"(pm[4][0][3])); SB();
  WAITVB(12);
  SB();
  asm volatile("v_mfma_f32_16x16x32_bf16 %0, %9, %10, %0\n\tbuffer_load_dwordx4 %1, %11, %12, %13 offen offset:768 sc1\n\tv_mfma_f32_16x16x32_bf16 %2, %14, %10, %2\n\tv_add_f32 %3, %15, %16\n\tv_add_f32 %4, %17, %18\n\tv_mfma_f32_16x16x32_bf16 %5, %19, %10, %5\n\tv_add_f32 %6, %20, %21\n\tv_add_f32 %7, %22, %23\n\tv_mfma_f32_16x16x32_bf16 %8, %24, %10, %8"
      : "+v"(acc[5][1]), "=&v"(p[2]), "+v"(acc[5][2]), "=&v"(gz[0]), "=&v"(gz[1]), "+v"(acc[5][3]), "=&v"(gz[2]), "=&v"(gz[3]), "+v"(acc[5][0]) : "a"(fw1[7][1]), "v"(p[3]), "v"(vF), "s"(rsW), "s"(sh[4]), "a"(fw1[7][2]), "v"(pm[4][1][0]), "v"(pm[4][2][0]), "v"(pm[4][1][1]), "v"(pm[4][2][1]), "a"(fw1[7][3]), "v"(pm[4][1][2]), "v"(pm[4][2][2]), "v"(pm[4][1][3]), "v"(pm[4][2][3]), "a"(fw1[7][0]) : "memory"); SB();
  asm volatile("v_add_f32 %0, %0, %1"
      : "+v"(gm[0]) : "v"(gz[0])); SB();
  WAITVB(12);
  SB();
  asm volatile("v_mfma_f32_16x16x32_bf16 %0, %10, %11, %0\n\tbuffer_load_dwordx4 %1, %12, %13, %14 offen offset:768 sc1\n\tv_mfma_f32_16x16x32_bf16 %2, %15, %11, %2\n\tv_add_f32 %3, %3, %4\n\tv_add_f32 %5, %5, %16\n\tv_mfma_f32_16x16x32_bf16 %6, %17, %11, %6\n\tv_add_f32 %7, %7, %18\n\tv_mul_f32 %8, %19, %19\n\tv_mfma_f32_16x16x32_bf16 %9, %20, %11, %9\n\tv_mul_f32 %4, %3, %3"
      : "+v"(acc[5][1]), "=&v"(p[3]), "+v"(acc[5][2]), "+v"(gm[1]), "+v"(gz[1]), "+v"(gm[2]), "+v"(acc[5][3]), "+v"(gm[3]), "=&v"(gz[0]), "+v"(acc[5][0]) : "a"(fw1[8][1]), "v"(p[4]), "v"(vF), "s"(rsW), "s"(sh[5]), "a"(fw1[8][2]), "v"(gz[2]), "a"(fw1[8][3]), "v"(gz[3]), "v"(gm[0]), "a"(fw1[8][0]) : "memory"); SB();
  WAITVB(12);
  SB();
  asm volatile("v_mfma_f32_16x16x32_bf16 %0, %9, %10, %0\n\tbuffer_load_dwordx4 %1, %11, %12, %13 offen offset:768 sc1\n\tv_mfma_f32_16x16x32_bf16 %2, %14, %10, %2\n\tv_mul_f32 %3, %15, %15\n\tv_mul_f32 %4, %16, %16\n\tv_mfma_f32_16x16x32_bf16 %5, %17, %10, %5\n\tv_fmaak_f32 %6, %18, %6, 0x40135761\n\tv_fmaak_f32 %7, %18, %7, 0x40135761\n\tv_mfma_f32_16x16x32_bf16 %8, %19, %10, %8\n\tv_fmaak_f32 %3, %18, %3, 0x40135761"
      : "+v"(acc[5][1]), "=&v"(p[4]), "+v"(acc[5][2]), "=&v"(gz[2]), "=&v"(gz[3]), "+v"(acc[5][3]), "+v"(gz[0]), "+v"(gz[1]), "+v"(acc[5][0]) : "a"(fw1[9][1]), "v"(p[5]), "v"(vF), "s"(rsW), "s"(sh[6]), "a"(fw1[9][2]), "v"(gm[2]), "v"(gm[3]), "a"(fw1[9][3]), "v"(gc), "a"(fw1[9][0]) : "memory"); SB();
  WAITVB(12);
  SB();
  asm volatile("v_mfma_f32_16x16x32_bf16 %0, %9, %10, %0\n\tbuffer_load_dwordx4 %1, %11, %12, %13 offen offset:768 sc1\n\tv_mfma_f32_16x16x32_bf16 %2, %14, %10, %2\n\tv_fmaak_f32 %3, %15, %3, 0x40135761\n\tv_mul_f32 %4, %4, %16\n\tv_mfma_f32_16x16x32_bf16 %5, %17, %10, %5\n\tv_mul_f32 %6, %6, %18\n\tv_mul_f32 %7, %7, %19\n\tv_mfma_f32_16x16x32_bf16 %8, %20, %10, %8\n\tv_mul_f32 %3, %3, %21"
      : "+v"(acc[5][1]), "=&v"(p[5]), "+v"(acc[5][2]), "+v"(gz[3]), "+v"(gz[0]), "+v"(acc[5][3]), "+v"(gz[1]), "+v"(gz[2]), "+v"(acc[5][0]) : "a"(fw1[10][1]), "v"(p[6]), "v"(vF), "s"(rsW), "s"(sh[7]), "a"(fw1[10][2]), "v"(gc), "v"(gm[0]), "a"(fw1[10][3]), "v"(gm[1]), "v"(gm[2]), "a"(fw1[10][0]), "v"(gm[3]) : "memory"); SB();
  WAITVB(12);
  SB();
  asm volatile("v_mfma_f32_16x16x32_bf16 %0, %7, %8, %0\n\tbuffer_load_dwordx4 %1, %9, %10, %11 offen offset:768 sc1\n\tv_mfma_f32_16x16x32_bf16 %2, %12, %8, %2\n\tv_exp_f32 %3, %3\n\tv_mfma_f32_16x16x32_bf16 %4, %13, %8, %4\n\tv_exp_f32 %5, %5\n\tv_mfma_f32_16x16x32_bf16 %6, %14, %8, %6"
      : "+v"(acc[5][1]), "=&v"(p[6]), "+v"(acc[5][2]), "+v"(gz[0]), "+v"(acc[5][3]), "+v"(gz[1]), "+v"(acc[5][0]) : "a"(fw1[11][1]), "v"(p[7]), "v"(vF), "s"(rsW), "s"(sh[8]), "a"(fw1[11][2]), "a"(fw1[11][3]), "a"(fw1[11][0]) : "memory"); SB();
  WAITVB(12);
  SB();
  asm volatile("v_mfma_f32_16x16x32_bf16 %0, %8, %9, %0\n\tbuffer_load_dwordx4 %1, %10, %11, %12 offen offset:768 sc1\n\tv_mfma_f32_16x16x32_bf16 %2, %13, %9, %2\n\tv_exp_f32 %3, %3\n\tv_mfma_f32_16x16x32_bf16 %4, %14, %9, %4\n\tv_exp_f32 %5, %5\n\tv_mfma_f32_16x16x32_bf16 %6, %15, %9, %6\n\tv_add_f32 %7, 1.0, %7"
      : "+v"(acc[5][1]), "=&v"(p[7]), "+v"(acc[5][2]), "+v"(gz[2]), "+v"(acc[5][3]), "+v"(gz[3]), "+v"(acc[5][0]), "+v"(gz[0]) : "a"(fw1[12][1]), "v"(p[8]), "v"(vF), "s"(rsW), "s"(sh[9]), "a"(fw1[12][2]), "a"(fw1[12][3]), "a"(fw1[12][0]) : "memory"); SB();
  WAITVB(12);
  SB();
  asm volatile("v_mfma_f32_16x16x32_bf16 %0, %8, %9, %0\n\tbuffer_load_dwordx4 %1, %10, %11, %12 offen offset:768 sc1\n\tv_mfma_f32_16x16x32_bf16 %2, %13, %9, %2\n\tv_add_f32 %3, 1.0, %3\n\tv_add_f32 %4, 1.0, %4\n\tv_mfma_f32_16x16x32_bf16 %5, %14, %9, %5\n\tv_add_f32 %6, 1.0, %6\n\tv_mfma_f32_16x16x32_bf16 %7, %15, %9, %7"
      : "+v"(acc[5][1]), "=&v"(p[8]), "+v"(acc[5][2]), "+v"(gz[1]), "+v"(gz[2]), "+v"(acc[5][3]), "+v"(gz[3]), "+v"(acc[5][0]) : "a"(fw1[13][1]), "v"(p[9]), "v"(vF), "s"(rsW), "s"(sh[10]), "a"(fw1[13][2]), "a"(fw1[13][3]), "a"(fw1[13][0]) : "memory"); SB();
  WAITVB(12);
  SB();
  asm volatile("v_mfma_f32_16x16x32_bf16 %0, %7, %8, %0\n\tbuffer_load_dwordx4 %1, %9, %10, %11 offen offset:768 sc1\n\tv_mfma_f32_16x16x32_bf16 %2, %12, %8, %2\n\tv_rcp_f32 %3, %3\n\tv_mfma_f32_16x16x32_bf16 %4, %13, %8, %4\n\tv_rcp_f32 %5, %5\n\tv_mfma_f32_16x16x32_bf16 %6, %14, %8, %6"
      : "+v"(acc[5][1]), "=&v"(p[9]), "+v"(acc[5][2]), "+v"(gz[0]), "+v"(acc[5][3]), "+v"(gz[1]), "+v"(acc[5][0]) : "a"(fw1[14][1]), "v"(p[10]), "v"(vF), "s"(rsW), "s"(sh[11]), "a"(fw1[14][2]), "a"(fw1[14][3]), "a"(fw1[14][0]) : "memory"); SB();
  WAITVB(12);
  SB();
  asm volatile("v_mfma_f32_16x16x32_bf16 %0, %9, %10, %0\n\tbuffer_load_dwordx4 %1, %11, %12, %13 offen offset:768 sc1\n\tv_mfma_f32_16x16x32_bf16 %2, %14, %10, %2\n\tv_rcp_f32 %3, %3\n\tv_mfma_f32_16x16x32_bf16 %4, %15, %10, %4\n\tv_rcp_f32 %5, %5\n\tv_mfma_f32_16x16x32_bf16 %6, %16, %10, %6\n\tv_fma_f32 %7, -%17, %7, %17\n\tv_fma_f32 %8, -%18, %8, %18"
      : "+v"(acc[5][1]), "=&v"(p[10]), "+v"(acc[5][2]), "+v"(gz[2]), "+v"(acc[5][3]), "+v"(gz[3]), "+v"(acc[5][0]), "+v"(gz[0]), "+v"(gz[1]) : "v"(fw1[15][1]), "v"(p[11]), "v"(vF), "s"(rsW), "s"(sh[12]), "v"(fw1[15][2]), "v"(fw1[15][3]), "v"(fw1[15][0]), "v"(gm[0]), "v"(gm[1]) : "memory"); SB();
  WAITVB(12);
  SB();
  asm volatile("v_mfma_f32_16x16x32_bf16 %0, %9, %10, 0\n\tbuffer_load_dwordx4 %1, %11, %12, %13 offen offset:768 sc1\n\tv_mfma_f32_16x16x32_bf16 %2, %14, %10, 0\n\tv_fma_f32 %3, -%15, %3, %15\n\tv_fma_f32 %4, -%16, %4, %16\n\tv_mfma_f32_16x16x32_bf16 %5, %17, %10, 0\n\tv_cvt_pk_bf16_f32 %6, %18, %19\n\tv_cvt_pk_bf16_f32 %7, %3, %4\n\tv_mfma_f32_16x16x32_bf16 %8, %20, %10, %21"
      : "=&v"(acc[6][1]), "=&v"(p[11]), "=&v"(acc[6][2]), "+v"(gz[2]), "+v"(gz[3]), "=&v"(acc[6][3]), "=&v"(uv[4][0]), "=&v"(uv[4][1]), "=&v"(acc[6][0]) : "a"(fw1[0][1]), "v"(p[12]), "v"(vF), "s"(rsW), "s"(sh[13]), "a"(fw1[0][2]), "v"(gm[2]), "v"(gm[3]), "a"(fw1[0][3]), "v"(gz[0]), "v"(gz[1]), "a"(fw1[0][0]), "v"(bb) : "memory"); SB();
  WAITVB(12);
  SB();
  asm volatile("v_mfma_f32_16x16x32_bf16 %0, %5, %6, %0\n\tbuffer_load_dwordx4 %1, %7, %8, %9 offen offset:768 sc1\n\tv_mfma_f32_16x16x32_bf16 %2, %10, %6, %2\n\tds_write_b128 %11, %12 offset:5120\n\tv_mfma_f32_16x16x32_bf16 %3, %13, %6, %3\n\tv_mfma_f32_16x16x32_bf16 %4, %14, %6, %4"
      : "+v"(acc[6][1]), "=&v"(p[12]), "+v"(acc[6][2]), "+v"(acc[6][3]), "+v"(acc[6][0]) : "a"(fw1[1][1]), "v"(p[13]), "v"(vF), "s"(rsW), "s"(sh[14]), "a"(fw1[1][2]), "v"(rw), "v"(acc[5][1]), "a"(fw1[1][3]), "a"(fw1[1][0]) : "memory"); SB();
  WAITVB(12);
  SB();
  asm volatile("v_mfma_f32_16x16x32_bf16 %0, %5, %6, %0\n\tbuffer_load_dwordx4 %1, %7, %8, %9 offen offset:768 sc1\n\tv_mfma_f32_16x16x32_bf16 %2, %10, %6, %2\n\tds_write_b128 %11, %12 offset:6144\n\tv_mfma_f32_16x16x32_bf16 %3, %13, %6, %3\n\tv_mfma_f32_16x16x32_bf16 %4, %14, %6, %4"
      : "+v"(acc[6][1]), "=&v"(p[13]), "+v"(acc[6][2]), "+v"(acc[6][3]), "+v"(acc[6][0]) : "a"(fw1[2][1]), "v"(p[0]), "v"(vF), "s"(rsW), "s"(sh[15]), "a"(fw1[2][2]), "v"(rw), "v"(acc[5][2]), "a"(fw1[2][3]), "a"(fw1[2][0]) : "memory"); SB();
  WAITVB(12);
  SB();
  asm volatile("v_mfma_f32_16x16x32_bf16 %0, %5, %6, %0\n\tv_permlane16_swap_b32 %1, %2\n\tv_permlane16_swap_b32 %3, %4"
      : "+v"(acc[6][1]), "+v"(uv[0][0]), "+v"(uv[1][0]), "+v"(uv[0][1]), "+v"(uv[1][1]) : "a"(fw1[3][1]), "v"(p[1])); SB();
  HS16(vU, sU, 0, ((u32x4){uv[0][0], uv[0][1], uv[1][0], uv[1][1]}));
  SB();
  asm volatile("v_mfma_f32_16x16x32_bf16 %0, %6, %7, %0\n\tds_write_b128 %8, %9 offset:7168\n\tv_mfma_f32_16x16x32_bf16 %1, %10, %7, %1\n\tv_permlane16_swap_b32 %2, %3\n\tv_permlane16_swap_b32 %4, %5"
      : "+v"(acc[6][2]), "+v"(acc[6][3]), "+v"(uv[2][0]), "+v"(uv[3][0]), "+v"(uv[2][1]), "+v"(uv[3][1]) : "a"(fw1[3][2]), "v"(p[1]), "v"(rw), "v"(acc[5][3]), "a"(fw1[3][3]) : "memory"); SB();
  HS16(vU, sU, 256, ((u32x4){uv[2][0], uv[2][1], uv[3][0], uv[3][1]}));
  SB();
  asm volatile("v_mfma_f32_16x16x32_bf16 %0, %1, %2, %0"
      : "+v"(acc[6][0]) : "a"(fw1[3][0]), "v"(p[1])); SB();
  WAITVB(13);
  SB();
  asm volatile("v_mfma_f32_16x16x32_bf16 %0, %2, %3, %0\n\ts_add_u32 m0, %4, 0\n\ts_nop 0\n\tv_mfma_f32_16x16x32_bf16 %1, %5, %3, %1"
      : "+v"(acc[6][1]), "+v"(acc[6][2]) : "a"(fw1[4][1]), "v"(p[2]), "s"(ldsO), "a"(fw1[4][2]) : "memory", "scc"); SB();
  BAR();
  SB();
  asm volatile("v_mfma_f32_16x16x32_bf16 %0, %5, %6, %0\n\tds_read_b128 %1, %7 offset:5120\n\tds_read_b128 %2, %8 offset:6144\n\tv_mfma_f32_16x16x32_bf16 %3, %9, %6, %3\n\tds_read_b128 %4, %10 offset:7168"
      : "+v"(acc[6][3]), "=&v"(pm[5][0]), "=&v"(pm[5][1]), "+v"(acc[6][0]), "=&v"(pm[5][2]) : "a"(fw1[4][3]), "v"(p[2]), "v"(rr1), "v"(rr2), "a"(fw1[4][0]), "v"(rr3) : "memory"); SB();
  WAITVB(12);
  SB();
  asm volatile("v_mfma_f32_16x16x32_bf16 %0, %4, %5, %0\n\tbuffer_load_dwordx4 %6, %7, %8 offen offset:0  lds\n\tv_mfma_f32_16x16x32_bf16 %1, %9, %5, %1\n\tv_mfma_f32_16x16x32_bf16 %2, %10, %5, %2\n\tbuffer_load_dwordx4 %11, %7, %8 offen offset:1024  lds\n\tv_mfma_f32_16x16x32_bf16 %3, %12, %5, %3"
      : "+v"(acc[6][1]), "+v"(acc[6][2]), "+v"(acc[6][3]), "+v"(acc[6][0]) : "a"(fw1[5][1]), "v"(p[3]), "v"(vW2[0]), "s"(rs2), "s"(sw[0]), "a"(fw1[5][2]), "a"(fw1[5][3]), "v"(vW2[1]), "a"(fw1[5][0]) : "memory"); SB();
  WAITVB(13);
  SB();
  asm volatile("v_mfma_f32_16x16x32_bf16 %0, %1, %2, %0"
      : "+v"(acc[6][1]) : "a"(fw1[6][1]), "v"(p[4])); SB();
  WAITL(0);
  asm volatile("v_add_f32 %0, %9, %10\n\tv_mfma_f32_16x16x32_bf16 %1, %11, %12, %1\n\tv_add_f32 %2, %13, %14\n\tv_add_f32 %3, %15, %16\n\tv_mfma_f32_16x16x32_bf16 %4, %17, %12, %4\n\tv_add_f32 %5, %18, %19\n\tv_add_f32 %6, %20, %21\n\tv_mfma_f32_16x16x32_bf16 %7, %22, %12, %7\n\tv_add_f32 %8, %23, %24"
      : "=&v"(gm[0]), "+v"(acc[6][2]), "=&v"(gm[1]), "=&v"(gm[2]), "+v"(acc[6][3]), "=&v"(gm[3]), "=&v"(gz[0]), "+v"(acc[6][0]), "=&v"(gz[1]) : "v"(acc[5][0][0]), "v"(pm[5][0][0]), "a"(fw1[6][2]), "v"(p[4]), "v"(acc[5][0][1]), "v"(pm[5][0][1]), "v"(acc[5][0][2]), "v"(pm[5][0][2]), "a"(fw1[6][3]), "v"(acc[5][0][3]), "v"(pm[5][0][3]), "v"(pm[5][1][0]), "v"(pm[5][2][0]), "a"(fw1[6][0]), "v"(pm[5][1][1]), "v"(pm[5][2][1])); SB();
  WAITVB(12);
  SB();
  asm volatile("v_mfma_f32_16x16x32_bf16 %0, %11, %12, %0\n\tv_add_f32 %1, %13, %14\n\tv_add_f32 %2, %15, %16\n\tv_mfma_f32_16x16x32_bf16 %3, %17, %12, %3\n\tv_add_f32 %4, %4, %5\n\tv_add_f32 %6, %6, %18\n\tv_mfma_f32_16x16x32_bf16 %7, %19, %12, %7\n\tv_add_f32 %8, %8, %1\n\tv_add_f32 %9, %9, %2\n\tv_mfma_f32_16x16x32_bf16 %10, %20, %12, %10\n\tv_mul_f32 %5, %4, %4"
      : "+v"(acc[6][1]), "=&v"(gz[2]), "=&v"(gz[3]), "+v"(acc[6][2]), "+v"(gm[0]), "+v"(gz[0]), "+v"(gm[1]), "+v"(acc[6][3]), "+v"(gm[2]), "+v"(gm[3]), "+v"(acc[6][0]) : "a"(fw1[7][1]), "v"(p[5]), "v"(pm[5][1][2]), "v"(pm[5][2][2]), "v"(pm[5][1][3]), "v"(pm[5][2][3]), "a"(fw1[7][2]), "v"(gz[1]), "a"(fw1[7][3]), "a"(fw1[7][0])); SB();
  WAITVB(11);
  SB();
  asm volatile("v_mfma_f32_16x16x32_bf16 %0, %8, %9, %0\n\tv_mul_f32 %1, %10, %10\n\tv_mul_f32 %2, %11, %11\n\tv_mfma_f32_16x16x32_bf16 %3, %12, %9, %3\n\tv_mul_f32 %4, %13, %13\n\tv_fmaak_f32 %5, %14, %5, 0x40135761\n\tv_mfma_f32_16x16x32_bf16 %6, %15, %9, %6\n\tv_fmaak_f32 %1, %14, %1, 0x40135761\n\tv_fmaak_f32 %2, %14, %2, 0x40135761\n\tv_mfma_f32_16x16x32_bf16 %7, %16, %9, %7\n\tv_fmaak_f32 %4, %14, %4, 0x40135761"
      : "+v"(acc[6][1]), "=&v"(gz[1]), "=&v"(gz[2]), "+v"(acc[6][2]), "=&v"(gz[3]), "+v"(gz[0]), "+v"(acc[6][3]), "+v"(acc[6][0]) : "a"(fw1[8][1]), "v"(p[6]), "v"(gm[1]), "v"(gm[2]), "a"(fw1[8][2]), "v"(gm[3]), "v"(gc), "a"(fw1[8][3]), "a"(fw1[8][0])); SB();
  WAITVB(10);
  SB();
  asm volatile("v_mfma_f32_16x16x32_bf16 %0, %8, %9, %0\n\tv_mul_f32 %1, %1, %10\n\tv_mul_f32 %2, %2, %11\n\tv_mfma_f32_16x16x32_bf16 %3, %12, %9, %3\n\tv_mul_f32 %4, %4, %13\n\tv_mul_f32 %5, %5, %14\n\tv_mfma_f32_16x16x32_bf16 %6, %15, %9, %6\n\tv_exp_f32 %1, %1\n\tv_mfma_f32_16x16x32_bf16 %7, %16, %9, %7"
      : "+v"(acc[6][1]), "+v"(gz[0]), "+v"(gz[1]), "+v"(acc[6][2]), "+v"(gz[2]), "+v"(gz[3]), "+v"(acc[6][3]), "+v"(acc[6][0]) : "a"(fw1[9][1]), "v"(p[7]), "v"(gm[0]), "v"(gm[1]), "a"(fw1[9][2]), "v"(gm[2]), "v"(gm[3]), "a"(fw1[9][3]), "a"(fw1[9][0])); SB();
  WAITVB(9);
  SB();
  asm volatile("v_mfma_f32_16x16x32_bf16 %0, %8, %9, %0\n\tv_exp_f32 %1, %1\n\tv_mfma_f32_16x16x32_bf16 %2, %10, %9, %2\n\tv_exp_f32 %3, %3\n\tv_mfma_f32_16x16x32_bf16 %4, %11, %9, %4\n\tv_exp_f32 %5, %5\n\tv_mfma_f32_16x16x32_bf16 %6, %12, %9, %6\n\tv_add_f32 %7, 1.0, %7"
      : "+v"(acc[6][1]), "+v"(gz[1]), "+v"(acc[6][2]), "+v"(gz[2]), "+v"(acc[6][3]), "+v"(gz[3]), "+v"(acc[6][0]), "+v"(gz[0]) : "a"(fw1[10][1]), "v"(p[8]), "a"(fw1[10][2]), "a"(fw1[10][3]), "a"(fw1[10][0])); SB();
  WAITVB(8);
  SB();
  asm volatile("v_mfma_f32_16x16x32_bf16 %0, %8, %9, %0\n\tv_add_f32 %1, 1.0, %1\n\tv_add_f32 %2, 1.0, %2\n\tv_mfma_f32_16x16x32_bf16 %3, %10, %9, %3\n\tv_add_f32 %4, 1.0, %4\n\tv_mfma_f32_16x16x32_bf16 %5, %11, %9, %5\n\tv_rcp_f32 %6, %6\n\tv_mfma_f32_16x16x32_bf16 %7, %12, %9, %7"
      : "+v"(acc[6][1]), "+v"(gz[1]), "+v"(gz[2]), "+v"(acc[6][2]), "+v"(gz[3]), "+v"(acc[6][3]), "+v"(gz[0]), "+v"(acc[6][0]) : "a"(fw1[11][1]), "v"(p[9]), "a"(fw1[11][2]), "a"(fw1[11][3]), "a"(fw1[11][0])); SB();
  WAITVB(7);
  SB();
  asm volatile("v_mfma_f32_16x16x32_bf16 %0, %8, %9, %0\n\tv_rcp_f32 %1, %1\n\tv_mfma_f32_16x16x32_bf16 %2, %10, %9, %2\n\tv_rcp_f32 %3, %3\n\tv_mfma_f32_16x16x32_bf16 %4, %11, %9, %4\n\tv_rcp_f32 %5, %5\n\tv_mfma_f32_16x16x32_bf16 %6, %12, %9, %6\n\tv_fma_f32 %7, -%13, %7, %13"
      : "+v"(acc[6][1]), "+v"(gz[1]), "+v"(acc[6][2]), "+v"(gz[2]), "+v"(acc[6][3]), "+v"(gz[3]), "+v"(acc[6][0]), "+v"(gz[0]) : "a"(fw1[12][1]), "v"(p[10]), "a"(fw1[12][2]), "a"(fw1[12][3]), "a"(fw1[12][0]), "v"(gm[0])); SB();
  WAITVB(6);
  SB();
  asm volatile("v_mfma_f32_16x16x32_bf16 %0, %9, %10, %0\n\tv_fma_f32 %1, -%11, %1, %11\n\tv_fma_f32 %2, -%12, %2, %12\n\tv_mfma_f32_16x16x32_bf16 %3, %13, %10, %3\n\tv_fma_f32 %4, -%14, %4, %14\n\tv_cvt_pk_bf16_f32 %5, %15, %1\n\tv_mfma_f32_16x16x32_bf16 %6, %16, %10, %6\n\tv_cvt_pk_bf16_f32 %7, %2, %4\n\tv_mfma_f32_16x16x32_bf16 %8, %17, %10, %8"
      : "+v"(acc[6][1]), "+v"(gz[1]), "+v"(gz[2]), "+v"(acc[6][2]), "+v"(gz[3]), "=&v"(uv[5][0]), "+v"(acc[6][3]), "=&v"(uv[5][1]), "+v"(acc[6][0]) : "a"(fw1[13][1]), "v"(p[11]), "v"(gm[1]), "v"(gm[2]), "a"(fw1[13][2]), "v"(gm[3]), "v"(gz[0]), "a"(fw1[13][3]), "a"(fw1[13][0])); SB();
  WAITVB(5);
  SB();
  asm volatile("v_mfma_f32_16x16x32_bf16 %0, %5, %6, %0\n\tv_permlane16_swap_b32 %1, %2\n\tv_permlane16_swap_b32 %3, %4"
      : "+v"(acc[6][1]), "+v"(uv[4][0]), "+v"(uv[5][0]), "+v"(uv[4][1]), "+v"(uv[5][1]) : "a"(fw1[14][1]), "v"(p[12])); SB();
  HS16(vU, sU, 512, ((u32x4){uv[4][0], uv[4][1], uv[5][0], uv[5][1]}));
  SB();
  asm volatile("v_mfma_f32_16x16x32_bf16 %0, %3, %4, %0\n\tbuffer_load_dwordx4 %5, %6, %7 offen offset:2048  lds\n\tv_mfma_f32_16x16x32_bf16 %1, %8, %4, %1\n\tv_mfma_f32_16x16x32_bf16 %2, %9, %4, %2"
      : "+v"(acc[6][2]), "+v"(acc[6][3]), "+v"(acc[6][0]) : "a"(fw1[14][2]), "v"(p[12]), "v"(vW2[2]), "s"(rs2), "s"(sw[0]), "a"(fw1[14][3]), "a"(fw1[14][0]) : "memory"); SB();
  WAITVB(6);
  SB();
  asm volatile("v_mfma_f32_16x16x32_bf16 %0, %4, %5, %0\n\tbuffer_load_dwordx4 %6, %7, %8 offen offset:3072  lds\n\tv_mfma_f32_16x16x32_bf16 %1, %9, %5, %1\n\tv_mfma_f32_16x16x32_bf16 %2, %10, %5, %2\n\ts_add_u32 m0, %11, 4096\n\ts_nop 0\n\tv_mfma_f32_16x16x32_bf16 %3, %12, %5, %3"
      : "+v"(acc[6][1]), "+v"(acc[6][2]), "+v"(acc[6][3]), "+v"(acc[6][0]) : "v"(fw1[15][1]), "v"(p[13]), "v"(vW2[3]), "s"(rs2), "s"(sw[0]), "v"(fw1[15][2]), "v"(fw1[15][3]), "s"(ldsO), "v"(fw1[15][0]) : "memory", "scc"); SB();
  STAMP(8);
  asm volatile("s_nop 4\n\tds_write_b128 %0, %1 offset:1024\n\tds_write_b128 %0, %2 offset:2048\n\tds_write_b128 %0, %3 offset:3072\n\tbuffer_load_dwordx4 %4, %5, %6 offen offset:0  lds\n\tbuffer_load_dwordx4 %7, %5, %6 offen offset:1024  lds\n\tbuffer_load_dwordx4 %8, %5, %6 offen offset:2048  lds\n\tbuffer_load_dwordx4 %9, %5, %6 offen offset:3072  lds"
      :  : "v"(rw), "v"(acc[6][1]), "v"(acc[6][2]), "v"(acc[6][3]), "v"(vW2[0]), "s"(rs2), "s"(sw[1]), "v"(vW2[1]), "v"(vW2[2]), "v"(vW2[3]) : "memory"); SB();
  BAR();
  asm volatile("ds_read_b128 %0, %3 offset:1024\n\tds_read_b128 %1, %4 offset:2048\n\tds_read_b128 %2, %5 offset:3072"
      : "=&v"(pm[6][0]), "=&v"(pm[6][1]), "=&v"(pm[6][2]) : "v"(rr1), "v"(rr2), "v"(rr3) : "memory"); SB();
  WAITL(0);
  asm volatile("v_add_f32 %0, %12, %13\n\tv_add_f32 %1, %14, %15\n\tv_add_f32 %2, %16, %17\n\tv_add_f32 %3, %18, %19\n\tv_add_f32 %4, %20, %21\n\tv_add_f32 %5, %22, %23\n\tv_add_f32 %6, %24, %25\n\tv_add_f32 %7, %26, %27\n\tv_add_f32 %0, %0, %4\n\tv_add_f32 %1, %1, %5\n\tv_add_f32 %2, %2, %6\n\tv_add_f32 %3, %3, %7\n\tv_mul_f32 %4, %0, %0\n\tv_mul_f32 %5, %1, %1\n\tv_mul_f32 %6, %2, %2\n\tv_mul_f32 %7, %3, %3\n\tv_fmaak_f32 %4, %28, %4, 0x40135761\n\tv_fmaak_f32 %5, %28, %5, 0x40135761\n\tv_fmaak_f32 %6, %28, %6, 0x40135761\n\tv_fmaak_f32 %7, %28, %7, 0x40135761\n\tv_mul_f32 %4, %4, %0\n\tv_mul_f32 %5, %5, %1\n\tv_mul_f32 %6, %6, %2\n\tv_mul_f32 %7, %7, %3\n\tv_exp_f32 %4, %4\n\tv_exp_f32 %5, %5\n\tv_exp_f32 %6, %6\n\tv_exp_f32 %7, %7\n\tv_add_f32 %4, 1.0, %4\n\tv_add_f32 %5, 1.0, %5\n\tv_add_f32 %6, 1.0, %6\n\tv_add_f32 %7, 1.0, %7\n\tv_rcp_f32 %4, %4\n\tv_rcp_f32 %5, %5\n\tv_rcp_f32 %6, %6\n\tv_rcp_f32 %7, %7\n\tv_fma_f32 %4, -%0, %4, %0\n\tv_fma_f32 %5, -%1, %5, %1\n\tv_fma_f32 %6, -%2, %6, %2\n\tv_fma_f32 %7, -%3, %7, %3\n\tv_cvt_pk_bf16_f32 %8, %4, %5\n\tv_cvt_pk_bf16_f32 %9, %6, %7\n\tv_cvt_pk_bf16_f32 %10, %4, %5\n\tv_cvt_pk_bf16_f32 %11, %6, %7\n\ts_nop 0\n\tv_permlane16_swap_b32 %8, %10\n\tv_permlane16_swap_b32 %9, %11"
      : "=&v"(gm[0]), "=&v"(gm[1]), "=&v"(gm[2]), "=&v"(gm[3]), "=&v"(gz[0]), "=&v"(gz[1]), "=&v"(gz[2]), "=&v"(gz[3]), "=&v"(uv[6][0]), "=&v"(uv[6][1]), "=&v"(uv[7][0]), "=&v"(uv[7][1]) : "v"(acc[6][0][0]), "v"(pm[6][0][0]), "v"(acc[6][0][1]), "v"(pm[6][0][1]), "v"(acc[6][0][2]), "v"(pm[6][0][2]), "v"(acc[6][0][3]), "v"(pm[6][0][3]), "v"(pm[6][1][0]), "v"(pm[6][2][0]), "v"(pm[6][1][1]), "v"(pm[6][2][1]), "v"(pm[6][1][2]), "v"(pm[6][2][2]), "v"(pm[6][1][3]), "v"(pm[6][2][3]), "v"(gc)); SB();
  HS16M(0xffff, 0xffff, vU, sU, 768, ((u32x4){uv[6][0], uv[6][1], uv[7][0], uv[7][1]}));
  STAMP(10);
  asm volatile("s_add_u32 m0, %0, 18432\n\ts_nop 0\n\tbuffer_load_dwordx4 %1, %2, %3 offen offset:0  lds\n\tbuffer_load_dwordx4 %4, %2, %3 offen offset:1024  lds\n\tbuffer_load_dwordx4 %5, %2, %3 offen offset:2048  lds\n\tbuffer_load_dwordx4 %6, %2, %3 offen offset:3072  lds\n\ts_add_u32 m0, %0, 34816\n\ts_nop 0\n\tbuffer_load_dwordx4 %1, %2, %7 offen offset:0  lds\n\tbuffer_load_dwordx4 %4, %2, %7 offen offset:1024  lds\n\tbuffer_load_dwordx4 %5, %2, %7 offen offset:2048  lds\n\tbuffer_load_dwordx4 %6, %2, %7 offen offset:3072  lds\n\ts_waitcnt vmcnt(13)"
      :  : "s"(ldsS), "v"(vW2[0]), "s"(rs2), "s"(sw[2]), "v"(vW2[1]), "v"(vW2[2]), "v"(vW2[3]), "s"(sw[3]) : "memory", "scc"); SB();
  fw2[0][0] = tr8u(tO[0] + 0, 512);
  fw2[0][1] = tr8u(tO[1] + 0, 512);
  fw2[0][2] = tr8u(tO[2] + 0, 512);
  fw2[0][3] = tr8u(tO[3] + 0, 512);
  asm volatile("s_waitcnt vmcnt(9)"
      :  :  : "memory"); SB();
  fw2[1][0] = tr8u(tO[0] + 4096, 512);
  fw2[1][1] = tr8u(tO[1] + 4096, 512);
  fw2[1][2] = tr8u(tO[2] + 4096, 512);
  fw2[1][3] = tr8u(tO[3] + 4096, 512);
  WAITV1(8, fq);
  STFU(sFU);
  STAMP(11);
  {
  if (__ballot(fq != nonce) != 0ull) poll1<4>(rsW, vFQ, nonce);
  unsigned fu;
  asm volatile("s_add_u32 m0, %0, 0\n\ts_nop 0\n\tbuffer_load_dwordx4 %1, %2, %3 offen offset:0  lds\n\tbuffer_load_dwordx4 %4, %2, %3 offen offset:1024  lds\n\tbuffer_load_dwordx4 %5, %2, %3 offen offset:2048  lds\n\tbuffer_load_dwordx4 %6, %2, %3 offen offset:3072  lds\n\ts_waitcnt vmcnt(9)"
      :  : "s"(ldsS), "v"(vW2[0]), "s"(rs2), "s"(sw[4]), "v"(vW2[1]), "v"(vW2[2]), "v"(vW2[3]) : "memory", "scc"); SB();
  fw2[2][0] = tr8u(tS[0] + 18432, 512);
  fw2[2][1] = tr8u(tS[1] + 18432, 512);
  fw2[2][2] = tr8u(tS[2] + 18432, 512);
  fw2[2][3] = tr8u(tS[3] + 18432, 512);
  WAITL(14);
  asm volatile("s_add_u32 m0, %0, 0\n\ts_nop 0\n\tbuffer_load_dwordx4 %1, %2, %3 offen offset:0  lds\n\tbuffer_load_dwordx4 %4, %2, %3 offen offset:1024  lds\n\tbuffer_load_dwordx4 %5, %2, %3 offen offset:2048  lds\n\tbuffer_load_dwordx4 %6, %2, %3 offen offset:3072  lds\n\ts_waitcnt vmcnt(9)"
      :  : "s"(ldsO), "v"(vW2[0]), "s"(rs2), "s"(sw[5]), "v"(vW2[1]), "v"(vW2[2]), "v"(vW2[3]) : "memory", "scc"); SB();
  fw2[3][0] = tr8u(tS[0] + 34816, 512);
  fw2[3][1] = tr8u(tS[1] + 34816, 512);
  fw2[3][2] = tr8u(tS[2] + 34816, 512);
  fw2[3][3] = tr8u(tS[3] + 34816, 512);
  WAITL(14);
  asm volatile("s_add_u32 m0, %0, 4096\n\ts_nop 0\n\tbuffer_load_dwordx4 %1, %2, %3 offen offset:0  lds\n\tbuffer_load_dwordx4 %4, %2, %3 offen offset:1024  lds\n\tbuffer_load_dwordx4 %5, %2, %3 offen offset:2048  lds\n\tbuffer_load_dwordx4 %6, %2, %3 offen offset:3072  lds\n\ts_waitcnt vmcnt(8)"
      :  : "s"(ldsO), "v"(vW2[0]), "s"(rs2), "s"(sw[6]), "v"(vW2[1]), "v"(vW2[2]), "v"(vW2[3]) : "memory", "scc"); SB();
  fw2[4][0] = tr8u(tS[0] + 0, 512);
  fw2[4][1] = tr8u(tS[1] + 0, 512);
  fw2[4][2] = tr8u(tS[2] + 0, 512);
  fw2[4][3] = tr8u(tS[3] + 0, 512);
  WAITL(14);
  asm volatile("s_add_u32 m0, %0, 18432\n\ts_nop 0\n\tbuffer_load_dwordx4 %1, %2, %3 offen offset:0  lds\n\tbuffer_load_dwordx4 %4, %2, %3 offen offset:1024  lds\n\tbuffer_load_dwordx4 %5, %2, %3 offen offset:2048  lds\n\tbuffer_load_dwordx4 %6, %2, %3 offen offset:3072  lds\n\ts_waitcnt vmcnt(8)"
      :  : "s"(ldsS), "v"(vW2[0]), "s"(rs2), "s"(sw[7]), "v"(vW2[1]), "v"(vW2[2]), "v"(vW2[3]) : "memory", "scc"); SB();
  fw2[5][0] = tr8u(tO[0] + 0, 512);
  fw2[5][1] = tr8u(tO[1] + 0, 512);
  fw2[5][2] = tr8u(tO[2] + 0, 512);
  fw2[5][3] = tr8u(tO[3] + 0, 512);
  WAITL(14);
  asm volatile("s_add_u32 m0, %0, 34816\n\ts_nop 0\n\tbuffer_load_dwordx4 %1, %2, %3 offen offset:0  lds\n\tbuffer_load_dwordx4 %4, %2, %3 offen offset:1024  lds\n\tbuffer_load_dwordx4 %5, %2, %3 offen offset:2048  lds\n\tbuffer_load_dwordx4 %6, %2, %3 offen offset:3072  lds\n\ts_waitcnt vmcnt(8)"
      :  : "s"(ldsS), "v"(vW2[0]), "s"(rs2), "s"(sw[8]), "v"(vW2[1]), "v"(vW2[2]), "v"(vW2[3]) : "memory", "scc"); SB();
  fw2[6][0] = tr8u(tO[0] + 4096, 512);
  fw2[6][1] = tr8u(tO[1] + 4096, 512);
  fw2[6][2] = tr8u(tO[2] + 4096, 512);
  fw2[6][3] = tr8u(tO[3] + 4096, 512);
  WAITL(14);
  asm volatile("s_add_u32 m0, %0, 0\n\ts_nop 0\n\tbuffer_load_dwordx4 %1, %2, %3 offen offset:0  lds\n\tbuffer_load_dwordx4 %4, %2, %3 offen offset:1024  lds\n\tbuffer_load_dwordx4 %5, %2, %3 offen offset:2048  lds\n\tbuffer_load_dwordx4 %6, %2, %3 offen offset:3072  lds\n\ts_waitcnt vmcnt(8)"
      :  : "s"(ldsS), "v"(vW2[0]), "s"(rs2), "s"(sw[9]), "v"(vW2[1]), "v"(vW2[2]), "v"(vW2[3]) : "memory", "scc"); SB();
  fw2[7][0] = tr8u(tS[0] + 18432, 512);
  fw2[7][1] = tr8u(tS[1] + 18432, 512);
  fw2[7][2] = tr8u(tS[2] + 18432, 512);
  fw2[7][3] = tr8u(tS[3] + 18432, 512);
  WAITL(14);
  asm volatile("s_add_u32 m0, %0, 0\n\ts_nop 0\n\tbuffer_load_dwordx4 %1, %2, %3 offen offset:0  lds\n\tbuffer_load_dwordx4 %4, %2, %3 offen offset:1024  lds\n\tbuffer_load_dwordx4 %5, %2, %3 offen offset:2048  lds\n\tbuffer_load_dwordx4 %6, %2, %3 offen offset:3072  lds\n\ts_waitcnt vmcnt(8)"
      :  : "s"(ldsO), "v"(vW2[0]), "s"(rs2), "s"(sw[10]), "v"(vW2[1]), "v"(vW2[2]), "v"(vW2[3]) : "memory", "scc"); SB();
  fw2[8][0] = tr8u(tS[0] + 34816, 512);
  fw2[8][1] = tr8u(tS[1] + 34816, 512);
  fw2[8][2] = tr8u(tS[2] + 34816, 512);
  fw2[8][3] = tr8u(tS[3] + 34816, 512);
  WAITL(8);
  asm volatile("s_add_u32 m0, %0, 18432\n\ts_nop 0\n\tbuffer_load_dwordx4 %1, %2, %3 offen offset:0  lds\n\tbuffer_load_dwordx4 %4, %2, %3 offen offset:1024  lds\n\tbuffer_load_dwordx4 %5, %2, %3 offen offset:2048  lds\n\tbuffer_load_dwordx4 %6, %2, %3 offen offset:3072  lds\n\ts_waitcnt vmcnt(8)"
      :  : "s"(ldsS), "v"(vW2[0]), "s"(rs2), "s"(sw[11]), "v"(vW2[1]), "v"(vW2[2]), "v"(vW2[3]) : "memory", "scc"); SB();
  fw2[9][0] = tr8u(tS[0] + 0, 512);
  fw2[9][1] = tr8u(tS[1] + 0, 512);
  fw2[9][2] = tr8u(tS[2] + 0, 512);
  fw2[9][3] = tr8u(tS[3] + 0, 512);
  WAITL(8);
  asm volatile("s_add_u32 m0, %0, 34816\n\ts_nop 0\n\tbuffer_load_dwordx4 %1, %2, %3 offen offset:0  lds\n\tbuffer_load_dwordx4 %4, %2, %3 offen offset:1024  lds\n\tbuffer_load_dwordx4 %5, %2, %3 offen offset:2048  lds\n\tbuffer_load_dwordx4 %6, %2, %3 offen offset:3072  lds\n\ts_waitcnt vmcnt(8)"
      :  : "s"(ldsS), "v"(vW2[0]), "s"(rs2), "s"(sw[12]), "v"(vW2[1]), "v"(vW2[2]), "v"(vW2[3]) : "memory", "scc"); SB();
  fw2[10][0] = tr8u(tO[0] + 0, 512);
  fw2[10][1] = tr8u(tO[1] + 0, 512);
  fw2[10][2] = tr8u(tO[2] + 0, 512);
  fw2[10][3] = tr8u(tO[3] + 0, 512);
  WAITL(8);
  asm volatile("s_add_u32 m0, %1, 0\n\ts_nop 0\n\tbuffer_load_dwordx4 %2, %3, %4 offen offset:0  lds\n\tbuffer_load_dwordx4 %5, %3, %4 offen offset:1024  lds\n\tbuffer_load_dwordx4 %6, %3, %4 offen offset:2048  lds\n\tbuffer_load_dwordx4 %7, %3, %4 offen offset:3072  lds\n\ts_add_u32 m0, %8, 8192\n\ts_add_u32 %0, %9, 8192\n\ts_mov_b64 exec, %10\n\tbuffer_load_dwordx4 %11, %12, %0 offen offset:0 sc1 lds\n\tbuffer_load_dwordx4 %11, %12, %0 offen offset:1024 sc1 lds\n\tbuffer_load_dwordx4 %11, %12, %0 offen offset:2048 sc1 lds\n\tbuffer_load_dwordx4 %11, %12, %0 offen offset:3072 sc1 lds\n\ts_mov_b64 exec, -1\n\ts_waitcnt vmcnt(12)"
      : "=&s"(sWt) : "s"(ldsS), "v"(vW2[0]), "s"(rs2), "s"(sw[13]), "v"(vW2[1]), "v"(vW2[2]), "v"(vW2[3]), "s"(ldsP), "s"(sWo), "s"(mWo), "v"(vWo), "s"(rsW) : "memory", "scc"); SB();
  fw2[11][0] = tr8u(tS[0] + 18432, 512);
  fw2[11][1] = tr8u(tS[1] + 18432, 512);
  fw2[11][2] = tr8u(tS[2] + 18432, 512);
  fw2[11][3] = tr8u(tS[3] + 18432, 512);
  asm volatile("buffer_load_dword %0, %2, %3, 0 offen sc1\n\ts_add_u32 m0, %4, 12288\n\ts_add_u32 %1, %5, 12288\n\ts_mov_b64 exec, %6\n\tbuffer_load_dwordx4 %7, %3, %1 offen offset:0 sc1 lds\n\tbuffer_load_dwordx4 %7, %3, %1 offen offset:1024 sc1 lds\n\ts_mov_b64 exec, -1\n\ts_waitcnt vmcnt(11)"
      : "=&v"(fu), "=&s"(sWt) : "v"(vFU), "s"(rsW), "s"(ldsP), "s"(sWo), "s"(mWo), "v"(vWo) : "memory", "scc"); SB();
  fw2[12][0] = tr8u(tS[0] + 34816, 512);
  fw2[12][1] = tr8u(tS[1] + 34816, 512);
  fw2[12][2] = tr8u(tS[2] + 34816, 512);
  fw2[12][3] = tr8u(tS[3] + 34816, 512);
  WAITL(14);
  asm volatile("s_add_u32 m0, %1, 0\n\ts_add_u32 %0, %2, 0\n\ts_mov_b64 exec, %3\n\tbuffer_load_dwordx4 %4, %5, %0 offen offset:0 sc1 lds\n\tbuffer_load_dwordx4 %4, %5, %0 offen offset:1024 sc1 lds\n\tbuffer_load_dwordx4 %4, %5, %0 offen offset:2048 sc1 lds\n\tbuffer_load_dwordx4 %4, %5, %0 offen offset:3072 sc1 lds\n\ts_mov_b64 exec, -1"
      : "=&s"(sWt) : "s"(ldsO), "s"(sWo), "s"(mWo), "v"(vWo), "s"(rsW) : "memory", "scc"); SB();
  u32x4 rsX = srd(x, 2048u * 896u); OPQ(rsX);
  WAITL(0);
  asm volatile("s_add_u32 m0, %2, 34816\n\ts_nop 0\n\tbuffer_load_dwordx4 %3, %4, %5 offen offset:0  lds\n\tbuffer_load_dwordx4 %6, %4, %5 offen offset:1024  lds\n\tglobal_load_ushort %0, %7, %8 offset:0\n\ts_add_u32 m0, %9, 4096\n\ts_add_u32 %1, %10, 4096\n\ts_mov_b64 exec, %11\n\tbuffer_load_dwordx4 %12, %13, %1 offen offset:0 sc1 lds\n\tbuffer_load_dwordx4 %12, %13, %1 offen offset:1024 sc1 lds\n\tbuffer_load_dwordx4 %12, %13, %1 offen offset:2048 sc1 lds\n\tbuffer_load_dwordx4 %12, %13, %1 offen offset:3072 sc1 lds\n\ts_mov_b64 exec, -1"
      : "=&v"(braw), "=&s"(sWt) : "s"(ldsS), "v"(vXa), "s"(rsX), "s"(0u), "v"(vXb), "v"(vB2), "s"(b2), "s"(ldsO), "s"(sWo), "s"(mWo), "v"(vWo), "s"(rsW) : "memory", "scc"); SB();
  STAMP(16);
  bf16x8 pu[12], fwo[56];
  f32x4 ac[2][4], as[4], py[4][4];
  float ye[4], ze[4], ge[16], xe[16], bias;
  u32x2 xv[4], ov[4]; unsigned fo, xta[4];
  WAITV1(13, fu);
  if (__ballot(fu != nonce) != 0ull) poll1<4>(rsW, vFU, nonce);
  STAMP(17);
  asm volatile("buffer_load_dwordx4 %0, %10, %11, %12 offen offset:0 sc1\n\tbuffer_load_dwordx4 %1, %10, %11, %13 offen offset:0 sc1\n\tbuffer_load_dwordx4 %2, %10, %11, %14 offen offset:0 sc1\n\tbuffer_load_dwordx4 %3, %10, %11, %15 offen offset:0 sc1\n\tbuffer_load_dwordx4 %4, %10, %11, %16 offen offset:0 sc1\n\tbuffer_load_dwordx4 %5, %10, %11, %17 offen offset:0 sc1\n\tbuffer_load_dwordx4 %6, %10, %11, %18 offen offset:0 sc1\n\tbuffer_load_dwordx4 %7, %10, %11, %19 offen offset:0 sc1\n\tbuffer_load_dwordx4 %8, %10, %11, %20 offen offset:0 sc1\n\tbuffer_load_dwordx4 %9, %10, %11, %21 offen offset:0 sc1"
      : "=&v"(pu[0]), "=&v"(pu[1]), "=&v"(pu[2]), "=&v"(pu[3]), "=&v"(pu[4]), "=&v"(pu[5]), "=&v"(pu[6]), "=&v"(pu[7]), "=&v"(pu[8]), "=&v"(pu[9]) : "v"(vFj[0]), "s"(rsW), "s"(su[0]), "s"(su[1]), "s"(su[2]), "s"(su[3]), "s"(su[4]), "s"(su[5]), "s"(su[6]), "s"(su[7]), "s"(su[8]), "s"(su[9]) : "memory"); SB();
  STAMP(39);
  WAITVB(9);
  SB();
  asm volatile("v_mfma_f32_16x16x32_bf16 %0, %3, %4, 0\n\tbuffer_load_dwordx4 %1, %5, %6, %7 offen offset:0 sc1\n\tv_mfma_f32_16x16x32_bf16 %2, %3, %8, 0\n\ts_waitcnt vmcnt(29)"
      : "=&v"(ac[0][0]), "=&v"(pu[10]), "=&v"(ac[0][1]) : "v"(pu[0]), "a"(fw2[0][0]), "v"(vFj[0]), "s"(rsW), "s"(su[10]), "a"(fw2[0][1]) : "memory"); SB();
  WAITVB(15);
  SB();
  asm volatile("v_mfma_f32_16x16x32_bf16 %0, %4, %5, 0\n\tbuffer_load_dwordx4 %1, %6, %7, %8 offen offset:0 sc1\n\tv_lshlrev_b32 %2, 16, %9\n\tv_mfma_f32_16x16x32_bf16 %3, %4, %10, 0"
      : "=&v"(ac[0][2]), "=&v"(pu[11]), "=&v"(bias), "=&v"(ac[0][3]) : "v"(pu[0]), "a"(fw2[0][2]), "v"(vFj[0]), "s"(rsW), "s"(su[11]), "v"(braw), "a"(fw2[0][3]) : "memory"); SB();
  WAITVB(10);
  SB();
  asm volatile("v_mfma_f32_16x16x32_bf16 %0, %3, %4, %0\n\tbuffer_load_dwordx4 %1, %5, %6, %7 offen offset:0 sc1\n\tv_mfma_f32_16x16x32_bf16 %2, %3, %8, %2"
      : "+v"(ac[0][0]), "=&v"(pu[0]), "+v"(ac[0][1]) : "v"(pu[1]), "a"(fw2[1][0]), "v"(vFj[0]), "s"(rsW), "s"(su[12]), "a"(fw2[1][1]) : "memory"); SB();
  fw2[13][0] = tr8u(tS[0] + 0, 512);
  SB();
  asm volatile("v_mfma_f32_16x16x32_bf16 %0, %1, %2, %0"
      : "+v"(ac[0][2]) : "v"(pu[1]), "a"(fw2[1][2])); SB();
  fw2[13][1] = tr8u(tS[1] + 0, 512);
  SB();
  asm volatile("v_mfma_f32_16x16x32_bf16 %0, %1, %2, %0"
      : "+v"(ac[0][3]) : "v"(pu[1]), "a"(fw2[1][3])); SB();
  WAITVB(10);
  SB();
  asm volatile("v_mfma_f32_16x16x32_bf16 %0, %3, %4, %0\n\tbuffer_load_dwordx4 %1, %5, %6, %7 offen offset:0 sc1\n\tv_mfma_f32_16x16x32_bf16 %2, %3, %8, %2"
      : "+v"(ac[0][0]), "=&v"(pu[1]), "+v"(ac[0][1]) : "v"(pu[2]), "a"(fw2[2][0]), "v"(vFj[0]), "s"(rsW), "s"(su[13]), "a"(fw2[2][1]) : "memory"); SB();
  fw2[13][2] = tr8u(tS[2] + 0, 512);
  SB();
  asm volatile("v_mfma_f32_16x16x32_bf16 %0, %1, %2, %0"
      : "+v"(ac[0][2]) : "v"(pu[2]), "a"(fw2[2][2])); SB();
  fw2[13][3] = tr8u(tS[3] + 0, 512);
  SB();
  asm volatile("v_mfma_f32_16x16x32_bf16 %0, %1, %2, %0"
      : "+v"(ac[0][3]) : "v"(pu[2]), "a"(fw2[2][3])); SB();
  WAITVB(10);
  SB();
  asm volatile("v_mfma_f32_16x16x32_bf16 %0, %3, %4, %0\n\tbuffer_load_dwordx4 %1, %5, %6, %7 offen offset:0 sc1\n\tv_mfma_f32_16x16x32_bf16 %2, %3, %8, %2\n\ts_waitcnt vmcnt(15)"
      : "+v"(ac[0][0]), "=&v"(pu[2]), "+v"(ac[0][1]) : "v"(pu[3]), "a"(fw2[3][0]), "v"(vFj[1]), "s"(rsW), "s"(su[0]), "a"(fw2[3][1]) : "memory"); SB();
  BAR();
  SB();
  asm volatile("v_mfma_f32_16x16x32_bf16 %0, %6, %7, %0\n\tv_xor_b32 %1, 0, %8\n\tds_read_b64 %2, %1 offset:0\n\tds_read_b128 %3, %9 offset:0\n\tv_mfma_f32_16x16x32_bf16 %4, %6, %10, %4\n\tds_read_b128 %5, %9 offset:8192"
      : "+v"(ac[0][2]), "=&v"(xta[0]), "=&v"(xv[0]), "=&a"(fwo[0]), "+v"(ac[0][3]), "=&a"(fwo[1]) : "v"(pu[3]), "a"(fw2[3][2]), "v"(xt), "v"(ro), "a"(fw2[3][3]) : "memory"); SB();
  WAITVB(10);
  SB();
  asm volatile("v_mfma_f32_16x16x32_bf16 %0, %11, %12, %0\n\tbuffer_load_dwordx4 %1, %13, %14, %15 offen offset:0 sc1\n\tv_mfma_f32_16x16x32_bf16 %2, %11, %16, %2\n\tv_xor_b32 %3, 32, %17\n\tds_read_b64 %4, %3 offset:0\n\tv_mfma_f32_16x16x32_bf16 %5, %11, %18, %5\n\tv_xor_b32 %6, 64, %17\n\tds_read_b64 %7, %6 offset:0\n\tds_read_b128 %8, %19 offset:16384\n\tv_mfma_f32_16x16x32_bf16 %9, %11, %20, %9\n\tds_read_b128 %10, %19 offset:1024"
      : "+v"(ac[0][0]), "=&v"(pu[3]), "+v"(ac[0][1]), "=&v"(xta[1]), "=&v"(xv[1]), "+v"(ac[0][2]), "=&v"(xta[2]), "=&v"(xv[2]), "=&a"(fwo[2]), "+v"(ac[0][3]), "=&a"(fwo[3]) : "v"(pu[4]), "a"(fw2[4][0]), "v"(vFj[1]), "s"(rsW), "s"(su[1]), "a"(fw2[4][1]), "v"(xt), "a"(fw2[4][2]), "v"(ro), "a"(fw2[4][3]) : "memory"); SB();
  WAITVB(10);
  SB();
  asm volatile("v_mfma_f32_16x16x32_bf16 %0, %10, %11, %0\n\tbuffer_load_dwordx4 %1, %12, %13, %14 offen offset:0 sc1\n\tv_mfma_f32_16x16x32_bf16 %2, %10, %15, %2\n\tds_read_b128 %3, %16 offset:9216\n\tds_read_b128 %4, %16 offset:17408\n\tv_mfma_f32_16x16x32_bf16 %5, %10, %17, %5\n\tds_read_b128 %6, %16 offset:2048\n\tds_read_b128 %7, %16 offset:10240\n\tds_read_b128 %8, %16 offset:18432\n\tv_mfma_f32_16x16x32_bf16 %9, %10, %18, %9"
      : "+v"(ac[0][0]), "=&v"(pu[4]), "+v"(ac[0][1]), "=&a"(fwo[4]), "=&a"(fwo[5]), "+v"(ac[0][2]), "=&a"(fwo[6]), "=&a"(fwo[7]), "=&a"(fwo[8]), "+v"(ac[0][3]) : "v"(pu[5]), "a"(fw2[5][0]), "v"(vFj[1]), "s"(rsW), "s"(su[2]), "a"(fw2[5][1]), "v"(ro), "a"(fw2[5][2]), "a"(fw2[5][3]) : "memory"); SB();
  WAITL(7);
  SB();
  WAITVB(10);
  SB();
  asm volatile("v_mfma_f32_16x16x32_bf16 %0, %11, %12, %0\n\tbuffer_load_dwordx4 %1, %13, %14, %15 offen offset:0 sc1\n\tv_mfma_f32_16x16x32_bf16 %2, %11, %16, %2\n\tv_lshlrev_b32 %3, 16, %17\n\tv_and_b32 %4, 0xffff0000, %17\n\tv_mfma_f32_16x16x32_bf16 %5, %11, %18, %5\n\tv_lshlrev_b32 %6, 16, %19\n\tv_and_b32 %7, 0xffff0000, %19\n\tv_lshlrev_b32 %8, 16, %20\n\tv_mfma_f32_16x16x32_bf16 %9, %11, %21, %9\n\tv_and_b32 %10, 0xffff0000, %20"
      : "+v"(ac[0][0]), "=&v"(pu[5]), "+v"(ac[0][1]), "=&v"(xe[0]), "=&v"(xe[1]), "+v"(ac[0][2]), "=&v"(xe[2]), "=&v"(xe[3]), "=&v"(xe[4]), "+v"(ac[0][3]), "=&v"(xe[5]) : "v"(pu[6]), "a"(fw2[6][0]), "v"(vFj[1]), "s"(rsW), "s"(su[3]), "a"(fw2[6][1]), "v"(xv[0][0]), "a"(fw2[6][2]), "v"(xv[0][1]), "v"(xv[1][0]), "a"(fw2[6][3]) : "memory"); SB();
  WAITVB(10);
  SB();
  asm volatile("v_mfma_f32_16x16x32_bf16 %0, %11, %12, %0\n\tbuffer_load_dwordx4 %1, %13, %14, %15 offen offset:0 sc1\n\tv_mfma_f32_16x16x32_bf16 %2, %11, %16, %2\n\tv_lshlrev_b32 %3, 16, %17\n\tv_and_b32 %4, 0xffff0000, %17\n\tv_mfma_f32_16x16x32_bf16 %5, %11, %18, %5\n\tv_lshlrev_b32 %6, 16, %19\n\tv_and_b32 %7, 0xffff0000, %19\n\tv_lshlrev_b32 %8, 16, %20\n\tv_mfma_f32_16x16x32_bf16 %9, %11, %21, %9\n\tv_and_b32 %10, 0xffff0000, %20"
      : "+v"(ac[0][0]), "=&v"(pu[6]), "+v"(ac[0][1]), "=&v"(xe[6]), "=&v"(xe[7]), "+v"(ac[0][2]), "=&v"(xe[8]), "=&v"(xe[9]), "=&v"(xe[10]), "+v"(ac[0][3]), "=&v"(xe[11]) : "v"(pu[7]), "a"(fw2[7][0]), "v"(vFj[1]), "s"(rsW), "s"(su[4]), "a"(fw2[7][1]), "v"(xv[1][1]), "a"(fw2[7][2]), "v"(xv[2][0]), "v"(xv[2][1]), "a"(fw2[7][3]) : "memory"); SB();
  WAITVB(10);
  SB();
  asm volatile("v_mfma_f32_16x16x32_bf16 %0, %11, %12, %0\n\tbuffer_load_dwordx4 %1, %13, %14, %15 offen offset:0 sc1\n\tv_mfma_f32_16x16x32_bf16 %2, %11, %16, %2\n\tds_read_b128 %3, %17 offset:3072\n\tds_read_b128 %4, %17 offset:11264\n\tv_mfma_f32_16x16x32_bf16 %5, %11, %18, %5\n\tds_read_b128 %6, %17 offset:19456\n\tds_read_b128 %7, %17 offset:4096\n\tds_read_b128 %8, %17 offset:12288\n\tv_mfma_f32_16x16x32_bf16 %9, %11, %19, %9\n\tds_read_b128 %10, %17 offset:20480"
      : "+v"(ac[0][0]), "=&v"(pu[7]), "+v"(ac[0][1]), "=&a"(fwo[9]), "=&a"(fwo[10]), "+v"(ac[0][2]), "=&a"(fwo[11]), "=&a"(fwo[12]), "=&a"(fwo[13]), "+v"(ac[0][3]), "=&a"(fwo[14]) : "v"(pu[8]), "a"(fw2[8][0]), "v"(vFj[1]), "s"(rsW), "s"(su[5]), "a"(fw2[8][1]), "v"(ro), "a"(fw2[8][2]), "a"(fw2[8][3]) : "memory"); SB();
  WAITVB(10);
  SB();
  asm volatile("v_mfma_f32_16x16x32_bf16 %0, %11, %12, %0\n\tbuffer_load_dwordx4 %1, %13, %14, %15 offen offset:0 sc1\n\tv_mfma_f32_16x16x32_bf16 %2, %11, %16, %2\n\tds_read_b128 %3, %17 offset:5120\n\tds_read_b128 %4, %17 offset:13312\n\tv_mfma_f32_16x16x32_bf16 %5, %11, %18, %5\n\tds_read_b128 %6, %17 offset:21504\n\tds_read_b128 %7, %17 offset:6144\n\tds_read_b128 %8, %17 offset:14336\n\tv_mfma_f32_16x16x32_bf16 %9, %11, %19, %9\n\tds_read_b128 %10, %17 offset:22528"
      : "+v"(ac[0][0]), "=&v"(pu[8]), "+v"(ac[0][1]), "=&a"(fwo[15]), "=&a"(fwo[16]), "+v"(ac[0][2]), "=&a"(fwo[17]), "=&a"(fwo[18]), "=&a"(fwo[19]), "+v"(ac[0][3]), "=&a"(fwo[20]) : "v"(pu[9]), "v"(fw2[9][0]), "v"(vFj[1]), "s"(rsW), "s"(su[6]), "v"(fw2[9][1]), "v"(ro), "v"(fw2[9][2]), "v"(fw2[9][3]) : "memory"); SB();
  WAITVB(10);
  SB();
  asm volatile("v_mfma_f32_16x16x32_bf16 %0, %6, %7, %0\n\tbuffer_load_dwordx4 %1, %8, %9, %10 offen offset:0 sc1\n\tv_mfma_f32_16x16x32_bf16 %2, %6, %11, %2\n\tds_read_b128 %3, %12 offset:7168\n\tv_mfma_f32_16x16x32_bf16 %4, %6, %13, %4\n\tv_mfma_f32_16x16x32_bf16 %5, %6, %14, %5"
      : "+v"(ac[0][0]), "=&v"(pu[9]), "+v"(ac[0][1]), "=&a"(fwo[21]), "+v"(ac[0][2]), "+v"(ac[0][3]) : "v"(pu[10]), "v"(fw2[10][0]), "v"(vFj[1]), "s"(rsW), "s"(su[7]), "v"(fw2[10][1]), "v"(ro), "v"(fw2[10][2]), "v"(fw2[10][3]) : "memory"); SB();
  WAITVB(10);
  SB();
  asm volatile("v_mfma_f32_16x16x32_bf16 %0, %5, %6, %0\n\tbuffer_load_dwordx4 %1, %7, %8, %9 offen offset:0 sc1\n\tv_mfma_f32_16x16x32_bf16 %2, %5, %10, %2\n\tv_mfma_f32_16x16x32_bf16 %3, %5, %11, %3\n\tv_mfma_f32_16x16x32_bf16 %4, %5, %12, %4"
      : "+v"(ac[0][0]), "=&v"(pu[10]), "+v"(ac[0][1]), "+v"(ac[0][2]), "+v"(ac[0][3]) : "v"(pu[11]), "v"(fw2[11][0]), "v"(vFj[1]), "s"(rsW), "s"(su[8]), "v"(fw2[11][1]), "v"(fw2[11][2]), "v"(fw2[11][3]) : "memory"); SB();
  WAITVB(10);
  SB();
  asm volatile("v_mfma_f32_16x16x32_bf16 %0, %5, %6, %0\n\tbuffer_load_dwordx4 %1, %7, %8, %9 offen offset:0 sc1\n\tv_mfma_f32_16x16x32_bf16 %2, %5, %10, %2\n\tv_mfma_f32_16x16x32_bf16 %3, %5, %11, %3\n\tv_mfma_f32_16x16x32_bf16 %4, %5, %12, %4"
      : "+v"(ac[0][0]), "=&v"(pu[11]), "+v"(ac[0][1]), "+v"(ac[0][2]), "+v"(ac[0][3]) : "v"(pu[0]), "v"(fw2[12][0]), "v"(vFj[1]), "s"(rsW), "s"(su[9]), "v"(fw2[12][1]), "v"(fw2[12][2]), "v"(fw2[12][3]) : "memory"); SB();
  WAITVB(10);
  SB();
  asm volatile("v_mfma_f32_16x16x32_bf16 %0, %5, %6, %0\n\tbuffer_load_dwordx4 %1, %7, %8, %9 offen offset:0 sc1\n\tv_mfma_f32_16x16x32_bf16 %2, %5, %10, %2\n\tv_mfma_f32_16x16x32_bf16 %3, %5, %11, %3\n\tv_mfma_f32_16x16x32_bf16 %4, %5, %12, %4"
      : "+v"(ac[0][0]), "=&v"(pu[0]), "+v"(ac[0][1]), "+v"(ac[0][2]), "+v"(ac[0][3]) : "v"(pu[1]), "v"(fw2[13][0]), "v"(vFj[1]), "s"(rsW), "s"(su[10]), "v"(fw2[13][1]), "v"(fw2[13][2]), "v"(fw2[13][3]) : "memory"); SB();
  STAMP(40);
  WAITVB(10);
  SB();
  asm volatile("v_mfma_f32_16x16x32_bf16 %0, %5, %6, 0\n\tbuffer_load_dwordx4 %1, %7, %8, %9 offen offset:0 sc1\n\tv_mfma_f32_16x16x32_bf16 %2, %5, %10, 0\n\tv_mfma_f32_16x16x32_bf16 %3, %5, %11, 0\n\tv_mfma_f32_16x16x32_bf16 %4, %5, %12, 0"
      : "=&v"(ac[1][0]), "=&v"(pu[1]), "=&v"(ac[1][1]), "=&v"(ac[1][2]), "=&v"(ac[1][3]) : "v"(pu[2]), "a"(fw2[0][0]), "v"(vFj[1]), "s"(rsW), "s"(su[11]), "a"(fw2[0][1]), "a"(fw2[0][2]), "a"(fw2[0][3]) : "memory"); SB();
  WAITVB(10);
  SB();
  asm volatile("v_mfma_f32_16x16x32_bf16 %0, %5, %6, %0\n\tbuffer_load_dwordx4 %1, %7, %8, %9 offen offset:0 sc1\n\tv_mfma_f32_16x16x32_bf16 %2, %5, %10, %2\n\tds_write_b128 %11, %12 offset:0\n\tv_mfma_f32_16x16x32_bf16 %3, %5, %13, %3\n\tv_mfma_f32_16x16x32_bf16 %4, %5, %14, %4"
      : "+v"(ac[1][0]), "=&v"(pu[2]), "+v"(ac[1][1]), "+v"(ac[1][2]), "+v"(ac[1][3]) : "v"(pu[3]), "a"(fw2[1][0]), "v"(vFj[1]), "s"(rsW), "s"(su[12]), "a"(fw2[1][1]), "v"(rwd), "v"(ac[0][0]), "a"(fw2[1][2]), "a"(fw2[1][3]) : "memory"); SB();
  WAITVB(10);
  SB();
  asm volatile("v_mfma_f32_16x16x32_bf16 %0, %6, %7, %0\n\tbuffer_load_dwordx4 %1, %8, %9, %10 offen offset:0 sc1\n\tv_mfma_f32_16x16x32_bf16 %2, %6, %11, %2\n\tbuffer_load_dword %3, %12, %9, 0 offen sc1\n\tv_mfma_f32_16x16x32_bf16 %4, %6, %13, %4\n\tds_write_b128 %14, %15 offset:1024\n\tv_mfma_f32_16x16x32_bf16 %5, %6, %16, %5"
      : "+v"(ac[1][0]), "=&v"(pu[3]), "+v"(ac[1][1]), "=&v"(fo), "+v"(ac[1][2]), "+v"(ac[1][3]) : "v"(pu[4]), "a"(fw2[2][0]), "v"(vFj[1]), "s"(rsW), "s"(su[13]), "a"(fw2[2][1]), "v"(vFO), "a"(fw2[2][2]), "v"(rwd), "v"(ac[0][1]), "a"(fw2[2][3]) : "memory"); SB();
  WAITVB(11);
  SB();
  asm volatile("v_mfma_f32_16x16x32_bf16 %0, %5, %6, %0\n\tbuffer_load_dwordx4 %1, %7, %8, %9 offen offset:0 sc1\n\tv_mfma_f32_16x16x32_bf16 %2, %5, %10, %2\n\tv_mfma_f32_16x16x32_bf16 %3, %5, %11, %3\n\tds_write_b128 %12, %13 offset:2048\n\tv_mfma_f32_16x16x32_bf16 %4, %5, %14, %4"
      : "+v"(ac[1][0]), "=&v"(pu[4]), "+v"(ac[1][1]), "+v"(ac[1][2]), "+v"(ac[1][3]) : "v"(pu[5]), "a"(fw2[3][0]), "v"(vFj[2]), "s"(rsW), "s"(su[0]), "a"(fw2[3][1]), "a"(fw2[3][2]), "v"(rwd), "v"(ac[0][2]), "a"(fw2[3][3]) : "memory"); SB();
  WAITVB(11);
  SB();
  asm volatile("v_mfma_f32_16x16x32_bf16 %0, %5, %6, %0\n\tbuffer_load_dwordx4 %1, %7, %8, %9 offen offset:0 sc1\n\tv_mfma_f32_16x16x32_bf16 %2, %5, %10, %2\n\tv_mfma_f32_16x16x32_bf16 %3, %5, %11, %3\n\tds_write_b128 %12, %13 offset:3072\n\tv_mfma_f32_16x16x32_bf16 %4, %5, %14, %4"
      : "+v"(ac[1][0]), "=&v"(pu[5]), "+v"(ac[1][1]), "+v"(ac[1][2]), "+v"(ac[1][3]) : "v"(pu[6]), "a"(fw2[4][0]), "v"(vFj[2]), "s"(rsW), "s"(su[1]), "a"(fw2[4][1]), "a"(fw2[4][2]), "v"(rwd), "v"(ac[0][3]), "a"(fw2[4][3]) : "memory"); SB();
  WAITVB(11);
  SB();
  asm volatile("v_mfma_f32_16x16x32_bf16 %0, %4, %5, %0\n\tbuffer_load_dwordx4 %1, %6, %7, %8 offen offset:0 sc1\n\tv_mfma_f32_16x16x32_bf16 %2, %4, %9, %2\n\tv_mfma_f32_16x16x32_bf16 %3, %4, %10, %3"
      : "+v"(ac[1][0]), "=&v"(pu[6]), "+v"(ac[1][1]), "+v"(ac[1][2]) : "v"(pu[7]), "a"(fw2[5][0]), "v"(vFj[2]), "s"(rsW), "s"(su[2]), "a"(fw2[5][1]), "a"(fw2[5][2]) : "memory"); SB();
  BAR();
  asm volatile("ds_read_b128 %0, %3 offset:0\n\tv_mfma_f32_16x16x32_bf16 %1, %4, %5, %1\n\tds_read_b128 %2, %3 offset:10240"
      : "=&v"(py[0][0]), "+v"(ac[1][3]), "=&v"(py[0][1]) : "v"(rr), "v"(pu[7]), "a"(fw2[5][3]) : "memory"); SB();
  WAITVB(11);
  SB();
  asm volatile("v_mfma_f32_16x16x32_bf16 %0, %7, %8, %0\n\tbuffer_load_dwordx4 %1, %9, %10, %11 offen offset:0 sc1\n\tv_mfma_f32_16x16x32_bf16 %2, %7, %12, %2\n\tds_read_b128 %3, %13 offset:20480\n\tds_read_b128 %4, %13 offset:30720\n\tv_mfma_f32_16x16x32_bf16 %5, %7, %14, %5\n\tv_mfma_f32_16x16x32_bf16 %6, %7, %15, %6"
      : "+v"(ac[1][0]), "=&v"(pu[7]), "+v"(ac[1][1]), "=&v"(py[0][2]), "=&v"(py[0][3]), "+v"(ac[1][2]), "+v"(ac[1][3]) : "v"(pu[8]), "a"(fw2[6][0]), "v"(vFj[2]), "s"(rsW), "s"(su[3]), "a"(fw2[6][1]), "v"(rr), "a"(fw2[6][2]), "a"(fw2[6][3]) : "memory"); SB();
  WAITVB(11);
  SB();
  asm volatile("v_mfma_f32_16x16x32_bf16 %0, %4, %5, %0\n\tbuffer_load_dwordx4 %1, %6, %7, %8 offen offset:0 sc1\n\tv_mfma_f32_16x16x32_bf16 %2, %4, %9, %2\n\tv_mfma_f32_16x16x32_bf16 %3, %4, %10, %3"
      : "+v"(ac[1][0]), "=&v"(pu[8]), "+v"(ac[1][1]), "+v"(ac[1][2]) : "v"(pu[9]), "a"(fw2[7][0]), "v"(vFj[2]), "s"(rsW), "s"(su[4]), "a"(fw2[7][1]), "a"(fw2[7][2]) : "memory"); SB();
  WAITL(0);
  asm volatile("v_add_f32 %0, %4, %5\n\tv_add_f32 %1, %6, %5\n\tv_mfma_f32_16x16x32_bf16 %2, %7, %8, %2\n\tv_add_f32 %3, %9, %5"
      : "=&v"(ye[0]), "=&v"(ye[1]), "+v"(ac[1][3]), "=&v"(ye[2]) : "v"(py[0][0][0]), "v"(bias), "v"(py[0][0][1]), "v"(pu[9]), "a"(fw2[7][3]), "v"(py[0][0][2])); SB();
  WAITVB(11);
  SB();
  asm volatile("v_mfma_f32_16x16x32_bf16 %0, %9, %10, %0\n\tbuffer_load_dwordx4 %1, %11, %12, %13 offen offset:0 sc1\n\tv_mfma_f32_16x16x32_bf16 %2, %9, %14, %2\n\tv_add_f32 %3, %15, %16\n\tv_add_f32 %4, %17, %18\n\tv_mfma_f32_16x16x32_bf16 %5, %9, %19, %5\n\tv_add_f32 %6, %20, %21\n\tv_add_f32 %7, %22, %23\n\tv_add_f32 %8, %24, %25"
      : "+v"(ac[1][0]), "=&v"(pu[9]), "+v"(ac[1][1]), "=&v"(ye[3]), "=&v"(ze[0]), "+v"(ac[1][2]), "=&v"(ze[1]), "=&v"(ze[2]), "=&v"(ze[3]) : "v"(pu[10]), "a"(fw2[8][0]), "v"(vFj[2]), "s"(rsW), "s"(su[5]), "a"(fw2[8][1]), "v"(py[0][0][3]), "v"(bias), "v"(py[0][1][0]), "v"(py[0][2][0]), "a"(fw2[8][2]), "v"(py[0][1][1]), "v"(py[0][2][1]), "v"(py[0][1][2]), "v"(py[0][2][2]), "v"(py[0][1][3]), "v"(py[0][2][3]) : "memory"); SB();
  asm volatile("v_mfma_f32_16x16x32_bf16 %0, %2, %3, %0\n\tv_add_f32 %1, %1, %4"
      : "+v"(ac[1][3]), "+v"(ye[0]) : "v"(pu[10]), "a"(fw2[8][3]), "v"(py[0][3][0])); SB();
  WAITVB(11);
  SB();
  asm volatile("v_mfma_f32_16x16x32_bf16 %0, %9, %10, %0\n\tbuffer_load_dwordx4 %1, %11, %12, %13 offen offset:0 sc1\n\tv_mfma_f32_16x16x32_bf16 %2, %9, %14, %2\n\tv_add_f32 %3, %3, %15\n\tv_add_f32 %4, %4, %16\n\tv_mfma_f32_16x16x32_bf16 %5, %9, %17, %5\n\tv_add_f32 %6, %6, %18\n\tv_add_f32 %7, %7, %19\n\tv_add_f32 %3, %3, %20\n\tv_mfma_f32_16x16x32_bf16 %8, %9, %21, %8"
      : "+v"(ac[1][0]), "=&v"(pu[10]), "+v"(ac[1][1]), "+v"(ye[1]), "+v"(ye[2]), "+v"(ac[1][2]), "+v"(ye[3]), "+v"(ye[0]), "+v"(ac[1][3]) : "v"(pu[11]), "v"(fw2[9][0]), "v"(vFj[2]), "s"(rsW), "s"(su[6]), "v"(fw2[9][1]), "v"(py[0][3][1]), "v"(py[0][3][2]), "v"(fw2[9][2]), "v"(py[0][3][3]), "v"(ze[0]), "v"(ze[1]), "v"(fw2[9][3]) : "memory"); SB();
  asm volatile("v_add_f32 %0, %0, %1"
      : "+v"(ye[2]) : "v"(ze[2])); SB();
  WAITVB(11);
  SB();
  asm volatile("v_mfma_f32_16x16x32_bf16 %0, %10, %11, %0\n\tbuffer_load_dwordx4 %1, %12, %13, %14 offen offset:0 sc1\n\tv_mfma_f32_16x16x32_bf16 %2, %10, %15, %2\n\tv_add_f32 %3, %3, %4\n\tv_mul_f32 %5, 0xbfb8aa3b, %16\n\tv_mfma_f32_16x16x32_bf16 %6, %10, %17, %6\n\tv_mul_f32 %7, 0xbfb8aa3b, %18\n\tv_mul_f32 %8, 0xbfb8aa3b, %19\n\tv_mul_f32 %4, 0xbfb8aa3b, %3\n\tv_mfma_f32_16x16x32_bf16 %9, %10, %20, %9"
      : "+v"(ac[1][0]), "=&v"(pu[11]), "+v"(ac[1][1]), "+v"(ye[3]), "+v"(ze[3]), "=&v"(ze[0]), "+v"(ac[1][2]), "=&v"(ze[1]), "=&v"(ze[2]), "+v"(ac[1][3]) : "v"(pu[0]), "v"(fw2[10][0]), "v"(vFj[2]), "s"(rsW), "s"(su[7]), "v"(fw2[10][1]), "v"(ye[0]), "v"(fw2[10][2]), "v"(ye[1]), "v"(ye[2]), "v"(fw2[10][3]) : "memory"); SB();
  WAITVB(11);
  SB();
  asm volatile("v_mfma_f32_16x16x32_bf16 %0, %7, %8, %0\n\tbuffer_load_dwordx4 %1, %9, %10, %11 offen offset:0 sc1\n\tv_mfma_f32_16x16x32_bf16 %2, %7, %12, %2\n\tv_exp_f32 %3, %3\n\tv_mfma_f32_16x16x32_bf16 %4, %7, %13, %4\n\tv_exp_f32 %5, %5\n\tv_mfma_f32_16x16x32_bf16 %6, %7, %14, %6"
      : "+v"(ac[1][0]), "=&v"(pu[0]), "+v"(ac[1][1]), "+v"(ze[0]), "+v"(ac[1][2]), "+v"(ze[1]), "+v"(ac[1][3]) : "v"(pu[1]), "v"(fw2[11][0]), "v"(vFj[2]), "s"(rsW), "s"(su[8]), "v"(fw2[11][1]), "v"(fw2[11][2]), "v"(fw2[11][3]) : "memory"); SB();
  WAITVB(11);
  SB();
  asm volatile("v_mfma_f32_16x16x32_bf16 %0, %9, %10, %0\n\tbuffer_load_dwordx4 %1, %11, %12, %13 offen offset:0 sc1\n\tv_mfma_f32_16x16x32_bf16 %2, %9, %14, %2\n\tv_exp_f32 %3, %3\n\tv_mfma_f32_16x16x32_bf16 %4, %9, %15, %4\n\tv_exp_f32 %5, %5\n\tv_add_f32 %6, 1.0, %6\n\tv_mfma_f32_16x16x32_bf16 %7, %9, %16, %7\n\tv_add_f32 %8, 1.0, %8"
      : "+v"(ac[1][0]), "=&v"(pu[1]), "+v"(ac[1][1]), "+v"(ze[2]), "+v"(ac[1][2]), "+v"(ze[3]), "+v"(ze[0]), "+v"(ac[1][3]), "+v"(ze[1]) : "v"(pu[2]), "v"(fw2[12][0]), "v"(vFj[2]), "s"(rsW), "s"(su[9]), "v"(fw2[12][1]), "v"(fw2[12][2]), "v"(fw2[12][3]) : "memory"); SB();
  WAITVB(11);
  SB();
  asm volatile("v_mfma_f32_16x16x32_bf16 %0, %9, %10, %0\n\tbuffer_load_dwordx4 %1, %11, %12, %13 offen offset:0 sc1\n\tv_mfma_f32_16x16x32_bf16 %2, %9, %14, %2\n\tv_add_f32 %3, 1.0, %3\n\tv_add_f32 %4, 1.0, %4\n\tv_mfma_f32_16x16x32_bf16 %5, %9, %15, %5\n\tv_rcp_f32 %6, %6\n\tv_mfma_f32_16x16x32_bf16 %7, %9, %16, %7\n\tv_rcp_f32 %8, %8"
      : "+v"(ac[1][0]), "=&v"(pu[2]), "+v"(ac[1][1]), "+v"(ze[2]), "+v"(ze[3]), "+v"(ac[1][2]), "+v"(ze[0]), "+v"(ac[1][3]), "+v"(ze[1]) : "v"(pu[3]), "v"(fw2[13][0]), "v"(vFj[2]), "s"(rsW), "s"(su[10]), "v"(fw2[13][1]), "v"(fw2[13][2]), "v"(fw2[13][3]) : "memory"); SB();
  STAMP(41);
  WAITVB(10);
  SB();
  asm volatile("v_mfma_f32_16x16x32_bf16 %0, %9, %10, 0\n\tbuffer_load_dwordx4 %1, %11, %12, %13 offen offset:0 sc1\n\tv_mfma_f32_16x16x32_bf16 %2, %9, %14, 0\n\tv_rcp_f32 %3, %3\n\tv_mfma_f32_16x16x32_bf16 %4, %9, %15, 0\n\tv_rcp_f32 %5, %5\n\tv_mul_f32 %6, %16, %17\n\tv_mfma_f32_16x16x32_bf16 %7, %9, %18, 0\n\tv_mul_f32 %8, %19, %20"
      : "=&v"(ac[0][0]), "=&v"(pu[3]), "=&v"(ac[0][1]), "+v"(ze[2]), "=&v"(ac[0][2]), "+v"(ze[3]), "=&v"(ge[0]), "=&v"(ac[0][3]), "=&v"(ge[1]) : "v"(pu[4]), "a"(fw2[0][0]), "v"(vFj[2]), "s"(rsW), "s"(su[11]), "a"(fw2[0][1]), "a"(fw2[0][2]), "v"(ye[0]), "v"(ze[0]), "a"(fw2[0][3]), "v"(ye[1]), "v"(ze[1]) : "memory"); SB();
  WAITVB(10);
  SB();
  asm volatile("v_mfma_f32_16x16x32_bf16 %0, %9, %10, %0\n\tbuffer_load_dwordx4 %1, %11, %12, %13 offen offset:0 sc1\n\tv_mfma_f32_16x16x32_bf16 %2, %9, %14, %2\n\tds_write_b128 %15, %16 offset:4096\n\tv_mfma_f32_16x16x32_bf16 %3, %9, %17, %3\n\tv_mul_f32 %4, %18, %19\n\tv_mul_f32 %5, %20, %21\n\tds_read_b128 %6, %22 offset:15360\n\tv_mfma_f32_16x16x32_bf16 %7, %9, %23, %7\n\tds_read_b128 %8, %22 offset:23552"
      : "+v"(ac[0][0]), "=&v"(pu[4]), "+v"(ac[0][1]), "+v"(ac[0][2]), "=&v"(ge[2]), "=&v"(ge[3]), "=&a"(fwo[22]), "+v"(ac[0][3]), "=&a"(fwo[23]) : "v"(pu[5]), "a"(fw2[1][0]), "v"(vFj[2]), "s"(rsW), "s"(su[12]), "a"(fw2[1][1]), "v"(rwd), "v"(ac[1][0]), "a"(fw2[1][2]), "v"(ye[2]), "v"(ze[2]), "v"(ye[3]), "v"(ze[3]), "v"(ro), "a"(fw2[1][3]) : "memory"); SB();
  WAITVB(10);
  SB();
  asm volatile("v_mfma_f32_16x16x32_bf16 %0, %9, %10, %0\n\tbuffer_load_dwordx4 %1, %11, %12, %13 offen offset:0 sc1\n\tv_mfma_f32_16x16x32_bf16 %2, %9, %14, %2\n\tds_write_b128 %15, %16 offset:5120\n\tv_mfma_f32_16x16x32_bf16 %3, %9, %17, %3\n\tds_read_b128 %4, %18 offset:32768\n\tds_read_b128 %5, %18 offset:38912\n\tds_read_b128 %6, %18 offset:45056\n\tv_mfma_f32_16x16x32_bf16 %7, %9, %19, %7\n\tds_read_b128 %8, %18 offset:33792"
      : "+v"(ac[0][0]), "=&v"(pu[5]), "+v"(ac[0][1]), "+v"(ac[0][2]), "=&a"(fwo[24]), "=&a"(fwo[25]), "=&a"(fwo[26]), "+v"(ac[0][3]), "=&a"(fwo[27]) : "v"(pu[6]), "a"(fw2[2][0]), "v"(vFj[2]), "s"(rsW), "s"(su[13]), "a"(fw2[2][1]), "v"(rwd), "v"(ac[1][1]), "a"(fw2[2][2]), "v"(ro), "a"(fw2[2][3]) : "memory"); SB();
  WAITVB(10);
  SB();
  asm volatile("v_mfma_f32_16x16x32_bf16 %0, %1, %2, %0"
      : "+v"(ac[0][0]) : "v"(pu[7]), "a"(fw2[3][0])); SB();
  WAITV1(14, fo);
  if (__ballot(fo != nonce) != 0ull) poll1<4>(rsW, vFO, nonce);
  STAMP(18);
  asm volatile("buffer_load_dwordx4 %0, %8, %9, %10 offen offset:0 sc1\n\tv_mfma_f32_16x16x32_bf16 %1, %11, %12, %1\n\tds_write_b128 %13, %14 offset:6144\n\tv_mfma_f32_16x16x32_bf16 %2, %11, %15, %2\n\tds_read_b128 %3, %16 offset:39936\n\tds_read_b128 %4, %16 offset:46080\n\tds_read_b128 %5, %16 offset:34816\n\tv_mfma_f32_16x16x32_bf16 %6, %11, %17, %6\n\tds_read_b128 %7, %16 offset:40960"
      : "=&v"(pu[6]), "+v"(ac[0][1]), "+v"(ac[0][2]), "=&a"(fwo[28]), "=&a"(fwo[29]), "=&a"(fwo[30]), "+v"(ac[0][3]), "=&a"(fwo[31]) : "v"(vF), "s"(rsW), "s"(so0), "v"(pu[7]), "a"(fw2[3][1]), "v"(rwd), "v"(ac[1][2]), "a"(fw2[3][2]), "v"(ro), "a"(fw2[3][3]) : "memory"); SB();
  WAITVB(10);
  SB();
  asm volatile("v_mfma_f32_16x16x32_bf16 %0, %9, %10, %0\n\tbuffer_load_dwordx4 %1, %11, %12, %13 offen offset:128 sc1\n\tv_mfma_f32_16x16x32_bf16 %2, %9, %14, %2\n\tds_write_b128 %15, %16 offset:7168\n\tv_mfma_f32_16x16x32_bf16 %3, %9, %17, %3\n\tds_read_b128 %4, %18 offset:47104\n\tds_read_b128 %5, %18 offset:35840\n\tds_read_b128 %6, %18 offset:41984\n\tv_mfma_f32_16x16x32_bf16 %7, %9, %19, %7\n\tds_read_b128 %8, %18 offset:48128"
      : "+v"(ac[0][0]), "=&v"(pu[7]), "+v"(ac[0][1]), "+v"(ac[0][2]), "=&a"(fwo[32]), "=&a"(fwo[33]), "=&a"(fwo[34]), "+v"(ac[0][3]), "=&a"(fwo[35]) : "v"(pu[8]), "a"(fw2[4][0]), "v"(vF), "s"(rsW), "s"(so0), "a"(fw2[4][1]), "v"(rwd), "v"(ac[1][3]), "a"(fw2[4][2]), "v"(ro), "a"(fw2[4][3]) : "memory"); SB();
  WAITVB(10);
  SB();
  asm volatile("v_mfma_f32_16x16x32_bf16 %0, %3, %4, %0\n\tbuffer_load_dwordx4 %1, %5, %6, %7 offen offset:256 sc1\n\tv_mfma_f32_16x16x32_bf16 %2, %3, %8, %2"
      : "+v"(ac[0][0]), "=&v"(pu[8]), "+v"(ac[0][1]) : "v"(pu[9]), "a"(fw2[5][0]), "v"(vF), "s"(rsW), "s"(so0), "a"(fw2[5][1]) : "memory"); SB();
  BAR();
  SB();
  asm volatile("v_mfma_f32_16x16x32_bf16 %0, %6, %7, %0\n\tds_read_b128 %1, %8 offset:4096\n\tds_read_b128 %2, %8 offset:14336\n\tds_read_b128 %3, %8 offset:24576\n\tv_mfma_f32_16x16x32_bf16 %4, %6, %9, %4\n\tds_read_b128 %5, %8 offset:34816"
      : "+v"(ac[0][2]), "=&v"(py[1][0]), "=&v"(py[1][1]), "=&v"(py[1][2]), "+v"(ac[0][3]), "=&v"(py[1][3]) : "v"(pu[9]), "a"(fw2[5][2]), "v"(rr), "a"(fw2[5][3]) : "memory"); SB();
  WAITVB(10);
  SB();
  asm volatile("v_mfma_f32_16x16x32_bf16 %0, %11, %12, %0\n\tbuffer_load_dwordx4 %1, %13, %14, %15 offen offset:384 sc1\n\tv_mfma_f32_16x16x32_bf16 %2, %11, %16, %2\n\tds_read_b128 %3, %17 offset:36864\n\tds_read_b128 %4, %17 offset:43008\n\tv_mfma_f32_16x16x32_bf16 %5, %11, %18, %5\n\tds_read_b128 %6, %17 offset:49152\n\tds_read_b128 %7, %17 offset:37888\n\tds_read_b128 %8, %17 offset:44032\n\tv_mfma_f32_16x16x32_bf16 %9, %11, %19, %9\n\tds_read_b128 %10, %17 offset:50176"
      : "+v"(ac[0][0]), "=&v"(pu[9]), "+v"(ac[0][1]), "=&a"(fwo[36]), "=&a"(fwo[37]), "+v"(ac[0][2]), "=&a"(fwo[38]), "=&a"(fwo[39]), "=&a"(fwo[40]), "+v"(ac[0][3]), "=&a"(fwo[41]) : "v"(pu[10]), "a"(fw2[6][0]), "v"(vF), "s"(rsW), "s"(so0), "a"(fw2[6][1]), "v"(ro), "a"(fw2[6][2]), "a"(fw2[6][3]) : "memory"); SB();
  WAITVB(10);
  SB();
  asm volatile("v_mfma_f32_16x16x32_bf16 %0, %3, %4, %0\n\tbuffer_load_dwordx4 %1, %5, %6, %7 offen offset:512 sc1\n\tv_mfma_f32_16x16x32_bf16 %2, %3, %8, %2"
      : "+v"(ac[0][0]), "=&v"(pu[10]), "+v"(ac[0][1]) : "v"(pu[11]), "a"(fw2[7][0]), "v"(vF), "s"(rsW), "s"(so0), "a"(fw2[7][1]) : "memory"); SB();
  WAITL(6);
  asm volatile("v_add_f32 %0, %7, %8\n\tv_mfma_f32_16x16x32_bf16 %1, %9, %10, %1\n\tv_add_f32 %2, %11, %8\n\tv_add_f32 %3, %12, %8\n\tv_add_f32 %4, %13, %8\n\tv_mfma_f32_16x16x32_bf16 %5, %9, %14, %5\n\tv_add_f32 %6, %15, %16"
      : "=&v"(ye[0]), "+v"(ac[0][2]), "=&v"(ye[1]), "=&v"(ye[2]), "=&v"(ye[3]), "+v"(ac[0][3]), "=&v"(ze[0]) : "v"(py[1][0][0]), "v"(bias), "v"(pu[11]), "a"(fw2[7][2]), "v"(py[1][0][1]), "v"(py[1][0][2]), "v"(py[1][0][3]), "a"(fw2[7][3]), "v"(py[1][1][0]), "v"(py[1][2][0])); SB();
  WAITVB(10);
  SB();
  asm volatile("v_mfma_f32_16x16x32_bf16 %0, %9, %10, %0\n\tbuffer_load_dwordx4 %1, %11, %12, %13 offen offset:640 sc1\n\tv_mfma_f32_16x16x32_bf16 %2, %9, %14, %2\n\tv_add_f32 %3, %15, %16\n\tv_add_f32 %4, %17, %18\n\tv_mfma_f32_16x16x32_bf16 %5, %9, %19, %5\n\tv_add_f32 %6, %20, %21\n\tv_add_f32 %7, %7, %22\n\tv_add_f32 %8, %8, %23"
      : "+v"(ac[0][0]), "=&v"(pu[11]), "+v"(ac[0][1]), "=&v"(ze[1]), "=&v"(ze[2]), "+v"(ac[0][2]), "=&v"(ze[3]), "+v"(ye[0]), "+v"(ye[1]) : "v"(pu[0]), "a"(fw2[8][0]), "v"(vF), "s"(rsW), "s"(so0), "a"(fw2[8][1]), "v"(py[1][1][1]), "v"(py[1][2][1]), "v"(py[1][1][2]), "v"(py[1][2][2]), "a"(fw2[8][2]), "v"(py[1][1][3]), "v"(py[1][2][3]), "v"(py[1][3][0]), "v"(py[1][3][1]) : "memory"); SB();
  asm volatile("v_mfma_f32_16x16x32_bf16 %0, %2, %3, %0\n\tv_add_f32 %1, %1, %4"
      : "+v"(ac[0][3]), "+v"(ye[2]) : "v"(pu[0]), "a"(fw2[8][3]), "v"(py[1][3][2])); SB();
  WAITVB(10);
  SB();
  asm volatile("v_mfma_f32_16x16x32_bf16 %0, %9, %10, %0\n\tbuffer_load_dwordx4 %1, %11, %12, %13 offen offset:768 sc1\n\tv_mfma_f32_16x16x32_bf16 %2, %9, %14, %2\n\tv_add_f32 %3, %3, %15\n\tv_add_f32 %4, %4, %16\n\tv_mfma_f32_16x16x32_bf16 %5, %9, %17, %5\n\tv_add_f32 %6, %6, %18\n\tv_add_f32 %7, %7, %19\n\tv_add_f32 %3, %3, %20\n\tv_mfma_f32_16x16x32_bf16 %8, %9, %21, %8"
      : "+v"(ac[0][0]), "=&v"(pu[0]), "+v"(ac[0][1]), "+v"(ye[3]), "+v"(ye[0]), "+v"(ac[0][2]), "+v"(ye[1]), "+v"(ye[2]), "+v"(ac[0][3]) : "v"(pu[1]), "v"(fw2[9][0]), "v"(vF), "s"(rsW), "s"(so0), "v"(fw2[9][1]), "v"(py[1][3][3]), "v"(ze[0]), "v"(fw2[9][2]), "v"(ze[1]), "v"(ze[2]), "v"(ze[3]), "v"(fw2[9][3]) : "memory"); SB();
  asm volatile("v_mul_f32 %0, 0xbfb8aa3b, %1"
      : "=&v"(ze[0]) : "v"(ye[0])); SB();
  WAITVB(10);
  SB();
  asm volatile("v_mfma_f32_16x16x32_bf16 %0, %9, %10, %0\n\tbuffer_load_dwordx4 %1, %11, %12, %13 offen offset:0 sc1\n\tv_mfma_f32_16x16x32_bf16 %2, %9, %14, %2\n\tv_mul_f32 %3, 0xbfb8aa3b, %15\n\tv_mul_f32 %4, 0xbfb8aa3b, %16\n\tv_mfma_f32_16x16x32_bf16 %5, %9, %17, %5\n\tv_mul_f32 %6, 0xbfb8aa3b, %18\n\tv_exp_f32 %7, %7\n\tv_mfma_f32_16x16x32_bf16 %8, %9, %19, %8"
      : "+v"(ac[0][0]), "=&v"(pu[1]), "+v"(ac[0][1]), "=&v"(ze[1]), "=&v"(ze[2]), "+v"(ac[0][2]), "=&v"(ze[3]), "+v"(ze[0]), "+v"(ac[0][3]) : "v"(pu[2]), "v"(fw2[10][0]), "v"(vF), "s"(rsW), "s"(so1), "v"(fw2[10][1]), "v"(ye[1]), "v"(ye[2]), "v"(fw2[10][2]), "v"(ye[3]), "v"(fw2[10][3]) : "memory"); SB();
  WAITVB(10);
  SB();
  asm volatile("v_mfma_f32_16x16x32_bf16 %0, %7, %8, %0\n\tbuffer_load_dwordx4 %1, %9, %10, %11 offen offset:128 sc1\n\tv_mfma_f32_16x16x32_bf16 %2, %7, %12, %2\n\tv_exp_f32 %3, %3\n\tv_mfma_f32_16x16x32_bf16 %4, %7, %13, %4\n\tv_exp_f32 %5, %5\n\tv_mfma_f32_16x16x32_bf16 %6, %7, %14, %6"
      : "+v"(ac[0][0]), "=&v"(pu[2]), "+v"(ac[0][1]), "+v"(ze[1]), "+v"(ac[0][2]), "+v"(ze[2]), "+v"(ac[0][3]) : "v"(pu[3]), "v"(fw2[11][0]), "v"(vF), "s"(rsW), "s"(so1), "v"(fw2[11][1]), "v"(fw2[11][2]), "v"(fw2[11][3]) : "memory"); SB();
  WAITVB(10);
  SB();
  asm volatile("v_mfma_f32_16x16x32_bf16 %0, %9, %10, %0\n\tbuffer_load_dwordx4 %1, %11, %12, %13 offen offset:256 sc1\n\tv_mfma_f32_16x16x32_bf16 %2, %9, %14, %2\n\tv_exp_f32 %3, %3\n\tv_mfma_f32_16x16x32_bf16 %4, %9, %15, %4\n\tv_add_f32 %5, 1.0, %5\n\tv_add_f32 %6, 1.0, %6\n\tv_add_f32 %7, 1.0, %7\n\tv_mfma_f32_16x16x32_bf16 %8, %9, %16, %8\n\tv_add_f32 %3, 1.0, %3"
      : "+v"(ac[0][0]), "=&v"(pu[3]), "+v"(ac[0][1]), "+v"(ze[3]), "+v"(ac[0][2]), "+v"(ze[0]), "+v"(ze[1]), "+v"(ze[2]), "+v"(ac[0][3]) : "v"(pu[4]), "v"(fw2[12][0]), "v"(vF), "s"(rsW), "s"(so1), "v"(fw2[12][1]), "v"(fw2[12][2]), "v"(fw2[12][3]) : "memory"); SB();
  WAITVB(10);
  SB();
  asm volatile("v_mfma_f32_16x16x32_bf16 %0, %8, %9, %0\n\tbuffer_load_dwordx4 %1, %10, %11, %12 offen offset:384 sc1\n\tv_mfma_f32_16x16x32_bf16 %2, %8, %13, %2\n\tv_rcp_f32 %3, %3\n\tv_mfma_f32_16x16x32_bf16 %4, %8, %14, %4\n\tv_rcp_f32 %5, %5\n\tv_mfma_f32_16x16x32_bf16 %6, %8, %15, %6\n\tv_rcp_f32 %7, %7"
      : "+v"(ac[0][0]), "=&v"(pu[4]), "+v"(ac[0][1]), "+v"(ze[0]), "+v"(ac[0][2]), "+v"(ze[1]), "+v"(ac[0][3]), "+v"(ze[2]) : "v"(pu[5]), "v"(fw2[13][0]), "v"(vF), "s"(rsW), "s"(so1), "v"(fw2[13][1]), "v"(fw2[13][2]), "v"(fw2[13][3]) : "memory"); SB();
  STAMP(42);
  WAITVB(10);
  SB();
  asm volatile("v_mfma_f32_16x16x32_bf16 %0, %7, %8, 0\n\tbuffer_load_dwordx4 %1, %9, %10, %11 offen offset:512 sc1\n\tv_mfma_f32_16x16x32_bf16 %2, %12, %8, 0\n\tv_rcp_f32 %3, %3\n\tv_mfma_f32_16x16x32_bf16 %4, %13, %8, 0\n\tv_mul_f32 %5, %14, %15\n\tv_mul_f32 %6, %16, %17"
      : "=&v"(as[0]), "=&v"(pu[5]), "=&v"(as[1]), "+v"(ze[3]), "=&v"(as[2]), "=&v"(ge[4]), "=&v"(ge[5]) : "a"(fwo[0]), "v"(pu[6]), "v"(vF), "s"(rsW), "s"(so1), "a"(fwo[1]), "a"(fwo[2]), "v"(ye[0]), "v"(ze[0]), "v"(ye[1]), "v"(ze[1]) : "memory"); SB();
  WAITVB(10);
  SB();
  asm volatile("v_mfma_f32_16x16x32_bf16 %0, %5, %6, %0\n\tbuffer_load_dwordx4 %1, %7, %8, %9 offen offset:640 sc1\n\tv_mfma_f32_16x16x32_bf16 %2, %10, %6, %2\n\tds_write_b128 %11, %12 offset:0\n\tv_mfma_f32_16x16x32_bf16 %3, %13, %6, %3\n\tv_mul_f32 %4, %14, %15"
      : "+v"(as[0]), "=&v"(pu[6]), "+v"(as[1]), "+v"(as[2]), "=&v"(ge[6]) : "a"(fwo[3]), "v"(pu[7]), "v"(vF), "s"(rsW), "s"(so1), "a"(fwo[4]), "v"(rwd), "v"(ac[0][0]), "a"(fwo[5]), "v"(ye[2]), "v"(ze[2]) : "memory"); SB();
  WAITVB(10);
  SB();
  asm volatile("v_mfma_f32_16x16x32_bf16 %0, %5, %6, %0\n\tbuffer_load_dwordx4 %1, %7, %8, %9 offen offset:768 sc1\n\tv_mul_f32 %2, %10, %11\n\tv_mfma_f32_16x16x32_bf16 %3, %12, %6, %3\n\tv_mfma_f32_16x16x32_bf16 %4, %13, %6, %4"
      : "+v"(as[0]), "=&v"(pu[7]), "=&v"(ge[7]), "+v"(as[1]), "+v"(as[2]) : "a"(fwo[6]), "v"(pu[8]), "v"(vF), "s"(rsW), "s"(so1), "v"(ye[3]), "v"(ze[3]), "a"(fwo[7]), "a"(fwo[8]) : "memory"); SB();
  WAITVB(10);
  SB();
  asm volatile("v_mfma_f32_16x16x32_bf16 %0, %3, %4, %0\n\tds_write_b128 %5, %6 offset:1024\n\tv_mfma_f32_16x16x32_bf16 %1, %7, %4, %1\n\tv_mfma_f32_16x16x32_bf16 %2, %8, %4, %2"
      : "+v"(as[0]), "+v"(as[1]), "+v"(as[2]) : "a"(fwo[9]), "v"(pu[9]), "v"(rwd), "v"(ac[0][1]), "a"(fwo[10]), "a"(fwo[11]) : "memory"); SB();
  WAITVB(9);
  SB();
  asm volatile("v_mfma_f32_16x16x32_bf16 %0, %3, %4, %0\n\tv_mfma_f32_16x16x32_bf16 %1, %5, %4, %1\n\tds_write_b128 %6, %7 offset:2048\n\tv_mfma_f32_16x16x32_bf16 %2, %8, %4, %2"
      : "+v"(as[0]), "+v"(as[1]), "+v"(as[2]) : "a"(fwo[12]), "v"(pu[10]), "a"(fwo[13]), "v"(rwd), "v"(ac[0][2]), "a"(fwo[14]) : "memory"); SB();
  WAITVB(8);
  SB();
  asm volatile("v_mfma_f32_16x16x32_bf16 %0, %3, %4, %0\n\tv_mfma_f32_16x16x32_bf16 %1, %5, %4, %1\n\tv_mfma_f32_16x16x32_bf16 %2, %6, %4, %2"
      : "+v"(as[0]), "+v"(as[1]), "+v"(as[2]) : "a"(fwo[15]), "v"(pu[11]), "a"(fwo[16]), "a"(fwo[17])); SB();
  WAITVB(7);
  SB();
  asm volatile("v_mfma_f32_16x16x32_bf16 %0, %3, %4, %0\n\tds_write_b128 %5, %6 offset:3072\n\tv_mfma_f32_16x16x32_bf16 %1, %7, %4, %1\n\tv_mfma_f32_16x16x32_bf16 %2, %8, %4, %2"
      : "+v"(as[0]), "+v"(as[1]), "+v"(as[2]) : "a"(fwo[18]), "v"(pu[0]), "v"(rwd), "v"(ac[0][3]), "a"(fwo[19]), "a"(fwo[20]) : "memory"); SB();
  WAITVB(6);
  SB();
  asm volatile("v_mfma_f32_16x16x32_bf16 %0, %2, %3, %0\n\tv_mfma_f32_16x16x32_bf16 %1, %4, %3, %1"
      : "+v"(as[0]), "+v"(as[1]) : "a"(fwo[21]), "v"(pu[1]), "a"(fwo[22])); SB();
  BAR();
  asm volatile("ds_read_b128 %0, %2 offset:0\n\tv_mfma_f32_16x16x32_bf16 %1, %3, %4, %1"
      : "=&v"(py[2][0]), "+v"(as[2]) : "v"(rr), "a"(fwo[23]), "v"(pu[1]) : "memory"); SB();
  WAITVB(5);
  SB();
  asm volatile("v_mfma_f32_16x16x32_bf16 %0, %6, %7, %0\n\tds_read_b128 %1, %8 offset:10240\n\tds_read_b128 %2, %8 offset:20480\n\tv_mfma_f32_16x16x32_bf16 %3, %9, %7, %3\n\tds_read_b128 %4, %8 offset:30720\n\tv_mfma_f32_16x16x32_bf16 %5, %10, %7, %5"
      : "+v"(as[0]), "=&v"(py[2][1]), "=&v"(py[2][2]), "+v"(as[1]), "=&v"(py[2][3]), "+v"(as[2]) : "a"(fwo[24]), "v"(pu[2]), "v"(rr), "a"(fwo[25]), "a"(fwo[26]) : "memory"); SB();
  WAITVB(4);
  SB();
  asm volatile("v_mfma_f32_16x16x32_bf16 %0, %3, %4, %0\n\tv_mfma_f32_16x16x32_bf16 %1, %5, %4, %1\n\tv_mfma_f32_16x16x32_bf16 %2, %6, %4, %2"
      : "+v"(as[0]), "+v"(as[1]), "+v"(as[2]) : "a"(fwo[27]), "v"(pu[3]), "a"(fwo[28]), "a"(fwo[29])); SB();
  WAITVB(3);
  SB();
  asm volatile("v_mfma_f32_16x16x32_bf16 %0, %1, %2, %0"
      : "+v"(as[0]) : "a"(fwo[30]), "v"(pu[4])); SB();
  WAITL(0);
  asm volatile("v_add_f32 %0, %7, %8\n\tv_add_f32 %1, %9, %8\n\tv_mfma_f32_16x16x32_bf16 %2, %10, %11, %2\n\tv_add_f32 %3, %12, %8\n\tv_add_f32 %4, %13, %8\n\tv_mfma_f32_16x16x32_bf16 %5, %14, %11, %5\n\tv_add_f32 %6, %15, %16"
      : "=&v"(ye[0]), "=&v"(ye[1]), "+v"(as[1]), "=&v"(ye[2]), "=&v"(ye[3]), "+v"(as[2]), "=&v"(ze[0]) : "v"(py[2][0][0]), "v"(bias), "v"(py[2][0][1]), "a"(fwo[31]), "v"(pu[4]), "v"(py[2][0][2]), "v"(py[2][0][3]), "a"(fwo[32]), "v"(py[2][1][0]), "v"(py[2][2][0])); SB();
  WAITVB(2);
  SB();
  asm volatile("v_mfma_f32_16x16x32_bf16 %0, %8, %9, %0\n\tv_add_f32 %1, %10, %11\n\tv_add_f32 %2, %12, %13\n\tv_mfma_f32_16x16x32_bf16 %3, %14, %9, %3\n\tv_add_f32 %4, %15, %16\n\tv_add_f32 %5, %5, %17\n\tv_add_f32 %6, %6, %18\n\tv_mfma_f32_16x16x32_bf16 %7, %19, %9, %7"
      : "+v"(as[0]), "=&v"(ze[1]), "=&v"(ze[2]), "+v"(as[1]), "=&v"(ze[3]), "+v"(ye[0]), "+v"(ye[1]), "+v"(as[2]) : "a"(fwo[33]), "v"(pu[5]), "v"(py[2][1][1]), "v"(py[2][2][1]), "v"(py[2][1][2]), "v"(py[2][2][2]), "a"(fwo[34]), "v"(py[2][1][3]), "v"(py[2][2][3]), "v"(py[2][3][0]), "v"(py[2][3][1]), "a"(fwo[35])); SB();
  WAITVB(1);
  SB();
  asm volatile("v_mfma_f32_16x16x32_bf16 %0, %7, %8, %0\n\tv_add_f32 %1, %1, %9\n\tv_add_f32 %2, %2, %10\n\tv_mfma_f32_16x16x32_bf16 %3, %11, %8, %3\n\tv_add_f32 %4, %4, %12\n\tv_add_f32 %5, %5, %13\n\tv_mfma_f32_16x16x32_bf16 %6, %14, %8, %6\n\tv_add_f32 %1, %1, %15\n\tv_add_f32 %2, %2, %16"
      : "+v"(as[0]), "+v"(ye[2]), "+v"(ye[3]), "+v"(as[1]), "+v"(ye[0]), "+v"(ye[1]), "+v"(as[2]) : "a"(fwo[36]), "v"(pu[6]), "v"(py[2][3][2]), "v"(py[2][3][3]), "a"(fwo[37]), "v"(ze[0]), "v"(ze[1]), "a"(fwo[38]), "v"(ze[2]), "v"(ze[3])); SB();
  WAITVB(0);
  SB();
  asm volatile("v_mfma_f32_16x16x32_bf16 %0, %7, %8, %0\n\tv_mul_f32 %1, 0xbfb8aa3b, %9\n\tv_mul_f32 %2, 0xbfb8aa3b, %10\n\tv_mfma_f32_16x16x32_bf16 %3, %11, %8, %3\n\tv_mul_f32 %4, 0xbfb8aa3b, %12\n\tv_mul_f32 %5, 0xbfb8aa3b, %13\n\tv_mfma_f32_16x16x32_bf16 %6, %14, %8, %6\n\tv_exp_f32 %1, %1"
      : "+v"(as[0]), "=&v"(ze[0]), "=&v"(ze[1]), "+v"(as[1]), "=&v"(ze[2]), "=&v"(ze[3]), "+v"(as[2]) : "a"(fwo[39]), "v"(pu[7]), "v"(ye[0]), "v"(ye[1]), "a"(fwo[40]), "v"(ye[2]), "v"(ye[3]), "a"(fwo[41])); SB();
  STAMP(44);
  asm volatile("v_exp_f32 %0, %0\n\tv_exp_f32 %1, %1\n\tv_exp_f32 %2, %2\n\tv_add_f32 %3, 1.0, %3\n\tv_add_f32 %0, 1.0, %0\n\tv_add_f32 %1, 1.0, %1\n\tv_add_f32 %2, 1.0, %2\n\tv_rcp_f32 %3, %3\n\tv_rcp_f32 %0, %0\n\tv_rcp_f32 %1, %1\n\tv_rcp_f32 %2, %2\n\tv_mul_f32 %4, %8, %3\n\tv_mul_f32 %5, %9, %0\n\tv_mul_f32 %6, %10, %1\n\tv_mul_f32 %7, %11, %2"
      : "+v"(ze[1]), "+v"(ze[2]), "+v"(ze[3]), "+v"(ze[0]), "=&v"(ge[8]), "=&v"(ge[9]), "=&v"(ge[10]), "=&v"(ge[11]) : "v"(ye[0]), "v"(ye[1]), "v"(ye[2]), "v"(ye[3])); SB();
  u32x4 rsO_ = srd(out, 2048u * 896u); OPQ(rsO_);
  asm volatile("v_fma_f32 %0, %9, %10, %0\n\tv_fma_f32 %1, %11, %12, %1\n\tv_fma_f32 %2, %13, %14, %2\n\tv_fma_f32 %3, %15, %16, %3\n\tv_cvt_pk_bf16_f32 %4, %0, %1\n\tv_cvt_pk_bf16_f32 %5, %2, %3\n\tv_fma_f32 %6, %17, %18, %6\n\tv_fma_f32 %7, %19, %20, %7\n\tv_fma_f32 %8, %21, %22, %8"
      : "+v"(xe[0]), "+v"(xe[1]), "+v"(xe[2]), "+v"(xe[3]), "=&v"(ov[0][0]), "=&v"(ov[0][1]), "+v"(xe[4]), "+v"(xe[5]), "+v"(xe[6]) : "v"(as[0][0]), "v"(ge[0]), "v"(as[0][1]), "v"(ge[1]), "v"(as[0][2]), "v"(ge[2]), "v"(as[0][3]), "v"(ge[3]), "v"(as[1][0]), "v"(ge[4]), "v"(as[1][1]), "v"(ge[5]), "v"(as[1][2]), "v"(ge[6])); SB();
  asm volatile("v_fma_f32 %0, %9, %10, %0\n\tv_cvt_pk_bf16_f32 %1, %11, %12\n\tv_cvt_pk_bf16_f32 %2, %13, %0\n\tds_write_b64 %14, %15 offset:0\n\tv_fma_f32 %3, %16, %17, %3\n\tv_fma_f32 %4, %18, %19, %4\n\tv_fma_f32 %5, %20, %21, %5\n\tv_fma_f32 %6, %22, %23, %6\n\tv_cvt_pk_bf16_f32 %7, %3, %4\n\tv_cvt_pk_bf16_f32 %8, %5, %6"
      : "+v"(xe[7]), "=&v"(ov[1][0]), "=&v"(ov[1][1]), "+v"(xe[8]), "+v"(xe[9]), "+v"(xe[10]), "+v"(xe[11]), "=&v"(ov[2][0]), "=&v"(ov[2][1]) : "v"(as[1][3]), "v"(ge[7]), "v"(xe[4]), "v"(xe[5]), "v"(xe[6]), "v"(xta[0]), "v"(ov[0]), "v"(as[2][0]), "v"(ge[8]), "v"(as[2][1]), "v"(ge[9]), "v"(as[2][2]), "v"(ge[10]), "v"(as[2][3]), "v"(ge[11]) : "memory"); SB();
  asm volatile("ds_write_b64 %0, %1 offset:0\n\tds_write_b64 %2, %3 offset:0"
      :  : "v"(xta[1]), "v"(ov[1]), "v"(xta[2]), "v"(ov[2]) : "memory"); SB();
  { u32x4 t0_, t1_; DSR16(t0_, xo, 0); DSR16(t1_, xo, 1024); WAITLB(1);
    BS16(vXa, rsO_, 0, t0_, "sc1"); WAITLB(0); BS16(vXb, rsO_, 1024, t1_, "sc1"); }
  STAMP(23);

  STAMP(12);
  STAMP_DUMP();
  }
  } else {
  asm volatile("; NEED B");
  unsigned nonce; asm volatile("s_load_dword %0, %1, 0x0" : "=&s"(nonce) : "s"(ws) : "memory");
  const int iw = (4 * xq + jb - 28) * 4 + w, ih = (iw * 37) >> 8;
  const unsigned ldsI = lds0 + w * SCW;
  { u32x4 rsO = srd(wo, 448u * 896u);
    const unsigned vO_ = (28 * (r8 >> 2) + (r8 & 3)) * 896 + ((lane & 7) ^ ((r8 & 2) << 1)) * 16, sO_ = (4 * iw + 28 * ih) * 896;
    DMA1(vO_, rsO, sO_, ldsI, 0, 0, "");
    DMA1(vO_, rsO, sO_, ldsI, 896, 128, "");
    DMA1(vO_, rsO, sO_, ldsI, 1792, 256, "");
    DMA1(vO_, rsO, sO_, ldsI, 2688, 384, "");
    DMA1(vO_, rsO, sO_, ldsI, 3584, 512, "");
    DMA1(vO_, rsO, sO_, ldsI, 4480, 640, "");
    DMA1(vO_, rsO, sO_, ldsI, 5376, 768, "");
  }
  u32x4 rsW = srd((char*)(((size_t)ws + 8191) & ~(size_t)4095) + 512, WS_END); OPQ(rsW);
  const unsigned trb = (8 * g4 + q4) * 128 + pq * 8, trf = ((q4 >> 1) | ((g4 & 1) << 1)) * 32;
  const unsigned vF = r8 * 1024 + (lane & 7) * 16;
  const int r2 = (3 - (int)((size_t)w2 >> 7)) & 7;
  const int st = 8 * xq + r2, dt = jb - 28, db0 = 16 + 3 * dt;
#undef BLK
#define BLK(j) (db0 + ((j) == 2 ? 2 - 2 * (dt & 1) : (j) + (dt & 1)))
  unsigned to = ldsI + q4 * 128 + ((g4 ^ (q4 & 2)) * 32) + pq * 8, vo[7], so_ = iw < 56 ? WS_WO + (iw >> 2) * 1024 + (iw & 3) * 32768 : 0x80000000u; OPQ(so_);
#pragma unroll
  for (int t = 0; t < 7; ++t) { const int k_ = 4 * t + g4, k7 = (k_ * 37) >> 8; vo[t] = k7 * (131072 - 896) + k_ * 128 + (n16 >> 3) * 16384 + (lane & 7) * 16; }
  u32x4 rs2 = srd(w2, 1792u * 4096u); OPQ(rs2);
  unsigned vW2[4];
#pragma unroll
  for (int i = 0; i < 4; ++i) vW2[i] = (8 * (r8 & 3) + 4 * (r8 >> 2)) * 4096 + st * 128 + ((lane & 7) ^ (2 * i)) * 16 + i * (4096 - 1024);
  int rot2 = 4 * dt + 2 * (st & 7) + 2; rot2 -= 14 * ((rot2 * 37) >> 9);
  bf16x8 fw2[14][4];
  unsigned ldsS = lds0 + S3B + w * 4096, ldsO = lds0 + WOB + w * 8192, ldsP = lds0 + WOB + 24576 + w * 6144, sWo, vWo = r8 * 16384 + (lane & 7) * 16, sWt;
  { const int k_ = BLK(w), k7 = (k_ * 37) >> 8; sWo = WS_WO + k7 * (131072 - 896) + k_ * 128; }
  unsigned fq, vFQ = WS_FQ + (lane < 56 ? lane : 55) * (4 * 4);
  asm volatile("" : "+v"(vW2[0]), "+v"(vW2[1]), "+v"(vW2[2]), "+v"(vW2[3]), "+v"(vFQ), "+v"(vWo)); asm volatile("" : "+s"(rot2), "+s"(ldsS), "+s"(ldsO), "+s"(ldsP), "+s"(sWo));
  unsigned long long mWo = w < 3 ? ~0ull : 1ull; OPQ(mWo);
  unsigned tS[4];
#pragma unroll
  for (int i = 0; i < 4; ++i) { tS[i] = ldsS + q4 * 1024 + g4 * 128 + pq * 8 + ((i ^ q4) * 32); OPV(tS[i]); }
  unsigned tO[4];
#pragma unroll
  for (int i = 0; i < 4; ++i) { tO[i] = ldsO + q4 * 1024 + g4 * 128 + pq * 8 + ((i ^ q4) * 32); OPV(tO[i]); }
  unsigned su[14], sw[14], sc_ = 14 * w + rot2; OPQ(sc_);
  { unsigned k_ = sc_ + (rot2 >= 14 ? -14 : 0); OPQ(k_); su[0] = k_ << 13; sw[0] = k_ << 17; OPQ(su[0]); OPQ(sw[0]); }
  { unsigned k_ = sc_ + (rot2 >= 13 ? -13 : 1); OPQ(k_); su[1] = k_ << 13; sw[1] = k_ << 17; OPQ(su[1]); OPQ(sw[1]); }
  { unsigned k_ = sc_ + (rot2 >= 12 ? -12 : 2); OPQ(k_); su[2] = k_ << 13; sw[2] = k_ << 17; OPQ(su[2]); OPQ(sw[2]); }
  { unsigned k_ = sc_ + (rot2 >= 11 ? -11 : 3); OPQ(k_); su[3] = k_ << 13; sw[3] = k_ << 17; OPQ(su[3]); OPQ(sw[3]); }
  { unsigned k_ = sc_ + (rot2 >= 10 ? -10 : 4); OPQ(k_); su[4] = k_ << 13; sw[4] = k_ << 17; OPQ(su[4]); OPQ(sw[4]); }
  { unsigned k_ = sc_ + (rot2 >= 9 ? -9 : 5); OPQ(k_); su[5] = k_ << 13; sw[5] = k_ << 17; OPQ(su[5]); OPQ(sw[5]); }
  { unsigned k_ = sc_ + (rot2 >= 8 ? -8 : 6); OPQ(k_); su[6] = k_ << 13; sw[6] = k_ << 17; OPQ(su[6]); OPQ(sw[6]); }
  { unsigned k_ = sc_ + (rot2 >= 7 ? -7 : 7); OPQ(k_); su[7] = k_ << 13; sw[7] = k_ << 17; OPQ(su[7]); OPQ(sw[7]); }
  { unsigned k_ = sc_ + (rot2 >= 6 ? -6 : 8); OPQ(k_); su[8] = k_ << 13; sw[8] = k_ << 17; OPQ(su[8]); OPQ(sw[8]); }
  { unsigned k_ = sc_ + (rot2 >= 5 ? -5 : 9); OPQ(k_); su[9] = k_ << 13; sw[9] = k_ << 17; OPQ(su[9]); OPQ(sw[9]); }
  { unsigned k_ = sc_ + (rot2 >= 4 ? -4 : 10); OPQ(k_); su[10] = k_ << 13; sw[10] = k_ << 17; OPQ(su[10]); OPQ(sw[10]); }
  { unsigned k_ = sc_ + (rot2 >= 3 ? -3 : 11); OPQ(k_); su[11] = k_ << 13; sw[11] = k_ << 17; OPQ(su[11]); OPQ(sw[11]); }
  { unsigned k_ = sc_ + (rot2 >= 2 ? -2 : 12); OPQ(k_); su[12] = k_ << 13; sw[12] = k_ << 17; OPQ(su[12]); OPQ(sw[12]); }
  { unsigned k_ = sc_ + (rot2 >= 1 ? -1 : 13); OPQ(k_); su[13] = k_ << 13; sw[13] = k_ << 17; OPQ(su[13]); OPQ(sw[13]); }
  unsigned vFU, vFO;
  { const int da = (db0 * 37) >> 8, db = ((db0 + 2) * 37) >> 8, l_ = lane < 56 ? lane : 55;
    vFU = WS_FU + ((l_ < 28 ? da * 128 + l_ : db * 128 + l_ - 28) + w * 28) * (4 * 4);
    vFO = WS_FO + ((2 * st + (w >> 1)) * 8 + (lane & 7)) * (4 * 16);
    asm volatile("" : "+v"(vFU), "+v"(vFO)); }
  unsigned vFj[4], so0 = WS_OT + (4 * st + w) * 16384, so1 = so0 + 8192;
#pragma unroll
  for (int j = 0; j < 3; ++j) { const int k_ = BLK(j), k7 = (k_ * 37) >> 8; vFj[j] = vF + (WS_UT + k7 * (56 * 8192 - 896) + k_ * 128); OPV(vFj[j]); }
  asm volatile("" : "+s"(so0), "+s"(so1));
  const int tau = 2 * (n16 & 7) + (n16 >> 3);
  unsigned rwd = lds0 + w * SCW + vL16, rr = lds0 + w * 1024 + (16 * g4 + tau) * 16, ro = lds0 + WOB + vL16;
  unsigned xo = ldsS + 34816 + vL16, braw, xt = ldsS + 34816 + tau * 128 + (((g4 >> 1) ^ (n16 & 7)) * 16) + (g4 & 1) * 8, vB2 = (64 * st + 16 * w + tau) * 2;
  asm volatile("" : "+v"(rwd), "+v"(rr), "+v"(ro), "+v"(xt), "+v"(xo), "+v"(vB2));
  unsigned vXa, vXb;
  { const int ga = (lane & 7) ^ (r8 >> 1), gb = ga ^ 4, r_ = (64 * st + 16 * w + r8) * 896;
    vXa = ga < 4 ? r_ + BLK(0) * 32 + ga * 16 : ga < 6 ? r_ + BLK(2) * 32 + (ga - 4) * 16 : 0x40000000;
    vXb = (gb < 4 ? r_ + BLK(0) * 32 + gb * 16 : gb < 6 ? r_ + BLK(2) * 32 + (gb - 4) * 16 : 0x40000000) + (8 * 896 - 1024); OPV(vXa); OPV(vXb); }
  asm volatile("s_waitcnt lgkmcnt(0)" : "+s"(nonce) :: "memory"); nonce += 1u;
  asm volatile("s_waitcnt vmcnt(6)"
      :  :  : "memory"); SB();
  { const bf16x8 q_ = tr8u(to + 0, 512);
    HS16(vo[0], so_, 0, __builtin_bit_cast(u32x4, q_)); }
  asm volatile("s_waitcnt vmcnt(6)"
      :  :  : "memory"); SB();
  { const bf16x8 q_ = tr8u(to + 1024, 512);
    HS16(vo[1], so_, 0, __builtin_bit_cast(u32x4, q_)); }
  asm volatile("s_waitcnt vmcnt(6)"
      :  :  : "memory"); SB();
  { const bf16x8 q_ = tr8u(to + 2048, 512);
    HS16(vo[2], so_, 0, __builtin_bit_cast(u32x4, q_)); }
  asm volatile("s_waitcnt vmcnt(6)"
      :  :  : "memory"); SB();
  { const bf16x8 q_ = tr8u(to + 3072, 512);
    HS16(vo[3], so_, 0, __builtin_bit_cast(u32x4, q_)); }
  asm volatile("s_waitcnt vmcnt(6)"
      :  :  : "memory"); SB();
  { const bf16x8 q_ = tr8u(to + 4096, 512);
    HS16(vo[4], so_, 0, __builtin_bit_cast(u32x4, q_)); }
  asm volatile("s_waitcnt vmcnt(6)"
      :  :  : "memory"); SB();
  { const bf16x8 q_ = tr8u(to + 5120, 512);
    HS16(vo[5], so_, 0, __builtin_bit_cast(u32x4, q_)); }
  asm volatile("s_waitcnt vmcnt(6)"
      :  :  : "memory"); SB();
  { const bf16x8 q_ = tr8u(to + 6144, 512);
    HS16(vo[6], so_, 0, __builtin_bit_cast(u32x4, q_)); }
  for (int k = 0; k < 4; ++k) __builtin_amdgcn_s_sleep(4);
  const unsigned vT = (lane & 7) == r2 ? 0x40000000u : (448 * w + 8 * dt + r8) * 4096 + (8 * xq + (lane & 7)) * 128; const int k0 = (2 * (lane & 7)) % 14;
  { const unsigned v_ = vT + (k0 + 0 >= 14 ? k0 + 0 - 14 : k0 + 0) * 131072; TOUCH(v_, rs2, 0u, 0); }
  { const unsigned v_ = vT + (k0 + 4 >= 14 ? k0 + 4 - 14 : k0 + 4) * 131072; TOUCH(v_, rs2, 0u, 0); }
  asm volatile("s_waitcnt vmcnt(1)"
      :  :  : "memory"); SB();
  { const unsigned v_ = vT + (k0 + 8 >= 14 ? k0 + 8 - 14 : k0 + 8) * 131072; TOUCH(v_, rs2, 0u, 0); }
  asm volatile("s_waitcnt vmcnt(3)"
      :  :  : "memory"); SB();
  STFQ(WS_FQ + iw * (4 * 4));
  STAMP(4);
  asm volatile("s_waitcnt vmcnt(1)"
      :  :  : "memory"); SB();
  { const unsigned v_ = vT + (k0 + 12 >= 14 ? k0 + 12 - 14 : k0 + 12) * 131072; TOUCH(v_, rs2, 0u, 0); }
  asm volatile("s_waitcnt vmcnt(1)"
      :  :  : "memory"); SB();
  { const unsigned v_ = vT + (k0 + 1 >= 14 ? k0 + 1 - 14 : k0 + 1) * 131072; TOUCH(v_, rs2, 0u, 0); }
  asm volatile("s_waitcnt vmcnt(1)"
      :  :  : "memory"); SB();
  { const unsigned v_ = vT + (k0 + 5 >= 14 ? k0 + 5 - 14 : k0 + 5) * 131072; TOUCH(v_, rs2, 0u, 0); }
  asm volatile("s_waitcnt vmcnt(1)"
      :  :  : "memory"); SB();
  { const unsigned v_ = vT + (k0 + 9 >= 14 ? k0 + 9 - 14 : k0 + 9) * 131072; TOUCH(v_, rs2, 0u, 0); }
  asm volatile("s_waitcnt vmcnt(1)"
      :  :  : "memory"); SB();
  { const unsigned v_ = vT + (k0 + 13 >= 14 ? k0 + 13 - 14 : k0 + 13) * 131072; TOUCH(v_, rs2, 0u, 0); }
  STAMP(3);
  asm volatile("s_add_u32 m0, %0, 0\n\ts_nop 0\n\tbuffer_load_dwordx4 %1, %2, %3 offen offset:0  lds\n\tbuffer_load_dwordx4 %4, %2, %3 offen offset:1024  lds\n\tbuffer_load_dwordx4 %5, %2, %3 offen offset:2048  lds\n\tbuffer_load_dwordx4 %6, %2, %3 offen offset:3072  lds\n\ts_add_u32 m0, %0, 4096\n\ts_nop 0\n\tbuffer_load_dwordx4 %1, %2, %7 offen offset:0  lds\n\tbuffer_load_dwordx4 %4, %2, %7 offen offset:1024  lds\n\tbuffer_load_dwordx4 %5, %2, %7 offen offset:2048  lds\n\tbuffer_load_dwordx4 %6, %2, %7 offen offset:3072  lds\n\ts_add_u32 m0, %8, 18432\n\ts_nop 0\n\tbuffer_load_dwordx4 %1, %2, %9 offen offset:0  lds\n\tbuffer_load_dwordx4 %4, %2, %9 offen offset:1024  lds\n\tbuffer_load_dwordx4 %5, %2, %9 offen offset:2048  lds\n\tbuffer_load_dwordx4 %6, %2, %9 offen offset:3072  lds\n\ts_add_u32 m0, %8, 34816\n\ts_nop 0\n\tbuffer_load_dwordx4 %1, %2, %10 offen offset:0  lds\n\tbuffer_load_dwordx4 %4, %2, %10 offen offset:1024  lds\n\tbuffer_load_dwordx4 %5, %2, %10 offen offset:2048  lds\n\tbuffer_load_dwordx4 %6, %2, %10 offen offset:3072  lds\n\ts_waitcnt vmcnt(12)"
      :  : "s"(ldsO), "v"(vW2[0]), "s"(rs2), "s"(sw[0]), "v"(vW2[1]), "v"(vW2[2]), "v"(vW2[3]), "s"(sw[1]), "s"(ldsS), "s"(sw[2]), "s"(sw[3]) : "memory", "scc"); SB();
  fw2[0][0] = tr8u(tO[0] + 0, 512);
  fw2[0][1] = tr8u(tO[1] + 0, 512);
  fw2[0][2] = tr8u(tO[2] + 0, 512);
  fw2[0][3] = tr8u(tO[3] + 0, 512);
  asm volatile("s_add_u32 m0, %0, 0\n\ts_nop 0\n\tbuffer_load_dwordx4 %1, %2, %3 offen offset:0  lds\n\tbuffer_load_dwordx4 %4, %2, %3 offen offset:1024  lds\n\tbuffer_load_dwordx4 %5, %2, %3 offen offset:2048  lds\n\tbuffer_load_dwordx4 %6, %2, %3 offen offset:3072  lds\n\ts_waitcnt vmcnt(12)"
      :  : "s"(ldsS), "v"(vW2[0]), "s"(rs2), "s"(sw[4]), "v"(vW2[1]), "v"(vW2[2]), "v"(vW2[3]) : "memory", "scc"); SB();
  fw2[1][0] = tr8u(tO[0] + 4096, 512);
  fw2[1][1] = tr8u(tO[1] + 4096, 512);
  fw2[1][2] = tr8u(tO[2] + 4096, 512);
  fw2[1][3] = tr8u(tO[3] + 4096, 512);
  WAITL(8);
  asm volatile("s_add_u32 m0, %0, 0\n\ts_nop 0\n\tbuffer_load_dwordx4 %1, %2, %3 offen offset:0  lds\n\tbuffer_load_dwordx4 %4, %2, %3 offen offset:1024  lds\n\tbuffer_load_dwordx4 %5, %2, %3 offen offset:2048  lds\n\tbuffer_load_dwordx4 %6, %2, %3 offen offset:3072  lds\n\ts_waitcnt vmcnt(12)"
      :  : "s"(ldsO), "v"(vW2[0]), "s"(rs2), "s"(sw[5]), "v"(vW2[1]), "v"(vW2[2]), "v"(vW2[3]) : "memory", "scc"); SB();
  fw2[2][0] = tr8u(tS[0] + 18432, 512);
  fw2[2][1] = tr8u(tS[1] + 18432, 512);
  fw2[2][2] = tr8u(tS[2] + 18432, 512);
  fw2[2][3] = tr8u(tS[3] + 18432, 512);
  WAITL(8);
  asm volatile("s_add_u32 m0, %0, 4096\n\ts_nop 0\n\tbuffer_load_dwordx4 %1, %2, %3 offen offset:0  lds\n\tbuffer_load_dwordx4 %4, %2, %3 offen offset:1024  lds\n\tbuffer_load_dwordx4 %5, %2, %3 offen offset:2048  lds\n\tbuffer_load_dwordx4 %6, %2, %3 offen offset:3072  lds\n\ts_waitcnt vmcnt(12)"
      :  : "s"(ldsO), "v"(vW2[0]), "s"(rs2), "s"(sw[6]), "v"(vW2[1]), "v"(vW2[2]), "v"(vW2[3]) : "memory", "scc"); SB();
  fw2[3][0] = tr8u(tS[0] + 34816, 512);
  fw2[3][1] = tr8u(tS[1] + 34816, 512);
  fw2[3][2] = tr8u(tS[2] + 34816, 512);
  fw2[3][3] = tr8u(tS[3] + 34816, 512);
  WAITL(8);
  asm volatile("s_add_u32 m0, %0, 18432\n\ts_nop 0\n\tbuffer_load_dwordx4 %1, %2, %3 offen offset:0  lds\n\tbuffer_load_dwordx4 %4, %2, %3 offen offset:1024  lds\n\tbuffer_load_dwordx4 %5, %2, %3 offen offset:2048  lds\n\tbuffer_load_dwordx4 %6, %2, %3 offen offset:3072  lds\n\ts_waitcnt vmcnt(12)"
      :  : "s"(ldsS), "v"(vW2[0]), "s"(rs2), "s"(sw[7]), "v"(vW2[1]), "v"(vW2[2]), "v"(vW2[3]) : "memory", "scc"); SB();
  fw2[4][0] = tr8u(tS[0] + 0, 512);
  fw2[4][1] = tr8u(tS[1] + 0, 512);
  fw2[4][2] = tr8u(tS[2] + 0, 512);
  fw2[4][3] = tr8u(tS[3] + 0, 512);
  WAITL(8);
  asm volatile("s_add_u32 m0, %0, 34816\n\ts_nop 0\n\tbuffer_load_dwordx4 %1, %2, %3 offen offset:0  lds\n\tbuffer_load_dwordx4 %4, %2, %3 offen offset:1024  lds\n\tbuffer_load_dwordx4 %5, %2, %3 offen offset:2048  lds\n\tbuffer_load_dwordx4 %6, %2, %3 offen offset:3072  lds\n\ts_waitcnt vmcnt(12)"
      :  : "s"(ldsS), "v"(vW2[0]), "s"(rs2), "s"(sw[8]), "v"(vW2[1]), "v"(vW2[2]), "v"(vW2[3]) : "memory", "scc"); SB();
  fw2[5][0] = tr8u(tO[0] + 0, 512);
  fw2[5][1] = tr8u(tO[1] + 0, 512);
  fw2[5][2] = tr8u(tO[2] + 0, 512);
  fw2[5][3] = tr8u(tO[3] + 0, 512);
  WAITL(8);
  asm volatile("s_add_u32 m0, %0, 0\n\ts_nop 0\n\tbuffer_load_dwordx4 %1, %2, %3 offen offset:0  lds\n\tbuffer_load_dwordx4 %4, %2, %3 offen offset:1024  lds\n\tbuffer_load_dwordx4 %5, %2, %3 offen offset:2048  lds\n\tbuffer_load_dwordx4 %6, %2, %3 offen offset:3072  lds\n\ts_waitcnt vmcnt(12)"
      :  : "s"(ldsS), "v"(vW2[0]), "s"(rs2), "s"(sw[9]), "v"(vW2[1]), "v"(vW2[2]), "v"(vW2[3]) : "memory", "scc"); SB();
  fw2[6][0] = tr8u(tO[0] + 4096, 512);
  fw2[6][1] = tr8u(tO[1] + 4096, 512);
  fw2[6][2] = tr8u(tO[2] + 4096, 512);
  fw2[6][3] = tr8u(tO[3] + 4096, 512);
  WAITL(8);
  asm volatile("s_add_u32 m0, %0, 0\n\ts_nop 0\n\tbuffer_load_dwordx4 %1, %2, %3 offen offset:0  lds\n\tbuffer_load_dwordx4 %4, %2, %3 offen offset:1024  lds\n\tbuffer_load_dwordx4 %5, %2, %3 offen offset:2048  lds\n\tbuffer_load_dwordx4 %6, %2, %3 offen offset:3072  lds\n\ts_waitcnt vmcnt(12)"
      :  : "s"(ldsO), "v"(vW2[0]), "s"(rs2), "s"(sw[10]), "v"(vW2[1]), "v"(vW2[2]), "v"(vW2[3]) : "memory", "scc"); SB();
  fw2[7][0] = tr8u(tS[0] + 18432, 512);
  fw2[7][1] = tr8u(tS[1] + 18432, 512);
  fw2[7][2] = tr8u(tS[2] + 18432, 512);
  fw2[7][3] = tr8u(tS[3] + 18432, 512);
  WAITL(0);
  asm volatile("s_add_u32 m0, %0, 18432\n\ts_nop 0\n\tbuffer_load_dwordx4 %1, %2, %3 offen offset:0  lds\n\tbuffer_load_dwordx4 %4, %2, %3 offen offset:1024  lds\n\tbuffer_load_dwordx4 %5, %2, %3 offen offset:2048  lds\n\tbuffer_load_dwordx4 %6, %2, %3 offen offset:3072  lds\n\ts_waitcnt vmcnt(12)"
      :  : "s"(ldsS), "v"(vW2[0]), "s"(rs2), "s"(sw[11]), "v"(vW2[1]), "v"(vW2[2]), "v"(vW2[3]) : "memory", "scc"); SB();
  fw2[8][0] = tr8u(tS[0] + 34816, 512);
  fw2[8][1] = tr8u(tS[1] + 34816, 512);
  fw2[8][2] = tr8u(tS[2] + 34816, 512);
  fw2[8][3] = tr8u(tS[3] + 34816, 512);
  WAITL(0);
  asm volatile("s_add_u32 m0, %0, 34816\n\ts_nop 0\n\tbuffer_load_dwordx4 %1, %2, %3 offen offset:0  lds\n\tbuffer_load_dwordx4 %4, %2, %3 offen offset:1024  lds\n\tbuffer_load_dwordx4 %5, %2, %3 offen offset:2048  lds\n\tbuffer_load_dwordx4 %6, %2, %3 offen offset:3072  lds\n\ts_waitcnt vmcnt(12)"
      :  : "s"(ldsS), "v"(vW2[0]), "s"(rs2), "s"(sw[12]), "v"(vW2[1]), "v"(vW2[2]), "v"(vW2[3]) : "memory", "scc"); SB();
  fw2[9][0] = tr8u(tS[0] + 0, 512);
  fw2[9][1] = tr8u(tS[1] + 0, 512);
  fw2[9][2] = tr8u(tS[2] + 0, 512);
  fw2[9][3] = tr8u(tS[3] + 0, 512);
  WAITL(0);
  asm volatile("s_add_u32 m0, %0, 0\n\ts_nop 0\n\tbuffer_load_dwordx4 %1, %2, %3 offen offset:0  lds\n\tbuffer_load_dwordx4 %4, %2, %3 offen offset:1024  lds\n\tbuffer_load_dwordx4 %5, %2, %3 offen offset:2048  lds\n\tbuffer_load_dwordx4 %6, %2, %3 offen offset:3072  lds\n\ts_waitcnt vmcnt(12)"
      :  : "s"(ldsS), "v"(vW2[0]), "s"(rs2), "s"(sw[13]), "v"(vW2[1]), "v"(vW2[2]), "v"(vW2[3]) : "memory", "scc"); SB();
  fw2[10][0] = tr8u(tO[0] + 0, 512);
  fw2[10][1] = tr8u(tO[1] + 0, 512);
  fw2[10][2] = tr8u(tO[2] + 0, 512);
  fw2[10][3] = tr8u(tO[3] + 0, 512);
  asm volatile("s_waitcnt vmcnt(8)"
      :  :  : "memory"); SB();
  fw2[11][0] = tr8u(tS[0] + 18432, 512);
  fw2[11][1] = tr8u(tS[1] + 18432, 512);
  fw2[11][2] = tr8u(tS[2] + 18432, 512);
  fw2[11][3] = tr8u(tS[3] + 18432, 512);
  asm volatile("s_waitcnt vmcnt(4)"
      :  :  : "memory"); SB();
  fw2[12][0] = tr8u(tS[0] + 34816, 512);
  fw2[12][1] = tr8u(tS[1] + 34816, 512);
  fw2[12][2] = tr8u(tS[2] + 34816, 512);
  fw2[12][3] = tr8u(tS[3] + 34816, 512);
  asm volatile("s_waitcnt vmcnt(0)"
      :  :  : "memory"); SB();
  fw2[13][0] = tr8u(tS[0] + 0, 512);
  fw2[13][1] = tr8u(tS[1] + 0, 512);
  fw2[13][2] = tr8u(tS[2] + 0, 512);
  fw2[13][3] = tr8u(tS[3] + 0, 512);
  poll0<16>(rsW, vFQ, nonce);
  asm volatile("s_add_u32 m0, %1, 8192\n\ts_add_u32 %0, %2, 8192\n\ts_mov_b64 exec, %3\n\tbuffer_load_dwordx4 %4, %5, %0 offen offset:0 sc1 lds\n\tbuffer_load_dwordx4 %4, %5, %0 offen offset:1024 sc1 lds\n\tbuffer_load_dwordx4 %4, %5, %0 offen offset:2048 sc1 lds\n\tbuffer_load_dwordx4 %4, %5, %0 offen offset:3072 sc1 lds\n\ts_mov_b64 exec, -1"
      : "=&s"(sWt) : "s"(ldsP), "s"(sWo), "s"(mWo), "v"(vWo), "s"(rsW) : "memory", "scc"); SB();
  u32x4 rsX = srd(x, 2048u * 896u); OPQ(rsX);
  WAITL(8);
  asm volatile("s_add_u32 m0, %2, 34816\n\ts_nop 0\n\tbuffer_load_dwordx4 %3, %4, %5 offen offset:0  lds\n\tbuffer_load_dwordx4 %6, %4, %5 offen offset:1024  lds\n\tglobal_load_ushort %0, %7, %8 offset:0\n\ts_add_u32 m0, %9, 12288\n\ts_add_u32 %1, %10, 12288\n\ts_mov_b64 exec, %11\n\tbuffer_load_dwordx4 %12, %13, %1 offen offset:0 sc1 lds\n\tbuffer_load_dwordx4 %12, %13, %1 offen offset:1024 sc1 lds\n\ts_mov_b64 exec, -1\n\ts_add_u32 m0, %14, 0\n\ts_add_u32 %1, %10, 0\n\ts_mov_b64 exec, %11\n\tbuffer_load_dwordx4 %12, %13, %1 offen offset:0 sc1 lds\n\tbuffer_load_dwordx4 %12, %13, %1 offen offset:1024 sc1 lds\n\tbuffer_load_dwordx4 %12, %13, %1 offen offset:2048 sc1 lds\n\tbuffer_load_dwordx4 %12, %13, %1 offen offset:3072 sc1 lds\n\ts_mov_b64 exec, -1\n\ts_add_u32 m0, %14, 4096\n\ts_add_u32 %1, %10, 4096\n\ts_mov_b64 exec, %11\n\tbuffer_load_dwordx4 %12, %13, %1 offen offset:0 sc1 lds\n\tbuffer_load_dwordx4 %12, %13, %1 offen offset:1024 sc1 lds\n\tbuffer_load_dwordx4 %12, %13, %1 offen offset:2048 sc1 lds\n\tbuffer_load_dwordx4 %12, %13, %1 offen offset:3072 sc1 lds\n\ts_mov_b64 exec, -1"
      : "=&v"(braw), "=&s"(sWt) : "s"(ldsS), "v"(vXa), "s"(rsX), "s"(0u), "v"(vXb), "v"(vB2), "s"(b2), "s"(ldsP), "s"(sWo), "s"(mWo), "v"(vWo), "s"(rsW), "s"(ldsO) : "memory", "scc"); SB();
  STAMP(16);
  asm volatile("s_waitcnt vmcnt(0)"
      :  :  : "memory"); SB();
  BAR();
  STAMP(5);
  for (int k = 0; k < 0; ++k) __builtin_amdgcn_s_sleep(4);
  {
  bf16x8 pu[12], fwo[56];
  f32x4 ac[2][4], as[4], py[4][4];
  float ye[4], ze[4], ge[16], xe[16], bias;
  u32x2 xv[4], ov[4]; unsigned fo, xta[4];
  poll1<8>(rsW, vFU, nonce);
  STAMP(17);
  asm volatile("buffer_load_dwordx4 %0, %10, %11, %12 offen offset:0 sc1\n\tbuffer_load_dwordx4 %1, %10, %11, %13 offen offset:0 sc1\n\tbuffer_load_dwordx4 %2, %10, %11, %14 offen offset:0 sc1\n\tbuffer_load_dwordx4 %3, %10, %11, %15 offen offset:0 sc1\n\tbuffer_load_dwordx4 %4, %10, %11, %16 offen offset:0 sc1\n\tbuffer_load_dwordx4 %5, %10, %11, %17 offen offset:0 sc1\n\tbuffer_load_dwordx4 %6, %10, %11, %18 offen offset:0 sc1\n\tbuffer_load_dwordx4 %7, %10, %11, %19 offen offset:0 sc1\n\tbuffer_load_dwordx4 %8, %10, %11, %20 offen offset:0 sc1\n\tbuffer_load_dwordx4 %9, %10, %11, %21 offen offset:0 sc1"
      : "=&v"(pu[0]), "=&v"(pu[1]), "=&v"(pu[2]), "=&v"(pu[3]), "=&v"(pu[4]), "=&v"(pu[5]), "=&v"(pu[6]), "=&v"(pu[7]), "=&v"(pu[8]), "=&v"(pu[9]) : "v"(vFj[0]), "s"(rsW), "s"(su[0]), "s"(su[1]), "s"(su[2]), "s"(su[3]), "s"(su[4]), "s"(su[5]), "s"(su[6]), "s"(su[7]), "s"(su[8]), "s"(su[9]) : "memory"); SB();
  STAMP(39);
  WAITVB(9);
  SB();
  asm volatile("v_mfma_f32_16x16x32_bf16 %0, %3, %4, 0\n\tbuffer_load_dwordx4 %1, %5, %6, %7 offen offset:0 sc1\n\tv_mfma_f32_16x16x32_bf16 %2, %3, %8, 0"
      : "=&v"(ac[0][0]), "=&v"(pu[10]), "=&v"(ac[0][1]) : "v"(pu[0]), "a"(fw2[0][0]), "v"(vFj[0]), "s"(rsW), "s"(su[10]), "a"(fw2[0][1]) : "memory"); SB();
  WAITVB(21);
  asm volatile("v_lshlrev_b32 %0, 16, %6\n\tv_mfma_f32_16x16x32_bf16 %1, %7, %8, 0\n\tbuffer_load_dwordx4 %2, %9, %10, %11 offen offset:0 sc1\n\tds_read_b128 %3, %12 offset:0\n\tv_mfma_f32_16x16x32_bf16 %4, %7, %13, 0\n\tds_read_b128 %5, %12 offset:8192"
      : "=&v"(bias), "=&v"(ac[0][2]), "=&v"(pu[11]), "=&a"(fwo[0]), "=&v"(ac[0][3]), "=&a"(fwo[1]) : "v"(braw), "v"(pu[0]), "a"(fw2[0][2]), "v"(vFj[0]), "s"(rsW), "s"(su[11]), "v"(ro), "a"(fw2[0][3]) : "memory"); SB();
  WAITVB(10);
  SB();
  asm volatile("v_mfma_f32_16x16x32_bf16 %0, %11, %12, %0\n\tbuffer_load_dwordx4 %1, %13, %14, %15 offen offset:0 sc1\n\tv_mfma_f32_16x16x32_bf16 %2, %11, %16, %2\n\tv_xor_b32 %3, 0, %17\n\tds_read_b64 %4, %3 offset:0\n\tv_mfma_f32_16x16x32_bf16 %5, %11, %18, %5\n\tv_xor_b32 %6, 32, %17\n\tds_read_b64 %7, %6 offset:0\n\tds_read_b128 %8, %19 offset:16384\n\tv_mfma_f32_16x16x32_bf16 %9, %11, %20, %9\n\tds_read_b128 %10, %19 offset:1024"
      : "+v"(ac[0][0]), "=&v"(pu[0]), "+v"(ac[0][1]), "=&v"(xta[0]), "=&v"(xv[0]), "+v"(ac[0][2]), "=&v"(xta[1]), "=&v"(xv[1]), "=&a"(fwo[2]), "+v"(ac[0][3]), "=&a"(fwo[3]) : "v"(pu[1]), "a"(fw2[1][0]), "v"(vFj[0]), "s"(rsW), "s"(su[12]), "a"(fw2[1][1]), "v"(xt), "a"(fw2[1][2]), "v"(ro), "a"(fw2[1][3]) : "memory"); SB();
  WAITVB(10);
  SB();
  asm volatile("v_mfma_f32_16x16x32_bf16 %0, %11, %12, %0\n\tbuffer_load_dwordx4 %1, %13, %14, %15 offen offset:0 sc1\n\tv_mfma_f32_16x16x32_bf16 %2, %11, %16, %2\n\tv_xor_b32 %3, 64, %17\n\tds_read_b64 %4, %3 offset:0\n\tv_mfma_f32_16x16x32_bf16 %5, %11, %18, %5\n\tds_read_b128 %6, %19 offset:9216\n\tds_read_b128 %7, %19 offset:17408\n\tds_read_b128 %8, %19 offset:2048\n\tv_mfma_f32_16x16x32_bf16 %9, %11, %20, %9\n\tds_read_b128 %10, %19 offset:10240"
      : "+v"(ac[0][0]), "=&v"(pu[1]), "+v"(ac[0][1]), "=&v"(xta[2]), "=&v"(xv[2]), "+v"(ac[0][2]), "=&a"(fwo[4]), "=&a"(fwo[5]), "=&a"(fwo[6]), "+v"(ac[0][3]), "=&a"(fwo[7]) : "v"(pu[2]), "a"(fw2[2][0]), "v"(vFj[0]), "s"(rsW), "s"(su[13]), "a"(fw2[2][1]), "v"(xt), "a"(fw2[2][2]), "v"(ro), "a"(fw2[2][3]) : "memory"); SB();
  WAITVB(10);
  SB();
  asm volatile("v_mfma_f32_16x16x32_bf16 %0, %6, %7, %0\n\tbuffer_load_dwordx4 %1, %8, %9, %10 offen offset:0 sc1\n\tv_mfma_f32_16x16x32_bf16 %2, %6, %11, %2\n\tds_read_b128 %3, %12 offset:18432\n\tds_read_b128 %4, %12 offset:3072\n\tv_mfma_f32_16x16x32_bf16 %5, %6, %13, %5"
      : "+v"(ac[0][0]), "=&v"(pu[2]), "+v"(ac[0][1]), "=&a"(fwo[8]), "=&a"(fwo[9]), "+v"(ac[0][2]) : "v"(pu[3]), "a"(fw2[3][0]), "v"(vFj[1]), "s"(rsW), "s"(su[0]), "a"(fw2[3][1]), "v"(ro), "a"(fw2[3][2]) : "memory"); SB();
  WAITL(6);
  asm volatile("v_lshlrev_b32 %0, 16, %4\n\tv_and_b32 %1, 0xffff0000, %4\n\tv_mfma_f32_16x16x32_bf16 %2, %5, %6, %2\n\tv_lshlrev_b32 %3, 16, %7"
      : "=&v"(xe[0]), "=&v"(xe[1]), "+v"(ac[0][3]), "=&v"(xe[2]) : "v"(xv[0][0]), "v"(pu[3]), "a"(fw2[3][3]), "v"(xv[0][1])); SB();
  WAITVB(10);
  SB();
  asm volatile("v_mfma_f32_16x16x32_bf16 %0, %11, %12, %0\n\tbuffer_load_dwordx4 %1, %13, %14, %15 offen offset:0 sc1\n\tv_mfma_f32_16x16x32_bf16 %2, %11, %16, %2\n\tv_and_b32 %3, 0xffff0000, %17\n\tv_lshlrev_b32 %4, 16, %18\n\tv_mfma_f32_16x16x32_bf16 %5, %11, %19, %5\n\tv_and_b32 %6, 0xffff0000, %18\n\tv_lshlrev_b32 %7, 16, %20\n\tv_and_b32 %8, 0xffff0000, %20\n\tv_mfma_f32_16x16x32_bf16 %9, %11, %21, %9\n\tv_lshlrev_b32 %10, 16, %22"
      : "+v"(ac[0][0]), "=&v"(pu[3]), "+v"(ac[0][1]), "=&v"(xe[3]), "=&v"(xe[4]), "+v"(ac[0][2]), "=&v"(xe[5]), "=&v"(xe[6]), "=&v"(xe[7]), "+v"(ac[0][3]), "=&v"(xe[8]) : "v"(pu[4]), "a"(fw2[4][0]), "v"(vFj[1]), "s"(rsW), "s"(su[1]), "a"(fw2[4][1]), "v"(xv[0][1]), "v"(xv[1][0]), "a"(fw2[4][2]), "v"(xv[1][1]), "a"(fw2[4][3]), "v"(xv[2][0]) : "memory"); SB();
  WAITVB(10);
  SB();
  asm volatile("v_mfma_f32_16x16x32_bf16 %0, %11, %12, %0\n\tbuffer_load_dwordx4 %1, %13, %14, %15 offen offset:0 sc1\n\tv_mfma_f32_16x16x32_bf16 %2, %11, %16, %2\n\tv_and_b32 %3, 0xffff0000, %17\n\tv_lshlrev_b32 %4, 16, %18\n\tv_mfma_f32_16x16x32_bf16 %5, %11, %19, %5\n\tv_and_b32 %6, 0xffff0000, %18\n\tds_read_b128 %7, %20 offset:11264\n\tds_read_b128 %8, %20 offset:19456\n\tv_mfma_f32_16x16x32_bf16 %9, %11, %21, %9\n\tds_read_b128 %10, %20 offset:4096"
      : "+v"(ac[0][0]), "=&v"(pu[4]), "+v"(ac[0][1]), "=&v"(xe[9]), "=&v"(xe[10]), "+v"(ac[0][2]), "=&v"(xe[11]), "=&a"(fwo[10]), "=&a"(fwo[11]), "+v"(ac[0][3]), "=&a"(fwo[12]) : "v"(pu[5]), "a"(fw2[5][0]), "v"(vFj[1]), "s"(rsW), "s"(su[2]), "a"(fw2[5][1]), "v"(xv[2][0]), "v"(xv[2][1]), "a"(fw2[5][2]), "v"(ro), "a"(fw2[5][3]) : "memory"); SB();
  WAITVB(10);
  SB();
  asm volatile("v_mfma_f32_16x16x32_bf16 %0, %11, %12, %0\n\tbuffer_load_dwordx4 %1, %13, %14, %15 offen offset:0 sc1\n\tv_mfma_f32_16x16x32_bf16 %2, %11, %16, %2\n\tds_read_b128 %3, %17 offset:12288\n\tds_read_b128 %4, %17 offset:20480\n\tv_mfma_f32_16x16x32_bf16 %5, %11, %18, %5\n\tds_read_b128 %6, %17 offset:5120\n\tds_read_b128 %7, %17 offset:13312\n\tds_read_b128 %8, %17 offset:21504\n\tv_mfma_f32_16x16x32_bf16 %9, %11, %19, %9\n\tds_read_b128 %10, %17 offset:6144"
      : "+v"(ac[0][0]), "=&v"(pu[5]), "+v"(ac[0][1]), "=&a"(fwo[13]), "=&a"(fwo[14]), "+v"(ac[0][2]), "=&a"(fwo[15]), "=&a"(fwo[16]), "=&a"(fwo[17]), "+v"(ac[0][3]), "=&a"(fwo[18]) : "v"(pu[6]), "a"(fw2[6][0]), "v"(vFj[1]), "s"(rsW), "s"(su[3]), "a"(fw2[6][1]), "v"(ro), "a"(fw2[6][2]), "a"(fw2[6][3]) : "memory"); SB();
  WAITVB(10);
  SB();
  asm volatile("v_mfma_f32_16x16x32_bf16 %0, %8, %9, %0\n\tbuffer_load_dwordx4 %1, %10, %11, %12 offen offset:0 sc1\n\tv_mfma_f32_16x16x32_bf16 %2, %8, %13, %2\n\tds_read_b128 %3, %14 offset:14336\n\tds_read_b128 %4, %14 offset:22528\n\tv_mfma_f32_16x16x32_bf16 %5, %8, %15, %5\n\tds_read_b128 %6, %14 offset:7168\n\tv_mfma_f32_16x16x32_bf16 %7, %8, %16, %7"
      : "+v"(ac[0][0]), "=&v"(pu[6]), "+v"(ac[0][1]), "=&a"(fwo[19]), "=&a"(fwo[20]), "+v"(ac[0][2]), "=&a"(fwo[21]), "+v"(ac[0][3]) : "v"(pu[7]), "a"(fw2[7][0]), "v"(vFj[1]), "s"(rsW), "s"(su[4]), "a"(fw2[7][1]), "v"(ro), "a"(fw2[7][2]), "a"(fw2[7][3]) : "memory"); SB();
  WAITVB(10);
  SB();
  asm volatile("v_mfma_f32_16x16x32_bf16 %0, %5, %6, %0\n\tbuffer_load_dwordx4 %1, %7, %8, %9 offen offset:0 sc1\n\tv_mfma_f32_16x16x32_bf16 %2, %5, %10, %2\n\tv_mfma_f32_16x16x32_bf16 %3, %5, %11, %3\n\tv_mfma_f32_16x16x32_bf16 %4, %5, %12, %4"
      : "+v"(ac[0][0]), "=&v"(pu[7]), "+v"(ac[0][1]), "+v"(ac[0][2]), "+v"(ac[0][3]) : "v"(pu[8]), "a"(fw2[8][0]), "v"(vFj[1]), "s"(rsW), "s"(su[5]), "a"(fw2[8][1]), "a"(fw2[8][2]), "a"(fw2[8][3]) : "memory"); SB();
  WAITVB(10);
  SB();
  asm volatile("v_mfma_f32_16x16x32_bf16 %0, %5, %6, %0\n\tbuffer_load_dwordx4 %1, %7, %8, %9 offen offset:0 sc1\n\tv_mfma_f32_16x16x32_bf16 %2, %5, %10, %2\n\tv_mfma_f32_16x16x32_bf16 %3, %5, %11, %3\n\tv_mfma_f32_16x16x32_bf16 %4, %5, %12, %4"
      : "+v"(ac[0][0]), "=&v"(pu[8]), "+v"(ac[0][1]), "+v"(ac[0][2]), "+v"(ac[0][3]) : "v"(pu[9]), "v"(fw2[9][0]), "v"(vFj[1]), "s"(rsW), "s"(su[6]), "v"(fw2[9][1]), "v"(fw2[9][2]), "v"(fw2[9][3]) : "memory"); SB();
  WAITVB(10);
  SB();
  asm volatile("v_mfma_f32_16x16x32_bf16 %0, %5, %6, %0\n\tbuffer_load_dwordx4 %1, %7, %8, %9 offen offset:0 sc1\n\tv_mfma_f32_16x16x32_bf16 %2, %5, %10, %2\n\tv_mfma_f32_16x16x32_bf16 %3, %5, %11, %3\n\tv_mfma_f32_16x16x32_bf16 %4, %5, %12, %4"
      : "+v"(ac[0][0]), "=&v"(pu[9]), "+v"(ac[0][1]), "+v"(ac[0][2]), "+v"(ac[0][3]) : "v"(pu[10]), "v"(fw2[10][0]), "v"(vFj[1]), "s"(rsW), "s"(su[7]), "v"(fw2[10][1]), "v"(fw2[10][2]), "v"(fw2[10][3]) : "memory"); SB();
  WAITVB(10);
  SB();
  asm volatile("v_mfma_f32_16x16x32_bf16 %0, %5, %6, %0\n\tbuffer_load_dwordx4 %1, %7, %8, %9 offen offset:0 sc1\n\tv_mfma_f32_16x16x32_bf16 %2, %5, %10, %2\n\tv_mfma_f32_16x16x32_bf16 %3, %5, %11, %3\n\tv_mfma_f32_16x16x32_bf16 %4, %5, %12, %4"
      : "+v"(ac[0][0]), "=&v"(pu[10]), "+v"(ac[0][1]), "+v"(ac[0][2]), "+v"(ac[0][3]) : "v"(pu[11]), "v"(fw2[11][0]), "v"(vFj[1]), "s"(rsW), "s"(su[8]), "v"(fw2[11][1]), "v"(fw2[11][2]), "v"(fw2[11][3]) : "memory"); SB();
  WAITVB(10);
  SB();
  asm volatile("v_mfma_f32_16x16x32_bf16 %0, %5, %6, %0\n\tbuffer_load_dwordx4 %1, %7, %8, %9 offen offset:0 sc1\n\tv_mfma_f32_16x16x32_bf16 %2, %5, %10, %2\n\tv_mfma_f32_16x16x32_bf16 %3, %5, %11, %3\n\tv_mfma_f32_16x16x32_bf16 %4, %5, %12, %4"
      : "+v"(ac[0][0]), "=&v"(pu[11]), "+v"(ac[0][1]), "+v"(ac[0][2]), "+v"(ac[0][3]) : "v"(pu[0]), "v"(fw2[12][0]), "v"(vFj[1]), "s"(rsW), "s"(su[9]), "v"(fw2[12][1]), "v"(fw2[12][2]), "v"(fw2[12][3]) : "memory"); SB();
  WAITVB(10);
  SB();
  asm volatile("v_mfma_f32_16x16x32_bf16 %0, %5, %6, %0\n\tbuffer_load_dwordx4 %1, %7, %8, %9 offen offset:0 sc1\n\tv_mfma_f32_16x16x32_bf16 %2, %5, %10, %2\n\tv_mfma_f32_16x16x32_bf16 %3, %5, %11, %3\n\tv_mfma_f32_16x16x32_bf16 %4, %5, %12, %4"
      : "+v"(ac[0][0]), "=&v"(pu[0]), "+v"(ac[0][1]), "+v"(ac[0][2]), "+v"(ac[0][3]) : "v"(pu[1]), "v"(fw2[13][0]), "v"(vFj[1]), "s"(rsW), "s"(su[10]), "v"(fw2[13][1]), "v"(fw2[13][2]), "v"(fw2[13][3]) : "memory"); SB();
  STAMP(40);
  WAITVB(10);
  SB();
  asm volatile("v_mfma_f32_16x16x32_bf16 %0, %5, %6, 0\n\tbuffer_load_dwordx4 %1, %7, %8, %9 offen offset:0 sc1\n\tv_mfma_f32_16x16x32_bf16 %2, %5, %10, 0\n\tv_mfma_f32_16x16x32_bf16 %3, %5, %11, 0\n\tv_mfma_f32_16x16x32_bf16 %4, %5, %12, 0"
      : "=&v"(ac[1][0]), "=&v"(pu[1]), "=&v"(ac[1][1]), "=&v"(ac[1][2]), "=&v"(ac[1][3]) : "v"(pu[2]), "a"(fw2[0][0]), "v"(vFj[1]), "s"(rsW), "s"(su[11]), "a"(fw2[0][1]), "a"(fw2[0][2]), "a"(fw2[0][3]) : "memory"); SB();
  WAITVB(10);
  SB();
  asm volatile("v_mfma_f32_16x16x32_bf16 %0, %5, %6, %0\n\tbuffer_load_dwordx4 %1, %7, %8, %9 offen offset:0 sc1\n\tv_mfma_f32_16x16x32_bf16 %2, %5, %10, %2\n\tds_write_b128 %11, %12 offset:0\n\tv_mfma_f32_16x16x32_bf16 %3, %5, %13, %3\n\tv_mfma_f32_16x16x32_bf16 %4, %5, %14, %4"
      : "+v"(ac[1][0]), "=&v"(pu[2]), "+v"(ac[1][1]), "+v"(ac[1][2]), "+v"(ac[1][3]) : "v"(pu[3]), "a"(fw2[1][0]), "v"(vFj[1]), "s"(rsW), "s"(su[12]), "a"(fw2[1][1]), "v"(rwd), "v"(ac[0][0]), "a"(fw2[1][2]), "a"(fw2[1][3]) : "memory"); SB();
  WAITVB(10);
  SB();
  asm volatile("v_mfma_f32_16x16x32_bf16 %0, %6, %7, %0\n\tbuffer_load_dwordx4 %1, %8, %9, %10 offen offset:0 sc1\n\tv_mfma_f32_16x16x32_bf16 %2, %6, %11, %2\n\tbuffer_load_dword %3, %12, %9, 0 offen sc1\n\tv_mfma_f32_16x16x32_bf16 %4, %6, %13, %4\n\tds_write_b128 %14, %15 offset:1024\n\tv_mfma_f32_16x16x32_bf16 %5, %6, %16, %5"
      : "+v"(ac[1][0]), "=&v"(pu[3]), "+v"(ac[1][1]), "=&v"(fo), "+v"(ac[1][2]), "+v"(ac[1][3]) : "v"(pu[4]), "a"(fw2[2][0]), "v"(vFj[1]), "s"(rsW), "s"(su[13]), "a"(fw2[2][1]), "v"(vFO), "a"(fw2[2][2]), "v"(rwd), "v"(ac[0][1]), "a"(fw2[2][3]) : "memory"); SB();
  WAITVB(11);
  SB();
  asm volatile("v_mfma_f32_16x16x32_bf16 %0, %5, %6, %0\n\tbuffer_load_dwordx4 %1, %7, %8, %9 offen offset:0 sc1\n\tv_mfma_f32_16x16x32_bf16 %2, %5, %10, %2\n\tv_mfma_f32_16x16x32_bf16 %3, %5, %11, %3\n\tds_write_b128 %12, %13 offset:2048\n\tv_mfma_f32_16x16x32_bf16 %4, %5, %14, %4"
      : "+v"(ac[1][0]), "=&v"(pu[4]), "+v"(ac[1][1]), "+v"(ac[1][2]), "+v"(ac[1][3]) : "v"(pu[5]), "a"(fw2[3][0]), "v"(vFj[2]), "s"(rsW), "s"(su[0]), "a"(fw2[3][1]), "a"(fw2[3][2]), "v"(rwd), "v"(ac[0][2]), "a"(fw2[3][3]) : "memory"); SB();
  WAITVB(11);
  SB();
  asm volatile("v_mfma_f32_16x16x32_bf16 %0, %5, %6, %0\n\tbuffer_load_dwordx4 %1, %7, %8, %9 offen offset:0 sc1\n\tv_mfma_f32_16x16x32_bf16 %2, %5, %10, %2\n\tv_mfma_f32_16x16x32_bf16 %3, %5, %11, %3\n\tds_write_b128 %12, %13 offset:3072\n\tv_mfma_f32_16x16x32_bf16 %4, %5, %14, %4"
      : "+v"(ac[1][0]), "=&v"(pu[5]), "+v"(ac[1][1]), "+v"(ac[1][2]), "+v"(ac[1][3]) : "v"(pu[6]), "a"(fw2[4][0]), "v"(vFj[2]), "s"(rsW), "s"(su[1]), "a"(fw2[4][1]), "a"(fw2[4][2]), "v"(rwd), "v"(ac[0][3]), "a"(fw2[4][3]) : "memory"); SB();
  WAITVB(11);
  SB();
  asm volatile("v_mfma_f32_16x16x32_bf16 %0, %4, %5, %0\n\tbuffer_load_dwordx4 %1, %6, %7, %8 offen offset:0 sc1\n\tv_mfma_f32_16x16x32_bf16 %2, %4, %9, %2\n\tv_mfma_f32_16x16x32_bf16 %3, %4, %10, %3"
      : "+v"(ac[1][0]), "=&v"(pu[6]), "+v"(ac[1][1]), "+v"(ac[1][2]) : "v"(pu[7]), "a"(fw2[5][0]), "v"(vFj[2]), "s"(rsW), "s"(su[2]), "a"(fw2[5][1]), "a"(fw2[5][2]) : "memory"); SB();
  BAR();
  asm volatile("ds_read_b128 %0, %3 offset:0\n\tv_mfma_f32_16x16x32_bf16 %1, %4, %5, %1\n\tds_read_b128 %2, %3 offset:10240"
      : "=&v"(py[0][0]), "+v"(ac[1][3]), "=&v"(py[0][1]) : "v"(rr), "v"(pu[7]), "a"(fw2[5][3]) : "memory"); SB();
  WAITVB(11);
  SB();
  asm volatile("v_mfma_f32_16x16x32_bf16 %0, %7, %8, %0\n\tbuffer_load_dwordx4 %1, %9, %10, %11 offen offset:0 sc1\n\tv_mfma_f32_16x16x32_bf16 %2, %7, %12, %2\n\tds_read_b128 %3, %13 offset:20480\n\tds_read_b128 %4, %13 offset:30720\n\tv_mfma_f32_16x16x32_bf16 %5, %7, %14, %5\n\tv_mfma_f32_16x16x32_bf16 %6, %7, %15, %6"
      : "+v"(ac[1][0]), "=&v"(pu[7]), "+v"(ac[1][1]), "=&v"(py[0][2]), "=&v"(py[0][3]), "+v"(ac[1][2]), "+v"(ac[1][3]) : "v"(pu[8]), "a"(fw2[6][0]), "v"(vFj[2]), "s"(rsW), "s"(su[3]), "a"(fw2[6][1]), "v"(rr), "a"(fw2[6][2]), "a"(fw2[6][3]) : "memory"); SB();
  WAITVB(11);
  SB();
  asm volatile("v_mfma_f32_16x16x32_bf16 %0, %4, %5, %0\n\tbuffer_load_dwordx4 %1, %6, %7, %8 offen offset:0 sc1\n\tv_mfma_f32_16x16x32_bf16 %2, %4, %9, %2\n\tv_mfma_f32_16x16x32_bf16 %3, %4, %10, %3"
      : "+v"(ac[1][0]), "=&v"(pu[8]), "+v"(ac[1][1]), "+v"(ac[1][2]) : "v"(pu[9]), "a"(fw2[7][0]), "v"(vFj[2]), "s"(rsW), "s"(su[4]), "a"(fw2[7][1]), "a"(fw2[7][2]) : "memory"); SB();
  WAITL(0);
  asm volatile("v_add_f32 %0, %4, %5\n\tv_add_f32 %1, %6, %5\n\tv_mfma_f32_16x16x32_bf16 %2, %7, %8, %2\n\tv_add_f32 %3, %9, %5"
      : "=&v"(ye[0]), "=&v"(ye[1]), "+v"(ac[1][3]), "=&v"(ye[2]) : "v"(py[0][0][0]), "v"(bias), "v"(py[0][0][1]), "v"(pu[9]), "a"(fw2[7][3]), "v"(py[0][0][2])); SB();
  WAITVB(11);
  SB();
  asm volatile("v_mfma_f32_16x16x32_bf16 %0, %9, %10, %0\n\tbuffer_load_dwordx4 %1, %11, %12, %13 offen offset:0 sc1\n\tv_mfma_f32_16x16x32_bf16 %2, %9, %14, %2\n\tv_add_f32 %3, %15, %16\n\tv_add_f32 %4, %17, %18\n\tv_mfma_f32_16x16x32_bf16 %5, %9, %19, %5\n\tv_add_f32 %6, %20, %21\n\tv_add_f32 %7, %22, %23\n\tv_add_f32 %8, %24, %25"
      : "+v"(ac[1][0]), "=&v"(pu[9]), "+v"(ac[1][1]), "=&v"(ye[3]), "=&v"(ze[0]), "+v"(ac[1][2]), "=&v"(ze[1]), "=&v"(ze[2]), "=&v"(ze[3]) : "v"(pu[10]), "a"(fw2[8][0]), "v"(vFj[2]), "s"(rsW), "s"(su[5]), "a"(fw2[8][1]), "v"(py[0][0][3]), "v"(bias), "v"(py[0][1][0]), "v"(py[0][2][0]), "a"(fw2[8][2]), "v"(py[0][1][1]), "v"(py[0][2][1]), "v"(py[0][1][2]), "v"(py[0][2][2]), "v"(py[0][1][3]), "v"(py[0][2][3]) : "memory"); SB();
  asm volatile("v_mfma_f32_16x16x32_bf16 %0, %2, %3, %0\n\tv_add_f32 %1, %1, %4"
      : "+v"(ac[1][3]), "+v"(ye[0]) : "v"(pu[10]), "a"(fw2[8][3]), "v"(py[0][3][0])); SB();
  WAITVB(11);
  SB();
  asm volatile("v_mfma_f32_16x16x32_bf16 %0, %9, %10, %0\n\tbuffer_load_dwordx4 %1, %11, %12, %13 offen offset:0 sc1\n\tv_mfma_f32_16x16x32_bf16 %2, %9, %14, %2\n\tv_add_f32 %3, %3, %15\n\tv_add_f32 %4, %4, %16\n\tv_mfma_f32_16x16x32_bf16 %5, %9, %17, %5\n\tv_add_f32 %6, %6, %18\n\tv_add_f32 %7, %7, %19\n\tv_add_f32 %3, %3, %20\n\tv_mfma_f32_16x16x32_bf16 %8, %9, %21, %8"
      : "+v"(ac[1][0]), "=&v"(pu[10]), "+v"(ac[1][1]), "+v"(ye[1]), "+v"(ye[2]), "+v"(ac[1][2]), "+v"(ye[3]), "+v"(ye[0]), "+v"(ac[1][3]) : "v"(pu[11]), "v"(fw2[9][0]), "v"(vFj[2]), "s"(rsW), "s"(su[6]), "v"(fw2[9][1]), "v"(py[0][3][1]), "v"(py[0][3][2]), "v"(fw2[9][2]), "v"(py[0][3][3]), "v"(ze[0]), "v"(ze[1]), "v"(fw2[9][3]) : "memory"); SB();
  asm volatile("v_add_f32 %0, %0, %1"
      : "+v"(ye[2]) : "v"(ze[2])); SB();
  WAITVB(11);
  SB();
  asm volatile("v_mfma_f32_16x16x32_bf16 %0, %10, %11, %0\n\tbuffer_load_dwordx4 %1, %12, %13, %14 offen offset:0 sc1\n\tv_mfma_f32_16x16x32_bf16 %2, %10, %15, %2\n\tv_add_f32 %3, %3, %4\n\tv_mul_f32 %5, 0xbfb8aa3b, %16\n\tv_mfma_f32_16x16x32_bf16 %6, %10, %17, %6\n\tv_mul_f32 %7, 0xbfb8aa3b, %18\n\tv_mul_f32 %8, 0xbfb8aa3b, %19\n\tv_mul_f32 %4, 0xbfb8aa3b, %3\n\tv_mfma_f32_16x16x32_bf16 %9, %10, %20, %9"
      : "+v"(ac[1][0]), "=&v"(pu[11]), "+v"(ac[1][1]), "+v"(ye[3]), "+v"(ze[3]), "=&v"(ze[0]), "+v"(ac[1][2]), "=&v"(ze[1]), "=&v"(ze[2]), "+v"(ac[1][3]) : "v"(pu[0]), "v"(fw2[10][0]), "v"(vFj[2]), "s"(rsW), "s"(su[7]), "v"(fw2[10][1]), "v"(ye[0]), "v"(fw2[10][2]), "v"(ye[1]), "v"(ye[2]), "v"(fw2[10][3]) : "memory"); SB();
  WAITVB(11);
  SB();
  asm volatile("v_mfma_f32_16x16x32_bf16 %0, %7, %8, %0\n\tbuffer_load_dwordx4 %1, %9, %10, %11 offen offset:0 sc1\n\tv_mfma_f32_16x16x32_bf16 %2, %7, %12, %2\n\tv_exp_f32 %3, %3\n\tv_mfma_f32_16x16x32_bf16 %4, %7, %13, %4\n\tv_exp_f32 %5, %5\n\tv_mfma_f32_16x16x32_bf16 %6, %7, %14, %6"
      : "+v"(ac[1][0]), "=&v"(pu[0]), "+v"(ac[1][1]), "+v"(ze[0]), "+v"(ac[1][2]), "+v"(ze[1]), "+v"(ac[1][3]) : "v"(pu[1]), "v"(fw2[11][0]), "v"(vFj[2]), "s"(rsW), "s"(su[8]), "v"(fw2[11][1]), "v"(fw2[11][2]), "v"(fw2[11][3]) : "memory"); SB();
  WAITVB(11);
  SB();
  asm volatile("v_mfma_f32_16x16x32_bf16 %0, %9, %10, %0\n\tbuffer_load_dwordx4 %1, %11, %12, %13 offen offset:0 sc1\n\tv_mfma_f32_16x16x32_bf16 %2, %9, %14, %2\n\tv_exp_f32 %3, %3\n\tv_mfma_f32_16x16x32_bf16 %4, %9, %15, %4\n\tv_exp_f32 %5, %5\n\tv_add_f32 %6, 1.0, %6\n\tv_mfma_f32_16x16x32_bf16 %7, %9, %16, %7\n\tv_add_f32 %8, 1.0, %8"
      : "+v"(ac[1][0]), "=&v"(pu[1]), "+v"(ac[1][1]), "+v"(ze[2]), "+v"(ac[1][2]), "+v"(ze[3]), "+v"(ze[0]), "+v"(ac[1][3]), "+v"(ze[1]) : "v"(pu[2]), "v"(fw2[12][0]), "v"(vFj[2]), "s"(rsW), "s"(su[9]), "v"(fw2[12][1]), "v"(fw2[12][2]), "v"(fw2[12][3]) : "memory"); SB();
  WAITVB(11);
  SB();
  asm volatile("v_mfma_f32_16x16x32_bf16 %0, %9, %10, %0\n\tbuffer_load_dwordx4 %1, %11, %12, %13 offen offset:0 sc1\n\tv_mfma_f32_16x16x32_bf16 %2, %9, %14, %2\n\tv_add_f32 %3, 1.0, %3\n\tv_add_f32 %4, 1.0, %4\n\tv_mfma_f32_16x16x32_bf16 %5, %9, %15, %5\n\tv_rcp_f32 %6, %6\n\tv_mfma_f32_16x16x32_bf16 %7, %9, %16, %7\n\tv_rcp_f32 %8, %8"
      : "+v"(ac[1][0]), "=&v"(pu[2]), "+v"(ac[1][1]), "+v"(ze[2]), "+v"(ze[3]), "+v"(ac[1][2]), "+v"(ze[0]), "+v"(ac[1][3]), "+v"(ze[1]) : "v"(pu[3]), "v"(fw2[13][0]), "v"(vFj[2]), "s"(rsW), "s"(su[10]), "v"(fw2[13][1]), "v"(fw2[13][2]), "v"(fw2[13][3]) : "memory"); SB();
  STAMP(41);
  WAITVB(10);
  SB();
  asm volatile("v_mfma_f32_16x16x32_bf16 %0, %9, %10, 0\n\tbuffer_load_dwordx4 %1, %11, %12, %13 offen offset:0 sc1\n\tv_mfma_f32_16x16x32_bf16 %2, %9, %14, 0\n\tv_rcp_f32 %3, %3\n\tv_mfma_f32_16x16x32_bf16 %4, %9, %15, 0\n\tv_rcp_f32 %5, %5\n\tv_mul_f32 %6, %16, %17\n\tv_mfma_f32_16x16x32_bf16 %7, %9, %18, 0\n\tv_mul_f32 %8, %19, %20"
      : "=&v"(ac[0][0]), "=&v"(pu[3]), "=&v"(ac[0][1]), "+v"(ze[2]), "=&v"(ac[0][2]), "+v"(ze[3]), "=&v"(ge[0]), "=&v"(ac[0][3]), "=&v"(ge[1]) : "v"(pu[4]), "a"(fw2[0][0]), "v"(vFj[2]), "s"(rsW), "s"(su[11]), "a"(fw2[0][1]), "a"(fw2[0][2]), "v"(ye[0]), "v"(ze[0]), "a"(fw2[0][3]), "v"(ye[1]), "v"(ze[1]) : "memory"); SB();
  WAITVB(10);
  SB();
  asm volatile("v_mfma_f32_16x16x32_bf16 %0, %9, %10, %0\n\tbuffer_load_dwordx4 %1, %11, %12, %13 offen offset:0 sc1\n\tv_mfma_f32_16x16x32_bf16 %2, %9, %14, %2\n\tds_write_b128 %15, %16 offset:4096\n\tv_mfma_f32_16x16x32_bf16 %3, %9, %17, %3\n\tv_mul_f32 %4, %18, %19\n\tv_mul_f32 %5, %20, %21\n\tds_read_b128 %6, %22 offset:15360\n\tv_mfma_f32_16x16x32_bf16 %7, %9, %23, %7\n\tds_read_b128 %8, %22 offset:23552"
      : "+v"(ac[0][0]), "=&v"(pu[4]), "+v"(ac[0][1]), "+v"(ac[0][2]), "=&v"(ge[2]), "=&v"(ge[3]), "=&a"(fwo[22]), "+v"(ac[0][3]), "=&a"(fwo[23]) : "v"(pu[5]), "a"(fw2[1][0]), "v"(vFj[2]), "s"(rsW), "s"(su[12]), "a"(fw2[1][1]), "v"(rwd), "v"(ac[1][0]), "a"(fw2[1][2]), "v"(ye[2]), "v"(ze[2]), "v"(ye[3]), "v"(ze[3]), "v"(ro), "a"(fw2[1][3]) : "memory"); SB();
  WAITVB(10);
  SB();
  asm volatile("v_mfma_f32_16x16x32_bf16 %0, %9, %10, %0\n\tbuffer_load_dwordx4 %1, %11, %12, %13 offen offset:0 sc1\n\tv_mfma_f32_16x16x32_bf16 %2, %9, %14, %2\n\tds_write_b128 %15, %16 offset:5120\n\tv_mfma_f32_16x16x32_bf16 %3, %9, %17, %3\n\tds_read_b128 %4, %18 offset:32768\n\tds_read_b128 %5, %18 offset:38912\n\tds_read_b128 %6, %18 offset:45056\n\tv_mfma_f32_16x16x32_bf16 %7, %9, %19, %7\n\tds_read_b128 %8, %18 offset:33792"
      : "+v"(ac[0][0]), "=&v"(pu[5]), "+v"(ac[0][1]), "+v"(ac[0][2]), "=&a"(fwo[24]), "=&a"(fwo[25]), "=&a"(fwo[26]), "+v"(ac[0][3]), "=&a"(fwo[27]) : "v"(pu[6]), "a"(fw2[2][0]), "v"(vFj[2]), "s"(rsW), "s"(su[13]), "a"(fw2[2][1]), "v"(rwd), "v"(ac[1][1]), "a"(fw2[2][2]), "v"(ro), "a"(fw2[2][3]) : "memory"); SB();
  WAITVB(10);
  SB();
  asm volatile("v_mfma_f32_16x16x32_bf16 %0, %1, %2, %0"
      : "+v"(ac[0][0]) : "v"(pu[7]), "a"(fw2[3][0])); SB();
  WAITV1(14, fo);
  if (__ballot(fo != nonce) != 0ull) poll1<4>(rsW, vFO, nonce);
  STAMP(18);
  asm volatile("buffer_load_dwordx4 %0, %8, %9, %10 offen offset:0 sc1\n\tv_mfma_f32_16x16x32_bf16 %1, %11, %12, %1\n\tds_write_b128 %13, %14 offset:6144\n\tv_mfma_f32_16x16x32_bf16 %2, %11, %15, %2\n\tds_read_b128 %3, %16 offset:39936\n\tds_read_b128 %4, %16 offset:46080\n\tds_read_b128 %5, %16 offset:34816\n\tv_mfma_f32_16x16x32_bf16 %6, %11, %17, %6\n\tds_read_b128 %7, %16 offset:40960"
      : "=&v"(pu[6]), "+v"(ac[0][1]), "+v"(ac[0][2]), "=&a"(fwo[28]), "=&a"(fwo[29]), "=&a"(fwo[30]), "+v"(ac[0][3]), "=&a"(fwo[31]) : "v"(vF), "s"(rsW), "s"(so0), "v"(pu[7]), "a"(fw2[3][1]), "v"(rwd), "v"(ac[1][2]), "a"(fw2[3][2]), "v"(ro), "a"(fw2[3][3]) : "memory"); SB();
  WAITVB(10);
  SB();
  asm volatile("v_mfma_f32_16x16x32_bf16 %0, %9, %10, %0\n\tbuffer_load_dwordx4 %1, %11, %12, %13 offen offset:128 sc1\n\tv_mfma_f32_16x16x32_bf16 %2, %9, %14, %2\n\tds_write_b128 %15, %16 offset:7168\n\tv_mfma_f32_16x16x32_bf16 %3, %9, %17, %3\n\tds_read_b128 %4, %18 offset:47104\n\tds_read_b128 %5, %18 offset:35840\n\tds_read_b128 %6, %18 offset:41984\n\tv_mfma_f32_16x16x32_bf16 %7, %9, %19, %7\n\tds_read_b128 %8, %18 offset:48128"
      : "+v"(ac[0][0]), "=&v"(pu[7]), "+v"(ac[0][1]), "+v"(ac[0][2]), "=&a"(fwo[32]), "=&a"(fwo[33]), "=&a"(fwo[34]), "+v"(ac[0][3]), "=&a"(fwo[35]) : "v"(pu[8]), "a"(fw2[4][0]), "v"(vF), "s"(rsW), "s"(so0), "a"(fw2[4][1]), "v"(rwd), "v"(ac[1][3]), "a"(fw2[4][2]), "v"(ro), "a"(fw2[4][3]) : "memory"); SB();
  WAITVB(10);
  SB();
  asm volatile("v_mfma_f32_16x16x32_bf16 %0, %3, %4, %0\n\tbuffer_load_dwordx4 %1, %5, %6, %7 offen offset:256 sc1\n\tv_mfma_f32_16x16x32_bf16 %2, %3, %8, %2"
      : "+v"(ac[0][0]), "=&v"(pu[8]), "+v"(ac[0][1]) : "v"(pu[9]), "a"(fw2[5][0]), "v"(vF), "s"(rsW), "s"(so0), "a"(fw2[5][1]) : "memory"); SB();
  BAR();
  SB();
  asm volatile("v_mfma_f32_16x16x32_bf16 %0, %6, %7, %0\n\tds_read_b128 %1, %8 offset:4096\n\tds_read_b128 %2, %8 offset:14336\n\tds_read_b128 %3, %8 offset:24576\n\tv_mfma_f32_16x16x32_bf16 %4, %6, %9, %4\n\tds_read_b128 %5, %8 offset:34816"
      : "+v"(ac[0][2]), "=&v"(py[1][0]), "=&v"(py[1][1]), "=&v"(py[1][2]), "+v"(ac[0][3]), "=&v"(py[1][3]) : "v"(pu[9]), "a"(fw2[5][2]), "v"(rr), "a"(fw2[5][3]) : "memory"); SB();
  WAITVB(10);
  SB();
  asm volatile("v_mfma_f32_16x16x32_bf16 %0, %11, %12, %0\n\tbuffer_load_dwordx4 %1, %13, %14, %15 offen offset:384 sc1\n\tv_mfma_f32_16x16x32_bf16 %2, %11, %16, %2\n\tds_read_b128 %3, %17 offset:36864\n\tds_read_b128 %4, %17 offset:43008\n\tv_mfma_f32_16x16x32_bf16 %5, %11, %18, %5\n\tds_read_b128 %6, %17 offset:49152\n\tds_read_b128 %7, %17 offset:37888\n\tds_read_b128 %8, %17 offset:44032\n\tv_mfma_f32_16x16x32_bf16 %9, %11, %19, %9\n\tds_read_b128 %10, %17 offset:50176"
      : "+v"(ac[0][0]), "=&v"(pu[9]), "+v"(ac[0][1]), "=&a"(fwo[36]), "=&a"(fwo[37]), "+v"(ac[0][2]), "=&a"(fwo[38]), "=&a"(fwo[39]), "=&a"(fwo[40]), "+v"(ac[0][3]), "=&a"(fwo[41]) : "v"(pu[10]), "a"(fw2[6][0]), "v"(vF), "s"(rsW), "s"(so0), "a"(fw2[6][1]), "v"(ro), "a"(fw2[6][2]), "a"(fw2[6][3]) : "memory"); SB();
  WAITVB(10);
  SB();
  asm volatile("v_mfma_f32_16x16x32_bf16 %0, %3, %4, %0\n\tbuffer_load_dwordx4 %1, %5, %6, %7 offen offset:512 sc1\n\tv_mfma_f32_16x16x32_bf16 %2, %3, %8, %2"
      : "+v"(ac[0][0]), "=&v"(pu[10]), "+v"(ac[0][1]) : "v"(pu[11]), "a"(fw2[7][0]), "v"(vF), "s"(rsW), "s"(so0), "a"(fw2[7][1]) : "memory"); SB();
  WAITL(6);
  asm volatile("v_add_f32 %0, %7, %8\n\tv_mfma_f32_16x16x32_bf16 %1, %9, %10, %1\n\tv_add_f32 %2, %11, %8\n\tv_add_f32 %3, %12, %8\n\tv_add_f32 %4, %13, %8\n\tv_mfma_f32_16x16x32_bf16 %5, %9, %14, %5\n\tv_add_f32 %6, %15, %16"
      : "=&v"(ye[0]), "+v"(ac[0][2]), "=&v"(ye[1]), "=&v"(ye[2]), "=&v"(ye[3]), "+v"(ac[0][3]), "=&v"(ze[0]) : "v"(py[1][0][0]), "v"(bias), "v"(pu[11]), "a"(fw2[7][2]), "v"(py[1][0][1]), "v"(py[1][0][2]), "v"(py[1][0][3]), "a"(fw2[7][3]), "v"(py[1][1][0]), "v"(py[1][2][0])); SB();
  WAITVB(10);
  SB();
  asm volatile("v_mfma_f32_16x16x32_bf16 %0, %9, %10, %0\n\tbuffer_load_dwordx4 %1, %11, %12, %13 offen offset:640 sc1\n\tv_mfma_f32_16x16x32_bf16 %2, %9, %14, %2\n\tv_add_f32 %3, %15, %16\n\tv_add_f32 %4, %17, %18\n\tv_mfma_f32_16x16x32_bf16 %5, %9, %19, %5\n\tv_add_f32 %6, %20, %21\n\tv_add_f32 %7, %7, %22\n\tv_add_f32 %8, %8, %23"
      : "+v"(ac[0][0]), "=&v"(pu[11]), "+v"(ac[0][1]), "=&v"(ze[1]), "=&v"(ze[2]), "+v"(ac[0][2]), "=&v"(ze[3]), "+v"(ye[0]), "+v"(ye[1]) : "v"(pu[0]), "a"(fw2[8][0]), "v"(vF), "s"(rsW), "s"(so0), "a"(fw2[8][1]), "v"(py[1][1][1]), "v"(py[1][2][1]), "v"(py[1][1][2]), "v"(py[1][2][2]), "a"(fw2[8][2]), "v"(py[1][1][3]), "v"(py[1][2][3]), "v"(py[1][3][0]), "v"(py[1][3][1]) : "memory"); SB();
  asm volatile("v_mfma_f32_16x16x32_bf16 %0, %2, %3, %0\n\tv_add_f32 %1, %1, %4"
      : "+v"(ac[0][3]), "+v"(ye[2]) : "v"(pu[0]), "a"(fw2[8][3]), "v"(py[1][3][2])); SB();
  WAITVB(10);
  SB();
  asm volatile("v_mfma_f32_16x16x32_bf16 %0, %9, %10, %0\n\tbuffer_load_dwordx4 %1, %11, %12, %13 offen offset:768 sc1\n\tv_mfma_f32_16x16x32_bf16 %2, %9, %14, %2\n\tv_add_f32 %3, %3, %15\n\tv_add_f32 %4, %4, %16\n\tv_mfma_f32_16x16x32_bf16 %5, %9, %17, %5\n\tv_add_f32 %6, %6, %18\n\tv_add_f32 %7, %7, %19\n\tv_add_f32 %3, %3, %20\n\tv_mfma_f32_16x16x32_bf16 %8, %9, %21, %8"
      : "+v"(ac[0][0]), "=&v"(pu[0]), "+v"(ac[0][1]), "+v"(ye[3]), "+v"(ye[0]), "+v"(ac[0][2]), "+v"(ye[1]), "+v"(ye[2]), "+v"(ac[0][3]) : "v"(pu[1]), "v"(fw2[9][0]), "v"(vF), "s"(rsW), "s"(so0), "v"(fw2[9][1]), "v"(py[1][3][3]), "v"(ze[0]), "v"(fw2[9][2]), "v"(ze[1]), "v"(ze[2]), "v"(ze[3]), "v"(fw2[9][3]) : "memory"); SB();
  asm volatile("v_mul_f32 %0, 0xbfb8aa3b, %1"
      : "=&v"(ze[0]) : "v"(ye[0])); SB();
  WAITVB(10);
  SB();
  asm volatile("v_mfma_f32_16x16x32_bf16 %0, %9, %10, %0\n\tbuffer_load_dwordx4 %1, %11, %12, %13 offen offset:0 sc1\n\tv_mfma_f32_16x16x32_bf16 %2, %9, %14, %2\n\tv_mul_f32 %3, 0xbfb8aa3b, %15\n\tv_mul_f32 %4, 0xbfb8aa3b, %16\n\tv_mfma_f32_16x16x32_bf16 %5, %9, %17, %5\n\tv_mul_f32 %6, 0xbfb8aa3b, %18\n\tv_exp_f32 %7, %7\n\tv_mfma_f32_16x16x32_bf16 %8, %9, %19, %8"
      : "+v"(ac[0][0]), "=&v"(pu[1]), "+v"(ac[0][1]), "=&v"(ze[1]), "=&v"(ze[2]), "+v"(ac[0][2]), "=&v"(ze[3]), "+v"(ze[0]), "+v"(ac[0][3]) : "v"(pu[2]), "v"(fw2[10][0]), "v"(vF), "s"(rsW), "s"(so1), "v"(fw2[10][1]), "v"(ye[1]), "v"(ye[2]), "v"(fw2[10][2]), "v"(ye[3]), "v"(fw2[10][3]) : "memory"); SB();
  WAITVB(10);
  SB();
  asm volatile("v_mfma_f32_16x16x32_bf16 %0, %7, %8, %0\n\tbuffer_load_dwordx4 %1, %9, %10, %11 offen offset:128 sc1\n\tv_mfma_f32_16x16x32_bf16 %2, %7, %12, %2\n\tv_exp_f32 %3, %3\n\tv_mfma_f32_16x16x32_bf16 %4, %7, %13, %4\n\tv_exp_f32 %5, %5\n\tv_mfma_f32_16x16x32_bf16 %6, %7, %14, %6"
      : "+v"(ac[0][0]), "=&v"(pu[2]), "+v"(ac[0][1]), "+v"(ze[1]), "+v"(ac[0][2]), "+v"(ze[2]), "+v"(ac[0][3]) : "v"(pu[3]), "v"(fw2[11][0]), "v"(vF), "s"(rsW), "s"(so1), "v"(fw2[11][1]), "v"(fw2[11][2]), "v"(fw2[11][3]) : "memory"); SB();
  WAITVB(10);
  SB();
  asm volatile("v_mfma_f32_16x16x32_bf16 %0, %9, %10, %0\n\tbuffer_load_dwordx4 %1, %11, %12, %13 offen offset:256 sc1\n\tv_mfma_f32_16x16x32_bf16 %2, %9, %14, %2\n\tv_exp_f32 %3, %3\n\tv_mfma_f32_16x16x32_bf16 %4, %9, %15, %4\n\tv_add_f32 %5, 1.0, %5\n\tv_add_f32 %6, 1.0, %6\n\tv_add_f32 %7, 1.0, %7\n\tv_mfma_f32_16x16x32_bf16 %8, %9, %16, %8\n\tv_add_f32 %3, 1.0, %3"
      : "+v"(ac[0][0]), "=&v"(pu[3]), "+v"(ac[0][1]), "+v"(ze[3]), "+v"(ac[0][2]), "+v"(ze[0]), "+v"(ze[1]), "+v"(ze[2]), "+v"(ac[0][3]) : "v"(pu[4]), "v"(fw2[12][0]), "v"(vF), "s"(rsW), "s"(so1), "v"(fw2[12][1]), "v"(fw2[12][2]), "v"(fw2[12][3]) : "memory"); SB();
  WAITVB(10);
  SB();
  asm volatile("v_mfma_f32_16x16x32_bf16 %0, %8, %9, %0\n\tbuffer_load_dwordx4 %1, %10, %11, %12 offen offset:384 sc1\n\tv_mfma_f32_16x16x32_bf16 %2, %8, %13, %2\n\tv_rcp_f32 %3, %3\n\tv_mfma_f32_16x16x32_bf16 %4, %8, %14, %4\n\tv_rcp_f32 %5, %5\n\tv_mfma_f32_16x16x32_bf16 %6, %8, %15, %6\n\tv_rcp_f32 %7, %7"
      : "+v"(ac[0][0]), "=&v"(pu[4]), "+v"(ac[0][1]), "+v"(ze[0]), "+v"(ac[0][2]), "+v"(ze[1]), "+v"(ac[0][3]), "+v"(ze[2]) : "v"(pu[5]), "v"(fw2[13][0]), "v"(vF), "s"(rsW), "s"(so1), "v"(fw2[13][1]), "v"(fw2[13][2]), "v"(fw2[13][3]) : "memory"); SB();
  STAMP(42);
  WAITVB(10);
  SB();
  asm volatile("v_mfma_f32_16x16x32_bf16 %0, %7, %8, 0\n\tbuffer_load_dwordx4 %1, %9, %10, %11 offen offset:512 sc1\n\tv_mfma_f32_16x16x32_bf16 %2, %12, %8, 0\n\tv_rcp_f32 %3, %3\n\tv_mfma_f32_16x16x32_bf16 %4, %13, %8, 0\n\tv_mul_f32 %5, %14, %15\n\tv_mul_f32 %6, %16, %17"
      : "=&v"(as[0]), "=&v"(pu[5]), "=&v"(as[1]), "+v"(ze[3]), "=&v"(as[2]), "=&v"(ge[4]), "=&v"(ge[5]) : "a"(fwo[0]), "v"(pu[6]), "v"(vF), "s"(rsW), "s"(so1), "a"(fwo[1]), "a"(fwo[2]), "v"(ye[0]), "v"(ze[0]), "v"(ye[1]), "v"(ze[1]) : "memory"); SB();
  WAITVB(10);
  SB();
  asm volatile("v_mfma_f32_16x16x32_bf16 %0, %5, %6, %0\n\tbuffer_load_dwordx4 %1, %7, %8, %9 offen offset:640 sc1\n\tv_mfma_f32_16x16x32_bf16 %2, %10, %6, %2\n\tds_write_b128 %11, %12 offset:0\n\tv_mfma_f32_16x16x32_bf16 %3, %13, %6, %3\n\tv_mul_f32 %4, %14, %15"
      : "+v"(as[0]), "=&v"(pu[6]), "+v"(as[1]), "+v"(as[2]), "=&v"(ge[6]) : "a"(fwo[3]), "v"(pu[7]), "v"(vF), "s"(rsW), "s"(so1), "a"(fwo[4]), "v"(rwd), "v"(ac[0][0]), "a"(fwo[5]), "v"(ye[2]), "v"(ze[2]) : "memory"); SB();
  WAITVB(10);
  SB();
  asm volatile("v_mfma_f32_16x16x32_bf16 %0, %5, %6, %0\n\tbuffer_load_dwordx4 %1, %7, %8, %9 offen offset:768 sc1\n\tv_mul_f32 %2, %10, %11\n\tv_mfma_f32_16x16x32_bf16 %3, %12, %6, %3\n\tv_mfma_f32_16x16x32_bf16 %4, %13, %6, %4"
      : "+v"(as[0]), "=&v"(pu[7]), "=&v"(ge[7]), "+v"(as[1]), "+v"(as[2]) : "a"(fwo[6]), "v"(pu[8]), "v"(vF), "s"(rsW), "s"(so1), "v"(ye[3]), "v"(ze[3]), "a"(fwo[7]), "a"(fwo[8]) : "memory"); SB();
  WAITVB(10);
  SB();
  asm volatile("v_mfma_f32_16x16x32_bf16 %0, %3, %4, %0\n\tds_write_b128 %5, %6 offset:1024\n\tv_mfma_f32_16x16x32_bf16 %1, %7, %4, %1\n\tv_mfma_f32_16x16x32_bf16 %2, %8, %4, %2"
      : "+v"(as[0]), "+v"(as[1]), "+v"(as[2]) : "a"(fwo[9]), "v"(pu[9]), "v"(rwd), "v"(ac[0][1]), "a"(fwo[10]), "a"(fwo[11]) : "memory"); SB();
  WAITVB(9);
  SB();
  asm volatile("v_mfma_f32_16x16x32_bf16 %0, %3, %4, %0\n\tv_mfma_f32_16x16x32_bf16 %1, %5, %4, %1\n\tds_write_b128 %6, %7 offset:2048\n\tv_mfma_f32_16x16x32_bf16 %2, %8, %4, %2"
      : "+v"(as[0]), "+v"(as[1]), "+v"(as[2]) : "a"(fwo[12]), "v"(pu[10]), "a"(fwo[13]), "v"(rwd), "v"(ac[0][2]), "a"(fwo[14]) : "memory"); SB();
  WAITVB(8);
  SB();
  asm volatile("v_mfma_f32_16x16x32_bf16 %0, %3, %4, %0\n\tv_mfma_f32_16x16x32_bf16 %1, %5, %4, %1\n\tv_mfma_f32_16x16x32_bf16 %2, %6, %4, %2"
      : "+v"(as[0]), "+v"(as[1]), "+v"(as[2]) : "a"(fwo[15]), "v"(pu[11]), "a"(fwo[16]), "a"(fwo[17])); SB();
  WAITVB(7);
  SB();
  asm volatile("v_mfma_f32_16x16x32_bf16 %0, %3, %4, %0\n\tds_write_b128 %5, %6 offset:3072\n\tv_mfma_f32_16x16x32_bf16 %1, %7, %4, %1\n\tv_mfma_f32_16x16x32_bf16 %2, %8, %4, %2"
      : "+v"(as[0]), "+v"(as[1]), "+v"(as[2]) : "a"(fwo[18]), "v"(pu[0]), "v"(rwd), "v"(ac[0][3]), "a"(fwo[19]), "a"(fwo[20]) : "memory"); SB();
  WAITVB(6);
  SB();
  asm volatile("v_mfma_f32_16x16x32_bf16 %0, %2, %3, %0\n\tv_mfma_f32_16x16x32_bf16 %1, %4, %3, %1"
      : "+v"(as[0]), "+v"(as[1]) : "a"(fwo[21]), "v"(pu[1]), "a"(fwo[22])); SB();
  BAR();
  asm volatile("ds_read_b128 %0, %2 offset:0\n\tv_mfma_f32_16x16x32_bf16 %1, %3, %4, %1"
      : "=&v"(py[2][0]), "+v"(as[2]) : "v"(rr), "a"(fwo[23]), "v"(pu[1]) : "memory"); SB();
  WAITVB(5);
  SB();
  asm volatile("v_mfma_f32_16x16x32_bf16 %0, %6, %7, %0\n\tds_read_b128 %1, %8 offset:10240\n\tds_read_b128 %2, %8 offset:20480\n\tv_mfma_f32_16x16x32_bf16 %3, %9, %7, %3\n\tds_read_b128 %4, %8 offset:30720\n\tv_mfma_f32_16x16x32_bf16 %5, %10, %7, %5"
      : "+v"(as[0]), "=&v"(py[2][1]), "=&v"(py[2][2]), "+v"(as[1]), "=&v"(py[2][3]), "+v"(as[2]) : "a"(fwo[24]), "v"(pu[2]), "v"(rr), "a"(fwo[25]), "a"(fwo[26]) : "memory"); SB();
  WAITVB(4);
  SB();
  asm volatile("v_mfma_f32_16x16x32_bf16 %0, %3, %4, %0\n\tv_mfma_f32_16x16x32_bf16 %1, %5, %4, %1\n\tv_mfma_f32_16x16x32_bf16 %2, %6, %4, %2"
      : "+v"(as[0]), "+v"(as[1]), "+v"(as[2]) : "a"(fwo[27]), "v"(pu[3]), "a"(fwo[28]), "a"(fwo[29])); SB();
  WAITVB(3);
  SB();
  asm volatile("v_mfma_f32_16x16x32_bf16 %0, %1, %2, %0"
      : "+v"(as[0]) : "a"(fwo[30]), "v"(pu[4])); SB();
  WAITL(0);
  asm volatile("v_add_f32 %0, %7, %8\n\tv_add_f32 %1, %9, %8\n\tv_mfma_f32_16x16x32_bf16 %2, %10, %11, %2\n\tv_add_f32 %3, %12, %8\n\tv_add_f32 %4, %13, %8\n\tv_mfma_f32_16x16x32_bf16 %5, %14, %11, %5\n\tv_add_f32 %6, %15, %16"
      : "=&v"(ye[0]), "=&v"(ye[1]), "+v"(as[1]), "=&v"(ye[2]), "=&v"(ye[3]), "+v"(as[2]), "=&v"(ze[0]) : "v"(py[2][0][0]), "v"(bias), "v"(py[2][0][1]), "a"(fwo[31]), "v"(pu[4]), "v"(py[2][0][2]), "v"(py[2][0][3]), "a"(fwo[32]), "v"(py[2][1][0]), "v"(py[2][2][0])); SB();
  WAITVB(2);
  SB();
  asm volatile("v_mfma_f32_16x16x32_bf16 %0, %8, %9, %0\n\tv_add_f32 %1, %10, %11\n\tv_add_f32 %2, %12, %13\n\tv_mfma_f32_16x16x32_bf16 %3, %14, %9, %3\n\tv_add_f32 %4, %15, %16\n\tv_add_f32 %5, %5, %17\n\tv_add_f32 %6, %6, %18\n\tv_mfma_f32_16x16x32_bf16 %7, %19, %9, %7"
      : "+v"(as[0]), "=&v"(ze[1]), "=&v"(ze[2]), "+v"(as[1]), "=&v"(ze[3]), "+v"(ye[0]), "+v"(ye[1]), "+v"(as[2]) : "a"(fwo[33]), "v"(pu[5]), "v"(py[2][1][1]), "v"(py[2][2][1]), "v"(py[2][1][2]), "v"(py[2][2][2]), "a"(fwo[34]), "v"(py[2][1][3]), "v"(py[2][2][3]), "v"(py[2][3][0]), "v"(py[2][3][1]), "a"(fwo[35])); SB();
  WAITVB(1);
  SB();
  asm volatile("v_mfma_f32_16x16x32_bf16 %0, %7, %8, %0\n\tv_add_f32 %1, %1, %9\n\tv_add_f32 %2, %2, %10\n\tv_mfma_f32_16x16x32_bf16 %3, %11, %8, %3\n\tv_add_f32 %4, %4, %12\n\tv_add_f32 %5, %5, %13\n\tv_mfma_f32_16x16x32_bf16 %6, %14, %8, %6\n\tv_add_f32 %1, %1, %15\n\tv_add_f32 %2, %2, %16"
      : "+v"(as[0]), "+v"(ye[2]), "+v"(ye[3]), "+v"(as[1]), "+v"(ye[0]), "+v"(ye[1]), "+v"(as[2]) : "a"(fwo[36]), "v"(pu[6]), "v"(py[2][3][2]), "v"(py[2][3][3]), "a"(fwo[37]), "v"(ze[0]), "v"(ze[1]), "a"(fwo[38]), "v"(ze[2]), "v"(ze[3])); SB();
  WAITVB(0);
  SB();
  asm volatile("v_mfma_f32_16x16x32_bf16 %0, %7, %8, %0\n\tv_mul_f32 %1, 0xbfb8aa3b, %9\n\tv_mul_f32 %2, 0xbfb8aa3b, %10\n\tv_mfma_f32_16x16x32_bf16 %3, %11, %8, %3\n\tv_mul_f32 %4, 0xbfb8aa3b, %12\n\tv_mul_f32 %5, 0xbfb8aa3b, %13\n\tv_mfma_f32_16x16x32_bf16 %6, %14, %8, %6\n\tv_exp_f32 %1, %1"
      : "+v"(as[0]), "=&v"(ze[0]), "=&v"(ze[1]), "+v"(as[1]), "=&v"(ze[2]), "=&v"(ze[3]), "+v"(as[2]) : "a"(fwo[39]), "v"(pu[7]), "v"(ye[0]), "v"(ye[1]), "a"(fwo[40]), "v"(ye[2]), "v"(ye[3]), "a"(fwo[41])); SB();
  STAMP(44);
  asm volatile("v_exp_f32 %0, %0\n\tv_exp_f32 %1, %1\n\tv_exp_f32 %2, %2\n\tv_add_f32 %3, 1.0, %3\n\tv_add_f32 %0, 1.0, %0\n\tv_add_f32 %1, 1.0, %1\n\tv_add_f32 %2, 1.0, %2\n\tv_rcp_f32 %3, %3\n\tv_rcp_f32 %0, %0\n\tv_rcp_f32 %1, %1\n\tv_rcp_f32 %2, %2\n\tv_mul_f32 %4, %8, %3\n\tv_mul_f32 %5, %9, %0\n\tv_mul_f32 %6, %10, %1\n\tv_mul_f32 %7, %11, %2"
      : "+v"(ze[1]), "+v"(ze[2]), "+v"(ze[3]), "+v"(ze[0]), "=&v"(ge[8]), "=&v"(ge[9]), "=&v"(ge[10]), "=&v"(ge[11]) : "v"(ye[0]), "v"(ye[1]), "v"(ye[2]), "v"(ye[3])); SB();
  u32x4 rsO_ = srd(out, 2048u * 896u); OPQ(rsO_);
  asm volatile("v_fma_f32 %0, %9, %10, %0\n\tv_fma_f32 %1, %11, %12, %1\n\tv_fma_f32 %2, %13, %14, %2\n\tv_fma_f32 %3, %15, %16, %3\n\tv_cvt_pk_bf16_f32 %4, %0, %1\n\tv_cvt_pk_bf16_f32 %5, %2, %3\n\tv_fma_f32 %6, %17, %18, %6\n\tv_fma_f32 %7, %19, %20, %7\n\tv_fma_f32 %8, %21, %22, %8"
      : "+v"(xe[0]), "+v"(xe[1]), "+v"(xe[2]), "+v"(xe[3]), "=&v"(ov[0][0]), "=&v"(ov[0][1]), "+v"(xe[4]), "+v"(xe[5]), "+v"(xe[6]) : "v"(as[0][0]), "v"(ge[0]), "v"(as[0][1]), "v"(ge[1]), "v"(as[0][2]), "v"(ge[2]), "v"(as[0][3]), "v"(ge[3]), "v"(as[1][0]), "v"(ge[4]), "v"(as[1][1]), "v"(ge[5]), "v"(as[1][2]), "v"(ge[6])); SB();
  asm volatile("v_fma_f32 %0, %9, %10, %0\n\tv_cvt_pk_bf16_f32 %1, %11, %12\n\tv_cvt_pk_bf16_f32 %2, %13, %0\n\tds_write_b64 %14, %15 offset:0\n\tv_fma_f32 %3, %16, %17, %3\n\tv_fma_f32 %4, %18, %19, %4\n\tv_fma_f32 %5, %20, %21, %5\n\tv_fma_f32 %6, %22, %23, %6\n\tv_cvt_pk_bf16_f32 %7, %3, %4\n\tv_cvt_pk_bf16_f32 %8, %5, %6"
      : "+v"(xe[7]), "=&v"(ov[1][0]), "=&v"(ov[1][1]), "+v"(xe[8]), "+v"(xe[9]), "+v"(xe[10]), "+v"(xe[11]), "=&v"(ov[2][0]), "=&v"(ov[2][1]) : "v"(as[1][3]), "v"(ge[7]), "v"(xe[4]), "v"(xe[5]), "v"(xe[6]), "v"(xta[0]), "v"(ov[0]), "v"(as[2][0]), "v"(ge[8]), "v"(as[2][1]), "v"(ge[9]), "v"(as[2][2]), "v"(ge[10]), "v"(as[2][3]), "v"(ge[11]) : "memory"); SB();
  asm volatile("ds_write_b64 %0, %1 offset:0\n\tds_write_b64 %2, %3 offset:0"
      :  : "v"(xta[1]), "v"(ov[1]), "v"(xta[2]), "v"(ov[2]) : "memory"); SB();
  { u32x4 t0_, t1_; DSR16(t0_, xo, 0); DSR16(t1_, xo, 1024); WAITLB(1);
    BS16(vXa, rsO_, 0, t0_, "sc1"); WAITLB(0); BS16(vXb, rsO_, 1024, t1_, "sc1"); }
  STAMP(23);
  if (b == 8 * 28 && w == 0) {
    unsigned f_[9], a_[9], bad_ = 0u;
#pragma unroll
    for (int k = 0; k < 9; ++k) { a_[k] = k < 8 ? WS_FU + ((k >> 1) * 128 + (k & 1) * 56 + (lane < 56 ? lane : 55)) * (4 * 4) : WS_FQ + lane * (4 * 4); HL4(f_[k], a_[k]); }
    asm volatile("s_waitcnt vmcnt(0)" : "+v"(f_[0]), "+v"(f_[1]), "+v"(f_[2]), "+v"(f_[3]), "+v"(f_[4]), "+v"(f_[5]), "+v"(f_[6]), "+v"(f_[7]), "+v"(f_[8]) :: "memory");
#pragma unroll
    for (int k = 0; k < 9; ++k) bad_ |= f_[k] ^ nonce;
    if (__ballot(bad_ != 0u) != 0ull) {
#pragma unroll
      for (int k = 0; k < 9; ++k) poll0<16>(rsW, a_[k], nonce);
    }
    const unsigned vZ = 0u; ST4(vZ, ws, nonce);
  }

  STAMP(12);
  STAMP_DUMP();
  }
  }
  asm volatile(".Lcpf_e:");
}

extern "C" void kernel_launch(void* const* d_in, const int* in_sizes, int n_in,
                              void* d_out, int out_size, void* d_ws, size_t ws_size,
                              hipStream_t stream) {
  fused_kernel<<<256, 256, 0, stream>>>((const char*)d_in[0], (const char*)d_in[1], (const char*)d_in[2], (const char*)d_in[4],
                                        (const char*)d_in[5], (const char*)d_in[6], (const char*)d_in[7], (char*)d_ws, (char*)d_out);
}
```

```cpp
#include <hip/hip_runtime.h>
#include <hip/hip_bf16.h>
#include <stdint.h>

typedef short bf16x8 __attribute__((ext_vector_type(8)));
typedef short s16x4 __attribute__((ext_vector_type(4)));
typedef float f32x4 __attribute__((ext_vector_type(4)));
typedef unsigned u32x4 __attribute__((ext_vector_type(4)));
typedef unsigned u32x2 __attribute__((ext_vector_type(2)));
typedef __bf16 bf16x2_t __attribute__((ext_vector_type(2)));
typedef float f32x2_t __attribute__((ext_vector_type(2)));
#define LDS_AS __attribute__((address_space(3)))
#define DEV __device__ __forceinline__

#define STAMP(i)
#define STAMP_DUMP()

#define SB() __builtin_amdgcn_sched_barrier(0)
#define WAITV(n) asm volatile("s_waitcnt vmcnt(" #n ")" ::: "memory")
#define WAITV1(n, a) asm volatile("s_waitcnt vmcnt(" #n ")" : "+v"(a) :: "memory")
#define WAITV2(n, a, b) asm volatile("s_waitcnt vmcnt(" #n ")" : "+v"(a), "+v"(b) :: "memory")
#define WAITV3(n, a, b, c) asm volatile("s_waitcnt vmcnt(" #n ")" : "+v"(a), "+v"(b), "+v"(c) :: "memory")
#define WAITV4(n, a, b, c, d) asm volatile("s_waitcnt vmcnt(" #n ")" : "+v"(a), "+v"(b), "+v"(c), "+v"(d) :: "memory")
#define WAITA3(n, a, b, c) asm volatile("s_waitcnt vmcnt(" #n ")" : "+a"(a), "+a"(b), "+a"(c) :: "memory")
#define WAITA1(n, a) asm volatile("s_waitcnt vmcnt(" #n ")" : "+a"(a) :: "memory")
#define WAITL(n) do { asm volatile("" ::: "memory"); __builtin_amdgcn_s_waitcnt(0xc07f | ((n) << 8)); asm volatile("" ::: "memory"); } while (0)
#define BAR() do { WAITL(0); __builtin_amdgcn_s_barrier(); asm volatile("" ::: "memory"); } while (0)
#define BAR0() do { asm volatile("" ::: "memory"); __builtin_amdgcn_s_barrier(); asm volatile("" ::: "memory"); } while (0)
#define OPQ(p) asm volatile("" : "+s"(p))
#define OPV(p) asm volatile("" : "+v"(p))
#define LD16(dst, voff, sb, imm) asm volatile("global_load_dwordx4 %0, %1, %2 offset:" #imm : "=&v"(dst) : "v"(voff), "s"(sb) : "memory")
#define LD8(dst, voff, sb, imm) asm volatile("global_load_dwordx2 %0, %1, %2 offset:" #imm : "=&v"(dst) : "v"(voff), "s"(sb) : "memory")
#define LD2(dst, voff, sb, imm) asm volatile("global_load_ushort %0, %1, %2 offset:" #imm : "=&v"(dst) : "v"(voff), "s"(sb) : "memory")
#define HL16V(dst, voff, so, imm) asm volatile("buffer_load_dwordx4 %0, %1, %2, %3 offen offset:" #imm " sc1" : "=&v"(dst) : "v"(voff), "s"(rsW), "s"(so) : "memory")
#define HL16A(dst, voff, so, imm) asm volatile("buffer_load_dwordx4 %0, %1, %2, %3 offen offset:" #imm " sc1" : "=&a"(dst) : "v"(voff), "s"(rsW), "s"(so) : "memory")
#define HL4(dst, voff) asm volatile("buffer_load_dword %0, %1, %2, 0 offen sc1" : "=&v"(dst) : "v"(voff), "s"(rsW) : "memory")
#define DMA1(v0, rs, so, lds, limm, imm, bits) \
  asm volatile("s_add_u32 m0, %3, %4\n\ts_nop 0\n\tbuffer_load_dwordx4 %0, %1, %2 offen offset:" #imm " " bits " lds" :: "v"(v0), "s"(rs), "s"(so), "s"(lds), "n"(limm) : "memory", "scc")
#define DMA1S(v0, rs, so, sadd, lds, limm, imm, bits) do { unsigned t_; \
  asm volatile("s_add_u32 m0, %4, %5\n\ts_add_u32 %0, %3, %6\n\tbuffer_load_dwordx4 %1, %2, %0 offen offset:" #imm " " bits " lds" \
               : "=&s"(t_) : "v"(v0), "s"(rs), "s"(so), "s"(lds), "n"(limm), "n"(sadd) : "memory", "scc"); } while (0)
#define DMA2S(v0, v1, rs, so, sadd, lds, limm) do { unsigned t_; \
  asm volatile("s_add_u32 m0, %5, %6\n\ts_add_u32 %0, %4, %7\n\tbuffer_load_dwordx4 %1, %3, %0 offen lds\n\tbuffer_load_dwordx4 %2, %3, %0 offen offset:1024 lds" \
               : "=&s"(t_) : "v"(v0), "v"(v1), "s"(rs), "s"(so), "s"(lds), "n"(limm), "n"(sadd) : "memory", "scc"); } while (0)
#define DMA4(v0, v1, rs, so, step, lds, limm) do { unsigned t_; \
  asm volatile("s_add_u32 m0, %5, %6\n\ts_add_u32 %0, %4, %7\n\tbuffer_load_dwordx4 %1, %3, %4 offen lds\n\tbuffer_load_dwordx4 %2, %3, %4 offen offset:1024 lds\n\t" \
               "buffer_load_dwordx4 %1, %3, %0 offen offset:2048 lds\n\tbuffer_load_dwordx4 %2, %3, %0 offen offset:3072 lds" \
               : "=&s"(t_) : "v"(v0), "v"(v1), "s"(rs), "s"(so), "s"(lds), "n"(limm), "n"(step) : "memory", "scc"); } while (0)
#define SETM0(lds, limm) asm volatile("s_add_u32 m0, %0, %1" :: "s"(lds), "n"(limm) : "memory", "scc")
#define SETM0S(lds, limm, t, so, sadd) asm volatile("s_add_u32 m0, %1, %2\n\ts_add_u32 %0, %3, %4" : "=&s"(t) : "s"(lds), "n"(limm), "s"(so), "n"(sadd) : "memory", "scc")
#define SETEX(m) asm volatile("s_mov_b64 exec, %0" :: "s"(m) : "memory")
#define ALLEX() asm volatile("s_mov_b64 exec, -1" ::: "memory")
#define DMAW2(v0, rs, so) asm volatile("s_cmp_eq_u32 %3, 3\n\ts_cbranch_scc1 .LBBa%=\n\tbuffer_load_dwordx4 %0, %1, %2 offen offset:0 sc1 lds\n\tbuffer_load_dwordx4 %0, %1, %2 offen offset:1024 sc1 lds\n\ts_branch .LBBb%=\n.LBBa%=:\n\tbuffer_load_dword %0, %1, %2 offen lds\n\tbuffer_load_dword %0, %1, %2 offen lds\n.LBBb%=:" \
  :: "v"(v0), "s"(rs), "s"(so), "s"(w) : "memory", "scc")
#define DMAW4(v0, rs, so) asm volatile("s_cmp_eq_u32 %3, 3\n\ts_cbranch_scc1 .LBBa%=\n\tbuffer_load_dwordx4 %0, %1, %2 offen offset:0 sc1 lds\n\tbuffer_load_dwordx4 %0, %1, %2 offen offset:1024 sc1 lds\n\tbuffer_load_dwordx4 %0, %1, %2 offen offset:2048 sc1 lds\n\tbuffer_load_dwordx4 %0, %1, %2 offen offset:3072 sc1 lds\n\ts_branch .LBBb%=\n.LBBa%=:\n\tbuffer_load_dword %0, %1, %2 offen lds\n\tbuffer_load_dword %0, %1, %2 offen lds\n\tbuffer_load_dword %0, %1, %2 offen lds\n\tbuffer_load_dword %0, %1, %2 offen lds\n.LBBb%=:" \
  :: "v"(v0), "s"(rs), "s"(so), "s"(w) : "memory", "scc")
#define DMAI(v0, rs, so, imm, bits) asm volatile("buffer_load_dwordx4 %0, %1, %2 offen offset:" #imm " " bits " lds" :: "v"(v0), "s"(rs), "s"(so) : "memory")
#define TOUCH(v0, rs, so, sadd) do { unsigned t_; \
  asm volatile("s_mov_b32 m0, %4\n\ts_add_u32 %0, %3, %5\n\tbuffer_load_dword %1, %2, %0 offen lds" : "=&s"(t_) : "v"(v0), "s"(rs), "s"(so), "s"(lds0 + DUMP), "n"(sadd) : "memory", "scc"); } while (0)
#define HS16(voff, so, imm, v) do { const u32x4 v_ = (v); \
  asm volatile("buffer_store_dwordx4 %0, %1, %2, %3 offen offset:" #imm " sc1\n\ts_nop 1" :: "v"(v_), "v"(voff), "s"(rsW), "s"(so) : "memory"); } while (0)
#define HS16M(lo, hi, voff, so, imm, v) do { const u32x4 v_ = (v); \
  asm volatile("s_mov_b32 exec_lo, " #lo "\n\ts_mov_b32 exec_hi, " #hi "\n\tbuffer_store_dwordx4 %0, %1, %2, %3 offen offset:" #imm " sc1\n\ts_mov_b64 exec, -1\n\ts_nop 0" \
               :: "v"(v_), "v"(voff), "s"(rsW), "s"(so) : "memory"); } while (0)
#define HS16H(hi, voff, so, imm, v) do { const u32x4 v_ = (v); \
  asm volatile("s_mov_b32 exec_hi, " #hi "\n\tbuffer_store_dwordx4 %0, %1, %2, %3 offen offset:" #imm " sc1\n\ts_mov_b32 exec_hi, -1\n\ts_nop 0" \
               :: "v"(v_), "v"(voff), "s"(rsW), "s"(so) : "memory"); } while (0)
#define ST16(voff, sb, imm, v) do { const u32x4 v_ = (v); asm volatile("global_store_dwordx4 %0, %1, %2 offset:" #imm " sc1\n\ts_nop 1" :: "v"(voff), "v"(v_), "s"(sb) : "memory"); } while (0)
#define BS16(voff, rs, imm, v, bits) do { const u32x4 v_ = (v); asm volatile("buffer_store_dwordx4 %0, %1, %2, 0 offen offset:" #imm " " bits "\n\ts_nop 1" :: "v"(v_), "v"(voff), "s"(rs) : "memory"); } while (0)
#define ST4(voff, sb, v) do { const unsigned v_ = (v); asm volatile("global_store_dword %0, %1, %2 sc1" :: "v"(voff), "v"(v_), "s"(sb) : "memory"); } while (0)
#define FLAG16(mask, so) do { const u32x4 v_ = {nonce, nonce, nonce, nonce}; \
  asm volatile("s_mov_b64 exec, " #mask "\n\tbuffer_store_dwordx4 %0, %1, %2, %3 offen sc1\n\ts_mov_b64 exec, -1\n\ts_nop 0" :: "v"(v_), "v"(vL16), "s"(rsW), "s"(so) : "memory"); } while (0)
#define FLAG4(so) do { const unsigned v_ = nonce; \
  asm volatile("s_mov_b64 exec, 1\n\tbuffer_store_dword %0, %1, %2, %3 offen sc1\n\ts_mov_b64 exec, -1" :: "v"(v_), "v"(vL16), "s"(rsW), "s"(so) : "memory"); } while (0)
#define STFA(so) FLAG16(15, so)
#define STFQ(so) FLAG16(1, so)
#define STFU(so) FLAG16(1, so)
#define STFO(so) FLAG16(15, so)
DEV u32x4 srd(const void* p, unsigned bytes) { return (u32x4){(unsigned)(size_t)p, (unsigned)((size_t)p >> 32), bytes, 0x20000u}; }
DEV float lo2f(unsigned v) { return __uint_as_float(v << 16); }
DEV float hi2f(unsigned v) { return __uint_as_float(v & 0xffff0000u); }
DEV unsigned pack2(float a, float b) { f32x2_t v = {a, b}; return __builtin_bit_cast(unsigned, __builtin_convertvector(v, bf16x2_t)); }
DEV float rbf(float a) { return __uint_as_float(pack2(0.f, a)); }
DEV f32x2_t rbf2(f32x2_t a) { return (f32x2_t){rbf(a[0]), rbf(a[1])}; }
DEV float dot2(unsigned a, unsigned b, float c) {
  return __builtin_amdgcn_fdot2_f32_bf16(__builtin_bit_cast(bf16x2_t, a), __builtin_bit_cast(bf16x2_t, b), c, false);
}
DEV bf16x8 cat8(s16x4 a, s16x4 b) { return __builtin_shufflevector(a, b, 0, 1, 2, 3, 4, 5, 6, 7); }
DEV bf16x8 tr8u(unsigned p, int second) {
  return cat8(__builtin_amdgcn_ds_read_tr16_b64_v4i16((LDS_AS s16x4*)p), __builtin_amdgcn_ds_read_tr16_b64_v4i16((LDS_AS s16x4*)(p + second)));
}
template <int CTRL> DEV float dpp_add(float v) {
  return v + __builtin_bit_cast(float, __builtin_amdgcn_update_dpp(0, __builtin_bit_cast(int, v), CTRL, 0xf, 0xf, true));
}
DEV float red16(float v) { return dpp_add<0x140>(dpp_add<0x141>(dpp_add<0x4E>(dpp_add<0xB1>(v)))); }
DEV unsigned ror8(unsigned v) { return (unsigned)__builtin_amdgcn_update_dpp(0, (int)v, 0x128, 0xf, 0xf, true); }
DEV float fmx(float a, float b) { return __builtin_elementwise_maximum(a, b); }
DEV float xmax2(float a, float b) {
  const auto r = __builtin_amdgcn_permlane16_swap(__float_as_uint(a), __float_as_uint(b), false, false);
  const float v = fmx(__uint_as_float(r[0]), __uint_as_float(r[1]));
  const auto s = __builtin_amdgcn_permlane32_swap(__float_as_uint(v), __float_as_uint(v), false, false);
  return fmx(__uint_as_float(s[0]), __uint_as_float(s[1]));
}
DEV float xsum2(float a, float b) {
  const auto r = __builtin_amdgcn_permlane16_swap(__float_as_uint(a), __float_as_uint(b), false, false);
  const float v = __uint_as_float(r[0]) + __uint_as_float(r[1]);
  const auto s = __builtin_amdgcn_permlane32_swap(__float_as_uint(v), __float_as_uint(v), false, false);
  return __uint_as_float(s[0]) + __uint_as_float(s[1]);
}
DEV f32x2_t xbcast2(float v) {
  const auto r = __builtin_amdgcn_permlane16_swap(__float_as_uint(v), __float_as_uint(v), false, false);
  return (f32x2_t){__uint_as_float(r[0]), __uint_as_float(r[1])};
}
#define MF(d, ca, a, cb, b) asm volatile("v_mfma_f32_16x16x32_bf16 %0, %1, %2, %0" : "+v"(d) : ca(a), cb(b))
#define MFZ(d, ca, a, cb, b) asm volatile("v_mfma_f32_16x16x32_bf16 %0, %1, %2, 0" : "=&v"(d) : ca(a), cb(b))
#define MFC(d, ca, a, cb, b, c) asm volatile("v_mfma_f32_16x16x32_bf16 %0, %1, %2, %3" : "=&v"(d) : ca(a), cb(b), "v"(c))
#define VADD(d, a, b) asm volatile("v_add_f32 %0, %1, %2" : "=v"(d) : "v"(a), "v"(b))
#define VADD1(d, a) asm volatile("v_add_f32 %0, 1.0, %1" : "=v"(d) : "v"(a))
#define VMUL(d, a, b) asm volatile("v_mul_f32 %0, %1, %2" : "=v"(d) : "v"(a), "v"(b))
#define VMULK(d, k, a) asm volatile("v_mul_f32 %0, " #k ", %1" : "=v"(d) : "v"(a))
#define VFMAK(d, a, b, k) asm volatile("v_fmaak_f32 %0, %1, %2, " #k : "=v"(d) : "v"(a), "v"(b))
#define VFMA(d, a, b, c) asm volatile("v_fma_f32 %0, %1, %2, %3" : "=v"(d) : "v"(a), "v"(b), "v"(c))
#define VFNMA(d, a, b, c) asm volatile("v_fma_f32 %0, -%1, %2, %3" : "=v"(d) : "v"(a), "v"(b), "v"(c))
#define VEXP(d, a) asm volatile("v_exp_f32 %0, %1" : "=v"(d) : "v"(a))
#define VRCP(d, a) asm volatile("v_rcp_f32 %0, %1" : "=v"(d) : "v"(a))
#define VRBF(d, a) asm volatile("v_cvt_pk_bf16_f32 %0, 0, %1" : "=v"(d) : "v"(a))
#define VPK(d, a, b) asm volatile("v_cvt_pk_bf16_f32 %0, %1, %2" : "=v"(d) : "v"(a), "v"(b))
#define VLO(d, a) asm volatile("v_lshlrev_b32 %0, 16, %1" : "=v"(d) : "v"(a))
#define VHI(d, a) asm volatile("v_and_b32 %0, 0xffff0000, %1" : "=v"(d) : "v"(a))
#define VADDU(d, k) asm volatile("v_add_u32 %0, " #k ", %0" : "+v"(d))
#define VADDS(d, k) asm volatile("v_add_u32 %0, %1, %0" : "+v"(d) : "s"(k))
#define VXOR(d, a, k) asm volatile("v_xor_b32 %0, " #k ", %1" : "=v"(d) : "v"(a))
#define VSWAP16(a, b) asm volatile("v_permlane16_swap_b32 %0, %1" : "+v"(a), "+v"(b))
#define DSR16(d, addr, off) asm volatile("ds_read_b128 %0, %1 offset:" #off : "=&v"(d) : "v"(addr) : "memory")
#define DSR16A(d, addr, off) asm volatile("ds_read_b128 %0, %1 offset:" #off : "=&a"(d) : "v"(addr) : "memory")
#define DSR8(d, addr, off) asm volatile("ds_read_b64 %0, %1 offset:" #off : "=&v"(d) : "v"(addr) : "memory")
#define DSW16(addr, off, d) asm volatile("ds_write_b128 %0, %1 offset:" #off :: "v"(addr), "v"(d) : "memory")
#define DSW8(addr, off, d) asm volatile("ds_write_b64 %0, %1 offset:" #off :: "v"(addr), "v"(d) : "memory")
#define WAITL2(n, a, b) asm volatile("s_waitcnt lgkmcnt(" #n ")" : "+v"(a), "+v"(b) :: "memory")
#define WAITL3(n, a, b, c) asm volatile("s_waitcnt lgkmcnt(" #n ")" : "+v"(a), "+v"(b), "+v"(c) :: "memory")
#define WAITL4(n, a, b, c, d) asm volatile("s_waitcnt lgkmcnt(" #n ")" : "+v"(a), "+v"(b), "+v"(c), "+v"(d) :: "memory")
#define WAITVB(n) __builtin_amdgcn_s_waitcnt(0x0f70 | ((n) & 15) | (((n) >> 4) << 14))
#define WAITLB(n) __builtin_amdgcn_s_waitcnt(0xc07f | ((n) << 8))
#define NOP(n) asm volatile("s_nop " #n)

constexpr int kSpinCap = 20000;
template <int SLEEP> DEV void poll1(const u32x4& rsW, unsigned voff, unsigned nonce) {
#pragma unroll 1
  for (int spin = 0; spin < kSpinCap; ++spin) {
    __builtin_amdgcn_s_sleep(SLEEP);
    unsigned v;
    asm volatile("buffer_load_dword %0, %1, %2, 0 offen sc1\n\ts_waitcnt vmcnt(0)" : "=&v"(v) : "v"(voff), "s"(rsW) : "memory");
    if (__ballot(v != nonce) == 0ull) return;
  }
}

template <int SLEEP> DEV void poll0(const u32x4& rsW, unsigned voff, unsigned nonce) {
  unsigned v;
  asm volatile("buffer_load_dword %0, %1, %2, 0 offen sc1\n\ts_waitcnt vmcnt(0)" : "=&v"(v) : "v"(voff), "s"(rsW) : "memory");
  if (__builtin_expect(__ballot(v != nonce) != 0ull, 0)) poll1<SLEEP>(rsW, voff, nonce);
}

DEV float ln_inv(float var) {
  const float sd = rbf(__builtin_amdgcn_sqrtf(rbf(rbf(fmaxf(var, 0.f)) + 1e-5f)));
  return __builtin_amdgcn_rcpf(sd);
}
DEV float ln_a(const unsigned (&p)[14], f32x2_t (&d)[14], float& dm) {
  float s0 = 0.f, s1 = 0.f;
#pragma unroll
  for (int i = 0; i < 14; i += 2) { s0 = dot2(p[i], 0x3f803f80u, s0); s1 = dot2(p[i + 1], 0x3f803f80u, s1); }
  const float mean = red16(s0 + s1) * (1.0f / 448.0f);
  const float mb = rbf(mean);
  dm = mean - mb;
  f32x2_t q = {0.f, 0.f}, q1 = {0.f, 0.f};
#pragma unroll
  for (int i = 0; i < 14; ++i) d[i] = (f32x2_t){lo2f(p[i]), hi2f(p[i])} - (f32x2_t){mb, mb};
#pragma unroll
  for (int i = 0; i < 14; i += 2) { q = d[i] * d[i] + q; q1 = d[i + 1] * d[i + 1] + q1; }
  q += q1;
  return q[0] + q[1];
}
DEV void ln_b(f32x2_t (&d)[14], float q, float dm, float& inv, float& a, float& c) {
  const float var = red16(q) * (1.0f / 448.0f) - dm * dm;
  inv = ln_inv(var);
  const float inv2 = ln_inv(var * inv * inv);
  a = inv * inv2; c = -rbf(dm * inv) * inv2;
#pragma unroll
  for (int i = 0; i < 14; ++i) { d[i] = rbf2(d[i]); asm volatile("" : "+v"(d[i])); }
}
DEV void ln_h(unsigned (&p)[14], const f32x2_t (&d)[14], float a, float c) {
#pragma unroll
  for (int i = 0; i < 14; ++i) {
    const f32x2_t r = __builtin_elementwise_fma(d[i], (f32x2_t){a, a}, (f32x2_t){c, c});
    p[i] = pack2(r[0], r[1]);
  }
}
DEV void ln_x(unsigned (&p)[14], const f32x2_t (&d)[14], float inv) {
#pragma unroll
  for (int i = 0; i < 14; ++i) { const f32x2_t r = d[i] * (f32x2_t){inv, inv}; p[i] = pack2(r[0], r[1]); }
}

constexpr unsigned WS_XN = 0;
constexpr unsigned WS_HT = WS_XN + 2048 * 1024;
constexpr unsigned WS_WQ = WS_HT + 4 * 64 * 8192;
constexpr unsigned WS_UT = WS_WQ + 8 * 4 * 6 * 8192;
constexpr unsigned WS_OT = WS_UT + 28 * 8 * 8192;
constexpr unsigned WS_WO = WS_OT + 128 * 2 * 8192;
constexpr unsigned WS_FA = WS_WO + 4 * 131072;
constexpr unsigned WS_FQ = WS_FA + 1536 * 4 * 16;
constexpr unsigned WS_FU = WS_FQ + 64 * 4 * 4;
constexpr unsigned WS_FO = WS_FU + 1024 * 4 * 4;
constexpr unsigned WS_END = WS_FO + 512 * 4 * 16;
constexpr int SCW = 10240;
constexpr int RB = 40960, RW = 30720;
constexpr int EW = 28672;
constexpr int WOB = 55296;
constexpr int S3B = 112640, TB = 131072;
constexpr int TP = 912;
constexpr int SK = 18432, SQ = 36864 - 512;
constexpr int DUMP = 130816;
constexpr int LDS_BYTES = 163840;

__global__ __launch_bounds__(256, 1) void fused_kernel(const char* __restrict__ x, const char* __restrict__ wqkv, const char* __restrict__ wo,
                                                       const char* __restrict__ w1, const char* __restrict__ b1,
                                                       const char* __restrict__ w2, const char* __restrict__ b2,
                                                       char* __restrict__ ws, char* __restrict__ out) {
  __shared__ __attribute__((aligned(16))) char smem[LDS_BYTES];
  const int lane = threadIdx.x & 63;
  const int w = __builtin_amdgcn_readfirstlane(threadIdx.x >> 6);
  const int b = blockIdx.x, xq = (b & 7) >> 1, jb = b >> 3;
  STAMP(0);
  { const char* pc_; unsigned vo_ = (threadIdx.x * 4 + (jb & 3)) * 128;
    asm volatile("s_getpc_b64 %0\n.Lcpf_b:\n\ts_mov_b32 m0, %2\n\tv_min_u32 %1, .Lcpf_e-.Lcpf_b-4, %1\n\tglobal_load_lds_dword %1, %0"
                 : "=&s"(pc_), "+v"(vo_) : "s"((unsigned)(size_t)(LDS_AS char*)smem + 40704) : "memory"); }
  const unsigned lds0 = (unsigned)(size_t)(LDS_AS char*)smem;
  const int n16 = lane & 15, g4 = lane >> 4, q4 = n16 >> 2, pq = lane & 3, r8 = lane >> 3;
  const unsigned vL16 = lane * 16;
  const unsigned wsw = ((lane & 7) ^ (r8 & 2)) * 16;
  const int th = xq >> 1, hq = xq & 1, pr_ = 64 * (1 - th) + 32 * hq + jb;
  unsigned vX = (4 * w + g4) * 896 + n16 * 16, vX3 = (4 * w + g4) * 896 + 768 + (n16 & 7) * 16 + (n16 >> 3) * 8, oX = pr_ * (16 * 896);
  asm volatile("" : "+v"(vX), "+v"(vX3)); asm volatile("" : "+s"(oX));
  u32x4 xa, xb, xc; u32x2 xd;
  const int d4 = (jb * 37) >> 8, fi = jb - 7 * d4, ft = 7 * xq + fi;
  const int rota = 4 * d4 + 2 * fi, rotg = 0 * fi;
#define KS1(c) ((((c) + rotg) & 0) + (((c) + rota) & 15) + 16 * w)
  const unsigned ldsR = lds0 + RB + w * RW;
  unsigned vW1a = r8 * 3584 + wsw + ft * 128, vW1b = (vW1a ^ 64) + (8 * 3584 - 1024);
  unsigned sp[3];
#pragma unroll
  for (int c = 0; c < 3; ++c) { sp[c] = KS1(c) * 114688; OPQ(sp[c]); }
  asm volatile("" : "+v"(vW1a), "+v"(vW1b) : "s"(ldsR));
  asm volatile("" :: "s"(x), "s"(wqkv), "s"(wo), "s"(w1), "s"(b1), "s"(w2), "s"(b2), "s"(ws), "s"(out));
  int cls = b & 1;
  if (cls) {
  asm volatile("; ROLE C");
  const char* sX = x + oX;
  LD16(xa, vX, sX, 0); LD16(xb, vX, sX, 256); LD16(xc, vX, sX, 512); LD8(xd, vX3, sX, 0);
  } else if (jb >= 28) {
    asm volatile("; ROLE B");
    const int b7 = (int)((size_t)w1 >> 7) + 7 * xq, f_ = ((3 - b7) & 3) + 4 * (jb & 1);
    const unsigned vT = (512 * w + 32 * ((4 * g4 + 2 * f_ + 2 + ((jb >> 1) & 1)) & 15) + 2 * n16 + (((b7 + f_) >> 2) & 1)) * 3584 + (7 * xq + f_) * 128;
    u32x4 rs1 = srd(w1, f_ < 7 ? 2048u * 3584u : 0u);
    TOUCH(vT, rs1, 0u, 0);
  } else {
  asm volatile("; ROLE B");
  u32x4 rs1 = srd(w1, 2048u * 3584u);
  DMA4(vW1a, vW1b, rs1, sp[0], 55296, ldsR, 0);
  DMA4(vW1a, vW1b, rs1, sp[1], 55296, ldsR, 4096);
  DMA4(vW1a, vW1b, rs1, sp[2], 55296, ldsR, 8192);
  }
  OPQ(cls);
  if (cls) {
  asm volatile("; NEED C");
  SB();
  const int kg = 32 * (1 - th) + jb, hpq = 4 * (1 - hq) + w;
  const unsigned ldsQ = lds0 + 16384 + w * 3072;
  unsigned vQ_ = r8 * 2688 + ((lane & 7) ^ ((r8 & 2) << 1)) * 16, sQ_ = kg * (8 * 2688) + hpq * 112;
  { u32x4 rsQ = srd(wqkv, 448u * 2688u);
    DMA1(vQ_, rsQ, sQ_, ldsQ, 0, 0, "");
    DMA1(vQ_, rsQ, sQ_, ldsQ, 128, 896, "");
    DMA1(vQ_, rsQ, sQ_, ldsQ, 256, 1792, "");
  }
  unsigned nonce; asm volatile("s_load_dword %0, %1, 0x0" : "=&s"(nonce) : "s"(ws) : "memory");
  const int tt = 8 * th + (jb >> 2), hp = 4 * hq + (jb & 3);
  const int st = 16 * (1 - th) + 8 * hq + (jb & 7), dt = jb >> 3, db0 = 4 * dt;
#define BLK(j) (db0 + ((j) ^ 0))
  u32x4 rsW = srd((char*)(((size_t)ws + 8191) & ~(size_t)4095) + 512, WS_END); OPQ(rsW);
  const unsigned trb = (8 * g4 + q4) * 128 + pq * 8, trf = ((q4 >> 1) | ((g4 & 1) << 1)) * 32;
  const unsigned vF = r8 * 1024 + (lane & 7) * 16;
  unsigned vN = (4 * w + g4) * 1024 + n16 * 16, sN = WS_XN + pr_ * 16384;
  unsigned hw = lds0 + (4 * w + g4) * TP + n16 * 16, hw3 = lds0 + (4 * w + g4) * TP + 768 + (n16 & 7) * 16 + (n16 >> 3) * 8;
  unsigned th_ = lds0 + q4 * TP + 224 * w + 64 * g4 + 32 * (pq >> 1) + 8 * (pq & 1), sH = WS_HT + w * 524288 + pr_ * 4096, sFA = WS_FA + (w * 128 + pr_) * (4 * 16);
  unsigned tq = ldsQ + q4 * 128 + ((g4 ^ (q4 & 2)) * 32) + pq * 8, vq = (r8 >> 1) * 49152 + (r8 & 1) * 1024 + (lane & 7) * 16, sq0, sq1, sq2;
  { const int ks = kg >> 2, k7 = ks >= 7; sq0 = WS_WQ + hpq * 196608 + k7 * (8192 - 896) + ks * 128 + (kg & 3) * 2048 + (((55 - kg) >> 31) & 0x80000000u); sq1 = sq0 + 16384; sq2 = sq0 + 32768; }
  asm volatile("" : "+v"(vN), "+v"(hw), "+v"(hw3), "+v"(th_), "+v"(tq), "+v"(vq)); asm volatile("" : "+s"(sN), "+s"(sH), "+s"(sFA), "+s"(sq0), "+s"(sq1), "+s"(sq2));
  WAITV4(3, xa, xb, xc, xd);
  STAMP(1);
  unsigned pr[14] = {xa[0], xa[1], xa[2], xa[3], xb[0], xb[1], xb[2], xb[3], xc[0], xc[1], xc[2], xc[3], xd[0], xd[1]};
  f32x2_t dd[14]; float dm, inv, la, lc;
  float qs_ = ln_a(pr, dd, dm); OPV(qs_);
  SB();
  SB();
  ln_b(dd, qs_, dm, inv, la, lc); OPV(la);
  SB();
  SB();
  ln_h(pr, dd, la, lc);
  *(LDS_AS u32x4*)hw = (u32x4){pr[0], pr[1], pr[2], pr[3]}; *(LDS_AS u32x4*)(hw + 256) = (u32x4){pr[4], pr[5], pr[6], pr[7]};
  *(LDS_AS u32x4*)(hw + 512) = (u32x4){pr[8], pr[9], pr[10], pr[11]}; *(LDS_AS u32x2*)hw3 = (u32x2){pr[12], pr[13]};
  SB();
  ln_x(pr, dd, inv);
  HS16(vN, sN, 0, ((u32x4){pr[0], pr[1], pr[2], pr[3]}));
  HS16(vN, sN, 256, ((u32x4){pr[4], pr[5], pr[6], pr[7]}));
  HS16(vN, sN, 512, ((u32x4){pr[8], pr[9], pr[10], pr[11]}));
  HS16M(0x00ff00ff, 0x00ff00ff, vN, sN, 768, ((u32x4){pr[12], pr[13], ror8(pr[12]), ror8(pr[13])}));
  asm volatile("s_waitcnt lgkmcnt(0)" : "+s"(nonce) :: "memory"); nonce += 1u;
  asm volatile("s_waitcnt vmcnt(4)"
      :  :  : "memory"); SB();
  const bf16x8 q0 = tr8u(tq, 512), q1 = tr8u(tq + 1024, 512), q2 = tr8u(tq + 2048, 512);
  BAR();
  STAMP(2);
  { const bf16x8 h0 = tr8u(th_, 4 * TP), h1 = tr8u(th_ + 16, 4 * TP), h2 = tr8u(th_ + 8 * TP, 4 * TP), h3 = tr8u(th_ + 8 * TP + 16, 4 * TP);
    HS16H(0xffffff, vL16, sH, 0, __builtin_bit_cast(u32x4, h0));
    HS16H(0xffffff, vL16, sH, 1024, __builtin_bit_cast(u32x4, h1));
    HS16H(0xffffff, vL16, sH, 2048, __builtin_bit_cast(u32x4, h2));
    HS16H(0xffffff, vL16, sH, 3072, __builtin_bit_cast(u32x4, h3));
  }
  HS16H(0xffffff, vq, sq0, 0, __builtin_bit_cast(u32x4, q0));
  HS16H(0xffffff, vq, sq1, 0, __builtin_bit_cast(u32x4, q1));
  HS16H(0xffffff, vq, sq2, 0, __builtin_bit_cast(u32x4, q2));
  STAMP(3);
  unsigned fw, fa, vFW, vFA;
  { const int k_ = lane < 56 ? lane : 55; vFW = WS_FA + 0 + ((hp & 3) * 128 + 64 * (k_ >> 5) + 32 * (1 - hq) + (k_ & 31)) * (4 * 16); }
  { const int k_ = 8 * tt - 1 + (n16 < 9 ? n16 : 8); vFA = WS_FA + 0 + (g4 * 128 + (k_ < 0 ? 0 : k_)) * (4 * 16); }
  unsigned vQ = w == 3 && (r8 & 1) ? 0x40000000u : vF      , sq[6];
#pragma unroll
  for (int t = 0; t < 6; ++t) { sq[t] = WS_WQ + ((hp * 4 + w) * 6 + t) * 8192; OPQ(sq[t]); }
  unsigned vXN = r8 * 1024 + ((lane & 7) ^ (4 * (w & 1) + (r8 >> 1))) * 16, sXa = WS_XN + (128 * tt - 16 + 8 * w) * 1024, sXb = sXa + (w >= 2 ? 128 - 147456 : 0);
  unsigned sXc = w >= 2 ? 0x80000000u : sXa, ldsX = lds0 + w * 1024, sXt;
  unsigned xr0 = lds0 + n16 * 128 + ((g4 ^ (n16 >> 1)) * 16), xr1 = xr0 ^ 64;
  unsigned sk0, sk1, sv, rk0, rk1, rv0, rv1, vS, sO, sFO = WS_FO + ((tt * 4 + w) * 8 + hp) * (4 * 16);
  { const int c = 16 * w + 4 * g4;
    const bool ok = c < 56;
    const int hd = ok ? (c >= 28) : (g4 & 1), d = ok ? c - 28 * hd : 28, row = (n16 >> 1) + 72 * (n16 & 1);
    sk0 = lds0 + hd * 9216 + row * 64 + (((d >> 3) ^ (n16 >> 3) ^ (2 * (n16 & 1))) << 4) + ((d >> 2) & 1) * 8; sk1 = sk0 ^ 32;
    sv = lds0 + (2 * hd + (d >> 4)) * 4608 + row * 32 + ((((d >> 2) & 3) ^ (n16 >> 3) ^ (2 * (n16 & 1))) << 3);
    rk0 = lds0 + (16 * w + n16) * 64 + ((g4 ^ q4) << 4); rk1 = (rk0 ^ 32) + 4608;
    rv0 = lds0 + (16 * w + 4 * g4 + q4) * 32 + (pq ^ (g4 & 1)) * 8; rv1 = (rv0 ^ 16) + 2304;
    const int ga = 7 * (hp & 3) + g4;
    vS = (n16 >> 3) * 16384 + (ga & 3) * 2048 + (ga >> 2) * 128 + (n16 & 7) * 16; sO = WS_OT + (8 * tt + 2 * w) * 16384 + (hp >> 2) * 8192; }
  bf16x8 wq[14][3];
  asm volatile("" : "+v"(vFW), "+v"(vFA), "+v"(vQ), "+v"(vXN), "+v"(xr0), "+v"(xr1)); asm volatile("" : "+s"(sXa), "+s"(sXb), "+s"(sXc), "+s"(ldsX));
  asm volatile("" : "+v"(sk0), "+v"(sk1), "+v"(sv), "+v"(rk0), "+v"(rk1), "+v"(rv0), "+v"(rv1), "+v"(vS)); asm volatile("" : "+s"(sO), "+s"(sFO));
  u32x4 rs2 = srd(w2, 1792u * 4096u); OPQ(rs2);
  unsigned vW2[4];
#pragma unroll
  for (int i = 0; i < 4; ++i) vW2[i] = (8 * (r8 & 3) + 4 * (r8 >> 2)) * 4096 + st * 128 + ((lane & 7) ^ (2 * i)) * 16 + i * (4096 - 1024);
  int rot2 = 4 * dt + 2 * (st & 7) + 0 * (b & 1); rot2 -= 14 * ((rot2 * 37) >> 9);
  bf16x8 fw2[14][4];
  unsigned ldsS = lds0 + S3B + w * 4096, ldsO = lds0 + WOB + w * 8192, ldsP = lds0 + WOB + 24576 + w * 6144, sWo, vWo = r8 * 16384 + (lane & 7) * 16, sWt;
  { const int k_ = BLK(w), k7 = (k_ * 37) >> 8; sWo = WS_WO + k7 * (131072 - 896) + k_ * 128; }
  unsigned fq, vFQ = WS_FQ + (lane < 56 ? lane : 55) * (4 * 4);
  asm volatile("" : "+v"(vW2[0]), "+v"(vW2[1]), "+v"(vW2[2]), "+v"(vW2[3]), "+v"(vFQ), "+v"(vWo)); asm volatile("" : "+s"(rot2), "+s"(ldsS), "+s"(ldsO), "+s"(ldsP), "+s"(sWo));
  unsigned tS[4];
#pragma unroll
  for (int i = 0; i < 4; ++i) { tS[i] = ldsS + q4 * 1024 + g4 * 128 + pq * 8 + ((i ^ q4) * 32); OPV(tS[i]); }
  unsigned tO[4];
#pragma unroll
  for (int i = 0; i < 4; ++i) { tO[i] = ldsO + q4 * 1024 + g4 * 128 + pq * 8 + ((i ^ q4) * 32); OPV(tO[i]); }
  unsigned su[14], sw[14], sc_ = 14 * w + rot2; OPQ(sc_);
  unsigned ldsZ = lds0 + w * 4096; OPQ(ldsZ);
  unsigned tZ[4];
#pragma unroll
  for (int i = 0; i < 4; ++i) { tZ[i] = ldsZ + q4 * 1024 + g4 * 128 + pq * 8 + ((i ^ q4) * 32); OPV(tZ[i]); }
  const int dk = 4 * g4 - n16;
  f32x4 cm; unsigned m1[4], mp[2], mq_ = g4 == 3 ? 0u : ~0u;
  const float cz = tt + w == 0 && lane < 32 ? -998244352.f : 0.f;
#pragma unroll
  for (int i = 0; i < 4; ++i) { m1[i] = (unsigned)((dk + i + 7) >> 31); cm[i] = __uint_as_float(~m1[i] & __float_as_uint((unsigned)(dk + i - 1) < 8u ? cz : -__builtin_inff())); }
  mp[0] = (m1[0] & 0xffffu) | (m1[1] & 0xffff0000u); mp[1] = (m1[2] & 0xffffu) | (m1[3] & 0xffff0000u);
  asm volatile("" : "+v"(cm), "+v"(m1[0]), "+v"(m1[1]), "+v"(m1[2]), "+v"(m1[3]), "+v"(mp[0]), "+v"(mp[1]), "+v"(mq_));
  asm volatile("s_waitcnt vmcnt(0)"
      :  :  : "memory"); SB();
  STFA(sFA + 0);
  STAMP(4);
  { const unsigned vT = lane < 14 ? ((jb * 4 + w) * 14 + lane) * 4096 + ((st & ~7) + ((3 - (int)((size_t)w2 >> 7)) & 7)) * 128 : 0x40000000u; TOUCH(vT, rs2, 0u, 0); }
  unsigned vFU, vFO;
  { const int da = (db0 * 37) >> 8, db = ((db0 + 3) * 37) >> 8, l_ = lane < 56 ? lane : 55;
    vFU = WS_FU + ((l_ < 28 ? da * 128 + l_ : db * 128 + l_ - 28) + w * 28) * (4 * 4);
    vFO = WS_FO + ((2 * st + (w >> 1)) * 8 + (lane & 7)) * (4 * 16);
    asm volatile("" : "+v"(vFU), "+v"(vFO)); }
  unsigned vFU1 = WS_FU + (512 + ((db0 * 37) >> 8) * 128 + w * 28 + (lane < 28 ? lane : 27)) * (4 * 4), fu2; OPV(vFU1);
  unsigned vFj[4], so0 = WS_OT + (4 * st + w) * 16384, so1 = so0 + 8192;
#pragma unroll
  for (int j = 0; j < 4; ++j) { const int k_ = BLK(j), k7 = (k_ * 37) >> 8; vFj[j] = vF + (WS_UT + k7 * (56 * 8192 - 896) + k_ * 128); OPV(vFj[j]); }
  asm volatile("" : "+s"(so0), "+s"(so1));
  const int tau = 2 * (n16 & 7) + (n16 >> 3);
  unsigned rwd = ldsS + vL16, rr = lds0 + S3B + w * 1024 + (16 * g4 + tau) * 16, ro = lds0 + WOB + vL16;
  unsigned xo = ldsS + 34816 + vL16, braw, xt = ldsS + 34816 + tau * 128 + (((g4 >> 1) ^ (n16 & 7)) * 16) + (g4 & 1) * 8, vB2 = (64 * st + 16 * w + tau) * 2;
  unsigned xs = xt ^ (32 * 0);
  asm volatile("" : "+v"(rwd), "+v"(rr), "+v"(ro), "+v"(xt), "+v"(xs), "+v"(xo), "+v"(vB2));
  unsigned vXa = (64 * st + 16 * w + r8) * 896 + db0 * 32 + ((lane & 7) ^ (r8 >> 1)) * 16, vXb = (vXa ^ 64) + (8 * 896 - 1024); OPV(vXa); OPV(vXb);
  { unsigned k_ = sc_ + (rot2 >= 14 ? -14 : 0); OPQ(k_); su[0] = k_ << 13; sw[0] = k_ << 17; OPQ(su[0]); OPQ(sw[0]); }
  { unsigned k_ = sc_ + (rot2 >= 13 ? -13 : 1); OPQ(k_); su[1] = k_ << 13; sw[1] = k_ << 17; OPQ(su[1]); OPQ(sw[1]); }
  { unsigned k_ = sc_ + (rot2 >= 12 ? -12 : 2); OPQ(k_); su[2] = k_ << 13; sw[2] = k_ << 17; OPQ(su[2]); OPQ(sw[2]); }
  { unsigned k_ = sc_ + (rot2 >= 11 ? -11 : 3); OPQ(k_); su[3] = k_ << 13; sw[3] = k_ << 17; OPQ(su[3]); OPQ(sw[3]); }
  { unsigned k_ = sc_ + (rot2 >= 10 ? -10 : 4); OPQ(k_); su[4] = k_ << 13; sw[4] = k_ << 17; OPQ(su[4]); OPQ(sw[4]); }
  { unsigned k_ = sc_ + (rot2 >= 9 ? -9 : 5); OPQ(k_); su[5] = k_ << 13; sw[5] = k_ << 17; OPQ(su[5]); OPQ(sw[5]); }
  { unsigned k_ = sc_ + (rot2 >= 8 ? -8 : 6); OPQ(k_); su[6] = k_ << 13; sw[6] = k_ << 17; OPQ(su[6]); OPQ(sw[6]); }
  { unsigned k_ = sc_ + (rot2 >= 7 ? -7 : 7); OPQ(k_); su[7] = k_ << 13; sw[7] = k_ << 17; OPQ(su[7]); OPQ(sw[7]); }
  { unsigned k_ = sc_ + (rot2 >= 6 ? -6 : 8); OPQ(k_); su[8] = k_ << 13; sw[8] = k_ << 17; OPQ(su[8]); OPQ(sw[8]); }
  { unsigned k_ = sc_ + (rot2 >= 5 ? -5 : 9); OPQ(k_); su[9] = k_ << 13; sw[9] = k_ << 17; OPQ(su[9]); OPQ(sw[9]); }
  { unsigned k_ = sc_ + (rot2 >= 4 ? -4 : 10); OPQ(k_); su[10] = k_ << 13; sw[10] = k_ << 17; OPQ(su[10]); OPQ(sw[10]); }
  { unsigned k_ = sc_ + (rot2 >= 3 ? -3 : 11); OPQ(k_); su[11] = k_ << 13; sw[11] = k_ << 17; OPQ(su[11]); OPQ(sw[11]); }
  { unsigned k_ = sc_ + (rot2 >= 2 ? -2 : 12); OPQ(k_); su[12] = k_ << 13; sw[12] = k_ << 17; OPQ(su[12]); OPQ(sw[12]); }
  { unsigned k_ = sc_ + (rot2 >= 1 ? -1 : 13); OPQ(k_); su[13] = k_ << 13; sw[13] = k_ << 17; OPQ(su[13]); OPQ(sw[13]); }
  unsigned vT2 = (lane & 31) * 4096 + st * 128 + (lane < 32 ? sw[2] : sw[3]); OPV(vT2);
  STAMP(5);
  __builtin_amdgcn_s_sleep(2);
  __builtin_amdgcn_s_sleep(2);
  __builtin_amdgcn_s_sleep(2);
  __builtin_amdgcn_s_sleep(2);
  asm volatile("buffer_load_dword %0, %2, %3, 0 offen sc1\n\tbuffer_load_dword %1, %4, %3, 0 offen sc1"
      : "=&v"(fw), "=&v"(fa) : "v"(vFW), "s"(rsW), "v"(vFA) : "memory"); SB();
  BAR();
  WAITV2(0, fw, fa);
  if (__builtin_expect(__ballot(fa != nonce) != 0ull, 0)) poll1<4>(rsW, vFA, nonce);
  STAMP(6);
  asm volatile("s_add_u32 m0, %1, 0\n\ts_add_u32 %0, %2, 0\n\tbuffer_load_dwordx4 %3, %4, %0 offen offset:0 sc1 lds\n\ts_add_u32 m0, %1, 4096\n\ts_add_u32 %0, %2, 32768\n\tbuffer_load_dwordx4 %3, %4, %0 offen offset:0 sc1 lds\n\ts_add_u32 m0, %1, 8192\n\ts_add_u32 %0, %2, 65536\n\tbuffer_load_dwordx4 %3, %4, %0 offen offset:0 sc1 lds\n\ts_add_u32 m0, %1, 12288\n\ts_add_u32 %0, %2, 98304\n\tbuffer_load_dwordx4 %3, %4, %0 offen offset:0 sc1 lds\n\ts_add_u32 m0, %1, 16384\n\ts_add_u32 %0, %5, 131072\n\tbuffer_load_dwordx4 %3, %4, %0 offen offset:0 sc1 lds"
      : "=&s"(sXt) : "s"(ldsX), "s"(sXa), "v"(vXN), "s"(rsW), "s"(sXb) : "memory", "scc"); SB();
  if (__builtin_expect(__ballot(fw != nonce) != 0ull, 0)) poll1<4>(rsW, vFW, nonce);
  STAMP(7);
  asm volatile("buffer_load_dwordx4 %0, %10, %11, %12 offen offset:0 sc1\n\tbuffer_load_dwordx4 %1, %10, %11, %13 offen offset:0 sc1\n\tbuffer_load_dwordx4 %2, %10, %11, %14 offen offset:0 sc1\n\tbuffer_load_dwordx4 %3, %10, %11, %12 offen offset:128 sc1\n\tbuffer_load_dwordx4 %4, %10, %11, %13 offen offset:128 sc1\n\tbuffer_load_dwordx4 %5, %10, %11, %14 offen offset:128 sc1\n\ts_add_u32 m0, %15, 20480\n\ts_add_u32 %6, %16, 16512\n\tbuffer_load_dwordx4 %17, %11, %6 offen offset:0 sc1 lds\n\ts_add_u32 m0, %15, 24576\n\ts_add_u32 %6, %16, 49280\n\tbuffer_load_dwordx4 %17, %11, %6 offen offset:0 sc1 lds\n\ts_add_u32 m0, %15, 28672\n\ts_add_u32 %6, %16, 82048\n\tbuffer_load_dwordx4 %17, %11, %6 offen offset:0 sc1 lds\n\ts_add_u32 m0, %15, 32768\n\ts_add_u32 %6, %16, 114816\n\tbuffer_load_dwordx4 %17, %11, %6 offen offset:0 sc1 lds\n\tbuffer_load_dwordx4 %7, %10, %11, %12 offen offset:256 sc1\n\tbuffer_load_dwordx4 %8, %10, %11, %13 offen offset:256 sc1\n\tbuffer_load_dwordx4 %9, %10, %11, %14 offen offset:256 sc1"
      : "=&a"(wq[0][0]), "=&a"(wq[0][1]), "=&a"(wq[0][2]), "=&a"(wq[1][0]), "=&a"(wq[1][1]), "=&a"(wq[1][2]), "=&s"(sXt), "=&a"(wq[2][0]), "=&a"(wq[2][1]), "=&a"(wq[2][2]) : "v"(vQ), "s"(rsW), "s"(sq[0]), "s"(sq[2]), "s"(sq[4]), "s"(ldsX), "s"(sXa), "v"(vXN) : "memory", "scc"); SB();
  f32x4 aq[8], ak[9], av[9]; bf16x8 xf[14][9]; u32x2 sg[27];
  asm volatile("s_waitcnt vmcnt(13)"
      :  :  : "memory"); SB();
  BAR0();
  STAMP(8);
  asm volatile("ds_read_b128 %0, %9 offset:0\n\tds_read_b128 %1, %9 offset:2048\n\tds_read_b128 %2, %9 offset:4096\n\tds_read_b128 %3, %9 offset:6144\n\tds_read_b128 %4, %9 offset:8192\n\tds_read_b128 %5, %9 offset:10240\n\tds_read_b128 %6, %9 offset:12288\n\tds_read_b128 %7, %9 offset:14336\n\tds_read_b128 %8, %9 offset:16384"
      : "=&v"(xf[0][0]), "=&v"(xf[0][1]), "=&v"(xf[0][2]), "=&v"(xf[0][3]), "=&v"(xf[0][4]), "=&v"(xf[0][5]), "=&v"(xf[0][6]), "=&v"(xf[0][7]), "=&v"(xf[0][8]) : "v"(xr0) : "memory"); SB();
  WAITVB(10);
  WAITL(8);
  SB();
  asm volatile("v_mfma_f32_16x16x32_bf16 %0, %4, %5, 0\n\ts_add_u32 m0, %6, 36864\n\ts_add_u32 %1, %7, 256\n\tv_mfma_f32_16x16x32_bf16 %2, %8, %5, 0\n\tds_read_b128 %3, %9 offset:0"
      : "=&v"(ak[0]), "=&s"(sXt), "=&v"(av[0]), "=&v"(xf[1][0]) : "a"(wq[0][1]), "v"(xf[0][0]), "s"(ldsX), "s"(sXa), "a"(wq[0][2]), "v"(xr1) : "memory", "scc"); SB();
  WAITL(8);
  SB();
  asm volatile("v_mfma_f32_16x16x32_bf16 %0, %4, %5, 0\n\tbuffer_load_dwordx4 %6, %7, %8 offen offset:0 sc1 lds\n\tv_mfma_f32_16x16x32_bf16 %1, %9, %5, 0\n\tds_read_b128 %2, %10 offset:2048\n\tv_mfma_f32_16x16x32_bf16 %3, %11, %5, 0"
      : "=&v"(ak[1]), "=&v"(av[1]), "=&v"(xf[1][1]), "=&v"(aq[0]) : "a"(wq[0][1]), "v"(xf[0][1]), "v"(vXN), "s"(rsW), "s"(sXt), "a"(wq[0][2]), "v"(xr1), "a"(wq[0][0]) : "memory"); SB();
  WAITL(8);
  SB();
  asm volatile("v_mfma_f32_16x16x32_bf16 %0, %6, %7, 0\n\tds_read_b128 %1, %8 offset:4096\n\tv_mfma_f32_16x16x32_bf16 %2, %9, %7, 0\n\tbuffer_load_dwordx4 %3, %10, %11, %12 offen offset:384 sc1\n\tv_mfma_f32_16x16x32_bf16 %4, %13, %7, 0\n\tds_read_b128 %5, %8 offset:6144"
      : "=&v"(ak[2]), "=&v"(xf[1][2]), "=&v"(av[2]), "=&a"(wq[3][0]), "=&v"(aq[1]), "=&v"(xf[1][3]) : "a"(wq[0][1]), "v"(xf[0][2]), "v"(xr1), "a"(wq[0][2]), "v"(vQ), "s"(rsW), "s"(sq[0]), "a"(wq[0][0]) : "memory"); SB();
  WAITL(9);
  SB();
  asm volatile("v_mfma_f32_16x16x32_bf16 %0, %5, %6, 0\n\ts_add_u32 m0, %7, 40960\n\ts_add_u32 %1, %8, 33024\n\tv_mfma_f32_16x16x32_bf16 %2, %9, %6, 0\n\tds_read_b128 %3, %10 offset:8192\n\tv_mfma_f32_16x16x32_bf16 %4, %11, %6, 0"
      : "=&v"(ak[3]), "=&s"(sXt), "=&v"(av[3]), "=&v"(xf[1][4]), "=&v"(aq[2]) : "a"(wq[0][1]), "v"(xf[0][3]), "s"(ldsX), "s"(sXa), "a"(wq[0][2]), "v"(xr1), "a"(wq[0][0]) : "memory", "scc"); SB();
  WAITL(9);
  SB();
  asm volatile("v_mfma_f32_16x16x32_bf16 %0, %5, %6, 0\n\tds_read_b128 %1, %7 offset:10240\n\tv_mfma_f32_16x16x32_bf16 %2, %8, %6, 0\n\tbuffer_load_dwordx4 %9, %10, %11 offen offset:0 sc1 lds\n\tv_mfma_f32_16x16x32_bf16 %3, %12, %6, 0\n\tds_read_b128 %4, %7 offset:12288"
      : "=&v"(ak[4]), "=&v"(xf[1][5]), "=&v"(av[4]), "=&v"(aq[3]), "=&v"(xf[1][6]) : "a"(wq[0][1]), "v"(xf[0][4]), "v"(xr1), "a"(wq[0][2]), "v"(vXN), "s"(rsW), "s"(sXt), "a"(wq[0][0]) : "memory"); SB();
  WAITL(10);
  SB();
  asm volatile("v_mfma_f32_16x16x32_bf16 %0, %5, %6, 0\n\tbuffer_load_dwordx4 %1, %7, %8, %9 offen offset:384 sc1\n\tv_mfma_f32_16x16x32_bf16 %2, %10, %6, 0\n\tds_read_b128 %3, %11 offset:14336\n\tv_mfma_f32_16x16x32_bf16 %4, %12, %6, 0"
      : "=&v"(ak[5]), "=&a"(wq[3][1]), "=&v"(av[5]), "=&v"(xf[1][7]), "=&v"(aq[4]) : "a"(wq[0][1]), "v"(xf[0][5]), "v"(vQ), "s"(rsW), "s"(sq[2]), "a"(wq[0][2]), "v"(xr1), "a"(wq[0][0]) : "memory"); SB();
  WAITL(10);
  SB();
  asm volatile("v_mfma_f32_16x16x32_bf16 %0, %5, %6, 0\n\tds_read_b128 %1, %7 offset:16384\n\tv_mfma_f32_16x16x32_bf16 %2, %8, %6, 0\n\ts_add_u32 m0, %9, 45056\n\ts_add_u32 %3, %10, 65792\n\tv_mfma_f32_16x16x32_bf16 %4, %11, %6, 0"
      : "=&v"(ak[6]), "=&v"(xf[1][8]), "=&v"(av[6]), "=&s"(sXt), "=&v"(aq[5]) : "a"(wq[0][1]), "v"(xf[0][6]), "v"(xr1), "a"(wq[0][2]), "s"(ldsX), "s"(sXa), "a"(wq[0][0]) : "memory", "scc"); SB();
  WAITL(10);
  SB();
  asm volatile("v_mfma_f32_16x16x32_bf16 %0, %4, %5, 0\n\tbuffer_load_dwordx4 %6, %7, %8 offen offset:0 sc1 lds\n\tv_mfma_f32_16x16x32_bf16 %1, %9, %5, 0\n\tbuffer_load_dwordx4 %2, %10, %7, %11 offen offset:384 sc1\n\tv_mfma_f32_16x16x32_bf16 %3, %12, %5, 0"
      : "=&v"(ak[7]), "=&v"(av[7]), "=&a"(wq[3][2]), "=&v"(aq[6]) : "a"(wq[0][1]), "v"(xf[0][7]), "v"(vXN), "s"(rsW), "s"(sXt), "a"(wq[0][2]), "v"(vQ), "s"(sq[4]), "a"(wq[0][0]) : "memory"); SB();
  WAITL(9);
  SB();
  asm volatile("v_mfma_f32_16x16x32_bf16 %0, %4, %5, 0\n\tbuffer_load_dwordx4 %1, %6, %7, %8 offen offset:512 sc1\n\tv_mfma_f32_16x16x32_bf16 %2, %9, %5, 0\n\tv_mfma_f32_16x16x32_bf16 %3, %10, %5, 0"
      : "=&v"(ak[8]), "=&a"(wq[4][0]), "=&v"(av[8]), "=&v"(aq[7]) : "a"(wq[0][1]), "v"(xf[0][8]), "v"(vQ), "s"(rsW), "s"(sq[0]), "a"(wq[0][2]), "a"(wq[0][0]) : "memory"); SB();
  WAITVB(14);
  WAITL(3);
  SB();
  asm volatile("v_mfma_f32_16x16x32_bf16 %0, %1, %2, %0\n\ts_waitcnt vmcnt(10)"
      : "+v"(ak[0]) : "a"(wq[1][1]), "v"(xf[1][0]) : "memory"); SB();
  BAR0();
  SB();
  asm volatile("v_mfma_f32_16x16x32_bf16 %0, %11, %12, %0\n\tds_read_b128 %1, %13 offset:18432\n\tv_mfma_f32_16x16x32_bf16 %2, %14, %15, %2\n\ts_add_u32 m0, %16, 49152\n\ts_add_u32 %3, %17, 98560\n\tv_mfma_f32_16x16x32_bf16 %4, %11, %15, %4\n\tds_read_b128 %5, %13 offset:20480\n\tv_mfma_f32_16x16x32_bf16 %6, %18, %15, %6\n\tbuffer_load_dwordx4 %19, %20, %3 offen offset:0 sc1 lds\n\tv_mfma_f32_16x16x32_bf16 %7, %14, %21, %7\n\tds_read_b128 %8, %13 offset:22528\n\tv_mfma_f32_16x16x32_bf16 %9, %11, %21, %9\n\tbuffer_load_dwordx4 %10, %22, %20, %23 offen offset:512 sc1"
      : "+v"(av[0]), "=&v"(xf[2][0]), "+v"(ak[1]), "=&s"(sXt), "+v"(av[1]), "=&v"(xf[2][1]), "+v"(aq[0]), "+v"(ak[2]), "=&v"(xf[2][2]), "+v"(av[2]), "=&a"(wq[4][1]) : "a"(wq[1][2]), "v"(xf[1][0]), "v"(xr0), "a"(wq[1][1]), "v"(xf[1][1]), "s"(ldsX), "s"(sXa), "a"(wq[1][0]), "v"(vXN), "s"(rsW), "v"(xf[1][2]), "v"(vQ), "s"(sq[2]) : "memory", "scc"); SB();
  asm volatile("v_mfma_f32_16x16x32_bf16 %0, %11, %12, %0\n\tds_read_b128 %1, %13 offset:24576\n\tv_mfma_f32_16x16x32_bf16 %2, %14, %15, %2\n\ts_add_u32 m0, %16, 53248\n\ts_add_u32 %3, %17, 131328\n\tv_mfma_f32_16x16x32_bf16 %4, %18, %15, %4\n\tds_read_b128 %5, %13 offset:26624\n\tv_mfma_f32_16x16x32_bf16 %6, %11, %15, %6\n\tbuffer_load_dwordx4 %19, %20, %3 offen offset:0 sc1 lds\n\tv_mfma_f32_16x16x32_bf16 %7, %14, %21, %7\n\tds_read_b128 %8, %13 offset:28672\n\tv_mfma_f32_16x16x32_bf16 %9, %18, %21, %9\n\tbuffer_load_dwordx4 %10, %22, %20, %23 offen offset:512 sc1"
      : "+v"(aq[1]), "=&v"(xf[2][3]), "+v"(ak[3]), "=&s"(sXt), "+v"(av[3]), "=&v"(xf[2][4]), "+v"(aq[2]), "+v"(ak[4]), "=&v"(xf[2][5]), "+v"(av[4]), "=&a"(wq[4][2]) : "a"(wq[1][0]), "v"(xf[1][2]), "v"(xr0), "a"(wq[1][1]), "v"(xf[1][3]), "s"(ldsX), "s"(sXb), "a"(wq[1][2]), "v"(vXN), "s"(rsW), "v"(xf[1][4]), "v"(vQ), "s"(sq[4]) : "memory", "scc"); SB();
  asm volatile("v_mfma_f32_16x16x32_bf16 %0, %7, %8, %0\n\tds_read_b128 %1, %9 offset:30720\n\tv_mfma_f32_16x16x32_bf16 %2, %10, %11, %2\n\ts_add_u32 m0, %12, 57344\n\ts_add_u32 %3, %13, 16768\n\tv_mfma_f32_16x16x32_bf16 %4, %14, %11, %4\n\tds_read_b128 %5, %9 offset:32768\n\tv_mfma_f32_16x16x32_bf16 %6, %7, %11, %6"
      : "+v"(aq[3]), "=&v"(xf[2][6]), "+v"(ak[5]), "=&s"(sXt), "+v"(av[5]), "=&v"(xf[2][7]), "+v"(aq[4]) : "a"(wq[1][0]), "v"(xf[1][4]), "v"(xr0), "a"(wq[1][1]), "v"(xf[1][5]), "s"(ldsX), "s"(sXa), "a"(wq[1][2]) : "memory", "scc"); SB();
  WAITL(8);
  SB();
  asm volatile("v_mfma_f32_16x16x32_bf16 %0, %10, %11, %0\n\tds_read_b128 %1, %12 offset:34816\n\tv_mfma_f32_16x16x32_bf16 %2, %13, %11, %2\n\tbuffer_load_dwordx4 %14, %15, %16 offen offset:0 sc1 lds\n\tv_mfma_f32_16x16x32_bf16 %3, %17, %11, %3\n\tbuffer_load_dwordx4 %4, %18, %15, %19 offen offset:640 sc1\n\tv_mfma_f32_16x16x32_bf16 %5, %10, %20, %5\n\tbuffer_load_dwordx4 %6, %18, %15, %21 offen offset:640 sc1\n\tv_mfma_f32_16x16x32_bf16 %7, %13, %20, %7\n\tv_mfma_f32_16x16x32_bf16 %8, %17, %20, %8\n\tv_mfma_f32_16x16x32_bf16 %9, %10, %22, %9"
      : "+v"(ak[6]), "=&v"(xf[2][8]), "+v"(av[6]), "+v"(aq[5]), "=&a"(wq[5][0]), "+v"(ak[7]), "=&a"(wq[5][1]), "+v"(av[7]), "+v"(aq[6]), "+v"(ak[8]) : "a"(wq[1][1]), "v"(xf[1][6]), "v"(xr0), "a"(wq[1][2]), "v"(vXN), "s"(rsW), "s"(sXt), "a"(wq[1][0]), "v"(vQ), "s"(sq[0]), "v"(xf[1][7]), "s"(sq[2]), "v"(xf[1][8]) : "memory"); SB();
  asm volatile("v_mfma_f32_16x16x32_bf16 %0, %2, %3, %0\n\tv_mfma_f32_16x16x32_bf16 %1, %4, %3, %1"
      : "+v"(av[8]), "+v"(aq[7]) : "a"(wq[1][2]), "v"(xf[1][8]), "a"(wq[1][0])); SB();
  STAMP(30);
  WAITVB(14);
  WAITL(3);
  SB();
  asm volatile("v_mfma_f32_16x16x32_bf16 %0, %11, %12, %0\n\ts_add_u32 m0, %13, 61440\n\ts_add_u32 %1, %14, 49536\n\tv_mfma_f32_16x16x32_bf16 %2, %15, %12, %2\n\tds_read_b128 %3, %16 offset:18432\n\tv_mfma_f32_16x16x32_bf16 %4, %11, %17, %4\n\tbuffer_load_dwordx4 %18, %19, %1 offen offset:0 sc1 lds\n\tv_mfma_f32_16x16x32_bf16 %5, %15, %17, %5\n\tds_read_b128 %6, %16 offset:20480\n\tv_mfma_f32_16x16x32_bf16 %7, %20, %17, %7\n\tbuffer_load_dwordx4 %8, %21, %19, %22 offen offset:640 sc1\n\tv_mfma_f32_16x16x32_bf16 %9, %11, %23, %9\n\tds_read_b128 %10, %16 offset:22528"
      : "+v"(ak[0]), "=&s"(sXt), "+v"(av[0]), "=&v"(xf[3][0]), "+v"(ak[1]), "+v"(av[1]), "=&v"(xf[3][1]), "+v"(aq[0]), "=&a"(wq[5][2]), "+v"(ak[2]), "=&v"(xf[3][2]) : "a"(wq[2][1]), "v"(xf[2][0]), "s"(ldsX), "s"(sXa), "a"(wq[2][2]), "v"(xr1), "v"(xf[2][1]), "v"(vXN), "s"(rsW), "a"(wq[2][0]), "v"(vQ), "s"(sq[4]), "v"(xf[2][2]) : "memory", "scc"); SB();
  asm volatile("v_mfma_f32_16x16x32_bf16 %0, %11, %12, %0\n\ts_add_u32 m0, %13, 65536\n\ts_add_u32 %1, %14, 82304\n\tv_mfma_f32_16x16x32_bf16 %2, %15, %12, %2\n\tds_read_b128 %3, %16 offset:24576\n\tv_mfma_f32_16x16x32_bf16 %4, %17, %18, %4\n\tbuffer_load_dwordx4 %19, %20, %1 offen offset:0 sc1 lds\n\tv_mfma_f32_16x16x32_bf16 %5, %11, %18, %5\n\tds_read_b128 %6, %16 offset:26624\n\tv_mfma_f32_16x16x32_bf16 %7, %15, %18, %7\n\tbuffer_load_dwordx4 %8, %21, %20, %22 offen offset:768 sc1\n\tv_mfma_f32_16x16x32_bf16 %9, %17, %23, %9\n\tds_read_b128 %10, %16 offset:28672"
      : "+v"(av[2]), "=&s"(sXt), "+v"(aq[1]), "=&v"(xf[3][3]), "+v"(ak[3]), "+v"(av[3]), "=&v"(xf[3][4]), "+v"(aq[2]), "=&a"(wq[6][0]), "+v"(ak[4]), "=&v"(xf[3][5]) : "a"(wq[2][2]), "v"(xf[2][2]), "s"(ldsX), "s"(sXa), "a"(wq[2][0]), "v"(xr1), "a"(wq[2][1]), "v"(xf[2][3]), "v"(vXN), "s"(rsW), "v"(vQ), "s"(sq[0]), "v"(xf[2][4]) : "memory", "scc"); SB();
  asm volatile("v_mfma_f32_16x16x32_bf16 %0, %8, %9, %0\n\tbuffer_load_dwordx4 %1, %10, %11, %12 offen offset:768 sc1\n\tv_mfma_f32_16x16x32_bf16 %2, %13, %9, %2\n\tds_read_b128 %3, %14 offset:30720\n\tv_mfma_f32_16x16x32_bf16 %4, %15, %16, %4\n\tv_mfma_f32_16x16x32_bf16 %5, %8, %16, %5\n\tds_read_b128 %6, %14 offset:32768\n\tv_mfma_f32_16x16x32_bf16 %7, %13, %16, %7"
      : "+v"(av[4]), "=&a"(wq[6][1]), "+v"(aq[3]), "=&v"(xf[3][6]), "+v"(ak[5]), "+v"(av[5]), "=&v"(xf[3][7]), "+v"(aq[4]) : "a"(wq[2][2]), "v"(xf[2][4]), "v"(vQ), "s"(rsW), "s"(sq[2]), "a"(wq[2][0]), "v"(xr1), "a"(wq[2][1]), "v"(xf[2][5]) : "memory"); SB();
  WAITL(8);
  SB();
  asm volatile("v_mfma_f32_16x16x32_bf16 %0, %10, %11, %0\n\tds_read_b128 %1, %12 offset:34816\n\tv_mfma_f32_16x16x32_bf16 %2, %13, %11, %2\n\tv_mfma_f32_16x16x32_bf16 %3, %14, %11, %3\n\tv_mfma_f32_16x16x32_bf16 %4, %10, %15, %4\n\tv_mfma_f32_16x16x32_bf16 %5, %13, %15, %5\n\tv_mfma_f32_16x16x32_bf16 %6, %14, %15, %6\n\tv_mfma_f32_16x16x32_bf16 %7, %10, %16, %7\n\tv_mfma_f32_16x16x32_bf16 %8, %13, %16, %8\n\tv_mfma_f32_16x16x32_bf16 %9, %14, %16, %9"
      : "+v"(ak[6]), "=&v"(xf[3][8]), "+v"(av[6]), "+v"(aq[5]), "+v"(ak[7]), "+v"(av[7]), "+v"(aq[6]), "+v"(ak[8]), "+v"(av[8]), "+v"(aq[7]) : "a"(wq[2][1]), "v"(xf[2][6]), "v"(xr1), "a"(wq[2][2]), "a"(wq[2][0]), "v"(xf[2][7]), "v"(xf[2][8]) : "memory"); SB();
  WAITVB(13);
  WAITL(3);
  SB();
  asm volatile("v_mfma_f32_16x16x32_bf16 %0, %1, %2, %0\n\ts_waitcnt vmcnt(9)"
      : "+v"(ak[0]) : "a"(wq[3][1]), "v"(xf[3][0]) : "memory"); SB();
  BAR0();
  SB();
  asm volatile("v_mfma_f32_16x16x32_bf16 %0, %11, %12, %0\n\tds_read_b128 %1, %13 offset:36864\n\tv_mfma_f32_16x16x32_bf16 %2, %14, %15, %2\n\ts_add_u32 m0, %16, 69632\n\ts_add_u32 %3, %17, 115072\n\tv_mfma_f32_16x16x32_bf16 %4, %11, %15, %4\n\tds_read_b128 %5, %13 offset:38912\n\tv_mfma_f32_16x16x32_bf16 %6, %18, %15, %6\n\tbuffer_load_dwordx4 %19, %20, %3 offen offset:0 sc1 lds\n\tv_mfma_f32_16x16x32_bf16 %7, %14, %21, %7\n\tds_read_b128 %8, %13 offset:40960\n\tv_mfma_f32_16x16x32_bf16 %9, %11, %21, %9\n\tbuffer_load_dwordx4 %10, %22, %20, %23 offen offset:768 sc1"
      : "+v"(av[0]), "=&v"(xf[4][0]), "+v"(ak[1]), "=&s"(sXt), "+v"(av[1]), "=&v"(xf[4][1]), "+v"(aq[0]), "+v"(ak[2]), "=&v"(xf[4][2]), "+v"(av[2]), "=&a"(wq[6][2]) : "a"(wq[3][2]), "v"(xf[3][0]), "v"(xr0), "a"(wq[3][1]), "v"(xf[3][1]), "s"(ldsX), "s"(sXa), "a"(wq[3][0]), "v"(vXN), "s"(rsW), "v"(xf[3][2]), "v"(vQ), "s"(sq[4]) : "memory", "scc"); SB();
  asm volatile("v_mfma_f32_16x16x32_bf16 %0, %11, %12, %0\n\tds_read_b128 %1, %13 offset:43008\n\tv_mfma_f32_16x16x32_bf16 %2, %14, %15, %2\n\ts_add_u32 m0, %16, 73728\n\ts_add_u32 %3, %17, 512\n\tv_mfma_f32_16x16x32_bf16 %4, %18, %15, %4\n\tds_read_b128 %5, %13 offset:45056\n\tv_mfma_f32_16x16x32_bf16 %6, %11, %15, %6\n\tbuffer_load_dwordx4 %19, %20, %3 offen offset:0 sc1 lds\n\tv_mfma_f32_16x16x32_bf16 %7, %14, %21, %7\n\tds_read_b128 %8, %13 offset:47104\n\tv_mfma_f32_16x16x32_bf16 %9, %18, %21, %9\n\tbuffer_load_dwordx4 %10, %22, %20, %23 offen offset:0 sc1"
      : "+v"(aq[1]), "=&v"(xf[4][3]), "+v"(ak[3]), "=&s"(sXt), "+v"(av[3]), "=&v"(xf[4][4]), "+v"(aq[2]), "+v"(ak[4]), "=&v"(xf[4][5]), "+v"(av[4]), "=&a"(wq[7][0]) : "a"(wq[3][0]), "v"(xf[3][2]), "v"(xr0), "a"(wq[3][1]), "v"(xf[3][3]), "s"(ldsX), "s"(sXa), "a"(wq[3][2]), "v"(vXN), "s"(rsW), "v"(xf[3][4]), "v"(vQ), "s"(sq[1]) : "memory", "scc"); SB();
  asm volatile("v_mfma_f32_16x16x32_bf16 %0, %7, %8, %0\n\tds_read_b128 %1, %9 offset:49152\n\tv_mfma_f32_16x16x32_bf16 %2, %10, %11, %2\n\ts_add_u32 m0, %12, 77824\n\ts_add_u32 %3, %13, 33280\n\tv_mfma_f32_16x16x32_bf16 %4, %14, %11, %4\n\tds_read_b128 %5, %9 offset:51200\n\tv_mfma_f32_16x16x32_bf16 %6, %7, %11, %6"
      : "+v"(aq[3]), "=&v"(xf[4][6]), "+v"(ak[5]), "=&s"(sXt), "+v"(av[5]), "=&v"(xf[4][7]), "+v"(aq[4]) : "a"(wq[3][0]), "v"(xf[3][4]), "v"(xr0), "a"(wq[3][1]), "v"(xf[3][5]), "s"(ldsX), "s"(sXa), "a"(wq[3][2]) : "memory", "scc"); SB();
  WAITL(8);
  SB();
  asm volatile("v_mfma_f32_16x16x32_bf16 %0, %10, %11, %0\n\tds_read_b128 %1, %12 offset:53248\n\tv_mfma_f32_16x16x32_bf16 %2, %13, %11, %2\n\tbuffer_load_dwordx4 %14, %15, %16 offen offset:0 sc1 lds\n\tv_mfma_f32_16x16x32_bf16 %3, %17, %11, %3\n\tbuffer_load_dwordx4 %4, %18, %15, %19 offen offset:0 sc1\n\tv_mfma_f32_16x16x32_bf16 %5, %10, %20, %5\n\tbuffer_load_dwordx4 %6, %18, %15, %21 offen offset:0 sc1\n\tv_mfma_f32_16x16x32_bf16 %7, %13, %20, %7\n\tv_mfma_f32_16x16x32_bf16 %8, %17, %20, %8\n\tv_mfma_f32_16x16x32_bf16 %9, %10, %22, %9"
      : "+v"(ak[6]), "=&v"(xf[4][8]), "+v"(av[6]), "+v"(aq[5]), "=&a"(wq[7][1]), "+v"(ak[7]), "=&a"(wq[7][2]), "+v"(av[7]), "+v"(aq[6]), "+v"(ak[8]) : "a"(wq[3][1]), "v"(xf[3][6]), "v"(xr0), "a"(wq[3][2]), "v"(vXN), "s"(rsW), "s"(sXt), "a"(wq[3][0]), "v"(vQ), "s"(sq[3]), "v"(xf[3][7]), "s"(sq[5]), "v"(xf[3][8]) : "memory"); SB();
  asm volatile("v_mfma_f32_16x16x32_bf16 %0, %2, %3, %0\n\tv_mfma_f32_16x16x32_bf16 %1, %4, %3, %1"
      : "+v"(av[8]), "+v"(aq[7]) : "a"(wq[3][2]), "v"(xf[3][8]), "a"(wq[3][0])); SB();
  WAITVB(15);
  WAITL(3);
  SB();
  asm volatile("v_mfma_f32_16x16x32_bf16 %0, %11, %12, %0\n\ts_add_u32 m0, %13, 81920\n\ts_add_u32 %1, %14, 66048\n\tv_mfma_f32_16x16x32_bf16 %2, %15, %12, %2\n\tds_read_b128 %3, %16 offset:36864\n\tv_mfma_f32_16x16x32_bf16 %4, %11, %17, %4\n\tbuffer_load_dwordx4 %18, %19, %1 offen offset:0 sc1 lds\n\tv_mfma_f32_16x16x32_bf16 %5, %15, %17, %5\n\tds_read_b128 %6, %16 offset:38912\n\tv_mfma_f32_16x16x32_bf16 %7, %20, %17, %7\n\tbuffer_load_dwordx4 %8, %21, %19, %22 offen offset:128 sc1\n\tv_mfma_f32_16x16x32_bf16 %9, %11, %23, %9\n\tds_read_b128 %10, %16 offset:40960"
      : "+v"(ak[0]), "=&s"(sXt), "+v"(av[0]), "=&v"(xf[5][0]), "+v"(ak[1]), "+v"(av[1]), "=&v"(xf[5][1]), "+v"(aq[0]), "=&a"(wq[8][0]), "+v"(ak[2]), "=&v"(xf[5][2]) : "a"(wq[4][1]), "v"(xf[4][0]), "s"(ldsX), "s"(sXa), "a"(wq[4][2]), "v"(xr1), "v"(xf[4][1]), "v"(vXN), "s"(rsW), "a"(wq[4][0]), "v"(vQ), "s"(sq[1]), "v"(xf[4][2]) : "memory", "scc"); SB();
  asm volatile("v_mfma_f32_16x16x32_bf16 %0, %11, %12, %0\n\ts_add_u32 m0, %13, 86016\n\ts_add_u32 %1, %14, 98816\n\tv_mfma_f32_16x16x32_bf16 %2, %15, %12, %2\n\tds_read_b128 %3, %16 offset:43008\n\tv_mfma_f32_16x16x32_bf16 %4, %17, %18, %4\n\tbuffer_load_dwordx4 %19, %20, %1 offen offset:0 sc1 lds\n\tv_mfma_f32_16x16x32_bf16 %5, %11, %18, %5\n\tds_read_b128 %6, %16 offset:45056\n\tv_mfma_f32_16x16x32_bf16 %7, %15, %18, %7\n\tbuffer_load_dwordx4 %8, %21, %20, %22 offen offset:128 sc1\n\tv_mfma_f32_16x16x32_bf16 %9, %17, %23, %9\n\tds_read_b128 %10, %16 offset:47104"
      : "+v"(av[2]), "=&s"(sXt), "+v"(aq[1]), "=&v"(xf[5][3]), "+v"(ak[3]), "+v"(av[3]), "=&v"(xf[5][4]), "+v"(aq[2]), "=&a"(wq[8][1]), "+v"(ak[4]), "=&v"(xf[5][5]) : "a"(wq[4][2]), "v"(xf[4][2]), "s"(ldsX), "s"(sXa), "a"(wq[4][0]), "v"(xr1), "a"(wq[4][1]), "v"(xf[4][3]), "v"(vXN), "s"(rsW), "v"(vQ), "s"(sq[3]), "v"(xf[4][4]) : "memory", "scc"); SB();
  asm volatile("v_mfma_f32_16x16x32_bf16 %0, %9, %10, %0\n\tbuffer_load_dwordx4 %1, %11, %12, %13 offen offset:128 sc1\n\tv_mfma_f32_16x16x32_bf16 %2, %14, %10, %2\n\tds_read_b128 %3, %15 offset:49152\n\tv_mfma_f32_16x16x32_bf16 %4, %16, %17, %4\n\tbuffer_load_dwordx4 %5, %11, %12, %18 offen offset:256 sc1\n\tv_mfma_f32_16x16x32_bf16 %6, %9, %17, %6\n\tds_read_b128 %7, %15 offset:51200\n\tv_mfma_f32_16x16x32_bf16 %8, %14, %17, %8"
      : "+v"(av[4]), "=&a"(wq[8][2]), "+v"(aq[3]), "=&v"(xf[5][6]), "+v"(ak[5]), "=&a"(wq[9][0]), "+v"(av[5]), "=&v"(xf[5][7]), "+v"(aq[4]) : "a"(wq[4][2]), "v"(xf[4][4]), "v"(vQ), "s"(rsW), "s"(sq[5]), "a"(wq[4][0]), "v"(xr1), "a"(wq[4][1]), "v"(xf[4][5]), "s"(sq[1]) : "memory"); SB();
  WAITL(8);
  SB();
  asm volatile("v_mfma_f32_16x16x32_bf16 %0, %10, %11, %0\n\tds_read_b128 %1, %12 offset:53248\n\tv_mfma_f32_16x16x32_bf16 %2, %13, %11, %2\n\tv_mfma_f32_16x16x32_bf16 %3, %14, %11, %3\n\tv_mfma_f32_16x16x32_bf16 %4, %10, %15, %4\n\tv_mfma_f32_16x16x32_bf16 %5, %13, %15, %5\n\tv_mfma_f32_16x16x32_bf16 %6, %14, %15, %6\n\tv_mfma_f32_16x16x32_bf16 %7, %10, %16, %7\n\tv_mfma_f32_16x16x32_bf16 %8, %13, %16, %8\n\tv_mfma_f32_16x16x32_bf16 %9, %14, %16, %9"
      : "+v"(ak[6]), "=&v"(xf[5][8]), "+v"(av[6]), "+v"(aq[5]), "+v"(ak[7]), "+v"(av[7]), "+v"(aq[6]), "+v"(ak[8]), "+v"(av[8]), "+v"(aq[7]) : "a"(wq[4][1]), "v"(xf[4][6]), "v"(xr1), "a"(wq[4][2]), "a"(wq[4][0]), "v"(xf[4][7]), "v"(xf[4][8]) : "memory"); SB();
  WAITVB(16);
  WAITL(3);
  SB();
  asm volatile("v_mfma_f32_16x16x32_bf16 %0, %3, %4, %0\n\tv_add_u32 %1, 55296, %1\n\tv_mfma_f32_16x16x32_bf16 %2, %5, %4, %2\n\ts_waitcnt vmcnt(12)"
      : "+v"(ak[0]), "+v"(xr0), "+v"(av[0]) : "a"(wq[5][1]), "v"(xf[5][0]), "a"(wq[5][2]) : "memory"); SB();
  BAR0();
  SB();
  asm volatile("v_mfma_f32_16x16x32_bf16 %0, %12, %13, %0\n\tds_read_b128 %1, %14 offset:0\n\tv_mfma_f32_16x16x32_bf16 %2, %15, %13, %2\n\tds_read_b128 %3, %14 offset:2048\n\tv_mfma_f32_16x16x32_bf16 %4, %16, %13, %4\n\ts_add_u32 m0, %17, 90112\n\ts_add_u32 %5, %18, 131584\n\tv_mfma_f32_16x16x32_bf16 %6, %12, %19, %6\n\tds_read_b128 %7, %14 offset:4096\n\tv_mfma_f32_16x16x32_bf16 %8, %15, %19, %8\n\tbuffer_load_dwordx4 %20, %21, %5 offen offset:0 sc1 lds\n\tv_mfma_f32_16x16x32_bf16 %9, %16, %19, %9\n\tds_read_b128 %10, %14 offset:6144\n\tv_mfma_f32_16x16x32_bf16 %11, %12, %22, %11"
      : "+v"(ak[1]), "=&v"(xf[6][0]), "+v"(av[1]), "=&v"(xf[6][1]), "+v"(aq[0]), "=&s"(sXt), "+v"(ak[2]), "=&v"(xf[6][2]), "+v"(av[2]), "+v"(aq[1]), "=&v"(xf[6][3]), "+v"(ak[3]) : "a"(wq[5][1]), "v"(xf[5][1]), "v"(xr0), "a"(wq[5][2]), "a"(wq[5][0]), "s"(ldsX), "s"(sXb), "v"(xf[5][2]), "v"(vXN), "s"(rsW), "v"(xf[5][3]) : "memory", "scc"); SB();
  asm volatile("buffer_load_dwordx4 %0, %11, %12, %13 offen offset:256 sc1\n\tv_mfma_f32_16x16x32_bf16 %1, %14, %15, %1\n\tds_read_b128 %2, %16 offset:8192\n\tv_mfma_f32_16x16x32_bf16 %3, %17, %15, %3\n\ts_add_u32 m0, %18, 94208\n\ts_add_u32 %4, %19, 17024\n\tv_mfma_f32_16x16x32_bf16 %5, %20, %21, %5\n\tds_read_b128 %6, %16 offset:10240\n\tv_mfma_f32_16x16x32_bf16 %7, %14, %21, %7\n\tbuffer_load_dwordx4 %22, %12, %4 offen offset:0 sc1 lds\n\tv_mfma_f32_16x16x32_bf16 %8, %17, %21, %8\n\tds_read_b128 %9, %16 offset:12288\n\tv_mfma_f32_16x16x32_bf16 %10, %20, %23, %10"
      : "=&a"(wq[9][1]), "+v"(av[3]), "=&v"(xf[6][4]), "+v"(aq[2]), "=&s"(sXt), "+v"(ak[4]), "=&v"(xf[6][5]), "+v"(av[4]), "+v"(aq[3]), "=&v"(xf[6][6]), "+v"(ak[5]) : "v"(vQ), "s"(rsW), "s"(sq[3]), "a"(wq[5][2]), "v"(xf[5][3]), "v"(xr0), "a"(wq[5][0]), "s"(ldsX), "s"(sXa), "a"(wq[5][1]), "v"(xf[5][4]), "v"(vXN), "v"(xf[5][5]) : "memory", "scc"); SB();
  asm volatile("buffer_load_dwordx4 %0, %4, %5, %6 offen offset:256 sc1\n\tv_mfma_f32_16x16x32_bf16 %1, %7, %8, %1\n\tds_read_b128 %2, %9 offset:14336\n\tv_mfma_f32_16x16x32_bf16 %3, %10, %8, %3"
      : "=&a"(wq[9][2]), "+v"(av[5]), "=&v"(xf[6][7]), "+v"(aq[4]) : "v"(vQ), "s"(rsW), "s"(sq[5]), "a"(wq[5][2]), "v"(xf[5][5]), "v"(xr0), "a"(wq[5][0]) : "memory"); SB();
  WAITL(8);
  SB();
  asm volatile("v_mfma_f32_16x16x32_bf16 %0, %10, %11, %0\n\tds_read_b128 %1, %12 offset:16384\n\tv_mfma_f32_16x16x32_bf16 %2, %13, %11, %2\n\ts_add_u32 m0, %14, 98304\n\ts_add_u32 %3, %15, 49792\n\tv_mfma_f32_16x16x32_bf16 %4, %16, %11, %4\n\tbuffer_load_dwordx4 %17, %18, %3 offen offset:0 sc1 lds\n\tv_mfma_f32_16x16x32_bf16 %5, %10, %19, %5\n\tbuffer_load_dwordx4 %6, %20, %18, %21 offen offset:384 sc1\n\tv_mfma_f32_16x16x32_bf16 %7, %13, %19, %7\n\tv_mfma_f32_16x16x32_bf16 %8, %16, %19, %8\n\tv_mfma_f32_16x16x32_bf16 %9, %10, %22, %9"
      : "+v"(ak[6]), "=&v"(xf[6][8]), "+v"(av[6]), "=&s"(sXt), "+v"(aq[5]), "+v"(ak[7]), "=&a"(wq[10][0]), "+v"(av[7]), "+v"(aq[6]), "+v"(ak[8]) : "a"(wq[5][1]), "v"(xf[5][6]), "v"(xr0), "a"(wq[5][2]), "s"(ldsX), "s"(sXa), "a"(wq[5][0]), "v"(vXN), "s"(rsW), "v"(xf[5][7]), "v"(vQ), "s"(sq[1]), "v"(xf[5][8]) : "memory", "scc"); SB();
  asm volatile("v_mfma_f32_16x16x32_bf16 %0, %2, %3, %0\n\tv_mfma_f32_16x16x32_bf16 %1, %4, %3, %1"
      : "+v"(av[8]), "+v"(aq[7]) : "a"(wq[5][2]), "v"(xf[5][8]), "a"(wq[5][0])); SB();
  STAMP(31);
  WAITVB(17);
  WAITL(3);
  SB();
  asm volatile("v_mfma_f32_16x16x32_bf16 %0, %12, %13, %0\n\tv_add_u32 %1, 55296, %1\n\tv_mfma_f32_16x16x32_bf16 %2, %14, %13, %2\n\tds_read_b128 %3, %1 offset:0\n\tv_mfma_f32_16x16x32_bf16 %4, %12, %15, %4\n\ts_add_u32 m0, %16, 102400\n\ts_add_u32 %5, %17, 82560\n\tv_mfma_f32_16x16x32_bf16 %6, %14, %15, %6\n\tds_read_b128 %7, %1 offset:2048\n\tv_mfma_f32_16x16x32_bf16 %8, %18, %15, %8\n\tbuffer_load_dwordx4 %19, %20, %5 offen offset:0 sc1 lds\n\tv_mfma_f32_16x16x32_bf16 %9, %12, %21, %9\n\tds_read_b128 %10, %1 offset:4096\n\tv_mfma_f32_16x16x32_bf16 %11, %14, %21, %11"
      : "+v"(ak[0]), "+v"(xr1), "+v"(av[0]), "=&v"(xf[7][0]), "+v"(ak[1]), "=&s"(sXt), "+v"(av[1]), "=&v"(xf[7][1]), "+v"(aq[0]), "+v"(ak[2]), "=&v"(xf[7][2]), "+v"(av[2]) : "a"(wq[6][1]), "v"(xf[6][0]), "a"(wq[6][2]), "v"(xf[6][1]), "s"(ldsX), "s"(sXa), "a"(wq[6][0]), "v"(vXN), "s"(rsW), "v"(xf[6][2]) : "memory", "scc"); SB();
  asm volatile("buffer_load_dwordx4 %0, %11, %12, %13 offen offset:384 sc1\n\tv_mfma_f32_16x16x32_bf16 %1, %14, %15, %1\n\tds_read_b128 %2, %16 offset:6144\n\tv_mfma_f32_16x16x32_bf16 %3, %17, %18, %3\n\ts_add_u32 m0, %19, 106496\n\ts_add_u32 %4, %20, 115328\n\tv_mfma_f32_16x16x32_bf16 %5, %21, %18, %5\n\tds_read_b128 %6, %16 offset:8192\n\tv_mfma_f32_16x16x32_bf16 %7, %14, %18, %7\n\tbuffer_load_dwordx4 %22, %12, %4 offen offset:0 sc1 lds\n\tv_mfma_f32_16x16x32_bf16 %8, %17, %23, %8\n\tds_read_b128 %9, %16 offset:10240\n\tv_mfma_f32_16x16x32_bf16 %10, %21, %23, %10"
      : "=&a"(wq[10][1]), "+v"(aq[1]), "=&v"(xf[7][3]), "+v"(ak[3]), "=&s"(sXt), "+v"(av[3]), "=&v"(xf[7][4]), "+v"(aq[2]), "+v"(ak[4]), "=&v"(xf[7][5]), "+v"(av[4]) : "v"(vQ), "s"(rsW), "s"(sq[3]), "a"(wq[6][0]), "v"(xf[6][2]), "v"(xr1), "a"(wq[6][1]), "v"(xf[6][3]), "s"(ldsX), "s"(sXa), "a"(wq[6][2]), "v"(vXN), "v"(xf[6][4]) : "memory", "scc"); SB();
  asm volatile("buffer_load_dwordx4 %0, %8, %9, %10 offen offset:384 sc1\n\tv_mfma_f32_16x16x32_bf16 %1, %11, %12, %1\n\tds_read_b128 %2, %13 offset:12288\n\tv_mfma_f32_16x16x32_bf16 %3, %14, %15, %3\n\tbuffer_load_dwordx4 %4, %8, %9, %16 offen offset:512 sc1\n\tv_mfma_f32_16x16x32_bf16 %5, %17, %15, %5\n\tds_read_b128 %6, %13 offset:14336\n\tv_mfma_f32_16x16x32_bf16 %7, %11, %15, %7"
      : "=&a"(wq[10][2]), "+v"(aq[3]), "=&v"(xf[7][6]), "+v"(ak[5]), "=&a"(wq[11][0]), "+v"(av[5]), "=&v"(xf[7][7]), "+v"(aq[4]) : "v"(vQ), "s"(rsW), "s"(sq[5]), "a"(wq[6][0]), "v"(xf[6][4]), "v"(xr1), "a"(wq[6][1]), "v"(xf[6][5]), "s"(sq[1]), "a"(wq[6][2]) : "memory"); SB();
  WAITL(8);
  SB();
  asm volatile("v_mfma_f32_16x16x32_bf16 %0, %11, %12, %0\n\tds_read_b128 %1, %13 offset:16384\n\tv_mfma_f32_16x16x32_bf16 %2, %14, %12, %2\n\tbuffer_load_dwordx4 %3, %15, %16, %17 offen offset:512 sc1\n\tv_mfma_f32_16x16x32_bf16 %4, %18, %12, %4\n\tv_mfma_f32_16x16x32_bf16 %5, %11, %19, %5\n\tv_mfma_f32_16x16x32_bf16 %6, %14, %19, %6\n\tv_mfma_f32_16x16x32_bf16 %7, %18, %19, %7\n\tv_mfma_f32_16x16x32_bf16 %8, %11, %20, %8\n\tv_mfma_f32_16x16x32_bf16 %9, %14, %20, %9\n\tv_mfma_f32_16x16x32_bf16 %10, %18, %20, %10"
      : "+v"(ak[6]), "=&v"(xf[7][8]), "+v"(av[6]), "=&a"(wq[11][1]), "+v"(aq[5]), "+v"(ak[7]), "+v"(av[7]), "+v"(aq[6]), "+v"(ak[8]), "+v"(av[8]), "+v"(aq[7]) : "a"(wq[6][1]), "v"(xf[6][6]), "v"(xr1), "a"(wq[6][2]), "v"(vQ), "s"(rsW), "s"(sq[3]), "a"(wq[6][0]), "v"(xf[6][7]), "v"(xf[6][8]) : "memory"); SB();
  WAITVB(18);
  WAITL(3);
  SB();
  asm volatile("v_mfma_f32_16x16x32_bf16 %0, %1, %2, %0\n\ts_waitcnt vmcnt(11)"
      : "+v"(ak[0]) : "a"(wq[7][1]), "v"(xf[7][0]) : "memory"); SB();
  BAR0();
  SB();
  asm volatile("v_mfma_f32_16x16x32_bf16 %0, %11, %12, %0\n\tds_read_b128 %1, %13 offset:18432\n\tv_mfma_f32_16x16x32_bf16 %2, %14, %15, %2\n\ts_add_u32 m0, %16, 110592\n\ts_add_u32 %3, %17, 768\n\tv_mfma_f32_16x16x32_bf16 %4, %11, %15, %4\n\tds_read_b128 %5, %13 offset:20480\n\tv_mfma_f32_16x16x32_bf16 %6, %18, %15, %6\n\tbuffer_load_dwordx4 %19, %20, %3 offen offset:0 sc1 lds\n\tv_mfma_f32_16x16x32_bf16 %7, %14, %21, %7\n\tds_read_b128 %8, %13 offset:22528\n\tv_mfma_f32_16x16x32_bf16 %9, %11, %21, %9\n\tbuffer_load_dwordx4 %10, %22, %20, %23 offen offset:512 sc1"
      : "+v"(av[0]), "=&v"(xf[8][0]), "+v"(ak[1]), "=&s"(sXt), "+v"(av[1]), "=&v"(xf[8][1]), "+v"(aq[0]), "+v"(ak[2]), "=&v"(xf[8][2]), "+v"(av[2]), "=&a"(wq[11][2]) : "a"(wq[7][2]), "v"(xf[7][0]), "v"(xr0), "a"(wq[7][1]), "v"(xf[7][1]), "s"(ldsX), "s"(sXa), "a"(wq[7][0]), "v"(vXN), "s"(rsW), "v"(xf[7][2]), "v"(vQ), "s"(sq[5]) : "memory", "scc"); SB();
  asm volatile("v_mfma_f32_16x16x32_bf16 %0, %11, %12, %0\n\tds_read_b128 %1, %13 offset:24576\n\tv_mfma_f32_16x16x32_bf16 %2, %14, %15, %2\n\ts_add_u32 m0, %16, 114688\n\ts_add_u32 %3, %17, 33536\n\tv_mfma_f32_16x16x32_bf16 %4, %18, %15, %4\n\tds_read_b128 %5, %13 offset:26624\n\tv_mfma_f32_16x16x32_bf16 %6, %11, %15, %6\n\tbuffer_load_dwordx4 %19, %20, %3 offen offset:0 sc1 lds\n\tv_mfma_f32_16x16x32_bf16 %7, %14, %21, %7\n\tds_read_b128 %8, %13 offset:28672\n\tv_mfma_f32_16x16x32_bf16 %9, %18, %21, %9\n\tbuffer_load_dwordx4 %10, %22, %20, %23 offen offset:640 sc1"
      : "+v"(aq[1]), "=&v"(xf[8][3]), "+v"(ak[3]), "=&s"(sXt), "+v"(av[3]), "=&v"(xf[8][4]), "+v"(aq[2]), "+v"(ak[4]), "=&v"(xf[8][5]), "+v"(av[4]), "=&a"(wq[12][0]) : "a"(wq[7][0]), "v"(xf[7][2]), "v"(xr0), "a"(wq[7][1]), "v"(xf[7][3]), "s"(ldsX), "s"(sXa), "a"(wq[7][2]), "v"(vXN), "s"(rsW), "v"(xf[7][4]), "v"(vQ), "s"(sq[1]) : "memory", "scc"); SB();
  asm volatile("v_mfma_f32_16x16x32_bf16 %0, %7, %8, %0\n\tds_read_b128 %1, %9 offset:30720\n\tv_mfma_f32_16x16x32_bf16 %2, %10, %11, %2\n\ts_add_u32 m0, %12, 118784\n\ts_add_u32 %3, %13, 66304\n\tv_mfma_f32_16x16x32_bf16 %4, %14, %11, %4\n\tds_read_b128 %5, %9 offset:32768\n\tv_mfma_f32_16x16x32_bf16 %6, %7, %11, %6"
      : "+v"(aq[3]), "=&v"(xf[8][6]), "+v"(ak[5]), "=&s"(sXt), "+v"(av[5]), "=&v"(xf[8][7]), "+v"(aq[4]) : "a"(wq[7][0]), "v"(xf[7][4]), "v"(xr0), "a"(wq[7][1]), "v"(xf[7][5]), "s"(ldsX), "s"(sXa), "a"(wq[7][2]) : "memory", "scc"); SB();
  WAITL(8);
  SB();
  asm volatile("v_mfma_f32_16x16x32_bf16 %0, %10, %11, %0\n\tds_read_b128 %1, %12 offset:34816\n\tv_mfma_f32_16x16x32_bf16 %2, %13, %11, %2\n\tbuffer_load_dwordx4 %14, %15, %16 offen offset:0 sc1 lds\n\tv_mfma_f32_16x16x32_bf16 %3, %17, %11, %3\n\tbuffer_load_dwordx4 %4, %18, %15, %19 offen offset:640 sc1\n\tv_mfma_f32_16x16x32_bf16 %5, %10, %20, %5\n\tbuffer_load_dwordx4 %6, %18, %15, %21 offen offset:640 sc1\n\tv_mfma_f32_16x16x32_bf16 %7, %13, %20, %7\n\tbuffer_load_dword %8, %22, %15, 0 offen sc1\n\tv_mfma_f32_16x16x32_bf16 %9, %17, %20, %9"
      : "+v"(ak[6]), "=&v"(xf[8][8]), "+v"(av[6]), "+v"(aq[5]), "=&a"(wq[12][1]), "+v"(ak[7]), "=&a"(wq[12][2]), "+v"(av[7]), "=&v"(fq), "+v"(aq[6]) : "a"(wq[7][1]), "v"(xf[7][6]), "v"(xr0), "a"(wq[7][2]), "v"(vXN), "s"(rsW), "s"(sXt), "a"(wq[7][0]), "v"(vQ), "s"(sq[3]), "v"(xf[7][7]), "s"(sq[5]), "v"(vFQ) : "memory"); SB();
  asm volatile("v_mfma_f32_16x16x32_bf16 %0, %3, %4, %0\n\tv_mfma_f32_16x16x32_bf16 %1, %5, %4, %1\n\tv_mfma_f32_16x16x32_bf16 %2, %6, %4, %2"
      : "+v"(ak[8]), "+v"(av[8]), "+v"(aq[7]) : "a"(wq[7][1]), "v"(xf[7][8]), "a"(wq[7][2]), "a"(wq[7][0])); SB();
  WAITVB(21);
  WAITL(3);
  SB();
  asm volatile("v_mfma_f32_16x16x32_bf16 %0, %11, %12, %0\n\ts_add_u32 m0, %13, 122880\n\ts_add_u32 %1, %14, 99072\n\tv_mfma_f32_16x16x32_bf16 %2, %15, %12, %2\n\tds_read_b128 %3, %16 offset:18432\n\tv_mfma_f32_16x16x32_bf16 %4, %11, %17, %4\n\tbuffer_load_dwordx4 %18, %19, %1 offen offset:0 sc1 lds\n\tv_mfma_f32_16x16x32_bf16 %5, %15, %17, %5\n\tds_read_b128 %6, %16 offset:20480\n\tv_mfma_f32_16x16x32_bf16 %7, %20, %17, %7\n\tbuffer_load_dwordx4 %8, %21, %19, %22 offen offset:768 sc1\n\tv_mfma_f32_16x16x32_bf16 %9, %11, %23, %9\n\tds_read_b128 %10, %16 offset:22528"
      : "+v"(ak[0]), "=&s"(sXt), "+v"(av[0]), "=&v"(xf[9][0]), "+v"(ak[1]), "+v"(av[1]), "=&v"(xf[9][1]), "+v"(aq[0]), "=&a"(wq[13][0]), "+v"(ak[2]), "=&v"(xf[9][2]) : "a"(wq[8][1]), "v"(xf[8][0]), "s"(ldsX), "s"(sXa), "a"(wq[8][2]), "v"(xr1), "v"(xf[8][1]), "v"(vXN), "s"(rsW), "a"(wq[8][0]), "v"(vQ), "s"(sq[1]), "v"(xf[8][2]) : "memory", "scc"); SB();
  asm volatile("v_mfma_f32_16x16x32_bf16 %0, %11, %12, %0\n\ts_add_u32 m0, %13, 126976\n\ts_add_u32 %1, %14, 131840\n\tv_mfma_f32_16x16x32_bf16 %2, %15, %12, %2\n\tds_read_b128 %3, %16 offset:24576\n\tv_mfma_f32_16x16x32_bf16 %4, %17, %18, %4\n\tbuffer_load_dwordx4 %19, %20, %1 offen offset:0 sc1 lds\n\tv_mfma_f32_16x16x32_bf16 %5, %11, %18, %5\n\tds_read_b128 %6, %16 offset:26624\n\tv_mfma_f32_16x16x32_bf16 %7, %15, %18, %7\n\tbuffer_load_dwordx4 %8, %21, %20, %22 offen offset:768 sc1\n\tv_mfma_f32_16x16x32_bf16 %9, %17, %23, %9\n\tds_read_b128 %10, %16 offset:28672"
      : "+v"(av[2]), "=&s"(sXt), "+v"(aq[1]), "=&v"(xf[9][3]), "+v"(ak[3]), "+v"(av[3]), "=&v"(xf[9][4]), "+v"(aq[2]), "=&a"(wq[13][1]), "+v"(ak[4]), "=&v"(xf[9][5]) : "a"(wq[8][2]), "v"(xf[8][2]), "s"(ldsX), "s"(sXc), "a"(wq[8][0]), "v"(xr1), "a"(wq[8][1]), "v"(xf[8][3]), "v"(vXN), "s"(rsW), "v"(vQ), "s"(sq[3]), "v"(xf[8][4]) : "memory", "scc"); SB();
  asm volatile("v_mfma_f32_16x16x32_bf16 %0, %8, %9, %0\n\tbuffer_load_dwordx4 %1, %10, %11, %12 offen offset:768 sc1\n\tv_mfma_f32_16x16x32_bf16 %2, %13, %9, %2\n\tds_read_b128 %3, %14 offset:30720\n\tv_mfma_f32_16x16x32_bf16 %4, %15, %16, %4\n\tv_mfma_f32_16x16x32_bf16 %5, %8, %16, %5\n\tds_read_b128 %6, %14 offset:32768\n\tv_mfma_f32_16x16x32_bf16 %7, %13, %16, %7"
      : "+v"(av[4]), "=&a"(wq[13][2]), "+v"(aq[3]), "=&v"(xf[9][6]), "+v"(ak[5]), "+v"(av[5]), "=&v"(xf[9][7]), "+v"(aq[4]) : "a"(wq[8][2]), "v"(xf[8][4]), "v"(vQ), "s"(rsW), "s"(sq[5]), "a"(wq[8][0]), "v"(xr1), "a"(wq[8][1]), "v"(xf[8][5]) : "memory"); SB();
  WAITL(8);
  SB();
  asm volatile("v_mfma_f32_16x16x32_bf16 %0, %10, %11, %0\n\tds_read_b128 %1, %12 offset:34816\n\tv_mfma_f32_16x16x32_bf16 %2, %13, %11, %2\n\tv_mfma_f32_16x16x32_bf16 %3, %14, %11, %3\n\tv_mfma_f32_16x16x32_bf16 %4, %10, %15, %4\n\tv_mfma_f32_16x16x32_bf16 %5, %13, %15, %5\n\tv_mfma_f32_16x16x32_bf16 %6, %14, %15, %6\n\tv_mfma_f32_16x16x32_bf16 %7, %10, %16, %7\n\tv_mfma_f32_16x16x32_bf16 %8, %13, %16, %8\n\tv_mfma_f32_16x16x32_bf16 %9, %14, %16, %9"
      : "+v"(ak[6]), "=&v"(xf[9][8]), "+v"(av[6]), "+v"(aq[5]), "+v"(ak[7]), "+v"(av[7]), "+v"(aq[6]), "+v"(ak[8]), "+v"(av[8]), "+v"(aq[7]) : "a"(wq[8][1]), "v"(xf[8][6]), "v"(xr1), "a"(wq[8][2]), "a"(wq[8][0]), "v"(xf[8][7]), "v"(xf[8][8]) : "memory"); SB();
  WAITVB(21);
  WAITL(3);
  SB();
  asm volatile("v_mfma_f32_16x16x32_bf16 %0, %3, %4, %0\n\ts_add_u32 m0, %5, 18432\n\tv_add_u32 %1, 36864, %1\n\tv_mfma_f32_16x16x32_bf16 %2, %6, %4, %2\n\ts_waitcnt vmcnt(16)"
      : "+v"(ak[0]), "+v"(xr0), "+v"(av[0]) : "a"(wq[9][1]), "v"(xf[9][0]), "s"(ldsS), "a"(wq[9][2]) : "memory", "scc"); SB();
  BAR0();
  SB();
  asm volatile("v_mfma_f32_16x16x32_bf16 %0, %11, %12, %0\n\tds_read_b128 %1, %13 offset:0\n\tv_mfma_f32_16x16x32_bf16 %2, %14, %12, %2\n\tds_read_b128 %3, %13 offset:2048\n\tv_mfma_f32_16x16x32_bf16 %4, %15, %12, %4\n\tbuffer_load_dwordx4 %16, %17, %18 offen offset:0  lds\n\tv_mfma_f32_16x16x32_bf16 %5, %11, %19, %5\n\tds_read_b128 %6, %13 offset:4096\n\tv_mfma_f32_16x16x32_bf16 %7, %14, %19, %7\n\tbuffer_load_dwordx4 %20, %17, %18 offen offset:1024  lds\n\tv_mfma_f32_16x16x32_bf16 %8, %15, %19, %8\n\tds_read_b128 %9, %13 offset:6144\n\tv_mfma_f32_16x16x32_bf16 %10, %11, %21, %10\n\tbuffer_load_dwordx4 %22, %17, %18 offen offset:2048  lds"
      : "+v"(ak[1]), "=&v"(xf[10][0]), "+v"(av[1]), "=&v"(xf[10][1]), "+v"(aq[0]), "+v"(ak[2]), "=&v"(xf[10][2]), "+v"(av[2]), "+v"(aq[1]), "=&v"(xf[10][3]), "+v"(ak[3]) : "a"(wq[9][1]), "v"(xf[9][1]), "v"(xr0), "a"(wq[9][2]), "a"(wq[9][0]), "v"(vW2[0]), "s"(rs2), "s"(sw[0]), "v"(xf[9][2]), "v"(vW2[1]), "v"(xf[9][3]), "v"(vW2[2]) : "memory"); SB();
  asm volatile("v_mfma_f32_16x16x32_bf16 %0, %9, %10, %0\n\tds_read_b128 %1, %11 offset:8192\n\tv_mfma_f32_16x16x32_bf16 %2, %12, %10, %2\n\tbuffer_load_dwordx4 %13, %14, %15 offen offset:3072  lds\n\tv_mfma_f32_16x16x32_bf16 %3, %16, %17, %3\n\tds_read_b128 %4, %11 offset:10240\n\ts_add_u32 m0, %18, 34816\n\tv_mfma_f32_16x16x32_bf16 %5, %9, %17, %5\n\tbuffer_load_dwordx4 %19, %14, %20 offen offset:0  lds\n\tv_mfma_f32_16x16x32_bf16 %6, %12, %17, %6\n\tds_read_b128 %7, %11 offset:12288\n\tv_mfma_f32_16x16x32_bf16 %8, %16, %21, %8\n\tbuffer_load_dwordx4 %22, %14, %20 offen offset:1024  lds"
      : "+v"(av[3]), "=&v"(xf[10][4]), "+v"(aq[2]), "+v"(ak[4]), "=&v"(xf[10][5]), "+v"(av[4]), "+v"(aq[3]), "=&v"(xf[10][6]), "+v"(ak[5]) : "a"(wq[9][2]), "v"(xf[9][3]), "v"(xr0), "a"(wq[9][0]), "v"(vW2[3]), "s"(rs2), "s"(sw[0]), "a"(wq[9][1]), "v"(xf[9][4]), "s"(ldsS), "v"(vW2[0]), "s"(sw[1]), "v"(xf[9][5]), "v"(vW2[1]) : "memory", "scc"); SB();
  asm volatile("v_mfma_f32_16x16x32_bf16 %0, %3, %4, %0\n\tds_read_b128 %1, %5 offset:14336\n\tv_mfma_f32_16x16x32_bf16 %2, %6, %4, %2"
      : "+v"(av[5]), "=&v"(xf[10][7]), "+v"(aq[4]) : "a"(wq[9][2]), "v"(xf[9][5]), "v"(xr0), "a"(wq[9][0]) : "memory"); SB();
  WAITL(8);
  SB();
  asm volatile("v_mfma_f32_16x16x32_bf16 %0, %10, %11, %0\n\tds_read_b128 %1, %12 offset:16384\n\tv_mfma_f32_16x16x32_bf16 %2, %13, %11, %2\n\tbuffer_load_dwordx4 %14, %15, %16 offen offset:2048  lds\n\tv_mfma_f32_16x16x32_bf16 %3, %17, %11, %3\n\tbuffer_load_dwordx4 %18, %15, %16 offen offset:3072  lds\n\tv_mfma_f32_16x16x32_bf16 %4, %10, %19, %4\n\tv_mfma_f32_16x16x32_bf16 %5, %13, %19, %5\n\tv_mfma_f32_16x16x32_bf16 %6, %17, %19, %6\n\tv_mfma_f32_16x16x32_bf16 %7, %10, %20, %7\n\tv_mfma_f32_16x16x32_bf16 %8, %13, %20, %8\n\tv_mfma_f32_16x16x32_bf16 %9, %17, %20, %9"
      : "+v"(ak[6]), "=&v"(xf[10][8]), "+v"(av[6]), "+v"(aq[5]), "+v"(ak[7]), "+v"(av[7]), "+v"(aq[6]), "+v"(ak[8]), "+v"(av[8]), "+v"(aq[7]) : "a"(wq[9][1]), "v"(xf[9][6]), "v"(xr0), "a"(wq[9][2]), "v"(vW2[2]), "s"(rs2), "s"(sw[1]), "a"(wq[9][0]), "v"(vW2[3]), "v"(xf[9][7]), "v"(xf[9][8]) : "memory"); SB();
  STAMP(32);
  WAITVB(23);
  WAITL(3);
  SB();
  asm volatile("v_mfma_f32_16x16x32_bf16 %0, %13, %14, %0\n\tv_add_u32 %1, 36864, %1\n\tv_mfma_f32_16x16x32_bf16 %2, %15, %14, %2\n\tds_read_b128 %3, %1 offset:0\n\tv_mfma_f32_16x16x32_bf16 %4, %13, %16, %4\n\tv_mfma_f32_16x16x32_bf16 %5, %15, %16, %5\n\tds_read_b128 %6, %1 offset:2048\n\tv_mfma_f32_16x16x32_bf16 %7, %17, %16, %7\n\tv_mfma_f32_16x16x32_bf16 %8, %13, %18, %8\n\tds_read_b128 %9, %1 offset:4096\n\tv_mfma_f32_16x16x32_bf16 %10, %15, %18, %10\n\tv_mfma_f32_16x16x32_bf16 %11, %17, %18, %11\n\tds_read_b128 %12, %1 offset:6144"
      : "+v"(ak[0]), "+v"(xr1), "+v"(av[0]), "=&v"(xf[11][0]), "+v"(ak[1]), "+v"(av[1]), "=&v"(xf[11][1]), "+v"(aq[0]), "+v"(ak[2]), "=&v"(xf[11][2]), "+v"(av[2]), "+v"(aq[1]), "=&v"(xf[11][3]) : "a"(wq[10][1]), "v"(xf[10][0]), "a"(wq[10][2]), "v"(xf[10][1]), "a"(wq[10][0]), "v"(xf[10][2]) : "memory"); SB();
  asm volatile("v_mfma_f32_16x16x32_bf16 %0, %13, %14, %0\n\tv_mfma_f32_16x16x32_bf16 %1, %15, %14, %1\n\tds_read_b128 %2, %16 offset:8192\n\tv_mfma_f32_16x16x32_bf16 %3, %17, %14, %3\n\tv_mfma_f32_16x16x32_bf16 %4, %13, %18, %4\n\tds_read_b128 %5, %16 offset:10240\n\tv_mfma_f32_16x16x32_bf16 %6, %15, %18, %6\n\tv_mfma_f32_16x16x32_bf16 %7, %17, %18, %7\n\tds_read_b128 %8, %16 offset:12288\n\tv_mfma_f32_16x16x32_bf16 %9, %13, %19, %9\n\tv_mfma_f32_16x16x32_bf16 %10, %15, %19, %10\n\tds_read_b128 %11, %16 offset:14336\n\tv_mfma_f32_16x16x32_bf16 %12, %17, %19, %12"
      : "+v"(ak[3]), "+v"(av[3]), "=&v"(xf[11][4]), "+v"(aq[2]), "+v"(ak[4]), "=&v"(xf[11][5]), "+v"(av[4]), "+v"(aq[3]), "=&v"(xf[11][6]), "+v"(ak[5]), "+v"(av[5]), "=&v"(xf[11][7]), "+v"(aq[4]) : "a"(wq[10][1]), "v"(xf[10][3]), "a"(wq[10][2]), "v"(xr1), "a"(wq[10][0]), "v"(xf[10][4]), "v"(xf[10][5]) : "memory"); SB();
  WAITL(8);
  SB();
  asm volatile("v_mfma_f32_16x16x32_bf16 %0, %3, %4, %0\n\tds_read_b128 %1, %5 offset:16384\n\tv_mfma_f32_16x16x32_bf16 %2, %6, %4, %2\n\ts_waitcnt vmcnt(10)"
      : "+v"(ak[6]), "=&v"(xf[11][8]), "+v"(av[6]) : "a"(wq[10][1]), "v"(xf[10][6]), "v"(xr1), "a"(wq[10][2]) : "memory"); SB();
  BAR0();
  SB();
  asm volatile("v_mfma_f32_16x16x32_bf16 %0, %9, %10, %0\n\tds_read_b128 %1, %11 offset:18432\n\tds_read_b128 %2, %12 offset:18432\n\tv_mfma_f32_16x16x32_bf16 %3, %13, %14, %3\n\tv_mfma_f32_16x16x32_bf16 %4, %15, %14, %4\n\tv_mfma_f32_16x16x32_bf16 %5, %9, %14, %5\n\tv_mfma_f32_16x16x32_bf16 %6, %13, %16, %6\n\tv_mfma_f32_16x16x32_bf16 %7, %15, %16, %7\n\tv_mfma_f32_16x16x32_bf16 %8, %9, %16, %8"
      : "+v"(aq[5]), "=&v"(xf[12][0]), "=&v"(xf[13][0]), "+v"(ak[7]), "+v"(av[7]), "+v"(aq[6]), "+v"(ak[8]), "+v"(av[8]), "+v"(aq[7]) : "a"(wq[10][0]), "v"(xf[10][6]), "v"(xr0), "v"(xr1), "a"(wq[10][1]), "v"(xf[10][7]), "a"(wq[10][2]), "v"(xf[10][8]) : "memory"); SB();
  STAMP(13);
  WAITVB(19);
  WAITL(0);
  SB();
  asm volatile("v_mfma_f32_16x16x32_bf16 %0, %1, %2, %0"
      : "+v"(ak[0]) : "a"(wq[11][1]), "v"(xf[11][0])); SB();
  TOUCH(vT2, rs2, 0u, 0);
  SB();
  asm volatile("v_mfma_f32_16x16x32_bf16 %0, %2, %3, %0\n\tds_read_b128 %1, %4 offset:20480"
      : "+v"(av[0]), "=&v"(xf[12][1]) : "a"(wq[11][2]), "v"(xf[11][0]), "v"(xr0) : "memory"); SB();
  WAITVB(15);
  SB();
  asm volatile("v_mfma_f32_16x16x32_bf16 %0, %3, %4, %0\n\tds_read_b128 %1, %5 offset:20480\n\tv_mfma_f32_16x16x32_bf16 %2, %6, %4, %2"
      : "+v"(ak[0]), "=&v"(xf[13][1]), "+v"(av[0]) : "a"(wq[12][1]), "v"(xf[12][0]), "v"(xr1), "a"(wq[12][2]) : "memory"); SB();
  WAITVB(9);
  SB();
  asm volatile("v_mfma_f32_16x16x32_bf16 %0, %2, %3, %0\n\tv_mfma_f32_16x16x32_bf16 %1, %4, %3, %1"
      : "+v"(ak[0]), "+v"(av[0]) : "a"(wq[13][1]), "v"(xf[13][0]), "a"(wq[13][2])); SB();
  WAITL(0);
  SB();
  asm volatile("v_mfma_f32_16x16x32_bf16 %0, %9, %10, %0\n\tds_read_b128 %1, %11 offset:22528\n\tds_read_b128 %2, %12 offset:22528\n\tv_mfma_f32_16x16x32_bf16 %3, %13, %10, %3\n\tv_mfma_f32_16x16x32_bf16 %4, %14, %10, %4\n\tv_mfma_f32_16x16x32_bf16 %0, %15, %16, %0\n\tv_mfma_f32_16x16x32_bf16 %3, %17, %16, %3\n\tv_cvt_pk_bf16_f32 %5, %18, %19\n\tv_cvt_pk_bf16_f32 %6, %20, %21\n\tv_mfma_f32_16x16x32_bf16 %4, %22, %16, %4\n\tv_cvt_pk_bf16_f32 %7, %23, %24\n\tv_cvt_pk_bf16_f32 %8, %25, %26"
      : "+v"(ak[1]), "=&v"(xf[12][2]), "=&v"(xf[13][2]), "+v"(av[1]), "+v"(aq[0]), "=&v"(sg[0][0]), "=&v"(sg[0][1]), "=&v"(sg[1][0]), "=&v"(sg[1][1]) : "a"(wq[11][1]), "v"(xf[11][1]), "v"(xr0), "v"(xr1), "a"(wq[11][2]), "a"(wq[11][0]), "a"(wq[12][1]), "v"(xf[12][1]), "a"(wq[12][2]), "v"(ak[0][0]), "v"(ak[0][1]), "v"(ak[0][2]), "v"(ak[0][3]), "a"(wq[12][0]), "v"(av[0][0]), "v"(av[0][1]), "v"(av[0][2]), "v"(av[0][3]) : "memory"); SB();
  asm volatile("v_mfma_f32_16x16x32_bf16 %0, %3, %4, %0\n\tv_mfma_f32_16x16x32_bf16 %1, %5, %4, %1\n\tv_mfma_f32_16x16x32_bf16 %2, %6, %4, %2"
      : "+v"(ak[1]), "+v"(av[1]), "+v"(aq[0]) : "a"(wq[13][1]), "v"(xf[13][1]), "a"(wq[13][2]), "a"(wq[13][0])); SB();
  WAITL(0);
  SB();
  asm volatile("v_mfma_f32_16x16x32_bf16 %0, %7, %8, %0\n\tds_read_b128 %1, %9 offset:24576\n\tds_read_b128 %2, %10 offset:24576\n\tv_mfma_f32_16x16x32_bf16 %3, %11, %8, %3\n\tds_write_b64 %12, %13 offset:18432\n\tv_mfma_f32_16x16x32_bf16 %4, %14, %8, %4\n\tv_mfma_f32_16x16x32_bf16 %0, %15, %16, %0\n\tv_mfma_f32_16x16x32_bf16 %3, %17, %16, %3\n\tds_write_b64 %18, %19 offset:0\n\tv_mfma_f32_16x16x32_bf16 %4, %20, %16, %4\n\tv_cvt_pk_bf16_f32 %5, %21, %22\n\tv_cvt_pk_bf16_f32 %6, %23, %24\n\tv_mfma_f32_16x16x32_bf16 %0, %25, %26, %0"
      : "+v"(ak[2]), "=&v"(xf[12][3]), "=&v"(xf[13][3]), "+v"(av[2]), "+v"(aq[1]), "=&v"(sg[3][0]), "=&v"(sg[3][1]) : "a"(wq[11][1]), "v"(xf[11][2]), "v"(xr0), "v"(xr1), "a"(wq[11][2]), "v"(sk0), "v"(sg[0]), "a"(wq[11][0]), "a"(wq[12][1]), "v"(xf[12][2]), "a"(wq[12][2]), "v"(sv), "v"(sg[1]), "a"(wq[12][0]), "v"(ak[1][0]), "v"(ak[1][1]), "v"(ak[1][2]), "v"(ak[1][3]), "a"(wq[13][1]), "v"(xf[13][2]) : "memory"); SB();
  asm volatile("v_cvt_pk_bf16_f32 %0, %6, %7\n\tv_cvt_pk_bf16_f32 %1, %8, %9\n\tv_mfma_f32_16x16x32_bf16 %2, %10, %11, %2\n\tv_cvt_pk_bf16_f32 %3, %12, %13\n\tv_cvt_pk_bf16_f32 %4, %14, %15\n\tv_mfma_f32_16x16x32_bf16 %5, %16, %11, %5"
      : "=&v"(sg[4][0]), "=&v"(sg[4][1]), "+v"(av[2]), "=&v"(sg[5][0]), "=&v"(sg[5][1]), "+v"(aq[1]) : "v"(av[1][0]), "v"(av[1][1]), "v"(av[1][2]), "v"(av[1][3]), "a"(wq[13][2]), "v"(xf[13][2]), "v"(aq[0][0]), "v"(aq[0][1]), "v"(aq[0][2]), "v"(aq[0][3]), "a"(wq[13][0])); SB();
  WAITL(2);
  SB();
  asm volatile("v_mfma_f32_16x16x32_bf16 %0, %7, %8, %0\n\tds_read_b128 %1, %9 offset:26624\n\tds_read_b128 %2, %10 offset:26624\n\tv_mfma_f32_16x16x32_bf16 %3, %11, %8, %3\n\tds_write_b64 %12, %13 offset:18944\n\tv_mfma_f32_16x16x32_bf16 %4, %14, %8, %4\n\tv_mfma_f32_16x16x32_bf16 %0, %15, %16, %0\n\tv_mfma_f32_16x16x32_bf16 %3, %17, %16, %3\n\tds_write_b64 %18, %19 offset:256\n\tv_mfma_f32_16x16x32_bf16 %4, %20, %16, %4\n\tv_cvt_pk_bf16_f32 %5, %21, %22\n\tv_cvt_pk_bf16_f32 %6, %23, %24\n\tv_mfma_f32_16x16x32_bf16 %0, %25, %26, %0"
      : "+v"(ak[3]), "=&v"(xf[12][4]), "=&v"(xf[13][4]), "+v"(av[3]), "+v"(aq[2]), "=&v"(sg[6][0]), "=&v"(sg[6][1]) : "a"(wq[11][1]), "v"(xf[11][3]), "v"(xr0), "v"(xr1), "a"(wq[11][2]), "v"(sk1), "v"(sg[3]), "a"(wq[11][0]), "a"(wq[12][1]), "v"(xf[12][3]), "a"(wq[12][2]), "v"(sv), "v"(sg[4]), "a"(wq[12][0]), "v"(ak[2][0]), "v"(ak[2][1]), "v"(ak[2][2]), "v"(ak[2][3]), "a"(wq[13][1]), "v"(xf[13][3]) : "memory"); SB();
  asm volatile("v_cvt_pk_bf16_f32 %0, %4, %5\n\tv_cvt_pk_bf16_f32 %1, %6, %7\n\tv_mfma_f32_16x16x32_bf16 %2, %8, %9, %2\n\tds_write_b64 %10, %11 offset:36864\n\tv_mfma_f32_16x16x32_bf16 %3, %12, %9, %3"
      : "=&v"(sg[7][0]), "=&v"(sg[7][1]), "+v"(av[3]), "+v"(aq[2]) : "v"(av[2][0]), "v"(av[2][1]), "v"(av[2][2]), "v"(av[2][3]), "a"(wq[13][2]), "v"(xf[13][3]), "v"(sk0), "v"(sg[5]), "a"(wq[13][0]) : "memory"); SB();
  WAITL(3);
  SB();
  asm volatile("v_mfma_f32_16x16x32_bf16 %0, %7, %8, %0\n\tds_read_b128 %1, %9 offset:28672\n\tds_read_b128 %2, %10 offset:28672\n\tv_mfma_f32_16x16x32_bf16 %3, %11, %8, %3\n\tds_write_b64 %12, %13 offset:19456\n\tv_cvt_pk_bf16_f32 %4, %14, %15\n\tv_cvt_pk_bf16_f32 %5, %16, %17\n\tv_mfma_f32_16x16x32_bf16 %6, %18, %8, %6\n\tv_mfma_f32_16x16x32_bf16 %0, %19, %20, %0\n\tv_mfma_f32_16x16x32_bf16 %3, %21, %20, %3\n\tds_write_b64 %22, %23 offset:512\n\tv_mfma_f32_16x16x32_bf16 %6, %24, %20, %6"
      : "+v"(ak[4]), "=&v"(xf[12][5]), "=&v"(xf[13][5]), "+v"(av[4]), "=&v"(sg[8][0]), "=&v"(sg[8][1]), "+v"(aq[3]) : "a"(wq[11][1]), "v"(xf[11][4]), "v"(xr0), "v"(xr1), "a"(wq[11][2]), "v"(sk0), "v"(sg[6]), "v"(aq[1][0]), "v"(aq[1][1]), "v"(aq[1][2]), "v"(aq[1][3]), "a"(wq[11][0]), "a"(wq[12][1]), "v"(xf[12][4]), "a"(wq[12][2]), "v"(sv), "v"(sg[7]), "a"(wq[12][0]) : "memory"); SB();
  asm volatile("v_cvt_pk_bf16_f32 %0, %7, %8\n\tv_cvt_pk_bf16_f32 %1, %9, %10\n\tv_mfma_f32_16x16x32_bf16 %2, %11, %12, %2\n\tv_cvt_pk_bf16_f32 %3, %13, %14\n\tv_cvt_pk_bf16_f32 %4, %15, %16\n\tv_mfma_f32_16x16x32_bf16 %5, %17, %12, %5\n\tds_write_b64 %18, %19 offset:37376\n\tv_mfma_f32_16x16x32_bf16 %6, %20, %12, %6"
      : "=&v"(sg[9][0]), "=&v"(sg[9][1]), "+v"(ak[4]), "=&v"(sg[10][0]), "=&v"(sg[10][1]), "+v"(av[4]), "+v"(aq[3]) : "v"(ak[3][0]), "v"(ak[3][1]), "v"(ak[3][2]), "v"(ak[3][3]), "a"(wq[13][1]), "v"(xf[13][4]), "v"(av[3][0]), "v"(av[3][1]), "v"(av[3][2]), "v"(av[3][3]), "a"(wq[13][2]), "v"(sk1), "v"(sg[8]), "a"(wq[13][0]) : "memory"); SB();
  WAITL(3);
  SB();
  asm volatile("v_mfma_f32_16x16x32_bf16 %0, %7, %8, %0\n\tds_read_b128 %1, %9 offset:30720\n\tds_read_b128 %2, %10 offset:30720\n\tv_mfma_f32_16x16x32_bf16 %3, %11, %8, %3\n\tds_write_b64 %12, %13 offset:19968\n\tv_cvt_pk_bf16_f32 %4, %14, %15\n\tv_cvt_pk_bf16_f32 %5, %16, %17\n\tv_mfma_f32_16x16x32_bf16 %6, %18, %8, %6\n\tv_mfma_f32_16x16x32_bf16 %0, %19, %20, %0\n\tv_mfma_f32_16x16x32_bf16 %3, %21, %20, %3\n\tds_write_b64 %22, %23 offset:768\n\tv_mfma_f32_16x16x32_bf16 %6, %24, %20, %6"
      : "+v"(ak[5]), "=&v"(xf[12][6]), "=&v"(xf[13][6]), "+v"(av[5]), "=&v"(sg[11][0]), "=&v"(sg[11][1]), "+v"(aq[4]) : "a"(wq[11][1]), "v"(xf[11][5]), "v"(xr0), "v"(xr1), "a"(wq[11][2]), "v"(sk1), "v"(sg[9]), "v"(aq[2][0]), "v"(aq[2][1]), "v"(aq[2][2]), "v"(aq[2][3]), "a"(wq[11][0]), "a"(wq[12][1]), "v"(xf[12][5]), "a"(wq[12][2]), "v"(sv), "v"(sg[10]), "a"(wq[12][0]) : "memory"); SB();
  asm volatile("v_cvt_pk_bf16_f32 %0, %7, %8\n\tv_cvt_pk_bf16_f32 %1, %9, %10\n\tv_mfma_f32_16x16x32_bf16 %2, %11, %12, %2\n\tv_cvt_pk_bf16_f32 %3, %13, %14\n\tv_cvt_pk_bf16_f32 %4, %15, %16\n\tv_mfma_f32_16x16x32_bf16 %5, %17, %12, %5\n\tds_write_b64 %18, %19 offset:37888\n\tv_mfma_f32_16x16x32_bf16 %6, %20, %12, %6"
      : "=&v"(sg[12][0]), "=&v"(sg[12][1]), "+v"(ak[5]), "=&v"(sg[13][0]), "=&v"(sg[13][1]), "+v"(av[5]), "+v"(aq[4]) : "v"(ak[4][0]), "v"(ak[4][1]), "v"(ak[4][2]), "v"(ak[4][3]), "a"(wq[13][1]), "v"(xf[13][5]), "v"(av[4][0]), "v"(av[4][1]), "v"(av[4][2]), "v"(av[4][3]), "a"(wq[13][2]), "v"(sk0), "v"(sg[11]), "a"(wq[13][0]) : "memory"); SB();
  WAITL(3);
  SB();
  asm volatile("v_mfma_f32_16x16x32_bf16 %0, %7, %8, %0\n\tds_read_b128 %1, %9 offset:32768\n\tds_read_b128 %2, %10 offset:32768\n\tv_mfma_f32_16x16x32_bf16 %3, %11, %8, %3\n\tds_write_b64 %12, %13 offset:20480\n\tv_cvt_pk_bf16_f32 %4, %14, %15\n\tv_cvt_pk_bf16_f32 %5, %16, %17\n\tv_mfma_f32_16x16x32_bf16 %6, %18, %8, %6\n\tv_mfma_f32_16x16x32_bf16 %0, %19, %20, %0\n\tv_mfma_f32_16x16x32_bf16 %3, %21, %20, %3\n\tds_write_b64 %22, %23 offset:1024\n\tv_mfma_f32_16x16x32_bf16 %6, %24, %20, %6"
      : "+v"(ak[6]), "=&v"(xf[12][7]), "=&v"(xf[13][7]), "+v"(av[6]), "=&v"(sg[14][0]), "=&v"(sg[14][1]), "+v"(aq[5]) : "a"(wq[11][1]), "v"(xf[11][6]), "v"(xr0), "v"(xr1), "a"(wq[11][2]), "v"(sk0), "v"(sg[12]), "v"(aq[3][0]), "v"(aq[3][1]), "v"(aq[3][2]), "v"(aq[3][3]), "a"(wq[11][0]), "a"(wq[12][1]), "v"(xf[12][6]), "a"(wq[12][2]), "v"(sv), "v"(sg[13]), "a"(wq[12][0]) : "memory"); SB();
  asm volatile("v_cvt_pk_bf16_f32 %0, %7, %8\n\tv_cvt_pk_bf16_f32 %1, %9, %10\n\tv_mfma_f32_16x16x32_bf16 %2, %11, %12, %2\n\tv_cvt_pk_bf16_f32 %3, %13, %14\n\tv_cvt_pk_bf16_f32 %4, %15, %16\n\tv_mfma_f32_16x16x32_bf16 %5, %17, %12, %5\n\tds_write_b64 %18, %19 offset:38400\n\tv_mfma_f32_16x16x32_bf16 %6, %20, %12, %6"
      : "=&v"(sg[15][0]), "=&v"(sg[15][1]), "+v"(ak[6]), "=&v"(sg[16][0]), "=&v"(sg[16][1]), "+v"(av[6]), "+v"(aq[5]) : "v"(ak[5][0]), "v"(ak[5][1]), "v"(ak[5][2]), "v"(ak[5][3]), "a"(wq[13][1]), "v"(xf[13][6]), "v"(av[5][0]), "v"(av[5][1]), "v"(av[5][2]), "v"(av[5][3]), "a"(wq[13][2]), "v"(sk1), "v"(sg[14]), "a"(wq[13][0]) : "memory"); SB();
  WAITL(3);
  SB();
  asm volatile("v_mfma_f32_16x16x32_bf16 %0, %7, %8, %0\n\tds_read_b128 %1, %9 offset:34816\n\tds_read_b128 %2, %10 offset:34816\n\tv_mfma_f32_16x16x32_bf16 %3, %11, %8, %3\n\tds_write_b64 %12, %13 offset:20992\n\tv_cvt_pk_bf16_f32 %4, %14, %15\n\tv_cvt_pk_bf16_f32 %5, %16, %17\n\tv_mfma_f32_16x16x32_bf16 %6, %18, %8, %6\n\tv_mfma_f32_16x16x32_bf16 %0, %19, %20, %0\n\tv_mfma_f32_16x16x32_bf16 %3, %21, %20, %3\n\tds_write_b64 %22, %23 offset:1280\n\tv_mfma_f32_16x16x32_bf16 %6, %24, %20, %6"
      : "+v"(ak[7]), "=&v"(xf[12][8]), "=&v"(xf[13][8]), "+v"(av[7]), "=&v"(sg[17][0]), "=&v"(sg[17][1]), "+v"(aq[6]) : "a"(wq[11][1]), "v"(xf[11][7]), "v"(xr0), "v"(xr1), "a"(wq[11][2]), "v"(sk1), "v"(sg[15]), "v"(aq[4][0]), "v"(aq[4][1]), "v"(aq[4][2]), "v"(aq[4][3]), "a"(wq[11][0]), "a"(wq[12][1]), "v"(xf[12][7]), "a"(wq[12][2]), "v"(sv), "v"(sg[16]), "a"(wq[12][0]) : "memory"); SB();
  asm volatile("v_cvt_pk_bf16_f32 %0, %7, %8\n\tv_cvt_pk_bf16_f32 %1, %9, %10\n\tv_mfma_f32_16x16x32_bf16 %2, %11, %12, %2\n\tv_cvt_pk_bf16_f32 %3, %13, %14\n\tv_cvt_pk_bf16_f32 %4, %15, %16\n\tv_mfma_f32_16x16x32_bf16 %5, %17, %12, %5\n\tds_write_b64 %18, %19 offset:38912\n\tv_mfma_f32_16x16x32_bf16 %6, %20, %12, %6"
      : "=&v"(sg[18][0]), "=&v"(sg[18][1]), "+v"(ak[7]), "=&v"(sg[19][0]), "=&v"(sg[19][1]), "+v"(av[7]), "+v"(aq[6]) : "v"(ak[6][0]), "v"(ak[6][1]), "v"(ak[6][2]), "v"(ak[6][3]), "a"(wq[13][1]), "v"(xf[13][7]), "v"(av[6][0]), "v"(av[6][1]), "v"(av[6][2]), "v"(av[6][3]), "a"(wq[13][2]), "v"(sk0), "v"(sg[17]), "a"(wq[13][0]) : "memory"); SB();
  WAITL(3);
  SB();
  asm volatile("v_mfma_f32_16x16x32_bf16 %0, %7, %8, %0\n\tv_cvt_pk_bf16_f32 %1, %9, %10\n\tv_cvt_pk_bf16_f32 %2, %11, %12\n\tv_mfma_f32_16x16x32_bf16 %3, %13, %8, %3\n\tds_write_b64 %14, %15 offset:21504\n\tv_mfma_f32_16x16x32_bf16 %4, %16, %8, %4\n\tv_mfma_f32_16x16x32_bf16 %0, %17, %18, %0\n\tv_mfma_f32_16x16x32_bf16 %3, %19, %18, %3\n\tds_write_b64 %20, %21 offset:1536\n\tv_mfma_f32_16x16x32_bf16 %4, %22, %18, %4\n\tv_cvt_pk_bf16_f32 %5, %23, %24\n\tv_cvt_pk_bf16_f32 %6, %25, %26"
      : "+v"(ak[8]), "=&v"(sg[20][0]), "=&v"(sg[20][1]), "+v"(av[8]), "+v"(aq[7]), "=&v"(sg[21][0]), "=&v"(sg[21][1]) : "a"(wq[11][1]), "v"(xf[11][8]), "v"(aq[5][0]), "v"(aq[5][1]), "v"(aq[5][2]), "v"(aq[5][3]), "a"(wq[11][2]), "v"(sk0), "v"(sg[18]), "a"(wq[11][0]), "a"(wq[12][1]), "v"(xf[12][8]), "a"(wq[12][2]), "v"(sv), "v"(sg[19]), "a"(wq[12][0]), "v"(ak[7][0]), "v"(ak[7][1]), "v"(ak[7][2]), "v"(ak[7][3]) : "memory"); SB();
  asm volatile("v_mfma_f32_16x16x32_bf16 %0, %7, %8, %0\n\tv_cvt_pk_bf16_f32 %1, %9, %10\n\tv_cvt_pk_bf16_f32 %2, %11, %12\n\tv_mfma_f32_16x16x32_bf16 %3, %13, %8, %3\n\tds_write_b64 %14, %15 offset:39424\n\tv_mfma_f32_16x16x32_bf16 %4, %16, %8, %4\n\tv_cvt_pk_bf16_f32 %5, %17, %18\n\tv_cvt_pk_bf16_f32 %6, %19, %20\n\ts_nop 5\n\tds_write_b64 %14, %21 offset:22016"
      : "+v"(ak[8]), "=&v"(sg[22][0]), "=&v"(sg[22][1]), "+v"(av[8]), "+v"(aq[7]), "=&v"(sg[23][0]), "=&v"(sg[23][1]) : "a"(wq[13][1]), "v"(xf[13][8]), "v"(av[7][0]), "v"(av[7][1]), "v"(av[7][2]), "v"(av[7][3]), "a"(wq[13][2]), "v"(sk1), "v"(sg[20]), "a"(wq[13][0]), "v"(aq[6][0]), "v"(aq[6][1]), "v"(aq[6][2]), "v"(aq[6][3]), "v"(sg[21]) : "memory"); SB();
  asm volatile("ds_write_b64 %6, %7 offset:1792\n\tds_write_b64 %8, %9 offset:39936\n\tv_cvt_pk_bf16_f32 %0, %10, %11\n\tv_cvt_pk_bf16_f32 %1, %12, %13\n\tv_cvt_pk_bf16_f32 %2, %14, %15\n\tv_cvt_pk_bf16_f32 %3, %16, %17\n\tv_cvt_pk_bf16_f32 %4, %18, %19\n\tv_cvt_pk_bf16_f32 %5, %20, %21"
      : "=&v"(sg[24][0]), "=&v"(sg[24][1]), "=&v"(sg[25][0]), "=&v"(sg[25][1]), "=&v"(sg[26][0]), "=&v"(sg[26][1]) : "v"(sv), "v"(sg[22]), "v"(sk0), "v"(sg[23]), "v"(ak[8][0]), "v"(ak[8][1]), "v"(ak[8][2]), "v"(ak[8][3]), "v"(av[8][0]), "v"(av[8][1]), "v"(av[8][2]), "v"(av[8][3]), "v"(aq[7][0]), "v"(aq[7][1]), "v"(aq[7][2]), "v"(aq[7][3]) : "memory"); SB();
  asm volatile("ds_write_b64 %0, %1 offset:22528\n\tds_write_b64 %2, %3 offset:2048\n\tds_write_b64 %4, %5 offset:40448"
      :  : "v"(sk0), "v"(sg[24]), "v"(sv), "v"(sg[25]), "v"(sk1), "v"(sg[26]) : "memory"); SB();
  WAITV1(14, fq);
  if (__builtin_expect(__ballot(fq != nonce) != 0ull, 0)) poll1<4>(rsW, vFQ, nonce);
  BAR();
  STAMP(14);
  unsigned fu; u32x4 qf[2][2]; bf16x8 kf[2][2][2], vf[2][2][2];
  qf[0][0] = *(LDS_AS const u32x4*)(rk0 + SQ + 512 + 0); kf[0][0][0] = *(LDS_AS const bf16x8*)(rk0 + SK + 0); kf[0][0][1] = *(LDS_AS const bf16x8*)(rk0 + SK + 1024);
  qf[0][1] = *(LDS_AS const u32x4*)(rk0 + SQ + 512 + 9216); kf[0][1][0] = *(LDS_AS const bf16x8*)(rk0 + SK + 9216); kf[0][1][1] = *(LDS_AS const bf16x8*)(rk0 + SK + 10240);
  qf[1][0] = *(LDS_AS const u32x4*)(rk1 + SQ + 512 + 0); kf[1][0][0] = *(LDS_AS const bf16x8*)(rk1 + SK + 0); kf[1][0][1] = *(LDS_AS const bf16x8*)(rk1 + SK + 1024);
  qf[1][1] = *(LDS_AS const u32x4*)(rk1 + SQ + 512 + 9216); kf[1][1][0] = *(LDS_AS const bf16x8*)(rk1 + SK + 9216); kf[1][1][1] = *(LDS_AS const bf16x8*)(rk1 + SK + 10240);
  SB();
  asm volatile("s_add_u32 m0, %0, 0\n\ts_nop 0\n\tbuffer_load_dwordx4 %1, %2, %3 offen offset:0  lds\n\tbuffer_load_dwordx4 %4, %2, %3 offen offset:1024  lds\n\tbuffer_load_dwordx4 %5, %2, %3 offen offset:2048  lds\n\tbuffer_load_dwordx4 %6, %2, %3 offen offset:3072  lds"
      :  : "s"(ldsO), "v"(vW2[0]), "s"(rs2), "s"(sw[2]), "v"(vW2[1]), "v"(vW2[2]), "v"(vW2[3]) : "memory", "scc"); SB();
  f32x4 sc[2][2][2];
  asm volatile("v_mfma_f32_16x16x32_bf16 %0, %2, %3, 0\n\tv_mfma_f32_16x16x32_bf16 %1, %4, %3, 0"
      : "=&v"(sc[0][0][0]), "=&v"(sc[0][0][1]) : "v"(kf[0][0][0]), "v"(qf[0][0]), "v"(kf[0][0][1])); SB();
  vf[0][0][0] = tr8u(rv0 + 0, 512);
  vf[0][0][1] = tr8u(rv0 + 4608, 512);
  SB();
  asm volatile("v_mfma_f32_16x16x32_bf16 %0, %2, %3, 0\n\tv_mfma_f32_16x16x32_bf16 %1, %4, %3, 0"
      : "=&v"(sc[0][1][0]), "=&v"(sc[0][1][1]) : "v"(kf[0][1][0]), "v"(qf[0][1]), "v"(kf[0][1][1])); SB();
  vf[0][1][0] = tr8u(rv0 + 9216, 512);
  vf[0][1][1] = tr8u(rv0 + 13824, 512);
  SB();
  asm volatile("v_mfma_f32_16x16x32_bf16 %0, %2, %3, 0\n\tv_mfma_f32_16x16x32_bf16 %1, %4, %3, 0"
      : "=&v"(sc[1][0][0]), "=&v"(sc[1][0][1]) : "v"(kf[1][0][0]), "v"(qf[1][0]), "v"(kf[1][0][1])); SB();
  vf[1][0][0] = tr8u(rv1 + 0, 512);
  vf[1][0][1] = tr8u(rv1 + 4608, 512);
  SB();
  asm volatile("v_mfma_f32_16x16x32_bf16 %0, %2, %3, 0\n\tv_mfma_f32_16x16x32_bf16 %1, %4, %3, 0"
      : "=&v"(sc[1][1][0]), "=&v"(sc[1][1][1]) : "v"(kf[1][1][0]), "v"(qf[1][1]), "v"(kf[1][1][1])); SB();
  vf[1][1][0] = tr8u(rv1 + 9216, 512);
  vf[1][1][1] = tr8u(rv1 + 13824, 512);
  SB();
  SB();
  asm volatile("s_add_u32 m0, %0, 4096\n\ts_nop 0\n\tbuffer_load_dwordx4 %1, %2, %3 offen offset:0  lds\n\tbuffer_load_dwordx4 %4, %2, %3 offen offset:1024  lds\n\tbuffer_load_dwordx4 %5, %2, %3 offen offset:2048  lds\n\tbuffer_load_dwordx4 %6, %2, %3 offen offset:3072  lds"
      :  : "s"(ldsO), "v"(vW2[0]), "s"(rs2), "s"(sw[3]), "v"(vW2[1]), "v"(vW2[2]), "v"(vW2[3]) : "memory", "scc"); SB();
  f32x2_t lg[2][2][2], mx[2], rs[2]; u32x4 pf[2][2]; f32x4 oo[2][2][2];
  const f32x2_t scale = {0.18898223650461362f, 0.18898223650461362f};
#define SEL(p, h, i) __uint_as_float((m1[i] & __float_as_uint(sc[p][h][1][i])) | (~m1[i] & __float_as_uint(sc[p][h][0][i])))
  lg[0][0][0] = rbf2(__builtin_elementwise_fma(rbf2((f32x2_t){SEL(0, 0, 0), SEL(0, 0, 1)}), scale, (f32x2_t){cm[0], cm[1]}));
  lg[0][0][1] = rbf2(__builtin_elementwise_fma(rbf2((f32x2_t){SEL(0, 0, 2), SEL(0, 0, 3)}), scale, (f32x2_t){cm[2], cm[3]}));
  OPV(lg[0][0][0]); OPV(lg[0][0][1]);
  SB();
  asm volatile("s_add_u32 m0, %0, 0\n\ts_nop 0\n\tbuffer_load_dwordx4 %1, %2, %3 offen offset:0  lds\n\tbuffer_load_dwordx4 %4, %2, %3 offen offset:1024  lds\n\tbuffer_load_dwordx4 %5, %2, %3 offen offset:2048  lds\n\tbuffer_load_dwordx4 %6, %2, %3 offen offset:3072  lds"
      :  : "s"(ldsS), "v"(vW2[0]), "s"(rs2), "s"(sw[4]), "v"(vW2[1]), "v"(vW2[2]), "v"(vW2[3]) : "memory", "scc"); SB();
  lg[0][1][0] = rbf2(__builtin_elementwise_fma(rbf2((f32x2_t){SEL(0, 1, 0), SEL(0, 1, 1)}), scale, (f32x2_t){cm[0], cm[1]}));
  lg[0][1][1] = rbf2(__builtin_elementwise_fma(rbf2((f32x2_t){SEL(0, 1, 2), SEL(0, 1, 3)}), scale, (f32x2_t){cm[2], cm[3]}));
  OPV(lg[0][1][0]); OPV(lg[0][1][1]);
  SB();
  asm volatile("s_waitcnt vmcnt(17)"
      :  :  : "memory"); SB();
  fw2[0][0] = tr8u(tS[0] + 18432, 512);
  fw2[0][1] = tr8u(tS[1] + 18432, 512);
  fw2[0][2] = tr8u(tS[2] + 18432, 512);
  fw2[0][3] = tr8u(tS[3] + 18432, 512);
  SB();
  lg[1][0][0] = rbf2(__builtin_elementwise_fma(rbf2((f32x2_t){SEL(1, 0, 0), SEL(1, 0, 1)}), scale, (f32x2_t){cm[0], cm[1]}));
  lg[1][0][1] = rbf2(__builtin_elementwise_fma(rbf2((f32x2_t){SEL(1, 0, 2), SEL(1, 0, 3)}), scale, (f32x2_t){cm[2], cm[3]}));
  OPV(lg[1][0][0]); OPV(lg[1][0][1]);
  SB();
  WAITL(0);
  asm volatile("s_add_u32 m0, %0, 18432\n\ts_nop 0\n\tbuffer_load_dwordx4 %1, %2, %3 offen offset:0  lds\n\tbuffer_load_dwordx4 %4, %2, %3 offen offset:1024  lds\n\tbuffer_load_dwordx4 %5, %2, %3 offen offset:2048  lds\n\tbuffer_load_dwordx4 %6, %2, %3 offen offset:3072  lds"
      :  : "s"(ldsS), "v"(vW2[0]), "s"(rs2), "s"(sw[5]), "v"(vW2[1]), "v"(vW2[2]), "v"(vW2[3]) : "memory", "scc"); SB();
  lg[1][1][0] = rbf2(__builtin_elementwise_fma(rbf2((f32x2_t){SEL(1, 1, 0), SEL(1, 1, 1)}), scale, (f32x2_t){cm[0], cm[1]}));
  lg[1][1][1] = rbf2(__builtin_elementwise_fma(rbf2((f32x2_t){SEL(1, 1, 2), SEL(1, 1, 3)}), scale, (f32x2_t){cm[2], cm[3]}));
  OPV(lg[1][1][0]); OPV(lg[1][1][1]);
  SB();
  asm volatile("s_waitcnt vmcnt(17)"
      :  :  : "memory"); SB();
  fw2[1][0] = tr8u(tS[0] + 34816, 512);
  fw2[1][1] = tr8u(tS[1] + 34816, 512);
  fw2[1][2] = tr8u(tS[2] + 34816, 512);
  fw2[1][3] = tr8u(tS[3] + 34816, 512);
  SB();
  #pragma unroll
  for (int p = 0; p < 2; ++p)
    mx[p] = xbcast2(xmax2(fmx(fmx(lg[p][0][0][0], lg[p][0][0][1]), fmx(lg[p][0][1][0], lg[p][0][1][1])), fmx(fmx(lg[p][1][0][0], lg[p][1][0][1]), fmx(lg[p][1][1][0], lg[p][1][1][1]))));
  OPV(mx[0]); OPV(mx[1]);
  SB();
  WAITL(0);
  asm volatile("s_add_u32 m0, %0, 34816\n\ts_nop 0\n\tbuffer_load_dwordx4 %1, %2, %3 offen offset:0  lds\n\tbuffer_load_dwordx4 %4, %2, %3 offen offset:1024  lds\n\tbuffer_load_dwordx4 %5, %2, %3 offen offset:2048  lds\n\tbuffer_load_dwordx4 %6, %2, %3 offen offset:3072  lds\n\ts_waitcnt vmcnt(16)"
      :  : "s"(ldsS), "v"(vW2[0]), "s"(rs2), "s"(sw[6]), "v"(vW2[1]), "v"(vW2[2]), "v"(vW2[3]) : "memory", "scc"); SB();
  fw2[2][0] = tr8u(tO[0] + 0, 512);
  fw2[2][1] = tr8u(tO[1] + 0, 512);
  fw2[2][2] = tr8u(tO[2] + 0, 512);
  fw2[2][3] = tr8u(tO[3] + 0, 512);
  SB();
  #pragma unroll
  for (int j = 0; j < 2; ++j) {
    const f32x2_t z = (lg[0][0][j] - (f32x2_t){mx[0][0], mx[0][0]}) * (f32x2_t){1.442695041f, 1.442695041f};
    lg[0][0][j] = (f32x2_t){__builtin_amdgcn_exp2f(z[0]), __builtin_amdgcn_exp2f(z[1])}; OPV(lg[0][0][j]);
  }
  SB();
  WAITL(0);
  asm volatile("s_add_u32 m0, %0, 0\n\ts_nop 0\n\tbuffer_load_dwordx4 %1, %2, %3 offen offset:0  lds\n\tbuffer_load_dwordx4 %4, %2, %3 offen offset:1024  lds\n\tbuffer_load_dwordx4 %5, %2, %3 offen offset:2048  lds\n\tbuffer_load_dwordx4 %6, %2, %3 offen offset:3072  lds"
      :  : "s"(ldsO), "v"(vW2[0]), "s"(rs2), "s"(sw[7]), "v"(vW2[1]), "v"(vW2[2]), "v"(vW2[3]) : "memory", "scc"); SB();
  #pragma unroll
  for (int j = 0; j < 2; ++j) {
    const f32x2_t z = (lg[0][1][j] - (f32x2_t){mx[0][1], mx[0][1]}) * (f32x2_t){1.442695041f, 1.442695041f};
    lg[0][1][j] = (f32x2_t){__builtin_amdgcn_exp2f(z[0]), __builtin_amdgcn_exp2f(z[1])}; OPV(lg[0][1][j]);
  }
  SB();
  asm volatile("s_waitcnt vmcnt(16)"
      :  :  : "memory"); SB();
  fw2[3][0] = tr8u(tO[0] + 4096, 512);
  fw2[3][1] = tr8u(tO[1] + 4096, 512);
  fw2[3][2] = tr8u(tO[2] + 4096, 512);
  fw2[3][3] = tr8u(tO[3] + 4096, 512);
  SB();
  #pragma unroll
  for (int j = 0; j < 2; ++j) {
    const f32x2_t z = (lg[1][0][j] - (f32x2_t){mx[1][0], mx[1][0]}) * (f32x2_t){1.442695041f, 1.442695041f};
    lg[1][0][j] = (f32x2_t){__builtin_amdgcn_exp2f(z[0]), __builtin_amdgcn_exp2f(z[1])}; OPV(lg[1][0][j]);
  }
  SB();
  WAITL(0);
  asm volatile("s_add_u32 m0, %0, 4096\n\ts_nop 0\n\tbuffer_load_dwordx4 %1, %2, %3 offen offset:0  lds\n\tbuffer_load_dwordx4 %4, %2, %3 offen offset:1024  lds\n\tbuffer_load_dwordx4 %5, %2, %3 offen offset:2048  lds\n\tbuffer_load_dwordx4 %6, %2, %3 offen offset:3072  lds"
      :  : "s"(ldsO), "v"(vW2[0]), "s"(rs2), "s"(sw[8]), "v"(vW2[1]), "v"(vW2[2]), "v"(vW2[3]) : "memory", "scc"); SB();
  #pragma unroll
  for (int j = 0; j < 2; ++j) {
    const f32x2_t z = (lg[1][1][j] - (f32x2_t){mx[1][1], mx[1][1]}) * (f32x2_t){1.442695041f, 1.442695041f};
    lg[1][1][j] = (f32x2_t){__builtin_amdgcn_exp2f(z[0]), __builtin_amdgcn_exp2f(z[1])}; OPV(lg[1][1][j]);
  }
  SB();
  asm volatile("s_waitcnt vmcnt(16)"
      :  :  : "memory"); SB();
  fw2[4][0] = tr8u(tS[0] + 0, 512);
  fw2[4][1] = tr8u(tS[1] + 0, 512);
  fw2[4][2] = tr8u(tS[2] + 0, 512);
  fw2[4][3] = tr8u(tS[3] + 0, 512);
  SB();
  #pragma unroll
  for (int p = 0; p < 2; ++p) {
    const f32x2_t e0 = lg[p][0][0] + lg[p][0][1], e1 = lg[p][1][0] + lg[p][1][1];
    rs[p] = xbcast2(__builtin_amdgcn_rcpf(xsum2(e0[0] + e0[1], e1[0] + e1[1])));
  }
  OPV(rs[0]); OPV(rs[1]);
  SB();
  WAITL(0);
  asm volatile("s_add_u32 m0, %0, 0\n\ts_nop 0\n\tbuffer_load_dwordx4 %1, %2, %3 offen offset:0  lds\n\tbuffer_load_dwordx4 %4, %2, %3 offen offset:1024  lds\n\tbuffer_load_dwordx4 %5, %2, %3 offen offset:2048  lds\n\tbuffer_load_dwordx4 %6, %2, %3 offen offset:3072  lds\n\ts_waitcnt vmcnt(16)"
      :  : "s"(ldsS), "v"(vW2[0]), "s"(rs2), "s"(sw[9]), "v"(vW2[1]), "v"(vW2[2]), "v"(vW2[3]) : "memory", "scc"); SB();
  fw2[5][0] = tr8u(tS[0] + 18432, 512);
  fw2[5][1] = tr8u(tS[1] + 18432, 512);
  fw2[5][2] = tr8u(tS[2] + 18432, 512);
  fw2[5][3] = tr8u(tS[3] + 18432, 512);
  SB();
  { const f32x2_t pa = lg[0][0][0] * (f32x2_t){rs[0][0], rs[0][0]}, pb = lg[0][0][1] * (f32x2_t){rs[0][0], rs[0][0]};
    const unsigned wa = pack2(pa[0], pa[1]), wb = pack2(pb[0], pb[1]);
    pf[0][0] = (u32x4){wa & ~mp[0], wb & ~mp[1], wa & mp[0], wb & mp[1]}; OPV(pf[0][0]); }
  { const f32x2_t pa = lg[0][1][0] * (f32x2_t){rs[0][1], rs[0][1]}, pb = lg[0][1][1] * (f32x2_t){rs[0][1], rs[0][1]};
    const unsigned wa = pack2(pa[0], pa[1]), wb = pack2(pb[0], pb[1]);
    pf[0][1] = (u32x4){wa & ~mp[0], wb & ~mp[1], wa & mp[0], wb & mp[1]}; OPV(pf[0][1]); }
  SB();
  WAITL(0);
  asm volatile("s_add_u32 m0, %4, 18432\n\ts_nop 0\n\tbuffer_load_dwordx4 %5, %6, %7 offen offset:0  lds\n\tbuffer_load_dwordx4 %8, %6, %7 offen offset:1024  lds\n\tbuffer_load_dwordx4 %9, %6, %7 offen offset:2048  lds\n\tbuffer_load_dwordx4 %10, %6, %7 offen offset:3072  lds\n\ts_nop 1\n\tv_mfma_f32_16x16x32_bf16 %0, %11, %12, 0\n\tv_mfma_f32_16x16x32_bf16 %1, %13, %14, 0\n\tv_mfma_f32_16x16x32_bf16 %2, %15, %12, 0\n\tv_mfma_f32_16x16x32_bf16 %3, %16, %14, 0"
      : "=&v"(oo[0][0][0]), "=&v"(oo[0][1][0]), "=&v"(oo[0][0][1]), "=&v"(oo[0][1][1]) : "s"(ldsS), "v"(vW2[0]), "s"(rs2), "s"(sw[10]), "v"(vW2[1]), "v"(vW2[2]), "v"(vW2[3]), "v"(vf[0][0][0]), "v"(pf[0][0]), "v"(vf[0][1][0]), "v"(pf[0][1]), "v"(vf[0][0][1]), "v"(vf[0][1][1]) : "memory", "scc"); SB();
  { const f32x2_t pa = lg[1][0][0] * (f32x2_t){rs[1][0], rs[1][0]}, pb = lg[1][0][1] * (f32x2_t){rs[1][0], rs[1][0]};
    const unsigned wa = pack2(pa[0], pa[1]), wb = pack2(pb[0], pb[1]);
    pf[1][0] = (u32x4){wa & ~mp[0], wb & ~mp[1], wa & mp[0], wb & mp[1]}; OPV(pf[1][0]); }
  { const f32x2_t pa = lg[1][1][0] * (f32x2_t){rs[1][1], rs[1][1]}, pb = lg[1][1][1] * (f32x2_t){rs[1][1], rs[1][1]};
    const unsigned wa = pack2(pa[0], pa[1]), wb = pack2(pb[0], pb[1]);
    pf[1][1] = (u32x4){wa & ~mp[0], wb & ~mp[1], wa & mp[0], wb & mp[1]}; OPV(pf[1][1]); }
  SB();
  asm volatile("s_waitcnt vmcnt(16)"
      :  :  : "memory"); SB();
  fw2[6][0] = tr8u(tS[0] + 34816, 512);
  fw2[6][1] = tr8u(tS[1] + 34816, 512);
  fw2[6][2] = tr8u(tS[2] + 34816, 512);
  fw2[6][3] = tr8u(tS[3] + 34816, 512);
  SB();
  asm volatile("s_nop 1\n\tv_mfma_f32_16x16x32_bf16 %0, %4, %5, 0\n\tv_mfma_f32_16x16x32_bf16 %1, %6, %7, 0\n\tv_mfma_f32_16x16x32_bf16 %2, %8, %5, 0\n\tv_mfma_f32_16x16x32_bf16 %3, %9, %7, 0"
      : "=&v"(oo[1][0][0]), "=&v"(oo[1][1][0]), "=&v"(oo[1][0][1]), "=&v"(oo[1][1][1]) : "v"(vf[1][0][0]), "v"(pf[1][0]), "v"(vf[1][1][0]), "v"(pf[1][1]), "v"(vf[1][0][1]), "v"(vf[1][1][1])); SB();
  HS16(vS, sO, 0, ((u32x4){pack2(oo[0][0][0][0], oo[0][0][0][1]), pack2(oo[0][0][0][2], oo[0][0][0][3]), pack2(oo[0][1][0][0], oo[0][1][0][1]), pack2(oo[0][1][0][2], oo[0][1][0][3])})); SB();
  HS16H(0xffff, vS, sO, 128, ((u32x4){pack2(oo[0][0][1][0], oo[0][0][1][1]), pack2(oo[0][0][1][2], oo[0][0][1][3]), pack2(oo[0][1][1][0], oo[0][1][1][1]), pack2(oo[0][1][1][2], oo[0][1][1][3])})); SB();
  HS16(vS, sO, 1024, ((u32x4){pack2(oo[1][0][0][0], oo[1][0][0][1]), pack2(oo[1][0][0][2], oo[1][0][0][3]), pack2(oo[1][1][0][0], oo[1][1][0][1]), pack2(oo[1][1][0][2], oo[1][1][0][3])})); SB();
  HS16H(0xffff, vS, sO, 1152, ((u32x4){pack2(oo[1][0][1][0], oo[1][0][1][1]), pack2(oo[1][0][1][2], oo[1][0][1][3]), pack2(oo[1][1][1][0], oo[1][1][1][1]), pack2(oo[1][1][1][2], oo[1][1][1][3])})); SB();
  STAMP(15);
  WAITL(0);
  asm volatile("s_add_u32 m0, %0, 34816\n\ts_nop 0\n\tbuffer_load_dwordx4 %1, %2, %3 offen offset:0  lds\n\tbuffer_load_dwordx4 %4, %2, %3 offen offset:1024  lds\n\tbuffer_load_dwordx4 %5, %2, %3 offen offset:2048  lds\n\tbuffer_load_dwordx4 %6, %2, %3 offen offset:3072  lds\n\ts_waitcnt vmcnt(20)"
      :  : "s"(ldsS), "v"(vW2[0]), "s"(rs2), "s"(sw[11]), "v"(vW2[1]), "v"(vW2[2]), "v"(vW2[3]) : "memory", "scc"); SB();
  fw2[7][0] = tr8u(tO[0] + 0, 512);
  fw2[7][1] = tr8u(tO[1] + 0, 512);
  fw2[7][2] = tr8u(tO[2] + 0, 512);
  fw2[7][3] = tr8u(tO[3] + 0, 512);
  asm volatile("s_waitcnt vmcnt(16)"
      :  :  : "memory"); SB();
  fw2[8][0] = tr8u(tO[0] + 4096, 512);
  fw2[8][1] = tr8u(tO[1] + 4096, 512);
  fw2[8][2] = tr8u(tO[2] + 4096, 512);
  fw2[8][3] = tr8u(tO[3] + 4096, 512);
  asm volatile("s_waitcnt vmcnt(12)"
      :  :  : "memory"); SB();
  fw2[9][0] = tr8u(tS[0] + 0, 512);
  fw2[9][1] = tr8u(tS[1] + 0, 512);
  fw2[9][2] = tr8u(tS[2] + 0, 512);
  fw2[9][3] = tr8u(tS[3] + 0, 512);
  WAITL(0);
  asm volatile("s_add_u32 m0, %0, 0\n\ts_nop 0\n\tbuffer_load_dwordx4 %1, %2, %3 offen offset:0  lds\n\tbuffer_load_dwordx4 %4, %2, %3 offen offset:1024  lds\n\tbuffer_load_dwordx4 %5, %2, %3 offen offset:2048  lds\n\tbuffer_load_dwordx4 %6, %2, %3 offen offset:3072  lds\n\ts_waitcnt vmcnt(12)"
      :  : "s"(ldsS), "v"(vW2[0]), "s"(rs2), "s"(sw[12]), "v"(vW2[1]), "v"(vW2[2]), "v"(vW2[3]) : "memory", "scc"); SB();
  fw2[10][0] = tr8u(tS[0] + 18432, 512);
  fw2[10][1] = tr8u(tS[1] + 18432, 512);
  fw2[10][2] = tr8u(tS[2] + 18432, 512);
  fw2[10][3] = tr8u(tS[3] + 18432, 512);
  WAITL(0);
  asm volatile("s_add_u32 m0, %1, 18432\n\ts_nop 0\n\tbuffer_load_dwordx4 %2, %3, %4 offen offset:0  lds\n\tbuffer_load_dwordx4 %5, %3, %4 offen offset:1024  lds\n\tbuffer_load_dwordx4 %6, %3, %4 offen offset:2048  lds\n\tbuffer_load_dwordx4 %7, %3, %4 offen offset:3072  lds\n\ts_add_u32 m0, %8, 8192\n\ts_add_u32 %0, %9, 8192\n\tbuffer_load_dwordx4 %10, %11, %0 offen offset:0 sc1 lds\n\tbuffer_load_dwordx4 %10, %11, %0 offen offset:1024 sc1 lds\n\tbuffer_load_dwordx4 %10, %11, %0 offen offset:2048 sc1 lds\n\tbuffer_load_dwordx4 %10, %11, %0 offen offset:3072 sc1 lds\n\ts_add_u32 m0, %8, 12288\n\ts_add_u32 %0, %9, 12288\n\tbuffer_load_dwordx4 %10, %11, %0 offen offset:0 sc1 lds\n\tbuffer_load_dwordx4 %10, %11, %0 offen offset:1024 sc1 lds\n\ts_add_u32 m0, %12, 0\n\ts_add_u32 %0, %9, 0\n\tbuffer_load_dwordx4 %10, %11, %0 offen offset:0 sc1 lds\n\tbuffer_load_dwordx4 %10, %11, %0 offen offset:1024 sc1 lds\n\tbuffer_load_dwordx4 %10, %11, %0 offen offset:2048 sc1 lds\n\tbuffer_load_dwordx4 %10, %11, %0 offen offset:3072 sc1 lds\n\ts_add_u32 m0, %12, 4096\n\ts_add_u32 %0, %9, 4096\n\tbuffer_load_dwordx4 %10, %11, %0 offen offset:0 sc1 lds\n\tbuffer_load_dwordx4 %10, %11, %0 offen offset:1024 sc1 lds\n\tbuffer_load_dwordx4 %10, %11, %0 offen offset:2048 sc1 lds\n\tbuffer_load_dwordx4 %10, %11, %0 offen offset:3072 sc1 lds\n\ts_waitcnt vmcnt(26)"
      : "=&s"(sWt) : "s"(ldsS), "v"(vW2[0]), "s"(rs2), "s"(sw[13]), "v"(vW2[1]), "v"(vW2[2]), "v"(vW2[3]), "s"(ldsP), "s"(sWo), "v"(vWo), "s"(rsW), "s"(ldsO) : "memory", "scc"); SB();
  STFO(sFO);
  STAMP(24);
  asm volatile("s_waitcnt vmcnt(23)"
      :  :  : "memory"); SB();
  fw2[11][0] = tr8u(tS[0] + 34816, 512);
  fw2[11][1] = tr8u(tS[1] + 34816, 512);
  fw2[11][2] = tr8u(tS[2] + 34816, 512);
  fw2[11][3] = tr8u(tS[3] + 34816, 512);
  asm volatile("buffer_load_dword %0, %1, %2, 0 offen sc1"
      : "=&v"(fu) : "v"(vFU), "s"(rsW) : "memory"); SB();
  u32x4 rsX = srd(x, 2048u * 896u); OPQ(rsX);
  WAITL(0);
  asm volatile("s_add_u32 m0, %1, 34816\n\ts_nop 0\n\tbuffer_load_dwordx4 %2, %3, %4 offen offset:0  lds\n\tbuffer_load_dwordx4 %5, %3, %4 offen offset:1024  lds\n\tglobal_load_ushort %0, %6, %7 offset:0"
      : "=&v"(braw) : "s"(ldsS), "v"(vXa), "s"(rsX), "s"(0u), "v"(vXb), "v"(vB2), "s"(b2) : "memory", "scc"); SB();
  STAMP(16);
  {
  bf16x8 pu[14], fwo[56];
  f32x4 ac[2][4], as[4], py[4][4];
  float ye[4], ze[4], ge[16], xe[16], bias;
  u32x2 ov[4]; unsigned fo;
  u32x2 xv[4]; unsigned xta[4];
  WAITV1(3, fu);
  if (__builtin_expect(__ballot(fu != nonce) != 0ull, 0)) poll1<4>(rsW, vFU, nonce);
  STAMP(17);
  asm volatile("buffer_load_dwordx4 %0, %10, %11, %12 offen offset:0 sc1\n\tbuffer_load_dwordx4 %1, %10, %11, %13 offen offset:0 sc1\n\tbuffer_load_dwordx4 %2, %10, %11, %14 offen offset:0 sc1\n\tbuffer_load_dwordx4 %3, %10, %11, %15 offen offset:0 sc1\n\tbuffer_load_dwordx4 %4, %10, %11, %16 offen offset:0 sc1\n\tbuffer_load_dwordx4 %5, %10, %11, %17 offen offset:0 sc1\n\tbuffer_load_dwordx4 %6, %10, %11, %18 offen offset:0 sc1\n\tbuffer_load_dwordx4 %7, %10, %11, %19 offen offset:0 sc1\n\tbuffer_load_dwordx4 %8, %10, %11, %20 offen offset:0 sc1\n\tbuffer_load_dwordx4 %9, %10, %11, %21 offen offset:0 sc1"
      : "=&v"(pu[0]), "=&v"(pu[1]), "=&v"(pu[2]), "=&v"(pu[3]), "=&v"(pu[4]), "=&v"(pu[5]), "=&v"(pu[6]), "=&v"(pu[7]), "=&v"(pu[8]), "=&v"(pu[9]) : "v"(vFj[0]), "s"(rsW), "s"(su[0]), "s"(su[1]), "s"(su[2]), "s"(su[3]), "s"(su[4]), "s"(su[5]), "s"(su[6]), "s"(su[7]), "s"(su[8]), "s"(su[9]) : "memory"); SB();
  STAMP(39);
  WAITVB(9);
  SB();
  asm volatile("v_mfma_f32_16x16x32_bf16 %0, %6, %7, 0\n\tbuffer_load_dwordx4 %1, %8, %9, %10 offen offset:0 sc1\n\tv_mfma_f32_16x16x32_bf16 %2, %6, %11, 0\n\ts_waitcnt vmcnt(34)\n\tv_mfma_f32_16x16x32_bf16 %3, %6, %12, 0\n\tbuffer_load_dwordx4 %4, %8, %9, %13 offen offset:0 sc1\n\tv_mfma_f32_16x16x32_bf16 %5, %6, %14, 0"
      : "=&v"(ac[0][0]), "=&v"(pu[10]), "=&v"(ac[0][1]), "=&v"(ac[0][2]), "=&v"(pu[11]), "=&v"(ac[0][3]) : "v"(pu[0]), "a"(fw2[0][0]), "v"(vFj[0]), "s"(rsW), "s"(su[10]), "a"(fw2[0][1]), "a"(fw2[0][2]), "s"(su[11]), "a"(fw2[0][3]) : "memory"); SB();
  WAITVB(10);
  SB();
  asm volatile("v_mfma_f32_16x16x32_bf16 %0, %3, %4, %0\n\tbuffer_load_dwordx4 %1, %5, %6, %7 offen offset:0 sc1\n\tv_mfma_f32_16x16x32_bf16 %2, %3, %8, %2"
      : "+v"(ac[0][0]), "=&v"(pu[12]), "+v"(ac[0][1]) : "v"(pu[1]), "a"(fw2[1][0]), "v"(vFj[0]), "s"(rsW), "s"(su[12]), "a"(fw2[1][1]) : "memory"); SB();
  fw2[12][0] = tr8u(tS[0] + 0, 512);
  SB();
  asm volatile("v_mfma_f32_16x16x32_bf16 %0, %3, %4, %0\n\tbuffer_load_dwordx4 %1, %5, %6, %7 offen offset:0 sc1\n\tv_mfma_f32_16x16x32_bf16 %2, %3, %8, %2"
      : "+v"(ac[0][2]), "=&v"(pu[13]), "+v"(ac[0][3]) : "v"(pu[1]), "a"(fw2[1][2]), "v"(vFj[0]), "s"(rsW), "s"(su[13]), "a"(fw2[1][3]) : "memory"); SB();
  WAITVB(11);
  SB();
  asm volatile("v_mfma_f32_16x16x32_bf16 %0, %3, %4, %0\n\tbuffer_load_dwordx4 %1, %5, %6, %7 offen offset:0 sc1\n\tv_mfma_f32_16x16x32_bf16 %2, %3, %8, %2"
      : "+v"(ac[0][0]), "=&v"(pu[0]), "+v"(ac[0][1]) : "v"(pu[2]), "a"(fw2[2][0]), "v"(vFj[1]), "s"(rsW), "s"(su[0]), "a"(fw2[2][1]) : "memory"); SB();
  fw2[12][1] = tr8u(tS[1] + 0, 512);
  SB();
  asm volatile("v_mfma_f32_16x16x32_bf16 %0, %3, %4, %0\n\tbuffer_load_dwordx4 %1, %5, %6, %7 offen offset:0 sc1\n\tv_mfma_f32_16x16x32_bf16 %2, %3, %8, %2"
      : "+v"(ac[0][2]), "=&v"(pu[1]), "+v"(ac[0][3]) : "v"(pu[2]), "a"(fw2[2][2]), "v"(vFj[1]), "s"(rsW), "s"(su[1]), "a"(fw2[2][3]) : "memory"); SB();
  fw2[12][2] = tr8u(tS[2] + 0, 512);
  fw2[12][3] = tr8u(tS[3] + 0, 512);
  asm volatile("s_waitcnt vmcnt(35)"
      :  :  : "memory"); SB();
  fw2[13][0] = tr8u(tS[0] + 18432, 512);
  fw2[13][1] = tr8u(tS[1] + 18432, 512);
  fw2[13][2] = tr8u(tS[2] + 18432, 512);
  fw2[13][3] = tr8u(tS[3] + 18432, 512);
  WAITVB(16);
  SB();
  WAITVB(12);
  SB();
  asm volatile("v_mfma_f32_16x16x32_bf16 %0, %10, %11, %0\n\tbuffer_load_dwordx4 %1, %12, %13, %14 offen offset:0 sc1\n\tv_mfma_f32_16x16x32_bf16 %2, %10, %15, %2\n\tv_lshlrev_b32 %3, 16, %16\n\tv_xor_b32 %4, 0, %17\n\tv_mfma_f32_16x16x32_bf16 %5, %10, %18, %5\n\tv_xor_b32 %6, 32, %17\n\tv_xor_b32 %7, 64, %17\n\tv_mfma_f32_16x16x32_bf16 %8, %10, %19, %8\n\tv_xor_b32 %9, 96, %17"
      : "+v"(ac[0][0]), "=&v"(pu[2]), "+v"(ac[0][1]), "=&v"(bias), "=&v"(xta[0]), "+v"(ac[0][2]), "=&v"(xta[1]), "=&v"(xta[2]), "+v"(ac[0][3]), "=&v"(xta[3]) : "v"(pu[3]), "a"(fw2[3][0]), "v"(vFj[1]), "s"(rsW), "s"(su[2]), "a"(fw2[3][1]), "v"(braw), "v"(xs), "a"(fw2[3][2]), "a"(fw2[3][3]) : "memory"); SB();
  WAITVB(12);
  SB();
  asm volatile("v_mfma_f32_16x16x32_bf16 %0, %9, %10, %0\n\tbuffer_load_dwordx4 %1, %11, %12, %13 offen offset:0 sc1\n\tv_mfma_f32_16x16x32_bf16 %2, %9, %14, %2\n\tds_read_b64 %3, %15 offset:0\n\tds_read_b64 %4, %16 offset:0\n\tv_mfma_f32_16x16x32_bf16 %5, %9, %17, %5\n\tds_read_b64 %6, %18 offset:0\n\tds_read_b64 %7, %19 offset:0\n\tv_mfma_f32_16x16x32_bf16 %8, %9, %20, %8"
      : "+v"(ac[0][0]), "=&v"(pu[3]), "+v"(ac[0][1]), "=&v"(xv[0]), "=&v"(xv[1]), "+v"(ac[0][2]), "=&v"(xv[2]), "=&v"(xv[3]), "+v"(ac[0][3]) : "v"(pu[4]), "a"(fw2[4][0]), "v"(vFj[1]), "s"(rsW), "s"(su[3]), "a"(fw2[4][1]), "v"(xta[0]), "v"(xta[1]), "a"(fw2[4][2]), "v"(xta[2]), "v"(xta[3]), "a"(fw2[4][3]) : "memory"); SB();
  WAITVB(12);
  SB();
  asm volatile("v_mfma_f32_16x16x32_bf16 %0, %5, %6, %0\n\tbuffer_load_dwordx4 %1, %7, %8, %9 offen offset:0 sc1\n\tv_mfma_f32_16x16x32_bf16 %2, %5, %10, %2\n\tv_mfma_f32_16x16x32_bf16 %3, %5, %11, %3\n\tv_mfma_f32_16x16x32_bf16 %4, %5, %12, %4"
      : "+v"(ac[0][0]), "=&v"(pu[4]), "+v"(ac[0][1]), "+v"(ac[0][2]), "+v"(ac[0][3]) : "v"(pu[5]), "a"(fw2[5][0]), "v"(vFj[1]), "s"(rsW), "s"(su[4]), "a"(fw2[5][1]), "a"(fw2[5][2]), "a"(fw2[5][3]) : "memory"); SB();
  WAITL(0);
  SB();
  WAITVB(12);
  SB();
  asm volatile("v_mfma_f32_16x16x32_bf16 %0, %10, %11, %0\n\tbuffer_load_dwordx4 %1, %12, %13, %14 offen offset:0 sc1\n\tv_mfma_f32_16x16x32_bf16 %2, %10, %15, %2\n\tv_lshlrev_b32 %3, 16, %16\n\tv_and_b32 %4, 0xffff0000, %16\n\tv_mfma_f32_16x16x32_bf16 %5, %10, %17, %5\n\tv_lshlrev_b32 %6, 16, %18\n\tv_and_b32 %7, 0xffff0000, %18\n\tv_mfma_f32_16x16x32_bf16 %8, %10, %19, %8\n\tv_lshlrev_b32 %9, 16, %20"
      : "+v"(ac[0][0]), "=&v"(pu[5]), "+v"(ac[0][1]), "=&v"(xe[0]), "=&v"(xe[1]), "+v"(ac[0][2]), "=&v"(xe[2]), "=&v"(xe[3]), "+v"(ac[0][3]), "=&v"(xe[4]) : "v"(pu[6]), "a"(fw2[6][0]), "v"(vFj[1]), "s"(rsW), "s"(su[5]), "a"(fw2[6][1]), "v"(xv[0][0]), "a"(fw2[6][2]), "v"(xv[0][1]), "a"(fw2[6][3]), "v"(xv[1][0]) : "memory"); SB();
  WAITVB(12);
  SB();
  asm volatile("v_mfma_f32_16x16x32_bf16 %0, %10, %11, %0\n\tbuffer_load_dwordx4 %1, %12, %13, %14 offen offset:0 sc1\n\tv_mfma_f32_16x16x32_bf16 %2, %10, %15, %2\n\tv_and_b32 %3, 0xffff0000, %16\n\tv_lshlrev_b32 %4, 16, %17\n\tv_mfma_f32_16x16x32_bf16 %5, %10, %18, %5\n\tv_and_b32 %6, 0xffff0000, %17\n\tv_lshlrev_b32 %7, 16, %19\n\tv_mfma_f32_16x16x32_bf16 %8, %10, %20, %8\n\tv_and_b32 %9, 0xffff0000, %19"
      : "+v"(ac[0][0]), "=&v"(pu[6]), "+v"(ac[0][1]), "=&v"(xe[5]), "=&v"(xe[6]), "+v"(ac[0][2]), "=&v"(xe[7]), "=&v"(xe[8]), "+v"(ac[0][3]), "=&v"(xe[9]) : "v"(pu[7]), "a"(fw2[7][0]), "v"(vFj[1]), "s"(rsW), "s"(su[6]), "a"(fw2[7][1]), "v"(xv[1][0]), "v"(xv[1][1]), "a"(fw2[7][2]), "v"(xv[2][0]), "a"(fw2[7][3]) : "memory"); SB();
  WAITVB(12);
  SB();
  asm volatile("v_mfma_f32_16x16x32_bf16 %0, %10, %11, %0\n\tbuffer_load_dwordx4 %1, %12, %13, %14 offen offset:0 sc1\n\tv_mfma_f32_16x16x32_bf16 %2, %10, %15, %2\n\tv_lshlrev_b32 %3, 16, %16\n\tv_and_b32 %4, 0xffff0000, %16\n\tv_mfma_f32_16x16x32_bf16 %5, %10, %17, %5\n\tv_lshlrev_b32 %6, 16, %18\n\tv_and_b32 %7, 0xffff0000, %18\n\tv_mfma_f32_16x16x32_bf16 %8, %10, %19, %8\n\tv_lshlrev_b32 %9, 16, %20"
      : "+v"(ac[0][0]), "=&v"(pu[7]), "+v"(ac[0][1]), "=&v"(xe[10]), "=&v"(xe[11]), "+v"(ac[0][2]), "=&v"(xe[12]), "=&v"(xe[13]), "+v"(ac[0][3]), "=&v"(xe[14]) : "v"(pu[8]), "a"(fw2[8][0]), "v"(vFj[1]), "s"(rsW), "s"(su[7]), "a"(fw2[8][1]), "v"(xv[2][1]), "a"(fw2[8][2]), "v"(xv[3][0]), "a"(fw2[8][3]), "v"(xv[3][1]) : "memory"); SB();
  WAITVB(12);
  SB();
  asm volatile("v_mfma_f32_16x16x32_bf16 %0, %6, %7, %0\n\tbuffer_load_dwordx4 %1, %8, %9, %10 offen offset:0 sc1\n\tv_mfma_f32_16x16x32_bf16 %2, %6, %11, %2\n\tv_and_b32 %3, 0xffff0000, %12\n\tv_mfma_f32_16x16x32_bf16 %4, %6, %13, %4\n\tv_mfma_f32_16x16x32_bf16 %5, %6, %14, %5"
      : "+v"(ac[0][0]), "=&v"(pu[8]), "+v"(ac[0][1]), "=&v"(xe[15]), "+v"(ac[0][2]), "+v"(ac[0][3]) : "v"(pu[9]), "v"(fw2[9][0]), "v"(vFj[1]), "s"(rsW), "s"(su[8]), "v"(fw2[9][1]), "v"(xv[3][1]), "v"(fw2[9][2]), "v"(fw2[9][3]) : "memory"); SB();
  WAITVB(12);
  SB();
  asm volatile("v_mfma_f32_16x16x32_bf16 %0, %5, %6, %0\n\tbuffer_load_dwordx4 %1, %7, %8, %9 offen offset:0 sc1\n\tv_mfma_f32_16x16x32_bf16 %2, %5, %10, %2\n\tv_mfma_f32_16x16x32_bf16 %3, %5, %11, %3\n\tv_mfma_f32_16x16x32_bf16 %4, %5, %12, %4"
      : "+v"(ac[0][0]), "=&v"(pu[9]), "+v"(ac[0][1]), "+v"(ac[0][2]), "+v"(ac[0][3]) : "v"(pu[10]), "v"(fw2[10][0]), "v"(vFj[1]), "s"(rsW), "s"(su[9]), "v"(fw2[10][1]), "v"(fw2[10][2]), "v"(fw2[10][3]) : "memory"); SB();
  WAITVB(12);
  SB();
  asm volatile("v_mfma_f32_16x16x32_bf16 %0, %5, %6, %0\n\tbuffer_load_dwordx4 %1, %7, %8, %9 offen offset:0 sc1\n\tv_mfma_f32_16x16x32_bf16 %2, %5, %10, %2\n\tv_mfma_f32_16x16x32_bf16 %3, %5, %11, %3\n\tv_mfma_f32_16x16x32_bf16 %4, %5, %12, %4"
      : "+v"(ac[0][0]), "=&v"(pu[10]), "+v"(ac[0][1]), "+v"(ac[0][2]), "+v"(ac[0][3]) : "v"(pu[11]), "v"(fw2[11][0]), "v"(vFj[1]), "s"(rsW), "s"(su[10]), "v"(fw2[11][1]), "v"(fw2[11][2]), "v"(fw2[11][3]) : "memory"); SB();
  WAITVB(12);
  SB();
  asm volatile("v_mfma_f32_16x16x32_bf16 %0, %4, %5, %0\n\tbuffer_load_dwordx4 %1, %6, %7, %8 offen offset:0 sc1\n\tv_mfma_f32_16x16x32_bf16 %2, %4, %9, %2\n\tv_mfma_f32_16x16x32_bf16 %3, %4, %10, %3"
      : "+v"(ac[0][0]), "=&v"(pu[11]), "+v"(ac[0][1]), "+v"(ac[0][2]) : "v"(pu[12]), "v"(fw2[12][0]), "v"(vFj[1]), "s"(rsW), "s"(su[11]), "v"(fw2[12][1]), "v"(fw2[12][2]) : "memory"); SB();
  u32x4 rsO_ = srd(out, 2048u * 896u); OPQ(rsO_);
  SB();
  asm volatile("v_mfma_f32_16x16x32_bf16 %0, %1, %2, %0"
      : "+v"(ac[0][3]) : "v"(pu[12]), "v"(fw2[12][3])); SB();
  WAITVB(12);
  SB();
  asm volatile("v_mfma_f32_16x16x32_bf16 %0, %5, %6, %0\n\tbuffer_load_dwordx4 %1, %7, %8, %9 offen offset:0 sc1\n\tv_mfma_f32_16x16x32_bf16 %2, %5, %10, %2\n\tv_mfma_f32_16x16x32_bf16 %3, %5, %11, %3\n\tv_mfma_f32_16x16x32_bf16 %4, %5, %12, %4"
      : "+v"(ac[0][0]), "=&v"(pu[12]), "+v"(ac[0][1]), "+v"(ac[0][2]), "+v"(ac[0][3]) : "v"(pu[13]), "v"(fw2[13][0]), "v"(vFj[1]), "s"(rsW), "s"(su[12]), "v"(fw2[13][1]), "v"(fw2[13][2]), "v"(fw2[13][3]) : "memory"); SB();
  STAMP(40);
  WAITVB(12);
  SB();
  asm volatile("v_mfma_f32_16x16x32_bf16 %0, %5, %6, 0\n\tbuffer_load_dwordx4 %1, %7, %8, %9 offen offset:0 sc1\n\tv_mfma_f32_16x16x32_bf16 %2, %5, %10, 0\n\tv_mfma_f32_16x16x32_bf16 %3, %5, %11, 0\n\tv_mfma_f32_16x16x32_bf16 %4, %5, %12, 0"
      : "=&v"(ac[1][0]), "=&v"(pu[13]), "=&v"(ac[1][1]), "=&v"(ac[1][2]), "=&v"(ac[1][3]) : "v"(pu[0]), "a"(fw2[0][0]), "v"(vFj[1]), "s"(rsW), "s"(su[13]), "a"(fw2[0][1]), "a"(fw2[0][2]), "a"(fw2[0][3]) : "memory"); SB();
  WAITVB(12);
  SB();
  asm volatile("v_mfma_f32_16x16x32_bf16 %0, %5, %6, %0\n\tbuffer_load_dwordx4 %1, %7, %8, %9 offen offset:0 sc1\n\tv_mfma_f32_16x16x32_bf16 %2, %5, %10, %2\n\tds_write_b128 %11, %12 offset:0\n\tv_mfma_f32_16x16x32_bf16 %3, %5, %13, %3\n\tv_mfma_f32_16x16x32_bf16 %4, %5, %14, %4"
      : "+v"(ac[1][0]), "=&v"(pu[0]), "+v"(ac[1][1]), "+v"(ac[1][2]), "+v"(ac[1][3]) : "v"(pu[1]), "a"(fw2[1][0]), "v"(vFj[2]), "s"(rsW), "s"(su[0]), "a"(fw2[1][1]), "v"(rwd), "v"(ac[0][0]), "a"(fw2[1][2]), "a"(fw2[1][3]) : "memory"); SB();
  WAITVB(12);
  SB();
  asm volatile("v_mfma_f32_16x16x32_bf16 %0, %5, %6, %0\n\tbuffer_load_dwordx4 %1, %7, %8, %9 offen offset:0 sc1\n\tv_mfma_f32_16x16x32_bf16 %2, %5, %10, %2\n\tds_write_b128 %11, %12 offset:1024\n\tv_mfma_f32_16x16x32_bf16 %3, %5, %13, %3\n\tv_mfma_f32_16x16x32_bf16 %4, %5, %14, %4"
      : "+v"(ac[1][0]), "=&v"(pu[1]), "+v"(ac[1][1]), "+v"(ac[1][2]), "+v"(ac[1][3]) : "v"(pu[2]), "a"(fw2[2][0]), "v"(vFj[2]), "s"(rsW), "s"(su[1]), "a"(fw2[2][1]), "v"(rwd), "v"(ac[0][1]), "a"(fw2[2][2]), "a"(fw2[2][3]) : "memory"); SB();
  WAITVB(12);
  SB();
  asm volatile("v_mfma_f32_16x16x32_bf16 %0, %5, %6, %0\n\tbuffer_load_dwordx4 %1, %7, %8, %9 offen offset:0 sc1\n\tv_mfma_f32_16x16x32_bf16 %2, %5, %10, %2\n\tds_write_b128 %11, %12 offset:2048\n\tv_mfma_f32_16x16x32_bf16 %3, %5, %13, %3\n\tv_mfma_f32_16x16x32_bf16 %4, %5, %14, %4"
      : "+v"(ac[1][0]), "=&v"(pu[2]), "+v"(ac[1][1]), "+v"(ac[1][2]), "+v"(ac[1][3]) : "v"(pu[3]), "a"(fw2[3][0]), "v"(vFj[2]), "s"(rsW), "s"(su[2]), "a"(fw2[3][1]), "v"(rwd), "v"(ac[0][2]), "a"(fw2[3][2]), "a"(fw2[3][3]) : "memory"); SB();
  WAITVB(12);
  SB();
  asm volatile("v_mfma_f32_16x16x32_bf16 %0, %5, %6, %0\n\tbuffer_load_dwordx4 %1, %7, %8, %9 offen offset:0 sc1\n\tv_mfma_f32_16x16x32_bf16 %2, %5, %10, %2\n\tds_write_b128 %11, %12 offset:3072\n\tv_mfma_f32_16x16x32_bf16 %3, %5, %13, %3\n\tv_mfma_f32_16x16x32_bf16 %4, %5, %14, %4"
      : "+v"(ac[1][0]), "=&v"(pu[3]), "+v"(ac[1][1]), "+v"(ac[1][2]), "+v"(ac[1][3]) : "v"(pu[4]), "a"(fw2[4][0]), "v"(vFj[2]), "s"(rsW), "s"(su[3]), "a"(fw2[4][1]), "v"(rwd), "v"(ac[0][3]), "a"(fw2[4][2]), "a"(fw2[4][3]) : "memory"); SB();
  WAITVB(12);
  SB();
  asm volatile("v_mfma_f32_16x16x32_bf16 %0, %3, %4, %0\n\tbuffer_load_dwordx4 %1, %5, %6, %7 offen offset:0 sc1\n\tv_mfma_f32_16x16x32_bf16 %2, %3, %8, %2\n\ts_waitcnt vmcnt(38)"
      : "+v"(ac[1][0]), "=&v"(pu[4]), "+v"(ac[1][1]) : "v"(pu[5]), "a"(fw2[5][0]), "v"(vFj[2]), "s"(rsW), "s"(su[4]), "a"(fw2[5][1]) : "memory"); SB();
  WAITL(0);
  BAR0();
  SB();
  asm volatile("v_mfma_f32_16x16x32_bf16 %0, %5, %6, %0\n\tds_read_b128 %1, %7 offset:0\n\tds_read_b128 %2, %7 offset:4096\n\tv_mfma_f32_16x16x32_bf16 %3, %5, %8, %3\n\tds_read_b128 %4, %7 offset:8192"
      : "+v"(ac[1][2]), "=&v"(py[0][0]), "=&v"(py[0][1]), "+v"(ac[1][3]), "=&v"(py[0][2]) : "v"(pu[5]), "a"(fw2[5][2]), "v"(rr), "a"(fw2[5][3]) : "memory"); SB();
  WAITVB(12);
  SB();
  asm volatile("v_mfma_f32_16x16x32_bf16 %0, %8, %9, %0\n\tbuffer_load_dwordx4 %1, %10, %11, %12 offen offset:0 sc1\n\tv_mfma_f32_16x16x32_bf16 %2, %8, %13, %2\n\tds_read_b128 %3, %14 offset:12288\n\tds_read_b128 %4, %15 offset:0\n\tv_mfma_f32_16x16x32_bf16 %5, %8, %16, %5\n\tv_mfma_f32_16x16x32_bf16 %6, %8, %17, %6\n\tds_read_b128 %7, %15 offset:8192"
      : "+v"(ac[1][0]), "=&v"(pu[5]), "+v"(ac[1][1]), "=&v"(py[0][3]), "=&a"(fwo[0]), "+v"(ac[1][2]), "+v"(ac[1][3]), "=&a"(fwo[1]) : "v"(pu[6]), "a"(fw2[6][0]), "v"(vFj[2]), "s"(rsW), "s"(su[5]), "a"(fw2[6][1]), "v"(rr), "v"(ro), "a"(fw2[6][2]), "a"(fw2[6][3]) : "memory"); SB();
  WAITVB(12);
  SB();
  asm volatile("v_mfma_f32_16x16x32_bf16 %0, %5, %6, %0\n\tbuffer_load_dwordx4 %1, %7, %8, %9 offen offset:0 sc1\n\tv_mfma_f32_16x16x32_bf16 %2, %5, %10, %2\n\tds_read_b128 %3, %11 offset:16384\n\tv_mfma_f32_16x16x32_bf16 %4, %5, %12, %4"
      : "+v"(ac[1][0]), "=&v"(pu[6]), "+v"(ac[1][1]), "=&a"(fwo[2]), "+v"(ac[1][2]) : "v"(pu[7]), "a"(fw2[7][0]), "v"(vFj[2]), "s"(rsW), "s"(su[6]), "a"(fw2[7][1]), "v"(ro), "a"(fw2[7][2]) : "memory"); SB();
  WAITL(3);
  asm volatile("v_add_f32 %0, %3, %4\n\tv_mfma_f32_16x16x32_bf16 %1, %5, %6, %1\n\tv_add_f32 %2, %7, %4"
      : "=&v"(ye[0]), "+v"(ac[1][3]), "=&v"(ye[1]) : "v"(py[0][0][0]), "v"(bias), "v"(pu[7]), "a"(fw2[7][3]), "v"(py[0][0][1])); SB();
  WAITVB(12);
  SB();
  asm volatile("v_mfma_f32_16x16x32_bf16 %0, %9, %10, %0\n\tbuffer_load_dwordx4 %1, %11, %12, %13 offen offset:0 sc1\n\tv_mfma_f32_16x16x32_bf16 %2, %9, %14, %2\n\tv_add_f32 %3, %15, %16\n\tv_add_f32 %4, %17, %16\n\tv_mfma_f32_16x16x32_bf16 %5, %9, %18, %5\n\tv_add_f32 %6, %19, %20\n\tv_add_f32 %7, %21, %22\n\tv_mfma_f32_16x16x32_bf16 %8, %9, %23, %8"
      : "+v"(ac[1][0]), "=&v"(pu[7]), "+v"(ac[1][1]), "=&v"(ye[2]), "=&v"(ye[3]), "+v"(ac[1][2]), "=&v"(ze[0]), "=&v"(ze[1]), "+v"(ac[1][3]) : "v"(pu[8]), "a"(fw2[8][0]), "v"(vFj[2]), "s"(rsW), "s"(su[7]), "a"(fw2[8][1]), "v"(py[0][0][2]), "v"(bias), "v"(py[0][0][3]), "a"(fw2[8][2]), "v"(py[0][1][0]), "v"(py[0][2][0]), "v"(py[0][1][1]), "v"(py[0][2][1]), "a"(fw2[8][3]) : "memory"); SB();
  asm volatile("v_add_f32 %0, %1, %2"
      : "=&v"(ze[2]) : "v"(py[0][1][2]), "v"(py[0][2][2])); SB();
  WAITVB(12);
  SB();
  asm volatile("v_mfma_f32_16x16x32_bf16 %0, %9, %10, %0\n\tbuffer_load_dwordx4 %1, %11, %12, %13 offen offset:0 sc1\n\tv_mfma_f32_16x16x32_bf16 %2, %9, %14, %2\n\tv_add_f32 %3, %15, %16\n\tv_add_f32 %4, %4, %17\n\tv_mfma_f32_16x16x32_bf16 %5, %9, %18, %5\n\tv_add_f32 %6, %6, %19\n\tv_add_f32 %7, %7, %20\n\tv_mfma_f32_16x16x32_bf16 %8, %9, %21, %8"
      : "+v"(ac[1][0]), "=&v"(pu[8]), "+v"(ac[1][1]), "=&v"(ze[3]), "+v"(ye[0]), "+v"(ac[1][2]), "+v"(ye[1]), "+v"(ye[2]), "+v"(ac[1][3]) : "v"(pu[9]), "v"(fw2[9][0]), "v"(vFj[2]), "s"(rsW), "s"(su[8]), "v"(fw2[9][1]), "v"(py[0][1][3]), "v"(py[0][2][3]), "v"(py[0][3][0]), "v"(fw2[9][2]), "v"(py[0][3][1]), "v"(py[0][3][2]), "v"(fw2[9][3]) : "memory"); SB();
  asm volatile("v_add_f32 %0, %0, %1"
      : "+v"(ye[3]) : "v"(py[0][3][3])); SB();
  WAITVB(12);
  SB();
  asm volatile("v_mfma_f32_16x16x32_bf16 %0, %10, %11, %0\n\tbuffer_load_dwordx4 %1, %12, %13, %14 offen offset:0 sc1\n\tv_mfma_f32_16x16x32_bf16 %2, %10, %15, %2\n\tv_add_f32 %3, %3, %4\n\tv_add_f32 %5, %5, %16\n\tv_mfma_f32_16x16x32_bf16 %6, %10, %17, %6\n\tv_add_f32 %7, %7, %18\n\tv_add_f32 %8, %8, %19\n\tv_mfma_f32_16x16x32_bf16 %9, %10, %20, %9\n\tv_mul_f32 %4, 0xbfb8aa3b, %3"
      : "+v"(ac[1][0]), "=&v"(pu[9]), "+v"(ac[1][1]), "+v"(ye[0]), "+v"(ze[0]), "+v"(ye[1]), "+v"(ac[1][2]), "+v"(ye[2]), "+v"(ye[3]), "+v"(ac[1][3]) : "v"(pu[10]), "v"(fw2[10][0]), "v"(vFj[2]), "s"(rsW), "s"(su[9]), "v"(fw2[10][1]), "v"(ze[1]), "v"(fw2[10][2]), "v"(ze[2]), "v"(ze[3]), "v"(fw2[10][3]) : "memory"); SB();
  WAITVB(12);
  SB();
  asm volatile("v_mfma_f32_16x16x32_bf16 %0, %9, %10, %0\n\tbuffer_load_dwordx4 %1, %11, %12, %13 offen offset:0 sc1\n\tv_mfma_f32_16x16x32_bf16 %2, %9, %14, %2\n\tv_mul_f32 %3, 0xbfb8aa3b, %15\n\tv_mul_f32 %4, 0xbfb8aa3b, %16\n\tv_mfma_f32_16x16x32_bf16 %5, %9, %17, %5\n\tds_read_b128 %6, %18 offset:24576\n\tv_mul_f32 %7, 0xbfb8aa3b, %19\n\tv_mfma_f32_16x16x32_bf16 %8, %9, %20, %8"
      : "+v"(ac[1][0]), "=&v"(pu[10]), "+v"(ac[1][1]), "=&v"(ze[1]), "=&v"(ze[2]), "+v"(ac[1][2]), "=&a"(fwo[3]), "=&v"(ze[3]), "+v"(ac[1][3]) : "v"(pu[11]), "v"(fw2[11][0]), "v"(vFj[2]), "s"(rsW), "s"(su[10]), "v"(fw2[11][1]), "v"(ye[1]), "v"(ye[2]), "v"(fw2[11][2]), "v"(ro), "v"(ye[3]), "v"(fw2[11][3]) : "memory"); SB();
  WAITVB(12);
  SB();
  asm volatile("v_mfma_f32_16x16x32_bf16 %0, %8, %9, %0\n\tbuffer_load_dwordx4 %1, %10, %11, %12 offen offset:0 sc1\n\tv_mfma_f32_16x16x32_bf16 %2, %8, %13, %2\n\tv_exp_f32 %3, %3\n\tv_mfma_f32_16x16x32_bf16 %4, %8, %14, %4\n\tv_exp_f32 %5, %5\n\tv_mfma_f32_16x16x32_bf16 %6, %8, %15, %6\n\tds_read_b128 %7, %16 offset:1024"
      : "+v"(ac[1][0]), "=&v"(pu[11]), "+v"(ac[1][1]), "+v"(ze[0]), "+v"(ac[1][2]), "+v"(ze[1]), "+v"(ac[1][3]), "=&a"(fwo[4]) : "v"(pu[12]), "v"(fw2[12][0]), "v"(vFj[2]), "s"(rsW), "s"(su[11]), "v"(fw2[12][1]), "v"(fw2[12][2]), "v"(fw2[12][3]), "v"(ro) : "memory"); SB();
  WAITVB(12);
  SB();
  asm volatile("v_mfma_f32_16x16x32_bf16 %0, %9, %10, %0\n\tbuffer_load_dwordx4 %1, %11, %12, %13 offen offset:0 sc1\n\tv_mfma_f32_16x16x32_bf16 %2, %9, %14, %2\n\tv_exp_f32 %3, %3\n\tv_mfma_f32_16x16x32_bf16 %4, %9, %15, %4\n\tv_exp_f32 %5, %5\n\tv_mfma_f32_16x16x32_bf16 %6, %9, %16, %6\n\tv_add_f32 %7, 1.0, %7\n\tv_add_f32 %8, 1.0, %8"
      : "+v"(ac[1][0]), "=&v"(pu[12]), "+v"(ac[1][1]), "+v"(ze[2]), "+v"(ac[1][2]), "+v"(ze[3]), "+v"(ac[1][3]), "+v"(ze[0]), "+v"(ze[1]) : "v"(pu[13]), "v"(fw2[13][0]), "v"(vFj[2]), "s"(rsW), "s"(su[12]), "v"(fw2[13][1]), "v"(fw2[13][2]), "v"(fw2[13][3]) : "memory"); SB();
  STAMP(41);
  WAITVB(12);
  SB();
  asm volatile("v_mfma_f32_16x16x32_bf16 %0, %9, %10, 0\n\tbuffer_load_dwordx4 %1, %11, %12, %13 offen offset:0 sc1\n\tv_mfma_f32_16x16x32_bf16 %2, %9, %14, 0\n\tbuffer_load_dword %3, %15, %12, 0 offen sc1\n\tv_mfma_f32_16x16x32_bf16 %4, %9, %16, 0\n\tv_add_f32 %5, 1.0, %5\n\tv_add_f32 %6, 1.0, %6\n\tv_mfma_f32_16x16x32_bf16 %7, %9, %17, 0\n\tds_read_b128 %8, %18 offset:9216"
      : "=&v"(ac[0][0]), "=&v"(pu[13]), "=&v"(ac[0][1]), "=&v"(fo), "=&v"(ac[0][2]), "+v"(ze[2]), "+v"(ze[3]), "=&v"(ac[0][3]), "=&a"(fwo[5]) : "v"(pu[0]), "a"(fw2[0][0]), "v"(vFj[2]), "s"(rsW), "s"(su[13]), "a"(fw2[0][1]), "v"(vFO), "a"(fw2[0][2]), "a"(fw2[0][3]), "v"(ro) : "memory"); SB();
  WAITVB(13);
  SB();
  asm volatile("v_mfma_f32_16x16x32_bf16 %0, %7, %8, %0\n\tbuffer_load_dwordx4 %1, %9, %10, %11 offen offset:0 sc1\n\tv_mfma_f32_16x16x32_bf16 %2, %7, %12, %2\n\tds_write_b128 %13, %14 offset:18432\n\tv_mfma_f32_16x16x32_bf16 %3, %7, %15, %3\n\tv_rcp_f32 %4, %4\n\tv_mfma_f32_16x16x32_bf16 %5, %7, %16, %5\n\tds_read_b128 %6, %17 offset:17408"
      : "+v"(ac[0][0]), "=&v"(pu[0]), "+v"(ac[0][1]), "+v"(ac[0][2]), "+v"(ze[0]), "+v"(ac[0][3]), "=&a"(fwo[6]) : "v"(pu[1]), "a"(fw2[1][0]), "v"(vFj[3]), "s"(rsW), "s"(su[0]), "a"(fw2[1][1]), "v"(rwd), "v"(ac[1][0]), "a"(fw2[1][2]), "a"(fw2[1][3]), "v"(ro) : "memory"); SB();
  WAITVB(13);
  SB();
  asm volatile("v_mfma_f32_16x16x32_bf16 %0, %7, %8, %0\n\tbuffer_load_dwordx4 %1, %9, %10, %11 offen offset:0 sc1\n\tv_mfma_f32_16x16x32_bf16 %2, %7, %12, %2\n\tds_write_b128 %13, %14 offset:19456\n\tv_mfma_f32_16x16x32_bf16 %3, %7, %15, %3\n\tv_rcp_f32 %4, %4\n\tv_mfma_f32_16x16x32_bf16 %5, %7, %16, %5\n\tds_read_b128 %6, %17 offset:25600"
      : "+v"(ac[0][0]), "=&v"(pu[1]), "+v"(ac[0][1]), "+v"(ac[0][2]), "+v"(ze[1]), "+v"(ac[0][3]), "=&a"(fwo[7]) : "v"(pu[2]), "a"(fw2[2][0]), "v"(vFj[3]), "s"(rsW), "s"(su[1]), "a"(fw2[2][1]), "v"(rwd), "v"(ac[1][1]), "a"(fw2[2][2]), "a"(fw2[2][3]), "v"(ro) : "memory"); SB();
  WAITVB(13);
  SB();
  asm volatile("v_mfma_f32_16x16x32_bf16 %0, %7, %8, %0\n\tbuffer_load_dwordx4 %1, %9, %10, %11 offen offset:0 sc1\n\tv_mfma_f32_16x16x32_bf16 %2, %7, %12, %2\n\tds_write_b128 %13, %14 offset:20480\n\tv_mfma_f32_16x16x32_bf16 %3, %7, %15, %3\n\tv_rcp_f32 %4, %4\n\tv_mfma_f32_16x16x32_bf16 %5, %7, %16, %5\n\tds_read_b128 %6, %17 offset:2048"
      : "+v"(ac[0][0]), "=&v"(pu[2]), "+v"(ac[0][1]), "+v"(ac[0][2]), "+v"(ze[2]), "+v"(ac[0][3]), "=&a"(fwo[8]) : "v"(pu[3]), "a"(fw2[3][0]), "v"(vFj[3]), "s"(rsW), "s"(su[2]), "a"(fw2[3][1]), "v"(rwd), "v"(ac[1][2]), "a"(fw2[3][2]), "a"(fw2[3][3]), "v"(ro) : "memory"); SB();
  WAITVB(13);
  SB();
  asm volatile("v_mfma_f32_16x16x32_bf16 %0, %7, %8, %0\n\tbuffer_load_dwordx4 %1, %9, %10, %11 offen offset:0 sc1\n\tv_mfma_f32_16x16x32_bf16 %2, %7, %12, %2\n\tds_write_b128 %13, %14 offset:21504\n\tv_mfma_f32_16x16x32_bf16 %3, %7, %15, %3\n\tv_rcp_f32 %4, %4\n\tv_mfma_f32_16x16x32_bf16 %5, %7, %16, %5\n\tv_mul_f32 %6, %17, %18"
      : "+v"(ac[0][0]), "=&v"(pu[3]), "+v"(ac[0][1]), "+v"(ac[0][2]), "+v"(ze[3]), "+v"(ac[0][3]), "=&v"(ge[0]) : "v"(pu[4]), "a"(fw2[4][0]), "v"(vFj[3]), "s"(rsW), "s"(su[3]), "a"(fw2[4][1]), "v"(rwd), "v"(ac[1][3]), "a"(fw2[4][2]), "a"(fw2[4][3]), "v"(ye[0]), "v"(ze[0]) : "memory"); SB();
  WAITVB(13);
  SB();
  asm volatile("v_mfma_f32_16x16x32_bf16 %0, %3, %4, %0\n\tbuffer_load_dwordx4 %1, %5, %6, %7 offen offset:0 sc1\n\tv_mfma_f32_16x16x32_bf16 %2, %3, %8, %2"
      : "+v"(ac[0][0]), "=&v"(pu[4]), "+v"(ac[0][1]) : "v"(pu[5]), "a"(fw2[5][0]), "v"(vFj[3]), "s"(rsW), "s"(su[4]), "a"(fw2[5][1]) : "memory"); SB();
  WAITL(0);
  BAR0();
  SB();
  asm volatile("v_mfma_f32_16x16x32_bf16 %0, %5, %6, %0\n\tds_read_b128 %1, %7 offset:18432\n\tds_read_b128 %2, %7 offset:22528\n\tv_mfma_f32_16x16x32_bf16 %3, %5, %8, %3\n\tds_read_b128 %4, %7 offset:26624"
      : "+v"(ac[0][2]), "=&v"(py[1][0]), "=&v"(py[1][1]), "+v"(ac[0][3]), "=&v"(py[1][2]) : "v"(pu[5]), "a"(fw2[5][2]), "v"(rr), "a"(fw2[5][3]) : "memory"); SB();
  WAITVB(13);
  SB();
  asm volatile("v_mfma_f32_16x16x32_bf16 %0, %10, %11, %0\n\tbuffer_load_dwordx4 %1, %12, %13, %14 offen offset:0 sc1\n\tv_mfma_f32_16x16x32_bf16 %2, %10, %15, %2\n\tds_read_b128 %3, %16 offset:30720\n\tv_mul_f32 %4, %17, %18\n\tv_mfma_f32_16x16x32_bf16 %5, %10, %19, %5\n\tv_mul_f32 %6, %20, %21\n\tv_mul_f32 %7, %22, %23\n\tv_mfma_f32_16x16x32_bf16 %8, %10, %24, %8\n\tds_read_b128 %9, %25 offset:10240"
      : "+v"(ac[0][0]), "=&v"(pu[5]), "+v"(ac[0][1]), "=&v"(py[1][3]), "=&v"(ge[1]), "+v"(ac[0][2]), "=&v"(ge[2]), "=&v"(ge[3]), "+v"(ac[0][3]), "=&a"(fwo[9]) : "v"(pu[6]), "a"(fw2[6][0]), "v"(vFj[3]), "s"(rsW), "s"(su[5]), "a"(fw2[6][1]), "v"(rr), "v"(ye[1]), "v"(ze[1]), "a"(fw2[6][2]), "v"(ye[2]), "v"(ze[2]), "v"(ye[3]), "v"(ze[3]), "a"(fw2[6][3]), "v"(ro) : "memory"); SB();
  WAITVB(13);
  SB();
  asm volatile("v_mfma_f32_16x16x32_bf16 %0, %5, %6, %0\n\tbuffer_load_dwordx4 %1, %7, %8, %9 offen offset:0 sc1\n\tv_mfma_f32_16x16x32_bf16 %2, %5, %10, %2\n\tds_read_b128 %3, %11 offset:18432\n\tv_mfma_f32_16x16x32_bf16 %4, %5, %12, %4"
      : "+v"(ac[0][0]), "=&v"(pu[6]), "+v"(ac[0][1]), "=&a"(fwo[10]), "+v"(ac[0][2]) : "v"(pu[7]), "a"(fw2[7][0]), "v"(vFj[3]), "s"(rsW), "s"(su[6]), "a"(fw2[7][1]), "v"(ro), "a"(fw2[7][2]) : "memory"); SB();
  WAITL(2);
  asm volatile("v_add_f32 %0, %3, %4\n\tv_mfma_f32_16x16x32_bf16 %1, %5, %6, %1\n\tv_add_f32 %2, %7, %4"
      : "=&v"(ye[0]), "+v"(ac[0][3]), "=&v"(ye[1]) : "v"(py[1][0][0]), "v"(bias), "v"(pu[7]), "a"(fw2[7][3]), "v"(py[1][0][1])); SB();
  WAITVB(13);
  SB();
  asm volatile("v_mfma_f32_16x16x32_bf16 %0, %9, %10, %0\n\tbuffer_load_dwordx4 %1, %11, %12, %13 offen offset:0 sc1\n\tv_mfma_f32_16x16x32_bf16 %2, %9, %14, %2\n\tv_add_f32 %3, %15, %16\n\tv_add_f32 %4, %17, %16\n\tv_mfma_f32_16x16x32_bf16 %5, %9, %18, %5\n\tv_add_f32 %6, %19, %20\n\tv_add_f32 %7, %21, %22\n\tv_mfma_f32_16x16x32_bf16 %8, %9, %23, %8"
      : "+v"(ac[0][0]), "=&v"(pu[7]), "+v"(ac[0][1]), "=&v"(ye[2]), "=&v"(ye[3]), "+v"(ac[0][2]), "=&v"(ze[0]), "=&v"(ze[1]), "+v"(ac[0][3]) : "v"(pu[8]), "a"(fw2[8][0]), "v"(vFj[3]), "s"(rsW), "s"(su[7]), "a"(fw2[8][1]), "v"(py[1][0][2]), "v"(bias), "v"(py[1][0][3]), "a"(fw2[8][2]), "v"(py[1][1][0]), "v"(py[1][2][0]), "v"(py[1][1][1]), "v"(py[1][2][1]), "a"(fw2[8][3]) : "memory"); SB();
  asm volatile("v_add_f32 %0, %1, %2"
      : "=&v"(ze[2]) : "v"(py[1][1][2]), "v"(py[1][2][2])); SB();
  WAITVB(13);
  SB();
  asm volatile("v_mfma_f32_16x16x32_bf16 %0, %9, %10, %0\n\tbuffer_load_dwordx4 %1, %11, %12, %13 offen offset:0 sc1\n\tv_mfma_f32_16x16x32_bf16 %2, %9, %14, %2\n\tv_add_f32 %3, %15, %16\n\tv_add_f32 %4, %4, %17\n\tv_mfma_f32_16x16x32_bf16 %5, %9, %18, %5\n\tv_add_f32 %6, %6, %19\n\tv_add_f32 %7, %7, %20\n\tv_mfma_f32_16x16x32_bf16 %8, %9, %21, %8"
      : "+v"(ac[0][0]), "=&v"(pu[8]), "+v"(ac[0][1]), "=&v"(ze[3]), "+v"(ye[0]), "+v"(ac[0][2]), "+v"(ye[1]), "+v"(ye[2]), "+v"(ac[0][3]) : "v"(pu[9]), "v"(fw2[9][0]), "v"(vFj[3]), "s"(rsW), "s"(su[8]), "v"(fw2[9][1]), "v"(py[1][1][3]), "v"(py[1][2][3]), "v"(py[1][3][0]), "v"(fw2[9][2]), "v"(py[1][3][1]), "v"(py[1][3][2]), "v"(fw2[9][3]) : "memory"); SB();
  asm volatile("v_add_f32 %0, %0, %1"
      : "+v"(ye[3]) : "v"(py[1][3][3])); SB();
  WAITVB(13);
  SB();
  asm volatile("v_mfma_f32_16x16x32_bf16 %0, %10, %11, %0\n\tbuffer_load_dwordx4 %1, %12, %13, %14 offen offset:0 sc1\n\tv_mfma_f32_16x16x32_bf16 %2, %10, %15, %2\n\tv_add_f32 %3, %3, %4\n\tv_add_f32 %5, %5, %16\n\tv_mfma_f32_16x16x32_bf16 %6, %10, %17, %6\n\tv_add_f32 %7, %7, %18\n\tv_add_f32 %8, %8, %19\n\tv_mfma_f32_16x16x32_bf16 %9, %10, %20, %9\n\tv_mul_f32 %4, 0xbfb8aa3b, %3"
      : "+v"(ac[0][0]), "=&v"(pu[9]), "+v"(ac[0][1]), "+v"(ye[0]), "+v"(ze[0]), "+v"(ye[1]), "+v"(ac[0][2]), "+v"(ye[2]), "+v"(ye[3]), "+v"(ac[0][3]) : "v"(pu[10]), "v"(fw2[10][0]), "v"(vFj[3]), "s"(rsW), "s"(su[9]), "v"(fw2[10][1]), "v"(ze[1]), "v"(fw2[10][2]), "v"(ze[2]), "v"(ze[3]), "v"(fw2[10][3]) : "memory"); SB();
  WAITVB(13);
  SB();
  asm volatile("v_mfma_f32_16x16x32_bf16 %0, %9, %10, %0\n\tbuffer_load_dwordx4 %1, %11, %12, %13 offen offset:0 sc1\n\tv_mfma_f32_16x16x32_bf16 %2, %9, %14, %2\n\tv_mul_f32 %3, 0xbfb8aa3b, %15\n\tv_mul_f32 %4, 0xbfb8aa3b, %16\n\tv_mfma_f32_16x16x32_bf16 %5, %9, %17, %5\n\tds_read_b128 %6, %18 offset:26624\n\tv_mul_f32 %7, 0xbfb8aa3b, %19\n\tv_mfma_f32_16x16x32_bf16 %8, %9, %20, %8"
      : "+v"(ac[0][0]), "=&v"(pu[10]), "+v"(ac[0][1]), "=&v"(ze[1]), "=&v"(ze[2]), "+v"(ac[0][2]), "=&a"(fwo[11]), "=&v"(ze[3]), "+v"(ac[0][3]) : "v"(pu[11]), "v"(fw2[11][0]), "v"(vFj[3]), "s"(rsW), "s"(su[10]), "v"(fw2[11][1]), "v"(ye[1]), "v"(ye[2]), "v"(fw2[11][2]), "v"(ro), "v"(ye[3]), "v"(fw2[11][3]) : "memory"); SB();
  WAITVB(13);
  SB();
  asm volatile("v_mfma_f32_16x16x32_bf16 %0, %8, %9, %0\n\tbuffer_load_dwordx4 %1, %10, %11, %12 offen offset:0 sc1\n\tv_mfma_f32_16x16x32_bf16 %2, %8, %13, %2\n\tv_exp_f32 %3, %3\n\tv_mfma_f32_16x16x32_bf16 %4, %8, %14, %4\n\tv_exp_f32 %5, %5\n\tv_mfma_f32_16x16x32_bf16 %6, %8, %15, %6\n\tds_read_b128 %7, %16 offset:3072"
      : "+v"(ac[0][0]), "=&v"(pu[11]), "+v"(ac[0][1]), "+v"(ze[0]), "+v"(ac[0][2]), "+v"(ze[1]), "+v"(ac[0][3]), "=&a"(fwo[12]) : "v"(pu[12]), "v"(fw2[12][0]), "v"(vFj[3]), "s"(rsW), "s"(su[11]), "v"(fw2[12][1]), "v"(fw2[12][2]), "v"(fw2[12][3]), "v"(ro) : "memory"); SB();
  WAITVB(13);
  SB();
  asm volatile("v_mfma_f32_16x16x32_bf16 %0, %9, %10, %0\n\tbuffer_load_dwordx4 %1, %11, %12, %13 offen offset:0 sc1\n\tv_mfma_f32_16x16x32_bf16 %2, %9, %14, %2\n\tv_exp_f32 %3, %3\n\tv_mfma_f32_16x16x32_bf16 %4, %9, %15, %4\n\tv_exp_f32 %5, %5\n\tv_mfma_f32_16x16x32_bf16 %6, %9, %16, %6\n\tv_add_f32 %7, 1.0, %7\n\tv_add_f32 %8, 1.0, %8"
      : "+v"(ac[0][0]), "=&v"(pu[12]), "+v"(ac[0][1]), "+v"(ze[2]), "+v"(ac[0][2]), "+v"(ze[3]), "+v"(ac[0][3]), "+v"(ze[0]), "+v"(ze[1]) : "v"(pu[13]), "v"(fw2[13][0]), "v"(vFj[3]), "s"(rsW), "s"(su[12]), "v"(fw2[13][1]), "v"(fw2[13][2]), "v"(fw2[13][3]) : "memory"); SB();
  STAMP(42);
  WAITVB(12);
  SB();
  asm volatile("v_mfma_f32_16x16x32_bf16 %0, %9, %10, 0\n\tbuffer_load_dwordx4 %1, %11, %12, %13 offen offset:0 sc1\n\tv_mfma_f32_16x16x32_bf16 %2, %9, %14, 0\n\tv_add_f32 %3, 1.0, %3\n\tv_add_f32 %4, 1.0, %4\n\tv_mfma_f32_16x16x32_bf16 %5, %9, %15, 0\n\tv_rcp_f32 %6, %6\n\tv_mfma_f32_16x16x32_bf16 %7, %9, %16, 0\n\tds_read_b128 %8, %17 offset:11264"
      : "=&v"(ac[1][0]), "=&v"(pu[13]), "=&v"(ac[1][1]), "+v"(ze[2]), "+v"(ze[3]), "=&v"(ac[1][2]), "+v"(ze[0]), "=&v"(ac[1][3]), "=&a"(fwo[13]) : "v"(pu[0]), "a"(fw2[0][0]), "v"(vFj[3]), "s"(rsW), "s"(su[13]), "a"(fw2[0][1]), "a"(fw2[0][2]), "a"(fw2[0][3]), "v"(ro) : "memory"); SB();
  WAITVB(12);
  SB();
  asm volatile("v_mfma_f32_16x16x32_bf16 %0, %1, %2, %0"
      : "+v"(ac[1][0]) : "v"(pu[1]), "a"(fw2[1][0])); SB();
  WAITV1(14, fo);
  if (__builtin_expect(__ballot(fo != nonce) != 0ull, 0)) poll1<4>(rsW, vFO, nonce);
  STAMP(18);
  asm volatile("buffer_load_dwordx4 %0, %6, %7, %8 offen offset:0 sc1\n\tv_mfma_f32_16x16x32_bf16 %1, %9, %10, %1\n\tds_write_b128 %11, %12 offset:0\n\tv_mfma_f32_16x16x32_bf16 %2, %9, %13, %2\n\tv_rcp_f32 %3, %3\n\tv_mfma_f32_16x16x32_bf16 %4, %9, %14, %4\n\tds_read_b128 %5, %15 offset:19456"
      : "=&v"(pu[0]), "+v"(ac[1][1]), "+v"(ac[1][2]), "+v"(ze[1]), "+v"(ac[1][3]), "=&a"(fwo[14]) : "v"(vF), "s"(rsW), "s"(so0), "v"(pu[1]), "a"(fw2[1][1]), "v"(rwd), "v"(ac[0][0]), "a"(fw2[1][2]), "a"(fw2[1][3]), "v"(ro) : "memory"); SB();
  WAITVB(12);
  SB();
  asm volatile("v_mfma_f32_16x16x32_bf16 %0, %7, %8, %0\n\tbuffer_load_dwordx4 %1, %9, %10, %11 offen offset:128 sc1\n\tv_mfma_f32_16x16x32_bf16 %2, %7, %12, %2\n\tds_write_b128 %13, %14 offset:1024\n\tv_mfma_f32_16x16x32_bf16 %3, %7, %15, %3\n\tv_rcp_f32 %4, %4\n\tv_mfma_f32_16x16x32_bf16 %5, %7, %16, %5\n\tds_read_b128 %6, %17 offset:27648"
      : "+v"(ac[1][0]), "=&v"(pu[1]), "+v"(ac[1][1]), "+v"(ac[1][2]), "+v"(ze[2]), "+v"(ac[1][3]), "=&a"(fwo[15]) : "v"(pu[2]), "a"(fw2[2][0]), "v"(vF), "s"(rsW), "s"(so0), "a"(fw2[2][1]), "v"(rwd), "v"(ac[0][1]), "a"(fw2[2][2]), "a"(fw2[2][3]), "v"(ro) : "memory"); SB();
  WAITVB(12);
  SB();
  asm volatile("v_mfma_f32_16x16x32_bf16 %0, %7, %8, %0\n\tbuffer_load_dwordx4 %1, %9, %10, %11 offen offset:256 sc1\n\tv_mfma_f32_16x16x32_bf16 %2, %7, %12, %2\n\tds_write_b128 %13, %14 offset:2048\n\tv_mfma_f32_16x16x32_bf16 %3, %7, %15, %3\n\tv_rcp_f32 %4, %4\n\tv_mfma_f32_16x16x32_bf16 %5, %7, %16, %5\n\tv_mul_f32 %6, %17, %18"
      : "+v"(ac[1][0]), "=&v"(pu[2]), "+v"(ac[1][1]), "+v"(ac[1][2]), "+v"(ze[3]), "+v"(ac[1][3]), "=&v"(ge[4]) : "v"(pu[3]), "a"(fw2[3][0]), "v"(vF), "s"(rsW), "s"(so0), "a"(fw2[3][1]), "v"(rwd), "v"(ac[0][2]), "a"(fw2[3][2]), "a"(fw2[3][3]), "v"(ye[0]), "v"(ze[0]) : "memory"); SB();
  WAITVB(12);
  SB();
  asm volatile("v_mfma_f32_16x16x32_bf16 %0, %8, %9, %0\n\tbuffer_load_dwordx4 %1, %10, %11, %12 offen offset:384 sc1\n\tv_mfma_f32_16x16x32_bf16 %2, %8, %13, %2\n\tds_write_b128 %14, %15 offset:3072\n\tv_mfma_f32_16x16x32_bf16 %3, %8, %16, %3\n\tv_mul_f32 %4, %17, %18\n\tv_mul_f32 %5, %19, %20\n\tv_mfma_f32_16x16x32_bf16 %6, %8, %21, %6\n\tv_mul_f32 %7, %22, %23"
      : "+v"(ac[1][0]), "=&v"(pu[3]), "+v"(ac[1][1]), "+v"(ac[1][2]), "=&v"(ge[5]), "=&v"(ge[6]), "+v"(ac[1][3]), "=&v"(ge[7]) : "v"(pu[4]), "a"(fw2[4][0]), "v"(vF), "s"(rsW), "s"(so0), "a"(fw2[4][1]), "v"(rwd), "v"(ac[0][3]), "a"(fw2[4][2]), "v"(ye[1]), "v"(ze[1]), "v"(ye[2]), "v"(ze[2]), "a"(fw2[4][3]), "v"(ye[3]), "v"(ze[3]) : "memory"); SB();
  WAITVB(12);
  SB();
  asm volatile("v_mfma_f32_16x16x32_bf16 %0, %3, %4, %0\n\tbuffer_load_dwordx4 %1, %5, %6, %7 offen offset:512 sc1\n\tv_mfma_f32_16x16x32_bf16 %2, %3, %8, %2"
      : "+v"(ac[1][0]), "=&v"(pu[4]), "+v"(ac[1][1]) : "v"(pu[5]), "a"(fw2[5][0]), "v"(vF), "s"(rsW), "s"(so0), "a"(fw2[5][1]) : "memory"); SB();
  WAITL(0);
  BAR0();
  SB();
  asm volatile("v_mfma_f32_16x16x32_bf16 %0, %5, %6, %0\n\tds_read_b128 %1, %7 offset:0\n\tds_read_b128 %2, %7 offset:4096\n\tv_mfma_f32_16x16x32_bf16 %3, %5, %8, %3\n\tds_read_b128 %4, %7 offset:8192"
      : "+v"(ac[1][2]), "=&v"(py[2][0]), "=&v"(py[2][1]), "+v"(ac[1][3]), "=&v"(py[2][2]) : "v"(pu[5]), "a"(fw2[5][2]), "v"(rr), "a"(fw2[5][3]) : "memory"); SB();
  WAITVB(12);
  SB();
  asm volatile("v_mfma_f32_16x16x32_bf16 %0, %8, %9, %0\n\tbuffer_load_dwordx4 %1, %10, %11, %12 offen offset:640 sc1\n\tv_mfma_f32_16x16x32_bf16 %2, %8, %13, %2\n\tds_read_b128 %3, %14 offset:12288\n\tds_read_b128 %4, %15 offset:4096\n\tv_mfma_f32_16x16x32_bf16 %5, %8, %16, %5\n\tv_mfma_f32_16x16x32_bf16 %6, %8, %17, %6\n\tds_read_b128 %7, %15 offset:12288"
      : "+v"(ac[1][0]), "=&v"(pu[5]), "+v"(ac[1][1]), "=&v"(py[2][3]), "=&a"(fwo[16]), "+v"(ac[1][2]), "+v"(ac[1][3]), "=&a"(fwo[17]) : "v"(pu[6]), "a"(fw2[6][0]), "v"(vF), "s"(rsW), "s"(so0), "a"(fw2[6][1]), "v"(rr), "v"(ro), "a"(fw2[6][2]), "a"(fw2[6][3]) : "memory"); SB();
  WAITVB(12);
  SB();
  asm volatile("v_mfma_f32_16x16x32_bf16 %0, %5, %6, %0\n\tbuffer_load_dwordx4 %1, %7, %8, %9 offen offset:768 sc1\n\tv_mfma_f32_16x16x32_bf16 %2, %5, %10, %2\n\tds_read_b128 %3, %11 offset:20480\n\tv_mfma_f32_16x16x32_bf16 %4, %5, %12, %4"
      : "+v"(ac[1][0]), "=&v"(pu[6]), "+v"(ac[1][1]), "=&a"(fwo[18]), "+v"(ac[1][2]) : "v"(pu[7]), "a"(fw2[7][0]), "v"(vF), "s"(rsW), "s"(so0), "a"(fw2[7][1]), "v"(ro), "a"(fw2[7][2]) : "memory"); SB();
  WAITL(3);
  asm volatile("v_add_f32 %0, %3, %4\n\tv_mfma_f32_16x16x32_bf16 %1, %5, %6, %1\n\tv_add_f32 %2, %7, %4"
      : "=&v"(ye[0]), "+v"(ac[1][3]), "=&v"(ye[1]) : "v"(py[2][0][0]), "v"(bias), "v"(pu[7]), "a"(fw2[7][3]), "v"(py[2][0][1])); SB();
  WAITVB(12);
  SB();
  asm volatile("v_mfma_f32_16x16x32_bf16 %0, %9, %10, %0\n\tbuffer_load_dwordx4 %1, %11, %12, %13 offen offset:0 sc1\n\tv_mfma_f32_16x16x32_bf16 %2, %9, %14, %2\n\tv_add_f32 %3, %15, %16\n\tv_add_f32 %4, %17, %16\n\tv_mfma_f32_16x16x32_bf16 %5, %9, %18, %5\n\tv_add_f32 %6, %19, %20\n\tv_add_f32 %7, %21, %22\n\tv_mfma_f32_16x16x32_bf16 %8, %9, %23, %8"
      : "+v"(ac[1][0]), "=&v"(pu[7]), "+v"(ac[1][1]), "=&v"(ye[2]), "=&v"(ye[3]), "+v"(ac[1][2]), "=&v"(ze[0]), "=&v"(ze[1]), "+v"(ac[1][3]) : "v"(pu[8]), "a"(fw2[8][0]), "v"(vF), "s"(rsW), "s"(so1), "a"(fw2[8][1]), "v"(py[2][0][2]), "v"(bias), "v"(py[2][0][3]), "a"(fw2[8][2]), "v"(py[2][1][0]), "v"(py[2][2][0]), "v"(py[2][1][1]), "v"(py[2][2][1]), "a"(fw2[8][3]) : "memory"); SB();
  asm volatile("v_add_f32 %0, %1, %2"
      : "=&v"(ze[2]) : "v"(py[2][1][2]), "v"(py[2][2][2])); SB();
  WAITVB(12);
  SB();
  asm volatile("v_mfma_f32_16x16x32_bf16 %0, %9, %10, %0\n\tbuffer_load_dwordx4 %1, %11, %12, %13 offen offset:128 sc1\n\tv_mfma_f32_16x16x32_bf16 %2, %9, %14, %2\n\tv_add_f32 %3, %15, %16\n\tv_add_f32 %4, %4, %17\n\tv_mfma_f32_16x16x32_bf16 %5, %9, %18, %5\n\tv_add_f32 %6, %6, %19\n\tv_add_f32 %7, %7, %20\n\tv_mfma_f32_16x16x32_bf16 %8, %9, %21, %8"
      : "+v"(ac[1][0]), "=&v"(pu[8]), "+v"(ac[1][1]), "=&v"(ze[3]), "+v"(ye[0]), "+v"(ac[1][2]), "+v"(ye[1]), "+v"(ye[2]), "+v"(ac[1][3]) : "v"(pu[9]), "v"(fw2[9][0]), "v"(vF), "s"(rsW), "s"(so1), "v"(fw2[9][1]), "v"(py[2][1][3]), "v"(py[2][2][3]), "v"(py[2][3][0]), "v"(fw2[9][2]), "v"(py[2][3][1]), "v"(py[2][3][2]), "v"(fw2[9][3]) : "memory"); SB();
  asm volatile("v_add_f32 %0, %0, %1"
      : "+v"(ye[3]) : "v"(py[2][3][3])); SB();
  WAITVB(12);
  SB();
  asm volatile("v_mfma_f32_16x16x32_bf16 %0, %10, %11, %0\n\tbuffer_load_dwordx4 %1, %12, %13, %14 offen offset:256 sc1\n\tv_mfma_f32_16x16x32_bf16 %2, %10, %15, %2\n\tv_add_f32 %3, %3, %4\n\tv_add_f32 %5, %5, %16\n\tv_mfma_f32_16x16x32_bf16 %6, %10, %17, %6\n\tv_add_f32 %7, %7, %18\n\tv_add_f32 %8, %8, %19\n\tv_mfma_f32_16x16x32_bf16 %9, %10, %20, %9\n\tv_mul_f32 %4, 0xbfb8aa3b, %3"
      : "+v"(ac[1][0]), "=&v"(pu[9]), "+v"(ac[1][1]), "+v"(ye[0]), "+v"(ze[0]), "+v"(ye[1]), "+v"(ac[1][2]), "+v"(ye[2]), "+v"(ye[3]), "+v"(ac[1][3]) : "v"(pu[10]), "v"(fw2[10][0]), "v"(vF), "s"(rsW), "s"(so1), "v"(fw2[10][1]), "v"(ze[1]), "v"(fw2[10][2]), "v"(ze[2]), "v"(ze[3]), "v"(fw2[10][3]) : "memory"); SB();
  WAITVB(12);
  SB();
  asm volatile("v_mfma_f32_16x16x32_bf16 %0, %9, %10, %0\n\tbuffer_load_dwordx4 %1, %11, %12, %13 offen offset:384 sc1\n\tv_mfma_f32_16x16x32_bf16 %2, %9, %14, %2\n\tv_mul_f32 %3, 0xbfb8aa3b, %15\n\tv_mul_f32 %4, 0xbfb8aa3b, %16\n\tv_mfma_f32_16x16x32_bf16 %5, %9, %17, %5\n\tds_read_b128 %6, %18 offset:28672\n\tv_mul_f32 %7, 0xbfb8aa3b, %19\n\tv_mfma_f32_16x16x32_bf16 %8, %9, %20, %8"
      : "+v"(ac[1][0]), "=&v"(pu[10]), "+v"(ac[1][1]), "=&v"(ze[1]), "=&v"(ze[2]), "+v"(ac[1][2]), "=&a"(fwo[19]), "=&v"(ze[3]), "+v"(ac[1][3]) : "v"(pu[11]), "v"(fw2[11][0]), "v"(vF), "s"(rsW), "s"(so1), "v"(fw2[11][1]), "v"(ye[1]), "v"(ye[2]), "v"(fw2[11][2]), "v"(ro), "v"(ye[3]), "v"(fw2[11][3]) : "memory"); SB();
  WAITVB(12);
  SB();
  asm volatile("v_mfma_f32_16x16x32_bf16 %0, %8, %9, %0\n\tbuffer_load_dwordx4 %1, %10, %11, %12 offen offset:512 sc1\n\tv_mfma_f32_16x16x32_bf16 %2, %8, %13, %2\n\tv_exp_f32 %3, %3\n\tv_mfma_f32_16x16x32_bf16 %4, %8, %14, %4\n\tv_exp_f32 %5, %5\n\tv_mfma_f32_16x16x32_bf16 %6, %8, %15, %6\n\tds_read_b128 %7, %16 offset:5120"
      : "+v"(ac[1][0]), "=&v"(pu[11]), "+v"(ac[1][1]), "+v"(ze[0]), "+v"(ac[1][2]), "+v"(ze[1]), "+v"(ac[1][3]), "=&a"(fwo[20]) : "v"(pu[12]), "v"(fw2[12][0]), "v"(vF), "s"(rsW), "s"(so1), "v"(fw2[12][1]), "v"(fw2[12][2]), "v"(fw2[12][3]), "v"(ro) : "memory"); SB();
  WAITVB(12);
  SB();
  asm volatile("v_mfma_f32_16x16x32_bf16 %0, %9, %10, %0\n\tbuffer_load_dwordx4 %1, %11, %12, %13 offen offset:640 sc1\n\tv_mfma_f32_16x16x32_bf16 %2, %9, %14, %2\n\tv_exp_f32 %3, %3\n\tv_mfma_f32_16x16x32_bf16 %4, %9, %15, %4\n\tv_exp_f32 %5, %5\n\tv_mfma_f32_16x16x32_bf16 %6, %9, %16, %6\n\tv_add_f32 %7, 1.0, %7\n\tv_add_f32 %8, 1.0, %8"
      : "+v"(ac[1][0]), "=&v"(pu[12]), "+v"(ac[1][1]), "+v"(ze[2]), "+v"(ac[1][2]), "+v"(ze[3]), "+v"(ac[1][3]), "+v"(ze[0]), "+v"(ze[1]) : "v"(pu[13]), "v"(fw2[13][0]), "v"(vF), "s"(rsW), "s"(so1), "v"(fw2[13][1]), "v"(fw2[13][2]), "v"(fw2[13][3]) : "memory"); SB();
  STAMP(43);
  WAITVB(12);
  SB();
  asm volatile("v_mfma_f32_16x16x32_bf16 %0, %9, %10, 0\n\tbuffer_load_dwordx4 %1, %11, %12, %13 offen offset:768 sc1\n\tv_mfma_f32_16x16x32_bf16 %2, %14, %10, 0\n\tv_add_f32 %3, 1.0, %3\n\tv_add_f32 %4, 1.0, %4\n\tv_mfma_f32_16x16x32_bf16 %5, %15, %10, 0\n\tv_rcp_f32 %6, %6\n\tv_mfma_f32_16x16x32_bf16 %7, %16, %10, 0\n\tds_read_b128 %8, %17 offset:13312"
      : "=&v"(as[0]), "=&v"(pu[13]), "=&v"(as[1]), "+v"(ze[2]), "+v"(ze[3]), "=&v"(as[2]), "+v"(ze[0]), "=&v"(as[3]), "=&a"(fwo[21]) : "a"(fwo[0]), "v"(pu[0]), "v"(vF), "s"(rsW), "s"(so1), "a"(fwo[1]), "a"(fwo[2]), "a"(fwo[3]), "v"(ro) : "memory"); SB();
  WAITVB(12);
  SB();
  asm volatile("v_mfma_f32_16x16x32_bf16 %0, %7, %8, %0\n\tds_write_b128 %9, %10 offset:18432\n\tv_mfma_f32_16x16x32_bf16 %1, %11, %8, %1\n\tv_rcp_f32 %2, %2\n\tv_mfma_f32_16x16x32_bf16 %3, %12, %8, %3\n\tv_rcp_f32 %4, %4\n\tv_mfma_f32_16x16x32_bf16 %5, %13, %8, %5\n\tds_read_b128 %6, %14 offset:21504"
      : "+v"(as[0]), "+v"(as[1]), "+v"(ze[1]), "+v"(as[2]), "+v"(ze[2]), "+v"(as[3]), "=&a"(fwo[22]) : "a"(fwo[4]), "v"(pu[1]), "v"(rwd), "v"(ac[1][0]), "a"(fwo[5]), "a"(fwo[6]), "a"(fwo[7]), "v"(ro) : "memory"); SB();
  WAITVB(11);
  SB();
  asm volatile("v_mfma_f32_16x16x32_bf16 %0, %8, %9, %0\n\tds_write_b128 %10, %11 offset:19456\n\tv_mfma_f32_16x16x32_bf16 %1, %12, %9, %1\n\tv_rcp_f32 %2, %2\n\tv_mfma_f32_16x16x32_bf16 %3, %13, %9, %3\n\tv_mul_f32 %4, %14, %15\n\tv_mul_f32 %5, %16, %17\n\tv_mfma_f32_16x16x32_bf16 %6, %18, %9, %6\n\tv_mul_f32 %7, %19, %20"
      : "+v"(as[0]), "+v"(as[1]), "+v"(ze[3]), "+v"(as[2]), "=&v"(ge[8]), "=&v"(ge[9]), "+v"(as[3]), "=&v"(ge[10]) : "a"(fwo[8]), "v"(pu[2]), "v"(rwd), "v"(ac[1][1]), "a"(fwo[9]), "a"(fwo[10]), "v"(ye[0]), "v"(ze[0]), "v"(ye[1]), "v"(ze[1]), "a"(fwo[11]), "v"(ye[2]), "v"(ze[2]) : "memory"); SB();
  WAITVB(10);
  SB();
  asm volatile("v_mfma_f32_16x16x32_bf16 %0, %6, %7, %0\n\tds_write_b128 %8, %9 offset:20480\n\tv_mfma_f32_16x16x32_bf16 %1, %10, %7, %1\n\tds_read_b128 %2, %11 offset:29696\n\tv_mul_f32 %3, %12, %13\n\tv_mfma_f32_16x16x32_bf16 %4, %14, %7, %4\n\tv_mfma_f32_16x16x32_bf16 %5, %15, %7, %5"
      : "+v"(as[0]), "+v"(as[1]), "=&a"(fwo[23]), "=&v"(ge[11]), "+v"(as[2]), "+v"(as[3]) : "a"(fwo[12]), "v"(pu[3]), "v"(rwd), "v"(ac[1][2]), "a"(fwo[13]), "v"(ro), "v"(ye[3]), "v"(ze[3]), "a"(fwo[14]), "a"(fwo[15]) : "memory"); SB();
  WAITVB(9);
  asm volatile("ds_read_b128 %0, %6 offset:6144\n\tds_read_b128 %1, %6 offset:14336\n\tds_read_b128 %2, %6 offset:22528\n\tds_read_b128 %3, %6 offset:30720\n\tds_read_b128 %4, %6 offset:7168\n\tds_read_b128 %5, %6 offset:15360"
      : "=&a"(fwo[24]), "=&a"(fwo[25]), "=&a"(fwo[26]), "=&a"(fwo[27]), "=&a"(fwo[28]), "=&a"(fwo[29]) : "v"(ro) : "memory"); SB();
  WAITL(6);
  SB();
  asm volatile("v_mfma_f32_16x16x32_bf16 %0, %6, %7, %0\n\tds_write_b128 %8, %9 offset:21504\n\tv_mfma_f32_16x16x32_bf16 %1, %10, %7, %1\n\tds_read_b128 %2, %11 offset:23552\n\tv_mfma_f32_16x16x32_bf16 %3, %12, %7, %3\n\tv_mfma_f32_16x16x32_bf16 %4, %13, %7, %4\n\tds_read_b128 %5, %11 offset:31744"
      : "+v"(as[0]), "+v"(as[1]), "=&a"(fwo[30]), "+v"(as[2]), "+v"(as[3]), "=&a"(fwo[31]) : "a"(fwo[16]), "v"(pu[4]), "v"(rwd), "v"(ac[1][3]), "a"(fwo[17]), "v"(ro), "a"(fwo[18]), "a"(fwo[19]) : "memory"); SB();
  WAITVB(8);
  SB();
  asm volatile("v_mfma_f32_16x16x32_bf16 %0, %1, %2, %0"
      : "+v"(as[0]) : "a"(fwo[20]), "v"(pu[5])); SB();
  WAITL(2);
  BAR0();
  SB();
  asm volatile("v_mfma_f32_16x16x32_bf16 %0, %7, %8, %0\n\tds_read_b128 %1, %9 offset:18432\n\tds_read_b128 %2, %9 offset:22528\n\tv_mfma_f32_16x16x32_bf16 %3, %10, %8, %3\n\tds_read_b128 %4, %9 offset:26624\n\tds_read_b128 %5, %9 offset:30720\n\tv_mfma_f32_16x16x32_bf16 %6, %11, %8, %6"
      : "+v"(as[1]), "=&v"(py[3][0]), "=&v"(py[3][1]), "+v"(as[2]), "=&v"(py[3][2]), "=&v"(py[3][3]), "+v"(as[3]) : "a"(fwo[21]), "v"(pu[5]), "v"(rr), "a"(fwo[22]), "a"(fwo[23]) : "memory"); SB();
  WAITVB(7);
  asm volatile("ds_read_b128 %0, %6 offset:32768\n\tds_read_b128 %1, %6 offset:38912\n\tds_read_b128 %2, %6 offset:45056\n\tds_read_b128 %3, %6 offset:51200\n\tds_read_b128 %4, %6 offset:33792\n\tds_read_b128 %5, %6 offset:39936"
      : "=&a"(fwo[32]), "=&a"(fwo[33]), "=&a"(fwo[34]), "=&a"(fwo[35]), "=&a"(fwo[36]), "=&a"(fwo[37]) : "v"(ro) : "memory"); SB();
  WAITL(10);
  SB();
  asm volatile("v_mfma_f32_16x16x32_bf16 %0, %6, %7, %0\n\tds_read_b128 %1, %8 offset:46080\n\tv_mfma_f32_16x16x32_bf16 %2, %9, %7, %2\n\tv_mfma_f32_16x16x32_bf16 %3, %10, %7, %3\n\tds_read_b128 %4, %8 offset:52224\n\tv_mfma_f32_16x16x32_bf16 %5, %11, %7, %5"
      : "+v"(as[0]), "=&a"(fwo[38]), "+v"(as[1]), "+v"(as[2]), "=&a"(fwo[39]), "+v"(as[3]) : "a"(fwo[24]), "v"(pu[6]), "v"(ro), "a"(fwo[25]), "a"(fwo[26]), "a"(fwo[27]) : "memory"); SB();
  WAITL(8);
  SB();
  WAITVB(6);
  SB();
  asm volatile("v_mfma_f32_16x16x32_bf16 %0, %10, %11, %0\n\tv_add_f32 %1, %12, %13\n\tv_add_f32 %2, %14, %13\n\tv_mfma_f32_16x16x32_bf16 %3, %15, %11, %3\n\tv_add_f32 %4, %16, %13\n\tv_add_f32 %5, %17, %13\n\tv_mfma_f32_16x16x32_bf16 %6, %18, %11, %6\n\tv_add_f32 %7, %19, %20\n\tv_add_f32 %8, %21, %22\n\tv_mfma_f32_16x16x32_bf16 %9, %23, %11, %9"
      : "+v"(as[0]), "=&v"(ye[0]), "=&v"(ye[1]), "+v"(as[1]), "=&v"(ye[2]), "=&v"(ye[3]), "+v"(as[2]), "=&v"(ze[0]), "=&v"(ze[1]), "+v"(as[3]) : "a"(fwo[28]), "v"(pu[7]), "v"(py[3][0][0]), "v"(bias), "v"(py[3][0][1]), "a"(fwo[29]), "v"(py[3][0][2]), "v"(py[3][0][3]), "a"(fwo[30]), "v"(py[3][1][0]), "v"(py[3][2][0]), "v"(py[3][1][1]), "v"(py[3][2][1]), "a"(fwo[31])); SB();
  WAITVB(5);
  asm volatile("ds_read_b128 %0, %6 offset:34816\n\tds_read_b128 %1, %6 offset:40960\n\tds_read_b128 %2, %6 offset:47104\n\tds_read_b128 %3, %6 offset:53248\n\tds_read_b128 %4, %6 offset:35840\n\tds_read_b128 %5, %6 offset:41984"
      : "=&a"(fwo[40]), "=&a"(fwo[41]), "=&a"(fwo[42]), "=&a"(fwo[43]), "=&a"(fwo[44]), "=&a"(fwo[45]) : "v"(ro) : "memory"); SB();
  WAITL(6);
  SB();
  asm volatile("v_mfma_f32_16x16x32_bf16 %0, %9, %10, %0\n\tv_add_f32 %1, %11, %12\n\tv_add_f32 %2, %13, %14\n\tv_mfma_f32_16x16x32_bf16 %3, %15, %10, %3\n\tv_add_f32 %4, %4, %16\n\tv_add_f32 %5, %5, %17\n\tv_mfma_f32_16x16x32_bf16 %6, %18, %10, %6\n\tv_add_f32 %7, %7, %19\n\tv_add_f32 %8, %8, %20"
      : "+v"(as[0]), "=&v"(ze[2]), "=&v"(ze[3]), "+v"(as[1]), "+v"(ye[0]), "+v"(ye[1]), "+v"(as[2]), "+v"(ye[2]), "+v"(ye[3]) : "a"(fwo[32]), "v"(pu[8]), "v"(py[3][1][2]), "v"(py[3][2][2]), "v"(py[3][1][3]), "v"(py[3][2][3]), "a"(fwo[33]), "v"(py[3][3][0]), "v"(py[3][3][1]), "a"(fwo[34]), "v"(py[3][3][2]), "v"(py[3][3][3])); SB();
  asm volatile("v_mfma_f32_16x16x32_bf16 %0, %2, %3, %0\n\tv_add_f32 %1, %1, %4"
      : "+v"(as[3]), "+v"(ye[0]) : "a"(fwo[35]), "v"(pu[8]), "v"(ze[0])); SB();
  WAITVB(4);
  SB();
  asm volatile("v_mfma_f32_16x16x32_bf16 %0, %10, %11, %0\n\tv_add_f32 %1, %1, %2\n\tv_add_f32 %3, %3, %4\n\tv_mfma_f32_16x16x32_bf16 %5, %12, %11, %5\n\tv_add_f32 %6, %6, %13\n\tv_mul_f32 %7, 0xbfb8aa3b, %14\n\tv_mfma_f32_16x16x32_bf16 %8, %15, %11, %8\n\tv_mul_f32 %2, 0xbfb8aa3b, %1\n\tv_mul_f32 %4, 0xbfb8aa3b, %3\n\tv_mfma_f32_16x16x32_bf16 %9, %16, %11, %9"
      : "+v"(as[0]), "+v"(ye[1]), "+v"(ze[1]), "+v"(ye[2]), "+v"(ze[2]), "+v"(as[1]), "+v"(ye[3]), "=&v"(ze[0]), "+v"(as[2]), "+v"(as[3]) : "a"(fwo[36]), "v"(pu[9]), "a"(fwo[37]), "v"(ze[3]), "v"(ye[0]), "a"(fwo[38]), "a"(fwo[39])); SB();
  WAITVB(3);
  asm volatile("ds_read_b128 %0, %8 offset:48128\n\tds_read_b128 %1, %8 offset:54272\n\tds_read_b128 %2, %8 offset:36864\n\tds_read_b128 %3, %8 offset:43008\n\tds_read_b128 %4, %8 offset:49152\n\tds_read_b128 %5, %8 offset:55296\n\tds_read_b128 %6, %8 offset:37888\n\tds_read_b128 %7, %8 offset:44032"
      : "=&a"(fwo[46]), "=&a"(fwo[47]), "=&a"(fwo[48]), "=&a"(fwo[49]), "=&a"(fwo[50]), "=&a"(fwo[51]), "=&a"(fwo[52]), "=&a"(fwo[53]) : "v"(ro) : "memory"); SB();
  WAITL(6);
  SB();
  asm volatile("v_mfma_f32_16x16x32_bf16 %0, %9, %10, %0\n\tds_read_b128 %1, %11 offset:50176\n\tv_mul_f32 %2, 0xbfb8aa3b, %12\n\tv_mfma_f32_16x16x32_bf16 %3, %13, %10, %3\n\tv_exp_f32 %4, %4\n\tv_mfma_f32_16x16x32_bf16 %5, %14, %10, %5\n\tv_exp_f32 %6, %6\n\tv_mfma_f32_16x16x32_bf16 %7, %15, %10, %7\n\tds_read_b128 %8, %11 offset:56320"
      : "+v"(as[0]), "=&a"(fwo[54]), "=&v"(ze[3]), "+v"(as[1]), "+v"(ze[0]), "+v"(as[2]), "+v"(ze[1]), "+v"(as[3]), "=&a"(fwo[55]) : "a"(fwo[40]), "v"(pu[10]), "v"(ro), "v"(ye[3]), "a"(fwo[41]), "a"(fwo[42]), "a"(fwo[43]) : "memory"); SB();
  WAITL(0);
  WAITVB(0);
  SB();
  asm volatile("v_mfma_f32_16x16x32_bf16 %0, %9, %10, %0\n\tv_exp_f32 %1, %1\n\tv_mfma_f32_16x16x32_bf16 %0, %11, %12, %0\n\tv_exp_f32 %2, %2\n\tv_mfma_f32_16x16x32_bf16 %0, %13, %14, %0\n\tv_add_f32 %3, 1.0, %3\n\tv_add_f32 %4, 1.0, %4\n\tv_mfma_f32_16x16x32_bf16 %5, %15, %10, %5\n\tv_add_f32 %1, 1.0, %1\n\tv_add_f32 %2, 1.0, %2\n\tv_mfma_f32_16x16x32_bf16 %5, %16, %12, %5\n\tv_rcp_f32 %3, %3\n\tv_mfma_f32_16x16x32_bf16 %5, %17, %14, %5\n\tv_rcp_f32 %4, %4\n\tv_mfma_f32_16x16x32_bf16 %6, %18, %10, %6\n\tv_rcp_f32 %1, %1\n\tv_mfma_f32_16x16x32_bf16 %6, %19, %12, %6\n\tv_rcp_f32 %2, %2\n\tv_mfma_f32_16x16x32_bf16 %6, %20, %14, %6\n\tv_mul_f32 %7, %21, %3\n\tv_mul_f32 %8, %22, %4"
      : "+v"(as[0]), "+v"(ze[2]), "+v"(ze[3]), "+v"(ze[0]), "+v"(ze[1]), "+v"(as[1]), "+v"(as[2]), "=&v"(ge[12]), "=&v"(ge[13]) : "a"(fwo[44]), "v"(pu[11]), "a"(fwo[48]), "v"(pu[12]), "a"(fwo[52]), "v"(pu[13]), "a"(fwo[45]), "a"(fwo[49]), "a"(fwo[53]), "a"(fwo[46]), "a"(fwo[50]), "a"(fwo[54]), "v"(ye[0]), "v"(ye[1])); SB();
  asm volatile("v_mfma_f32_16x16x32_bf16 %0, %7, %8, %0\n\tv_mul_f32 %1, %9, %10\n\tv_mul_f32 %2, %11, %12\n\tv_mfma_f32_16x16x32_bf16 %0, %13, %14, %0\n\tv_fma_f32 %3, %15, %16, %3\n\tv_fma_f32 %4, %17, %18, %4\n\tv_mfma_f32_16x16x32_bf16 %0, %19, %20, %0\n\tv_fma_f32 %5, %21, %22, %5\n\tv_fma_f32 %6, %23, %24, %6"
      : "+v"(as[3]), "=&v"(ge[14]), "=&v"(ge[15]), "+v"(xe[0]), "+v"(xe[1]), "+v"(xe[2]), "+v"(xe[3]) : "a"(fwo[47]), "v"(pu[11]), "v"(ye[2]), "v"(ze[2]), "v"(ye[3]), "v"(ze[3]), "a"(fwo[51]), "v"(pu[12]), "v"(as[0][0]), "v"(ge[0]), "v"(as[0][1]), "v"(ge[1]), "a"(fwo[55]), "v"(pu[13]), "v"(as[0][2]), "v"(ge[2]), "v"(as[0][3]), "v"(ge[3])); SB();
  STAMP(44);
  asm volatile("v_cvt_pk_bf16_f32 %0, %8, %9\n\tv_cvt_pk_bf16_f32 %1, %10, %11\n\tv_fma_f32 %2, %12, %13, %2\n\tv_fma_f32 %3, %14, %15, %3\n\tv_fma_f32 %4, %16, %17, %4\n\tv_fma_f32 %5, %18, %19, %5\n\tv_cvt_pk_bf16_f32 %6, %2, %3\n\tv_cvt_pk_bf16_f32 %7, %4, %5"
      : "=&v"(ov[0][0]), "=&v"(ov[0][1]), "+v"(xe[4]), "+v"(xe[5]), "+v"(xe[6]), "+v"(xe[7]), "=&v"(ov[1][0]), "=&v"(ov[1][1]) : "v"(xe[0]), "v"(xe[1]), "v"(xe[2]), "v"(xe[3]), "v"(as[1][0]), "v"(ge[4]), "v"(as[1][1]), "v"(ge[5]), "v"(as[1][2]), "v"(ge[6]), "v"(as[1][3]), "v"(ge[7])); SB();
  asm volatile("ds_write_b64 %8, %9 offset:0\n\tv_fma_f32 %0, %10, %11, %0\n\tv_fma_f32 %1, %12, %13, %1\n\tv_fma_f32 %2, %14, %15, %2\n\tv_fma_f32 %3, %16, %17, %3\n\tv_cvt_pk_bf16_f32 %4, %0, %1\n\tv_cvt_pk_bf16_f32 %5, %2, %3\n\tds_write_b64 %18, %19 offset:0\n\tv_fma_f32 %6, %20, %21, %6\n\tv_fma_f32 %7, %22, %23, %7"
      : "+v"(xe[8]), "+v"(xe[9]), "+v"(xe[10]), "+v"(xe[11]), "=&v"(ov[2][0]), "=&v"(ov[2][1]), "+v"(xe[12]), "+v"(xe[13]) : "v"(xta[0]), "v"(ov[0]), "v"(as[2][0]), "v"(ge[8]), "v"(as[2][1]), "v"(ge[9]), "v"(as[2][2]), "v"(ge[10]), "v"(as[2][3]), "v"(ge[11]), "v"(xta[1]), "v"(ov[1]), "v"(as[3][0]), "v"(ge[12]), "v"(as[3][1]), "v"(ge[13]) : "memory"); SB();
  asm volatile("v_fma_f32 %0, %4, %5, %0\n\tv_fma_f32 %1, %6, %7, %1\n\tv_cvt_pk_bf16_f32 %2, %8, %9\n\tv_cvt_pk_bf16_f32 %3, %0, %1\n\tds_write_b64 %10, %11 offset:0"
      : "+v"(xe[14]), "+v"(xe[15]), "=&v"(ov[3][0]), "=&v"(ov[3][1]) : "v"(as[3][2]), "v"(ge[14]), "v"(as[3][3]), "v"(ge[15]), "v"(xe[12]), "v"(xe[13]), "v"(xta[2]), "v"(ov[2]) : "memory"); SB();
  asm volatile("ds_write_b64 %0, %1 offset:0"
      :  : "v"(xta[3]), "v"(ov[3]) : "memory"); SB();
  { u32x4 t0_, t1_; DSR16(t0_, xo, 0); DSR16(t1_, xo, 1024); WAITLB(1);
    BS16(vXa, rsO_, 0, t0_, "sc1"); WAITLB(0); BS16(vXb, rsO_, 1024, t1_, "sc1"); }
  STAMP(23);

  STAMP(12);
  STAMP_DUMP();
  }
  } else if (jb < 28) {
  asm volatile("; NEED B");
  u32x4 rs1 = srd(w1, 2048u * 3584u);
  STAMP(1);
  unsigned nonce; asm volatile("s_load_dword %0, %1, 0x0" : "=&s"(nonce) : "s"(ws) : "memory");
  const unsigned trb = (8 * g4 + q4) * 128 + pq * 8, trf = ((q4 >> 1) | ((g4 & 1) << 1)) * 32;
  const unsigned vF = r8 * 1024 + (lane & 7) * 16;
  unsigned tX[4];
#pragma unroll
  for (int i = 0; i < 4; ++i) { tX[i] = ldsR + trb + ((i * 32 ^ w * 32) ^ trf); OPV(tX[i]); }
  bf16x8 fw1[16][4], p[14]; u32x2 bv; unsigned sh[16], sh0 = WS_HT + d4 * 524288; OPQ(sh0);
  asm volatile("s_waitcnt lgkmcnt(0)" : "+s"(nonce) :: "memory"); nonce += 1u;
  asm volatile("s_waitcnt vmcnt(8)"
      :  :  : "memory"); SB();
  fw1[0][0] = tr8u(tX[0] + 0, 512);
  fw1[0][1] = tr8u(tX[1] + 0, 512);
  fw1[0][2] = tr8u(tX[2] + 0, 512);
  fw1[0][3] = tr8u(tX[3] + 0, 512);
  STAMP(50);
  { int k_ = KS1(3); OPQ(k_); DMA4(vW1a, vW1b, rs1, k_ * 114688, 55296, ldsR, 12288); sh[3] = sh0 + k_ * 8192; OPQ(sh[3]); }
  SB();
  { const unsigned vB = (ft * 64 + w * 16 + g4 * 4) * 2;
    LD8(bv, vB, b1, 0); }
  u32x4 rsW = srd((char*)(((size_t)ws + 8191) & ~(size_t)4095) + 512, WS_END); OPQ(rsW);
  #pragma unroll
  for (int c = 0; c < 3; ++c) { sh[c] = sh0 + KS1(c) * 8192; OPQ(sh[c]); }
  unsigned fh, vFH = WS_FA + (d4 * 128 + w * 32 + (lane & 31)) * (4 * 16);
  unsigned rw = lds0 + w * SCW + vL16, rr1 = lds0 + (w ^ 1) * SCW + vL16, rr2 = lds0 + (w ^ 2) * SCW + vL16, rr3 = lds0 + (w ^ 3) * SCW + vL16;
  unsigned vU = (g4 & 1) * 128 + ((g4 >> 1) * 2 + (n16 >> 3)) * 1024 + (n16 & 7) * 16, sU, sFU = WS_FU + (d4 * 128 + ft * 4 + w) * (4 * 4);
  sU = WS_UT + (d4 * 56 + 2 * ft) * 8192 + w * 4096;
  const int sp1 = (fi & 1);
  unsigned vHa = vF + (d4 == 1 ? 128 : 0) + 128 * sp1, vHc = vF + (d4 == 1 ? 128 : 0) + 128 - 128 * sp1, vHb = vF + (d4 == 1 ? 0 : 768), vU6 = vU + (d4 == 1 ? 0 : 768); vU = (vU ^ (128 * sp1)) + (d4 == 1 ? 128 : 0); OPV(vHa); OPV(vHc); OPV(vHb); OPV(vU6);
  float gc = 0.10294324f;
  asm volatile("" : "+v"(vFH), "+v"(rw), "+v"(rr1), "+v"(rr2), "+v"(rr3), "+v"(vU), "+v"(gc)); asm volatile("" : "+s"(sU), "+s"(sFU));
  const int r2 = (3 - (int)((size_t)w2 >> 7)) & 7;
  const int st = 8 * xq + ((r2 + 1 + fi) & 7), dt = d4, db0 = 16 + 3 * dt;
#undef BLK
#define BLK(j) (db0 + ((j) == 2 ? 2 - 2 * (dt & 1) : ((j) ^ 0) + (dt & 1)))
  SB();
  asm volatile("s_waitcnt vmcnt(9)"
      :  :  : "memory"); SB();
  fw1[1][0] = tr8u(tX[0] + 4096, 512);
  fw1[1][1] = tr8u(tX[1] + 4096, 512);
  fw1[1][2] = tr8u(tX[2] + 4096, 512);
  fw1[1][3] = tr8u(tX[3] + 4096, 512);
  { int k_ = KS1(4); OPQ(k_); DMA4(vW1a, vW1b, rs1, k_ * 114688, 55296, ldsR, 16384); sh[4] = sh0 + k_ * 8192; OPQ(sh[4]); }
  SB();
  u32x4 rs2 = srd(w2, 1792u * 4096u); OPQ(rs2);
  unsigned vW2[4];
#pragma unroll
  for (int i = 0; i < 4; ++i) vW2[i] = (8 * (r8 & 3) + 4 * (r8 >> 2)) * 4096 + st * 128 + ((lane & 7) ^ (2 * i)) * 16 + i * (4096 - 1024);
  int rot2 = 4 * dt + 2 * (st & 7) + 2; rot2 -= 14 * ((rot2 * 37) >> 9);
  bf16x8 fw2[14][4];
  unsigned ldsS = lds0 + S3B + w * 4096, ldsO = lds0 + WOB + w * 8192, ldsP = lds0 + WOB + 24576 + w * 6144, sWo, vWo = r8 * 16384 + (lane & 7) * 16, sWt;
  { const int k_ = BLK(w), k7 = (k_ * 37) >> 8; sWo = WS_WO + k7 * (131072 - 896) + k_ * 128; }
  unsigned fq, vFQ = WS_FQ + (lane < 56 ? lane : 55) * (4 * 4);
  asm volatile("" : "+v"(vW2[0]), "+v"(vW2[1]), "+v"(vW2[2]), "+v"(vW2[3]), "+v"(vFQ), "+v"(vWo)); asm volatile("" : "+s"(rot2), "+s"(ldsS), "+s"(ldsO), "+s"(ldsP), "+s"(sWo));
  if (w == 3) vWo = 0x40000000u; OPV(vWo);
  unsigned tS[4];
#pragma unroll
  for (int i = 0; i < 4; ++i) { tS[i] = ldsS + q4 * 1024 + g4 * 128 + pq * 8 + ((i ^ q4) * 32); OPV(tS[i]); }
  unsigned tO[4];
#pragma unroll
  for (int i = 0; i < 4; ++i) { tO[i] = ldsO + q4 * 1024 + g4 * 128 + pq * 8 + ((i ^ q4) * 32); OPV(tO[i]); }
  unsigned su[14], sw[14], sc_ = 14 * w + rot2; OPQ(sc_);
  SB();
  asm volatile("s_waitcnt vmcnt(9)"
      :  :  : "memory"); SB();
  fw1[2][0] = tr8u(tX[0] + 8192, 512);
  fw1[2][1] = tr8u(tX[1] + 8192, 512);
  fw1[2][2] = tr8u(tX[2] + 8192, 512);
  fw1[2][3] = tr8u(tX[3] + 8192, 512);
  { int k_ = KS1(5); OPQ(k_); DMA4(vW1a, vW1b, rs1, k_ * 114688, 55296, ldsR, 20480); sh[5] = sh0 + k_ * 8192; OPQ(sh[5]); }
  SB();
  unsigned vFU, vFO;
  { const int da = (db0 * 37) >> 8, db = ((db0 + 2) * 37) >> 8, l_ = lane < 56 ? lane : 55;
    vFU = WS_FU + ((l_ < 28 ? da * 128 + l_ : db * 128 + l_ - 28) + w * 28) * (4 * 4);
    vFO = WS_FO + ((2 * st + (w >> 1)) * 8 + (lane & 7)) * (4 * 16);
    asm volatile("" : "+v"(vFU), "+v"(vFO)); }
  unsigned vFU1 = WS_FU + (512 + ((db0 * 37) >> 8) * 128 + w * 28 + (lane < 28 ? lane : 27)) * (4 * 4), fu2; OPV(vFU1);
  unsigned vFj[4], so0 = WS_OT + (4 * st + w) * 16384, so1 = so0 + 8192;
#pragma unroll
  for (int j = 0; j < 3; ++j) { const int k_ = BLK(j), k7 = (k_ * 37) >> 8; vFj[j] = vF + (WS_UT + k7 * (56 * 8192 - 896) + k_ * 128); OPV(vFj[j]); }
  asm volatile("" : "+s"(so0), "+s"(so1));
  SB();
  asm volatile("s_waitcnt vmcnt(9)"
      :  :  : "memory"); SB();
  fw1[3][0] = tr8u(tX[0] + 12288, 512);
  fw1[3][1] = tr8u(tX[1] + 12288, 512);
  fw1[3][2] = tr8u(tX[2] + 12288, 512);
  fw1[3][3] = tr8u(tX[3] + 12288, 512);
  STAMP(51);
  { int k_ = KS1(6); OPQ(k_); DMA4(vW1a, vW1b, rs1, k_ * 114688, 55296, ldsR, 24576); sh[6] = sh0 + k_ * 8192; OPQ(sh[6]); }
  SB();
  const int tau = 2 * (n16 & 7) + (n16 >> 3);
  unsigned rwd = ldsS + vL16, rr = lds0 + S3B + w * 1024 + (16 * g4 + tau) * 16, ro = lds0 + WOB + vL16;
  unsigned xo = ldsS + 34816 + vL16, braw, xt = ldsS + 34816 + tau * 128 + (((g4 >> 1) ^ (n16 & 7)) * 16) + (g4 & 1) * 8, vB2 = (64 * st + 16 * w + tau) * 2;
  unsigned xs = xt ^ (32 * 0);
  asm volatile("" : "+v"(rwd), "+v"(rr), "+v"(ro), "+v"(xt), "+v"(xs), "+v"(xo), "+v"(vB2));
  SB();
  asm volatile("s_waitcnt vmcnt(8)"
      :  :  : "memory"); SB();
  fw1[4][0] = tr8u(tX[0] + 16384, 512);
  fw1[4][1] = tr8u(tX[1] + 16384, 512);
  fw1[4][2] = tr8u(tX[2] + 16384, 512);
  fw1[4][3] = tr8u(tX[3] + 16384, 512);
  WAITL(8);
  { int k_ = KS1(7); OPQ(k_); DMA4(vW1a, vW1b, rs1, k_ * 114688, 55296, ldsR, 0); sh[7] = sh0 + k_ * 8192; OPQ(sh[7]); }
  SB();
  unsigned vXa, vXb;
  { const int ga = (lane & 7) ^ (r8 >> 1), gb = ga ^ 4, r_ = (64 * st + 16 * w + r8) * 896;
    vXa = ga < 4 ? r_ + (db0 + (dt & 1)) * 32 + ga * 16 : ga < 6 ? r_ + BLK(2) * 32 + (ga - 4) * 16 : 0x40000000;
    vXb = (gb < 4 ? r_ + (db0 + (dt & 1)) * 32 + gb * 16 : gb < 6 ? r_ + BLK(2) * 32 + (gb - 4) * 16 : 0x40000000) + (8 * 896 - 1024); OPV(vXa); OPV(vXb); }
  SB();
  asm volatile("s_waitcnt vmcnt(8)"
      :  :  : "memory"); SB();
  fw1[5][0] = tr8u(tX[0] + 20480, 512);
  fw1[5][1] = tr8u(tX[1] + 20480, 512);
  fw1[5][2] = tr8u(tX[2] + 20480, 512);
  fw1[5][3] = tr8u(tX[3] + 20480, 512);
  WAITL(8);
  { int k_ = KS1(8); OPQ(k_); DMA4(vW1a, vW1b, rs1, k_ * 114688, 55296, ldsR, 4096); sh[8] = sh0 + k_ * 8192; OPQ(sh[8]); }
  SB();
  { unsigned k_ = sc_ + (rot2 >= 14 ? -14 : 0); OPQ(k_); su[0] = k_ << 13; sw[0] = k_ << 17; OPQ(su[0]); OPQ(sw[0]); }
  { unsigned k_ = sc_ + (rot2 >= 13 ? -13 : 1); OPQ(k_); su[1] = k_ << 13; sw[1] = k_ << 17; OPQ(su[1]); OPQ(sw[1]); }
  { unsigned k_ = sc_ + (rot2 >= 12 ? -12 : 2); OPQ(k_); su[2] = k_ << 13; sw[2] = k_ << 17; OPQ(su[2]); OPQ(sw[2]); }
  SB();
  asm volatile("s_waitcnt vmcnt(8)"
      :  :  : "memory"); SB();
  fw1[6][0] = tr8u(tX[0] + 24576, 512);
  fw1[6][1] = tr8u(tX[1] + 24576, 512);
  fw1[6][2] = tr8u(tX[2] + 24576, 512);
  fw1[6][3] = tr8u(tX[3] + 24576, 512);
  WAITV1(20, bv);
  f32x4 bb = {lo2f(bv[0]), hi2f(bv[0]), lo2f(bv[1]), hi2f(bv[1])}; OPV(bb);
  WAITL(8);
  { int k_ = KS1(9); OPQ(k_); DMA4(vW1a, vW1b, rs1, k_ * 114688, 55296, ldsR, 8192); sh[9] = sh0 + k_ * 8192; OPQ(sh[9]); }
  SB();
  { unsigned k_ = sc_ + (rot2 >= 11 ? -11 : 3); OPQ(k_); su[3] = k_ << 13; sw[3] = k_ << 17; OPQ(su[3]); OPQ(sw[3]); }
  { unsigned k_ = sc_ + (rot2 >= 10 ? -10 : 4); OPQ(k_); su[4] = k_ << 13; sw[4] = k_ << 17; OPQ(su[4]); OPQ(sw[4]); }
  { unsigned k_ = sc_ + (rot2 >= 9 ? -9 : 5); OPQ(k_); su[5] = k_ << 13; sw[5] = k_ << 17; OPQ(su[5]); OPQ(sw[5]); }
  SB();
  asm volatile("s_waitcnt vmcnt(8)"
      :  :  : "memory"); SB();
  fw1[7][0] = tr8u(tX[0] + 0, 512);
  fw1[7][1] = tr8u(tX[1] + 0, 512);
  fw1[7][2] = tr8u(tX[2] + 0, 512);
  fw1[7][3] = tr8u(tX[3] + 0, 512);
  STAMP(52);
  WAITL(8);
  { int k_ = KS1(10); OPQ(k_); DMA4(vW1a, vW1b, rs1, k_ * 114688, 55296, ldsR, 12288); sh[10] = sh0 + k_ * 8192; OPQ(sh[10]); }
  SB();
  { unsigned k_ = sc_ + (rot2 >= 8 ? -8 : 6); OPQ(k_); su[6] = k_ << 13; sw[6] = k_ << 17; OPQ(su[6]); OPQ(sw[6]); }
  { unsigned k_ = sc_ + (rot2 >= 7 ? -7 : 7); OPQ(k_); su[7] = k_ << 13; sw[7] = k_ << 17; OPQ(su[7]); OPQ(sw[7]); }
  { unsigned k_ = sc_ + (rot2 >= 6 ? -6 : 8); OPQ(k_); su[8] = k_ << 13; sw[8] = k_ << 17; OPQ(su[8]); OPQ(sw[8]); }
  SB();
  asm volatile("s_waitcnt vmcnt(8)"
      :  :  : "memory"); SB();
  fw1[8][0] = tr8u(tX[0] + 4096, 512);
  fw1[8][1] = tr8u(tX[1] + 4096, 512);
  fw1[8][2] = tr8u(tX[2] + 4096, 512);
  fw1[8][3] = tr8u(tX[3] + 4096, 512);
  WAITL(8);
  { int k_ = KS1(11); OPQ(k_); DMA4(vW1a, vW1b, rs1, k_ * 114688, 55296, ldsR, 16384); sh[11] = sh0 + k_ * 8192; OPQ(sh[11]); }
  SB();
  { unsigned k_ = sc_ + (rot2 >= 5 ? -5 : 9); OPQ(k_); su[9] = k_ << 13; sw[9] = k_ << 17; OPQ(su[9]); OPQ(sw[9]); }
  { unsigned k_ = sc_ + (rot2 >= 4 ? -4 : 10); OPQ(k_); su[10] = k_ << 13; sw[10] = k_ << 17; OPQ(su[10]); OPQ(sw[10]); }
  { unsigned k_ = sc_ + (rot2 >= 3 ? -3 : 11); OPQ(k_); su[11] = k_ << 13; sw[11] = k_ << 17; OPQ(su[11]); OPQ(sw[11]); }
  SB();
  asm volatile("s_waitcnt vmcnt(8)"
      :  :  : "memory"); SB();
  fw1[9][0] = tr8u(tX[0] + 8192, 512);
  fw1[9][1] = tr8u(tX[1] + 8192, 512);
  fw1[9][2] = tr8u(tX[2] + 8192, 512);
  fw1[9][3] = tr8u(tX[3] + 8192, 512);
  WAITL(8);
  { int k_ = KS1(12); OPQ(k_); DMA4(vW1a, vW1b, rs1, k_ * 114688, 55296, ldsR, 20480); sh[12] = sh0 + k_ * 8192; OPQ(sh[12]); }
  SB();
  { unsigned k_ = sc_ + (rot2 >= 2 ? -2 : 12); OPQ(k_); su[12] = k_ << 13; sw[12] = k_ << 17; OPQ(su[12]); OPQ(sw[12]); }
  { unsigned k_ = sc_ + (rot2 >= 1 ? -1 : 13); OPQ(k_); su[13] = k_ << 13; sw[13] = k_ << 17; OPQ(su[13]); OPQ(sw[13]); }
  SB();
  asm volatile("s_waitcnt vmcnt(8)"
      :  :  : "memory"); SB();
  fw1[10][0] = tr8u(tX[0] + 12288, 512);
  fw1[10][1] = tr8u(tX[1] + 12288, 512);
  fw1[10][2] = tr8u(tX[2] + 12288, 512);
  fw1[10][3] = tr8u(tX[3] + 12288, 512);
  WAITL(8);
  { int k_ = KS1(13); OPQ(k_); DMA4(vW1a, vW1b, rs1, k_ * 114688, 55296, ldsR, 24576); sh[13] = sh0 + k_ * 8192; OPQ(sh[13]); }
  asm volatile("s_waitcnt vmcnt(8)"
      :  :  : "memory"); SB();
  fw1[11][0] = tr8u(tX[0] + 16384, 512);
  fw1[11][1] = tr8u(tX[1] + 16384, 512);
  fw1[11][2] = tr8u(tX[2] + 16384, 512);
  fw1[11][3] = tr8u(tX[3] + 16384, 512);
  STAMP(53);
  WAITL(8);
  { int k_ = KS1(14); OPQ(k_); DMA4(vW1a, vW1b, rs1, k_ * 114688, 55296, ldsR, 0); sh[14] = sh0 + k_ * 8192; OPQ(sh[14]); }
  asm volatile("s_waitcnt vmcnt(8)"
      :  :  : "memory"); SB();
  fw1[12][0] = tr8u(tX[0] + 20480, 512);
  fw1[12][1] = tr8u(tX[1] + 20480, 512);
  fw1[12][2] = tr8u(tX[2] + 20480, 512);
  fw1[12][3] = tr8u(tX[3] + 20480, 512);
  WAITL(8);
  { int k_ = KS1(15); OPQ(k_); DMA4(vW1a, vW1b, rs1, k_ * 114688, 55296, ldsR, 4096); sh[15] = sh0 + k_ * 8192; OPQ(sh[15]); }
  asm volatile("buffer_load_dword %0, %1, %2, 0 offen sc1\n\ts_waitcnt vmcnt(9)"
      : "=&v"(fh) : "v"(vFH), "s"(rsW) : "memory"); SB();
  fw1[13][0] = tr8u(tX[0] + 24576, 512);
  fw1[13][1] = tr8u(tX[1] + 24576, 512);
  fw1[13][2] = tr8u(tX[2] + 24576, 512);
  fw1[13][3] = tr8u(tX[3] + 24576, 512);
  WAITV1(0, fh);
  if (__builtin_expect(__ballot(fh != nonce) != 0ull, 0)) poll1<4>(rsW, vFH, nonce);
  STAMP(6);
  asm volatile("buffer_load_dwordx4 %0, %4, %5, %6 offen offset:0 sc1\n\tbuffer_load_dwordx4 %1, %4, %5, %7 offen offset:0 sc1\n\tbuffer_load_dwordx4 %2, %4, %5, %8 offen offset:0 sc1\n\tbuffer_load_dwordx4 %3, %4, %5, %9 offen offset:0 sc1\n\ts_waitcnt vmcnt(9)"
      : "=&v"(p[0]), "=&v"(p[1]), "=&v"(p[2]), "=&v"(p[3]) : "v"(vHa), "s"(rsW), "s"(sh[0]), "s"(sh[1]), "s"(sh[2]), "s"(sh[3]) : "memory"); SB();
  fw1[14][0] = tr8u(tX[0] + 0, 512);
  fw1[14][1] = tr8u(tX[1] + 0, 512);
  fw1[14][2] = tr8u(tX[2] + 0, 512);
  fw1[14][3] = tr8u(tX[3] + 0, 512);
  asm volatile("buffer_load_dwordx4 %0, %4, %5, %6 offen offset:0 sc1\n\tbuffer_load_dwordx4 %1, %4, %5, %7 offen offset:0 sc1\n\tbuffer_load_dwordx4 %2, %4, %5, %8 offen offset:0 sc1\n\tbuffer_load_dwordx4 %3, %4, %5, %9 offen offset:0 sc1\n\ts_waitcnt vmcnt(9)"
      : "=&v"(p[4]), "=&v"(p[5]), "=&v"(p[6]), "=&v"(p[7]) : "v"(vHa), "s"(rsW), "s"(sh[4]), "s"(sh[5]), "s"(sh[6]), "s"(sh[7]) : "memory"); SB();
  fw1[15][0] = tr8u(tX[0] + 4096, 512);
  fw1[15][1] = tr8u(tX[1] + 4096, 512);
  fw1[15][2] = tr8u(tX[2] + 4096, 512);
  fw1[15][3] = tr8u(tX[3] + 4096, 512);
  STAMP(5);
  asm volatile("buffer_load_dwordx4 %0, %4, %5, %6 offen offset:0 sc1\n\tbuffer_load_dwordx4 %1, %4, %5, %7 offen offset:0 sc1\n\tbuffer_load_dwordx4 %2, %4, %5, %8 offen offset:0 sc1\n\tbuffer_load_dwordx4 %3, %4, %5, %9 offen offset:0 sc1"
      : "=&v"(p[8]), "=&v"(p[9]), "=&v"(p[10]), "=&v"(p[11]) : "v"(vHa), "s"(rsW), "s"(sh[8]), "s"(sh[9]), "s"(sh[10]), "s"(sh[11]) : "memory"); SB();
  f32x4 acc[7][4], pm[7][3]; float gm[4], gz[4]; unsigned uv[8][2];
  WAITVB(11);
  SB();
  asm volatile("v_mfma_f32_16x16x32_bf16 %0, %6, %7, 0\n\tbuffer_load_dwordx4 %1, %8, %9, %10 offen offset:0 sc1\n\tv_mfma_f32_16x16x32_bf16 %2, %11, %7, 0\n\tv_mfma_f32_16x16x32_bf16 %3, %12, %7, 0\n\tbuffer_load_dwordx4 %4, %8, %9, %13 offen offset:0 sc1\n\tv_mfma_f32_16x16x32_bf16 %5, %14, %7, %15"
      : "=&v"(acc[0][1]), "=&v"(p[12]), "=&v"(acc[0][2]), "=&v"(acc[0][3]), "=&v"(p[13]), "=&v"(acc[0][0]) : "a"(fw1[0][1]), "v"(p[0]), "v"(vHa), "s"(rsW), "s"(sh[12]), "a"(fw1[0][2]), "a"(fw1[0][3]), "s"(sh[13]), "a"(fw1[0][0]), "v"(bb) : "memory"); SB();
  WAITVB(12);
  SB();
  asm volatile("v_mfma_f32_16x16x32_bf16 %0, %5, %6, %0\n\tbuffer_load_dwordx4 %1, %7, %8, %9 offen offset:0 sc1\n\tv_mfma_f32_16x16x32_bf16 %2, %10, %6, %2\n\tv_mfma_f32_16x16x32_bf16 %3, %11, %6, %3\n\tv_mfma_f32_16x16x32_bf16 %4, %12, %6, %4"
      : "+v"(acc[0][1]), "=&v"(p[0]), "+v"(acc[0][2]), "+v"(acc[0][3]), "+v"(acc[0][0]) : "a"(fw1[1][1]), "v"(p[1]), "v"(vHa), "s"(rsW), "s"(sh[14]), "a"(fw1[1][2]), "a"(fw1[1][3]), "a"(fw1[1][0]) : "memory"); SB();
  WAITVB(12);
  SB();
  asm volatile("v_mfma_f32_16x16x32_bf16 %0, %5, %6, %0\n\tbuffer_load_dwordx4 %1, %7, %8, %9 offen offset:0 sc1\n\tv_mfma_f32_16x16x32_bf16 %2, %10, %6, %2\n\tv_mfma_f32_16x16x32_bf16 %3, %11, %6, %3\n\tv_mfma_f32_16x16x32_bf16 %4, %12, %6, %4"
      : "+v"(acc[0][1]), "=&v"(p[1]), "+v"(acc[0][2]), "+v"(acc[0][3]), "+v"(acc[0][0]) : "a"(fw1[2][1]), "v"(p[2]), "v"(vHa), "s"(rsW), "s"(sh[15]), "a"(fw1[2][2]), "a"(fw1[2][3]), "a"(fw1[2][0]) : "memory"); SB();
  WAITVB(12);
  SB();
  asm volatile("v_mfma_f32_16x16x32_bf16 %0, %5, %6, %0\n\tbuffer_load_dwordx4 %1, %7, %8, %9 offen offset:0 sc1\n\tv_mfma_f32_16x16x32_bf16 %2, %10, %6, %2\n\tv_mfma_f32_16x16x32_bf16 %3, %11, %6, %3\n\tv_mfma_f32_16x16x32_bf16 %4, %12, %6, %4"
      : "+v"(acc[0][1]), "=&v"(p[2]), "+v"(acc[0][2]), "+v"(acc[0][3]), "+v"(acc[0][0]) : "a"(fw1[3][1]), "v"(p[3]), "v"(vHc), "s"(rsW), "s"(sh[0]), "a"(fw1[3][2]), "a"(fw1[3][3]), "a"(fw1[3][0]) : "memory"); SB();
  WAITVB(12);
  SB();
  asm volatile("v_mfma_f32_16x16x32_bf16 %0, %5, %6, %0\n\tbuffer_load_dwordx4 %1, %7, %8, %9 offen offset:0 sc1\n\tv_mfma_f32_16x16x32_bf16 %2, %10, %6, %2\n\tv_mfma_f32_16x16x32_bf16 %3, %11, %6, %3\n\tv_mfma_f32_16x16x32_bf16 %4, %12, %6, %4"
      : "+v"(acc[0][1]), "=&v"(p[3]), "+v"(acc[0][2]), "+v"(acc[0][3]), "+v"(acc[0][0]) : "a"(fw1[4][1]), "v"(p[4]), "v"(vHc), "s"(rsW), "s"(sh[1]), "a"(fw1[4][2]), "a"(fw1[4][3]), "a"(fw1[4][0]) : "memory"); SB();
  WAITVB(12);
  SB();
  asm volatile("v_mfma_f32_16x16x32_bf16 %0, %5, %6, %0\n\tbuffer_load_dwordx4 %1, %7, %8, %9 offen offset:0 sc1\n\tv_mfma_f32_16x16x32_bf16 %2, %10, %6, %2\n\tv_mfma_f32_16x16x32_bf16 %3, %11, %6, %3\n\tv_mfma_f32_16x16x32_bf16 %4, %12, %6, %4"
      : "+v"(acc[0][1]), "=&v"(p[4]), "+v"(acc[0][2]), "+v"(acc[0][3]), "+v"(acc[0][0]) : "a"(fw1[5][1]), "v"(p[5]), "v"(vHc), "s"(rsW), "s"(sh[2]), "a"(fw1[5][2]), "a"(fw1[5][3]), "a"(fw1[5][0]) : "memory"); SB();
  WAITVB(12);
  SB();
  asm volatile("v_mfma_f32_16x16x32_bf16 %0, %5, %6, %0\n\tbuffer_load_dwordx4 %1, %7, %8, %9 offen offset:0 sc1\n\tv_mfma_f32_16x16x32_bf16 %2, %10, %6, %2\n\tv_mfma_f32_16x16x32_bf16 %3, %11, %6, %3\n\tv_mfma_f32_16x16x32_bf16 %4, %12, %6, %4"
      : "+v"(acc[0][1]), "=&v"(p[5]), "+v"(acc[0][2]), "+v"(acc[0][3]), "+v"(acc[0][0]) : "a"(fw1[6][1]), "v"(p[6]), "v"(vHc), "s"(rsW), "s"(sh[3]), "a"(fw1[6][2]), "a"(fw1[6][3]), "a"(fw1[6][0]) : "memory"); SB();
  WAITVB(12);
  SB();
  asm volatile("v_mfma_f32_16x16x32_bf16 %0, %5, %6, %0\n\tbuffer_load_dwordx4 %1, %7, %8, %9 offen offset:0 sc1\n\tv_mfma_f32_16x16x32_bf16 %2, %10, %6, %2\n\tv_mfma_f32_16x16x32_bf16 %3, %11, %6, %3\n\tv_mfma_f32_16x16x32_bf16 %4, %12, %6, %4"
      : "+v"(acc[0][1]), "=&v"(p[6]), "+v"(acc[0][2]), "+v"(acc[0][3]), "+v"(acc[0][0]) : "a"(fw1[7][1]), "v"(p[7]), "v"(vHc), "s"(rsW), "s"(sh[4]), "a"(fw1[7][2]), "a"(fw1[7][3]), "a"(fw1[7][0]) : "memory"); SB();
  WAITVB(12);
  SB();
  asm volatile("v_mfma_f32_16x16x32_bf16 %0, %5, %6, %0\n\tbuffer_load_dwordx4 %1, %7, %8, %9 offen offset:0 sc1\n\tv_mfma_f32_16x16x32_bf16 %2, %10, %6, %2\n\tv_mfma_f32_16x16x32_bf16 %3, %11, %6, %3\n\tv_mfma_f32_16x16x32_bf16 %4, %12, %6, %4"
      : "+v"(acc[0][1]), "=&v"(p[7]), "+v"(acc[0][2]), "+v"(acc[0][3]), "+v"(acc[0][0]) : "a"(fw1[8][1]), "v"(p[8]), "v"(vHc), "s"(rsW), "s"(sh[5]), "a"(fw1[8][2]), "a"(fw1[8][3]), "a"(fw1[8][0]) : "memory"); SB();
  WAITVB(12);
  SB();
  asm volatile("v_mfma_f32_16x16x32_bf16 %0, %5, %6, %0\n\tbuffer_load_dwordx4 %1, %7, %8, %9 offen offset:0 sc1\n\tv_mfma_f32_16x16x32_bf16 %2, %10, %6, %2\n\tv_mfma_f32_16x16x32_bf16 %3, %11, %6, %3\n\tv_mfma_f32_16x16x32_bf16 %4, %12, %6, %4"
      : "+v"(acc[0][1]), "=&v"(p[8]), "+v"(acc[0][2]), "+v"(acc[0][3]), "+v"(acc[0][0]) : "a"(fw1[9][1]), "v"(p[9]), "v"(vHc), "s"(rsW), "s"(sh[6]), "a"(fw1[9][2]), "a"(fw1[9][3]), "a"(fw1[9][0]) : "memory"); SB();
  WAITVB(12);
  SB();
  asm volatile("v_mfma_f32_16x16x32_bf16 %0, %5, %6, %0\n\tbuffer_load_dwordx4 %1, %7, %8, %9 offen offset:0 sc1\n\tv_mfma_f32_16x16x32_bf16 %2, %10, %6, %2\n\tv_mfma_f32_16x16x32_bf16 %3, %11, %6, %3\n\tv_mfma_f32_16x16x32_bf16 %4, %12, %6, %4"
      : "+v"(acc[0][1]), "=&v"(p[9]), "+v"(acc[0][2]), "+v"(acc[0][3]), "+v"(acc[0][0]) : "a"(fw1[10][1]), "v"(p[10]), "v"(vHc), "s"(rsW), "s"(sh[7]), "a"(fw1[10][2]), "a"(fw1[10][3]), "a"(fw1[10][0]) : "memory"); SB();
  WAITVB(12);
  SB();
  asm volatile("v_mfma_f32_16x16x32_bf16 %0, %5, %6, %0\n\tbuffer_load_dwordx4 %1, %7, %8, %9 offen offset:0 sc1\n\tv_mfma_f32_16x16x32_bf16 %2, %10, %6, %2\n\tv_mfma_f32_16x16x32_bf16 %3, %11, %6, %3\n\tv_mfma_f32_16x16x32_bf16 %4, %12, %6, %4"
      : "+v"(acc[0][1]), "=&v"(p[10]), "+v"(acc[0][2]), "+v"(acc[0][3]), "+v"(acc[0][0]) : "a"(fw1[11][1]), "v"(p[11]), "v"(vHc), "s"(rsW), "s"(sh[8]), "a"(fw1[11][2]), "a"(fw1[11][3]), "a"(fw1[11][0]) : "memory"); SB();
  WAITVB(12);
  SB();
  asm volatile("v_mfma_f32_16x16x32_bf16 %0, %5, %6, %0\n\tbuffer_load_dwordx4 %1, %7, %8, %9 offen offset:0 sc1\n\tv_mfma_f32_16x16x32_bf16 %2, %10, %6, %2\n\tv_mfma_f32_16x16x32_bf16 %3, %11, %6, %3\n\tv_mfma_f32_16x16x32_bf16 %4, %12, %6, %4"
      : "+v"(acc[0][1]), "=&v"(p[11]), "+v"(acc[0][2]), "+v"(acc[0][3]), "+v"(acc[0][0]) : "a"(fw1[12][1]), "v"(p[12]), "v"(vHc), "s"(rsW), "s"(sh[9]), "a"(fw1[12][2]), "a"(fw1[12][3]), "a"(fw1[12][0]) : "memory"); SB();
  WAITVB(12);
  SB();
  asm volatile("v_mfma_f32_16x16x32_bf16 %0, %5, %6, %0\n\tbuffer_load_dwordx4 %1, %7, %8, %9 offen offset:0 sc1\n\tv_mfma_f32_16x16x32_bf16 %2, %10, %6, %2\n\tv_mfma_f32_16x16x32_bf16 %3, %11, %6, %3\n\tv_mfma_f32_16x16x32_bf16 %4, %12, %6, %4"
      : "+v"(acc[0][1]), "=&v"(p[12]), "+v"(acc[0][2]), "+v"(acc[0][3]), "+v"(acc[0][0]) : "a"(fw1[13][1]), "v"(p[13]), "v"(vHc), "s"(rsW), "s"(sh[10]), "a"(fw1[13][2]), "a"(fw1[13][3]), "a"(fw1[13][0]) : "memory"); SB();
  WAITVB(12);
  SB();
  asm volatile("v_mfma_f32_16x16x32_bf16 %0, %5, %6, %0\n\tbuffer_load_dwordx4 %1, %7, %8, %9 offen offset:0 sc1\n\tv_mfma_f32_16x16x32_bf16 %2, %10, %6, %2\n\tv_mfma_f32_16x16x32_bf16 %3, %11, %6, %3\n\tv_mfma_f32_16x16x32_bf16 %4, %12, %6, %4"
      : "+v"(acc[0][1]), "=&v"(p[13]), "+v"(acc[0][2]), "+v"(acc[0][3]), "+v"(acc[0][0]) : "a"(fw1[14][1]), "v"(p[0]), "v"(vHc), "s"(rsW), "s"(sh[11]), "a"(fw1[14][2]), "a"(fw1[14][3]), "a"(fw1[14][0]) : "memory"); SB();
  WAITVB(12);
  SB();
  asm volatile("v_mfma_f32_16x16x32_bf16 %0, %5, %6, %0\n\tbuffer_load_dwordx4 %1, %7, %8, %9 offen offset:0 sc1\n\tv_mfma_f32_16x16x32_bf16 %2, %10, %6, %2\n\tv_mfma_f32_16x16x32_bf16 %3, %11, %6, %3\n\tv_mfma_f32_16x16x32_bf16 %4, %12, %6, %4"
      : "+v"(acc[0][1]), "=&v"(p[0]), "+v"(acc[0][2]), "+v"(acc[0][3]), "+v"(acc[0][0]) : "v"(fw1[15][1]), "v"(p[1]), "v"(vHc), "s"(rsW), "s"(sh[12]), "v"(fw1[15][2]), "v"(fw1[15][3]), "v"(fw1[15][0]) : "memory"); SB();
  STAMP(7);
  WAITVB(12);
  SB();
  asm volatile("v_mfma_f32_16x16x32_bf16 %0, %5, %6, 0\n\tbuffer_load_dwordx4 %1, %7, %8, %9 offen offset:0 sc1\n\tv_mfma_f32_16x16x32_bf16 %2, %10, %6, 0\n\tv_mfma_f32_16x16x32_bf16 %3, %11, %6, 0\n\tv_mfma_f32_16x16x32_bf16 %4, %12, %6, %13"
      : "=&v"(acc[1][1]), "=&v"(p[1]), "=&v"(acc[1][2]), "=&v"(acc[1][3]), "=&v"(acc[1][0]) : "a"(fw1[0][1]), "v"(p[2]), "v"(vHc), "s"(rsW), "s"(sh[13]), "a"(fw1[0][2]), "a"(fw1[0][3]), "a"(fw1[0][0]), "v"(bb) : "memory"); SB();
  WAITVB(12);
  SB();
  asm volatile("v_mfma_f32_16x16x32_bf16 %0, %5, %6, %0\n\tbuffer_load_dwordx4 %1, %7, %8, %9 offen offset:0 sc1\n\tv_mfma_f32_16x16x32_bf16 %2, %10, %6, %2\n\tds_write_b128 %11, %12 offset:1024\n\tv_mfma_f32_16x16x32_bf16 %3, %13, %6, %3\n\tv_mfma_f32_16x16x32_bf16 %4, %14, %6, %4"
      : "+v"(acc[1][1]), "=&v"(p[2]), "+v"(acc[1][2]), "+v"(acc[1][3]), "+v"(acc[1][0]) : "a"(fw1[1][1]), "v"(p[3]), "v"(vHc), "s"(rsW), "s"(sh[14]), "a"(fw1[1][2]), "v"(rw), "v"(acc[0][1]), "a"(fw1[1][3]), "a"(fw1[1][0]) : "memory"); SB();
  WAITVB(12);
  SB();
  asm volatile("v_mfma_f32_16x16x32_bf16 %0, %5, %6, %0\n\tbuffer_load_dwordx4 %1, %7, %8, %9 offen offset:0 sc1\n\tv_mfma_f32_16x16x32_bf16 %2, %10, %6, %2\n\tds_write_b128 %11, %12 offset:2048\n\tv_mfma_f32_16x16x32_bf16 %3, %13, %6, %3\n\tv_mfma_f32_16x16x32_bf16 %4, %14, %6, %4"
      : "+v"(acc[1][1]), "=&v"(p[3]), "+v"(acc[1][2]), "+v"(acc[1][3]), "+v"(acc[1][0]) : "a"(fw1[2][1]), "v"(p[4]), "v"(vHc), "s"(rsW), "s"(sh[15]), "a"(fw1[2][2]), "v"(rw), "v"(acc[0][2]), "a"(fw1[2][3]), "a"(fw1[2][0]) : "memory"); SB();
  WAITVB(12);
  SB();
  asm volatile("v_mfma_f32_16x16x32_bf16 %0, %5, %6, %0\n\tbuffer_load_dwordx4 %1, %7, %8, %9 offen offset:512 sc1\n\tv_mfma_f32_16x16x32_bf16 %2, %10, %6, %2\n\tds_write_b128 %11, %12 offset:3072\n\tv_mfma_f32_16x16x32_bf16 %3, %13, %6, %3\n\tv_mfma_f32_16x16x32_bf16 %4, %14, %6, %4"
      : "+v"(acc[1][1]), "=&v"(p[4]), "+v"(acc[1][2]), "+v"(acc[1][3]), "+v"(acc[1][0]) : "a"(fw1[3][1]), "v"(p[5]), "v"(vHa), "s"(rsW), "s"(sh[0]), "a"(fw1[3][2]), "v"(rw), "v"(acc[0][3]), "a"(fw1[3][3]), "a"(fw1[3][0]) : "memory"); SB();
  WAITVB(12);
  SB();
  asm volatile("v_mfma_f32_16x16x32_bf16 %0, %3, %4, %0\n\tbuffer_load_dwordx4 %1, %5, %6, %7 offen offset:512 sc1\n\tv_mfma_f32_16x16x32_bf16 %2, %8, %4, %2"
      : "+v"(acc[1][1]), "=&v"(p[5]), "+v"(acc[1][2]) : "a"(fw1[4][1]), "v"(p[6]), "v"(vHa), "s"(rsW), "s"(sh[1]), "a"(fw1[4][2]) : "memory"); SB();
  BAR();
  SB();
  asm volatile("v_mfma_f32_16x16x32_bf16 %0, %5, %6, %0\n\tds_read_b128 %1, %7 offset:1024\n\tds_read_b128 %2, %8 offset:2048\n\tv_mfma_f32_16x16x32_bf16 %3, %9, %6, %3\n\tds_read_b128 %4, %10 offset:3072"
      : "+v"(acc[1][3]), "=&v"(pm[0][0]), "=&v"(pm[0][1]), "+v"(acc[1][0]), "=&v"(pm[0][2]) : "a"(fw1[4][3]), "v"(p[6]), "v"(rr1), "v"(rr2), "a"(fw1[4][0]), "v"(rr3) : "memory"); SB();
  WAITVB(12);
  SB();
  asm volatile("v_mfma_f32_16x16x32_bf16 %0, %5, %6, %0\n\tbuffer_load_dwordx4 %1, %7, %8, %9 offen offset:512 sc1\n\tv_mfma_f32_16x16x32_bf16 %2, %10, %6, %2\n\tv_mfma_f32_16x16x32_bf16 %3, %11, %6, %3\n\tv_mfma_f32_16x16x32_bf16 %4, %12, %6, %4"
      : "+v"(acc[1][1]), "=&v"(p[6]), "+v"(acc[1][2]), "+v"(acc[1][3]), "+v"(acc[1][0]) : "a"(fw1[5][1]), "v"(p[7]), "v"(vHa), "s"(rsW), "s"(sh[2]), "a"(fw1[5][2]), "a"(fw1[5][3]), "a"(fw1[5][0]) : "memory"); SB();
  WAITVB(12);
  SB();
  asm volatile("v_mfma_f32_16x16x32_bf16 %0, %3, %4, %0\n\tbuffer_load_dwordx4 %1, %5, %6, %7 offen offset:512 sc1\n\tv_mfma_f32_16x16x32_bf16 %2, %8, %4, %2"
      : "+v"(acc[1][1]), "=&v"(p[7]), "+v"(acc[1][2]) : "a"(fw1[6][1]), "v"(p[8]), "v"(vHa), "s"(rsW), "s"(sh[3]), "a"(fw1[6][2]) : "memory"); SB();
  WAITL(0);
  asm volatile("v_add_f32 %0, %6, %7\n\tv_mfma_f32_16x16x32_bf16 %1, %8, %9, %1\n\tv_add_f32 %2, %10, %11\n\tv_add_f32 %3, %12, %13\n\tv_mfma_f32_16x16x32_bf16 %4, %14, %9, %4\n\tv_add_f32 %5, %15, %16"
      : "=&v"(gm[0]), "+v"(acc[1][3]), "=&v"(gm[1]), "=&v"(gm[2]), "+v"(acc[1][0]), "=&v"(gm[3]) : "v"(acc[0][0][0]), "v"(pm[0][0][0]), "a"(fw1[6][3]), "v"(p[8]), "v"(acc[0][0][1]), "v"(pm[0][0][1]), "v"(acc[0][0][2]), "v"(pm[0][0][2]), "a"(fw1[6][0]), "v"(acc[0][0][3]), "v"(pm[0][0][3])); SB();
  WAITVB(12);
  SB();
  asm volatile("v_mfma_f32_16x16x32_bf16 %0, %9, %10, %0\n\tbuffer_load_dwordx4 %1, %11, %12, %13 offen offset:512 sc1\n\tv_mfma_f32_16x16x32_bf16 %2, %14, %10, %2\n\tv_add_f32 %3, %15, %16\n\tv_add_f32 %4, %17, %18\n\tv_mfma_f32_16x16x32_bf16 %5, %19, %10, %5\n\tv_add_f32 %6, %20, %21\n\tv_add_f32 %7, %22, %23\n\tv_mfma_f32_16x16x32_bf16 %8, %24, %10, %8"
      : "+v"(acc[1][1]), "=&v"(p[8]), "+v"(acc[1][2]), "=&v"(gz[0]), "=&v"(gz[1]), "+v"(acc[1][3]), "=&v"(gz[2]), "=&v"(gz[3]), "+v"(acc[1][0]) : "a"(fw1[7][1]), "v"(p[9]), "v"(vHa), "s"(rsW), "s"(sh[4]), "a"(fw1[7][2]), "v"(pm[0][1][0]), "v"(pm[0][2][0]), "v"(pm[0][1][1]), "v"(pm[0][2][1]), "a"(fw1[7][3]), "v"(pm[0][1][2]), "v"(pm[0][2][2]), "v"(pm[0][1][3]), "v"(pm[0][2][3]), "a"(fw1[7][0]) : "memory"); SB();
  asm volatile("v_add_f32 %0, %0, %1"
      : "+v"(gm[0]) : "v"(gz[0])); SB();
  WAITVB(12);
  SB();
  asm volatile("v_mfma_f32_16x16x32_bf16 %0, %10, %11, %0\n\tbuffer_load_dwordx4 %1, %12, %13, %14 offen offset:512 sc1\n\tv_mfma_f32_16x16x32_bf16 %2, %15, %11, %2\n\tv_add_f32 %3, %3, %4\n\tv_add_f32 %5, %5, %16\n\tv_mfma_f32_16x16x32_bf16 %6, %17, %11, %6\n\tv_add_f32 %7, %7, %18\n\tv_mul_f32 %8, %19, %19\n\tv_mfma_f32_16x16x32_bf16 %9, %20, %11, %9\n\tv_mul_f32 %4, %3, %3"
      : "+v"(acc[1][1]), "=&v"(p[9]), "+v"(acc[1][2]), "+v"(gm[1]), "+v"(gz[1]), "+v"(gm[2]), "+v"(acc[1][3]), "+v"(gm[3]), "=&v"(gz[0]), "+v"(acc[1][0]) : "a"(fw1[8][1]), "v"(p[10]), "v"(vHa), "s"(rsW), "s"(sh[5]), "a"(fw1[8][2]), "v"(gz[2]), "a"(fw1[8][3]), "v"(gz[3]), "v"(gm[0]), "a"(fw1[8][0]) : "memory"); SB();
  WAITVB(12);
  SB();
  asm volatile("v_mfma_f32_16x16x32_bf16 %0, %9, %10, %0\n\tbuffer_load_dwordx4 %1, %11, %12, %13 offen offset:512 sc1\n\tv_mfma_f32_16x16x32_bf16 %2, %14, %10, %2\n\tv_mul_f32 %3, %15, %15\n\tv_mul_f32 %4, %16, %16\n\tv_mfma_f32_16x16x32_bf16 %5, %17, %10, %5\n\tv_fmaak_f32 %6, %18, %6, 0x40135761\n\tv_fmaak_f32 %7, %18, %7, 0x40135761\n\tv_mfma_f32_16x16x32_bf16 %8, %19, %10, %8\n\tv_fmaak_f32 %3, %18, %3, 0x40135761"
      : "+v"(acc[1][1]), "=&v"(p[10]), "+v"(acc[1][2]), "=&v"(gz[2]), "=&v"(gz[3]), "+v"(acc[1][3]), "+v"(gz[0]), "+v"(gz[1]), "+v"(acc[1][0]) : "a"(fw1[9][1]), "v"(p[11]), "v"(vHa), "s"(rsW), "s"(sh[6]), "a"(fw1[9][2]), "v"(gm[2]), "v"(gm[3]), "a"(fw1[9][3]), "v"(gc), "a"(fw1[9][0]) : "memory"); SB();
  WAITVB(12);
  SB();
  asm volatile("v_mfma_f32_16x16x32_bf16 %0, %9, %10, %0\n\tbuffer_load_dwordx4 %1, %11, %12, %13 offen offset:512 sc1\n\tv_mfma_f32_16x16x32_bf16 %2, %14, %10, %2\n\tv_fmaak_f32 %3, %15, %3, 0x40135761\n\tv_mul_f32 %4, %4, %16\n\tv_mfma_f32_16x16x32_bf16 %5, %17, %10, %5\n\tv_mul_f32 %6, %6, %18\n\tv_mul_f32 %7, %7, %19\n\tv_mfma_f32_16x16x32_bf16 %8, %20, %10, %8\n\tv_mul_f32 %3, %3, %21"
      : "+v"(acc[1][1]), "=&v"(p[11]), "+v"(acc[1][2]), "+v"(gz[3]), "+v"(gz[0]), "+v"(acc[1][3]), "+v"(gz[1]), "+v"(gz[2]), "+v"(acc[1][0]) : "a"(fw1[10][1]), "v"(p[12]), "v"(vHa), "s"(rsW), "s"(sh[7]), "a"(fw1[10][2]), "v"(gc), "v"(gm[0]), "a"(fw1[10][3]), "v"(gm[1]), "v"(gm[2]), "a"(fw1[10][0]), "v"(gm[3]) : "memory"); SB();
  WAITVB(12);
  SB();
  asm volatile("v_mfma_f32_16x16x32_bf16 %0, %7, %8, %0\n\tbuffer_load_dwordx4 %1, %9, %10, %11 offen offset:512 sc1\n\tv_mfma_f32_16x16x32_bf16 %2, %12, %8, %2\n\tv_exp_f32 %3, %3\n\tv_mfma_f32_16x16x32_bf16 %4, %13, %8, %4\n\tv_exp_f32 %5, %5\n\tv_mfma_f32_16x16x32_bf16 %6, %14, %8, %6"
      : "+v"(acc[1][1]), "=&v"(p[12]), "+v"(acc[1][2]), "+v"(gz[0]), "+v"(acc[1][3]), "+v"(gz[1]), "+v"(acc[1][0]) : "a"(fw1[11][1]), "v"(p[13]), "v"(vHa), "s"(rsW), "s"(sh[8]), "a"(fw1[11][2]), "a"(fw1[11][3]), "a"(fw1[11][0]) : "memory"); SB();
  WAITVB(12);
  SB();
  asm volatile("v_mfma_f32_16x16x32_bf16 %0, %8, %9, %0\n\tbuffer_load_dwordx4 %1, %10, %11, %12 offen offset:512 sc1\n\tv_mfma_f32_16x16x32_bf16 %2, %13, %9, %2\n\tv_exp_f32 %3, %3\n\tv_mfma_f32_16x16x32_bf16 %4, %14, %9, %4\n\tv_exp_f32 %5, %5\n\tv_mfma_f32_16x16x32_bf16 %6, %15, %9, %6\n\tv_add_f32 %7, 1.0, %7"
      : "+v"(acc[1][1]), "=&v"(p[13]), "+v"(acc[1][2]), "+v"(gz[2]), "+v"(acc[1][3]), "+v"(gz[3]), "+v"(acc[1][0]), "+v"(gz[0]) : "a"(fw1[12][1]), "v"(p[0]), "v"(vHa), "s"(rsW), "s"(sh[9]), "a"(fw1[12][2]), "a"(fw1[12][3]), "a"(fw1[12][0]) : "memory"); SB();
  WAITVB(12);
  SB();
  asm volatile("v_mfma_f32_16x16x32_bf16 %0, %8, %9, %0\n\tbuffer_load_dwordx4 %1, %10, %11, %12 offen offset:512 sc1\n\tv_mfma_f32_16x16x32_bf16 %2, %13, %9, %2\n\tv_add_f32 %3, 1.0, %3\n\tv_add_f32 %4, 1.0, %4\n\tv_mfma_f32_16x16x32_bf16 %5, %14, %9, %5\n\tv_add_f32 %6, 1.0, %6\n\tv_mfma_f32_16x16x32_bf16 %7, %15, %9, %7"
      : "+v"(acc[1][1]), "=&v"(p[0]), "+v"(acc[1][2]), "+v"(gz[1]), "+v"(gz[2]), "+v"(acc[1][3]), "+v"(gz[3]), "+v"(acc[1][0]) : "a"(fw1[13][1]), "v"(p[1]), "v"(vHa), "s"(rsW), "s"(sh[10]), "a"(fw1[13][2]), "a"(fw1[13][3]), "a"(fw1[13][0]) : "memory"); SB();
  WAITVB(12);
  SB();
  asm volatile("v_mfma_f32_16x16x32_bf16 %0, %7, %8, %0\n\tbuffer_load_dwordx4 %1, %9, %10, %11 offen offset:512 sc1\n\tv_mfma_f32_16x16x32_bf16 %2, %12, %8, %2\n\tv_rcp_f32 %3, %3\n\tv_mfma_f32_16x16x32_bf16 %4, %13, %8, %4\n\tv_rcp_f32 %5, %5\n\tv_mfma_f32_16x16x32_bf16 %6, %14, %8, %6"
      : "+v"(acc[1][1]), "=&v"(p[1]), "+v"(acc[1][2]), "+v"(gz[0]), "+v"(acc[1][3]), "+v"(gz[1]), "+v"(acc[1][0]) : "a"(fw1[14][1]), "v"(p[2]), "v"(vHa), "s"(rsW), "s"(sh[11]), "a"(fw1[14][2]), "a"(fw1[14][3]), "a"(fw1[14][0]) : "memory"); SB();
  WAITVB(12);
  SB();
  asm volatile("v_mfma_f32_16x16x32_bf16 %0, %9, %10, %0\n\tbuffer_load_dwordx4 %1, %11, %12, %13 offen offset:512 sc1\n\tv_mfma_f32_16x16x32_bf16 %2, %14, %10, %2\n\tv_rcp_f32 %3, %3\n\tv_mfma_f32_16x16x32_bf16 %4, %15, %10, %4\n\tv_rcp_f32 %5, %5\n\tv_mfma_f32_16x16x32_bf16 %6, %16, %10, %6\n\tv_fma_f32 %7, -%17, %7, %17\n\tv_fma_f32 %8, -%18, %8, %18"
      : "+v"(acc[1][1]), "=&v"(p[2]), "+v"(acc[1][2]), "+v"(gz[2]), "+v"(acc[1][3]), "+v"(gz[3]), "+v"(acc[1][0]), "+v"(gz[0]), "+v"(gz[1]) : "v"(fw1[15][1]), "v"(p[3]), "v"(vHa), "s"(rsW), "s"(sh[12]), "v"(fw1[15][2]), "v"(fw1[15][3]), "v"(fw1[15][0]), "v"(gm[0]), "v"(gm[1]) : "memory"); SB();
  WAITVB(12);
  SB();
  asm volatile("v_mfma_f32_16x16x32_bf16 %0, %9, %10, 0\n\tbuffer_load_dwordx4 %1, %11, %12, %13 offen offset:512 sc1\n\tv_mfma_f32_16x16x32_bf16 %2, %14, %10, 0\n\tv_fma_f32 %3, -%15, %3, %15\n\tv_fma_f32 %4, -%16, %4, %16\n\tv_mfma_f32_16x16x32_bf16 %5, %17, %10, 0\n\tv_cvt_pk_bf16_f32 %6, %18, %19\n\tv_cvt_pk_bf16_f32 %7, %3, %4\n\tv_mfma_f32_16x16x32_bf16 %8, %20, %10, %21"
      : "=&v"(acc[2][1]), "=&v"(p[3]), "=&v"(acc[2][2]), "+v"(gz[2]), "+v"(gz[3]), "=&v"(acc[2][3]), "=&v"(uv[0][0]), "=&v"(uv[0][1]), "=&v"(acc[2][0]) : "a"(fw1[0][1]), "v"(p[4]), "v"(vHa), "s"(rsW), "s"(sh[13]), "a"(fw1[0][2]), "v"(gm[2]), "v"(gm[3]), "a"(fw1[0][3]), "v"(gz[0]), "v"(gz[1]), "a"(fw1[0][0]), "v"(bb) : "memory"); SB();
  WAITVB(12);
  SB();
  asm volatile("v_mfma_f32_16x16x32_bf16 %0, %5, %6, %0\n\tbuffer_load_dwordx4 %1, %7, %8, %9 offen offset:512 sc1\n\tv_mfma_f32_16x16x32_bf16 %2, %10, %6, %2\n\tds_write_b128 %11, %12 offset:5120\n\tv_mfma_f32_16x16x32_bf16 %3, %13, %6, %3\n\tv_mfma_f32_16x16x32_bf16 %4, %14, %6, %4"
      : "+v"(acc[2][1]), "=&v"(p[4]), "+v"(acc[2][2]), "+v"(acc[2][3]), "+v"(acc[2][0]) : "a"(fw1[1][1]), "v"(p[5]), "v"(vHa), "s"(rsW), "s"(sh[14]), "a"(fw1[1][2]), "v"(rw), "v"(acc[1][1]), "a"(fw1[1][3]), "a"(fw1[1][0]) : "memory"); SB();
  WAITVB(12);
  SB();
  asm volatile("v_mfma_f32_16x16x32_bf16 %0, %5, %6, %0\n\tbuffer_load_dwordx4 %1, %7, %8, %9 offen offset:512 sc1\n\tv_mfma_f32_16x16x32_bf16 %2, %10, %6, %2\n\tds_write_b128 %11, %12 offset:6144\n\tv_mfma_f32_16x16x32_bf16 %3, %13, %6, %3\n\tv_mfma_f32_16x16x32_bf16 %4, %14, %6, %4"
      : "+v"(acc[2][1]), "=&v"(p[5]), "+v"(acc[2][2]), "+v"(acc[2][3]), "+v"(acc[2][0]) : "a"(fw1[2][1]), "v"(p[6]), "v"(vHa), "s"(rsW), "s"(sh[15]), "a"(fw1[2][2]), "v"(rw), "v"(acc[1][2]), "a"(fw1[2][3]), "a"(fw1[2][0]) : "memory"); SB();
  WAITVB(12);
  SB();
  asm volatile("v_mfma_f32_16x16x32_bf16 %0, %5, %6, %0\n\tbuffer_load_dwordx4 %1, %7, %8, %9 offen offset:512 sc1\n\tv_mfma_f32_16x16x32_bf16 %2, %10, %6, %2\n\tds_write_b128 %11, %12 offset:7168\n\tv_mfma_f32_16x16x32_bf16 %3, %13, %6, %3\n\tv_mfma_f32_16x16x32_bf16 %4, %14, %6, %4"
      : "+v"(acc[2][1]), "=&v"(p[6]), "+v"(acc[2][2]), "+v"(acc[2][3]), "+v"(acc[2][0]) : "a"(fw1[3][1]), "v"(p[7]), "v"(vHc), "s"(rsW), "s"(sh[0]), "a"(fw1[3][2]), "v"(rw), "v"(acc[1][3]), "a"(fw1[3][3]), "a"(fw1[3][0]) : "memory"); SB();
  WAITVB(12);
  SB();
  asm volatile("v_mfma_f32_16x16x32_bf16 %0, %3, %4, %0\n\tbuffer_load_dwordx4 %1, %5, %6, %7 offen offset:512 sc1\n\tv_mfma_f32_16x16x32_bf16 %2, %8, %4, %2"
      : "+v"(acc[2][1]), "=&v"(p[7]), "+v"(acc[2][2]) : "a"(fw1[4][1]), "v"(p[8]), "v"(vHc), "s"(rsW), "s"(sh[1]), "a"(fw1[4][2]) : "memory"); SB();
  BAR();
  SB();
  asm volatile("v_mfma_f32_16x16x32_bf16 %0, %5, %6, %0\n\tds_read_b128 %1, %7 offset:5120\n\tds_read_b128 %2, %8 offset:6144\n\tv_mfma_f32_16x16x32_bf16 %3, %9, %6, %3\n\tds_read_b128 %4, %10 offset:7168"
      : "+v"(acc[2][3]), "=&v"(pm[1][0]), "=&v"(pm[1][1]), "+v"(acc[2][0]), "=&v"(pm[1][2]) : "a"(fw1[4][3]), "v"(p[8]), "v"(rr1), "v"(rr2), "a"(fw1[4][0]), "v"(rr3) : "memory"); SB();
  WAITVB(12);
  SB();
  asm volatile("v_mfma_f32_16x16x32_bf16 %0, %5, %6, %0\n\tbuffer_load_dwordx4 %1, %7, %8, %9 offen offset:512 sc1\n\tv_mfma_f32_16x16x32_bf16 %2, %10, %6, %2\n\tv_mfma_f32_16x16x32_bf16 %3, %11, %6, %3\n\tv_mfma_f32_16x16x32_bf16 %4, %12, %6, %4"
      : "+v"(acc[2][1]), "=&v"(p[8]), "+v"(acc[2][2]), "+v"(acc[2][3]), "+v"(acc[2][0]) : "a"(fw1[5][1]), "v"(p[9]), "v"(vHc), "s"(rsW), "s"(sh[2]), "a"(fw1[5][2]), "a"(fw1[5][3]), "a"(fw1[5][0]) : "memory"); SB();
  WAITVB(12);
  SB();
  asm volatile("v_mfma_f32_16x16x32_bf16 %0, %3, %4, %0\n\tbuffer_load_dwordx4 %1, %5, %6, %7 offen offset:512 sc1\n\tv_mfma_f32_16x16x32_bf16 %2, %8, %4, %2"
      : "+v"(acc[2][1]), "=&v"(p[9]), "+v"(acc[2][2]) : "a"(fw1[6][1]), "v"(p[10]), "v"(vHc), "s"(rsW), "s"(sh[3]), "a"(fw1[6][2]) : "memory"); SB();
  WAITL(0);
  asm volatile("v_add_f32 %0, %6, %7\n\tv_mfma_f32_16x16x32_bf16 %1, %8, %9, %1\n\tv_add_f32 %2, %10, %11\n\tv_add_f32 %3, %12, %13\n\tv_mfma_f32_16x16x32_bf16 %4, %14, %9, %4\n\tv_add_f32 %5, %15, %16"
      : "=&v"(gm[0]), "+v"(acc[2][3]), "=&v"(gm[1]), "=&v"(gm[2]), "+v"(acc[2][0]), "=&v"(gm[3]) : "v"(acc[1][0][0]), "v"(pm[1][0][0]), "a"(fw1[6][3]), "v"(p[10]), "v"(acc[1][0][1]), "v"(pm[1][0][1]), "v"(acc[1][0][2]), "v"(pm[1][0][2]), "a"(fw1[6][0]), "v"(acc[1][0][3]), "v"(pm[1][0][3])); SB();
  WAITVB(12);
  SB();
  asm volatile("v_mfma_f32_16x16x32_bf16 %0, %9, %10, %0\n\tbuffer_load_dwordx4 %1, %11, %12, %13 offen offset:512 sc1\n\tv_mfma_f32_16x16x32_bf16 %2, %14, %10, %2\n\tv_add_f32 %3, %15, %16\n\tv_add_f32 %4, %17, %18\n\tv_mfma_f32_16x16x32_bf16 %5, %19, %10, %5\n\tv_add_f32 %6, %20, %21\n\tv_add_f32 %7, %22, %23\n\tv_mfma_f32_16x16x32_bf16 %8, %24, %10, %8"
      : "+v"(acc[2][1]), "=&v"(p[10]), "+v"(acc[2][2]), "=&v"(gz[0]), "=&v"(gz[1]), "+v"(acc[2][3]), "=&v"(gz[2]), "=&v"(gz[3]), "+v"(acc[2][0]) : "a"(fw1[7][1]), "v"(p[11]), "v"(vHc), "s"(rsW), "s"(sh[4]), "a"(fw1[7][2]), "v"(pm[1][1][0]), "v"(pm[1][2][0]), "v"(pm[1][1][1]), "v"(pm[1][2][1]), "a"(fw1[7][3]), "v"(pm[1][1][2]), "v"(pm[1][2][2]), "v"(pm[1][1][3]), "v"(pm[1][2][3]), "a"(fw1[7][0]) : "memory"); SB();
  asm volatile("v_add_f32 %0, %0, %1"
      : "+v"(gm[0]) : "v"(gz[0])); SB();
  WAITVB(12);
  SB();
  asm volatile("v_mfma_f32_16x16x32_bf16 %0, %10, %11, %0\n\tbuffer_load_dwordx4 %1, %12, %13, %14 offen offset:512 sc1\n\tv_mfma_f32_16x16x32_bf16 %2, %15, %11, %2\n\tv_add_f32 %3, %3, %4\n\tv_add_f32 %5, %5, %16\n\tv_mfma_f32_16x16x32_bf16 %6, %17, %11, %6\n\tv_add_f32 %7, %7, %18\n\tv_mul_f32 %8, %19, %19\n\tv_mfma_f32_16x16x32_bf16 %9, %20, %11, %9\n\tv_mul_f32 %4, %3, %3"
      : "+v"(acc[2][1]), "=&v"(p[11]), "+v"(acc[2][2]), "+v"(gm[1]), "+v"(gz[1]), "+v"(gm[2]), "+v"(acc[2][3]), "+v"(gm[3]), "=&v"(gz[0]), "+v"(acc[2][0]) : "a"(fw1[8][1]), "v"(p[12]), "v"(vHc), "s"(rsW), "s"(sh[5]), "a"(fw1[8][2]), "v"(gz[2]), "a"(fw1[8][3]), "v"(gz[3]), "v"(gm[0]), "a"(fw1[8][0]) : "memory"); SB();
  WAITVB(12);
  SB();
  asm volatile("v_mfma_f32_16x16x32_bf16 %0, %9, %10, %0\n\tbuffer_load_dwordx4 %1, %11, %12, %13 offen offset:512 sc1\n\tv_mfma_f32_16x16x32_bf16 %2, %14, %10, %2\n\tv_mul_f32 %3, %15, %15\n\tv_mul_f32 %4, %16, %16\n\tv_mfma_f32_16x16x32_bf16 %5, %17, %10, %5\n\tv_fmaak_f32 %6, %18, %6, 0x40135761\n\tv_fmaak_f32 %7, %18, %7, 0x40135761\n\tv_mfma_f32_16x16x32_bf16 %8, %19, %10, %8\n\tv_fmaak_f32 %3, %18, %3, 0x40135761"
      : "+v"(acc[2][1]), "=&v"(p[12]), "+v"(acc[2][2]), "=&v"(gz[2]), "=&v"(gz[3]), "+v"(acc[2][3]), "+v"(gz[0]), "+v"(gz[1]), "+v"(acc[2][0]) : "a"(fw1[9][1]), "v"(p[13]), "v"(vHc), "s"(rsW), "s"(sh[6]), "a"(fw1[9][2]), "v"(gm[2]), "v"(gm[3]), "a"(fw1[9][3]), "v"(gc), "a"(fw1[9][0]) : "memory"); SB();
  WAITVB(12);
  SB();
  asm volatile("v_mfma_f32_16x16x32_bf16 %0, %9, %10, %0\n\tbuffer_load_dwordx4 %1, %11, %12, %13 offen offset:512 sc1\n\tv_mfma_f32_16x16x32_bf16 %2, %14, %10, %2\n\tv_fmaak_f32 %3, %15, %3, 0x40135761\n\tv_mul_f32 %4, %4, %16\n\tv_mfma_f32_16x16x32_bf16 %5, %17, %10, %5\n\tv_mul_f32 %6, %6, %18\n\tv_mul_f32 %7, %7, %19\n\tv_mfma_f32_16x16x32_bf16 %8, %20, %10, %8\n\tv_mul_f32 %3, %3, %21"
      : "+v"(acc[2][1]), "=&v"(p[13]), "+v"(acc[2][2]), "+v"(gz[3]), "+v"(gz[0]), "+v"(acc[2][3]), "+v"(gz[1]), "+v"(gz[2]), "+v"(acc[2][0]) : "a"(fw1[10][1]), "v"(p[0]), "v"(vHc), "s"(rsW), "s"(sh[7]), "a"(fw1[10][2]), "v"(gc), "v"(gm[0]), "a"(fw1[10][3]), "v"(gm[1]), "v"(gm[2]), "a"(fw1[10][0]), "v"(gm[3]) : "memory"); SB();
  WAITVB(12);
  SB();
  asm volatile("v_mfma_f32_16x16x32_bf16 %0, %7, %8, %0\n\tbuffer_load_dwordx4 %1, %9, %10, %11 offen offset:512 sc1\n\tv_mfma_f32_16x16x32_bf16 %2, %12, %8, %2\n\tv_exp_f32 %3, %3\n\tv_mfma_f32_16x16x32_bf16 %4, %13, %8, %4\n\tv_exp_f32 %5, %5\n\tv_mfma_f32_16x16x32_bf16 %6, %14, %8, %6"
      : "+v"(acc[2][1]), "=&v"(p[0]), "+v"(acc[2][2]), "+v"(gz[0]), "+v"(acc[2][3]), "+v"(gz[1]), "+v"(acc[2][0]) : "a"(fw1[11][1]), "v"(p[1]), "v"(vHc), "s"(rsW), "s"(sh[8]), "a"(fw1[11][2]), "a"(fw1[11][3]), "a"(fw1[11][0]) : "memory"); SB();
  WAITVB(12);
  SB();
  asm volatile("v_mfma_f32_16x16x32_bf16 %0, %8, %9, %0\n\tbuffer_load_dwordx4 %1, %10, %11, %12 offen offset:512 sc1\n\tv_mfma_f32_16x16x32_bf16 %2, %13, %9, %2\n\tv_exp_f32 %3, %3\n\tv_mfma_f32_16x16x32_bf16 %4, %14, %9, %4\n\tv_exp_f32 %5, %5\n\tv_mfma_f32_16x16x32_bf16 %6, %15, %9, %6\n\tv_add_f32 %7, 1.0, %7"
      : "+v"(acc[2][1]), "=&v"(p[1]), "+v"(acc[2][2]), "+v"(gz[2]), "+v"(acc[2][3]), "+v"(gz[3]), "+v"(acc[2][0]), "+v"(gz[0]) : "a"(fw1[12][1]), "v"(p[2]), "v"(vHc), "s"(rsW), "s"(sh[9]), "a"(fw1[12][2]), "a"(fw1[12][3]), "a"(fw1[12][0]) : "memory"); SB();
  WAITVB(12);
  SB();
  asm volatile("v_mfma_f32_16x16x32_bf16 %0, %8, %9, %0\n\tbuffer_load_dwordx4 %1, %10, %11, %12 offen offset:512 sc1\n\tv_mfma_f32_16x16x32_bf16 %2, %13, %9, %2\n\tv_add_f32 %3, 1.0, %3\n\tv_add_f32 %4, 1.0, %4\n\tv_mfma_f32_16x16x32_bf16 %5, %14, %9, %5\n\tv_add_f32 %6, 1.0, %6\n\tv_mfma_f32_16x16x32_bf16 %7, %15, %9, %7"
      : "+v"(acc[2][1]), "=&v"(p[2]), "+v"(acc[2][2]), "+v"(gz[1]), "+v"(gz[2]), "+v"(acc[2][3]), "+v"(gz[3]), "+v"(acc[2][0]) : "a"(fw1[13][1]), "v"(p[3]), "v"(vHc), "s"(rsW), "s"(sh[10]), "a"(fw1[13][2]), "a"(fw1[13][3]), "a"(fw1[13][0]) : "memory"); SB();
  WAITVB(12);
  SB();
  asm volatile("v_mfma_f32_16x16x32_bf16 %0, %7, %8, %0\n\tbuffer_load_dwordx4 %1, %9, %10, %11 offen offset:512 sc1\n\tv_mfma_f32_16x16x32_bf16 %2, %12, %8, %2\n\tv_rcp_f32 %3, %3\n\tv_mfma_f32_16x16x32_bf16 %4, %13, %8, %4\n\tv_rcp_f32 %5, %5\n\tv_mfma_f32_16x16x32_bf16 %6, %14, %8, %6"
      : "+v"(acc[2][1]), "=&v"(p[3]), "+v"(acc[2][2]), "+v"(gz[0]), "+v"(acc[2][3]), "+v"(gz[1]), "+v"(acc[2][0]) : "a"(fw1[14][1]), "v"(p[4]), "v"(vHc), "s"(rsW), "s"(sh[11]), "a"(fw1[14][2]), "a"(fw1[14][3]), "a"(fw1[14][0]) : "memory"); SB();
  WAITVB(12);
  SB();
  asm volatile("v_mfma_f32_16x16x32_bf16 %0, %9, %10, %0\n\tbuffer_load_dwordx4 %1, %11, %12, %13 offen offset:512 sc1\n\tv_mfma_f32_16x16x32_bf16 %2, %14, %10, %2\n\tv_rcp_f32 %3, %3\n\tv_mfma_f32_16x16x32_bf16 %4, %15, %10, %4\n\tv_rcp_f32 %5, %5\n\tv_mfma_f32_16x16x32_bf16 %6, %16, %10, %6\n\tv_fma_f32 %7, -%17, %7, %17\n\tv_fma_f32 %8, -%18, %8, %18"
      : "+v"(acc[2][1]), "=&v"(p[4]), "+v"(acc[2][2]), "+v"(gz[2]), "+v"(acc[2][3]), "+v"(gz[3]), "+v"(acc[2][0]), "+v"(gz[0]), "+v"(gz[1]) : "v"(fw1[15][1]), "v"(p[5]), "v"(vHc), "s"(rsW), "s"(sh[12]), "v"(fw1[15][2]), "v"(fw1[15][3]), "v"(fw1[15][0]), "v"(gm[0]), "v"(gm[1]) : "memory"); SB();
  WAITVB(12);
  SB();
  asm volatile("v_mfma_f32_16x16x32_bf16 %0, %9, %10, 0\n\tbuffer_load_dwordx4 %1, %11, %12, %13 offen offset:512 sc1\n\tv_mfma_f32_16x16x32_bf16 %2, %14, %10, 0\n\tv_fma_f32 %3, -%15, %3, %15\n\tv_fma_f32 %4, -%16, %4, %16\n\tv_mfma_f32_16x16x32_bf16 %5, %17, %10, 0\n\tv_cvt_pk_bf16_f32 %6, %18, %19\n\tv_cvt_pk_bf16_f32 %7, %3, %4\n\tv_mfma_f32_16x16x32_bf16 %8, %20, %10, %21"
      : "=&v"(acc[3][1]), "=&v"(p[5]), "=&v"(acc[3][2]), "+v"(gz[2]), "+v"(gz[3]), "=&v"(acc[3][3]), "=&v"(uv[1][0]), "=&v"(uv[1][1]), "=&v"(acc[3][0]) : "a"(fw1[0][1]), "v"(p[6]), "v"(vHc), "s"(rsW), "s"(sh[13]), "a"(fw1[0][2]), "v"(gm[2]), "v"(gm[3]), "a"(fw1[0][3]), "v"(gz[0]), "v"(gz[1]), "a"(fw1[0][0]), "v"(bb) : "memory"); SB();
  WAITVB(12);
  SB();
  asm volatile("v_mfma_f32_16x16x32_bf16 %0, %6, %7, %0\n\tbuffer_load_dwordx4 %1, %8, %9, %10 offen offset:512 sc1\n\tv_mfma_f32_16x16x32_bf16 %2, %11, %7, %2\n\tds_write_b128 %12, %13 offset:1024\n\tv_mfma_f32_16x16x32_bf16 %3, %14, %7, %3\n\tbuffer_load_dword %4, %15, %9, 0 offen sc1\n\tv_mfma_f32_16x16x32_bf16 %5, %16, %7, %5"
      : "+v"(acc[3][1]), "=&v"(p[6]), "+v"(acc[3][2]), "+v"(acc[3][3]), "=&v"(fq), "+v"(acc[3][0]) : "a"(fw1[1][1]), "v"(p[7]), "v"(vHc), "s"(rsW), "s"(sh[14]), "a"(fw1[1][2]), "v"(rw), "v"(acc[2][1]), "a"(fw1[1][3]), "v"(vFQ), "a"(fw1[1][0]) : "memory"); SB();
  WAITVB(13);
  SB();
  asm volatile("v_mfma_f32_16x16x32_bf16 %0, %5, %6, %0\n\tbuffer_load_dwordx4 %1, %7, %8, %9 offen offset:512 sc1\n\tv_mfma_f32_16x16x32_bf16 %2, %10, %6, %2\n\tds_write_b128 %11, %12 offset:2048\n\tv_mfma_f32_16x16x32_bf16 %3, %13, %6, %3\n\tv_mfma_f32_16x16x32_bf16 %4, %14, %6, %4"
      : "+v"(acc[3][1]), "=&v"(p[7]), "+v"(acc[3][2]), "+v"(acc[3][3]), "+v"(acc[3][0]) : "a"(fw1[2][1]), "v"(p[8]), "v"(vHc), "s"(rsW), "s"(sh[15]), "a"(fw1[2][2]), "v"(rw), "v"(acc[2][2]), "a"(fw1[2][3]), "a"(fw1[2][0]) : "memory"); SB();
  WAITVB(13);
  SB();
  asm volatile("v_mfma_f32_16x16x32_bf16 %0, %5, %6, %0\n\tbuffer_load_dwordx4 %1, %7, %8, %9 offen offset:256 sc1\n\tv_mfma_f32_16x16x32_bf16 %2, %10, %6, %2\n\tds_write_b128 %11, %12 offset:3072\n\tv_mfma_f32_16x16x32_bf16 %3, %13, %6, %3\n\tv_mfma_f32_16x16x32_bf16 %4, %14, %6, %4"
      : "+v"(acc[3][1]), "=&v"(p[8]), "+v"(acc[3][2]), "+v"(acc[3][3]), "+v"(acc[3][0]) : "a"(fw1[3][1]), "v"(p[9]), "v"(vHa), "s"(rsW), "s"(sh[0]), "a"(fw1[3][2]), "v"(rw), "v"(acc[2][3]), "a"(fw1[3][3]), "a"(fw1[3][0]) : "memory"); SB();
  WAITVB(13);
  SB();
  asm volatile("v_mfma_f32_16x16x32_bf16 %0, %3, %4, %0\n\tbuffer_load_dwordx4 %1, %5, %6, %7 offen offset:256 sc1\n\tv_mfma_f32_16x16x32_bf16 %2, %8, %4, %2"
      : "+v"(acc[3][1]), "=&v"(p[9]), "+v"(acc[3][2]) : "a"(fw1[4][1]), "v"(p[10]), "v"(vHa), "s"(rsW), "s"(sh[1]), "a"(fw1[4][2]) : "memory"); SB();
  BAR();
  SB();
  asm volatile("v_mfma_f32_16x16x32_bf16 %0, %5, %6, %0\n\tds_read_b128 %1, %7 offset:1024\n\tds_read_b128 %2, %8 offset:2048\n\tv_mfma_f32_16x16x32_bf16 %3, %9, %6, %3\n\tds_read_b128 %4, %10 offset:3072"
      : "+v"(acc[3][3]), "=&v"(pm[2][0]), "=&v"(pm[2][1]), "+v"(acc[3][0]), "=&v"(pm[2][2]) : "a"(fw1[4][3]), "v"(p[10]), "v"(rr1), "v"(rr2), "a"(fw1[4][0]), "v"(rr3) : "memory"); SB();
  WAITVB(13);
  SB();
  asm volatile("v_mfma_f32_16x16x32_bf16 %0, %5, %6, %0\n\tbuffer_load_dwordx4 %1, %7, %8, %9 offen offset:256 sc1\n\tv_mfma_f32_16x16x32_bf16 %2, %10, %6, %2\n\tv_mfma_f32_16x16x32_bf16 %3, %11, %6, %3\n\tv_mfma_f32_16x16x32_bf16 %4, %12, %6, %4"
      : "+v"(acc[3][1]), "=&v"(p[10]), "+v"(acc[3][2]), "+v"(acc[3][3]), "+v"(acc[3][0]) : "a"(fw1[5][1]), "v"(p[11]), "v"(vHa), "s"(rsW), "s"(sh[2]), "a"(fw1[5][2]), "a"(fw1[5][3]), "a"(fw1[5][0]) : "memory"); SB();
  WAITVB(13);
  SB();
  asm volatile("v_mfma_f32_16x16x32_bf16 %0, %3, %4, %0\n\tbuffer_load_dwordx4 %1, %5, %6, %7 offen offset:256 sc1\n\tv_mfma_f32_16x16x32_bf16 %2, %8, %4, %2"
      : "+v"(acc[3][1]), "=&v"(p[11]), "+v"(acc[3][2]) : "a"(fw1[6][1]), "v"(p[12]), "v"(vHa), "s"(rsW), "s"(sh[3]), "a"(fw1[6][2]) : "memory"); SB();
  WAITL(0);
  asm volatile("v_add_f32 %0, %6, %7\n\tv_mfma_f32_16x16x32_bf16 %1, %8, %9, %1\n\tv_add_f32 %2, %10, %11\n\tv_add_f32 %3, %12, %13\n\tv_mfma_f32_16x16x32_bf16 %4, %14, %9, %4\n\tv_add_f32 %5, %15, %16"
      : "=&v"(gm[0]), "+v"(acc[3][3]), "=&v"(gm[1]), "=&v"(gm[2]), "+v"(acc[3][0]), "=&v"(gm[3]) : "v"(acc[2][0][0]), "v"(pm[2][0][0]), "a"(fw1[6][3]), "v"(p[12]), "v"(acc[2][0][1]), "v"(pm[2][0][1]), "v"(acc[2][0][2]), "v"(pm[2][0][2]), "a"(fw1[6][0]), "v"(acc[2][0][3]), "v"(pm[2][0][3])); SB();
  WAITVB(13);
  SB();
  asm volatile("v_mfma_f32_16x16x32_bf16 %0, %9, %10, %0\n\tbuffer_load_dwordx4 %1, %11, %12, %13 offen offset:256 sc1\n\tv_mfma_f32_16x16x32_bf16 %2, %14, %10, %2\n\tv_add_f32 %3, %15, %16\n\tv_add_f32 %4, %17, %18\n\tv_mfma_f32_16x16x32_bf16 %5, %19, %10, %5\n\tv_add_f32 %6, %20, %21\n\tv_add_f32 %7, %22, %23\n\tv_mfma_f32_16x16x32_bf16 %8, %24, %10, %8"
      : "+v"(acc[3][1]), "=&v"(p[12]), "+v"(acc[3][2]), "=&v"(gz[0]), "=&v"(gz[1]), "+v"(acc[3][3]), "=&v"(gz[2]), "=&v"(gz[3]), "+v"(acc[3][0]) : "a"(fw1[7][1]), "v"(p[13]), "v"(vHa), "s"(rsW), "s"(sh[4]), "a"(fw1[7][2]), "v"(pm[2][1][0]), "v"(pm[2][2][0]), "v"(pm[2][1][1]), "v"(pm[2][2][1]), "a"(fw1[7][3]), "v"(pm[2][1][2]), "v"(pm[2][2][2]), "v"(pm[2][1][3]), "v"(pm[2][2][3]), "a"(fw1[7][0]) : "memory"); SB();
  asm volatile("v_add_f32 %0, %0, %1"
      : "+v"(gm[0]) : "v"(gz[0])); SB();
  WAITVB(13);
  SB();
  asm volatile("v_mfma_f32_16x16x32_bf16 %0, %10, %11, %0\n\tbuffer_load_dwordx4 %1, %12, %13, %14 offen offset:256 sc1\n\tv_mfma_f32_16x16x32_bf16 %2, %15, %11, %2\n\tv_add_f32 %3, %3, %4\n\tv_add_f32 %5, %5, %16\n\tv_mfma_f32_16x16x32_bf16 %6, %17, %11, %6\n\tv_add_f32 %7, %7, %18\n\tv_mul_f32 %8, %19, %19\n\tv_mfma_f32_16x16x32_bf16 %9, %20, %11, %9\n\tv_mul_f32 %4, %3, %3"
      : "+v"(acc[3][1]), "=&v"(p[13]), "+v"(acc[3][2]), "+v"(gm[1]), "+v"(gz[1]), "+v"(gm[2]), "+v"(acc[3][3]), "+v"(gm[3]), "=&v"(gz[0]), "+v"(acc[3][0]) : "a"(fw1[8][1]), "v"(p[0]), "v"(vHa), "s"(rsW), "s"(sh[5]), "a"(fw1[8][2]), "v"(gz[2]), "a"(fw1[8][3]), "v"(gz[3]), "v"(gm[0]), "a"(fw1[8][0]) : "memory"); SB();
  WAITVB(13);
  SB();
  asm volatile("v_mfma_f32_16x16x32_bf16 %0, %9, %10, %0\n\tbuffer_load_dwordx4 %1, %11, %12, %13 offen offset:256 sc1\n\tv_mfma_f32_16x16x32_bf16 %2, %14, %10, %2\n\tv_mul_f32 %3, %15, %15\n\tv_mul_f32 %4, %16, %16\n\tv_mfma_f32_16x16x32_bf16 %5, %17, %10, %5\n\tv_fmaak_f32 %6, %18, %6, 0x40135761\n\tv_fmaak_f32 %7, %18, %7, 0x40135761\n\tv_mfma_f32_16x16x32_bf16 %8, %19, %10, %8\n\tv_fmaak_f32 %3, %18, %3, 0x40135761"
      : "+v"(acc[3][1]), "=&v"(p[0]), "+v"(acc[3][2]), "=&v"(gz[2]), "=&v"(gz[3]), "+v"(acc[3][3]), "+v"(gz[0]), "+v"(gz[1]), "+v"(acc[3][0]) : "a"(fw1[9][1]), "v"(p[1]), "v"(vHa), "s"(rsW), "s"(sh[6]), "a"(fw1[9][2]), "v"(gm[2]), "v"(gm[3]), "a"(fw1[9][3]), "v"(gc), "a"(fw1[9][0]) : "memory"); SB();
  WAITVB(13);
  SB();
  asm volatile("v_mfma_f32_16x16x32_bf16 %0, %9, %10, %0\n\tbuffer_load_dwordx4 %1, %11, %12, %13 offen offset:256 sc1\n\tv_mfma_f32_16x16x32_bf16 %2, %14, %10, %2\n\tv_fmaak_f32 %3, %15, %3, 0x40135761\n\tv_mul_f32 %4, %4, %16\n\tv_mfma_f32_16x16x32_bf16 %5, %17, %10, %5\n\tv_mul_f32 %6, %6, %18\n\tv_mul_f32 %7, %7, %19\n\tv_mfma_f32_16x16x32_bf16 %8, %20, %10, %8\n\tv_mul_f32 %3, %3, %21"
      : "+v"(acc[3][1]), "=&v"(p[1]), "+v"(acc[3][2]), "+v"(gz[3]), "+v"(gz[0]), "+v"(acc[3][3]), "+v"(gz[1]), "+v"(gz[2]), "+v"(acc[3][0]) : "a"(fw1[10][1]), "v"(p[2]), "v"(vHa), "s"(rsW), "s"(sh[7]), "a"(fw1[10][2]), "v"(gc), "v"(gm[0]), "a"(fw1[10][3]), "v"(gm[1]), "v"(gm[2]), "a"(fw1[10][0]), "v"(gm[3]) : "memory"); SB();
  WAITVB(13);
  SB();
  asm volatile("v_mfma_f32_16x16x32_bf16 %0, %7, %8, %0\n\tbuffer_load_dwordx4 %1, %9, %10, %11 offen offset:256 sc1\n\tv_mfma_f32_16x16x32_bf16 %2, %12, %8, %2\n\tv_exp_f32 %3, %3\n\tv_mfma_f32_16x16x32_bf16 %4, %13, %8, %4\n\tv_exp_f32 %5, %5\n\tv_mfma_f32_16x16x32_bf16 %6, %14, %8, %6"
      : "+v"(acc[3][1]), "=&v"(p[2]), "+v"(acc[3][2]), "+v"(gz[0]), "+v"(acc[3][3]), "+v"(gz[1]), "+v"(acc[3][0]) : "a"(fw1[11][1]), "v"(p[3]), "v"(vHa), "s"(rsW), "s"(sh[8]), "a"(fw1[11][2]), "a"(fw1[11][3]), "a"(fw1[11][0]) : "memory"); SB();
  WAITVB(13);
  SB();
  asm volatile("v_mfma_f32_16x16x32_bf16 %0, %8, %9, %0\n\tbuffer_load_dwordx4 %1, %10, %11, %12 offen offset:256 sc1\n\tv_mfma_f32_16x16x32_bf16 %2, %13, %9, %2\n\tv_exp_f32 %3, %3\n\tv_mfma_f32_16x16x32_bf16 %4, %14, %9, %4\n\tv_exp_f32 %5, %5\n\tv_mfma_f32_16x16x32_bf16 %6, %15, %9, %6\n\tv_add_f32 %7, 1.0, %7"
      : "+v"(acc[3][1]), "=&v"(p[3]), "+v"(acc[3][2]), "+v"(gz[2]), "+v"(acc[3][3]), "+v"(gz[3]), "+v"(acc[3][0]), "+v"(gz[0]) : "a"(fw1[12][1]), "v"(p[4]), "v"(vHa), "s"(rsW), "s"(sh[9]), "a"(fw1[12][2]), "a"(fw1[12][3]), "a"(fw1[12][0]) : "memory"); SB();
  WAITVB(13);
  SB();
  asm volatile("v_mfma_f32_16x16x32_bf16 %0, %8, %9, %0\n\tbuffer_load_dwordx4 %1, %10, %11, %12 offen offset:256 sc1\n\tv_mfma_f32_16x16x32_bf16 %2, %13, %9, %2\n\tv_add_f32 %3, 1.0, %3\n\tv_add_f32 %4, 1.0, %4\n\tv_mfma_f32_16x16x32_bf16 %5, %14, %9, %5\n\tv_add_f32 %6, 1.0, %6\n\tv_mfma_f32_16x16x32_bf16 %7, %15, %9, %7"
      : "+v"(acc[3][1]), "=&v"(p[4]), "+v"(acc[3][2]), "+v"(gz[1]), "+v"(gz[2]), "+v"(acc[3][3]), "+v"(gz[3]), "+v"(acc[3][0]) : "a"(fw1[13][1]), "v"(p[5]), "v"(vHa), "s"(rsW), "s"(sh[10]), "a"(fw1[13][2]), "a"(fw1[13][3]), "a"(fw1[13][0]) : "memory"); SB();
  WAITVB(13);
  SB();
  asm volatile("v_mfma_f32_16x16x32_bf16 %0, %7, %8, %0\n\tbuffer_load_dwordx4 %1, %9, %10, %11 offen offset:256 sc1\n\tv_mfma_f32_16x16x32_bf16 %2, %12, %8, %2\n\tv_rcp_f32 %3, %3\n\tv_mfma_f32_16x16x32_bf16 %4, %13, %8, %4\n\tv_rcp_f32 %5, %5\n\tv_mfma_f32_16x16x32_bf16 %6, %14, %8, %6"
      : "+v"(acc[3][1]), "=&v"(p[5]), "+v"(acc[3][2]), "+v"(gz[0]), "+v"(acc[3][3]), "+v"(gz[1]), "+v"(acc[3][0]) : "a"(fw1[14][1]), "v"(p[6]), "v"(vHa), "s"(rsW), "s"(sh[11]), "a"(fw1[14][2]), "a"(fw1[14][3]), "a"(fw1[14][0]) : "memory"); SB();
  WAITVB(12);
  SB();
  asm volatile("v_mfma_f32_16x16x32_bf16 %0, %9, %10, %0\n\tbuffer_load_dwordx4 %1, %11, %12, %13 offen offset:256 sc1\n\tv_mfma_f32_16x16x32_bf16 %2, %14, %10, %2\n\tv_rcp_f32 %3, %3\n\tv_mfma_f32_16x16x32_bf16 %4, %15, %10, %4\n\tv_rcp_f32 %5, %5\n\tv_mfma_f32_16x16x32_bf16 %6, %16, %10, %6\n\tv_fma_f32 %7, -%17, %7, %17\n\tv_fma_f32 %8, -%18, %8, %18"
      : "+v"(acc[3][1]), "=&v"(p[6]), "+v"(acc[3][2]), "+v"(gz[2]), "+v"(acc[3][3]), "+v"(gz[3]), "+v"(acc[3][0]), "+v"(gz[0]), "+v"(gz[1]) : "v"(fw1[15][1]), "v"(p[7]), "v"(vHa), "s"(rsW), "s"(sh[12]), "v"(fw1[15][2]), "v"(fw1[15][3]), "v"(fw1[15][0]), "v"(gm[0]), "v"(gm[1]) : "memory"); SB();
  STAMP(9);
  WAITVB(12);
  SB();
  asm volatile("v_mfma_f32_16x16x32_bf16 %0, %9, %10, 0\n\tbuffer_load_dwordx4 %1, %11, %12, %13 offen offset:256 sc1\n\tv_mfma_f32_16x16x32_bf16 %2, %14, %10, 0\n\tv_fma_f32 %3, -%15, %3, %15\n\tv_fma_f32 %4, -%16, %4, %16\n\tv_mfma_f32_16x16x32_bf16 %5, %17, %10, 0\n\tv_cvt_pk_bf16_f32 %6, %18, %19\n\tv_cvt_pk_bf16_f32 %7, %3, %4\n\tv_mfma_f32_16x16x32_bf16 %8, %20, %10, %21"
      : "=&v"(acc[4][1]), "=&v"(p[7]), "=&v"(acc[4][2]), "+v"(gz[2]), "+v"(gz[3]), "=&v"(acc[4][3]), "=&v"(uv[2][0]), "=&v"(uv[2][1]), "=&v"(acc[4][0]) : "a"(fw1[0][1]), "v"(p[8]), "v"(vHa), "s"(rsW), "s"(sh[13]), "a"(fw1[0][2]), "v"(gm[2]), "v"(gm[3]), "a"(fw1[0][3]), "v"(gz[0]), "v"(gz[1]), "a"(fw1[0][0]), "v"(bb) : "memory"); SB();
  WAITVB(12);
  SB();
  asm volatile("v_mfma_f32_16x16x32_bf16 %0, %5, %6, %0\n\tbuffer_load_dwordx4 %1, %7, %8, %9 offen offset:256 sc1\n\tv_mfma_f32_16x16x32_bf16 %2, %10, %6, %2\n\tds_write_b128 %11, %12 offset:5120\n\tv_mfma_f32_16x16x32_bf16 %3, %13, %6, %3\n\tv_mfma_f32_16x16x32_bf16 %4, %14, %6, %4"
      : "+v"(acc[4][1]), "=&v"(p[8]), "+v"(acc[4][2]), "+v"(acc[4][3]), "+v"(acc[4][0]) : "a"(fw1[1][1]), "v"(p[9]), "v"(vHa), "s"(rsW), "s"(sh[14]), "a"(fw1[1][2]), "v"(rw), "v"(acc[3][1]), "a"(fw1[1][3]), "a"(fw1[1][0]) : "memory"); SB();
  WAITVB(12);
  SB();
  asm volatile("v_mfma_f32_16x16x32_bf16 %0, %5, %6, %0\n\tbuffer_load_dwordx4 %1, %7, %8, %9 offen offset:256 sc1\n\tv_mfma_f32_16x16x32_bf16 %2, %10, %6, %2\n\tds_write_b128 %11, %12 offset:6144\n\tv_mfma_f32_16x16x32_bf16 %3, %13, %6, %3\n\tv_mfma_f32_16x16x32_bf16 %4, %14, %6, %4"
      : "+v"(acc[4][1]), "=&v"(p[9]), "+v"(acc[4][2]), "+v"(acc[4][3]), "+v"(acc[4][0]) : "a"(fw1[2][1]), "v"(p[10]), "v"(vHa), "s"(rsW), "s"(sh[15]), "a"(fw1[2][2]), "v"(rw), "v"(acc[3][2]), "a"(fw1[2][3]), "a"(fw1[2][0]) : "memory"); SB();
  WAITVB(12);
  SB();
  asm volatile("v_mfma_f32_16x16x32_bf16 %0, %5, %6, %0\n\tbuffer_load_dwordx4 %1, %7, %8, %9 offen offset:256 sc1\n\tv_mfma_f32_16x16x32_bf16 %2, %10, %6, %2\n\tds_write_b128 %11, %12 offset:7168\n\tv_mfma_f32_16x16x32_bf16 %3, %13, %6, %3\n\tv_mfma_f32_16x16x32_bf16 %4, %14, %6, %4"
      : "+v"(acc[4][1]), "=&v"(p[10]), "+v"(acc[4][2]), "+v"(acc[4][3]), "+v"(acc[4][0]) : "a"(fw1[3][1]), "v"(p[11]), "v"(vHc), "s"(rsW), "s"(sh[0]), "a"(fw1[3][2]), "v"(rw), "v"(acc[3][3]), "a"(fw1[3][3]), "a"(fw1[3][0]) : "memory"); SB();
  WAITVB(12);
  SB();
  asm volatile("v_mfma_f32_16x16x32_bf16 %0, %3, %4, %0\n\tbuffer_load_dwordx4 %1, %5, %6, %7 offen offset:256 sc1\n\tv_mfma_f32_16x16x32_bf16 %2, %8, %4, %2"
      : "+v"(acc[4][1]), "=&v"(p[11]), "+v"(acc[4][2]) : "a"(fw1[4][1]), "v"(p[12]), "v"(vHc), "s"(rsW), "s"(sh[1]), "a"(fw1[4][2]) : "memory"); SB();
  BAR();
  SB();
  asm volatile("v_mfma_f32_16x16x32_bf16 %0, %5, %6, %0\n\tds_read_b128 %1, %7 offset:5120\n\tds_read_b128 %2, %8 offset:6144\n\tv_mfma_f32_16x16x32_bf16 %3, %9, %6, %3\n\tds_read_b128 %4, %10 offset:7168"
      : "+v"(acc[4][3]), "=&v"(pm[3][0]), "=&v"(pm[3][1]), "+v"(acc[4][0]), "=&v"(pm[3][2]) : "a"(fw1[4][3]), "v"(p[12]), "v"(rr1), "v"(rr2), "a"(fw1[4][0]), "v"(rr3) : "memory"); SB();
  WAITVB(12);
  SB();
  asm volatile("v_mfma_f32_16x16x32_bf16 %0, %5, %6, %0\n\tbuffer_load_dwordx4 %1, %7, %8, %9 offen offset:256 sc1\n\tv_mfma_f32_16x16x32_bf16 %2, %10, %6, %2\n\tv_mfma_f32_16x16x32_bf16 %3, %11, %6, %3\n\tv_mfma_f32_16x16x32_bf16 %4, %12, %6, %4"
      : "+v"(acc[4][1]), "=&v"(p[12]), "+v"(acc[4][2]), "+v"(acc[4][3]), "+v"(acc[4][0]) : "a"(fw1[5][1]), "v"(p[13]), "v"(vHc), "s"(rsW), "s"(sh[2]), "a"(fw1[5][2]), "a"(fw1[5][3]), "a"(fw1[5][0]) : "memory"); SB();
  WAITVB(12);
  SB();
  asm volatile("v_mfma_f32_16x16x32_bf16 %0, %3, %4, %0\n\tbuffer_load_dwordx4 %1, %5, %6, %7 offen offset:256 sc1\n\tv_mfma_f32_16x16x32_bf16 %2, %8, %4, %2"
      : "+v"(acc[4][1]), "=&v"(p[13]), "+v"(acc[4][2]) : "a"(fw1[6][1]), "v"(p[0]), "v"(vHc), "s"(rsW), "s"(sh[3]), "a"(fw1[6][2]) : "memory"); SB();
  WAITL(0);
  asm volatile("v_add_f32 %0, %6, %7\n\tv_mfma_f32_16x16x32_bf16 %1, %8, %9, %1\n\tv_add_f32 %2, %10, %11\n\tv_add_f32 %3, %12, %13\n\tv_mfma_f32_16x16x32_bf16 %4, %14, %9, %4\n\tv_add_f32 %5, %15, %16"
      : "=&v"(gm[0]), "+v"(acc[4][3]), "=&v"(gm[1]), "=&v"(gm[2]), "+v"(acc[4][0]), "=&v"(gm[3]) : "v"(acc[3][0][0]), "v"(pm[3][0][0]), "a"(fw1[6][3]), "v"(p[0]), "v"(acc[3][0][1]), "v"(pm[3][0][1]), "v"(acc[3][0][2]), "v"(pm[3][0][2]), "a"(fw1[6][0]), "v"(acc[3][0][3]), "v"(pm[3][0][3])); SB();
  WAITVB(12);
  SB();
  asm volatile("v_mfma_f32_16x16x32_bf16 %0, %9, %10, %0\n\tbuffer_load_dwordx4 %1, %11, %12, %13 offen offset:256 sc1\n\tv_mfma_f32_16x16x32_bf16 %2, %14, %10, %2\n\tv_add_f32 %3, %15, %16\n\tv_add_f32 %4, %17, %18\n\tv_mfma_f32_16x16x32_bf16 %5, %19, %10, %5\n\tv_add_f32 %6, %20, %21\n\tv_add_f32 %7, %22, %23\n\tv_mfma_f32_16x16x32_bf16 %8, %24, %10, %8"
      : "+v"(acc[4][1]), "=&v"(p[0]), "+v"(acc[4][2]), "=&v"(gz[0]), "=&v"(gz[1]), "+v"(acc[4][3]), "=&v"(gz[2]), "=&v"(gz[3]), "+v"(acc[4][0]) : "a"(fw1[7][1]), "v"(p[1]), "v"(vHc), "s"(rsW), "s"(sh[4]), "a"(fw1[7][2]), "v"(pm[3][1][0]), "v"(pm[3][2][0]), "v"(pm[3][1][1]), "v"(pm[3][2][1]), "a"(fw1[7][3]), "v"(pm[3][1][2]), "v"(pm[3][2][2]), "v"(pm[3][1][3]), "v"(pm[3][2][3]), "a"(fw1[7][0]) : "memory"); SB();
  asm volatile("v_add_f32 %0, %0, %1"
      : "+v"(gm[0]) : "v"(gz[0])); SB();
  WAITVB(12);
  SB();
  asm volatile("v_mfma_f32_16x16x32_bf16 %0, %10, %11, %0\n\tbuffer_load_dwordx4 %1, %12, %13, %14 offen offset:256 sc1\n\tv_mfma_f32_16x16x32_bf16 %2, %15, %11, %2\n\tv_add_f32 %3, %3, %4\n\tv_add_f32 %5, %5, %16\n\tv_mfma_f32_16x16x32_bf16 %6, %17, %11, %6\n\tv_add_f32 %7, %7, %18\n\tv_mul_f32 %8, %19, %19\n\tv_mfma_f32_16x16x32_bf16 %9, %20, %11, %9\n\tv_mul_f32 %4, %3, %3"
      : "+v"(acc[4][1]), "=&v"(p[1]), "+v"(acc[4][2]), "+v"(gm[1]), "+v"(gz[1]), "+v"(gm[2]), "+v"(acc[4][3]), "+v"(gm[3]), "=&v"(gz[0]), "+v"(acc[4][0]) : "a"(fw1[8][1]), "v"(p[2]), "v"(vHc), "s"(rsW), "s"(sh[5]), "a"(fw1[8][2]), "v"(gz[2]), "a"(fw1[8][3]), "v"(gz[3]), "v"(gm[0]), "a"(fw1[8][0]) : "memory"); SB();
  WAITVB(12);
  SB();
  asm volatile("v_mfma_f32_16x16x32_bf16 %0, %9, %10, %0\n\tbuffer_load_dwordx4 %1, %11, %12, %13 offen offset:256 sc1\n\tv_mfma_f32_16x16x32_bf16 %2, %14, %10, %2\n\tv_mul_f32 %3, %15, %15\n\tv_mul_f32 %4, %16, %16\n\tv_mfma_f32_16x16x32_bf16 %5, %17, %10, %5\n\tv_fmaak_f32 %6, %18, %6, 0x40135761\n\tv_fmaak_f32 %7, %18, %7, 0x40135761\n\tv_mfma_f32_16x16x32_bf16 %8, %19, %10, %8\n\tv_fmaak_f32 %3, %18, %3, 0x40135761"
      : "+v"(acc[4][1]), "=&v"(p[2]), "+v"(acc[4][2]), "=&v"(gz[2]), "=&v"(gz[3]), "+v"(acc[4][3]), "+v"(gz[0]), "+v"(gz[1]), "+v"(acc[4][0]) : "a"(fw1[9][1]), "v"(p[3]), "v"(vHc), "s"(rsW), "s"(sh[6]), "a"(fw1[9][2]), "v"(gm[2]), "v"(gm[3]), "a"(fw1[9][3]), "v"(gc), "a"(fw1[9][0]) : "memory"); SB();
  WAITVB(12);
  SB();
  asm volatile("v_mfma_f32_16x16x32_bf16 %0, %9, %10, %0\n\tbuffer_load_dwordx4 %1, %11, %12, %13 offen offset:256 sc1\n\tv_mfma_f32_16x16x32_bf16 %2, %14, %10, %2\n\tv_fmaak_f32 %3, %15, %3, 0x40135761\n\tv_mul_f32 %4, %4, %16\n\tv_mfma_f32_16x16x32_bf16 %5, %17, %10, %5\n\tv_mul_f32 %6, %6, %18\n\tv_mul_f32 %7, %7, %19\n\tv_mfma_f32_16x16x32_bf16 %8, %20, %10, %8\n\tv_mul_f32 %3, %3, %21"
      : "+v"(acc[4][1]), "=&v"(p[3]), "+v"(acc[4][2]), "+v"(gz[3]), "+v"(gz[0]), "+v"(acc[4][3]), "+v"(gz[1]), "+v"(gz[2]), "+v"(acc[4][0]) : "a"(fw1[10][1]), "v"(p[4]), "v"(vHc), "s"(rsW), "s"(sh[7]), "a"(fw1[10][2]), "v"(gc), "v"(gm[0]), "a"(fw1[10][3]), "v"(gm[1]), "v"(gm[2]), "a"(fw1[10][0]), "v"(gm[3]) : "memory"); SB();
  WAITVB(12);
  SB();
  asm volatile("v_mfma_f32_16x16x32_bf16 %0, %7, %8, %0\n\tbuffer_load_dwordx4 %1, %9, %10, %11 offen offset:256 sc1\n\tv_mfma_f32_16x16x32_bf16 %2, %12, %8, %2\n\tv_exp_f32 %3, %3\n\tv_mfma_f32_16x16x32_bf16 %4, %13, %8, %4\n\tv_exp_f32 %5, %5\n\tv_mfma_f32_16x16x32_bf16 %6, %14, %8, %6"
      : "+v"(acc[4][1]), "=&v"(p[4]), "+v"(acc[4][2]), "+v"(gz[0]), "+v"(acc[4][3]), "+v"(gz[1]), "+v"(acc[4][0]) : "a"(fw1[11][1]), "v"(p[5]), "v"(vHc), "s"(rsW), "s"(sh[8]), "a"(fw1[11][2]), "a"(fw1[11][3]), "a"(fw1[11][0]) : "memory"); SB();
  WAITVB(12);
  SB();
  asm volatile("v_mfma_f32_16x16x32_bf16 %0, %8, %9, %0\n\tbuffer_load_dwordx4 %1, %10, %11, %12 offen offset:256 sc1\n\tv_mfma_f32_16x16x32_bf16 %2, %13, %9, %2\n\tv_exp_f32 %3, %3\n\tv_mfma_f32_16x16x32_bf16 %4, %14, %9, %4\n\tv_exp_f32 %5, %5\n\tv_mfma_f32_16x16x32_bf16 %6, %15, %9, %6\n\tv_add_f32 %7, 1.0, %7"
      : "+v"(acc[4][1]), "=&v"(p[5]), "+v"(acc[4][2]), "+v"(gz[2]), "+v"(acc[4][3]), "+v"(gz[3]), "+v"(acc[4][0]), "+v"(gz[0]) : "a"(fw1[12][1]), "v"(p[6]), "v"(vHc), "s"(rsW), "s"(sh[9]), "a"(fw1[12][2]), "a"(fw1[12][3]), "a"(fw1[12][0]) : "memory"); SB();
  WAITVB(12);
  SB();
  asm volatile("v_mfma_f32_16x16x32_bf16 %0, %8, %9, %0\n\tbuffer_load_dwordx4 %1, %10, %11, %12 offen offset:256 sc1\n\tv_mfma_f32_16x16x32_bf16 %2, %13, %9, %2\n\tv_add_f32 %3, 1.0, %3\n\tv_add_f32 %4, 1.0, %4\n\tv_mfma_f32_16x16x32_bf16 %5, %14, %9, %5\n\tv_add_f32 %6, 1.0, %6\n\tv_mfma_f32_16x16x32_bf16 %7, %15, %9, %7"
      : "+v"(acc[4][1]), "=&v"(p[6]), "+v"(acc[4][2]), "+v"(gz[1]), "+v"(gz[2]), "+v"(acc[4][3]), "+v"(gz[3]), "+v"(acc[4][0]) : "a"(fw1[13][1]), "v"(p[7]), "v"(vHc), "s"(rsW), "s"(sh[10]), "a"(fw1[13][2]), "a"(fw1[13][3]), "a"(fw1[13][0]) : "memory"); SB();
  WAITVB(12);
  SB();
  asm volatile("v_mfma_f32_16x16x32_bf16 %0, %7, %8, %0\n\tbuffer_load_dwordx4 %1, %9, %10, %11 offen offset:256 sc1\n\tv_mfma_f32_16x16x32_bf16 %2, %12, %8, %2\n\tv_rcp_f32 %3, %3\n\tv_mfma_f32_16x16x32_bf16 %4, %13, %8, %4\n\tv_rcp_f32 %5, %5\n\tv_mfma_f32_16x16x32_bf16 %6, %14, %8, %6"
      : "+v"(acc[4][1]), "=&v"(p[7]), "+v"(acc[4][2]), "+v"(gz[0]), "+v"(acc[4][3]), "+v"(gz[1]), "+v"(acc[4][0]) : "a"(fw1[14][1]), "v"(p[8]), "v"(vHc), "s"(rsW), "s"(sh[11]), "a"(fw1[14][2]), "a"(fw1[14][3]), "a"(fw1[14][0]) : "memory"); SB();
  WAITVB(12);
  SB();
  asm volatile("v_mfma_f32_16x16x32_bf16 %0, %9, %10, %0\n\tbuffer_load_dwordx4 %1, %11, %12, %13 offen offset:256 sc1\n\tv_mfma_f32_16x16x32_bf16 %2, %14, %10, %2\n\tv_rcp_f32 %3, %3\n\tv_mfma_f32_16x16x32_bf16 %4, %15, %10, %4\n\tv_rcp_f32 %5, %5\n\tv_mfma_f32_16x16x32_bf16 %6, %16, %10, %6\n\tv_fma_f32 %7, -%17, %7, %17\n\tv_fma_f32 %8, -%18, %8, %18"
      : "+v"(acc[4][1]), "=&v"(p[8]), "+v"(acc[4][2]), "+v"(gz[2]), "+v"(acc[4][3]), "+v"(gz[3]), "+v"(acc[4][0]), "+v"(gz[0]), "+v"(gz[1]) : "v"(fw1[15][1]), "v"(p[9]), "v"(vHc), "s"(rsW), "s"(sh[12]), "v"(fw1[15][2]), "v"(fw1[15][3]), "v"(fw1[15][0]), "v"(gm[0]), "v"(gm[1]) : "memory"); SB();
  WAITVB(12);
  SB();
  asm volatile("v_mfma_f32_16x16x32_bf16 %0, %9, %10, 0\n\tbuffer_load_dwordx4 %1, %11, %12, %13 offen offset:256 sc1\n\tv_mfma_f32_16x16x32_bf16 %2, %14, %10, 0\n\tv_fma_f32 %3, -%15, %3, %15\n\tv_fma_f32 %4, -%16, %4, %16\n\tv_mfma_f32_16x16x32_bf16 %5, %17, %10, 0\n\tv_cvt_pk_bf16_f32 %6, %18, %19\n\tv_cvt_pk_bf16_f32 %7, %3, %4\n\tv_mfma_f32_16x16x32_bf16 %8, %20, %10, %21"
      : "=&v"(acc[5][1]), "=&v"(p[9]), "=&v"(acc[5][2]), "+v"(gz[2]), "+v"(gz[3]), "=&v"(acc[5][3]), "=&v"(uv[3][0]), "=&v"(uv[3][1]), "=&v"(acc[5][0]) : "a"(fw1[0][1]), "v"(p[10]), "v"(vHc), "s"(rsW), "s"(sh[13]), "a"(fw1[0][2]), "v"(gm[2]), "v"(gm[3]), "a"(fw1[0][3]), "v"(gz[0]), "v"(gz[1]), "a"(fw1[0][0]), "v"(bb) : "memory"); SB();
  WAITVB(12);
  SB();
  asm volatile("v_mfma_f32_16x16x32_bf16 %0, %5, %6, %0\n\tbuffer_load_dwordx4 %1, %7, %8, %9 offen offset:256 sc1\n\tv_mfma_f32_16x16x32_bf16 %2, %10, %6, %2\n\tds_write_b128 %11, %12 offset:1024\n\tv_mfma_f32_16x16x32_bf16 %3, %13, %6, %3\n\tv_mfma_f32_16x16x32_bf16 %4, %14, %6, %4"
      : "+v"(acc[5][1]), "=&v"(p[10]), "+v"(acc[5][2]), "+v"(acc[5][3]), "+v"(acc[5][0]) : "a"(fw1[1][1]), "v"(p[11]), "v"(vHc), "s"(rsW), "s"(sh[14]), "a"(fw1[1][2]), "v"(rw), "v"(acc[4][1]), "a"(fw1[1][3]), "a"(fw1[1][0]) : "memory"); SB();
  WAITVB(12);
  SB();
  asm volatile("v_mfma_f32_16x16x32_bf16 %0, %5, %6, %0\n\tbuffer_load_dwordx4 %1, %7, %8, %9 offen offset:256 sc1\n\tv_mfma_f32_16x16x32_bf16 %2, %10, %6, %2\n\tds_write_b128 %11, %12 offset:2048\n\tv_mfma_f32_16x16x32_bf16 %3, %13, %6, %3\n\tv_mfma_f32_16x16x32_bf16 %4, %14, %6, %4"
      : "+v"(acc[5][1]), "=&v"(p[11]), "+v"(acc[5][2]), "+v"(acc[5][3]), "+v"(acc[5][0]) : "a"(fw1[2][1]), "v"(p[12]), "v"(vHc), "s"(rsW), "s"(sh[15]), "a"(fw1[2][2]), "v"(rw), "v"(acc[4][2]), "a"(fw1[2][3]), "a"(fw1[2][0]) : "memory"); SB();
  WAITVB(12);
  SB();
  asm volatile("v_mfma_f32_16x16x32_bf16 %0, %5, %6, %0\n\tbuffer_load_dwordx4 %1, %7, %8, %9 offen offset:0 sc1\n\tv_mfma_f32_16x16x32_bf16 %2, %10, %6, %2\n\tds_write_b128 %11, %12 offset:3072\n\tv_mfma_f32_16x16x32_bf16 %3, %13, %6, %3\n\tv_mfma_f32_16x16x32_bf16 %4, %14, %6, %4"
      : "+v"(acc[5][1]), "=&v"(p[12]), "+v"(acc[5][2]), "+v"(acc[5][3]), "+v"(acc[5][0]) : "a"(fw1[3][1]), "v"(p[13]), "v"(vHb), "s"(rsW), "s"(sh[0]), "a"(fw1[3][2]), "v"(rw), "v"(acc[4][3]), "a"(fw1[3][3]), "a"(fw1[3][0]) : "memory"); SB();
  WAITVB(12);
  SB();
  asm volatile("v_mfma_f32_16x16x32_bf16 %0, %3, %4, %0\n\tbuffer_load_dwordx4 %1, %5, %6, %7 offen offset:0 sc1\n\tv_mfma_f32_16x16x32_bf16 %2, %8, %4, %2"
      : "+v"(acc[5][1]), "=&v"(p[13]), "+v"(acc[5][2]) : "a"(fw1[4][1]), "v"(p[0]), "v"(vHb), "s"(rsW), "s"(sh[1]), "a"(fw1[4][2]) : "memory"); SB();
  BAR();
  SB();
  asm volatile("v_mfma_f32_16x16x32_bf16 %0, %5, %6, %0\n\tds_read_b128 %1, %7 offset:1024\n\tds_read_b128 %2, %8 offset:2048\n\tv_mfma_f32_16x16x32_bf16 %3, %9, %6, %3\n\tds_read_b128 %4, %10 offset:3072"
      : "+v"(acc[5][3]), "=&v"(pm[4][0]), "=&v"(pm[4][1]), "+v"(acc[5][0]), "=&v"(pm[4][2]) : "a"(fw1[4][3]), "v"(p[0]), "v"(rr1), "v"(rr2), "a"(fw1[4][0]), "v"(rr3) : "memory"); SB();
  WAITVB(12);
  SB();
  asm volatile("v_mfma_f32_16x16x32_bf16 %0, %5, %6, %0\n\tbuffer_load_dwordx4 %1, %7, %8, %9 offen offset:0 sc1\n\tv_mfma_f32_16x16x32_bf16 %2, %10, %6, %2\n\tv_mfma_f32_16x16x32_bf16 %3, %11, %6, %3\n\tv_mfma_f32_16x16x32_bf16 %4, %12, %6, %4"
      : "+v"(acc[5][1]), "=&v"(p[0]), "+v"(acc[5][2]), "+v"(acc[5][3]), "+v"(acc[5][0]) : "a"(fw1[5][1]), "v"(p[1]), "v"(vHb), "s"(rsW), "s"(sh[2]), "a"(fw1[5][2]), "a"(fw1[5][3]), "a"(fw1[5][0]) : "memory"); SB();
  WAITVB(12);
  SB();
  asm volatile("v_mfma_f32_16x16x32_bf16 %0, %3, %4, %0\n\tbuffer_load_dwordx4 %1, %5, %6, %7 offen offset:0 sc1\n\tv_mfma_f32_16x16x32_bf16 %2, %8, %4, %2"
      : "+v"(acc[5][1]), "=&v"(p[1]), "+v"(acc[5][2]) : "a"(fw1[6][1]), "v"(p[2]), "v"(vHb), "s"(rsW), "s"(sh[3]), "a"(fw1[6][2]) : "memory"); SB();
  WAITL(0);
  asm volatile("v_add_f32 %0, %6, %7\n\tv_mfma_f32_16x16x32_bf16 %1, %8, %9, %1\n\tv_add_f32 %2, %10, %11\n\tv_add_f32 %3, %12, %13\n\tv_mfma_f32_16x16x32_bf16 %4, %14, %9, %4\n\tv_add_f32 %5, %15, %16"
      : "=&v"(gm[0]), "+v"(acc[5][3]), "=&v"(gm[1]), "=&v"(gm[2]), "+v"(acc[5][0]), "=&v"(gm[3]) : "v"(acc[4][0][0]), "v"(pm[4][0][0]), "a"(fw1[6][3]), "v"(p[2]), "v"(acc[4][0][1]), "v"(pm[4][0][1]), "v"(acc[4][0][2]), "v"(pm[4][0][2]), "a"(fw1[6][0]), "v"(acc[4][0][3]), "v"(pm[4][0][3])); SB();
  WAITVB(12);
  SB();
  asm volatile("v_mfma_f32_16x16x32_bf16 %0, %9, %10, %0\n\tbuffer_load_dwordx4 %1, %11, %12, %13 offen offset:0 sc1\n\tv_mfma_f32_16x16x32_bf16 %2, %14, %10, %2\n\tv_add_f32 %3, %15, %16\n\tv_add_f32 %4, %17, %18\n\tv_mfma_f32_16x16x32_bf16 %5, %19, %10, %5\n\tv_add_f32 %6, %20, %21\n\tv_add_f32 %7, %22, %23\n\tv_mfma_f32_16x16x32_bf16 %8, %24, %10, %8"
      : "+v"(acc[5][1]), "=&v"(p[2]), "+v"(acc[5][2]), "=&v"(gz[0]), "=&v"(gz[1]), "+v"(acc[5][3]), "=&v"(gz[2]), "=&v"(gz[3]), "+v"(acc[5][0]) : "a"(fw1[7][1]), "v"(p[3]), "v"(vHb), "s"(rsW), "s"(sh[4]), "a"(fw1[7][2]), "v"(pm[4][1][0]), "v"(pm[4][2][0]), "v"(pm[4][1][1]), "v"(pm[4][2][1]), "a"(fw1[7][3]), "v"(pm[4][1][2]), "v"(pm[4][2][2]), "v"(pm[4][1][3]), "v"(pm[4][2][3]), "a"(fw1[7][0]) : "memory"); SB();
  asm volatile("v_add_f32 %0, %0, %1"
      : "+v"(gm[0]) : "v"(gz[0])); SB();
  WAITVB(12);
  SB();
  asm volatile("v_mfma_f32_16x16x32_bf16 %0, %10, %11, %0\n\tbuffer_load_dwordx4 %1, %12, %13, %14 offen offset:0 sc1\n\tv_mfma_f32_16x16x32_bf16 %2, %15, %11, %2\n\tv_add_f32 %3, %3, %4\n\tv_add_f32 %5, %5, %16\n\tv_mfma_f32_16x16x32_bf16 %6, %17, %11, %6\n\tv_add_f32 %7, %7, %18\n\tv_mul_f32 %8, %19, %19\n\tv_mfma_f32_16x16x32_bf16 %9, %20, %11, %9\n\tv_mul_f32 %4, %3, %3"
      : "+v"(acc[5][1]), "=&v"(p[3]), "+v"(acc[5][2]), "+v"(gm[1]), "+v"(gz[1]), "+v"(gm[2]), "+v"(acc[5][3]), "+v"(gm[3]), "=&v"(gz[0]), "+v"(acc[5][0]) : "a"(fw1[8][1]), "v"(p[4]), "v"(vHb), "s"(rsW), "s"(sh[5]), "a"(fw1[8][2]), "v"(gz[2]), "a"(fw1[8][3]), "v"(gz[3]), "v"(gm[0]), "a"(fw1[8][0]) : "memory"); SB();
  WAITVB(12);
  SB();
  asm volatile("v_mfma_f32_16x16x32_bf16 %0, %9, %10, %0\n\tbuffer_load_dwordx4 %1, %11, %12, %13 offen offset:0 sc1\n\tv_mfma_f32_16x16x32_bf16 %2, %14, %10, %2\n\tv_mul_f32 %3, %15, %15\n\tv_mul_f32 %4, %16, %16\n\tv_mfma_f32_16x16x32_bf16 %5, %17, %10, %5\n\tv_fmaak_f32 %6, %18, %6, 0x40135761\n\tv_fmaak_f32 %7, %18, %7, 0x40135761\n\tv_mfma_f32_16x16x32_bf16 %8, %19, %10, %8\n\tv_fmaak_f32 %3, %18, %3, 0x40135761"
      : "+v"(acc[5][1]), "=&v"(p[4]), "+v"(acc[5][2]), "=&v"(gz[2]), "=&v"(gz[3]), "+v"(acc[5][3]), "+v"(gz[0]), "+v"(gz[1]), "+v"(acc[5][0]) : "a"(fw1[9][1]), "v"(p[5]), "v"(vHb), "s"(rsW), "s"(sh[6]), "a"(fw1[9][2]), "v"(gm[2]), "v"(gm[3]), "a"(fw1[9][3]), "v"(gc), "a"(fw1[9][0]) : "memory"); SB();
  WAITVB(12);
  SB();
  asm volatile("v_mfma_f32_16x16x32_bf16 %0, %9, %10, %0\n\tbuffer_load_dwordx4 %1, %11, %12, %13 offen offset:0 sc1\n\tv_mfma_f32_16x16x32_bf16 %2, %14, %10, %2\n\tv_fmaak_f32 %3, %15, %3, 0x40135761\n\tv_mul_f32 %4, %4, %16\n\tv_mfma_f32_16x16x32_bf16 %5, %17, %10, %5\n\tv_mul_f32 %6, %6, %18\n\tv_mul_f32 %7, %7, %19\n\tv_mfma_f32_16x16x32_bf16 %8, %20, %10, %8\n\tv_mul_f32 %3, %3, %21"
      : "+v"(acc[5][1]), "=&v"(p[5]), "+v"(acc[5][2]), "+v"(gz[3]), "+v"(gz[0]), "+v"(acc[5][3]), "+v"(gz[1]), "+v"(gz[2]), "+v"(acc[5][0]) : "a"(fw1[10][1]), "v"(p[6]), "v"(vHb), "s"(rsW), "s"(sh[7]), "a"(fw1[10][2]), "v"(gc), "v"(gm[0]), "a"(fw1[10][3]), "v"(gm[1]), "v"(gm[2]), "a"(fw1[10][0]), "v"(gm[3]) : "memory"); SB();
  WAITVB(12);
  SB();
  asm volatile("v_mfma_f32_16x16x32_bf16 %0, %7, %8, %0\n\tbuffer_load_dwordx4 %1, %9, %10, %11 offen offset:0 sc1\n\tv_mfma_f32_16x16x32_bf16 %2, %12, %8, %2\n\tv_exp_f32 %3, %3\n\tv_mfma_f32_16x16x32_bf16 %4, %13, %8, %4\n\tv_exp_f32 %5, %5\n\tv_mfma_f32_16x16x32_bf16 %6, %14, %8, %6"
      : "+v"(acc[5][1]), "=&v"(p[6]), "+v"(acc[5][2]), "+v"(gz[0]), "+v"(acc[5][3]), "+v"(gz[1]), "+v"(acc[5][0]) : "a"(fw1[11][1]), "v"(p[7]), "v"(vHb), "s"(rsW), "s"(sh[8]), "a"(fw1[11][2]), "a"(fw1[11][3]), "a"(fw1[11][0]) : "memory"); SB();
  WAITVB(12);
  SB();
  asm volatile("v_mfma_f32_16x16x32_bf16 %0, %8, %9, %0\n\tbuffer_load_dwordx4 %1, %10, %11, %12 offen offset:0 sc1\n\tv_mfma_f32_16x16x32_bf16 %2, %13, %9, %2\n\tv_exp_f32 %3, %3\n\tv_mfma_f32_16x16x32_bf16 %4, %14, %9, %4\n\tv_exp_f32 %5, %5\n\tv_mfma_f32_16x16x32_bf16 %6, %15, %9, %6\n\tv_add_f32 %7, 1.0, %7"
      : "+v"(acc[5][1]), "=&v"(p[7]), "+v"(acc[5][2]), "+v"(gz[2]), "+v"(acc[5][3]), "+v"(gz[3]), "+v"(acc[5][0]), "+v"(gz[0]) : "a"(fw1[12][1]), "v"(p[8]), "v"(vHb), "s"(rsW), "s"(sh[9]), "a"(fw1[12][2]), "a"(fw1[12][3]), "a"(fw1[12][0]) : "memory"); SB();
  WAITVB(12);
  SB();
  asm volatile("v_mfma_f32_16x16x32_bf16 %0, %8, %9, %0\n\tbuffer_load_dwordx4 %1, %10, %11, %12 offen offset:0 sc1\n\tv_mfma_f32_16x16x32_bf16 %2, %13, %9, %2\n\tv_add_f32 %3, 1.0, %3\n\tv_add_f32 %4, 1.0, %4\n\tv_mfma_f32_16x16x32_bf16 %5, %14, %9, %5\n\tv_add_f32 %6, 1.0, %6\n\tv_mfma_f32_16x16x32_bf16 %7, %15, %9, %7"
      : "+v"(acc[5][1]), "=&v"(p[8]), "+v"(acc[5][2]), "+v"(gz[1]), "+v"(gz[2]), "+v"(acc[5][3]), "+v"(gz[3]), "+v"(acc[5][0]) : "a"(fw1[13][1]), "v"(p[9]), "v"(vHb), "s"(rsW), "s"(sh[10]), "a"(fw1[13][2]), "a"(fw1[13][3]), "a"(fw1[13][0]) : "memory"); SB();
  WAITVB(12);
  SB();
  asm volatile("v_mfma_f32_16x16x32_bf16 %0, %7, %8, %0\n\tbuffer_load_dwordx4 %1, %9, %10, %11 offen offset:0 sc1\n\tv_mfma_f32_16x16x32_bf16 %2, %12, %8, %2\n\tv_rcp_f32 %3, %3\n\tv_mfma_f32_16x16x32_bf16 %4, %13, %8, %4\n\tv_rcp_f32 %5, %5\n\tv_mfma_f32_16x16x32_bf16 %6, %14, %8, %6"
      : "+v"(acc[5][1]), "=&v"(p[9]), "+v"(acc[5][2]), "+v"(gz[0]), "+v"(acc[5][3]), "+v"(gz[1]), "+v"(acc[5][0]) : "a"(fw1[14][1]), "v"(p[10]), "v"(vHb), "s"(rsW), "s"(sh[11]), "a"(fw1[14][2]), "a"(fw1[14][3]), "a"(fw1[14][0]) : "memory"); SB();
  WAITVB(12);
  SB();
  asm volatile("v_mfma_f32_16x16x32_bf16 %0, %9, %10, %0\n\tbuffer_load_dwordx4 %1, %11, %12, %13 offen offset:0 sc1\n\tv_mfma_f32_16x16x32_bf16 %2, %14, %10, %2\n\tv_rcp_f32 %3, %3\n\tv_mfma_f32_16x16x32_bf16 %4, %15, %10, %4\n\tv_rcp_f32 %5, %5\n\tv_mfma_f32_16x16x32_bf16 %6, %16, %10, %6\n\tv_fma_f32 %7, -%17, %7, %17\n\tv_fma_f32 %8, -%18, %8, %18"
      : "+v"(acc[5][1]), "=&v"(p[10]), "+v"(acc[5][2]), "+v"(gz[2]), "+v"(acc[5][3]), "+v"(gz[3]), "+v"(acc[5][0]), "+v"(gz[0]), "+v"(gz[1]) : "v"(fw1[15][1]), "v"(p[11]), "v"(vHb), "s"(rsW), "s"(sh[12]), "v"(fw1[15][2]), "v"(fw1[15][3]), "v"(fw1[15][0]), "v"(gm[0]), "v"(gm[1]) : "memory"); SB();
  WAITVB(12);
  SB();
  asm volatile("v_mfma_f32_16x16x32_bf16 %0, %9, %10, 0\n\tbuffer_load_dwordx4 %1, %11, %12, %13 offen offset:0 sc1\n\tv_mfma_f32_16x16x32_bf16 %2, %14, %10, 0\n\tv_fma_f32 %3, -%15, %3, %15\n\tv_fma_f32 %4, -%16, %4, %16\n\tv_mfma_f32_16x16x32_bf16 %5, %17, %10, 0\n\tv_cvt_pk_bf16_f32 %6, %18, %19\n\tv_cvt_pk_bf16_f32 %7, %3, %4\n\tv_mfma_f32_16x16x32_bf16 %8, %20, %10, %21"
      : "=&v"(acc[6][1]), "=&v"(p[11]), "=&v"(acc[6][2]), "+v"(gz[2]), "+v"(gz[3]), "=&v"(acc[6][3]), "=&v"(uv[4][0]), "=&v"(uv[4][1]), "=&v"(acc[6][0]) : "a"(fw1[0][1]), "v"(p[12]), "v"(vHb), "s"(rsW), "s"(sh[13]), "a"(fw1[0][2]), "v"(gm[2]), "v"(gm[3]), "a"(fw1[0][3]), "v"(gz[0]), "v"(gz[1]), "a"(fw1[0][0]), "v"(bb) : "memory"); SB();
  WAITVB(12);
  SB();
  asm volatile("v_mfma_f32_16x16x32_bf16 %0, %5, %6, %0\n\tbuffer_load_dwordx4 %1, %7, %8, %9 offen offset:0 sc1\n\tv_mfma_f32_16x16x32_bf16 %2, %10, %6, %2\n\tds_write_b128 %11, %12 offset:5120\n\tv_mfma_f32_16x16x32_bf16 %3, %13, %6, %3\n\tv_mfma_f32_16x16x32_bf16 %4, %14, %6, %4"
      : "+v"(acc[6][1]), "=&v"(p[12]), "+v"(acc[6][2]), "+v"(acc[6][3]), "+v"(acc[6][0]) : "a"(fw1[1][1]), "v"(p[13]), "v"(vHb), "s"(rsW), "s"(sh[14]), "a"(fw1[1][2]), "v"(rw), "v"(acc[5][1]), "a"(fw1[1][3]), "a"(fw1[1][0]) : "memory"); SB();
  WAITVB(12);
  SB();
  asm volatile("v_mfma_f32_16x16x32_bf16 %0, %5, %6, %0\n\tbuffer_load_dwordx4 %1, %7, %8, %9 offen offset:0 sc1\n\tv_mfma_f32_16x16x32_bf16 %2, %10, %6, %2\n\tds_write_b128 %11, %12 offset:6144\n\tv_mfma_f32_16x16x32_bf16 %3, %13, %6, %3\n\tv_mfma_f32_16x16x32_bf16 %4, %14, %6, %4"
      : "+v"(acc[6][1]), "=&v"(p[13]), "+v"(acc[6][2]), "+v"(acc[6][3]), "+v"(acc[6][0]) : "a"(fw1[2][1]), "v"(p[0]), "v"(vHb), "s"(rsW), "s"(sh[15]), "a"(fw1[2][2]), "v"(rw), "v"(acc[5][2]), "a"(fw1[2][3]), "a"(fw1[2][0]) : "memory"); SB();
  WAITVB(12);
  SB();
  asm volatile("v_mfma_f32_16x16x32_bf16 %0, %5, %6, %0\n\tv_permlane16_swap_b32 %1, %2\n\tv_permlane16_swap_b32 %3, %4"
      : "+v"(acc[6][1]), "+v"(uv[0][0]), "+v"(uv[1][0]), "+v"(uv[0][1]), "+v"(uv[1][1]) : "a"(fw1[3][1]), "v"(p[1])); SB();
  HS16(vU, sU, 0, ((u32x4){uv[0][0], uv[0][1], uv[1][0], uv[1][1]}));
  SB();
  asm volatile("v_mfma_f32_16x16x32_bf16 %0, %6, %7, %0\n\tds_write_b128 %8, %9 offset:7168\n\tv_mfma_f32_16x16x32_bf16 %1, %10, %7, %1\n\tv_permlane16_swap_b32 %2, %3\n\tv_permlane16_swap_b32 %4, %5"
      : "+v"(acc[6][2]), "+v"(acc[6][3]), "+v"(uv[2][0]), "+v"(uv[3][0]), "+v"(uv[2][1]), "+v"(uv[3][1]) : "a"(fw1[3][2]), "v"(p[1]), "v"(rw), "v"(acc[5][3]), "a"(fw1[3][3]) : "memory"); SB();
  HS16(vU, sU, 512, ((u32x4){uv[2][0], uv[2][1], uv[3][0], uv[3][1]}));
  SB();
  asm volatile("v_mfma_f32_16x16x32_bf16 %0, %1, %2, %0"
      : "+v"(acc[6][0]) : "a"(fw1[3][0]), "v"(p[1])); SB();
  WAITVB(13);
  SB();
  asm volatile("v_mfma_f32_16x16x32_bf16 %0, %2, %3, %0\n\ts_add_u32 m0, %4, 0\n\ts_nop 0\n\tv_mfma_f32_16x16x32_bf16 %1, %5, %3, %1"
      : "+v"(acc[6][1]), "+v"(acc[6][2]) : "a"(fw1[4][1]), "v"(p[2]), "s"(ldsO), "a"(fw1[4][2]) : "memory", "scc"); SB();
  BAR();
  SB();
  asm volatile("v_mfma_f32_16x16x32_bf16 %0, %5, %6, %0\n\tds_read_b128 %1, %7 offset:5120\n\tds_read_b128 %2, %8 offset:6144\n\tv_mfma_f32_16x16x32_bf16 %3, %9, %6, %3\n\tds_read_b128 %4, %10 offset:7168"
      : "+v"(acc[6][3]), "=&v"(pm[5][0]), "=&v"(pm[5][1]), "+v"(acc[6][0]), "=&v"(pm[5][2]) : "a"(fw1[4][3]), "v"(p[2]), "v"(rr1), "v"(rr2), "a"(fw1[4][0]), "v"(rr3) : "memory"); SB();
  WAITVB(12);
  SB();
  asm volatile("v_mfma_f32_16x16x32_bf16 %0, %4, %5, %0\n\tbuffer_load_dwordx4 %6, %7, %8 offen offset:0  lds\n\tv_mfma_f32_16x16x32_bf16 %1, %9, %5, %1\n\tv_mfma_f32_16x16x32_bf16 %2, %10, %5, %2\n\tbuffer_load_dwordx4 %11, %7, %8 offen offset:1024  lds\n\tv_mfma_f32_16x16x32_bf16 %3, %12, %5, %3"
      : "+v"(acc[6][1]), "+v"(acc[6][2]), "+v"(acc[6][3]), "+v"(acc[6][0]) : "a"(fw1[5][1]), "v"(p[3]), "v"(vW2[0]), "s"(rs2), "s"(sw[0]), "a"(fw1[5][2]), "a"(fw1[5][3]), "v"(vW2[1]), "a"(fw1[5][0]) : "memory"); SB();
  WAITVB(13);
  SB();
  asm volatile("v_mfma_f32_16x16x32_bf16 %0, %1, %2, %0"
      : "+v"(acc[6][1]) : "a"(fw1[6][1]), "v"(p[4])); SB();
  WAITL(0);
  asm volatile("v_add_f32 %0, %9, %10\n\tv_mfma_f32_16x16x32_bf16 %1, %11, %12, %1\n\tv_add_f32 %2, %13, %14\n\tv_add_f32 %3, %15, %16\n\tv_mfma_f32_16x16x32_bf16 %4, %17, %12, %4\n\tv_add_f32 %5, %18, %19\n\tv_add_f32 %6, %20, %21\n\tv_mfma_f32_16x16x32_bf16 %7, %22, %12, %7\n\tv_add_f32 %8, %23, %24"
      : "=&v"(gm[0]), "+v"(acc[6][2]), "=&v"(gm[1]), "=&v"(gm[2]), "+v"(acc[6][3]), "=&v"(gm[3]), "=&v"(gz[0]), "+v"(acc[6][0]), "=&v"(gz[1]) : "v"(acc[5][0][0]), "v"(pm[5][0][0]), "a"(fw1[6][2]), "v"(p[4]), "v"(acc[5][0][1]), "v"(pm[5][0][1]), "v"(acc[5][0][2]), "v"(pm[5][0][2]), "a"(fw1[6][3]), "v"(acc[5][0][3]), "v"(pm[5][0][3]), "v"(pm[5][1][0]), "v"(pm[5][2][0]), "a"(fw1[6][0]), "v"(pm[5][1][1]), "v"(pm[5][2][1])); SB();
  WAITVB(12);
  SB();
  asm volatile("v_mfma_f32_16x16x32_bf16 %0, %11, %12, %0\n\tv_add_f32 %1, %13, %14\n\tv_add_f32 %2, %15, %16\n\tv_mfma_f32_16x16x32_bf16 %3, %17, %12, %3\n\tv_add_f32 %4, %4, %5\n\tv_add_f32 %6, %6, %18\n\tv_mfma_f32_16x16x32_bf16 %7, %19, %12, %7\n\tv_add_f32 %8, %8, %1\n\tv_add_f32 %9, %9, %2\n\tv_mfma_f32_16x16x32_bf16 %10, %20, %12, %10\n\tv_mul_f32 %5, %4, %4"
      : "+v"(acc[6][1]), "=&v"(gz[2]), "=&v"(gz[3]), "+v"(acc[6][2]), "+v"(gm[0]), "+v"(gz[0]), "+v"(gm[1]), "+v"(acc[6][3]), "+v"(gm[2]), "+v"(gm[3]), "+v"(acc[6][0]) : "a"(fw1[7][1]), "v"(p[5]), "v"(pm[5][1][2]), "v"(pm[5][2][2]), "v"(pm[5][1][3]), "v"(pm[5][2][3]), "a"(fw1[7][2]), "v"(gz[1]), "a"(fw1[7][3]), "a"(fw1[7][0])); SB();
  WAITVB(11);
  SB();
  asm volatile("v_mfma_f32_16x16x32_bf16 %0, %8, %9, %0\n\tv_mul_f32 %1, %10, %10\n\tv_mul_f32 %2, %11, %11\n\tv_mfma_f32_16x16x32_bf16 %3, %12, %9, %3\n\tv_mul_f32 %4, %13, %13\n\tv_fmaak_f32 %5, %14, %5, 0x40135761\n\tv_mfma_f32_16x16x32_bf16 %6, %15, %9, %6\n\tv_fmaak_f32 %1, %14, %1, 0x40135761\n\tv_fmaak_f32 %2, %14, %2, 0x40135761\n\tv_mfma_f32_16x16x32_bf16 %7, %16, %9, %7\n\tv_fmaak_f32 %4, %14, %4, 0x40135761"
      : "+v"(acc[6][1]), "=&v"(gz[1]), "=&v"(gz[2]), "+v"(acc[6][2]), "=&v"(gz[3]), "+v"(gz[0]), "+v"(acc[6][3]), "+v"(acc[6][0]) : "a"(fw1[8][1]), "v"(p[6]), "v"(gm[1]), "v"(gm[2]), "a"(fw1[8][2]), "v"(gm[3]), "v"(gc), "a"(fw1[8][3]), "a"(fw1[8][0])); SB();
  WAITVB(10);
  SB();
  asm volatile("v_mfma_f32_16x16x32_bf16 %0, %8, %9, %0\n\tv_mul_f32 %1, %1, %10\n\tv_mul_f32 %2, %2, %11\n\tv_mfma_f32_16x16x32_bf16 %3, %12, %9, %3\n\tv_mul_f32 %4, %4, %13\n\tv_mul_f32 %5, %5, %14\n\tv_mfma_f32_16x16x32_bf16 %6, %15, %9, %6\n\tv_exp_f32 %1, %1\n\tv_mfma_f32_16x16x32_bf16 %7, %16, %9, %7"
      : "+v"(acc[6][1]), "+v"(gz[0]), "+v"(gz[1]), "+v"(acc[6][2]), "+v"(gz[2]), "+v"(gz[3]), "+v"(acc[6][3]), "+v"(acc[6][0]) : "a"(fw1[9][1]), "v"(p[7]), "v"(gm[0]), "v"(gm[1]), "a"(fw1[9][2]), "v"(gm[2]), "v"(gm[3]), "a"(fw1[9][3]), "a"(fw1[9][0])); SB();
  WAITVB(9);
  SB();
  asm volatile("v_mfma_f32_16x16x32_bf16 %0, %8, %9, %0\n\tv_exp_f32 %1, %1\n\tv_mfma_f32_16x16x32_bf16 %2, %10, %9, %2\n\tv_exp_f32 %3, %3\n\tv_mfma_f32_16x16x32_bf16 %4, %11, %9, %4\n\tv_exp_f32 %5, %5\n\tv_mfma_f32_16x16x32_bf16 %6, %12, %9, %6\n\tv_add_f32 %7, 1.0, %7"
      : "+v"(acc[6][1]), "+v"(gz[1]), "+v"(acc[6][2]), "+v"(gz[2]), "+v"(acc[6][3]), "+v"(gz[3]), "+v"(acc[6][0]), "+v"(gz[0]) : "a"(fw1[10][1]), "v"(p[8]), "a"(fw1[10][2]), "a"(fw1[10][3]), "a"(fw1[10][0])); SB();
  WAITVB(8);
  SB();
  asm volatile("v_mfma_f32_16x16x32_bf16 %0, %8, %9, %0\n\tv_add_f32 %1, 1.0, %1\n\tv_add_f32 %2, 1.0, %2\n\tv_mfma_f32_16x16x32_bf16 %3, %10, %9, %3\n\tv_add_f32 %4, 1.0, %4\n\tv_mfma_f32_16x16x32_bf16 %5, %11, %9, %5\n\tv_rcp_f32 %6, %6\n\tv_mfma_f32_16x16x32_bf16 %7, %12, %9, %7"
      : "+v"(acc[6][1]), "+v"(gz[1]), "+v"(gz[2]), "+v"(acc[6][2]), "+v"(gz[3]), "+v"(acc[6][3]), "+v"(gz[0]), "+v"(acc[6][0]) : "a"(fw1[11][1]), "v"(p[9]), "a"(fw1[11][2]), "a"(fw1[11][3]), "a"(fw1[11][0])); SB();
  WAITVB(7);
  SB();
  asm volatile("v_mfma_f32_16x16x32_bf16 %0, %8, %9, %0\n\tv_rcp_f32 %1, %1\n\tv_mfma_f32_16x16x32_bf16 %2, %10, %9, %2\n\tv_rcp_f32 %3, %3\n\tv_mfma_f32_16x16x32_bf16 %4, %11, %9, %4\n\tv_rcp_f32 %5, %5\n\tv_mfma_f32_16x16x32_bf16 %6, %12, %9, %6\n\tv_fma_f32 %7, -%13, %7, %13"
      : "+v"(acc[6][1]), "+v"(gz[1]), "+v"(acc[6][2]), "+v"(gz[2]), "+v"(acc[6][3]), "+v"(gz[3]), "+v"(acc[6][0]), "+v"(gz[0]) : "a"(fw1[12][1]), "v"(p[10]), "a"(fw1[12][2]), "a"(fw1[12][3]), "a"(fw1[12][0]), "v"(gm[0])); SB();
  WAITVB(6);
  SB();
  asm volatile("v_mfma_f32_16x16x32_bf16 %0, %9, %10, %0\n\tv_fma_f32 %1, -%11, %1, %11\n\tv_fma_f32 %2, -%12, %2, %12\n\tv_mfma_f32_16x16x32_bf16 %3, %13, %10, %3\n\tv_fma_f32 %4, -%14, %4, %14\n\tv_cvt_pk_bf16_f32 %5, %15, %1\n\tv_mfma_f32_16x16x32_bf16 %6, %16, %10, %6\n\tv_cvt_pk_bf16_f32 %7, %2, %4\n\tv_mfma_f32_16x16x32_bf16 %8, %17, %10, %8"
      : "+v"(acc[6][1]), "+v"(gz[1]), "+v"(gz[2]), "+v"(acc[6][2]), "+v"(gz[3]), "=&v"(uv[5][0]), "+v"(acc[6][3]), "=&v"(uv[5][1]), "+v"(acc[6][0]) : "a"(fw1[13][1]), "v"(p[11]), "v"(gm[1]), "v"(gm[2]), "a"(fw1[13][2]), "v"(gm[3]), "v"(gz[0]), "a"(fw1[13][3]), "a"(fw1[13][0])); SB();
  WAITVB(5);
  SB();
  asm volatile("v_mfma_f32_16x16x32_bf16 %0, %5, %6, %0\n\tv_permlane16_swap_b32 %1, %2\n\tv_permlane16_swap_b32 %3, %4"
      : "+v"(acc[6][1]), "+v"(uv[4][0]), "+v"(uv[5][0]), "+v"(uv[4][1]), "+v"(uv[5][1]) : "a"(fw1[14][1]), "v"(p[12])); SB();
  HS16(vU, sU, 256, ((u32x4){uv[4][0], uv[4][1], uv[5][0], uv[5][1]}));
  SB();
  asm volatile("v_mfma_f32_16x16x32_bf16 %0, %3, %4, %0\n\tbuffer_load_dwordx4 %5, %6, %7 offen offset:2048  lds\n\tv_mfma_f32_16x16x32_bf16 %1, %8, %4, %1\n\tv_mfma_f32_16x16x32_bf16 %2, %9, %4, %2"
      : "+v"(acc[6][2]), "+v"(acc[6][3]), "+v"(acc[6][0]) : "a"(fw1[14][2]), "v"(p[12]), "v"(vW2[2]), "s"(rs2), "s"(sw[0]), "a"(fw1[14][3]), "a"(fw1[14][0]) : "memory"); SB();
  WAITVB(6);
  SB();
  asm volatile("v_mfma_f32_16x16x32_bf16 %0, %4, %5, %0\n\tbuffer_load_dwordx4 %6, %7, %8 offen offset:3072  lds\n\tv_mfma_f32_16x16x32_bf16 %1, %9, %5, %1\n\tv_mfma_f32_16x16x32_bf16 %2, %10, %5, %2\n\ts_add_u32 m0, %11, 4096\n\ts_nop 0\n\tv_mfma_f32_16x16x32_bf16 %3, %12, %5, %3"
      : "+v"(acc[6][1]), "+v"(acc[6][2]), "+v"(acc[6][3]), "+v"(acc[6][0]) : "v"(fw1[15][1]), "v"(p[13]), "v"(vW2[3]), "s"(rs2), "s"(sw[0]), "v"(fw1[15][2]), "v"(fw1[15][3]), "s"(ldsO), "v"(fw1[15][0]) : "memory", "scc"); SB();
  STAMP(8);
  asm volatile("s_nop 4\n\tds_write_b128 %0, %1 offset:1024\n\tds_write_b128 %0, %2 offset:2048\n\tds_write_b128 %0, %3 offset:3072\n\tbuffer_load_dwordx4 %4, %5, %6 offen offset:0  lds\n\tbuffer_load_dwordx4 %7, %5, %6 offen offset:1024  lds\n\tbuffer_load_dwordx4 %8, %5, %6 offen offset:2048  lds\n\tbuffer_load_dwordx4 %9, %5, %6 offen offset:3072  lds\n\ts_add_u32 m0, %10, 18432\n\ts_nop 0\n\tbuffer_load_dwordx4 %4, %5, %11 offen offset:0  lds\n\tbuffer_load_dwordx4 %7, %5, %11 offen offset:1024  lds\n\tbuffer_load_dwordx4 %8, %5, %11 offen offset:2048  lds\n\tbuffer_load_dwordx4 %9, %5, %11 offen offset:3072  lds"
      :  : "v"(rw), "v"(acc[6][1]), "v"(acc[6][2]), "v"(acc[6][3]), "v"(vW2[0]), "s"(rs2), "s"(sw[1]), "v"(vW2[1]), "v"(vW2[2]), "v"(vW2[3]), "s"(ldsS), "s"(sw[2]) : "memory", "scc"); SB();
  BAR();
  asm volatile("ds_read_b128 %0, %3 offset:1024\n\tds_read_b128 %1, %4 offset:2048\n\tds_read_b128 %2, %5 offset:3072"
      : "=&v"(pm[6][0]), "=&v"(pm[6][1]), "=&v"(pm[6][2]) : "v"(rr1), "v"(rr2), "v"(rr3) : "memory"); SB();
  WAITL(0);
  asm volatile("v_add_f32 %0, %12, %13\n\tv_add_f32 %1, %14, %15\n\tv_add_f32 %2, %16, %17\n\tv_add_f32 %3, %18, %19\n\tv_add_f32 %4, %20, %21\n\tv_add_f32 %5, %22, %23\n\tv_add_f32 %6, %24, %25\n\tv_add_f32 %7, %26, %27\n\tv_add_f32 %0, %0, %4\n\tv_add_f32 %1, %1, %5\n\tv_add_f32 %2, %2, %6\n\tv_add_f32 %3, %3, %7\n\tv_mul_f32 %4, %0, %0\n\tv_mul_f32 %5, %1, %1\n\tv_mul_f32 %6, %2, %2\n\tv_mul_f32 %7, %3, %3\n\tv_fmaak_f32 %4, %28, %4, 0x40135761\n\tv_fmaak_f32 %5, %28, %5, 0x40135761\n\tv_fmaak_f32 %6, %28, %6, 0x40135761\n\tv_fmaak_f32 %7, %28, %7, 0x40135761\n\tv_mul_f32 %4, %4, %0\n\tv_mul_f32 %5, %5, %1\n\tv_mul_f32 %6, %6, %2\n\tv_mul_f32 %7, %7, %3\n\tv_exp_f32 %4, %4\n\tv_exp_f32 %5, %5\n\tv_exp_f32 %6, %6\n\tv_exp_f32 %7, %7\n\tv_add_f32 %4, 1.0, %4\n\tv_add_f32 %5, 1.0, %5\n\tv_add_f32 %6, 1.0, %6\n\tv_add_f32 %7, 1.0, %7\n\tv_rcp_f32 %4, %4\n\tv_rcp_f32 %5, %5\n\tv_rcp_f32 %6, %6\n\tv_rcp_f32 %7, %7\n\tv_fma_f32 %4, -%0, %4, %0\n\tv_fma_f32 %5, -%1, %5, %1\n\tv_fma_f32 %6, -%2, %6, %2\n\tv_fma_f32 %7, -%3, %7, %3\n\tv_cvt_pk_bf16_f32 %8, %4, %5\n\tv_cvt_pk_bf16_f32 %9, %6, %7\n\tv_cvt_pk_bf16_f32 %10, %4, %5\n\tv_cvt_pk_bf16_f32 %11, %6, %7\n\ts_nop 0\n\tv_permlane16_swap_b32 %8, %10\n\tv_permlane16_swap_b32 %9, %11"
      : "=&v"(gm[0]), "=&v"(gm[1]), "=&v"(gm[2]), "=&v"(gm[3]), "=&v"(gz[0]), "=&v"(gz[1]), "=&v"(gz[2]), "=&v"(gz[3]), "=&v"(uv[6][0]), "=&v"(uv[6][1]), "=&v"(uv[7][0]), "=&v"(uv[7][1]) : "v"(acc[6][0][0]), "v"(pm[6][0][0]), "v"(acc[6][0][1]), "v"(pm[6][0][1]), "v"(acc[6][0][2]), "v"(pm[6][0][2]), "v"(acc[6][0][3]), "v"(pm[6][0][3]), "v"(pm[6][1][0]), "v"(pm[6][2][0]), "v"(pm[6][1][1]), "v"(pm[6][2][1]), "v"(pm[6][1][2]), "v"(pm[6][2][2]), "v"(pm[6][1][3]), "v"(pm[6][2][3]), "v"(gc)); SB();
  HS16M(0xffff, 0xffff, vU6, sU, 0, ((u32x4){uv[6][0], uv[6][1], uv[7][0], uv[7][1]}));
  STAMP(10);
  asm volatile("s_add_u32 m0, %0, 34816\n\ts_nop 0\n\tbuffer_load_dwordx4 %1, %2, %3 offen offset:0  lds\n\tbuffer_load_dwordx4 %4, %2, %3 offen offset:1024  lds\n\tbuffer_load_dwordx4 %5, %2, %3 offen offset:2048  lds\n\tbuffer_load_dwordx4 %6, %2, %3 offen offset:3072  lds\n\ts_add_u32 m0, %0, 0\n\ts_nop 0\n\tbuffer_load_dwordx4 %1, %2, %7 offen offset:0  lds\n\tbuffer_load_dwordx4 %4, %2, %7 offen offset:1024  lds\n\tbuffer_load_dwordx4 %5, %2, %7 offen offset:2048  lds\n\tbuffer_load_dwordx4 %6, %2, %7 offen offset:3072  lds\n\ts_waitcnt vmcnt(17)"
      :  : "s"(ldsS), "v"(vW2[0]), "s"(rs2), "s"(sw[3]), "v"(vW2[1]), "v"(vW2[2]), "v"(vW2[3]), "s"(sw[4]) : "memory", "scc"); SB();
  fw2[0][0] = tr8u(tO[0] + 0, 512);
  fw2[0][1] = tr8u(tO[1] + 0, 512);
  fw2[0][2] = tr8u(tO[2] + 0, 512);
  fw2[0][3] = tr8u(tO[3] + 0, 512);
  WAITL(0);
  asm volatile("s_add_u32 m0, %0, 0\n\ts_nop 0\n\tbuffer_load_dwordx4 %1, %2, %3 offen offset:0  lds\n\tbuffer_load_dwordx4 %4, %2, %3 offen offset:1024  lds\n\tbuffer_load_dwordx4 %5, %2, %3 offen offset:2048  lds\n\tbuffer_load_dwordx4 %6, %2, %3 offen offset:3072  lds\n\ts_waitcnt vmcnt(17)"
      :  : "s"(ldsO), "v"(vW2[0]), "s"(rs2), "s"(sw[5]), "v"(vW2[1]), "v"(vW2[2]), "v"(vW2[3]) : "memory", "scc"); SB();
  fw2[1][0] = tr8u(tO[0] + 4096, 512);
  fw2[1][1] = tr8u(tO[1] + 4096, 512);
  fw2[1][2] = tr8u(tO[2] + 4096, 512);
  fw2[1][3] = tr8u(tO[3] + 4096, 512);
  asm volatile("s_waitcnt vmcnt(13)"
      :  :  : "memory"); SB();
  fw2[2][0] = tr8u(tS[0] + 18432, 512);
  fw2[2][1] = tr8u(tS[1] + 18432, 512);
  fw2[2][2] = tr8u(tS[2] + 18432, 512);
  fw2[2][3] = tr8u(tS[3] + 18432, 512);
  WAITV1(12, fq);
  STFU(sFU);
  STAMP(11);
  {
  if (__builtin_expect(__ballot(fq != nonce) != 0ull, 0)) poll1<4>(rsW, vFQ, nonce);
  unsigned fu;
  asm volatile("s_waitcnt vmcnt(9)"
      :  :  : "memory"); SB();
  fw2[3][0] = tr8u(tS[0] + 34816, 512);
  fw2[3][1] = tr8u(tS[1] + 34816, 512);
  fw2[3][2] = tr8u(tS[2] + 34816, 512);
  fw2[3][3] = tr8u(tS[3] + 34816, 512);
  WAITL(14);
  asm volatile("s_add_u32 m0, %0, 4096\n\ts_nop 0\n\tbuffer_load_dwordx4 %1, %2, %3 offen offset:0  lds\n\tbuffer_load_dwordx4 %4, %2, %3 offen offset:1024  lds\n\tbuffer_load_dwordx4 %5, %2, %3 offen offset:2048  lds\n\tbuffer_load_dwordx4 %6, %2, %3 offen offset:3072  lds\n\ts_waitcnt vmcnt(9)"
      :  : "s"(ldsO), "v"(vW2[0]), "s"(rs2), "s"(sw[6]), "v"(vW2[1]), "v"(vW2[2]), "v"(vW2[3]) : "memory", "scc"); SB();
  fw2[4][0] = tr8u(tS[0] + 0, 512);
  fw2[4][1] = tr8u(tS[1] + 0, 512);
  fw2[4][2] = tr8u(tS[2] + 0, 512);
  fw2[4][3] = tr8u(tS[3] + 0, 512);
  WAITL(14);
  asm volatile("s_add_u32 m0, %0, 18432\n\ts_nop 0\n\tbuffer_load_dwordx4 %1, %2, %3 offen offset:0  lds\n\tbuffer_load_dwordx4 %4, %2, %3 offen offset:1024  lds\n\tbuffer_load_dwordx4 %5, %2, %3 offen offset:2048  lds\n\tbuffer_load_dwordx4 %6, %2, %3 offen offset:3072  lds\n\ts_waitcnt vmcnt(9)"
      :  : "s"(ldsS), "v"(vW2[0]), "s"(rs2), "s"(sw[7]), "v"(vW2[1]), "v"(vW2[2]), "v"(vW2[3]) : "memory", "scc"); SB();
  fw2[5][0] = tr8u(tO[0] + 0, 512);
  fw2[5][1] = tr8u(tO[1] + 0, 512);
  fw2[5][2] = tr8u(tO[2] + 0, 512);
  fw2[5][3] = tr8u(tO[3] + 0, 512);
  WAITL(14);
  asm volatile("s_add_u32 m0, %0, 34816\n\ts_nop 0\n\tbuffer_load_dwordx4 %1, %2, %3 offen offset:0  lds\n\tbuffer_load_dwordx4 %4, %2, %3 offen offset:1024  lds\n\tbuffer_load_dwordx4 %5, %2, %3 offen offset:2048  lds\n\tbuffer_load_dwordx4 %6, %2, %3 offen offset:3072  lds\n\ts_waitcnt vmcnt(8)"
      :  : "s"(ldsS), "v"(vW2[0]), "s"(rs2), "s"(sw[8]), "v"(vW2[1]), "v"(vW2[2]), "v"(vW2[3]) : "memory", "scc"); SB();
  fw2[6][0] = tr8u(tO[0] + 4096, 512);
  fw2[6][1] = tr8u(tO[1] + 4096, 512);
  fw2[6][2] = tr8u(tO[2] + 4096, 512);
  fw2[6][3] = tr8u(tO[3] + 4096, 512);
  WAITL(14);
  asm volatile("s_add_u32 m0, %0, 0\n\ts_nop 0\n\tbuffer_load_dwordx4 %1, %2, %3 offen offset:0  lds\n\tbuffer_load_dwordx4 %4, %2, %3 offen offset:1024  lds\n\tbuffer_load_dwordx4 %5, %2, %3 offen offset:2048  lds\n\tbuffer_load_dwordx4 %6, %2, %3 offen offset:3072  lds\n\ts_waitcnt vmcnt(8)"
      :  : "s"(ldsS), "v"(vW2[0]), "s"(rs2), "s"(sw[9]), "v"(vW2[1]), "v"(vW2[2]), "v"(vW2[3]) : "memory", "scc"); SB();
  fw2[7][0] = tr8u(tS[0] + 18432, 512);
  fw2[7][1] = tr8u(tS[1] + 18432, 512);
  fw2[7][2] = tr8u(tS[2] + 18432, 512);
  fw2[7][3] = tr8u(tS[3] + 18432, 512);
  WAITL(14);
  asm volatile("s_add_u32 m0, %0, 0\n\ts_nop 0\n\tbuffer_load_dwordx4 %1, %2, %3 offen offset:0  lds\n\tbuffer_load_dwordx4 %4, %2, %3 offen offset:1024  lds\n\tbuffer_load_dwordx4 %5, %2, %3 offen offset:2048  lds\n\tbuffer_load_dwordx4 %6, %2, %3 offen offset:3072  lds\n\ts_waitcnt vmcnt(8)"
      :  : "s"(ldsO), "v"(vW2[0]), "s"(rs2), "s"(sw[10]), "v"(vW2[1]), "v"(vW2[2]), "v"(vW2[3]) : "memory", "scc"); SB();
  fw2[8][0] = tr8u(tS[0] + 34816, 512);
  fw2[8][1] = tr8u(tS[1] + 34816, 512);
  fw2[8][2] = tr8u(tS[2] + 34816, 512);
  fw2[8][3] = tr8u(tS[3] + 34816, 512);
  WAITL(8);
  asm volatile("s_add_u32 m0, %0, 18432\n\ts_nop 0\n\tbuffer_load_dwordx4 %1, %2, %3 offen offset:0  lds\n\tbuffer_load_dwordx4 %4, %2, %3 offen offset:1024  lds\n\tbuffer_load_dwordx4 %5, %2, %3 offen offset:2048  lds\n\tbuffer_load_dwordx4 %6, %2, %3 offen offset:3072  lds\n\ts_waitcnt vmcnt(8)"
      :  : "s"(ldsS), "v"(vW2[0]), "s"(rs2), "s"(sw[11]), "v"(vW2[1]), "v"(vW2[2]), "v"(vW2[3]) : "memory", "scc"); SB();
  fw2[9][0] = tr8u(tS[0] + 0, 512);
  fw2[9][1] = tr8u(tS[1] + 0, 512);
  fw2[9][2] = tr8u(tS[2] + 0, 512);
  fw2[9][3] = tr8u(tS[3] + 0, 512);
  WAITL(8);
  asm volatile("s_add_u32 m0, %0, 34816\n\ts_nop 0\n\tbuffer_load_dwordx4 %1, %2, %3 offen offset:0  lds\n\tbuffer_load_dwordx4 %4, %2, %3 offen offset:1024  lds\n\tbuffer_load_dwordx4 %5, %2, %3 offen offset:2048  lds\n\tbuffer_load_dwordx4 %6, %2, %3 offen offset:3072  lds\n\ts_waitcnt vmcnt(8)"
      :  : "s"(ldsS), "v"(vW2[0]), "s"(rs2), "s"(sw[12]), "v"(vW2[1]), "v"(vW2[2]), "v"(vW2[3]) : "memory", "scc"); SB();
  fw2[10][0] = tr8u(tO[0] + 0, 512);
  fw2[10][1] = tr8u(tO[1] + 0, 512);
  fw2[10][2] = tr8u(tO[2] + 0, 512);
  fw2[10][3] = tr8u(tO[3] + 0, 512);
  WAITL(8);
  asm volatile("s_add_u32 m0, %1, 0\n\ts_nop 0\n\tbuffer_load_dwordx4 %2, %3, %4 offen offset:0  lds\n\tbuffer_load_dwordx4 %5, %3, %4 offen offset:1024  lds\n\tbuffer_load_dwordx4 %6, %3, %4 offen offset:2048  lds\n\tbuffer_load_dwordx4 %7, %3, %4 offen offset:3072  lds\n\ts_add_u32 m0, %8, 8192\n\ts_add_u32 %0, %9, 8192"
      : "=&s"(sWt) : "s"(ldsS), "v"(vW2[0]), "s"(rs2), "s"(sw[13]), "v"(vW2[1]), "v"(vW2[2]), "v"(vW2[3]), "s"(ldsP), "s"(sWo) : "memory", "scc"); SB();
  DMAW4(vWo, rsW, sWt);
  asm volatile("s_waitcnt vmcnt(12)"
      :  :  : "memory"); SB();
  fw2[11][0] = tr8u(tS[0] + 18432, 512);
  fw2[11][1] = tr8u(tS[1] + 18432, 512);
  fw2[11][2] = tr8u(tS[2] + 18432, 512);
  fw2[11][3] = tr8u(tS[3] + 18432, 512);
  asm volatile("buffer_load_dword %0, %2, %3, 0 offen sc1\n\ts_add_u32 m0, %4, 12288\n\ts_add_u32 %1, %5, 12288"
      : "=&v"(fu), "=&s"(sWt) : "v"(vFU), "s"(rsW), "s"(ldsP), "s"(sWo) : "memory", "scc"); SB();
  DMAW2(vWo, rsW, sWt);
  asm volatile("s_waitcnt vmcnt(11)"
      :  :  : "memory"); SB();
  fw2[12][0] = tr8u(tS[0] + 34816, 512);
  fw2[12][1] = tr8u(tS[1] + 34816, 512);
  fw2[12][2] = tr8u(tS[2] + 34816, 512);
  fw2[12][3] = tr8u(tS[3] + 34816, 512);
  WAITL(14);
  asm volatile("s_add_u32 m0, %1, 0\n\ts_add_u32 %0, %2, 0"
      : "=&s"(sWt) : "s"(ldsO), "s"(sWo) : "memory", "scc"); SB();
  DMAW4(vWo, rsW, sWt);
  u32x4 rsX = srd(x, 2048u * 896u); OPQ(rsX);
  WAITL(0);
  asm volatile("s_add_u32 m0, %2, 34816\n\ts_nop 0\n\tbuffer_load_dwordx4 %3, %4, %5 offen offset:0  lds\n\tbuffer_load_dwordx4 %6, %4, %5 offen offset:1024  lds\n\tglobal_load_ushort %0, %7, %8 offset:0\n\ts_add_u32 m0, %9, 4096\n\ts_add_u32 %1, %10, 4096"
      : "=&v"(braw), "=&s"(sWt) : "s"(ldsS), "v"(vXa), "s"(rsX), "s"(0u), "v"(vXb), "v"(vB2), "s"(b2), "s"(ldsO), "s"(sWo) : "memory", "scc"); SB();
  DMAW4(vWo, rsW, sWt);
  asm volatile("s_waitcnt vmcnt(18)"
      :  :  : "memory"); SB();
  fw2[13][0] = tr8u(tS[0] + 0, 512);
  fw2[13][1] = tr8u(tS[1] + 0, 512);
  fw2[13][2] = tr8u(tS[2] + 0, 512);
  fw2[13][3] = tr8u(tS[3] + 0, 512);
  STAMP(16);
  bf16x8 pu[14], fwo[56];
  f32x4 ac[2][4], as[4], py[4][4];
  float ye[4], ze[4], ge[16], xe[16], bias;
  u32x2 ov[4]; unsigned fo;
  u32x2 xv[4]; unsigned xta[4];
  WAITV1(13, fu);
  if (__builtin_expect(__ballot(fu != nonce) != 0ull, 0)) poll1<4>(rsW, vFU, nonce);
  STAMP(17);
  asm volatile("buffer_load_dwordx4 %0, %10, %11, %12 offen offset:0 sc1\n\tbuffer_load_dwordx4 %1, %10, %11, %13 offen offset:0 sc1\n\tbuffer_load_dwordx4 %2, %10, %11, %14 offen offset:0 sc1\n\tbuffer_load_dwordx4 %3, %10, %11, %15 offen offset:0 sc1\n\tbuffer_load_dwordx4 %4, %10, %11, %16 offen offset:0 sc1\n\tbuffer_load_dwordx4 %5, %10, %11, %17 offen offset:0 sc1\n\tbuffer_load_dwordx4 %6, %10, %11, %18 offen offset:0 sc1\n\tbuffer_load_dwordx4 %7, %10, %11, %19 offen offset:0 sc1\n\tbuffer_load_dwordx4 %8, %10, %11, %20 offen offset:0 sc1\n\tbuffer_load_dwordx4 %9, %10, %11, %21 offen offset:0 sc1"
      : "=&v"(pu[0]), "=&v"(pu[1]), "=&v"(pu[2]), "=&v"(pu[3]), "=&v"(pu[4]), "=&v"(pu[5]), "=&v"(pu[6]), "=&v"(pu[7]), "=&v"(pu[8]), "=&v"(pu[9]) : "v"(vFj[0]), "s"(rsW), "s"(su[0]), "s"(su[1]), "s"(su[2]), "s"(su[3]), "s"(su[4]), "s"(su[5]), "s"(su[6]), "s"(su[7]), "s"(su[8]), "s"(su[9]) : "memory"); SB();
  STAMP(39);
  WAITVB(9);
  SB();
  asm volatile("v_mfma_f32_16x16x32_bf16 %0, %6, %7, 0\n\tbuffer_load_dwordx4 %1, %8, %9, %10 offen offset:0 sc1\n\tv_mfma_f32_16x16x32_bf16 %2, %6, %11, 0\n\tv_mfma_f32_16x16x32_bf16 %3, %6, %12, 0\n\tbuffer_load_dwordx4 %4, %8, %9, %13 offen offset:0 sc1\n\tv_mfma_f32_16x16x32_bf16 %5, %6, %14, 0"
      : "=&v"(ac[0][0]), "=&v"(pu[10]), "=&v"(ac[0][1]), "=&v"(ac[0][2]), "=&v"(pu[11]), "=&v"(ac[0][3]) : "v"(pu[0]), "a"(fw2[0][0]), "v"(vFj[0]), "s"(rsW), "s"(su[10]), "a"(fw2[0][1]), "a"(fw2[0][2]), "s"(su[11]), "a"(fw2[0][3]) : "memory"); SB();
  WAITVB(10);
  SB();
  asm volatile("v_mfma_f32_16x16x32_bf16 %0, %6, %7, %0\n\tbuffer_load_dwordx4 %1, %8, %9, %10 offen offset:0 sc1\n\tv_mfma_f32_16x16x32_bf16 %2, %6, %11, %2\n\tv_mfma_f32_16x16x32_bf16 %3, %6, %12, %3\n\tbuffer_load_dwordx4 %4, %8, %9, %13 offen offset:0 sc1\n\tv_mfma_f32_16x16x32_bf16 %5, %6, %14, %5"
      : "+v"(ac[0][0]), "=&v"(pu[12]), "+v"(ac[0][1]), "+v"(ac[0][2]), "=&v"(pu[13]), "+v"(ac[0][3]) : "v"(pu[1]), "a"(fw2[1][0]), "v"(vFj[0]), "s"(rsW), "s"(su[12]), "a"(fw2[1][1]), "a"(fw2[1][2]), "s"(su[13]), "a"(fw2[1][3]) : "memory"); SB();
  WAITVB(11);
  SB();
  asm volatile("v_mfma_f32_16x16x32_bf16 %0, %3, %4, %0\n\tbuffer_load_dwordx4 %1, %5, %6, %7 offen offset:0 sc1\n\tv_mfma_f32_16x16x32_bf16 %2, %3, %8, %2"
      : "+v"(ac[0][0]), "=&v"(pu[0]), "+v"(ac[0][1]) : "v"(pu[2]), "a"(fw2[2][0]), "v"(vFj[1]), "s"(rsW), "s"(su[0]), "a"(fw2[2][1]) : "memory"); SB();
  WAITVB(19);
  asm volatile("v_lshlrev_b32 %0, 16, %5\n\tv_mfma_f32_16x16x32_bf16 %1, %6, %7, %1\n\tbuffer_load_dwordx4 %2, %8, %9, %10 offen offset:0 sc1\n\tv_mfma_f32_16x16x32_bf16 %3, %6, %11, %3\n\tv_xor_b32 %4, 0, %12"
      : "=&v"(bias), "+v"(ac[0][2]), "=&v"(pu[1]), "+v"(ac[0][3]), "=&v"(xta[0]) : "v"(braw), "v"(pu[2]), "a"(fw2[2][2]), "v"(vFj[1]), "s"(rsW), "s"(su[1]), "a"(fw2[2][3]), "v"(xs) : "memory"); SB();
  WAITVB(12);
  SB();
  asm volatile("v_mfma_f32_16x16x32_bf16 %0, %10, %11, %0\n\tbuffer_load_dwordx4 %1, %12, %13, %14 offen offset:0 sc1\n\tv_mfma_f32_16x16x32_bf16 %2, %10, %15, %2\n\tv_xor_b32 %3, 32, %16\n\tv_xor_b32 %4, 64, %17\n\tv_mfma_f32_16x16x32_bf16 %5, %10, %18, %5\n\tds_read_b64 %6, %19 offset:0\n\tds_read_b64 %7, %3 offset:0\n\tv_mfma_f32_16x16x32_bf16 %8, %10, %20, %8\n\tds_read_b64 %9, %4 offset:0"
      : "+v"(ac[0][0]), "=&v"(pu[2]), "+v"(ac[0][1]), "=&v"(xta[1]), "=&v"(xta[2]), "+v"(ac[0][2]), "=&v"(xv[0]), "=&v"(xv[1]), "+v"(ac[0][3]), "=&v"(xv[2]) : "v"(pu[3]), "a"(fw2[3][0]), "v"(vFj[1]), "s"(rsW), "s"(su[2]), "a"(fw2[3][1]), "v"(xs), "v"(xt), "a"(fw2[3][2]), "v"(xta[0]), "a"(fw2[3][3]) : "memory"); SB();
  WAITVB(12);
  SB();
  asm volatile("v_mfma_f32_16x16x32_bf16 %0, %5, %6, %0\n\tbuffer_load_dwordx4 %1, %7, %8, %9 offen offset:0 sc1\n\tv_mfma_f32_16x16x32_bf16 %2, %5, %10, %2\n\tv_mfma_f32_16x16x32_bf16 %3, %5, %11, %3\n\tv_mfma_f32_16x16x32_bf16 %4, %5, %12, %4"
      : "+v"(ac[0][0]), "=&v"(pu[3]), "+v"(ac[0][1]), "+v"(ac[0][2]), "+v"(ac[0][3]) : "v"(pu[4]), "a"(fw2[4][0]), "v"(vFj[1]), "s"(rsW), "s"(su[3]), "a"(fw2[4][1]), "a"(fw2[4][2]), "a"(fw2[4][3]) : "memory"); SB();
  WAITVB(12);
  SB();
  asm volatile("v_mfma_f32_16x16x32_bf16 %0, %3, %4, %0\n\tbuffer_load_dwordx4 %1, %5, %6, %7 offen offset:0 sc1\n\tv_mfma_f32_16x16x32_bf16 %2, %3, %8, %2"
      : "+v"(ac[0][0]), "=&v"(pu[4]), "+v"(ac[0][1]) : "v"(pu[5]), "a"(fw2[5][0]), "v"(vFj[1]), "s"(rsW), "s"(su[4]), "a"(fw2[5][1]) : "memory"); SB();
  WAITL(0);
  asm volatile("v_lshlrev_b32 %0, 16, %6\n\tv_mfma_f32_16x16x32_bf16 %1, %7, %8, %1\n\tv_and_b32 %2, 0xffff0000, %6\n\tv_lshlrev_b32 %3, 16, %9\n\tv_mfma_f32_16x16x32_bf16 %4, %7, %10, %4\n\tv_and_b32 %5, 0xffff0000, %9"
      : "=&v"(xe[0]), "+v"(ac[0][2]), "=&v"(xe[1]), "=&v"(xe[2]), "+v"(ac[0][3]), "=&v"(xe[3]) : "v"(xv[0][0]), "v"(pu[5]), "a"(fw2[5][2]), "v"(xv[0][1]), "a"(fw2[5][3])); SB();
  WAITVB(12);
  SB();
  asm volatile("v_mfma_f32_16x16x32_bf16 %0, %10, %11, %0\n\tbuffer_load_dwordx4 %1, %12, %13, %14 offen offset:0 sc1\n\tv_mfma_f32_16x16x32_bf16 %2, %10, %15, %2\n\tv_lshlrev_b32 %3, 16, %16\n\tv_and_b32 %4, 0xffff0000, %16\n\tv_mfma_f32_16x16x32_bf16 %5, %10, %17, %5\n\tv_lshlrev_b32 %6, 16, %18\n\tv_and_b32 %7, 0xffff0000, %18\n\tv_mfma_f32_16x16x32_bf16 %8, %10, %19, %8\n\tv_lshlrev_b32 %9, 16, %20"
      : "+v"(ac[0][0]), "=&v"(pu[5]), "+v"(ac[0][1]), "=&v"(xe[4]), "=&v"(xe[5]), "+v"(ac[0][2]), "=&v"(xe[6]), "=&v"(xe[7]), "+v"(ac[0][3]), "=&v"(xe[8]) : "v"(pu[6]), "a"(fw2[6][0]), "v"(vFj[1]), "s"(rsW), "s"(su[5]), "a"(fw2[6][1]), "v"(xv[1][0]), "a"(fw2[6][2]), "v"(xv[1][1]), "a"(fw2[6][3]), "v"(xv[2][0]) : "memory"); SB();
  WAITVB(12);
  SB();
  asm volatile("v_mfma_f32_16x16x32_bf16 %0, %8, %9, %0\n\tbuffer_load_dwordx4 %1, %10, %11, %12 offen offset:0 sc1\n\tv_mfma_f32_16x16x32_bf16 %2, %8, %13, %2\n\tv_and_b32 %3, 0xffff0000, %14\n\tv_lshlrev_b32 %4, 16, %15\n\tv_mfma_f32_16x16x32_bf16 %5, %8, %16, %5\n\tv_and_b32 %6, 0xffff0000, %15\n\tv_mfma_f32_16x16x32_bf16 %7, %8, %17, %7"
      : "+v"(ac[0][0]), "=&v"(pu[6]), "+v"(ac[0][1]), "=&v"(xe[9]), "=&v"(xe[10]), "+v"(ac[0][2]), "=&v"(xe[11]), "+v"(ac[0][3]) : "v"(pu[7]), "a"(fw2[7][0]), "v"(vFj[1]), "s"(rsW), "s"(su[6]), "a"(fw2[7][1]), "v"(xv[2][0]), "v"(xv[2][1]), "a"(fw2[7][2]), "a"(fw2[7][3]) : "memory"); SB();
  WAITVB(12);
  SB();
  asm volatile("v_mfma_f32_16x16x32_bf16 %0, %5, %6, %0\n\tbuffer_load_dwordx4 %1, %7, %8, %9 offen offset:0 sc1\n\tv_mfma_f32_16x16x32_bf16 %2, %5, %10, %2\n\tv_mfma_f32_16x16x32_bf16 %3, %5, %11, %3\n\tv_mfma_f32_16x16x32_bf16 %4, %5, %12, %4"
      : "+v"(ac[0][0]), "=&v"(pu[7]), "+v"(ac[0][1]), "+v"(ac[0][2]), "+v"(ac[0][3]) : "v"(pu[8]), "a"(fw2[8][0]), "v"(vFj[1]), "s"(rsW), "s"(su[7]), "a"(fw2[8][1]), "a"(fw2[8][2]), "a"(fw2[8][3]) : "memory"); SB();
  WAITVB(12);
  SB();
  asm volatile("v_mfma_f32_16x16x32_bf16 %0, %5, %6, %0\n\tbuffer_load_dwordx4 %1, %7, %8, %9 offen offset:0 sc1\n\tv_mfma_f32_16x16x32_bf16 %2, %5, %10, %2\n\tv_mfma_f32_16x16x32_bf16 %3, %5, %11, %3\n\tv_mfma_f32_16x16x32_bf16 %4, %5, %12, %4"
      : "+v"(ac[0][0]), "=&v"(pu[8]), "+v"(ac[0][1]), "+v"(ac[0][2]), "+v"(ac[0][3]) : "v"(pu[9]), "v"(fw2[9][0]), "v"(vFj[1]), "s"(rsW), "s"(su[8]), "v"(fw2[9][1]), "v"(fw2[9][2]), "v"(fw2[9][3]) : "memory"); SB();
  WAITVB(12);
  SB();
  asm volatile("v_mfma_f32_16x16x32_bf16 %0, %5, %6, %0\n\tbuffer_load_dwordx4 %1, %7, %8, %9 offen offset:0 sc1\n\tv_mfma_f32_16x16x32_bf16 %2, %5, %10, %2\n\tv_mfma_f32_16x16x32_bf16 %3, %5, %11, %3\n\tv_mfma_f32_16x16x32_bf16 %4, %5, %12, %4"
      : "+v"(ac[0][0]), "=&v"(pu[9]), "+v"(ac[0][1]), "+v"(ac[0][2]), "+v"(ac[0][3]) : "v"(pu[10]), "v"(fw2[10][0]), "v"(vFj[1]), "s"(rsW), "s"(su[9]), "v"(fw2[10][1]), "v"(fw2[10][2]), "v"(fw2[10][3]) : "memory"); SB();
  WAITVB(12);
  SB();
  asm volatile("v_mfma_f32_16x16x32_bf16 %0, %5, %6, %0\n\tbuffer_load_dwordx4 %1, %7, %8, %9 offen offset:0 sc1\n\tv_mfma_f32_16x16x32_bf16 %2, %5, %10, %2\n\tv_mfma_f32_16x16x32_bf16 %3, %5, %11, %3\n\tv_mfma_f32_16x16x32_bf16 %4, %5, %12, %4"
      : "+v"(ac[0][0]), "=&v"(pu[10]), "+v"(ac[0][1]), "+v"(ac[0][2]), "+v"(ac[0][3]) : "v"(pu[11]), "v"(fw2[11][0]), "v"(vFj[1]), "s"(rsW), "s"(su[10]), "v"(fw2[11][1]), "v"(fw2[11][2]), "v"(fw2[11][3]) : "memory"); SB();
  WAITVB(12);
  SB();
  asm volatile("v_mfma_f32_16x16x32_bf16 %0, %4, %5, %0\n\tbuffer_load_dwordx4 %1, %6, %7, %8 offen offset:0 sc1\n\tv_mfma_f32_16x16x32_bf16 %2, %4, %9, %2\n\tv_mfma_f32_16x16x32_bf16 %3, %4, %10, %3"
      : "+v"(ac[0][0]), "=&v"(pu[11]), "+v"(ac[0][1]), "+v"(ac[0][2]) : "v"(pu[12]), "v"(fw2[12][0]), "v"(vFj[1]), "s"(rsW), "s"(su[11]), "v"(fw2[12][1]), "v"(fw2[12][2]) : "memory"); SB();
  u32x4 rsO_ = srd(out, 2048u * 896u); OPQ(rsO_);
  SB();
  asm volatile("v_mfma_f32_16x16x32_bf16 %0, %1, %2, %0"
      : "+v"(ac[0][3]) : "v"(pu[12]), "v"(fw2[12][3])); SB();
  WAITVB(12);
  SB();
  asm volatile("v_mfma_f32_16x16x32_bf16 %0, %5, %6, %0\n\tbuffer_load_dwordx4 %1, %7, %8, %9 offen offset:0 sc1\n\tv_mfma_f32_16x16x32_bf16 %2, %5, %10, %2\n\tv_mfma_f32_16x16x32_bf16 %3, %5, %11, %3\n\tv_mfma_f32_16x16x32_bf16 %4, %5, %12, %4"
      : "+v"(ac[0][0]), "=&v"(pu[12]), "+v"(ac[0][1]), "+v"(ac[0][2]), "+v"(ac[0][3]) : "v"(pu[13]), "v"(fw2[13][0]), "v"(vFj[1]), "s"(rsW), "s"(su[12]), "v"(fw2[13][1]), "v"(fw2[13][2]), "v"(fw2[13][3]) : "memory"); SB();
  STAMP(40);
  WAITVB(12);
  SB();
  asm volatile("v_mfma_f32_16x16x32_bf16 %0, %6, %7, 0\n\tbuffer_load_dwordx4 %1, %8, %9, %10 offen offset:0 sc1\n\tv_mfma_f32_16x16x32_bf16 %2, %6, %11, 0\n\tbuffer_load_dword %3, %12, %9, 0 offen sc1\n\tv_mfma_f32_16x16x32_bf16 %4, %6, %13, 0\n\tv_mfma_f32_16x16x32_bf16 %5, %6, %14, 0"
      : "=&v"(ac[1][0]), "=&v"(pu[13]), "=&v"(ac[1][1]), "=&v"(fo), "=&v"(ac[1][2]), "=&v"(ac[1][3]) : "v"(pu[0]), "a"(fw2[0][0]), "v"(vFj[1]), "s"(rsW), "s"(su[13]), "a"(fw2[0][1]), "v"(vFO), "a"(fw2[0][2]), "a"(fw2[0][3]) : "memory"); SB();
  WAITVB(13);
  SB();
  asm volatile("v_mfma_f32_16x16x32_bf16 %0, %5, %6, %0\n\tbuffer_load_dwordx4 %1, %7, %8, %9 offen offset:0 sc1\n\tv_mfma_f32_16x16x32_bf16 %2, %5, %10, %2\n\tds_write_b128 %11, %12 offset:0\n\tv_mfma_f32_16x16x32_bf16 %3, %5, %13, %3\n\tv_mfma_f32_16x16x32_bf16 %4, %5, %14, %4"
      : "+v"(ac[1][0]), "=&v"(pu[0]), "+v"(ac[1][1]), "+v"(ac[1][2]), "+v"(ac[1][3]) : "v"(pu[1]), "a"(fw2[1][0]), "v"(vFj[2]), "s"(rsW), "s"(su[0]), "a"(fw2[1][1]), "v"(rwd), "v"(ac[0][0]), "a"(fw2[1][2]), "a"(fw2[1][3]) : "memory"); SB();
  WAITVB(13);
  SB();
  asm volatile("v_mfma_f32_16x16x32_bf16 %0, %5, %6, %0\n\tbuffer_load_dwordx4 %1, %7, %8, %9 offen offset:0 sc1\n\tv_mfma_f32_16x16x32_bf16 %2, %5, %10, %2\n\tds_write_b128 %11, %12 offset:1024\n\tv_mfma_f32_16x16x32_bf16 %3, %5, %13, %3\n\tv_mfma_f32_16x16x32_bf16 %4, %5, %14, %4"
      : "+v"(ac[1][0]), "=&v"(pu[1]), "+v"(ac[1][1]), "+v"(ac[1][2]), "+v"(ac[1][3]) : "v"(pu[2]), "a"(fw2[2][0]), "v"(vFj[2]), "s"(rsW), "s"(su[1]), "a"(fw2[2][1]), "v"(rwd), "v"(ac[0][1]), "a"(fw2[2][2]), "a"(fw2[2][3]) : "memory"); SB();
  WAITVB(13);
  SB();
  asm volatile("v_mfma_f32_16x16x32_bf16 %0, %5, %6, %0\n\tbuffer_load_dwordx4 %1, %7, %8, %9 offen offset:0 sc1\n\tv_mfma_f32_16x16x32_bf16 %2, %5, %10, %2\n\tds_write_b128 %11, %12 offset:2048\n\tv_mfma_f32_16x16x32_bf16 %3, %5, %13, %3\n\tv_mfma_f32_16x16x32_bf16 %4, %5, %14, %4"
      : "+v"(ac[1][0]), "=&v"(pu[2]), "+v"(ac[1][1]), "+v"(ac[1][2]), "+v"(ac[1][3]) : "v"(pu[3]), "a"(fw2[3][0]), "v"(vFj[2]), "s"(rsW), "s"(su[2]), "a"(fw2[3][1]), "v"(rwd), "v"(ac[0][2]), "a"(fw2[3][2]), "a"(fw2[3][3]) : "memory"); SB();
  WAITVB(13);
  SB();
  asm volatile("v_mfma_f32_16x16x32_bf16 %0, %5, %6, %0\n\tbuffer_load_dwordx4 %1, %7, %8, %9 offen offset:0 sc1\n\tv_mfma_f32_16x16x32_bf16 %2, %5, %10, %2\n\tds_write_b128 %11, %12 offset:3072\n\tv_mfma_f32_16x16x32_bf16 %3, %5, %13, %3\n\tv_mfma_f32_16x16x32_bf16 %4, %5, %14, %4"
      : "+v"(ac[1][0]), "=&v"(pu[3]), "+v"(ac[1][1]), "+v"(ac[1][2]), "+v"(ac[1][3]) : "v"(pu[4]), "a"(fw2[4][0]), "v"(vFj[2]), "s"(rsW), "s"(su[3]), "a"(fw2[4][1]), "v"(rwd), "v"(ac[0][3]), "a"(fw2[4][2]), "a"(fw2[4][3]) : "memory"); SB();
  WAITVB(13);
  SB();
  asm volatile("v_mfma_f32_16x16x32_bf16 %0, %3, %4, %0\n\tbuffer_load_dwordx4 %1, %5, %6, %7 offen offset:0 sc1\n\tv_mfma_f32_16x16x32_bf16 %2, %3, %8, %2\n\ts_waitcnt vmcnt(34)"
      : "+v"(ac[1][0]), "=&v"(pu[4]), "+v"(ac[1][1]) : "v"(pu[5]), "a"(fw2[5][0]), "v"(vFj[2]), "s"(rsW), "s"(su[4]), "a"(fw2[5][1]) : "memory"); SB();
  WAITL(0);
  BAR0();
  SB();
  asm volatile("v_mfma_f32_16x16x32_bf16 %0, %5, %6, %0\n\tds_read_b128 %1, %7 offset:0\n\tds_read_b128 %2, %7 offset:4096\n\tv_mfma_f32_16x16x32_bf16 %3, %5, %8, %3\n\tds_read_b128 %4, %7 offset:8192"
      : "+v"(ac[1][2]), "=&v"(py[0][0]), "=&v"(py[0][1]), "+v"(ac[1][3]), "=&v"(py[0][2]) : "v"(pu[5]), "a"(fw2[5][2]), "v"(rr), "a"(fw2[5][3]) : "memory"); SB();
  WAITVB(13);
  SB();
  asm volatile("v_mfma_f32_16x16x32_bf16 %0, %8, %9, %0\n\tbuffer_load_dwordx4 %1, %10, %11, %12 offen offset:0 sc1\n\tv_mfma_f32_16x16x32_bf16 %2, %8, %13, %2\n\tds_read_b128 %3, %14 offset:12288\n\tds_read_b128 %4, %15 offset:0\n\tv_mfma_f32_16x16x32_bf16 %5, %8, %16, %5\n\tv_mfma_f32_16x16x32_bf16 %6, %8, %17, %6\n\tds_read_b128 %7, %15 offset:8192"
      : "+v"(ac[1][0]), "=&v"(pu[5]), "+v"(ac[1][1]), "=&v"(py[0][3]), "=&a"(fwo[0]), "+v"(ac[1][2]), "+v"(ac[1][3]), "=&a"(fwo[1]) : "v"(pu[6]), "a"(fw2[6][0]), "v"(vFj[2]), "s"(rsW), "s"(su[5]), "a"(fw2[6][1]), "v"(rr), "v"(ro), "a"(fw2[6][2]), "a"(fw2[6][3]) : "memory"); SB();
  WAITVB(13);
  SB();
  asm volatile("v_mfma_f32_16x16x32_bf16 %0, %5, %6, %0\n\tbuffer_load_dwordx4 %1, %7, %8, %9 offen offset:0 sc1\n\tv_mfma_f32_16x16x32_bf16 %2, %5, %10, %2\n\tds_read_b128 %3, %11 offset:16384\n\tv_mfma_f32_16x16x32_bf16 %4, %5, %12, %4"
      : "+v"(ac[1][0]), "=&v"(pu[6]), "+v"(ac[1][1]), "=&a"(fwo[2]), "+v"(ac[1][2]) : "v"(pu[7]), "a"(fw2[7][0]), "v"(vFj[2]), "s"(rsW), "s"(su[6]), "a"(fw2[7][1]), "v"(ro), "a"(fw2[7][2]) : "memory"); SB();
  WAITL(3);
  asm volatile("v_add_f32 %0, %3, %4\n\tv_mfma_f32_16x16x32_bf16 %1, %5, %6, %1\n\tv_add_f32 %2, %7, %4"
      : "=&v"(ye[0]), "+v"(ac[1][3]), "=&v"(ye[1]) : "v"(py[0][0][0]), "v"(bias), "v"(pu[7]), "a"(fw2[7][3]), "v"(py[0][0][1])); SB();
  WAITVB(13);
  SB();
  asm volatile("v_mfma_f32_16x16x32_bf16 %0, %9, %10, %0\n\tbuffer_load_dwordx4 %1, %11, %12, %13 offen offset:0 sc1\n\tv_mfma_f32_16x16x32_bf16 %2, %9, %14, %2\n\tv_add_f32 %3, %15, %16\n\tv_add_f32 %4, %17, %16\n\tv_mfma_f32_16x16x32_bf16 %5, %9, %18, %5\n\tv_add_f32 %6, %19, %20\n\tv_add_f32 %7, %21, %22\n\tv_mfma_f32_16x16x32_bf16 %8, %9, %23, %8"
      : "+v"(ac[1][0]), "=&v"(pu[7]), "+v"(ac[1][1]), "=&v"(ye[2]), "=&v"(ye[3]), "+v"(ac[1][2]), "=&v"(ze[0]), "=&v"(ze[1]), "+v"(ac[1][3]) : "v"(pu[8]), "a"(fw2[8][0]), "v"(vFj[2]), "s"(rsW), "s"(su[7]), "a"(fw2[8][1]), "v"(py[0][0][2]), "v"(bias), "v"(py[0][0][3]), "a"(fw2[8][2]), "v"(py[0][1][0]), "v"(py[0][2][0]), "v"(py[0][1][1]), "v"(py[0][2][1]), "a"(fw2[8][3]) : "memory"); SB();
  asm volatile("v_add_f32 %0, %1, %2"
      : "=&v"(ze[2]) : "v"(py[0][1][2]), "v"(py[0][2][2])); SB();
  WAITVB(13);
  SB();
  asm volatile("v_mfma_f32_16x16x32_bf16 %0, %9, %10, %0\n\tbuffer_load_dwordx4 %1, %11, %12, %13 offen offset:0 sc1\n\tv_mfma_f32_16x16x32_bf16 %2, %9, %14, %2\n\tv_add_f32 %3, %15, %16\n\tv_add_f32 %4, %4, %17\n\tv_mfma_f32_16x16x32_bf16 %5, %9, %18, %5\n\tv_add_f32 %6, %6, %19\n\tv_add_f32 %7, %7, %20\n\tv_mfma_f32_16x16x32_bf16 %8, %9, %21, %8"
      : "+v"(ac[1][0]), "=&v"(pu[8]), "+v"(ac[1][1]), "=&v"(ze[3]), "+v"(ye[0]), "+v"(ac[1][2]), "+v"(ye[1]), "+v"(ye[2]), "+v"(ac[1][3]) : "v"(pu[9]), "v"(fw2[9][0]), "v"(vFj[2]), "s"(rsW), "s"(su[8]), "v"(fw2[9][1]), "v"(py[0][1][3]), "v"(py[0][2][3]), "v"(py[0][3][0]), "v"(fw2[9][2]), "v"(py[0][3][1]), "v"(py[0][3][2]), "v"(fw2[9][3]) : "memory"); SB();
  asm volatile("v_add_f32 %0, %0, %1"
      : "+v"(ye[3]) : "v"(py[0][3][3])); SB();
  WAITVB(13);
  SB();
  asm volatile("v_mfma_f32_16x16x32_bf16 %0, %10, %11, %0\n\tbuffer_load_dwordx4 %1, %12, %13, %14 offen offset:0 sc1\n\tv_mfma_f32_16x16x32_bf16 %2, %10, %15, %2\n\tv_add_f32 %3, %3, %4\n\tv_add_f32 %5, %5, %16\n\tv_mfma_f32_16x16x32_bf16 %6, %10, %17, %6\n\tv_add_f32 %7, %7, %18\n\tv_add_f32 %8, %8, %19\n\tv_mfma_f32_16x16x32_bf16 %9, %10, %20, %9\n\tv_mul_f32 %4, 0xbfb8aa3b, %3"
      : "+v"(ac[1][0]), "=&v"(pu[9]), "+v"(ac[1][1]), "+v"(ye[0]), "+v"(ze[0]), "+v"(ye[1]), "+v"(ac[1][2]), "+v"(ye[2]), "+v"(ye[3]), "+v"(ac[1][3]) : "v"(pu[10]), "v"(fw2[10][0]), "v"(vFj[2]), "s"(rsW), "s"(su[9]), "v"(fw2[10][1]), "v"(ze[1]), "v"(fw2[10][2]), "v"(ze[2]), "v"(ze[3]), "v"(fw2[10][3]) : "memory"); SB();
  WAITVB(13);
  SB();
  asm volatile("v_mfma_f32_16x16x32_bf16 %0, %9, %10, %0\n\tbuffer_load_dwordx4 %1, %11, %12, %13 offen offset:0 sc1\n\tv_mfma_f32_16x16x32_bf16 %2, %9, %14, %2\n\tv_mul_f32 %3, 0xbfb8aa3b, %15\n\tv_mul_f32 %4, 0xbfb8aa3b, %16\n\tv_mfma_f32_16x16x32_bf16 %5, %9, %17, %5\n\tds_read_b128 %6, %18 offset:1024\n\tv_mul_f32 %7, 0xbfb8aa3b, %19\n\tv_mfma_f32_16x16x32_bf16 %8, %9, %20, %8"
      : "+v"(ac[1][0]), "=&v"(pu[10]), "+v"(ac[1][1]), "=&v"(ze[1]), "=&v"(ze[2]), "+v"(ac[1][2]), "=&a"(fwo[3]), "=&v"(ze[3]), "+v"(ac[1][3]) : "v"(pu[11]), "v"(fw2[11][0]), "v"(vFj[2]), "s"(rsW), "s"(su[10]), "v"(fw2[11][1]), "v"(ye[1]), "v"(ye[2]), "v"(fw2[11][2]), "v"(ro), "v"(ye[3]), "v"(fw2[11][3]) : "memory"); SB();
  WAITVB(13);
  SB();
  asm volatile("v_mfma_f32_16x16x32_bf16 %0, %8, %9, %0\n\tbuffer_load_dwordx4 %1, %10, %11, %12 offen offset:0 sc1\n\tv_mfma_f32_16x16x32_bf16 %2, %8, %13, %2\n\tv_exp_f32 %3, %3\n\tv_mfma_f32_16x16x32_bf16 %4, %8, %14, %4\n\tv_exp_f32 %5, %5\n\tv_mfma_f32_16x16x32_bf16 %6, %8, %15, %6\n\tds_read_b128 %7, %16 offset:9216"
      : "+v"(ac[1][0]), "=&v"(pu[11]), "+v"(ac[1][1]), "+v"(ze[0]), "+v"(ac[1][2]), "+v"(ze[1]), "+v"(ac[1][3]), "=&a"(fwo[4]) : "v"(pu[12]), "v"(fw2[12][0]), "v"(vFj[2]), "s"(rsW), "s"(su[11]), "v"(fw2[12][1]), "v"(fw2[12][2]), "v"(fw2[12][3]), "v"(ro) : "memory"); SB();
  WAITVB(13);
  SB();
  asm volatile("v_mfma_f32_16x16x32_bf16 %0, %9, %10, %0\n\tbuffer_load_dwordx4 %1, %11, %12, %13 offen offset:0 sc1\n\tv_mfma_f32_16x16x32_bf16 %2, %9, %14, %2\n\tv_exp_f32 %3, %3\n\tv_mfma_f32_16x16x32_bf16 %4, %9, %15, %4\n\tv_exp_f32 %5, %5\n\tv_mfma_f32_16x16x32_bf16 %6, %9, %16, %6\n\tv_add_f32 %7, 1.0, %7\n\tv_add_f32 %8, 1.0, %8"
      : "+v"(ac[1][0]), "=&v"(pu[12]), "+v"(ac[1][1]), "+v"(ze[2]), "+v"(ac[1][2]), "+v"(ze[3]), "+v"(ac[1][3]), "+v"(ze[0]), "+v"(ze[1]) : "v"(pu[13]), "v"(fw2[13][0]), "v"(vFj[2]), "s"(rsW), "s"(su[12]), "v"(fw2[13][1]), "v"(fw2[13][2]), "v"(fw2[13][3]) : "memory"); SB();
  STAMP(41);
  WAITVB(12);
  SB();
  asm volatile("v_mfma_f32_16x16x32_bf16 %0, %9, %10, 0\n\tbuffer_load_dwordx4 %1, %11, %12, %13 offen offset:0 sc1\n\tv_mfma_f32_16x16x32_bf16 %2, %9, %14, 0\n\tv_add_f32 %3, 1.0, %3\n\tv_add_f32 %4, 1.0, %4\n\tv_mfma_f32_16x16x32_bf16 %5, %9, %15, 0\n\tv_rcp_f32 %6, %6\n\tv_mfma_f32_16x16x32_bf16 %7, %9, %16, 0\n\tds_read_b128 %8, %17 offset:17408"
      : "=&v"(ac[0][0]), "=&v"(pu[13]), "=&v"(ac[0][1]), "+v"(ze[2]), "+v"(ze[3]), "=&v"(ac[0][2]), "+v"(ze[0]), "=&v"(ac[0][3]), "=&a"(fwo[5]) : "v"(pu[0]), "a"(fw2[0][0]), "v"(vFj[2]), "s"(rsW), "s"(su[13]), "a"(fw2[0][1]), "a"(fw2[0][2]), "a"(fw2[0][3]), "v"(ro) : "memory"); SB();
  WAITVB(12);
  SB();
  asm volatile("v_mfma_f32_16x16x32_bf16 %0, %1, %2, %0"
      : "+v"(ac[0][0]) : "v"(pu[1]), "a"(fw2[1][0])); SB();
  WAITV1(14, fo);
  if (__builtin_expect(__ballot(fo != nonce) != 0ull, 0)) poll1<4>(rsW, vFO, nonce);
  STAMP(18);
  asm volatile("buffer_load_dwordx4 %0, %6, %7, %8 offen offset:0 sc1\n\tv_mfma_f32_16x16x32_bf16 %1, %9, %10, %1\n\tds_write_b128 %11, %12 offset:18432\n\tv_mfma_f32_16x16x32_bf16 %2, %9, %13, %2\n\tv_rcp_f32 %3, %3\n\tv_mfma_f32_16x16x32_bf16 %4, %9, %14, %4\n\tds_read_b128 %5, %15 offset:2048"
      : "=&v"(pu[0]), "+v"(ac[0][1]), "+v"(ac[0][2]), "+v"(ze[1]), "+v"(ac[0][3]), "=&a"(fwo[6]) : "v"(vF), "s"(rsW), "s"(so0), "v"(pu[1]), "a"(fw2[1][1]), "v"(rwd), "v"(ac[1][0]), "a"(fw2[1][2]), "a"(fw2[1][3]), "v"(ro) : "memory"); SB();
  WAITVB(12);
  SB();
  asm volatile("v_mfma_f32_16x16x32_bf16 %0, %7, %8, %0\n\tbuffer_load_dwordx4 %1, %9, %10, %11 offen offset:128 sc1\n\tv_mfma_f32_16x16x32_bf16 %2, %7, %12, %2\n\tds_write_b128 %13, %14 offset:19456\n\tv_mfma_f32_16x16x32_bf16 %3, %7, %15, %3\n\tv_rcp_f32 %4, %4\n\tv_mfma_f32_16x16x32_bf16 %5, %7, %16, %5\n\tds_read_b128 %6, %17 offset:10240"
      : "+v"(ac[0][0]), "=&v"(pu[1]), "+v"(ac[0][1]), "+v"(ac[0][2]), "+v"(ze[2]), "+v"(ac[0][3]), "=&a"(fwo[7]) : "v"(pu[2]), "a"(fw2[2][0]), "v"(vF), "s"(rsW), "s"(so0), "a"(fw2[2][1]), "v"(rwd), "v"(ac[1][1]), "a"(fw2[2][2]), "a"(fw2[2][3]), "v"(ro) : "memory"); SB();
  WAITVB(12);
  SB();
  asm volatile("v_mfma_f32_16x16x32_bf16 %0, %7, %8, %0\n\tbuffer_load_dwordx4 %1, %9, %10, %11 offen offset:256 sc1\n\tv_mfma_f32_16x16x32_bf16 %2, %7, %12, %2\n\tds_write_b128 %13, %14 offset:20480\n\tv_mfma_f32_16x16x32_bf16 %3, %7, %15, %3\n\tv_rcp_f32 %4, %4\n\tv_mfma_f32_16x16x32_bf16 %5, %7, %16, %5\n\tv_mul_f32 %6, %17, %18"
      : "+v"(ac[0][0]), "=&v"(pu[2]), "+v"(ac[0][1]), "+v"(ac[0][2]), "+v"(ze[3]), "+v"(ac[0][3]), "=&v"(ge[0]) : "v"(pu[3]), "a"(fw2[3][0]), "v"(vF), "s"(rsW), "s"(so0), "a"(fw2[3][1]), "v"(rwd), "v"(ac[1][2]), "a"(fw2[3][2]), "a"(fw2[3][3]), "v"(ye[0]), "v"(ze[0]) : "memory"); SB();
  WAITVB(12);
  SB();
  asm volatile("v_mfma_f32_16x16x32_bf16 %0, %8, %9, %0\n\tbuffer_load_dwordx4 %1, %10, %11, %12 offen offset:384 sc1\n\tv_mfma_f32_16x16x32_bf16 %2, %8, %13, %2\n\tds_write_b128 %14, %15 offset:21504\n\tv_mfma_f32_16x16x32_bf16 %3, %8, %16, %3\n\tv_mul_f32 %4, %17, %18\n\tv_mul_f32 %5, %19, %20\n\tv_mfma_f32_16x16x32_bf16 %6, %8, %21, %6\n\tv_mul_f32 %7, %22, %23"
      : "+v"(ac[0][0]), "=&v"(pu[3]), "+v"(ac[0][1]), "+v"(ac[0][2]), "=&v"(ge[1]), "=&v"(ge[2]), "+v"(ac[0][3]), "=&v"(ge[3]) : "v"(pu[4]), "a"(fw2[4][0]), "v"(vF), "s"(rsW), "s"(so0), "a"(fw2[4][1]), "v"(rwd), "v"(ac[1][3]), "a"(fw2[4][2]), "v"(ye[1]), "v"(ze[1]), "v"(ye[2]), "v"(ze[2]), "a"(fw2[4][3]), "v"(ye[3]), "v"(ze[3]) : "memory"); SB();
  WAITVB(12);
  SB();
  asm volatile("v_mfma_f32_16x16x32_bf16 %0, %3, %4, %0\n\tbuffer_load_dwordx4 %1, %5, %6, %7 offen offset:512 sc1\n\tv_mfma_f32_16x16x32_bf16 %2, %3, %8, %2"
      : "+v"(ac[0][0]), "=&v"(pu[4]), "+v"(ac[0][1]) : "v"(pu[5]), "a"(fw2[5][0]), "v"(vF), "s"(rsW), "s"(so0), "a"(fw2[5][1]) : "memory"); SB();
  WAITL(0);
  BAR0();
  SB();
  asm volatile("v_mfma_f32_16x16x32_bf16 %0, %5, %6, %0\n\tds_read_b128 %1, %7 offset:18432\n\tds_read_b128 %2, %7 offset:22528\n\tv_mfma_f32_16x16x32_bf16 %3, %5, %8, %3\n\tds_read_b128 %4, %7 offset:26624"
      : "+v"(ac[0][2]), "=&v"(py[1][0]), "=&v"(py[1][1]), "+v"(ac[0][3]), "=&v"(py[1][2]) : "v"(pu[5]), "a"(fw2[5][2]), "v"(rr), "a"(fw2[5][3]) : "memory"); SB();
  WAITVB(12);
  SB();
  asm volatile("v_mfma_f32_16x16x32_bf16 %0, %8, %9, %0\n\tbuffer_load_dwordx4 %1, %10, %11, %12 offen offset:640 sc1\n\tv_mfma_f32_16x16x32_bf16 %2, %8, %13, %2\n\tds_read_b128 %3, %14 offset:30720\n\tds_read_b128 %4, %15 offset:18432\n\tv_mfma_f32_16x16x32_bf16 %5, %8, %16, %5\n\tv_mfma_f32_16x16x32_bf16 %6, %8, %17, %6\n\tds_read_b128 %7, %15 offset:3072"
      : "+v"(ac[0][0]), "=&v"(pu[5]), "+v"(ac[0][1]), "=&v"(py[1][3]), "=&a"(fwo[8]), "+v"(ac[0][2]), "+v"(ac[0][3]), "=&a"(fwo[9]) : "v"(pu[6]), "a"(fw2[6][0]), "v"(vF), "s"(rsW), "s"(so0), "a"(fw2[6][1]), "v"(rr), "v"(ro), "a"(fw2[6][2]), "a"(fw2[6][3]) : "memory"); SB();
  WAITVB(12);
  SB();
  asm volatile("v_mfma_f32_16x16x32_bf16 %0, %5, %6, %0\n\tbuffer_load_dwordx4 %1, %7, %8, %9 offen offset:768 sc1\n\tv_mfma_f32_16x16x32_bf16 %2, %5, %10, %2\n\tds_read_b128 %3, %11 offset:11264\n\tv_mfma_f32_16x16x32_bf16 %4, %5, %12, %4"
      : "+v"(ac[0][0]), "=&v"(pu[6]), "+v"(ac[0][1]), "=&a"(fwo[10]), "+v"(ac[0][2]) : "v"(pu[7]), "a"(fw2[7][0]), "v"(vF), "s"(rsW), "s"(so0), "a"(fw2[7][1]), "v"(ro), "a"(fw2[7][2]) : "memory"); SB();
  WAITL(3);
  asm volatile("v_add_f32 %0, %3, %4\n\tv_mfma_f32_16x16x32_bf16 %1, %5, %6, %1\n\tv_add_f32 %2, %7, %4"
      : "=&v"(ye[0]), "+v"(ac[0][3]), "=&v"(ye[1]) : "v"(py[1][0][0]), "v"(bias), "v"(pu[7]), "a"(fw2[7][3]), "v"(py[1][0][1])); SB();
  WAITVB(12);
  SB();
  asm volatile("v_mfma_f32_16x16x32_bf16 %0, %9, %10, %0\n\tbuffer_load_dwordx4 %1, %11, %12, %13 offen offset:0 sc1\n\tv_mfma_f32_16x16x32_bf16 %2, %9, %14, %2\n\tv_add_f32 %3, %15, %16\n\tv_add_f32 %4, %17, %16\n\tv_mfma_f32_16x16x32_bf16 %5, %9, %18, %5\n\tv_add_f32 %6, %19, %20\n\tv_add_f32 %7, %21, %22\n\tv_mfma_f32_16x16x32_bf16 %8, %9, %23, %8"
      : "+v"(ac[0][0]), "=&v"(pu[7]), "+v"(ac[0][1]), "=&v"(ye[2]), "=&v"(ye[3]), "+v"(ac[0][2]), "=&v"(ze[0]), "=&v"(ze[1]), "+v"(ac[0][3]) : "v"(pu[8]), "a"(fw2[8][0]), "v"(vF), "s"(rsW), "s"(so1), "a"(fw2[8][1]), "v"(py[1][0][2]), "v"(bias), "v"(py[1][0][3]), "a"(fw2[8][2]), "v"(py[1][1][0]), "v"(py[1][2][0]), "v"(py[1][1][1]), "v"(py[1][2][1]), "a"(fw2[8][3]) : "memory"); SB();
  asm volatile("v_add_f32 %0, %1, %2"
      : "=&v"(ze[2]) : "v"(py[1][1][2]), "v"(py[1][2][2])); SB();
  WAITVB(12);
  SB();
  asm volatile("v_mfma_f32_16x16x32_bf16 %0, %9, %10, %0\n\tbuffer_load_dwordx4 %1, %11, %12, %13 offen offset:128 sc1\n\tv_mfma_f32_16x16x32_bf16 %2, %9, %14, %2\n\tv_add_f32 %3, %15, %16\n\tv_add_f32 %4, %4, %17\n\tv_mfma_f32_16x16x32_bf16 %5, %9, %18, %5\n\tv_add_f32 %6, %6, %19\n\tv_add_f32 %7, %7, %20\n\tv_mfma_f32_16x16x32_bf16 %8, %9, %21, %8"
      : "+v"(ac[0][0]), "=&v"(pu[8]), "+v"(ac[0][1]), "=&v"(ze[3]), "+v"(ye[0]), "+v"(ac[0][2]), "+v"(ye[1]), "+v"(ye[2]), "+v"(ac[0][3]) : "v"(pu[9]), "v"(fw2[9][0]), "v"(vF), "s"(rsW), "s"(so1), "v"(fw2[9][1]), "v"(py[1][1][3]), "v"(py[1][2][3]), "v"(py[1][3][0]), "v"(fw2[9][2]), "v"(py[1][3][1]), "v"(py[1][3][2]), "v"(fw2[9][3]) : "memory"); SB();
  asm volatile("v_add_f32 %0, %0, %1"
      : "+v"(ye[3]) : "v"(py[1][3][3])); SB();
  WAITVB(12);
  SB();
  asm volatile("v_mfma_f32_16x16x32_bf16 %0, %10, %11, %0\n\tbuffer_load_dwordx4 %1, %12, %13, %14 offen offset:256 sc1\n\tv_mfma_f32_16x16x32_bf16 %2, %10, %15, %2\n\tv_add_f32 %3, %3, %4\n\tv_add_f32 %5, %5, %16\n\tv_mfma_f32_16x16x32_bf16 %6, %10, %17, %6\n\tv_add_f32 %7, %7, %18\n\tv_add_f32 %8, %8, %19\n\tv_mfma_f32_16x16x32_bf16 %9, %10, %20, %9\n\tv_mul_f32 %4, 0xbfb8aa3b, %3"
      : "+v"(ac[0][0]), "=&v"(pu[9]), "+v"(ac[0][1]), "+v"(ye[0]), "+v"(ze[0]), "+v"(ye[1]), "+v"(ac[0][2]), "+v"(ye[2]), "+v"(ye[3]), "+v"(ac[0][3]) : "v"(pu[10]), "v"(fw2[10][0]), "v"(vF), "s"(rsW), "s"(so1), "v"(fw2[10][1]), "v"(ze[1]), "v"(fw2[10][2]), "v"(ze[2]), "v"(ze[3]), "v"(fw2[10][3]) : "memory"); SB();
  WAITVB(12);
  SB();
  asm volatile("v_mfma_f32_16x16x32_bf16 %0, %9, %10, %0\n\tbuffer_load_dwordx4 %1, %11, %12, %13 offen offset:384 sc1\n\tv_mfma_f32_16x16x32_bf16 %2, %9, %14, %2\n\tv_mul_f32 %3, 0xbfb8aa3b, %15\n\tv_mul_f32 %4, 0xbfb8aa3b, %16\n\tv_mfma_f32_16x16x32_bf16 %5, %9, %17, %5\n\tds_read_b128 %6, %18 offset:19456\n\tv_mul_f32 %7, 0xbfb8aa3b, %19\n\tv_mfma_f32_16x16x32_bf16 %8, %9, %20, %8"
      : "+v"(ac[0][0]), "=&v"(pu[10]), "+v"(ac[0][1]), "=&v"(ze[1]), "=&v"(ze[2]), "+v"(ac[0][2]), "=&a"(fwo[11]), "=&v"(ze[3]), "+v"(ac[0][3]) : "v"(pu[11]), "v"(fw2[11][0]), "v"(vF), "s"(rsW), "s"(so1), "v"(fw2[11][1]), "v"(ye[1]), "v"(ye[2]), "v"(fw2[11][2]), "v"(ro), "v"(ye[3]), "v"(fw2[11][3]) : "memory"); SB();
  WAITVB(12);
  SB();
  asm volatile("v_mfma_f32_16x16x32_bf16 %0, %8, %9, %0\n\tbuffer_load_dwordx4 %1, %10, %11, %12 offen offset:512 sc1\n\tv_mfma_f32_16x16x32_bf16 %2, %8, %13, %2\n\tv_exp_f32 %3, %3\n\tv_mfma_f32_16x16x32_bf16 %4, %8, %14, %4\n\tv_exp_f32 %5, %5\n\tv_mfma_f32_16x16x32_bf16 %6, %8, %15, %6\n\tds_read_b128 %7, %16 offset:4096"
      : "+v"(ac[0][0]), "=&v"(pu[11]), "+v"(ac[0][1]), "+v"(ze[0]), "+v"(ac[0][2]), "+v"(ze[1]), "+v"(ac[0][3]), "=&a"(fwo[12]) : "v"(pu[12]), "v"(fw2[12][0]), "v"(vF), "s"(rsW), "s"(so1), "v"(fw2[12][1]), "v"(fw2[12][2]), "v"(fw2[12][3]), "v"(ro) : "memory"); SB();
  WAITVB(12);
  SB();
  asm volatile("v_mfma_f32_16x16x32_bf16 %0, %9, %10, %0\n\tbuffer_load_dwordx4 %1, %11, %12, %13 offen offset:640 sc1\n\tv_mfma_f32_16x16x32_bf16 %2, %9, %14, %2\n\tv_exp_f32 %3, %3\n\tv_mfma_f32_16x16x32_bf16 %4, %9, %15, %4\n\tv_exp_f32 %5, %5\n\tv_mfma_f32_16x16x32_bf16 %6, %9, %16, %6\n\tv_add_f32 %7, 1.0, %7\n\tv_add_f32 %8, 1.0, %8"
      : "+v"(ac[0][0]), "=&v"(pu[12]), "+v"(ac[0][1]), "+v"(ze[2]), "+v"(ac[0][2]), "+v"(ze[3]), "+v"(ac[0][3]), "+v"(ze[0]), "+v"(ze[1]) : "v"(pu[13]), "v"(fw2[13][0]), "v"(vF), "s"(rsW), "s"(so1), "v"(fw2[13][1]), "v"(fw2[13][2]), "v"(fw2[13][3]) : "memory"); SB();
  STAMP(42);
  WAITVB(12);
  asm volatile("ds_read_b128 %0, %8 offset:12288\n\tv_mfma_f32_16x16x32_bf16 %1, %9, %10, 0\n\tbuffer_load_dwordx4 %2, %11, %12, %13 offen offset:768 sc1\n\tv_mfma_f32_16x16x32_bf16 %3, %14, %10, 0\n\tv_add_f32 %4, 1.0, %4\n\tv_add_f32 %5, 1.0, %5\n\tv_mfma_f32_16x16x32_bf16 %6, %15, %10, 0\n\tds_read_b128 %7, %8 offset:20480"
      : "=&a"(fwo[13]), "=&v"(as[0]), "=&v"(pu[13]), "=&v"(as[1]), "+v"(ze[2]), "+v"(ze[3]), "=&v"(as[2]), "=&a"(fwo[14]) : "v"(ro), "a"(fwo[0]), "v"(pu[0]), "v"(vF), "s"(rsW), "s"(so1), "a"(fwo[1]), "a"(fwo[2]) : "memory"); SB();
  WAITVB(12);
  SB();
  asm volatile("v_mfma_f32_16x16x32_bf16 %0, %5, %6, %0\n\tds_write_b128 %7, %8 offset:0\n\tv_mfma_f32_16x16x32_bf16 %1, %9, %6, %1\n\tv_rcp_f32 %2, %2\n\tv_mfma_f32_16x16x32_bf16 %3, %10, %6, %3\n\tds_read_b128 %4, %11 offset:5120"
      : "+v"(as[0]), "+v"(as[1]), "+v"(ze[0]), "+v"(as[2]), "=&a"(fwo[15]) : "a"(fwo[3]), "v"(pu[1]), "v"(rwd), "v"(ac[0][0]), "a"(fwo[4]), "a"(fwo[5]), "v"(ro) : "memory"); SB();
  WAITVB(11);
  asm volatile("ds_read_b128 %0, %4 offset:13312\n\tds_read_b128 %1, %4 offset:21504\n\tds_read_b128 %2, %4 offset:6144\n\tds_read_b128 %3, %4 offset:14336"
      : "=&a"(fwo[16]), "=&a"(fwo[17]), "=&a"(fwo[18]), "=&a"(fwo[19]) : "v"(ro) : "memory"); SB();
  WAITL(9);
  SB();
  asm volatile("v_mfma_f32_16x16x32_bf16 %0, %5, %6, %0\n\tds_write_b128 %7, %8 offset:1024\n\tv_mfma_f32_16x16x32_bf16 %1, %9, %6, %1\n\tv_rcp_f32 %2, %2\n\tv_mfma_f32_16x16x32_bf16 %3, %10, %6, %3\n\tds_read_b128 %4, %11 offset:22528"
      : "+v"(as[0]), "+v"(as[1]), "+v"(ze[1]), "+v"(as[2]), "=&a"(fwo[20]) : "a"(fwo[6]), "v"(pu[2]), "v"(rwd), "v"(ac[0][1]), "a"(fwo[7]), "a"(fwo[8]), "v"(ro) : "memory"); SB();
  WAITVB(10);
  SB();
  asm volatile("v_mfma_f32_16x16x32_bf16 %0, %5, %6, %0\n\tds_write_b128 %7, %8 offset:2048\n\tv_mfma_f32_16x16x32_bf16 %1, %9, %6, %1\n\tv_rcp_f32 %2, %2\n\tv_mfma_f32_16x16x32_bf16 %3, %10, %6, %3\n\tds_read_b128 %4, %11 offset:7168"
      : "+v"(as[0]), "+v"(as[1]), "+v"(ze[2]), "+v"(as[2]), "=&a"(fwo[21]) : "a"(fwo[9]), "v"(pu[3]), "v"(rwd), "v"(ac[0][2]), "a"(fwo[10]), "a"(fwo[11]), "v"(ro) : "memory"); SB();
  WAITVB(9);
  asm volatile("ds_read_b128 %0, %4 offset:15360\n\tds_read_b128 %1, %4 offset:23552\n\tds_read_b128 %2, %4 offset:32768\n\tds_read_b128 %3, %4 offset:38912"
      : "=&a"(fwo[22]), "=&a"(fwo[23]), "=&a"(fwo[24]), "=&a"(fwo[25]) : "v"(ro) : "memory"); SB();
  WAITL(10);
  SB();
  asm volatile("v_mfma_f32_16x16x32_bf16 %0, %5, %6, %0\n\tds_write_b128 %7, %8 offset:3072\n\tv_mfma_f32_16x16x32_bf16 %1, %9, %6, %1\n\tv_rcp_f32 %2, %2\n\tv_mfma_f32_16x16x32_bf16 %3, %10, %6, %3\n\tv_mul_f32 %4, %11, %12"
      : "+v"(as[0]), "+v"(as[1]), "+v"(ze[3]), "+v"(as[2]), "=&v"(ge[4]) : "a"(fwo[12]), "v"(pu[4]), "v"(rwd), "v"(ac[0][3]), "a"(fwo[13]), "a"(fwo[14]), "v"(ye[0]), "v"(ze[0]) : "memory"); SB();
  WAITVB(8);
  SB();
  asm volatile("v_mfma_f32_16x16x32_bf16 %0, %4, %5, %0\n\tv_mul_f32 %1, %6, %7\n\tv_mul_f32 %2, %8, %9\n\tv_mfma_f32_16x16x32_bf16 %3, %10, %5, %3"
      : "+v"(as[0]), "=&v"(ge[5]), "=&v"(ge[6]), "+v"(as[1]) : "a"(fwo[15]), "v"(pu[5]), "v"(ye[1]), "v"(ze[1]), "v"(ye[2]), "v"(ze[2]), "a"(fwo[16])); SB();
  WAITL(0);
  BAR0();
  SB();
  asm volatile("v_mfma_f32_16x16x32_bf16 %0, %1, %2, %0"
      : "+v"(as[2]) : "a"(fwo[17]), "v"(pu[5])); SB();
  WAITVB(7);
  asm volatile("ds_read_b128 %0, %14 offset:45056\n\tds_read_b128 %1, %14 offset:33792\n\tds_read_b128 %2, %14 offset:39936\n\tds_read_b128 %3, %14 offset:46080\n\tds_read_b128 %4, %14 offset:34816\n\tds_read_b128 %5, %14 offset:40960\n\tv_mfma_f32_16x16x32_bf16 %6, %15, %16, %6\n\tds_read_b128 %7, %17 offset:0\n\tds_read_b128 %8, %17 offset:4096\n\tv_mfma_f32_16x16x32_bf16 %9, %18, %16, %9\n\tds_read_b128 %10, %17 offset:8192\n\tds_read_b128 %11, %17 offset:12288\n\tv_mfma_f32_16x16x32_bf16 %12, %19, %16, %12\n\tv_mul_f32 %13, %20, %21"
      : "=&a"(fwo[26]), "=&a"(fwo[27]), "=&a"(fwo[28]), "=&a"(fwo[29]), "=&a"(fwo[30]), "=&a"(fwo[31]), "+v"(as[0]), "=&v"(py[2][0]), "=&v"(py[2][1]), "+v"(as[1]), "=&v"(py[2][2]), "=&v"(py[2][3]), "+v"(as[2]), "=&v"(ge[7]) : "v"(ro), "a"(fwo[18]), "v"(pu[6]), "v"(rr), "a"(fwo[19]), "a"(fwo[20]), "v"(ye[3]), "v"(ze[3]) : "memory"); SB();
  WAITVB(6);
  SB();
  asm volatile("v_mfma_f32_16x16x32_bf16 %0, %4, %5, %0\n\tds_read_b128 %1, %6 offset:47104\n\tv_mfma_f32_16x16x32_bf16 %2, %7, %5, %2\n\tv_mfma_f32_16x16x32_bf16 %3, %8, %5, %3"
      : "+v"(as[0]), "=&a"(fwo[32]), "+v"(as[1]), "+v"(as[2]) : "a"(fwo[21]), "v"(pu[7]), "v"(ro), "a"(fwo[22]), "a"(fwo[23]) : "memory"); SB();
  WAITVB(5);
  asm volatile("ds_read_b128 %0, %5 offset:35840\n\tds_read_b128 %1, %5 offset:41984\n\tds_read_b128 %2, %5 offset:48128\n\tds_read_b128 %3, %5 offset:36864\n\tds_read_b128 %4, %5 offset:43008"
      : "=&a"(fwo[33]), "=&a"(fwo[34]), "=&a"(fwo[35]), "=&a"(fwo[36]), "=&a"(fwo[37]) : "v"(ro) : "memory"); SB();
  WAITL(12);
  SB();
  asm volatile("v_mfma_f32_16x16x32_bf16 %0, %1, %2, %0"
      : "+v"(as[0]) : "a"(fwo[24]), "v"(pu[8])); SB();
  WAITL(6);
  asm volatile("v_add_f32 %0, %6, %7\n\tv_mfma_f32_16x16x32_bf16 %1, %8, %9, %1\n\tv_add_f32 %2, %10, %7\n\tv_add_f32 %3, %11, %7\n\tv_mfma_f32_16x16x32_bf16 %4, %12, %9, %4\n\tv_add_f32 %5, %13, %7"
      : "=&v"(ye[0]), "+v"(as[1]), "=&v"(ye[1]), "=&v"(ye[2]), "+v"(as[2]), "=&v"(ye[3]) : "v"(py[2][0][0]), "v"(bias), "a"(fwo[25]), "v"(pu[8]), "v"(py[2][0][1]), "v"(py[2][0][2]), "a"(fwo[26]), "v"(py[2][0][3])); SB();
  WAITVB(4);
  SB();
  asm volatile("v_mfma_f32_16x16x32_bf16 %0, %7, %8, %0\n\tv_add_f32 %1, %9, %10\n\tv_add_f32 %2, %11, %12\n\tv_mfma_f32_16x16x32_bf16 %3, %13, %8, %3\n\tv_add_f32 %4, %14, %15\n\tv_add_f32 %5, %16, %17\n\tv_mfma_f32_16x16x32_bf16 %6, %18, %8, %6"
      : "+v"(as[0]), "=&v"(ze[0]), "=&v"(ze[1]), "+v"(as[1]), "=&v"(ze[2]), "=&v"(ze[3]), "+v"(as[2]) : "a"(fwo[27]), "v"(pu[9]), "v"(py[2][1][0]), "v"(py[2][2][0]), "v"(py[2][1][1]), "v"(py[2][2][1]), "a"(fwo[28]), "v"(py[2][1][2]), "v"(py[2][2][2]), "v"(py[2][1][3]), "v"(py[2][2][3]), "a"(fwo[29])); SB();
  WAITVB(3);
  asm volatile("ds_read_b128 %0, %4 offset:49152\n\tds_read_b128 %1, %4 offset:37888\n\tds_read_b128 %2, %4 offset:44032\n\tds_read_b128 %3, %4 offset:50176"
      : "=&a"(fwo[38]), "=&a"(fwo[39]), "=&a"(fwo[40]), "=&a"(fwo[41]) : "v"(ro) : "memory"); SB();
  WAITL(6);
  SB();
  asm volatile("v_mfma_f32_16x16x32_bf16 %0, %7, %8, %0\n\tv_add_f32 %1, %1, %9\n\tv_add_f32 %2, %2, %10\n\tv_mfma_f32_16x16x32_bf16 %3, %11, %8, %3\n\tv_add_f32 %4, %4, %12\n\tv_add_f32 %5, %5, %13\n\tv_mfma_f32_16x16x32_bf16 %6, %14, %8, %6"
      : "+v"(as[0]), "+v"(ye[0]), "+v"(ye[1]), "+v"(as[1]), "+v"(ye[2]), "+v"(ye[3]), "+v"(as[2]) : "a"(fwo[30]), "v"(pu[10]), "v"(py[2][3][0]), "v"(py[2][3][1]), "a"(fwo[31]), "v"(py[2][3][2]), "v"(py[2][3][3]), "a"(fwo[32])); SB();
  WAITL(0);
  WAITVB(0);
  SB();
  asm volatile("v_mfma_f32_16x16x32_bf16 %0, %10, %11, %0\n\tv_add_f32 %1, %1, %2\n\tv_add_f32 %3, %3, %4\n\tv_mfma_f32_16x16x32_bf16 %0, %12, %13, %0\n\tv_add_f32 %5, %5, %6\n\tv_add_f32 %7, %7, %8\n\tv_mfma_f32_16x16x32_bf16 %0, %14, %15, %0\n\tv_mul_f32 %2, 0xbfb8aa3b, %1\n\tv_mul_f32 %4, 0xbfb8aa3b, %3\n\tv_mfma_f32_16x16x32_bf16 %9, %16, %11, %9\n\tv_mul_f32 %6, 0xbfb8aa3b, %5\n\tv_mul_f32 %8, 0xbfb8aa3b, %7\n\tv_mfma_f32_16x16x32_bf16 %9, %17, %13, %9\n\tv_exp_f32 %2, %2\n\tv_mfma_f32_16x16x32_bf16 %9, %18, %15, %9\n\tv_exp_f32 %4, %4"
      : "+v"(as[0]), "+v"(ye[0]), "+v"(ze[0]), "+v"(ye[1]), "+v"(ze[1]), "+v"(ye[2]), "+v"(ze[2]), "+v"(ye[3]), "+v"(ze[3]), "+v"(as[1]) : "a"(fwo[33]), "v"(pu[11]), "a"(fwo[36]), "v"(pu[12]), "a"(fwo[39]), "v"(pu[13]), "a"(fwo[34]), "a"(fwo[37]), "a"(fwo[40])); SB();
  asm volatile("v_mfma_f32_16x16x32_bf16 %0, %5, %6, %0\n\tv_exp_f32 %1, %1\n\tv_mfma_f32_16x16x32_bf16 %0, %7, %8, %0\n\tv_exp_f32 %2, %2\n\tv_mfma_f32_16x16x32_bf16 %0, %9, %10, %0\n\tv_add_f32 %3, 1.0, %3\n\tv_add_f32 %4, 1.0, %4"
      : "+v"(as[2]), "+v"(ze[2]), "+v"(ze[3]), "+v"(ze[0]), "+v"(ze[1]) : "a"(fwo[35]), "v"(pu[11]), "a"(fwo[38]), "v"(pu[12]), "a"(fwo[41]), "v"(pu[13])); SB();
  STAMP(44);
  asm volatile("v_add_f32 %0, 1.0, %0\n\tv_add_f32 %1, 1.0, %1\n\tv_rcp_f32 %2, %2\n\tv_rcp_f32 %3, %3\n\tv_rcp_f32 %0, %0\n\tv_rcp_f32 %1, %1\n\tv_mul_f32 %4, %11, %2\n\tv_mul_f32 %5, %12, %3\n\tv_mul_f32 %6, %13, %0\n\tv_mul_f32 %7, %14, %1\n\tv_fma_f32 %8, %15, %16, %8\n\tv_fma_f32 %9, %17, %18, %9\n\tv_fma_f32 %10, %19, %20, %10"
      : "+v"(ze[2]), "+v"(ze[3]), "+v"(ze[0]), "+v"(ze[1]), "=&v"(ge[8]), "=&v"(ge[9]), "=&v"(ge[10]), "=&v"(ge[11]), "+v"(xe[0]), "+v"(xe[1]), "+v"(xe[2]) : "v"(ye[0]), "v"(ye[1]), "v"(ye[2]), "v"(ye[3]), "v"(as[0][0]), "v"(ge[0]), "v"(as[0][1]), "v"(ge[1]), "v"(as[0][2]), "v"(ge[2])); SB();
  asm volatile("v_fma_f32 %0, %9, %10, %0\n\tv_cvt_pk_bf16_f32 %1, %11, %12\n\tv_cvt_pk_bf16_f32 %2, %13, %0\n\tv_fma_f32 %3, %14, %15, %3\n\tv_fma_f32 %4, %16, %17, %4\n\tv_fma_f32 %5, %18, %19, %5\n\tv_fma_f32 %6, %20, %21, %6\n\tv_cvt_pk_bf16_f32 %7, %3, %4\n\tv_cvt_pk_bf16_f32 %8, %5, %6"
      : "+v"(xe[3]), "=&v"(ov[0][0]), "=&v"(ov[0][1]), "+v"(xe[4]), "+v"(xe[5]), "+v"(xe[6]), "+v"(xe[7]), "=&v"(ov[1][0]), "=&v"(ov[1][1]) : "v"(as[0][3]), "v"(ge[3]), "v"(xe[0]), "v"(xe[1]), "v"(xe[2]), "v"(as[1][0]), "v"(ge[4]), "v"(as[1][1]), "v"(ge[5]), "v"(as[1][2]), "v"(ge[6]), "v"(as[1][3]), "v"(ge[7])); SB();
  asm volatile("ds_write_b64 %6, %7 offset:0\n\tv_fma_f32 %0, %8, %9, %0\n\tv_fma_f32 %1, %10, %11, %1\n\tv_fma_f32 %2, %12, %13, %2\n\tv_fma_f32 %3, %14, %15, %3\n\tv_cvt_pk_bf16_f32 %4, %0, %1\n\tv_cvt_pk_bf16_f32 %5, %2, %3\n\tds_write_b64 %16, %17 offset:0"
      : "+v"(xe[8]), "+v"(xe[9]), "+v"(xe[10]), "+v"(xe[11]), "=&v"(ov[2][0]), "=&v"(ov[2][1]) : "v"(xta[0]), "v"(ov[0]), "v"(as[2][0]), "v"(ge[8]), "v"(as[2][1]), "v"(ge[9]), "v"(as[2][2]), "v"(ge[10]), "v"(as[2][3]), "v"(ge[11]), "v"(xta[1]), "v"(ov[1]) : "memory"); SB();
  asm volatile("ds_write_b64 %0, %1 offset:0"
      :  : "v"(xta[2]), "v"(ov[2]) : "memory"); SB();
  { u32x4 t0_, t1_; DSR16(t0_, xo, 0); DSR16(t1_, xo, 1024); WAITLB(1);
    BS16(vXa, rsO_, 0, t0_, ""); WAITLB(0); BS16(vXb, rsO_, 1024, t1_, ""); }
  STAMP(23);

  STAMP(12);
  STAMP_DUMP();
  }
  } else {
  asm volatile("; NEED B");
  unsigned nonce; asm volatile("s_load_dword %0, %1, 0x0" : "=&s"(nonce) : "s"(ws) : "memory");
  const int iw = (4 * xq + jb - 28) * 4 + w, ih = (iw * 37) >> 8;
  const unsigned ldsI = lds0 + w * SCW;
  { u32x4 rsO = srd(wo, 448u * 896u);
    const unsigned vO_ = (28 * (r8 >> 2) + (r8 & 3)) * 896 + ((lane & 7) ^ ((r8 & 2) << 1)) * 16, sO_ = (4 * iw + 28 * ih) * 896;
    DMA1(vO_, rsO, sO_, ldsI, 0, 0, "");
    DMA1(vO_, rsO, sO_, ldsI, 896, 128, "");
    DMA1(vO_, rsO, sO_, ldsI, 1792, 256, "");
    DMA1(vO_, rsO, sO_, ldsI, 2688, 384, "");
    DMA1(vO_, rsO, sO_, ldsI, 3584, 512, "");
    DMA1(vO_, rsO, sO_, ldsI, 4480, 640, "");
    DMA1(vO_, rsO, sO_, ldsI, 5376, 768, "");
  }
  u32x4 rsW = srd((char*)(((size_t)ws + 8191) & ~(size_t)4095) + 512, WS_END); OPQ(rsW);
  const unsigned trb = (8 * g4 + q4) * 128 + pq * 8, trf = ((q4 >> 1) | ((g4 & 1) << 1)) * 32;
  const unsigned vF = r8 * 1024 + (lane & 7) * 16;
  const int r2 = (3 - (int)((size_t)w2 >> 7)) & 7;
  const int st = 8 * xq + r2, dt = jb - 28, db0 = 16 + 3 * dt;
#undef BLK
#define BLK(j) (db0 + ((j) == 2 ? 2 - 2 * (dt & 1) : ((j) ^ 0) + (dt & 1)))
  unsigned to = ldsI + q4 * 128 + ((g4 ^ (q4 & 2)) * 32) + pq * 8, vo[7], so_ = iw < 56 ? WS_WO + (iw >> 2) * 1024 + (iw & 3) * 32768 : 0x80000000u; OPQ(so_);
#pragma unroll
  for (int t = 0; t < 7; ++t) { const int k_ = 4 * t + g4, k7 = (k_ * 37) >> 8; vo[t] = k7 * (131072 - 896) + k_ * 128 + (n16 >> 3) * 16384 + (lane & 7) * 16; }
  u32x4 rs2 = srd(w2, 1792u * 4096u); OPQ(rs2);
  unsigned vW2[4];
#pragma unroll
  for (int i = 0; i < 4; ++i) vW2[i] = (8 * (r8 & 3) + 4 * (r8 >> 2)) * 4096 + st * 128 + ((lane & 7) ^ (2 * i)) * 16 + i * (4096 - 1024);
  int rot2 = 4 * dt + 2 * (st & 7) + 2; rot2 -= 14 * ((rot2 * 37) >> 9);
  bf16x8 fw2[14][4];
  unsigned ldsS = lds0 + S3B + w * 4096, ldsO = lds0 + WOB + w * 8192, ldsP = lds0 + WOB + 24576 + w * 6144, sWo, vWo = r8 * 16384 + (lane & 7) * 16, sWt;
  { const int k_ = BLK(w), k7 = (k_ * 37) >> 8; sWo = WS_WO + k7 * (131072 - 896) + k_ * 128; }
  unsigned fq, vFQ = WS_FQ + (lane < 56 ? lane : 55) * (4 * 4);
  asm volatile("" : "+v"(vW2[0]), "+v"(vW2[1]), "+v"(vW2[2]), "+v"(vW2[3]), "+v"(vFQ), "+v"(vWo)); asm volatile("" : "+s"(rot2), "+s"(ldsS), "+s"(ldsO), "+s"(ldsP), "+s"(sWo));
  if (w == 3) vWo = 0x40000000u; OPV(vWo);
  unsigned tS[4];
#pragma unroll
  for (int i = 0; i < 4; ++i) { tS[i] = ldsS + q4 * 1024 + g4 * 128 + pq * 8 + ((i ^ q4) * 32); OPV(tS[i]); }
  unsigned tO[4];
#pragma unroll
  for (int i = 0; i < 4; ++i) { tO[i] = ldsO + q4 * 1024 + g4 * 128 + pq * 8 + ((i ^ q4) * 32); OPV(tO[i]); }
  unsigned su[14], sw[14], sc_ = 14 * w + rot2; OPQ(sc_);
  { unsigned k_ = sc_ + (rot2 >= 14 ? -14 : 0); OPQ(k_); su[0] = k_ << 13; sw[0] = k_ << 17; OPQ(su[0]); OPQ(sw[0]); }
  { unsigned k_ = sc_ + (rot2 >= 13 ? -13 : 1); OPQ(k_); su[1] = k_ << 13; sw[1] = k_ << 17; OPQ(su[1]); OPQ(sw[1]); }
  { unsigned k_ = sc_ + (rot2 >= 12 ? -12 : 2); OPQ(k_); su[2] = k_ << 13; sw[2] = k_ << 17; OPQ(su[2]); OPQ(sw[2]); }
  { unsigned k_ = sc_ + (rot2 >= 11 ? -11 : 3); OPQ(k_); su[3] = k_ << 13; sw[3] = k_ << 17; OPQ(su[3]); OPQ(sw[3]); }
  { unsigned k_ = sc_ + (rot2 >= 10 ? -10 : 4); OPQ(k_); su[4] = k_ << 13; sw[4] = k_ << 17; OPQ(su[4]); OPQ(sw[4]); }
  { unsigned k_ = sc_ + (rot2 >= 9 ? -9 : 5); OPQ(k_); su[5] = k_ << 13; sw[5] = k_ << 17; OPQ(su[5]); OPQ(sw[5]); }
  { unsigned k_ = sc_ + (rot2 >= 8 ? -8 : 6); OPQ(k_); su[6] = k_ << 13; sw[6] = k_ << 17; OPQ(su[6]); OPQ(sw[6]); }
  { unsigned k_ = sc_ + (rot2 >= 7 ? -7 : 7); OPQ(k_); su[7] = k_ << 13; sw[7] = k_ << 17; OPQ(su[7]); OPQ(sw[7]); }
  { unsigned k_ = sc_ + (rot2 >= 6 ? -6 : 8); OPQ(k_); su[8] = k_ << 13; sw[8] = k_ << 17; OPQ(su[8]); OPQ(sw[8]); }
  { unsigned k_ = sc_ + (rot2 >= 5 ? -5 : 9); OPQ(k_); su[9] = k_ << 13; sw[9] = k_ << 17; OPQ(su[9]); OPQ(sw[9]); }
  { unsigned k_ = sc_ + (rot2 >= 4 ? -4 : 10); OPQ(k_); su[10] = k_ << 13; sw[10] = k_ << 17; OPQ(su[10]); OPQ(sw[10]); }
  { unsigned k_ = sc_ + (rot2 >= 3 ? -3 : 11); OPQ(k_); su[11] = k_ << 13; sw[11] = k_ << 17; OPQ(su[11]); OPQ(sw[11]); }
  { unsigned k_ = sc_ + (rot2 >= 2 ? -2 : 12); OPQ(k_); su[12] = k_ << 13; sw[12] = k_ << 17; OPQ(su[12]); OPQ(sw[12]); }
  { unsigned k_ = sc_ + (rot2 >= 1 ? -1 : 13); OPQ(k_); su[13] = k_ << 13; sw[13] = k_ << 17; OPQ(su[13]); OPQ(sw[13]); }
  unsigned vFU, vFO;
  { const int da = (db0 * 37) >> 8, db = ((db0 + 2) * 37) >> 8, l_ = lane < 56 ? lane : 55;
    vFU = WS_FU + ((l_ < 28 ? da * 128 + l_ : db * 128 + l_ - 28) + w * 28) * (4 * 4);
    vFO = WS_FO + ((2 * st + (w >> 1)) * 8 + (lane & 7)) * (4 * 16);
    asm volatile("" : "+v"(vFU), "+v"(vFO)); }
  unsigned vFU1 = WS_FU + (512 + ((db0 * 37) >> 8) * 128 + w * 28 + (lane < 28 ? lane : 27)) * (4 * 4), fu2; OPV(vFU1);
  unsigned vFj[4], so0 = WS_OT + (4 * st + w) * 16384, so1 = so0 + 8192;
#pragma unroll
  for (int j = 0; j < 3; ++j) { const int k_ = BLK(j), k7 = (k_ * 37) >> 8; vFj[j] = vF + (WS_UT + k7 * (56 * 8192 - 896) + k_ * 128); OPV(vFj[j]); }
  asm volatile("" : "+s"(so0), "+s"(so1));
  const int tau = 2 * (n16 & 7) + (n16 >> 3);
  unsigned rwd = ldsS + vL16, rr = lds0 + S3B + w * 1024 + (16 * g4 + tau) * 16, ro = lds0 + WOB + vL16;
  unsigned xo = ldsS + 34816 + vL16, braw, xt = ldsS + 34816 + tau * 128 + (((g4 >> 1) ^ (n16 & 7)) * 16) + (g4 & 1) * 8, vB2 = (64 * st + 16 * w + tau) * 2;
  unsigned xs = xt ^ (32 * 0);
  asm volatile("" : "+v"(rwd), "+v"(rr), "+v"(ro), "+v"(xt), "+v"(xs), "+v"(xo), "+v"(vB2));
  unsigned vXa, vXb;
  { const int ga = (lane & 7) ^ (r8 >> 1), gb = ga ^ 4, r_ = (64 * st + 16 * w + r8) * 896;
    vXa = ga < 4 ? r_ + (db0 + (dt & 1)) * 32 + ga * 16 : ga < 6 ? r_ + BLK(2) * 32 + (ga - 4) * 16 : 0x40000000;
    vXb = (gb < 4 ? r_ + (db0 + (dt & 1)) * 32 + gb * 16 : gb < 6 ? r_ + BLK(2) * 32 + (gb - 4) * 16 : 0x40000000) + (8 * 896 - 1024); OPV(vXa); OPV(vXb); }
  asm volatile("s_waitcnt lgkmcnt(0)" : "+s"(nonce) :: "memory"); nonce += 1u;
  asm volatile("s_waitcnt vmcnt(6)"
      :  :  : "memory"); SB();
  { const bf16x8 q_ = tr8u(to + 0, 512);
    HS16(vo[0], so_, 0, __builtin_bit_cast(u32x4, q_)); }
  asm volatile("s_waitcnt vmcnt(6)"
      :  :  : "memory"); SB();
  { const bf16x8 q_ = tr8u(to + 1024, 512);
    HS16(vo[1], so_, 0, __builtin_bit_cast(u32x4, q_)); }
  asm volatile("s_waitcnt vmcnt(6)"
      :  :  : "memory"); SB();
  { const bf16x8 q_ = tr8u(to + 2048, 512);
    HS16(vo[2], so_, 0, __builtin_bit_cast(u32x4, q_)); }
  asm volatile("s_waitcnt vmcnt(6)"
      :  :  : "memory"); SB();
  { const bf16x8 q_ = tr8u(to + 3072, 512);
    HS16(vo[3], so_, 0, __builtin_bit_cast(u32x4, q_)); }
  asm volatile("s_waitcnt vmcnt(6)"
      :  :  : "memory"); SB();
  { const bf16x8 q_ = tr8u(to + 4096, 512);
    HS16(vo[4], so_, 0, __builtin_bit_cast(u32x4, q_)); }
  asm volatile("s_waitcnt vmcnt(6)"
      :  :  : "memory"); SB();
  { const bf16x8 q_ = tr8u(to + 5120, 512);
    HS16(vo[5], so_, 0, __builtin_bit_cast(u32x4, q_)); }
  asm volatile("s_waitcnt vmcnt(6)"
      :  :  : "memory"); SB();
  { const bf16x8 q_ = tr8u(to + 6144, 512);
    HS16(vo[6], so_, 0, __builtin_bit_cast(u32x4, q_)); }
  for (int k = 0; k < 4; ++k) __builtin_amdgcn_s_sleep(4);
  const unsigned vT = (lane & 7) == r2 ? 0x40000000u : (448 * w + 8 * dt + r8) * 4096 + (8 * xq + (lane & 7)) * 128; const int k0 = (2 * (lane & 7)) % 14;
  { const unsigned v_ = vT + (k0 + 0 >= 14 ? k0 + 0 - 14 : k0 + 0) * 131072; TOUCH(v_, rs2, 0u, 0); }
  { const unsigned v_ = vT + (k0 + 4 >= 14 ? k0 + 4 - 14 : k0 + 4) * 131072; TOUCH(v_, rs2, 0u, 0); }
  asm volatile("s_waitcnt vmcnt(1)"
      :  :  : "memory"); SB();
  { const unsigned v_ = vT + (k0 + 8 >= 14 ? k0 + 8 - 14 : k0 + 8) * 131072; TOUCH(v_, rs2, 0u, 0); }
  asm volatile("s_waitcnt vmcnt(3)"
      :  :  : "memory"); SB();
  STFQ(WS_FQ + iw * (4 * 4));
  STAMP(4);
  asm volatile("s_waitcnt vmcnt(1)"
      :  :  : "memory"); SB();
  { const unsigned v_ = vT + (k0 + 12 >= 14 ? k0 + 12 - 14 : k0 + 12) * 131072; TOUCH(v_, rs2, 0u, 0); }
  asm volatile("s_waitcnt vmcnt(1)"
      :  :  : "memory"); SB();
  { const unsigned v_ = vT + (k0 + 1 >= 14 ? k0 + 1 - 14 : k0 + 1) * 131072; TOUCH(v_, rs2, 0u, 0); }
  asm volatile("s_waitcnt vmcnt(1)"
      :  :  : "memory"); SB();
  { const unsigned v_ = vT + (k0 + 5 >= 14 ? k0 + 5 - 14 : k0 + 5) * 131072; TOUCH(v_, rs2, 0u, 0); }
  asm volatile("s_waitcnt vmcnt(1)"
      :  :  : "memory"); SB();
  { const unsigned v_ = vT + (k0 + 9 >= 14 ? k0 + 9 - 14 : k0 + 9) * 131072; TOUCH(v_, rs2, 0u, 0); }
  asm volatile("s_waitcnt vmcnt(1)"
      :  :  : "memory"); SB();
  { const unsigned v_ = vT + (k0 + 13 >= 14 ? k0 + 13 - 14 : k0 + 13) * 131072; TOUCH(v_, rs2, 0u, 0); }
  STAMP(3);
  asm volatile("s_add_u32 m0, %0, 0\n\ts_nop 0\n\tbuffer_load_dwordx4 %1, %2, %3 offen offset:0  lds\n\tbuffer_load_dwordx4 %4, %2, %3 offen offset:1024  lds\n\tbuffer_load_dwordx4 %5, %2, %3 offen offset:2048  lds\n\tbuffer_load_dwordx4 %6, %2, %3 offen offset:3072  lds\n\ts_add_u32 m0, %0, 4096\n\ts_nop 0\n\tbuffer_load_dwordx4 %1, %2, %7 offen offset:0  lds\n\tbuffer_load_dwordx4 %4, %2, %7 offen offset:1024  lds\n\tbuffer_load_dwordx4 %5, %2, %7 offen offset:2048  lds\n\tbuffer_load_dwordx4 %6, %2, %7 offen offset:3072  lds\n\ts_add_u32 m0, %8, 18432\n\ts_nop 0\n\tbuffer_load_dwordx4 %1, %2, %9 offen offset:0  lds\n\tbuffer_load_dwordx4 %4, %2, %9 offen offset:1024  lds\n\tbuffer_load_dwordx4 %5, %2, %9 offen offset:2048  lds\n\tbuffer_load_dwordx4 %6, %2, %9 offen offset:3072  lds\n\ts_add_u32 m0, %8, 34816\n\ts_nop 0\n\tbuffer_load_dwordx4 %1, %2, %10 offen offset:0  lds\n\tbuffer_load_dwordx4 %4, %2, %10 offen offset:1024  lds\n\tbuffer_load_dwordx4 %5, %2, %10 offen offset:2048  lds\n\tbuffer_load_dwordx4 %6, %2, %10 offen offset:3072  lds\n\ts_waitcnt vmcnt(12)"
      :  : "s"(ldsO), "v"(vW2[0]), "s"(rs2), "s"(sw[0]), "v"(vW2[1]), "v"(vW2[2]), "v"(vW2[3]), "s"(sw[1]), "s"(ldsS), "s"(sw[2]), "s"(sw[3]) : "memory", "scc"); SB();
  fw2[0][0] = tr8u(tO[0] + 0, 512);
  fw2[0][1] = tr8u(tO[1] + 0, 512);
  fw2[0][2] = tr8u(tO[2] + 0, 512);
  fw2[0][3] = tr8u(tO[3] + 0, 512);
  asm volatile("s_add_u32 m0, %0, 0\n\ts_nop 0\n\tbuffer_load_dwordx4 %1, %2, %3 offen offset:0  lds\n\tbuffer_load_dwordx4 %4, %2, %3 offen offset:1024  lds\n\tbuffer_load_dwordx4 %5, %2, %3 offen offset:2048  lds\n\tbuffer_load_dwordx4 %6, %2, %3 offen offset:3072  lds\n\ts_waitcnt vmcnt(12)"
      :  : "s"(ldsS), "v"(vW2[0]), "s"(rs2), "s"(sw[4]), "v"(vW2[1]), "v"(vW2[2]), "v"(vW2[3]) : "memory", "scc"); SB();
  fw2[1][0] = tr8u(tO[0] + 4096, 512);
  fw2[1][1] = tr8u(tO[1] + 4096, 512);
  fw2[1][2] = tr8u(tO[2] + 4096, 512);
  fw2[1][3] = tr8u(tO[3] + 4096, 512);
  WAITL(8);
  asm volatile("s_add_u32 m0, %0, 0\n\ts_nop 0\n\tbuffer_load_dwordx4 %1, %2, %3 offen offset:0  lds\n\tbuffer_load_dwordx4 %4, %2, %3 offen offset:1024  lds\n\tbuffer_load_dwordx4 %5, %2, %3 offen offset:2048  lds\n\tbuffer_load_dwordx4 %6, %2, %3 offen offset:3072  lds\n\ts_waitcnt vmcnt(12)"
      :  : "s"(ldsO), "v"(vW2[0]), "s"(rs2), "s"(sw[5]), "v"(vW2[1]), "v"(vW2[2]), "v"(vW2[3]) : "memory", "scc"); SB();
  fw2[2][0] = tr8u(tS[0] + 18432, 512);
  fw2[2][1] = tr8u(tS[1] + 18432, 512);
  fw2[2][2] = tr8u(tS[2] + 18432, 512);
  fw2[2][3] = tr8u(tS[3] + 18432, 512);
  WAITL(8);
  asm volatile("s_add_u32 m0, %0, 4096\n\ts_nop 0\n\tbuffer_load_dwordx4 %1, %2, %3 offen offset:0  lds\n\tbuffer_load_dwordx4 %4, %2, %3 offen offset:1024  lds\n\tbuffer_load_dwordx4 %5, %2, %3 offen offset:2048  lds\n\tbuffer_load_dwordx4 %6, %2, %3 offen offset:3072  lds\n\ts_waitcnt vmcnt(12)"
      :  : "s"(ldsO), "v"(vW2[0]), "s"(rs2), "s"(sw[6]), "v"(vW2[1]), "v"(vW2[2]), "v"(vW2[3]) : "memory", "scc"); SB();
  fw2[3][0] = tr8u(tS[0] + 34816, 512);
  fw2[3][1] = tr8u(tS[1] + 34816, 512);
  fw2[3][2] = tr8u(tS[2] + 34816, 512);
  fw2[3][3] = tr8u(tS[3] + 34816, 512);
  WAITL(8);
  asm volatile("s_add_u32 m0, %0, 18432\n\ts_nop 0\n\tbuffer_load_dwordx4 %1, %2, %3 offen offset:0  lds\n\tbuffer_load_dwordx4 %4, %2, %3 offen offset:1024  lds\n\tbuffer_load_dwordx4 %5, %2, %3 offen offset:2048  lds\n\tbuffer_load_dwordx4 %6, %2, %3 offen offset:3072  lds\n\ts_waitcnt vmcnt(12)"
      :  : "s"(ldsS), "v"(vW2[0]), "s"(rs2), "s"(sw[7]), "v"(vW2[1]), "v"(vW2[2]), "v"(vW2[3]) : "memory", "scc"); SB();
  fw2[4][0] = tr8u(tS[0] + 0, 512);
  fw2[4][1] = tr8u(tS[1] + 0, 512);
  fw2[4][2] = tr8u(tS[2] + 0, 512);
  fw2[4][3] = tr8u(tS[3] + 0, 512);
  WAITL(8);
  asm volatile("s_add_u32 m0, %0, 34816\n\ts_nop 0\n\tbuffer_load_dwordx4 %1, %2, %3 offen offset:0  lds\n\tbuffer_load_dwordx4 %4, %2, %3 offen offset:1024  lds\n\tbuffer_load_dwordx4 %5, %2, %3 offen offset:2048  lds\n\tbuffer_load_dwordx4 %6, %2, %3 offen offset:3072  lds\n\ts_waitcnt vmcnt(12)"
      :  : "s"(ldsS), "v"(vW2[0]), "s"(rs2), "s"(sw[8]), "v"(vW2[1]), "v"(vW2[2]), "v"(vW2[3]) : "memory", "scc"); SB();
  fw2[5][0] = tr8u(tO[0] + 0, 512);
  fw2[5][1] = tr8u(tO[1] + 0, 512);
  fw2[5][2] = tr8u(tO[2] + 0, 512);
  fw2[5][3] = tr8u(tO[3] + 0, 512);
  WAITL(8);
  asm volatile("s_add_u32 m0, %0, 0\n\ts_nop 0\n\tbuffer_load_dwordx4 %1, %2, %3 offen offset:0  lds\n\tbuffer_load_dwordx4 %4, %2, %3 offen offset:1024  lds\n\tbuffer_load_dwordx4 %5, %2, %3 offen offset:2048  lds\n\tbuffer_load_dwordx4 %6, %2, %3 offen offset:3072  lds\n\ts_waitcnt vmcnt(12)"
      :  : "s"(ldsS), "v"(vW2[0]), "s"(rs2), "s"(sw[9]), "v"(vW2[1]), "v"(vW2[2]), "v"(vW2[3]) : "memory", "scc"); SB();
  fw2[6][0] = tr8u(tO[0] + 4096, 512);
  fw2[6][1] = tr8u(tO[1] + 4096, 512);
  fw2[6][2] = tr8u(tO[2] + 4096, 512);
  fw2[6][3] = tr8u(tO[3] + 4096, 512);
  WAITL(8);
  asm volatile("s_add_u32 m0, %0, 0\n\ts_nop 0\n\tbuffer_load_dwordx4 %1, %2, %3 offen offset:0  lds\n\tbuffer_load_dwordx4 %4, %2, %3 offen offset:1024  lds\n\tbuffer_load_dwordx4 %5, %2, %3 offen offset:2048  lds\n\tbuffer_load_dwordx4 %6, %2, %3 offen offset:3072  lds\n\ts_waitcnt vmcnt(12)"
      :  : "s"(ldsO), "v"(vW2[0]), "s"(rs2), "s"(sw[10]), "v"(vW2[1]), "v"(vW2[2]), "v"(vW2[3]) : "memory", "scc"); SB();
  fw2[7][0] = tr8u(tS[0] + 18432, 512);
  fw2[7][1] = tr8u(tS[1] + 18432, 512);
  fw2[7][2] = tr8u(tS[2] + 18432, 512);
  fw2[7][3] = tr8u(tS[3] + 18432, 512);
  WAITL(0);
  asm volatile("s_add_u32 m0, %0, 18432\n\ts_nop 0\n\tbuffer_load_dwordx4 %1, %2, %3 offen offset:0  lds\n\tbuffer_load_dwordx4 %4, %2, %3 offen offset:1024  lds\n\tbuffer_load_dwordx4 %5, %2, %3 offen offset:2048  lds\n\tbuffer_load_dwordx4 %6, %2, %3 offen offset:3072  lds\n\ts_waitcnt vmcnt(12)"
      :  : "s"(ldsS), "v"(vW2[0]), "s"(rs2), "s"(sw[11]), "v"(vW2[1]), "v"(vW2[2]), "v"(vW2[3]) : "memory", "scc"); SB();
  fw2[8][0] = tr8u(tS[0] + 34816, 512);
  fw2[8][1] = tr8u(tS[1] + 34816, 512);
  fw2[8][2] = tr8u(tS[2] + 34816, 512);
  fw2[8][3] = tr8u(tS[3] + 34816, 512);
  WAITL(0);
  asm volatile("s_add_u32 m0, %0, 34816\n\ts_nop 0\n\tbuffer_load_dwordx4 %1, %2, %3 offen offset:0  lds\n\tbuffer_load_dwordx4 %4, %2, %3 offen offset:1024  lds\n\tbuffer_load_dwordx4 %5, %2, %3 offen offset:2048  lds\n\tbuffer_load_dwordx4 %6, %2, %3 offen offset:3072  lds\n\ts_waitcnt vmcnt(12)"
      :  : "s"(ldsS), "v"(vW2[0]), "s"(rs2), "s"(sw[12]), "v"(vW2[1]), "v"(vW2[2]), "v"(vW2[3]) : "memory", "scc"); SB();
  fw2[9][0] = tr8u(tS[0] + 0, 512);
  fw2[9][1] = tr8u(tS[1] + 0, 512);
  fw2[9][2] = tr8u(tS[2] + 0, 512);
  fw2[9][3] = tr8u(tS[3] + 0, 512);
  WAITL(0);
  asm volatile("s_add_u32 m0, %0, 0\n\ts_nop 0\n\tbuffer_load_dwordx4 %1, %2, %3 offen offset:0  lds\n\tbuffer_load_dwordx4 %4, %2, %3 offen offset:1024  lds\n\tbuffer_load_dwordx4 %5, %2, %3 offen offset:2048  lds\n\tbuffer_load_dwordx4 %6, %2, %3 offen offset:3072  lds\n\ts_waitcnt vmcnt(12)"
      :  : "s"(ldsS), "v"(vW2[0]), "s"(rs2), "s"(sw[13]), "v"(vW2[1]), "v"(vW2[2]), "v"(vW2[3]) : "memory", "scc"); SB();
  fw2[10][0] = tr8u(tO[0] + 0, 512);
  fw2[10][1] = tr8u(tO[1] + 0, 512);
  fw2[10][2] = tr8u(tO[2] + 0, 512);
  fw2[10][3] = tr8u(tO[3] + 0, 512);
  asm volatile("s_waitcnt vmcnt(8)"
      :  :  : "memory"); SB();
  fw2[11][0] = tr8u(tS[0] + 18432, 512);
  fw2[11][1] = tr8u(tS[1] + 18432, 512);
  fw2[11][2] = tr8u(tS[2] + 18432, 512);
  fw2[11][3] = tr8u(tS[3] + 18432, 512);
  asm volatile("s_waitcnt vmcnt(4)"
      :  :  : "memory"); SB();
  fw2[12][0] = tr8u(tS[0] + 34816, 512);
  fw2[12][1] = tr8u(tS[1] + 34816, 512);
  fw2[12][2] = tr8u(tS[2] + 34816, 512);
  fw2[12][3] = tr8u(tS[3] + 34816, 512);
  asm volatile("s_waitcnt vmcnt(0)"
      :  :  : "memory"); SB();
  fw2[13][0] = tr8u(tS[0] + 0, 512);
  fw2[13][1] = tr8u(tS[1] + 0, 512);
  fw2[13][2] = tr8u(tS[2] + 0, 512);
  fw2[13][3] = tr8u(tS[3] + 0, 512);
  poll0<16>(rsW, vFQ, nonce);
  asm volatile("s_add_u32 m0, %1, 8192\n\ts_add_u32 %0, %2, 8192"
      : "=&s"(sWt) : "s"(ldsP), "s"(sWo) : "memory", "scc"); SB();
  DMAW4(vWo, rsW, sWt);
  u32x4 rsX = srd(x, 2048u * 896u); OPQ(rsX);
  WAITL(8);
  asm volatile("s_add_u32 m0, %2, 34816\n\ts_nop 0\n\tbuffer_load_dwordx4 %3, %4, %5 offen offset:0  lds\n\tbuffer_load_dwordx4 %6, %4, %5 offen offset:1024  lds\n\tglobal_load_ushort %0, %7, %8 offset:0\n\ts_add_u32 m0, %9, 12288\n\ts_add_u32 %1, %10, 12288"
      : "=&v"(braw), "=&s"(sWt) : "s"(ldsS), "v"(vXa), "s"(rsX), "s"(0u), "v"(vXb), "v"(vB2), "s"(b2), "s"(ldsP), "s"(sWo) : "memory", "scc"); SB();
  DMAW2(vWo, rsW, sWt);
  asm volatile("s_add_u32 m0, %1, 0\n\ts_add_u32 %0, %2, 0"
      : "=&s"(sWt) : "s"(ldsO), "s"(sWo) : "memory", "scc"); SB();
  DMAW4(vWo, rsW, sWt);
  asm volatile("s_add_u32 m0, %1, 4096\n\ts_add_u32 %0, %2, 4096"
      : "=&s"(sWt) : "s"(ldsO), "s"(sWo) : "memory", "scc"); SB();
  DMAW4(vWo, rsW, sWt);
  STAMP(16);
  asm volatile("s_waitcnt vmcnt(0)"
      :  :  : "memory"); SB();
  BAR();
  STAMP(5);
  for (int k = 0; k < 0; ++k) __builtin_amdgcn_s_sleep(4);
  {
  unsigned fz[3], vZ0 = WS_FU + (w * 128 + (lane < 56 ? lane : 55)) * (4 * 4), vZ1 = vZ0 + 56 * (4 * 4), vZ2 = WS_FQ + lane * (4 * 4); OPV(vZ0); OPV(vZ1); OPV(vZ2);
  bf16x8 pu[14], fwo[56];
  f32x4 ac[2][4], as[4], py[4][4];
  float ye[4], ze[4], ge[16], xe[16], bias;
  u32x2 ov[4]; unsigned fo;
  u32x2 xv[4]; unsigned xta[4];
  poll0<8>(rsW, vFU, nonce);
  STAMP(17);
  asm volatile("buffer_load_dwordx4 %0, %10, %11, %12 offen offset:0 sc1\n\tbuffer_load_dwordx4 %1, %10, %11, %13 offen offset:0 sc1\n\tbuffer_load_dwordx4 %2, %10, %11, %14 offen offset:0 sc1\n\tbuffer_load_dwordx4 %3, %10, %11, %15 offen offset:0 sc1\n\tbuffer_load_dwordx4 %4, %10, %11, %16 offen offset:0 sc1\n\tbuffer_load_dwordx4 %5, %10, %11, %17 offen offset:0 sc1\n\tbuffer_load_dwordx4 %6, %10, %11, %18 offen offset:0 sc1\n\tbuffer_load_dwordx4 %7, %10, %11, %19 offen offset:0 sc1\n\tbuffer_load_dwordx4 %8, %10, %11, %20 offen offset:0 sc1\n\tbuffer_load_dwordx4 %9, %10, %11, %21 offen offset:0 sc1"
      : "=&v"(pu[0]), "=&v"(pu[1]), "=&v"(pu[2]), "=&v"(pu[3]), "=&v"(pu[4]), "=&v"(pu[5]), "=&v"(pu[6]), "=&v"(pu[7]), "=&v"(pu[8]), "=&v"(pu[9]) : "v"(vFj[0]), "s"(rsW), "s"(su[0]), "s"(su[1]), "s"(su[2]), "s"(su[3]), "s"(su[4]), "s"(su[5]), "s"(su[6]), "s"(su[7]), "s"(su[8]), "s"(su[9]) : "memory"); SB();
  STAMP(39);
  WAITVB(9);
  SB();
  asm volatile("v_mfma_f32_16x16x32_bf16 %0, %8, %9, 0\n\tbuffer_load_dwordx4 %1, %10, %11, %12 offen offset:0 sc1\n\tv_mfma_f32_16x16x32_bf16 %2, %8, %13, 0\n\tds_read_b128 %3, %14 offset:0\n\tv_mfma_f32_16x16x32_bf16 %4, %8, %15, 0\n\tbuffer_load_dwordx4 %5, %10, %11, %16 offen offset:0 sc1\n\tv_mfma_f32_16x16x32_bf16 %6, %8, %17, 0\n\tds_read_b128 %7, %14 offset:8192"
      : "=&v"(ac[0][0]), "=&v"(pu[10]), "=&v"(ac[0][1]), "=&a"(fwo[0]), "=&v"(ac[0][2]), "=&v"(pu[11]), "=&v"(ac[0][3]), "=&a"(fwo[1]) : "v"(pu[0]), "a"(fw2[0][0]), "v"(vFj[0]), "s"(rsW), "s"(su[10]), "a"(fw2[0][1]), "v"(ro), "a"(fw2[0][2]), "s"(su[11]), "a"(fw2[0][3]) : "memory"); SB();
  WAITVB(10);
  SB();
  asm volatile("v_mfma_f32_16x16x32_bf16 %0, %8, %9, %0\n\tbuffer_load_dwordx4 %1, %10, %11, %12 offen offset:0 sc1\n\tv_mfma_f32_16x16x32_bf16 %2, %8, %13, %2\n\tds_read_b128 %3, %14 offset:16384\n\tv_mfma_f32_16x16x32_bf16 %4, %8, %15, %4\n\tbuffer_load_dwordx4 %5, %10, %11, %16 offen offset:0 sc1\n\tv_mfma_f32_16x16x32_bf16 %6, %8, %17, %6\n\tds_read_b128 %7, %14 offset:1024"
      : "+v"(ac[0][0]), "=&v"(pu[12]), "+v"(ac[0][1]), "=&a"(fwo[2]), "+v"(ac[0][2]), "=&v"(pu[13]), "+v"(ac[0][3]), "=&a"(fwo[3]) : "v"(pu[1]), "a"(fw2[1][0]), "v"(vFj[0]), "s"(rsW), "s"(su[12]), "a"(fw2[1][1]), "v"(ro), "a"(fw2[1][2]), "s"(su[13]), "a"(fw2[1][3]) : "memory"); SB();
  WAITVB(11);
  SB();
  asm volatile("v_mfma_f32_16x16x32_bf16 %0, %3, %4, %0\n\tbuffer_load_dwordx4 %1, %5, %6, %7 offen offset:0 sc1\n\tv_mfma_f32_16x16x32_bf16 %2, %3, %8, %2"
      : "+v"(ac[0][0]), "=&v"(pu[0]), "+v"(ac[0][1]) : "v"(pu[2]), "a"(fw2[2][0]), "v"(vFj[1]), "s"(rsW), "s"(su[0]), "a"(fw2[2][1]) : "memory"); SB();
  WAITVB(25);
  asm volatile("v_lshlrev_b32 %0, 16, %5\n\tv_mfma_f32_16x16x32_bf16 %1, %6, %7, %1\n\tbuffer_load_dwordx4 %2, %8, %9, %10 offen offset:0 sc1\n\tv_mfma_f32_16x16x32_bf16 %3, %6, %11, %3\n\tv_xor_b32 %4, 0, %12"
      : "=&v"(bias), "+v"(ac[0][2]), "=&v"(pu[1]), "+v"(ac[0][3]), "=&v"(xta[0]) : "v"(braw), "v"(pu[2]), "a"(fw2[2][2]), "v"(vFj[1]), "s"(rsW), "s"(su[1]), "a"(fw2[2][3]), "v"(xs) : "memory"); SB();
  WAITVB(12);
  SB();
  asm volatile("v_mfma_f32_16x16x32_bf16 %0, %10, %11, %0\n\tbuffer_load_dwordx4 %1, %12, %13, %14 offen offset:0 sc1\n\tv_mfma_f32_16x16x32_bf16 %2, %10, %15, %2\n\tv_xor_b32 %3, 32, %16\n\tv_xor_b32 %4, 64, %17\n\tv_mfma_f32_16x16x32_bf16 %5, %10, %18, %5\n\tds_read_b64 %6, %19 offset:0\n\tds_read_b64 %7, %3 offset:0\n\tv_mfma_f32_16x16x32_bf16 %8, %10, %20, %8\n\tds_read_b64 %9, %4 offset:0"
      : "+v"(ac[0][0]), "=&v"(pu[2]), "+v"(ac[0][1]), "=&v"(xta[1]), "=&v"(xta[2]), "+v"(ac[0][2]), "=&v"(xv[0]), "=&v"(xv[1]), "+v"(ac[0][3]), "=&v"(xv[2]) : "v"(pu[3]), "a"(fw2[3][0]), "v"(vFj[1]), "s"(rsW), "s"(su[2]), "a"(fw2[3][1]), "v"(xs), "v"(xt), "a"(fw2[3][2]), "v"(xta[0]), "a"(fw2[3][3]) : "memory"); SB();
  WAITVB(12);
  SB();
  asm volatile("v_mfma_f32_16x16x32_bf16 %0, %7, %8, %0\n\tbuffer_load_dwordx4 %1, %9, %10, %11 offen offset:0 sc1\n\tv_mfma_f32_16x16x32_bf16 %2, %7, %12, %2\n\tds_read_b128 %3, %13 offset:9216\n\tv_mfma_f32_16x16x32_bf16 %4, %7, %14, %4\n\tv_mfma_f32_16x16x32_bf16 %5, %7, %15, %5\n\tds_read_b128 %6, %13 offset:17408"
      : "+v"(ac[0][0]), "=&v"(pu[3]), "+v"(ac[0][1]), "=&a"(fwo[4]), "+v"(ac[0][2]), "+v"(ac[0][3]), "=&a"(fwo[5]) : "v"(pu[4]), "a"(fw2[4][0]), "v"(vFj[1]), "s"(rsW), "s"(su[3]), "a"(fw2[4][1]), "v"(ro), "a"(fw2[4][2]), "a"(fw2[4][3]) : "memory"); SB();
  WAITVB(12);
  SB();
  asm volatile("v_mfma_f32_16x16x32_bf16 %0, %3, %4, %0\n\tbuffer_load_dwordx4 %1, %5, %6, %7 offen offset:0 sc1\n\tv_mfma_f32_16x16x32_bf16 %2, %3, %8, %2"
      : "+v"(ac[0][0]), "=&v"(pu[4]), "+v"(ac[0][1]) : "v"(pu[5]), "a"(fw2[5][0]), "v"(vFj[1]), "s"(rsW), "s"(su[4]), "a"(fw2[5][1]) : "memory"); SB();
  WAITL(2);
  asm volatile("v_lshlrev_b32 %0, 16, %6\n\tv_mfma_f32_16x16x32_bf16 %1, %7, %8, %1\n\tv_and_b32 %2, 0xffff0000, %6\n\tv_lshlrev_b32 %3, 16, %9\n\tv_mfma_f32_16x16x32_bf16 %4, %7, %10, %4\n\tv_and_b32 %5, 0xffff0000, %9"
      : "=&v"(xe[0]), "+v"(ac[0][2]), "=&v"(xe[1]), "=&v"(xe[2]), "+v"(ac[0][3]), "=&v"(xe[3]) : "v"(xv[0][0]), "v"(pu[5]), "a"(fw2[5][2]), "v"(xv[0][1]), "a"(fw2[5][3])); SB();
  WAITVB(12);
  SB();
  asm volatile("v_mfma_f32_16x16x32_bf16 %0, %10, %11, %0\n\tbuffer_load_dwordx4 %1, %12, %13, %14 offen offset:0 sc1\n\tv_mfma_f32_16x16x32_bf16 %2, %10, %15, %2\n\tv_lshlrev_b32 %3, 16, %16\n\tv_and_b32 %4, 0xffff0000, %16\n\tv_mfma_f32_16x16x32_bf16 %5, %10, %17, %5\n\tv_lshlrev_b32 %6, 16, %18\n\tv_and_b32 %7, 0xffff0000, %18\n\tv_mfma_f32_16x16x32_bf16 %8, %10, %19, %8\n\tv_lshlrev_b32 %9, 16, %20"
      : "+v"(ac[0][0]), "=&v"(pu[5]), "+v"(ac[0][1]), "=&v"(xe[4]), "=&v"(xe[5]), "+v"(ac[0][2]), "=&v"(xe[6]), "=&v"(xe[7]), "+v"(ac[0][3]), "=&v"(xe[8]) : "v"(pu[6]), "a"(fw2[6][0]), "v"(vFj[1]), "s"(rsW), "s"(su[5]), "a"(fw2[6][1]), "v"(xv[1][0]), "a"(fw2[6][2]), "v"(xv[1][1]), "a"(fw2[6][3]), "v"(xv[2][0]) : "memory"); SB();
  WAITVB(12);
  SB();
  asm volatile("v_mfma_f32_16x16x32_bf16 %0, %9, %10, %0\n\tbuffer_load_dwordx4 %1, %11, %12, %13 offen offset:0 sc1\n\tv_mfma_f32_16x16x32_bf16 %2, %9, %14, %2\n\tv_and_b32 %3, 0xffff0000, %15\n\tv_lshlrev_b32 %4, 16, %16\n\tv_mfma_f32_16x16x32_bf16 %5, %9, %17, %5\n\tds_read_b128 %6, %18 offset:2048\n\tv_and_b32 %7, 0xffff0000, %16\n\tv_mfma_f32_16x16x32_bf16 %8, %9, %19, %8"
      : "+v"(ac[0][0]), "=&v"(pu[6]), "+v"(ac[0][1]), "=&v"(xe[9]), "=&v"(xe[10]), "+v"(ac[0][2]), "=&a"(fwo[6]), "=&v"(xe[11]), "+v"(ac[0][3]) : "v"(pu[7]), "a"(fw2[7][0]), "v"(vFj[1]), "s"(rsW), "s"(su[6]), "a"(fw2[7][1]), "v"(xv[2][0]), "v"(xv[2][1]), "a"(fw2[7][2]), "v"(ro), "a"(fw2[7][3]) : "memory"); SB();
  WAITVB(12);
  SB();
  asm volatile("v_mfma_f32_16x16x32_bf16 %0, %7, %8, %0\n\tbuffer_load_dwordx4 %1, %9, %10, %11 offen offset:0 sc1\n\tv_mfma_f32_16x16x32_bf16 %2, %7, %12, %2\n\tds_read_b128 %3, %13 offset:10240\n\tv_mfma_f32_16x16x32_bf16 %4, %7, %14, %4\n\tv_mfma_f32_16x16x32_bf16 %5, %7, %15, %5\n\tds_read_b128 %6, %13 offset:18432"
      : "+v"(ac[0][0]), "=&v"(pu[7]), "+v"(ac[0][1]), "=&a"(fwo[7]), "+v"(ac[0][2]), "+v"(ac[0][3]), "=&a"(fwo[8]) : "v"(pu[8]), "a"(fw2[8][0]), "v"(vFj[1]), "s"(rsW), "s"(su[7]), "a"(fw2[8][1]), "v"(ro), "a"(fw2[8][2]), "a"(fw2[8][3]) : "memory"); SB();
  WAITVB(12);
  SB();
  asm volatile("v_mfma_f32_16x16x32_bf16 %0, %7, %8, %0\n\tbuffer_load_dwordx4 %1, %9, %10, %11 offen offset:0 sc1\n\tv_mfma_f32_16x16x32_bf16 %2, %7, %12, %2\n\tds_read_b128 %3, %13 offset:3072\n\tv_mfma_f32_16x16x32_bf16 %4, %7, %14, %4\n\tv_mfma_f32_16x16x32_bf16 %5, %7, %15, %5\n\tds_read_b128 %6, %13 offset:11264"
      : "+v"(ac[0][0]), "=&v"(pu[8]), "+v"(ac[0][1]), "=&a"(fwo[9]), "+v"(ac[0][2]), "+v"(ac[0][3]), "=&a"(fwo[10]) : "v"(pu[9]), "v"(fw2[9][0]), "v"(vFj[1]), "s"(rsW), "s"(su[8]), "v"(fw2[9][1]), "v"(ro), "v"(fw2[9][2]), "v"(fw2[9][3]) : "memory"); SB();
  WAITVB(12);
  SB();
  asm volatile("v_mfma_f32_16x16x32_bf16 %0, %7, %8, %0\n\tbuffer_load_dwordx4 %1, %9, %10, %11 offen offset:0 sc1\n\tv_mfma_f32_16x16x32_bf16 %2, %7, %12, %2\n\tds_read_b128 %3, %13 offset:19456\n\tv_mfma_f32_16x16x32_bf16 %4, %7, %14, %4\n\tv_mfma_f32_16x16x32_bf16 %5, %7, %15, %5\n\tds_read_b128 %6, %13 offset:4096"
      : "+v"(ac[0][0]), "=&v"(pu[9]), "+v"(ac[0][1]), "=&a"(fwo[11]), "+v"(ac[0][2]), "+v"(ac[0][3]), "=&a"(fwo[12]) : "v"(pu[10]), "v"(fw2[10][0]), "v"(vFj[1]), "s"(rsW), "s"(su[9]), "v"(fw2[10][1]), "v"(ro), "v"(fw2[10][2]), "v"(fw2[10][3]) : "memory"); SB();
  WAITVB(12);
  SB();
  asm volatile("v_mfma_f32_16x16x32_bf16 %0, %7, %8, %0\n\tbuffer_load_dwordx4 %1, %9, %10, %11 offen offset:0 sc1\n\tv_mfma_f32_16x16x32_bf16 %2, %7, %12, %2\n\tds_read_b128 %3, %13 offset:12288\n\tv_mfma_f32_16x16x32_bf16 %4, %7, %14, %4\n\tv_mfma_f32_16x16x32_bf16 %5, %7, %15, %5\n\tds_read_b128 %6, %13 offset:20480"
      : "+v"(ac[0][0]), "=&v"(pu[10]), "+v"(ac[0][1]), "=&a"(fwo[13]), "+v"(ac[0][2]), "+v"(ac[0][3]), "=&a"(fwo[14]) : "v"(pu[11]), "v"(fw2[11][0]), "v"(vFj[1]), "s"(rsW), "s"(su[10]), "v"(fw2[11][1]), "v"(ro), "v"(fw2[11][2]), "v"(fw2[11][3]) : "memory"); SB();
  WAITVB(12);
  SB();
  asm volatile("v_mfma_f32_16x16x32_bf16 %0, %5, %6, %0\n\tbuffer_load_dwordx4 %1, %7, %8, %9 offen offset:0 sc1\n\tv_mfma_f32_16x16x32_bf16 %2, %5, %10, %2\n\tds_read_b128 %3, %11 offset:5120\n\tv_mfma_f32_16x16x32_bf16 %4, %5, %12, %4"
      : "+v"(ac[0][0]), "=&v"(pu[11]), "+v"(ac[0][1]), "=&a"(fwo[15]), "+v"(ac[0][2]) : "v"(pu[12]), "v"(fw2[12][0]), "v"(vFj[1]), "s"(rsW), "s"(su[11]), "v"(fw2[12][1]), "v"(ro), "v"(fw2[12][2]) : "memory"); SB();
  u32x4 rsO_ = srd(out, 2048u * 896u); OPQ(rsO_);
  SB();
  asm volatile("v_mfma_f32_16x16x32_bf16 %0, %2, %3, %0\n\tds_read_b128 %1, %4 offset:13312"
      : "+v"(ac[0][3]), "=&a"(fwo[16]) : "v"(pu[12]), "v"(fw2[12][3]), "v"(ro) : "memory"); SB();
  WAITVB(12);
  SB();
  asm volatile("v_mfma_f32_16x16x32_bf16 %0, %7, %8, %0\n\tbuffer_load_dwordx4 %1, %9, %10, %11 offen offset:0 sc1\n\tv_mfma_f32_16x16x32_bf16 %2, %7, %12, %2\n\tds_read_b128 %3, %13 offset:21504\n\tv_mfma_f32_16x16x32_bf16 %4, %7, %14, %4\n\tv_mfma_f32_16x16x32_bf16 %5, %7, %15, %5\n\tds_read_b128 %6, %13 offset:6144"
      : "+v"(ac[0][0]), "=&v"(pu[12]), "+v"(ac[0][1]), "=&a"(fwo[17]), "+v"(ac[0][2]), "+v"(ac[0][3]), "=&a"(fwo[18]) : "v"(pu[13]), "v"(fw2[13][0]), "v"(vFj[1]), "s"(rsW), "s"(su[12]), "v"(fw2[13][1]), "v"(ro), "v"(fw2[13][2]), "v"(fw2[13][3]) : "memory"); SB();
  STAMP(40);
  WAITVB(12);
  SB();
  asm volatile("v_mfma_f32_16x16x32_bf16 %0, %7, %8, 0\n\tbuffer_load_dwordx4 %1, %9, %10, %11 offen offset:0 sc1\n\tv_mfma_f32_16x16x32_bf16 %2, %7, %12, 0\n\tbuffer_load_dword %3, %13, %10, 0 offen sc1\n\tv_mfma_f32_16x16x32_bf16 %4, %7, %14, 0\n\tds_read_b128 %5, %15 offset:14336\n\tv_mfma_f32_16x16x32_bf16 %6, %7, %16, 0"
      : "=&v"(ac[1][0]), "=&v"(pu[13]), "=&v"(ac[1][1]), "=&v"(fo), "=&v"(ac[1][2]), "=&a"(fwo[19]), "=&v"(ac[1][3]) : "v"(pu[0]), "a"(fw2[0][0]), "v"(vFj[1]), "s"(rsW), "s"(su[13]), "a"(fw2[0][1]), "v"(vFO), "a"(fw2[0][2]), "v"(ro), "a"(fw2[0][3]) : "memory"); SB();
  WAITVB(13);
  SB();
  asm volatile("v_mfma_f32_16x16x32_bf16 %0, %5, %6, %0\n\tbuffer_load_dwordx4 %1, %7, %8, %9 offen offset:0 sc1\n\tv_mfma_f32_16x16x32_bf16 %2, %5, %10, %2\n\tds_write_b128 %11, %12 offset:0\n\tv_mfma_f32_16x16x32_bf16 %3, %5, %13, %3\n\tv_mfma_f32_16x16x32_bf16 %4, %5, %14, %4"
      : "+v"(ac[1][0]), "=&v"(pu[0]), "+v"(ac[1][1]), "+v"(ac[1][2]), "+v"(ac[1][3]) : "v"(pu[1]), "a"(fw2[1][0]), "v"(vFj[2]), "s"(rsW), "s"(su[0]), "a"(fw2[1][1]), "v"(rwd), "v"(ac[0][0]), "a"(fw2[1][2]), "a"(fw2[1][3]) : "memory"); SB();
  WAITVB(13);
  SB();
  asm volatile("v_mfma_f32_16x16x32_bf16 %0, %5, %6, %0\n\tbuffer_load_dwordx4 %1, %7, %8, %9 offen offset:0 sc1\n\tv_mfma_f32_16x16x32_bf16 %2, %5, %10, %2\n\tds_write_b128 %11, %12 offset:1024\n\tv_mfma_f32_16x16x32_bf16 %3, %5, %13, %3\n\tv_mfma_f32_16x16x32_bf16 %4, %5, %14, %4"
      : "+v"(ac[1][0]), "=&v"(pu[1]), "+v"(ac[1][1]), "+v"(ac[1][2]), "+v"(ac[1][3]) : "v"(pu[2]), "a"(fw2[2][0]), "v"(vFj[2]), "s"(rsW), "s"(su[1]), "a"(fw2[2][1]), "v"(rwd), "v"(ac[0][1]), "a"(fw2[2][2]), "a"(fw2[2][3]) : "memory"); SB();
  WAITVB(13);
  SB();
  asm volatile("v_mfma_f32_16x16x32_bf16 %0, %5, %6, %0\n\tbuffer_load_dwordx4 %1, %7, %8, %9 offen offset:0 sc1\n\tv_mfma_f32_16x16x32_bf16 %2, %5, %10, %2\n\tds_write_b128 %11, %12 offset:2048\n\tv_mfma_f32_16x16x32_bf16 %3, %5, %13, %3\n\tv_mfma_f32_16x16x32_bf16 %4, %5, %14, %4"
      : "+v"(ac[1][0]), "=&v"(pu[2]), "+v"(ac[1][1]), "+v"(ac[1][2]), "+v"(ac[1][3]) : "v"(pu[3]), "a"(fw2[3][0]), "v"(vFj[2]), "s"(rsW), "s"(su[2]), "a"(fw2[3][1]), "v"(rwd), "v"(ac[0][2]), "a"(fw2[3][2]), "a"(fw2[3][3]) : "memory"); SB();
  WAITVB(13);
  SB();
  asm volatile("v_mfma_f32_16x16x32_bf16 %0, %5, %6, %0\n\tbuffer_load_dwordx4 %1, %7, %8, %9 offen offset:0 sc1\n\tv_mfma_f32_16x16x32_bf16 %2, %5, %10, %2\n\tds_write_b128 %11, %12 offset:3072\n\tv_mfma_f32_16x16x32_bf16 %3, %5, %13, %3\n\tv_mfma_f32_16x16x32_bf16 %4, %5, %14, %4"
      : "+v"(ac[1][0]), "=&v"(pu[3]), "+v"(ac[1][1]), "+v"(ac[1][2]), "+v"(ac[1][3]) : "v"(pu[4]), "a"(fw2[4][0]), "v"(vFj[2]), "s"(rsW), "s"(su[3]), "a"(fw2[4][1]), "v"(rwd), "v"(ac[0][3]), "a"(fw2[4][2]), "a"(fw2[4][3]) : "memory"); SB();
  WAITVB(13);
  SB();
  asm volatile("v_mfma_f32_16x16x32_bf16 %0, %3, %4, %0\n\tbuffer_load_dwordx4 %1, %5, %6, %7 offen offset:0 sc1\n\tv_mfma_f32_16x16x32_bf16 %2, %3, %8, %2"
      : "+v"(ac[1][0]), "=&v"(pu[4]), "+v"(ac[1][1]) : "v"(pu[5]), "a"(fw2[5][0]), "v"(vFj[2]), "s"(rsW), "s"(su[4]), "a"(fw2[5][1]) : "memory"); SB();
  WAITL(0);
  BAR0();
  SB();
  asm volatile("v_mfma_f32_16x16x32_bf16 %0, %5, %6, %0\n\tds_read_b128 %1, %7 offset:0\n\tds_read_b128 %2, %7 offset:4096\n\tv_mfma_f32_16x16x32_bf16 %3, %5, %8, %3\n\tds_read_b128 %4, %7 offset:8192"
      : "+v"(ac[1][2]), "=&v"(py[0][0]), "=&v"(py[0][1]), "+v"(ac[1][3]), "=&v"(py[0][2]) : "v"(pu[5]), "a"(fw2[5][2]), "v"(rr), "a"(fw2[5][3]) : "memory"); SB();
  WAITVB(13);
  SB();
  asm volatile("v_mfma_f32_16x16x32_bf16 %0, %9, %10, %0\n\tbuffer_load_dwordx4 %1, %11, %12, %13 offen offset:0 sc1\n\tv_mfma_f32_16x16x32_bf16 %2, %9, %14, %2\n\tbuffer_load_dword %3, %15, %12, 0 offen sc1\n\tbuffer_load_dword %4, %16, %12, 0 offen sc1\n\tbuffer_load_dword %5, %17, %12, 0 offen sc1\n\tv_mfma_f32_16x16x32_bf16 %6, %9, %18, %6\n\tds_read_b128 %7, %19 offset:12288\n\tv_mfma_f32_16x16x32_bf16 %8, %9, %20, %8"
      : "+v"(ac[1][0]), "=&v"(pu[5]), "+v"(ac[1][1]), "=&v"(fz[0]), "=&v"(fz[1]), "=&v"(fz[2]), "+v"(ac[1][2]), "=&v"(py[0][3]), "+v"(ac[1][3]) : "v"(pu[6]), "a"(fw2[6][0]), "v"(vFj[2]), "s"(rsW), "s"(su[5]), "a"(fw2[6][1]), "v"(vZ0), "v"(vZ1), "v"(vZ2), "a"(fw2[6][2]), "v"(rr), "a"(fw2[6][3]) : "memory"); SB();
  WAITVB(16);
  SB();
  asm volatile("v_mfma_f32_16x16x32_bf16 %0, %5, %6, %0\n\tbuffer_load_dwordx4 %1, %7, %8, %9 offen offset:0 sc1\n\tv_mfma_f32_16x16x32_bf16 %2, %5, %10, %2\n\tv_mfma_f32_16x16x32_bf16 %3, %5, %11, %3\n\tv_mfma_f32_16x16x32_bf16 %4, %5, %12, %4"
      : "+v"(ac[1][0]), "=&v"(pu[6]), "+v"(ac[1][1]), "+v"(ac[1][2]), "+v"(ac[1][3]) : "v"(pu[7]), "a"(fw2[7][0]), "v"(vFj[2]), "s"(rsW), "s"(su[6]), "a"(fw2[7][1]), "a"(fw2[7][2]), "a"(fw2[7][3]) : "memory"); SB();
  WAITL(0);
  SB();
  WAITVB(16);
  SB();
  asm volatile("v_mfma_f32_16x16x32_bf16 %0, %10, %11, %0\n\tbuffer_load_dwordx4 %1, %12, %13, %14 offen offset:0 sc1\n\tv_mfma_f32_16x16x32_bf16 %2, %10, %15, %2\n\tv_add_f32 %3, %16, %17\n\tv_add_f32 %4, %18, %17\n\tv_mfma_f32_16x16x32_bf16 %5, %10, %19, %5\n\tv_add_f32 %6, %20, %17\n\tv_add_f32 %7, %21, %17\n\tv_mfma_f32_16x16x32_bf16 %8, %10, %22, %8\n\tv_add_f32 %9, %23, %24"
      : "+v"(ac[1][0]), "=&v"(pu[7]), "+v"(ac[1][1]), "=&v"(ye[0]), "=&v"(ye[1]), "+v"(ac[1][2]), "=&v"(ye[2]), "=&v"(ye[3]), "+v"(ac[1][3]), "=&v"(ze[0]) : "v"(pu[8]), "a"(fw2[8][0]), "v"(vFj[2]), "s"(rsW), "s"(su[7]), "a"(fw2[8][1]), "v"(py[0][0][0]), "v"(bias), "v"(py[0][0][1]), "a"(fw2[8][2]), "v"(py[0][0][2]), "v"(py[0][0][3]), "a"(fw2[8][3]), "v"(py[0][1][0]), "v"(py[0][2][0]) : "memory"); SB();
  WAITVB(16);
  SB();
  asm volatile("v_mfma_f32_16x16x32_bf16 %0, %9, %10, %0\n\tbuffer_load_dwordx4 %1, %11, %12, %13 offen offset:0 sc1\n\tv_mfma_f32_16x16x32_bf16 %2, %9, %14, %2\n\tv_add_f32 %3, %15, %16\n\tv_add_f32 %4, %17, %18\n\tv_mfma_f32_16x16x32_bf16 %5, %9, %19, %5\n\tv_add_f32 %6, %20, %21\n\tv_add_f32 %7, %7, %22\n\tv_mfma_f32_16x16x32_bf16 %8, %9, %23, %8"
      : "+v"(ac[1][0]), "=&v"(pu[8]), "+v"(ac[1][1]), "=&v"(ze[1]), "=&v"(ze[2]), "+v"(ac[1][2]), "=&v"(ze[3]), "+v"(ye[0]), "+v"(ac[1][3]) : "v"(pu[9]), "v"(fw2[9][0]), "v"(vFj[2]), "s"(rsW), "s"(su[8]), "v"(fw2[9][1]), "v"(py[0][1][1]), "v"(py[0][2][1]), "v"(py[0][1][2]), "v"(py[0][2][2]), "v"(fw2[9][2]), "v"(py[0][1][3]), "v"(py[0][2][3]), "v"(py[0][3][0]), "v"(fw2[9][3]) : "memory"); SB();
  asm volatile("v_add_f32 %0, %0, %1"
      : "+v"(ye[1]) : "v"(py[0][3][1])); SB();
  WAITVB(16);
  SB();
  asm volatile("v_mfma_f32_16x16x32_bf16 %0, %9, %10, %0\n\tbuffer_load_dwordx4 %1, %11, %12, %13 offen offset:0 sc1\n\tv_mfma_f32_16x16x32_bf16 %2, %9, %14, %2\n\tv_add_f32 %3, %3, %15\n\tv_add_f32 %4, %4, %16\n\tv_mfma_f32_16x16x32_bf16 %5, %9, %17, %5\n\tv_add_f32 %6, %6, %18\n\tv_add_f32 %7, %7, %19\n\tv_mfma_f32_16x16x32_bf16 %8, %9, %20, %8\n\tv_add_f32 %3, %3, %21"
      : "+v"(ac[1][0]), "=&v"(pu[9]), "+v"(ac[1][1]), "+v"(ye[2]), "+v"(ye[3]), "+v"(ac[1][2]), "+v"(ye[0]), "+v"(ye[1]), "+v"(ac[1][3]) : "v"(pu[10]), "v"(fw2[10][0]), "v"(vFj[2]), "s"(rsW), "s"(su[9]), "v"(fw2[10][1]), "v"(py[0][3][2]), "v"(py[0][3][3]), "v"(fw2[10][2]), "v"(ze[0]), "v"(ze[1]), "v"(fw2[10][3]), "v"(ze[2]) : "memory"); SB();
  WAITVB(16);
  SB();
  asm volatile("v_mfma_f32_16x16x32_bf16 %0, %10, %11, %0\n\tbuffer_load_dwordx4 %1, %12, %13, %14 offen offset:0 sc1\n\tv_mfma_f32_16x16x32_bf16 %2, %10, %15, %2\n\tv_add_f32 %3, %3, %4\n\tv_mul_f32 %5, 0xbfb8aa3b, %16\n\tv_mfma_f32_16x16x32_bf16 %6, %10, %17, %6\n\tv_mul_f32 %7, 0xbfb8aa3b, %18\n\tv_mul_f32 %8, 0xbfb8aa3b, %19\n\tv_mfma_f32_16x16x32_bf16 %9, %10, %20, %9\n\tv_mul_f32 %4, 0xbfb8aa3b, %3"
      : "+v"(ac[1][0]), "=&v"(pu[10]), "+v"(ac[1][1]), "+v"(ye[3]), "+v"(ze[3]), "=&v"(ze[0]), "+v"(ac[1][2]), "=&v"(ze[1]), "=&v"(ze[2]), "+v"(ac[1][3]) : "v"(pu[11]), "v"(fw2[11][0]), "v"(vFj[2]), "s"(rsW), "s"(su[10]), "v"(fw2[11][1]), "v"(ye[0]), "v"(fw2[11][2]), "v"(ye[1]), "v"(ye[2]), "v"(fw2[11][3]) : "memory"); SB();
  WAITVB(16);
  SB();
  asm volatile("v_mfma_f32_16x16x32_bf16 %0, %7, %8, %0\n\tbuffer_load_dwordx4 %1, %9, %10, %11 offen offset:0 sc1\n\tv_mfma_f32_16x16x32_bf16 %2, %7, %12, %2\n\tv_exp_f32 %3, %3\n\tv_mfma_f32_16x16x32_bf16 %4, %7, %13, %4\n\tv_exp_f32 %5, %5\n\tv_mfma_f32_16x16x32_bf16 %6, %7, %14, %6"
      : "+v"(ac[1][0]), "=&v"(pu[11]), "+v"(ac[1][1]), "+v"(ze[0]), "+v"(ac[1][2]), "+v"(ze[1]), "+v"(ac[1][3]) : "v"(pu[12]), "v"(fw2[12][0]), "v"(vFj[2]), "s"(rsW), "s"(su[11]), "v"(fw2[12][1]), "v"(fw2[12][2]), "v"(fw2[12][3]) : "memory"); SB();
  WAITVB(16);
  SB();
  asm volatile("v_mfma_f32_16x16x32_bf16 %0, %9, %10, %0\n\tbuffer_load_dwordx4 %1, %11, %12, %13 offen offset:0 sc1\n\tv_mfma_f32_16x16x32_bf16 %2, %9, %14, %2\n\tv_exp_f32 %3, %3\n\tv_mfma_f32_16x16x32_bf16 %4, %9, %15, %4\n\tv_exp_f32 %5, %5\n\tv_mfma_f32_16x16x32_bf16 %6, %9, %16, %6\n\tv_add_f32 %7, 1.0, %7\n\tv_add_f32 %8, 1.0, %8"
      : "+v"(ac[1][0]), "=&v"(pu[12]), "+v"(ac[1][1]), "+v"(ze[2]), "+v"(ac[1][2]), "+v"(ze[3]), "+v"(ac[1][3]), "+v"(ze[0]), "+v"(ze[1]) : "v"(pu[13]), "v"(fw2[13][0]), "v"(vFj[2]), "s"(rsW), "s"(su[12]), "v"(fw2[13][1]), "v"(fw2[13][2]), "v"(fw2[13][3]) : "memory"); SB();
  STAMP(41);
  WAITVB(15);
  SB();
  asm volatile("v_mfma_f32_16x16x32_bf16 %0, %8, %9, 0\n\tbuffer_load_dwordx4 %1, %10, %11, %12 offen offset:0 sc1\n\tv_mfma_f32_16x16x32_bf16 %2, %8, %13, 0\n\tv_add_f32 %3, 1.0, %3\n\tv_add_f32 %4, 1.0, %4\n\tv_mfma_f32_16x16x32_bf16 %5, %8, %14, 0\n\tv_rcp_f32 %6, %6\n\tv_mfma_f32_16x16x32_bf16 %7, %8, %15, 0"
      : "=&v"(ac[0][0]), "=&v"(pu[13]), "=&v"(ac[0][1]), "+v"(ze[2]), "+v"(ze[3]), "=&v"(ac[0][2]), "+v"(ze[0]), "=&v"(ac[0][3]) : "v"(pu[0]), "a"(fw2[0][0]), "v"(vFj[2]), "s"(rsW), "s"(su[13]), "a"(fw2[0][1]), "a"(fw2[0][2]), "a"(fw2[0][3]) : "memory"); SB();
  WAITVB(15);
  SB();
  asm volatile("v_mfma_f32_16x16x32_bf16 %0, %1, %2, %0"
      : "+v"(ac[0][0]) : "v"(pu[1]), "a"(fw2[1][0])); SB();
  WAITV1(17, fo);
  if (__builtin_expect(__ballot(fo != nonce) != 0ull, 0)) poll1<4>(rsW, vFO, nonce);
  STAMP(18);
  asm volatile("buffer_load_dwordx4 %0, %6, %7, %8 offen offset:0 sc1\n\tv_mfma_f32_16x16x32_bf16 %1, %9, %10, %1\n\tds_write_b128 %11, %12 offset:18432\n\tv_mfma_f32_16x16x32_bf16 %2, %9, %13, %2\n\tv_rcp_f32 %3, %3\n\tv_mfma_f32_16x16x32_bf16 %4, %9, %14, %4\n\tds_read_b128 %5, %15 offset:22528"
      : "=&v"(pu[0]), "+v"(ac[0][1]), "+v"(ac[0][2]), "+v"(ze[1]), "+v"(ac[0][3]), "=&a"(fwo[20]) : "v"(vF), "s"(rsW), "s"(so0), "v"(pu[1]), "a"(fw2[1][1]), "v"(rwd), "v"(ac[1][0]), "a"(fw2[1][2]), "a"(fw2[1][3]), "v"(ro) : "memory"); SB();
  WAITVB(15);
  SB();
  asm volatile("v_mfma_f32_16x16x32_bf16 %0, %7, %8, %0\n\tbuffer_load_dwordx4 %1, %9, %10, %11 offen offset:128 sc1\n\tv_mfma_f32_16x16x32_bf16 %2, %7, %12, %2\n\tds_write_b128 %13, %14 offset:19456\n\tv_mfma_f32_16x16x32_bf16 %3, %7, %15, %3\n\tv_rcp_f32 %4, %4\n\tv_mfma_f32_16x16x32_bf16 %5, %7, %16, %5\n\tds_read_b128 %6, %17 offset:7168"
      : "+v"(ac[0][0]), "=&v"(pu[1]), "+v"(ac[0][1]), "+v"(ac[0][2]), "+v"(ze[2]), "+v"(ac[0][3]), "=&a"(fwo[21]) : "v"(pu[2]), "a"(fw2[2][0]), "v"(vF), "s"(rsW), "s"(so0), "a"(fw2[2][1]), "v"(rwd), "v"(ac[1][1]), "a"(fw2[2][2]), "a"(fw2[2][3]), "v"(ro) : "memory"); SB();
  WAITVB(15);
  SB();
  asm volatile("v_mfma_f32_16x16x32_bf16 %0, %7, %8, %0\n\tbuffer_load_dwordx4 %1, %9, %10, %11 offen offset:256 sc1\n\tv_mfma_f32_16x16x32_bf16 %2, %7, %12, %2\n\tds_write_b128 %13, %14 offset:20480\n\tv_mfma_f32_16x16x32_bf16 %3, %7, %15, %3\n\tv_rcp_f32 %4, %4\n\tv_mfma_f32_16x16x32_bf16 %5, %7, %16, %5\n\tv_mul_f32 %6, %17, %18"
      : "+v"(ac[0][0]), "=&v"(pu[2]), "+v"(ac[0][1]), "+v"(ac[0][2]), "+v"(ze[3]), "+v"(ac[0][3]), "=&v"(ge[0]) : "v"(pu[3]), "a"(fw2[3][0]), "v"(vF), "s"(rsW), "s"(so0), "a"(fw2[3][1]), "v"(rwd), "v"(ac[1][2]), "a"(fw2[3][2]), "a"(fw2[3][3]), "v"(ye[0]), "v"(ze[0]) : "memory"); SB();
  WAITVB(15);
  SB();
  asm volatile("v_mfma_f32_16x16x32_bf16 %0, %8, %9, %0\n\tbuffer_load_dwordx4 %1, %10, %11, %12 offen offset:384 sc1\n\tv_mfma_f32_16x16x32_bf16 %2, %8, %13, %2\n\tds_write_b128 %14, %15 offset:21504\n\tv_mfma_f32_16x16x32_bf16 %3, %8, %16, %3\n\tv_mul_f32 %4, %17, %18\n\tv_mul_f32 %5, %19, %20\n\tv_mfma_f32_16x16x32_bf16 %6, %8, %21, %6\n\tv_mul_f32 %7, %22, %23"
      : "+v"(ac[0][0]), "=&v"(pu[3]), "+v"(ac[0][1]), "+v"(ac[0][2]), "=&v"(ge[1]), "=&v"(ge[2]), "+v"(ac[0][3]), "=&v"(ge[3]) : "v"(pu[4]), "a"(fw2[4][0]), "v"(vF), "s"(rsW), "s"(so0), "a"(fw2[4][1]), "v"(rwd), "v"(ac[1][3]), "a"(fw2[4][2]), "v"(ye[1]), "v"(ze[1]), "v"(ye[2]), "v"(ze[2]), "a"(fw2[4][3]), "v"(ye[3]), "v"(ze[3]) : "memory"); SB();
  WAITVB(15);
  SB();
  asm volatile("v_mfma_f32_16x16x32_bf16 %0, %3, %4, %0\n\tbuffer_load_dwordx4 %1, %5, %6, %7 offen offset:512 sc1\n\tv_mfma_f32_16x16x32_bf16 %2, %3, %8, %2"
      : "+v"(ac[0][0]), "=&v"(pu[4]), "+v"(ac[0][1]) : "v"(pu[5]), "a"(fw2[5][0]), "v"(vF), "s"(rsW), "s"(so0), "a"(fw2[5][1]) : "memory"); SB();
  WAITL(0);
  BAR0();
  SB();
  asm volatile("v_mfma_f32_16x16x32_bf16 %0, %5, %6, %0\n\tds_read_b128 %1, %7 offset:18432\n\tds_read_b128 %2, %7 offset:22528\n\tv_mfma_f32_16x16x32_bf16 %3, %5, %8, %3\n\tds_read_b128 %4, %7 offset:26624"
      : "+v"(ac[0][2]), "=&v"(py[1][0]), "=&v"(py[1][1]), "+v"(ac[0][3]), "=&v"(py[1][2]) : "v"(pu[5]), "a"(fw2[5][2]), "v"(rr), "a"(fw2[5][3]) : "memory"); SB();
  WAITVB(12);
  SB();
  asm volatile("v_mfma_f32_16x16x32_bf16 %0, %8, %9, %0\n\tbuffer_load_dwordx4 %1, %10, %11, %12 offen offset:640 sc1\n\tv_mfma_f32_16x16x32_bf16 %2, %8, %13, %2\n\tds_read_b128 %3, %14 offset:30720\n\tds_read_b128 %4, %15 offset:15360\n\tv_mfma_f32_16x16x32_bf16 %5, %8, %16, %5\n\tv_mfma_f32_16x16x32_bf16 %6, %8, %17, %6\n\tds_read_b128 %7, %15 offset:23552"
      : "+v"(ac[0][0]), "=&v"(pu[5]), "+v"(ac[0][1]), "=&v"(py[1][3]), "=&a"(fwo[22]), "+v"(ac[0][2]), "+v"(ac[0][3]), "=&a"(fwo[23]) : "v"(pu[6]), "a"(fw2[6][0]), "v"(vF), "s"(rsW), "s"(so0), "a"(fw2[6][1]), "v"(rr), "v"(ro), "a"(fw2[6][2]), "a"(fw2[6][3]) : "memory"); SB();
  WAITVB(12);
  SB();
  asm volatile("v_mfma_f32_16x16x32_bf16 %0, %5, %6, %0\n\tbuffer_load_dwordx4 %1, %7, %8, %9 offen offset:768 sc1\n\tv_mfma_f32_16x16x32_bf16 %2, %5, %10, %2\n\tds_read_b128 %3, %11 offset:32768\n\tv_mfma_f32_16x16x32_bf16 %4, %5, %12, %4"
      : "+v"(ac[0][0]), "=&v"(pu[6]), "+v"(ac[0][1]), "=&a"(fwo[24]), "+v"(ac[0][2]) : "v"(pu[7]), "a"(fw2[7][0]), "v"(vF), "s"(rsW), "s"(so0), "a"(fw2[7][1]), "v"(ro), "a"(fw2[7][2]) : "memory"); SB();
  WAITL(3);
  asm volatile("v_add_f32 %0, %3, %4\n\tv_mfma_f32_16x16x32_bf16 %1, %5, %6, %1\n\tv_add_f32 %2, %7, %4"
      : "=&v"(ye[0]), "+v"(ac[0][3]), "=&v"(ye[1]) : "v"(py[1][0][0]), "v"(bias), "v"(pu[7]), "a"(fw2[7][3]), "v"(py[1][0][1])); SB();
  WAITVB(12);
  SB();
  asm volatile("v_mfma_f32_16x16x32_bf16 %0, %9, %10, %0\n\tbuffer_load_dwordx4 %1, %11, %12, %13 offen offset:0 sc1\n\tv_mfma_f32_16x16x32_bf16 %2, %9, %14, %2\n\tv_add_f32 %3, %15, %16\n\tv_add_f32 %4, %17, %16\n\tv_mfma_f32_16x16x32_bf16 %5, %9, %18, %5\n\tv_add_f32 %6, %19, %20\n\tv_add_f32 %7, %21, %22\n\tv_mfma_f32_16x16x32_bf16 %8, %9, %23, %8"
      : "+v"(ac[0][0]), "=&v"(pu[7]), "+v"(ac[0][1]), "=&v"(ye[2]), "=&v"(ye[3]), "+v"(ac[0][2]), "=&v"(ze[0]), "=&v"(ze[1]), "+v"(ac[0][3]) : "v"(pu[8]), "a"(fw2[8][0]), "v"(vF), "s"(rsW), "s"(so1), "a"(fw2[8][1]), "v"(py[1][0][2]), "v"(bias), "v"(py[1][0][3]), "a"(fw2[8][2]), "v"(py[1][1][0]), "v"(py[1][2][0]), "v"(py[1][1][1]), "v"(py[1][2][1]), "a"(fw2[8][3]) : "memory"); SB();
  asm volatile("v_add_f32 %0, %1, %2"
      : "=&v"(ze[2]) : "v"(py[1][1][2]), "v"(py[1][2][2])); SB();
  WAITVB(12);
  SB();
  asm volatile("v_mfma_f32_16x16x32_bf16 %0, %9, %10, %0\n\tbuffer_load_dwordx4 %1, %11, %12, %13 offen offset:128 sc1\n\tv_mfma_f32_16x16x32_bf16 %2, %9, %14, %2\n\tv_add_f32 %3, %15, %16\n\tv_add_f32 %4, %4, %17\n\tv_mfma_f32_16x16x32_bf16 %5, %9, %18, %5\n\tv_add_f32 %6, %6, %19\n\tv_add_f32 %7, %7, %20\n\tv_mfma_f32_16x16x32_bf16 %8, %9, %21, %8"
      : "+v"(ac[0][0]), "=&v"(pu[8]), "+v"(ac[0][1]), "=&v"(ze[3]), "+v"(ye[0]), "+v"(ac[0][2]), "+v"(ye[1]), "+v"(ye[2]), "+v"(ac[0][3]) : "v"(pu[9]), "v"(fw2[9][0]), "v"(vF), "s"(rsW), "s"(so1), "v"(fw2[9][1]), "v"(py[1][1][3]), "v"(py[1][2][3]), "v"(py[1][3][0]), "v"(fw2[9][2]), "v"(py[1][3][1]), "v"(py[1][3][2]), "v"(fw2[9][3]) : "memory"); SB();
  asm volatile("v_add_f32 %0, %0, %1"
      : "+v"(ye[3]) : "v"(py[1][3][3])); SB();
  WAITVB(12);
  SB();
  asm volatile("v_mfma_f32_16x16x32_bf16 %0, %10, %11, %0\n\tbuffer_load_dwordx4 %1, %12, %13, %14 offen offset:256 sc1\n\tv_mfma_f32_16x16x32_bf16 %2, %10, %15, %2\n\tv_add_f32 %3, %3, %4\n\tv_add_f32 %5, %5, %16\n\tv_mfma_f32_16x16x32_bf16 %6, %10, %17, %6\n\tv_add_f32 %7, %7, %18\n\tv_add_f32 %8, %8, %19\n\tv_mfma_f32_16x16x32_bf16 %9, %10, %20, %9\n\tv_mul_f32 %4, 0xbfb8aa3b, %3"
      : "+v"(ac[0][0]), "=&v"(pu[9]), "+v"(ac[0][1]), "+v"(ye[0]), "+v"(ze[0]), "+v"(ye[1]), "+v"(ac[0][2]), "+v"(ye[2]), "+v"(ye[3]), "+v"(ac[0][3]) : "v"(pu[10]), "v"(fw2[10][0]), "v"(vF), "s"(rsW), "s"(so1), "v"(fw2[10][1]), "v"(ze[1]), "v"(fw2[10][2]), "v"(ze[2]), "v"(ze[3]), "v"(fw2[10][3]) : "memory"); SB();
  WAITVB(12);
  SB();
  asm volatile("v_mfma_f32_16x16x32_bf16 %0, %9, %10, %0\n\tbuffer_load_dwordx4 %1, %11, %12, %13 offen offset:384 sc1\n\tv_mfma_f32_16x16x32_bf16 %2, %9, %14, %2\n\tv_mul_f32 %3, 0xbfb8aa3b, %15\n\tv_mul_f32 %4, 0xbfb8aa3b, %16\n\tv_mfma_f32_16x16x32_bf16 %5, %9, %17, %5\n\tds_read_b128 %6, %18 offset:38912\n\tv_mul_f32 %7, 0xbfb8aa3b, %19\n\tv_mfma_f32_16x16x32_bf16 %8, %9, %20, %8"
      : "+v"(ac[0][0]), "=&v"(pu[10]), "+v"(ac[0][1]), "=&v"(ze[1]), "=&v"(ze[2]), "+v"(ac[0][2]), "=&a"(fwo[25]), "=&v"(ze[3]), "+v"(ac[0][3]) : "v"(pu[11]), "v"(fw2[11][0]), "v"(vF), "s"(rsW), "s"(so1), "v"(fw2[11][1]), "v"(ye[1]), "v"(ye[2]), "v"(fw2[11][2]), "v"(ro), "v"(ye[3]), "v"(fw2[11][3]) : "memory"); SB();
  WAITVB(12);
  SB();
  asm volatile("v_mfma_f32_16x16x32_bf16 %0, %3, %4, %0\n\tbuffer_load_dwordx4 %1, %5, %6, %7 offen offset:512 sc1\n\tv_mfma_f32_16x16x32_bf16 %2, %3, %8, %2"
      : "+v"(ac[0][0]), "=&v"(pu[11]), "+v"(ac[0][1]) : "v"(pu[12]), "v"(fw2[12][0]), "v"(vF), "s"(rsW), "s"(so1), "v"(fw2[12][1]) : "memory"); SB();
  WAITV3(20, fz[0], fz[1], fz[2]);
  if (__builtin_expect(__ballot(((fz[0] ^ nonce) | (fz[1] ^ nonce) | (fz[2] ^ nonce)) != 0u) != 0ull, 0)) { poll0<16>(rsW, vZ0, nonce); poll0<16>(rsW, vZ1, nonce); poll0<16>(rsW, vZ2, nonce); }
  SB();
  asm volatile("v_mfma_f32_16x16x32_bf16 %0, %4, %5, %0\n\tv_exp_f32 %1, %1\n\tv_mfma_f32_16x16x32_bf16 %2, %4, %6, %2\n\tds_read_b128 %3, %7 offset:45056"
      : "+v"(ac[0][2]), "+v"(ze[0]), "+v"(ac[0][3]), "=&a"(fwo[26]) : "v"(pu[12]), "v"(fw2[12][2]), "v"(fw2[12][3]), "v"(ro) : "memory"); SB();
  WAITVB(12);
  SB();
  asm volatile("v_mfma_f32_16x16x32_bf16 %0, %8, %9, %0\n\tbuffer_load_dwordx4 %1, %10, %11, %12 offen offset:640 sc1\n\tv_mfma_f32_16x16x32_bf16 %2, %8, %13, %2\n\tv_exp_f32 %3, %3\n\tv_mfma_f32_16x16x32_bf16 %4, %8, %14, %4\n\tv_exp_f32 %5, %5\n\tv_mfma_f32_16x16x32_bf16 %6, %8, %15, %6\n\tv_exp_f32 %7, %7"
      : "+v"(ac[0][0]), "=&v"(pu[12]), "+v"(ac[0][1]), "+v"(ze[1]), "+v"(ac[0][2]), "+v"(ze[2]), "+v"(ac[0][3]), "+v"(ze[3]) : "v"(pu[13]), "v"(fw2[13][0]), "v"(vF), "s"(rsW), "s"(so1), "v"(fw2[13][1]), "v"(fw2[13][2]), "v"(fw2[13][3]) : "memory"); SB();
  STAMP(42);
  WAITVB(12);
  SB();
  asm volatile("v_mfma_f32_16x16x32_bf16 %0, %7, %8, 0\n\tbuffer_load_dwordx4 %1, %9, %10, %11 offen offset:768 sc1\n\tv_mfma_f32_16x16x32_bf16 %2, %12, %8, 0\n\tv_add_f32 %3, 1.0, %3\n\tv_add_f32 %4, 1.0, %4\n\tv_mfma_f32_16x16x32_bf16 %5, %13, %8, 0\n\tv_add_f32 %6, 1.0, %6"
      : "=&v"(as[0]), "=&v"(pu[13]), "=&v"(as[1]), "+v"(ze[0]), "+v"(ze[1]), "=&v"(as[2]), "+v"(ze[2]) : "a"(fwo[0]), "v"(pu[0]), "v"(vF), "s"(rsW), "s"(so1), "a"(fwo[1]), "a"(fwo[2]) : "memory"); SB();
  WAITVB(12);
  SB();
  asm volatile("v_mfma_f32_16x16x32_bf16 %0, %5, %6, %0\n\tds_write_b128 %7, %8 offset:0\n\tv_mfma_f32_16x16x32_bf16 %1, %9, %6, %1\n\tds_read_b128 %2, %10 offset:33792\n\tv_add_f32 %3, 1.0, %3\n\tv_mfma_f32_16x16x32_bf16 %4, %11, %6, %4"
      : "+v"(as[0]), "+v"(as[1]), "=&a"(fwo[27]), "+v"(ze[3]), "+v"(as[2]) : "a"(fwo[3]), "v"(pu[1]), "v"(rwd), "v"(ac[0][0]), "a"(fwo[4]), "v"(ro), "a"(fwo[5]) : "memory"); SB();
  WAITVB(11);
  SB();
  asm volatile("v_mfma_f32_16x16x32_bf16 %0, %5, %6, %0\n\tds_write_b128 %7, %8 offset:1024\n\tv_mfma_f32_16x16x32_bf16 %1, %9, %6, %1\n\tv_rcp_f32 %2, %2\n\tv_mfma_f32_16x16x32_bf16 %3, %10, %6, %3\n\tds_read_b128 %4, %11 offset:39936"
      : "+v"(as[0]), "+v"(as[1]), "+v"(ze[0]), "+v"(as[2]), "=&a"(fwo[28]) : "a"(fwo[6]), "v"(pu[2]), "v"(rwd), "v"(ac[0][1]), "a"(fwo[7]), "a"(fwo[8]), "v"(ro) : "memory"); SB();
  WAITVB(10);
  SB();
  asm volatile("v_mfma_f32_16x16x32_bf16 %0, %5, %6, %0\n\tds_write_b128 %7, %8 offset:2048\n\tv_mfma_f32_16x16x32_bf16 %1, %9, %6, %1\n\tv_rcp_f32 %2, %2\n\tv_mfma_f32_16x16x32_bf16 %3, %10, %6, %3\n\tds_read_b128 %4, %11 offset:46080"
      : "+v"(as[0]), "+v"(as[1]), "+v"(ze[1]), "+v"(as[2]), "=&a"(fwo[29]) : "a"(fwo[9]), "v"(pu[3]), "v"(rwd), "v"(ac[0][2]), "a"(fwo[10]), "a"(fwo[11]), "v"(ro) : "memory"); SB();
  WAITVB(9);
  SB();
  asm volatile("v_mfma_f32_16x16x32_bf16 %0, %5, %6, %0\n\tds_write_b128 %7, %8 offset:3072\n\tv_mfma_f32_16x16x32_bf16 %1, %9, %6, %1\n\tv_rcp_f32 %2, %2\n\tv_mfma_f32_16x16x32_bf16 %3, %10, %6, %3\n\tds_read_b128 %4, %11 offset:34816"
      : "+v"(as[0]), "+v"(as[1]), "+v"(ze[2]), "+v"(as[2]), "=&a"(fwo[30]) : "a"(fwo[12]), "v"(pu[4]), "v"(rwd), "v"(ac[0][3]), "a"(fwo[13]), "a"(fwo[14]), "v"(ro) : "memory"); SB();
  WAITVB(8);
  SB();
  asm volatile("v_mfma_f32_16x16x32_bf16 %0, %3, %4, %0\n\tv_rcp_f32 %1, %1\n\tv_mfma_f32_16x16x32_bf16 %2, %5, %4, %2"
      : "+v"(as[0]), "+v"(ze[3]), "+v"(as[1]) : "a"(fwo[15]), "v"(pu[5]), "a"(fwo[16])); SB();
  WAITL(1);
  BAR0();
  SB();
  asm volatile("v_mfma_f32_16x16x32_bf16 %0, %1, %2, %0"
      : "+v"(as[2]) : "a"(fwo[17]), "v"(pu[5])); SB();
  WAITVB(7);
  asm volatile("ds_read_b128 %0, %9 offset:40960\n\tv_mfma_f32_16x16x32_bf16 %1, %10, %11, %1\n\tds_read_b128 %2, %12 offset:0\n\tds_read_b128 %3, %12 offset:4096\n\tv_mfma_f32_16x16x32_bf16 %4, %13, %11, %4\n\tds_read_b128 %5, %12 offset:8192\n\tds_read_b128 %6, %12 offset:12288\n\tv_mfma_f32_16x16x32_bf16 %7, %14, %11, %7\n\tv_mul_f32 %8, %15, %16"
      : "=&a"(fwo[31]), "+v"(as[0]), "=&v"(py[2][0]), "=&v"(py[2][1]), "+v"(as[1]), "=&v"(py[2][2]), "=&v"(py[2][3]), "+v"(as[2]), "=&v"(ge[4]) : "v"(ro), "a"(fwo[18]), "v"(pu[6]), "v"(rr), "a"(fwo[19]), "a"(fwo[20]), "v"(ye[0]), "v"(ze[0]) : "memory"); SB();
  WAITVB(6);
  SB();
  asm volatile("v_mfma_f32_16x16x32_bf16 %0, %7, %8, %0\n\tv_mul_f32 %1, %9, %10\n\tv_mul_f32 %2, %11, %12\n\tv_mfma_f32_16x16x32_bf16 %3, %13, %8, %3\n\tds_read_b128 %4, %14 offset:47104\n\tv_mul_f32 %5, %15, %16\n\tv_mfma_f32_16x16x32_bf16 %6, %17, %8, %6"
      : "+v"(as[0]), "=&v"(ge[5]), "=&v"(ge[6]), "+v"(as[1]), "=&a"(fwo[32]), "=&v"(ge[7]), "+v"(as[2]) : "a"(fwo[21]), "v"(pu[7]), "v"(ye[1]), "v"(ze[1]), "v"(ye[2]), "v"(ze[2]), "a"(fwo[22]), "v"(ro), "v"(ye[3]), "v"(ze[3]), "a"(fwo[23]) : "memory"); SB();
  WAITVB(5);
  asm volatile("ds_read_b128 %0, %6 offset:35840\n\tds_read_b128 %1, %6 offset:41984\n\tds_read_b128 %2, %6 offset:48128\n\tds_read_b128 %3, %6 offset:36864\n\tds_read_b128 %4, %6 offset:43008\n\tv_mfma_f32_16x16x32_bf16 %5, %7, %8, %5"
      : "=&a"(fwo[33]), "=&a"(fwo[34]), "=&a"(fwo[35]), "=&a"(fwo[36]), "=&a"(fwo[37]), "+v"(as[0]) : "v"(ro), "a"(fwo[24]), "v"(pu[8]) : "memory"); SB();
  WAITL(6);
  asm volatile("v_add_f32 %0, %6, %7\n\tv_mfma_f32_16x16x32_bf16 %1, %8, %9, %1\n\tv_add_f32 %2, %10, %7\n\tv_add_f32 %3, %11, %7\n\tv_mfma_f32_16x16x32_bf16 %4, %12, %9, %4\n\tv_add_f32 %5, %13, %7"
      : "=&v"(ye[0]), "+v"(as[1]), "=&v"(ye[1]), "=&v"(ye[2]), "+v"(as[2]), "=&v"(ye[3]) : "v"(py[2][0][0]), "v"(bias), "a"(fwo[25]), "v"(pu[8]), "v"(py[2][0][1]), "v"(py[2][0][2]), "a"(fwo[26]), "v"(py[2][0][3])); SB();
  WAITVB(4);
  SB();
  asm volatile("v_mfma_f32_16x16x32_bf16 %0, %7, %8, %0\n\tv_add_f32 %1, %9, %10\n\tv_add_f32 %2, %11, %12\n\tv_mfma_f32_16x16x32_bf16 %3, %13, %8, %3\n\tv_add_f32 %4, %14, %15\n\tv_add_f32 %5, %16, %17\n\tv_mfma_f32_16x16x32_bf16 %6, %18, %8, %6"
      : "+v"(as[0]), "=&v"(ze[0]), "=&v"(ze[1]), "+v"(as[1]), "=&v"(ze[2]), "=&v"(ze[3]), "+v"(as[2]) : "a"(fwo[27]), "v"(pu[9]), "v"(py[2][1][0]), "v"(py[2][2][0]), "v"(py[2][1][1]), "v"(py[2][2][1]), "a"(fwo[28]), "v"(py[2][1][2]), "v"(py[2][2][2]), "v"(py[2][1][3]), "v"(py[2][2][3]), "a"(fwo[29])); SB();
  WAITVB(3);
  asm volatile("ds_read_b128 %0, %4 offset:49152\n\tds_read_b128 %1, %4 offset:37888\n\tds_read_b128 %2, %4 offset:44032\n\tds_read_b128 %3, %4 offset:50176"
      : "=&a"(fwo[38]), "=&a"(fwo[39]), "=&a"(fwo[40]), "=&a"(fwo[41]) : "v"(ro) : "memory"); SB();
  WAITL(6);
  SB();
  asm volatile("v_mfma_f32_16x16x32_bf16 %0, %7, %8, %0\n\tv_add_f32 %1, %1, %9\n\tv_add_f32 %2, %2, %10\n\tv_mfma_f32_16x16x32_bf16 %3, %11, %8, %3\n\tv_add_f32 %4, %4, %12\n\tv_add_f32 %5, %5, %13\n\tv_mfma_f32_16x16x32_bf16 %6, %14, %8, %6"
      : "+v"(as[0]), "+v"(ye[0]), "+v"(ye[1]), "+v"(as[1]), "+v"(ye[2]), "+v"(ye[3]), "+v"(as[2]) : "a"(fwo[30]), "v"(pu[10]), "v"(py[2][3][0]), "v"(py[2][3][1]), "a"(fwo[31]), "v"(py[2][3][2]), "v"(py[2][3][3]), "a"(fwo[32])); SB();
  WAITL(0);
  WAITVB(0);
  SB();
  asm volatile("v_mfma_f32_16x16x32_bf16 %0, %10, %11, %0\n\tv_add_f32 %1, %1, %2\n\tv_add_f32 %3, %3, %4\n\tv_mfma_f32_16x16x32_bf16 %0, %12, %13, %0\n\tv_add_f32 %5, %5, %6\n\tv_add_f32 %7, %7, %8\n\tv_mfma_f32_16x16x32_bf16 %0, %14, %15, %0\n\tv_mul_f32 %2, 0xbfb8aa3b, %1\n\tv_mul_f32 %4, 0xbfb8aa3b, %3\n\tv_mfma_f32_16x16x32_bf16 %9, %16, %11, %9\n\tv_mul_f32 %6, 0xbfb8aa3b, %5\n\tv_mul_f32 %8, 0xbfb8aa3b, %7\n\tv_mfma_f32_16x16x32_bf16 %9, %17, %13, %9\n\tv_exp_f32 %2, %2\n\tv_mfma_f32_16x16x32_bf16 %9, %18, %15, %9\n\tv_exp_f32 %4, %4"
      : "+v"(as[0]), "+v"(ye[0]), "+v"(ze[0]), "+v"(ye[1]), "+v"(ze[1]), "+v"(ye[2]), "+v"(ze[2]), "+v"(ye[3]), "+v"(ze[3]), "+v"(as[1]) : "a"(fwo[33]), "v"(pu[11]), "a"(fwo[36]), "v"(pu[12]), "a"(fwo[39]), "v"(pu[13]), "a"(fwo[34]), "a"(fwo[37]), "a"(fwo[40])); SB();
  asm volatile("v_mfma_f32_16x16x32_bf16 %0, %5, %6, %0\n\tv_exp_f32 %1, %1\n\tv_mfma_f32_16x16x32_bf16 %0, %7, %8, %0\n\tv_exp_f32 %2, %2\n\tv_mfma_f32_16x16x32_bf16 %0, %9, %10, %0\n\tv_add_f32 %3, 1.0, %3\n\tv_add_f32 %4, 1.0, %4"
      : "+v"(as[2]), "+v"(ze[2]), "+v"(ze[3]), "+v"(ze[0]), "+v"(ze[1]) : "a"(fwo[35]), "v"(pu[11]), "a"(fwo[38]), "v"(pu[12]), "a"(fwo[41]), "v"(pu[13])); SB();
  STAMP(44);
  asm volatile("v_add_f32 %0, 1.0, %0\n\tv_add_f32 %1, 1.0, %1\n\tv_rcp_f32 %2, %2\n\tv_rcp_f32 %3, %3\n\tv_rcp_f32 %0, %0\n\tv_rcp_f32 %1, %1\n\tv_mul_f32 %4, %11, %2\n\tv_mul_f32 %5, %12, %3\n\tv_mul_f32 %6, %13, %0\n\tv_mul_f32 %7, %14, %1\n\tv_fma_f32 %8, %15, %16, %8\n\tv_fma_f32 %9, %17, %18, %9\n\tv_fma_f32 %10, %19, %20, %10"
      : "+v"(ze[2]), "+v"(ze[3]), "+v"(ze[0]), "+v"(ze[1]), "=&v"(ge[8]), "=&v"(ge[9]), "=&v"(ge[10]), "=&v"(ge[11]), "+v"(xe[0]), "+v"(xe[1]), "+v"(xe[2]) : "v"(ye[0]), "v"(ye[1]), "v"(ye[2]), "v"(ye[3]), "v"(as[0][0]), "v"(ge[0]), "v"(as[0][1]), "v"(ge[1]), "v"(as[0][2]), "v"(ge[2])); SB();
  asm volatile("v_fma_f32 %0, %9, %10, %0\n\tv_cvt_pk_bf16_f32 %1, %11, %12\n\tv_cvt_pk_bf16_f32 %2, %13, %0\n\tv_fma_f32 %3, %14, %15, %3\n\tv_fma_f32 %4, %16, %17, %4\n\tv_fma_f32 %5, %18, %19, %5\n\tv_fma_f32 %6, %20, %21, %6\n\tv_cvt_pk_bf16_f32 %7, %3, %4\n\tv_cvt_pk_bf16_f32 %8, %5, %6"
      : "+v"(xe[3]), "=&v"(ov[0][0]), "=&v"(ov[0][1]), "+v"(xe[4]), "+v"(xe[5]), "+v"(xe[6]), "+v"(xe[7]), "=&v"(ov[1][0]), "=&v"(ov[1][1]) : "v"(as[0][3]), "v"(ge[3]), "v"(xe[0]), "v"(xe[1]), "v"(xe[2]), "v"(as[1][0]), "v"(ge[4]), "v"(as[1][1]), "v"(ge[5]), "v"(as[1][2]), "v"(ge[6]), "v"(as[1][3]), "v"(ge[7])); SB();
  asm volatile("ds_write_b64 %6, %7 offset:0\n\tv_fma_f32 %0, %8, %9, %0\n\tv_fma_f32 %1, %10, %11, %1\n\tv_fma_f32 %2, %12, %13, %2\n\tv_fma_f32 %3, %14, %15, %3\n\tv_cvt_pk_bf16_f32 %4, %0, %1\n\tv_cvt_pk_bf16_f32 %5, %2, %3\n\tds_write_b64 %16, %17 offset:0"
      : "+v"(xe[8]), "+v"(xe[9]), "+v"(xe[10]), "+v"(xe[11]), "=&v"(ov[2][0]), "=&v"(ov[2][1]) : "v"(xta[0]), "v"(ov[0]), "v"(as[2][0]), "v"(ge[8]), "v"(as[2][1]), "v"(ge[9]), "v"(as[2][2]), "v"(ge[10]), "v"(as[2][3]), "v"(ge[11]), "v"(xta[1]), "v"(ov[1]) : "memory"); SB();
  asm volatile("ds_write_b64 %0, %1 offset:0"
      :  : "v"(xta[2]), "v"(ov[2]) : "memory"); SB();
  { u32x4 t0_, t1_; DSR16(t0_, xo, 0); DSR16(t1_, xo, 1024); WAITLB(1);
    BS16(vXa, rsO_, 0, t0_, ""); WAITLB(0); BS16(vXb, rsO_, 1024, t1_, ""); }
  STAMP(23);
  if (b == 8 * 28 && w == 0) { const unsigned vZ = 0u; ST4(vZ, ws, nonce); }

  STAMP(12);
  STAMP_DUMP();
  }
  }
  asm volatile(".Lcpf_e:");
}

extern "C" void kernel_launch(void* const* d_in, const int* in_sizes, int n_in,
                              void* d_out, int out_size, void* d_ws, size_t ws_size,
                              hipStream_t stream) {
  fused_kernel<<<256, 256, 0, stream>>>((const char*)d_in[0], (const char*)d_in[1], (const char*)d_in[2], (const char*)d_in[4],
                                        (const char*)d_in[5], (const char*)d_in[6], (const char*)d_in[7], (char*)d_ws, (char*)d_out);
}
```

```cpp
#include <hip/hip_runtime.h>
#include <hip/hip_bf16.h>
#include <stdint.h>

typedef short bf16x8 __attribute__((ext_vector_type(8)));
typedef short s16x4 __attribute__((ext_vector_type(4)));
typedef float f32x4 __attribute__((ext_vector_type(4)));
typedef unsigned u32x4 __attribute__((ext_vector_type(4)));
typedef unsigned u32x2 __attribute__((ext_vector_type(2)));
typedef __bf16 bf16x2_t __attribute__((ext_vector_type(2)));
typedef float f32x2_t __attribute__((ext_vector_type(2)));
#define LDS_AS __attribute__((address_space(3)))
#define DEV __device__ __forceinline__

#define STAMP(i)
#define STAMP_DUMP()

#define SB() __builtin_amdgcn_sched_barrier(0)
#define WAITV(n) asm volatile("s_waitcnt vmcnt(" #n ")" ::: "memory")
#define WAITV1(n, a) asm volatile("s_waitcnt vmcnt(" #n ")" : "+v"(a) :: "memory")
#define WAITV2(n, a, b) asm volatile("s_waitcnt vmcnt(" #n ")" : "+v"(a), "+v"(b) :: "memory")
#define WAITV3(n, a, b, c) asm volatile("s_waitcnt vmcnt(" #n ")" : "+v"(a), "+v"(b), "+v"(c) :: "memory")
#define WAITV4(n, a, b, c, d) asm volatile("s_waitcnt vmcnt(" #n ")" : "+v"(a), "+v"(b), "+v"(c), "+v"(d) :: "memory")
#define WAITA3(n, a, b, c) asm volatile("s_waitcnt vmcnt(" #n ")" : "+a"(a), "+a"(b), "+a"(c) :: "memory")
#define WAITA1(n, a) asm volatile("s_waitcnt vmcnt(" #n ")" : "+a"(a) :: "memory")
#define WAITL(n) do { asm volatile("" ::: "memory"); __builtin_amdgcn_s_waitcnt(0xc07f | ((n) << 8)); asm volatile("" ::: "memory"); } while (0)
#define BAR() do { WAITL(0); __builtin_amdgcn_s_barrier(); asm volatile("" ::: "memory"); } while (0)
#define BAR0() do { asm volatile("" ::: "memory"); __builtin_amdgcn_s_barrier(); asm volatile("" ::: "memory"); } while (0)
#define OPQ(p) asm volatile("" : "+s"(p))
#define OPV(p) asm volatile("" : "+v"(p))
#define LD16(dst, voff, sb, imm) asm volatile("global_load_dwordx4 %0, %1, %2 offset:" #imm : "=&v"(dst) : "v"(voff), "s"(sb) : "memory")
#define LD8(dst, voff, sb, imm) asm volatile("global_load_dwordx2 %0, %1, %2 offset:" #imm : "=&v"(dst) : "v"(voff), "s"(sb) : "memory")
#define LD2(dst, voff, sb, imm) asm volatile("global_load_ushort %0, %1, %2 offset:" #imm : "=&v"(dst) : "v"(voff), "s"(sb) : "memory")
#define HL16V(dst, voff, so, imm) asm volatile("buffer_load_dwordx4 %0, %1, %2, %3 offen offset:" #imm " sc1" : "=&v"(dst) : "v"(voff), "s"(rsW), "s"(so) : "memory")
#define HL16A(dst, voff, so, imm) asm volatile("buffer_load_dwordx4 %0, %1, %2, %3 offen offset:" #imm " sc1" : "=&a"(dst) : "v"(voff), "s"(rsW), "s"(so) : "memory")
#define HL4(dst, voff) asm volatile("buffer_load_dword %0, %1, %2, 0 offen sc1" : "=&v"(dst) : "v"(voff), "s"(rsW) : "memory")
#define DMA1(v0, rs, so, lds, limm, imm, bits) \
  asm volatile("s_add_u32 m0, %3, %4\n\ts_nop 0\n\tbuffer_load_dwordx4 %0, %1, %2 offen offset:" #imm " " bits " lds" :: "v"(v0), "s"(rs), "s"(so), "s"(lds), "n"(limm) : "memory", "scc")
#define DMA1S(v0, rs, so, sadd, lds, limm, imm, bits) do { unsigned t_; \
  asm volatile("s_add_u32 m0, %4, %5\n\ts_add_u32 %0, %3, %6\n\tbuffer_load_dwordx4 %1, %2, %0 offen offset:" #imm " " bits " lds" \
               : "=&s"(t_) : "v"(v0), "s"(rs), "s"(so), "s"(lds), "n"(limm), "n"(sadd) : "memory", "scc"); } while (0)
#define DMA2S(v0, v1, rs, so, sadd, lds, limm) do { unsigned t_; \
  asm volatile("s_add_u32 m0, %5, %6\n\ts_add_u32 %0, %4, %7\n\tbuffer_load_dwordx4 %1, %3, %0 offen lds\n\tbuffer_load_dwordx4 %2, %3, %0 offen offset:1024 lds" \
               : "=&s"(t_) : "v"(v0), "v"(v1), "s"(rs), "s"(so), "s"(lds), "n"(limm), "n"(sadd) : "memory", "scc"); } while (0)
#define DMA4(v0, v1, rs, so, step, lds, limm) do { unsigned t_; \
  asm volatile("s_add_u32 m0, %5, %6\n\ts_add_u32 %0, %4, %7\n\tbuffer_load_dwordx4 %1, %3, %4 offen lds\n\tbuffer_load_dwordx4 %2, %3, %4 offen offset:1024 lds\n\t" \
               "buffer_load_dwordx4 %1, %3, %0 offen offset:2048 lds\n\tbuffer_load_dwordx4 %2, %3, %0 offen offset:3072 lds" \
               : "=&s"(t_) : "v"(v0), "v"(v1), "s"(rs), "s"(so), "s"(lds), "n"(limm), "n"(step) : "memory", "scc"); } while (0)
#define SETM0(lds, limm) asm volatile("s_add_u32 m0, %0, %1" :: "s"(lds), "n"(limm) : "memory", "scc")
#define SETM0S(lds, limm, t, so, sadd) asm volatile("s_add_u32 m0, %1, %2\n\ts_add_u32 %0, %3, %4" : "=&s"(t) : "s"(lds), "n"(limm), "s"(so), "n"(sadd) : "memory", "scc")
#define SETEX(m) asm volatile("s_mov_b64 exec, %0" :: "s"(m) : "memory")
#define ALLEX() asm volatile("s_mov_b64 exec, -1" ::: "memory")
#define DMAW2(v0, rs, so) asm volatile("s_cmp_eq_u32 %3, 3\n\ts_cbranch_scc1 .LBBa%=\n\tbuffer_load_dwordx4 %0, %1, %2 offen offset:0 sc1 lds\n\tbuffer_load_dwordx4 %0, %1, %2 offen offset:1024 sc1 lds\n\ts_branch .LBBb%=\n.LBBa%=:\n\tbuffer_load_dword %0, %1, %2 offen lds\n\tbuffer_load_dword %0, %1, %2 offen lds\n.LBBb%=:" \
  :: "v"(v0), "s"(rs), "s"(so), "s"(w) : "memory", "scc")
#define DMAW4(v0, rs, so) asm volatile("s_cmp_eq_u32 %3, 3\n\ts_cbranch_scc1 .LBBa%=\n\tbuffer_load_dwordx4 %0, %1, %2 offen offset:0 sc1 lds\n\tbuffer_load_dwordx4 %0, %1, %2 offen offset:1024 sc1 lds\n\tbuffer_load_dwordx4 %0, %1, %2 offen offset:2048 sc1 lds\n\tbuffer_load_dwordx4 %0, %1, %2 offen offset:3072 sc1 lds\n\ts_branch .LBBb%=\n.LBBa%=:\n\tbuffer_load_dword %0, %1, %2 offen lds\n\tbuffer_load_dword %0, %1, %2 offen lds\n\tbuffer_load_dword %0, %1, %2 offen lds\n\tbuffer_load_dword %0, %1, %2 offen lds\n.LBBb%=:" \
  :: "v"(v0), "s"(rs), "s"(so), "s"(w) : "memory", "scc")
#define DMAI(v0, rs, so, imm, bits) asm volatile("buffer_load_dwordx4 %0, %1, %2 offen offset:" #imm " " bits " lds" :: "v"(v0), "s"(rs), "s"(so) : "memory")
#define TOUCH(v0, rs, so, sadd) do { unsigned t_; \
  asm volatile("s_mov_b32 m0, %4\n\ts_add_u32 %0, %3, %5\n\tbuffer_load_dword %1, %2, %0 offen lds" : "=&s"(t_) : "v"(v0), "s"(rs), "s"(so), "s"(lds0 + DUMP), "n"(sadd) : "memory", "scc"); } while (0)
#define TOUCH2(v0, rs) asm volatile("s_mov_b32 m0, %2\n\ts_nop 0\n\tbuffer_load_dword %0, %1, 0 offen lds" :: "v"(v0), "s"(rs), "s"(lds0 + 40448) : "memory")
#define HS16(voff, so, imm, v) do { const u32x4 v_ = (v); \
  asm volatile("buffer_store_dwordx4 %0, %1, %2, %3 offen offset:" #imm " sc1\n\ts_nop 1" :: "v"(v_), "v"(voff), "s"(rsW), "s"(so) : "memory"); } while (0)
#define HS16M(lo, hi, voff, so, imm, v) do { const u32x4 v_ = (v); \
  asm volatile("s_mov_b32 exec_lo, " #lo "\n\ts_mov_b32 exec_hi, " #hi "\n\tbuffer_store_dwordx4 %0, %1, %2, %3 offen offset:" #imm " sc1\n\ts_mov_b64 exec, -1\n\ts_nop 0" \
               :: "v"(v_), "v"(voff), "s"(rsW), "s"(so) : "memory"); } while (0)
#define HS16H(hi, voff, so, imm, v) do { const u32x4 v_ = (v); \
  asm volatile("s_mov_b32 exec_hi, " #hi "\n\tbuffer_store_dwordx4 %0, %1, %2, %3 offen offset:" #imm " sc1\n\ts_mov_b32 exec_hi, -1\n\ts_nop 0" \
               :: "v"(v_), "v"(voff), "s"(rsW), "s"(so) : "memory"); } while (0)
#define ST16(voff, sb, imm, v) do { const u32x4 v_ = (v); asm volatile("global_store_dwordx4 %0, %1, %2 offset:" #imm " sc1\n\ts_nop 1" :: "v"(voff), "v"(v_), "s"(sb) : "memory"); } while (0)
#define BS16(voff, rs, imm, v, bits) do { const u32x4 v_ = (v); asm volatile("buffer_store_dwordx4 %0, %1, %2, 0 offen offset:" #imm " " bits "\n\ts_nop 1" :: "v"(v_), "v"(voff), "s"(rs) : "memory"); } while (0)
#define ST4(voff, sb, v) do { const unsigned v_ = (v); asm volatile("global_store_dword %0, %1, %2 sc1" :: "v"(voff), "v"(v_), "s"(sb) : "memory"); } while (0)
#define FLAG16(mask, so) do { const u32x4 v_ = {nonce, nonce, nonce, nonce}; \
  asm volatile("s_mov_b64 exec, " #mask "\n\tbuffer_store_dwordx4 %0, %1, %2, %3 offen sc1\n\ts_mov_b64 exec, -1\n\ts_nop 0" :: "v"(v_), "v"(vL16), "s"(rsW), "s"(so) : "memory"); } while (0)
#define FLAG4(so) do { const unsigned v_ = nonce; \
  asm volatile("s_mov_b64 exec, 1\n\tbuffer_store_dword %0, %1, %2, %3 offen sc1\n\ts_mov_b64 exec, -1" :: "v"(v_), "v"(vL16), "s"(rsW), "s"(so) : "memory"); } while (0)
#define STFA(so) FLAG16(15, so)
#define STFQ(so) FLAG16(1, so)
#define STFU(so) FLAG16(1, so)
#define STFO(so) FLAG16(15, so)
DEV u32x4 srd(const void* p, unsigned bytes) { return (u32x4){(unsigned)(size_t)p, (unsigned)((size_t)p >> 32), bytes, 0x20000u}; }
DEV float lo2f(unsigned v) { return __uint_as_float(v << 16); }
DEV float hi2f(unsigned v) { return __uint_as_float(v & 0xffff0000u); }
DEV unsigned pack2(float a, float b) { f32x2_t v = {a, b}; return __builtin_bit_cast(unsigned, __builtin_convertvector(v, bf16x2_t)); }
DEV float rbf(float a) { return __uint_as_float(pack2(0.f, a)); }
DEV f32x2_t rbf2(f32x2_t a) { return (f32x2_t){rbf(a[0]), rbf(a[1])}; }
DEV float dot2(unsigned a, unsigned b, float c) {
  return __builtin_amdgcn_fdot2_f32_bf16(__builtin_bit_cast(bf16x2_t, a), __builtin_bit_cast(bf16x2_t, b), c, false);
}
DEV bf16x8 cat8(s16x4 a, s16x4 b) { return __builtin_shufflevector(a, b, 0, 1, 2, 3, 4, 5, 6, 7); }
DEV bf16x8 tr8u(unsigned p, int second) {
  return cat8(__builtin_amdgcn_ds_read_tr16_b64_v4i16((LDS_AS s16x4*)p), __builtin_amdgcn_ds_read_tr16_b64_v4i16((LDS_AS s16x4*)(p + second)));
}
template <int CTRL> DEV float dpp_add(float v) {
  return v + __builtin_bit_cast(float, __builtin_amdgcn_update_dpp(0, __builtin_bit_cast(int, v), CTRL, 0xf, 0xf, true));
}
DEV float red16(float v) { return dpp_add<0x140>(dpp_add<0x141>(dpp_add<0x4E>(dpp_add<0xB1>(v)))); }
DEV unsigned ror8(unsigned v) { return (unsigned)__builtin_amdgcn_update_dpp(0, (int)v, 0x128, 0xf, 0xf, true); }
DEV float fmx(float a, float b) { return __builtin_elementwise_maximum(a, b); }
DEV float xmax2(float a, float b) {
  const auto r = __builtin_amdgcn_permlane16_swap(__float_as_uint(a), __float_as_uint(b), false, false);
  const float v = fmx(__uint_as_float(r[0]), __uint_as_float(r[1]));
  const auto s = __builtin_amdgcn_permlane32_swap(__float_as_uint(v), __float_as_uint(v), false, false);
  return fmx(__uint_as_float(s[0]), __uint_as_float(s[1]));
}
DEV float xsum2(float a, float b) {
  const auto r = __builtin_amdgcn_permlane16_swap(__float_as_uint(a), __float_as_uint(b), false, false);
  const float v = __uint_as_float(r[0]) + __uint_as_float(r[1]);
  const auto s = __builtin_amdgcn_permlane32_swap(__float_as_uint(v), __float_as_uint(v), false, false);
  return __uint_as_float(s[0]) + __uint_as_float(s[1]);
}
DEV f32x2_t xbcast2(float v) {
  const auto r = __builtin_amdgcn_permlane16_swap(__float_as_uint(v), __float_as_uint(v), false, false);
  return (f32x2_t){__uint_as_float(r[0]), __uint_as_float(r[1])};
}
#define MF(d, ca, a, cb, b) asm volatile("v_mfma_f32_16x16x32_bf16 %0, %1, %2, %0" : "+v"(d) : ca(a), cb(b))
#define MFZ(d, ca, a, cb, b) asm volatile("v_mfma_f32_16x16x32_bf16 %0, %1, %2, 0" : "=&v"(d) : ca(a), cb(b))
#define MFC(d, ca, a, cb, b, c) asm volatile("v_mfma_f32_16x16x32_bf16 %0, %1, %2, %3" : "=&v"(d) : ca(a), cb(b), "v"(c))
#define VADD(d, a, b) asm volatile("v_add_f32 %0, %1, %2" : "=v"(d) : "v"(a), "v"(b))
#define VADD1(d, a) asm volatile("v_add_f32 %0, 1.0, %1" : "=v"(d) : "v"(a))
#define VMUL(d, a, b) asm volatile("v_mul_f32 %0, %1, %2" : "=v"(d) : "v"(a), "v"(b))
#define VMULK(d, k, a) asm volatile("v_mul_f32 %0, " #k ", %1" : "=v"(d) : "v"(a))
#define VFMAK(d, a, b, k) asm volatile("v_fmaak_f32 %0, %1, %2, " #k : "=v"(d) : "v"(a), "v"(b))
#define VFMA(d, a, b, c) asm volatile("v_fma_f32 %0, %1, %2, %3" : "=v"(d) : "v"(a), "v"(b), "v"(c))
#define VFNMA(d, a, b, c) asm volatile("v_fma_f32 %0, -%1, %2, %3" : "=v"(d) : "v"(a), "v"(b), "v"(c))
#define VEXP(d, a) asm volatile("v_exp_f32 %0, %1" : "=v"(d) : "v"(a))
#define VRCP(d, a) asm volatile("v_rcp_f32 %0, %1" : "=v"(d) : "v"(a))
#define VRBF(d, a) asm volatile("v_cvt_pk_bf16_f32 %0, 0, %1" : "=v"(d) : "v"(a))
#define VPK(d, a, b) asm volatile("v_cvt_pk_bf16_f32 %0, %1, %2" : "=v"(d) : "v"(a), "v"(b))
#define VLO(d, a) asm volatile("v_lshlrev_b32 %0, 16, %1" : "=v"(d) : "v"(a))
#define VHI(d, a) asm volatile("v_and_b32 %0, 0xffff0000, %1" : "=v"(d) : "v"(a))
#define VADDU(d, k) asm volatile("v_add_u32 %0, " #k ", %0" : "+v"(d))
#define VADDS(d, k) asm volatile("v_add_u32 %0, %1, %0" : "+v"(d) : "s"(k))
#define VXOR(d, a, k) asm volatile("v_xor_b32 %0, " #k ", %1" : "=v"(d) : "v"(a))
#define VSWAP16(a, b) asm volatile("v_permlane16_swap_b32 %0, %1" : "+v"(a), "+v"(b))
#define DSR16(d, addr, off) asm volatile("ds_read_b128 %0, %1 offset:" #off : "=&v"(d) : "v"(addr) : "memory")
#define DSR16A(d, addr, off) asm volatile("ds_read_b128 %0, %1 offset:" #off : "=&a"(d) : "v"(addr) : "memory")
#define DSR8(d, addr, off) asm volatile("ds_read_b64 %0, %1 offset:" #off : "=&v"(d) : "v"(addr) : "memory")
#define DSW16(addr, off, d) asm volatile("ds_write_b128 %0, %1 offset:" #off :: "v"(addr), "v"(d) : "memory")
#define DSW16A(addr, off, d) asm volatile("ds_write_b128 %0, %1 offset:" #off :: "v"(addr), "a"(d) : "memory")
#define DSW8(addr, off, d) asm volatile("ds_write_b64 %0, %1 offset:" #off :: "v"(addr), "v"(d) : "memory")
#define WAITL2(n, a, b) asm volatile("s_waitcnt lgkmcnt(" #n ")" : "+v"(a), "+v"(b) :: "memory")
#define WAITL3(n, a, b, c) asm volatile("s_waitcnt lgkmcnt(" #n ")" : "+v"(a), "+v"(b), "+v"(c) :: "memory")
#define WAITL4(n, a, b, c, d) asm volatile("s_waitcnt lgkmcnt(" #n ")" : "+v"(a), "+v"(b), "+v"(c), "+v"(d) :: "memory")
#define WAITVB(n) __builtin_amdgcn_s_waitcnt(0x0f70 | ((n) & 15) | (((n) >> 4) << 14))
#define WAITLB(n) __builtin_amdgcn_s_waitcnt(0xc07f | ((n) << 8))
#define NOP(n) asm volatile("s_nop " #n)

constexpr int kSpinCap = 20000;
template <int SLEEP> DEV void poll1(const u32x4& rsW, unsigned voff, unsigned nonce) {
#pragma unroll 1
  for (int spin = 0; spin < kSpinCap; ++spin) {
    __builtin_amdgcn_s_sleep(SLEEP);
    unsigned v;
    asm volatile("buffer_load_dword %0, %1, %2, 0 offen sc1\n\ts_waitcnt vmcnt(0)" : "=&v"(v) : "v"(voff), "s"(rsW) : "memory");
    if (__ballot(v != nonce) == 0ull) return;
  }
}

template <int SLEEP> DEV void poll1(const u32x4& rsW, unsigned voff, unsigned voff2, unsigned nonce) {
#pragma unroll 1
  for (int spin = 0; spin < kSpinCap; ++spin) {
    __builtin_amdgcn_s_sleep(SLEEP);
    unsigned v, v2;
    asm volatile("buffer_load_dword %0, %2, %4, 0 offen sc1\n\tbuffer_load_dword %1, %3, %4, 0 offen sc1\n\ts_waitcnt vmcnt(0)" : "=&v"(v), "=&v"(v2) : "v"(voff), "v"(voff2), "s"(rsW) : "memory");
    if (__ballot(((v ^ nonce) | (v2 ^ nonce)) != 0u) == 0ull) return;
  }
}
template <int SLEEP> DEV void poll0(const u32x4& rsW, unsigned voff, unsigned nonce) {
  unsigned v;
  asm volatile("buffer_load_dword %0, %1, %2, 0 offen sc1\n\ts_waitcnt vmcnt(0)" : "=&v"(v) : "v"(voff), "s"(rsW) : "memory");
  if (__ballot(v != nonce) != 0ull) poll1<SLEEP>(rsW, voff, nonce);
}

DEV float ln_inv(float var) {
  const float sd = rbf(__builtin_amdgcn_sqrtf(rbf(rbf(fmaxf(var, 0.f)) + 1e-5f)));
  return __builtin_amdgcn_rcpf(sd);
}
DEV float ln_a(const unsigned (&p)[14], f32x2_t (&d)[14], float& dm) {
  float s0 = 0.f, s1 = 0.f;
#pragma unroll
  for (int i = 0; i < 14; i += 2) { s0 = dot2(p[i], 0x3f803f80u, s0); s1 = dot2(p[i + 1], 0x3f803f80u, s1); }
  const float mean = red16(s0 + s1) * (1.0f / 448.0f);
  const float mb = rbf(mean);
  dm = mean - mb;
  f32x2_t q = {0.f, 0.f}, q1 = {0.f, 0.f};
#pragma unroll
  for (int i = 0; i < 14; ++i) d[i] = (f32x2_t){lo2f(p[i]), hi2f(p[i])} - (f32x2_t){mb, mb};
#pragma unroll
  for (int i = 0; i < 14; i += 2) { q = d[i] * d[i] + q; q1 = d[i + 1] * d[i + 1] + q1; }
  q += q1;
  return q[0] + q[1];
}
DEV void ln_b(f32x2_t (&d)[14], float q, float dm, float& inv, float& a, float& c) {
  const float var = red16(q) * (1.0f / 448.0f) - dm * dm;
  inv = ln_inv(var);
  const float inv2 = ln_inv(var * inv * inv);
  a = inv * inv2; c = -rbf(dm * inv) * inv2;
#pragma unroll
  for (int i = 0; i < 14; ++i) { d[i] = rbf2(d[i]); asm volatile("" : "+v"(d[i])); }
}
DEV void ln_h(unsigned (&p)[14], const f32x2_t (&d)[14], float a, float c) {
#pragma unroll
  for (int i = 0; i < 14; ++i) {
    const f32x2_t r = __builtin_elementwise_fma(d[i], (f32x2_t){a, a}, (f32x2_t){c, c});
    p[i] = pack2(r[0], r[1]);
  }
}
DEV void ln_x(unsigned (&p)[14], const f32x2_t (&d)[14], float inv) {
#pragma unroll
  for (int i = 0; i < 14; ++i) { const f32x2_t r = d[i] * (f32x2_t){inv, inv}; p[i] = pack2(r[0], r[1]); }
}

constexpr unsigned WS_XN = 0;
constexpr unsigned WS_HT = WS_XN + 2048 * 1024;
constexpr unsigned WS_WQ = WS_HT + 4 * 64 * 8192;
constexpr unsigned WS_UT = WS_WQ + 8 * 4 * 6 * 8192;
constexpr unsigned WS_OT = WS_UT + 28 * 8 * 8192;
constexpr unsigned WS_WO = WS_OT + 128 * 2 * 8192;
constexpr unsigned WS_FA = WS_WO + 4 * 131072;
constexpr unsigned WS_FQ = WS_FA + 1536 * 4 * 16;
constexpr unsigned WS_FU = WS_FQ + 64 * 4 * 4;
constexpr unsigned WS_FO = WS_FU + 1024 * 4 * 4;
constexpr unsigned WS_END = WS_FO + 512 * 4 * 16;
constexpr int SCW = 10240;
constexpr int RB = 40960, RW = 30720;
constexpr int EW = 28672;
constexpr int WOB = 55296;
constexpr int S3B = 112640, TB = 131072;
constexpr int TP = 912;
constexpr int SK = 18432, SQ = 36864 - 512;
constexpr int DUMP = 130816;
constexpr int LDS_BYTES = 163840;

__global__ __launch_bounds__(256, 1) void fused_kernel(const char* __restrict__ x, const char* __restrict__ wqkv, const char* __restrict__ wo,
                                                       const char* __restrict__ w1, const char* __restrict__ b1,
                                                       const char* __restrict__ w2, const char* __restrict__ b2,
                                                       char* __restrict__ ws, char* __restrict__ out) {
  __shared__ __attribute__((aligned(16))) char smem[LDS_BYTES];
  const int lane = threadIdx.x & 63;
  const int w = __builtin_amdgcn_readfirstlane(threadIdx.x >> 6);
  const int b = blockIdx.x, xq = (b & 7) >> 1, jb = b >> 3;
  STAMP(0);
  { const char* pc_; unsigned vo_ = (threadIdx.x * 4 + (jb & 3)) * 128;
    asm volatile("s_getpc_b64 %0\n.Lcpf_b:\n\ts_mov_b32 m0, %2\n\tv_min_u32 %1, .Lcpf_e-.Lcpf_b-4, %1\n\tglobal_load_lds_dword %1, %0"
                 : "=&s"(pc_), "+v"(vo_) : "s"((unsigned)(size_t)(LDS_AS char*)smem + 40704) : "memory"); }
  const unsigned lds0 = (unsigned)(size_t)(LDS_AS char*)smem;
  const int n16 = lane & 15, g4 = lane >> 4, q4 = n16 >> 2, pq = lane & 3, r8 = lane >> 3;
  const unsigned vL16 = lane * 16;
  const unsigned wsw = ((lane & 7) ^ (r8 & 2)) * 16;
  const int th = xq >> 1, hq = xq & 1, pr_ = 64 * (1 - th) + 32 * hq + jb;
  unsigned vX = (4 * w + g4) * 896 + n16 * 16, vX3 = (4 * w + g4) * 896 + 768 + (n16 & 7) * 16 + (n16 >> 3) * 8, oX = pr_ * (16 * 896);
  asm volatile("" : "+v"(vX), "+v"(vX3)); asm volatile("" : "+s"(oX));
  u32x4 xa, xb, xc; u32x2 xd;
  const int d4 = (jb * 37) >> 8, fi = jb - 7 * d4, ft = 7 * xq + fi;
  const int rota = 4 * d4 + 2 * fi, rotg = 0 * fi;
#define KS1(c) ((((c) + rotg) & 0) + (((c) + rota) & 15) + 16 * w)
  const unsigned ldsR = lds0 + RB + w * RW;
  unsigned vW1a = r8 * 3584 + wsw + ft * 128, vW1b = (vW1a ^ 64) + (8 * 3584 - 1024);
  unsigned sp[3];
#pragma unroll
  for (int c = 0; c < 3; ++c) { sp[c] = KS1(c) * 114688; OPQ(sp[c]); }
  asm volatile("" : "+v"(vW1a), "+v"(vW1b) : "s"(ldsR));
  asm volatile("" :: "s"(x), "s"(wqkv), "s"(wo), "s"(w1), "s"(b1), "s"(w2), "s"(b2), "s"(ws), "s"(out));
  int cls = b & 1;
  if (cls) {
  asm volatile("; ROLE C");
  const char* sX = x + oX;
  LD16(xa, vX, sX, 0); LD16(xb, vX, sX, 256); LD16(xc, vX, sX, 512); LD8(xd, vX3, sX, 0);
  } else if (jb >= 28) {
    asm volatile("; ROLE B");
    const int b7 = (int)((size_t)w1 >> 7) + 7 * xq, f_ = ((3 - b7) & 3) + 4 * (jb & 1);
    const unsigned vT = (512 * w + 32 * ((4 * g4 + 2 * f_ + 2 + ((jb >> 1) & 1)) & 15) + 2 * n16 + (((b7 + f_) >> 2) & 1)) * 3584 + (7 * xq + f_) * 128;
    u32x4 rs1 = srd(w1, f_ < 7 ? 2048u * 3584u : 0u);
    TOUCH(vT, rs1, 0u, 0);
  } else {
  asm volatile("; ROLE B");
  u32x4 rs1 = srd(w1, 2048u * 3584u);
  DMA4(vW1a, vW1b, rs1, sp[0], 55296, ldsR, 0);
  DMA4(vW1a, vW1b, rs1, sp[1], 55296, ldsR, 4096);
  DMA4(vW1a, vW1b, rs1, sp[2], 55296, ldsR, 8192);
  }
  OPQ(cls);
  if (cls) {
  asm volatile("; NEED C");
  SB();
  const int kg = 32 * (1 - th) + jb, hpq = 4 * (1 - hq) + w;
  const unsigned ldsQ = lds0 + 16384 + w * 3072;
  unsigned vQ_ = r8 * 2688 + ((lane & 7) ^ ((r8 & 2) << 1)) * 16, sQ_ = kg * (8 * 2688) + hpq * 112;
  { u32x4 rsQ = srd(wqkv, 448u * 2688u);
    DMA1(vQ_, rsQ, sQ_, ldsQ, 0, 0, "");
    DMA1(vQ_, rsQ, sQ_, ldsQ, 128, 896, "");
    DMA1(vQ_, rsQ, sQ_, ldsQ, 256, 1792, "");
  }
  unsigned nonce; asm volatile("s_load_dword %0, %1, 0x0" : "=&s"(nonce) : "s"(ws) : "memory");
  const int tt = 8 * th + (jb >> 2), hp = 4 * hq + (jb & 3);
  const int st = 16 * (1 - th) + 8 * hq + (jb & 7), dt = jb >> 3, db0 = 4 * dt;
#define BLK(j) (db0 + ((j) ^ 0))
  u32x4 rsW = srd((char*)(((size_t)ws + 8191) & ~(size_t)4095) + 512, WS_END); OPQ(rsW);
  const unsigned trb = (8 * g4 + q4) * 128 + pq * 8, trf = ((q4 >> 1) | ((g4 & 1) << 1)) * 32;
  const unsigned vF = r8 * 1024 + (lane & 7) * 16;
  unsigned vN = (4 * w + g4) * 1024 + n16 * 16, sN = WS_XN + pr_ * 16384;
  unsigned hw = lds0 + (4 * w + g4) * TP + n16 * 16, hw3 = lds0 + (4 * w + g4) * TP + 768 + (n16 & 7) * 16 + (n16 >> 3) * 8;
  unsigned th_ = lds0 + q4 * TP + 224 * w + 64 * g4 + 32 * (pq >> 1) + 8 * (pq & 1), sH = WS_HT + w * 524288 + pr_ * 4096, sFA = WS_FA + (w * 128 + pr_) * (4 * 16);
  unsigned tq = ldsQ + q4 * 128 + ((g4 ^ (q4 & 2)) * 32) + pq * 8, vq = (r8 >> 1) * 49152 + (r8 & 1) * 1024 + (lane & 7) * 16, sq0, sq1, sq2;
  { const int ks = kg >> 2, k7 = ks >= 7; sq0 = WS_WQ + hpq * 196608 + k7 * (8192 - 896) + ks * 128 + (kg & 3) * 2048 + (((55 - kg) >> 31) & 0x80000000u); sq1 = sq0 + 16384; sq2 = sq0 + 32768; }
  asm volatile("" : "+v"(vN), "+v"(hw), "+v"(hw3), "+v"(th_), "+v"(tq), "+v"(vq)); asm volatile("" : "+s"(sN), "+s"(sH), "+s"(sFA), "+s"(sq0), "+s"(sq1), "+s"(sq2));
  WAITV4(3, xa, xb, xc, xd);
  STAMP(1);
  unsigned pr[14] = {xa[0], xa[1], xa[2], xa[3], xb[0], xb[1], xb[2], xb[3], xc[0], xc[1], xc[2], xc[3], xd[0], xd[1]};
  f32x2_t dd[14]; float dm, inv, la, lc;
  float qs_ = ln_a(pr, dd, dm); OPV(qs_);
  SB();
  SB();
  ln_b(dd, qs_, dm, inv, la, lc); OPV(la);
  SB();
  SB();
  ln_h(pr, dd, la, lc);
  *(LDS_AS u32x4*)hw = (u32x4){pr[0], pr[1], pr[2], pr[3]}; *(LDS_AS u32x4*)(hw + 256) = (u32x4){pr[4], pr[5], pr[6], pr[7]};
  *(LDS_AS u32x4*)(hw + 512) = (u32x4){pr[8], pr[9], pr[10], pr[11]}; *(LDS_AS u32x2*)hw3 = (u32x2){pr[12], pr[13]};
  SB();
  ln_x(pr, dd, inv);
  HS16(vN, sN, 0, ((u32x4){pr[0], pr[1], pr[2], pr[3]}));
  HS16(vN, sN, 256, ((u32x4){pr[4], pr[5], pr[6], pr[7]}));
  HS16(vN, sN, 512, ((u32x4){pr[8], pr[9], pr[10], pr[11]}));
  HS16M(0x00ff00ff, 0x00ff00ff, vN, sN, 768, ((u32x4){pr[12], pr[13], ror8(pr[12]), ror8(pr[13])}));
  asm volatile("s_waitcnt lgkmcnt(0)" : "+s"(nonce) :: "memory"); nonce += 1u;
  asm volatile("s_waitcnt vmcnt(4)"
      :  :  : "memory"); SB();
  const bf16x8 q0 = tr8u(tq, 512), q1 = tr8u(tq + 1024, 512), q2 = tr8u(tq + 2048, 512);
  BAR();
  STAMP(2);
  { const bf16x8 h0 = tr8u(th_, 4 * TP), h1 = tr8u(th_ + 16, 4 * TP), h2 = tr8u(th_ + 8 * TP, 4 * TP), h3 = tr8u(th_ + 8 * TP + 16, 4 * TP);
    HS16H(0xffffff, vL16, sH, 0, __builtin_bit_cast(u32x4, h0));
    HS16H(0xffffff, vL16, sH, 1024, __builtin_bit_cast(u32x4, h1));
    HS16H(0xffffff, vL16, sH, 2048, __builtin_bit_cast(u32x4, h2));
    HS16H(0xffffff, vL16, sH, 3072, __builtin_bit_cast(u32x4, h3));
  }
  HS16H(0xffffff, vq, sq0, 0, __builtin_bit_cast(u32x4, q0));
  HS16H(0xffffff, vq, sq1, 0, __builtin_bit_cast(u32x4, q1));
  HS16H(0xffffff, vq, sq2, 0, __builtin_bit_cast(u32x4, q2));
  STAMP(3);
  unsigned fw, fa, vFW, vFA;
  { const int k_ = lane < 56 ? lane : 55; vFW = WS_FA + 0 + ((hp & 3) * 128 + 64 * (k_ >> 5) + 32 * (1 - hq) + (k_ & 31)) * (4 * 16); }
  { const int k_ = 8 * tt - 1 + (n16 < 9 ? n16 : 8); vFA = WS_FA + 0 + (g4 * 128 + (k_ < 0 ? 0 : k_)) * (4 * 16); }
  unsigned vQ = w == 3 && (r8 & 1) ? 0x40000000u : vF      , sq[6];
#pragma unroll
  for (int t = 0; t < 6; ++t) { sq[t] = WS_WQ + ((hp * 4 + w) * 6 + t) * 8192; OPQ(sq[t]); }
  unsigned vXN = r8 * 1024 + ((lane & 7) ^ (4 * (w & 1) + (r8 >> 1))) * 16, sXa = WS_XN + (128 * tt - 16 + 8 * w) * 1024, sXb = sXa + (w >= 2 ? 128 - 147456 : 0);
  unsigned sXc = w >= 2 ? 0x80000000u : sXa, ldsX = lds0 + w * 1024, sXt;
  unsigned xr0 = lds0 + n16 * 128 + ((g4 ^ (n16 >> 1)) * 16), xr1 = xr0 ^ 64;
  unsigned sk0, sk1, sv, rk0, rk1, rv0, rv1, vS, sO, sFO = WS_FO + ((tt * 4 + w) * 8 + hp) * (4 * 16);
  { const int c = 16 * w + 4 * g4;
    const bool ok = c < 56;
    const int hd = ok ? (c >= 28) : (g4 & 1), d = ok ? c - 28 * hd : 28, row = (n16 >> 1) + 72 * (n16 & 1);
    sk0 = lds0 + hd * 9216 + row * 64 + (((d >> 3) ^ (n16 >> 3) ^ (2 * (n16 & 1))) << 4) + ((d >> 2) & 1) * 8; sk1 = sk0 ^ 32;
    sv = lds0 + (2 * hd + (d >> 4)) * 4608 + row * 32 + ((((d >> 2) & 3) ^ (n16 >> 3) ^ (2 * (n16 & 1))) << 3);
    rk0 = lds0 + (16 * w + n16) * 64 + ((g4 ^ q4) << 4); rk1 = (rk0 ^ 32) + 4608;
    rv0 = lds0 + (16 * w + 4 * g4 + q4) * 32 + (pq ^ (g4 & 1)) * 8; rv1 = (rv0 ^ 16) + 2304;
    const int ga = 7 * (hp & 3) + g4;
    vS = (n16 >> 3) * 16384 + (ga & 3) * 2048 + (ga >> 2) * 128 + (n16 & 7) * 16; sO = WS_OT + (8 * tt + 2 * w) * 16384 + (hp >> 2) * 8192; }
  bf16x8 wq[14][3];
  asm volatile("" : "+v"(vFW), "+v"(vFA), "+v"(vQ), "+v"(vXN), "+v"(xr0), "+v"(xr1)); asm volatile("" : "+s"(sXa), "+s"(sXb), "+s"(sXc), "+s"(ldsX));
  asm volatile("" : "+v"(sk0), "+v"(sk1), "+v"(sv), "+v"(rk0), "+v"(rk1), "+v"(rv0), "+v"(rv1), "+v"(vS)); asm volatile("" : "+s"(sO), "+s"(sFO));
  u32x4 rs2 = srd(w2, 1792u * 4096u); OPQ(rs2);
  unsigned vW2[4];
#pragma unroll
  for (int i = 0; i < 4; ++i) vW2[i] = (8 * (r8 & 3) + 4 * (r8 >> 2)) * 4096 + st * 128 + ((lane & 7) ^ (2 * i)) * 16 + i * (4096 - 1024);
  int rot2 = 4 * dt + 2 * (st & 7) + 0 * (b & 1); rot2 -= 14 * ((rot2 * 37) >> 9);
  bf16x8 fw2[14][4];
  unsigned ldsS = lds0 + S3B + w * 4096, ldsO = lds0 + WOB + w * 8192, ldsP = lds0 + WOB + 24576 + w * 6144, sWo, vWo = r8 * 16384 + (lane & 7) * 16, sWt;
  { const int k_ = BLK(w), k7 = (k_ * 37) >> 8; sWo = WS_WO + k7 * (131072 - 896) + k_ * 128; }
  unsigned fq, vFQ = WS_FQ + (lane < 56 ? lane : 55) * (4 * 4);
  asm volatile("" : "+v"(vW2[0]), "+v"(vW2[1]), "+v"(vW2[2]), "+v"(vW2[3]), "+v"(vFQ), "+v"(vWo)); asm volatile("" : "+s"(rot2), "+s"(ldsS), "+s"(ldsO), "+s"(ldsP), "+s"(sWo));
  unsigned tS[4];
#pragma unroll
  for (int i = 0; i < 4; ++i) { tS[i] = ldsS + q4 * 1024 + g4 * 128 + pq * 8 + ((i ^ q4) * 32); OPV(tS[i]); }
  unsigned tO[4];
#pragma unroll
  for (int i = 0; i < 4; ++i) { tO[i] = ldsO + q4 * 1024 + g4 * 128 + pq * 8 + ((i ^ q4) * 32); OPV(tO[i]); }
  unsigned su[14], sw[14], sc_ = 14 * w + rot2; OPQ(sc_);
  unsigned ldsZ = lds0 + w * 4096; OPQ(ldsZ);
  unsigned tZ[4];
#pragma unroll
  for (int i = 0; i < 4; ++i) { tZ[i] = ldsZ + q4 * 1024 + g4 * 128 + pq * 8 + ((i ^ q4) * 32); OPV(tZ[i]); }
  const int dk = 4 * g4 - n16;
  f32x4 cm; unsigned m1[4], mp[2], mq_ = g4 == 3 ? 0u : ~0u;
  const float cz = tt + w == 0 && lane < 32 ? -998244352.f : 0.f;
#pragma unroll
  for (int i = 0; i < 4; ++i) { m1[i] = (unsigned)((dk + i + 7) >> 31); cm[i] = __uint_as_float(~m1[i] & __float_as_uint((unsigned)(dk + i - 1) < 8u ? cz : -__builtin_inff())); }
  mp[0] = (m1[0] & 0xffffu) | (m1[1] & 0xffff0000u); mp[1] = (m1[2] & 0xffffu) | (m1[3] & 0xffff0000u);
  asm volatile("" : "+v"(cm), "+v"(m1[0]), "+v"(m1[1]), "+v"(m1[2]), "+v"(m1[3]), "+v"(mp[0]), "+v"(mp[1]), "+v"(mq_));
  asm volatile("s_waitcnt vmcnt(0)"
      :  :  : "memory"); SB();
  STFA(sFA + 0);
  STAMP(4);
  { const unsigned vT = lane < 14 ? ((jb * 4 + w) * 14 + lane) * 4096 + ((st & ~7) + ((3 - (int)((size_t)w2 >> 7)) & 7)) * 128 : 0x40000000u; TOUCH(vT, rs2, 0u, 0); }
  unsigned vFU, vFO;
  { const int da = (db0 * 37) >> 8, db = ((db0 + 3) * 37) >> 8, l_ = lane < 56 ? lane : 55;
    vFU = WS_FU + ((l_ < 28 ? da * 128 + l_ : db * 128 + l_ - 28) + w * 28) * (4 * 4);
    vFO = WS_FO + ((2 * st + (w >> 1)) * 8 + (lane & 7)) * (4 * 16);
    asm volatile("" : "+v"(vFU), "+v"(vFO)); }
  unsigned vFU1 = WS_FU + (512 + ((db0 * 37) >> 8) * 128 + w * 28 + (lane < 28 ? lane : 27)) * (4 * 4), fu2; OPV(vFU1);
  unsigned vFj[4], so0 = WS_OT + (4 * st + w) * 16384, so1 = so0 + 8192;
#pragma unroll
  for (int j = 0; j < 4; ++j) { const int k_ = BLK(j), k7 = (k_ * 37) >> 8; vFj[j] = vF + (WS_UT + k7 * (56 * 8192 - 896) + k_ * 128); OPV(vFj[j]); }
  asm volatile("" : "+s"(so0), "+s"(so1));
  const int tau = 2 * (n16 & 7) + (n16 >> 3);
  unsigned rwd = ldsS + vL16, rr = lds0 + S3B + w * 1024 + (16 * g4 + tau) * 16, ro = lds0 + WOB + vL16;
  unsigned xo = ldsS + 34816 + vL16, braw, xt = ldsS + 34816 + tau * 128 + (((g4 >> 1) ^ (n16 & 7)) * 16) + (g4 & 1) * 8, vB2 = (64 * st + 16 * w + tau) * 2;
  unsigned xs = xt ^ (32 * 0);
  asm volatile("" : "+v"(rwd), "+v"(rr), "+v"(ro), "+v"(xt), "+v"(xs), "+v"(xo), "+v"(vB2));
  unsigned vXa = (64 * st + 16 * w + r8) * 896 + db0 * 32 + ((lane & 7) ^ (r8 >> 1)) * 16, vXb = (vXa ^ 64) + (8 * 896 - 1024); OPV(vXa); OPV(vXb);
  { unsigned k_ = sc_ + (rot2 >= 14 ? -14 : 0); OPQ(k_); su[0] = k_ << 13; sw[0] = k_ << 17; OPQ(su[0]); OPQ(sw[0]); }
  { unsigned k_ = sc_ + (rot2 >= 13 ? -13 : 1); OPQ(k_); su[1] = k_ << 13; sw[1] = k_ << 17; OPQ(su[1]); OPQ(sw[1]); }
  { unsigned k_ = sc_ + (rot2 >= 12 ? -12 : 2); OPQ(k_); su[2] = k_ << 13; sw[2] = k_ << 17; OPQ(su[2]); OPQ(sw[2]); }
  { unsigned k_ = sc_ + (rot2 >= 11 ? -11 : 3); OPQ(k_); su[3] = k_ << 13; sw[3] = k_ << 17; OPQ(su[3]); OPQ(sw[3]); }
  { unsigned k_ = sc_ + (rot2 >= 10 ? -10 : 4); OPQ(k_); su[4] = k_ << 13; sw[4] = k_ << 17; OPQ(su[4]); OPQ(sw[4]); }
  { unsigned k_ = sc_ + (rot2 >= 9 ? -9 : 5); OPQ(k_); su[5] = k_ << 13; sw[5] = k_ << 17; OPQ(su[5]); OPQ(sw[5]); }
  { unsigned k_ = sc_ + (rot2 >= 8 ? -8 : 6); OPQ(k_); su[6] = k_ << 13; sw[6] = k_ << 17; OPQ(su[6]); OPQ(sw[6]); }
  { unsigned k_ = sc_ + (rot2 >= 7 ? -7 : 7); OPQ(k_); su[7] = k_ << 13; sw[7] = k_ << 17; OPQ(su[7]); OPQ(sw[7]); }
  { unsigned k_ = sc_ + (rot2 >= 6 ? -6 : 8); OPQ(k_); su[8] = k_ << 13; sw[8] = k_ << 17; OPQ(su[8]); OPQ(sw[8]); }
  { unsigned k_ = sc_ + (rot2 >= 5 ? -5 : 9); OPQ(k_); su[9] = k_ << 13; sw[9] = k_ << 17; OPQ(su[9]); OPQ(sw[9]); }
  { unsigned k_ = sc_ + (rot2 >= 4 ? -4 : 10); OPQ(k_); su[10] = k_ << 13; sw[10] = k_ << 17; OPQ(su[10]); OPQ(sw[10]); }
  { unsigned k_ = sc_ + (rot2 >= 3 ? -3 : 11); OPQ(k_); su[11] = k_ << 13; sw[11] = k_ << 17; OPQ(su[11]); OPQ(sw[11]); }
  { unsigned k_ = sc_ + (rot2 >= 2 ? -2 : 12); OPQ(k_); su[12] = k_ << 13; sw[12] = k_ << 17; OPQ(su[12]); OPQ(sw[12]); }
  { unsigned k_ = sc_ + (rot2 >= 1 ? -1 : 13); OPQ(k_); su[13] = k_ << 13; sw[13] = k_ << 17; OPQ(su[13]); OPQ(sw[13]); }
  unsigned sWq[4] = {sWo, sWo + 4096, sWo + 8192, sWo + 12288}, wwa = ldsO + vL16, wwb = ldsP + vL16; OPQ(sWq[1]); OPQ(sWq[2]); OPQ(sWq[3]); OPV(wwa); OPV(wwb);
  unsigned vT2 = (lane & 31) * 4096 + st * 128 + (lane < 32 ? sw[2] : sw[3]); OPV(vT2);
  u32x2 xv[4]; unsigned xta[4];
  u32x4 rsX = srd(x, 2048u * 896u); OPQ(rsX);
  asm volatile("s_add_u32 m0, %1, 34816\n\ts_nop 0\n\tbuffer_load_dwordx4 %2, %3, %4 offen offset:0  lds\n\tbuffer_load_dwordx4 %5, %3, %4 offen offset:1024  lds\n\tglobal_load_ushort %0, %6, %7 offset:0"
      : "=&v"(braw) : "s"(ldsS), "v"(vXa), "s"(rsX), "s"(0u), "v"(vXb), "v"(vB2), "s"(b2) : "memory", "scc"); SB();
  STAMP(5);
  __builtin_amdgcn_s_sleep(2);
  __builtin_amdgcn_s_sleep(2);
  __builtin_amdgcn_s_sleep(2);
  __builtin_amdgcn_s_sleep(2);
  asm volatile("buffer_load_dword %0, %2, %3, 0 offen sc1\n\tbuffer_load_dword %1, %4, %3, 0 offen sc1"
      : "=&v"(fw), "=&v"(fa) : "v"(vFW), "s"(rsW), "v"(vFA) : "memory"); SB();
  BAR();
  WAITV2(0, fw, fa);
  fa = (fa ^ nonce) | (fw ^ nonce) ? ~nonce : nonce; if (__ballot(fa != nonce) != 0ull) poll1<1>(rsW, vFA, vFW, nonce);
  STAMP(6);
  asm volatile("s_nop 0\n\ts_add_u32 m0, %1, 0\n\ts_add_u32 %0, %2, 0\n\tbuffer_load_dwordx4 %3, %4, %0 offen offset:0 sc1 lds\n\ts_add_u32 m0, %1, 4096\n\ts_add_u32 %0, %2, 32768\n\tbuffer_load_dwordx4 %3, %4, %0 offen offset:0 sc1 lds\n\ts_add_u32 m0, %1, 8192\n\ts_add_u32 %0, %2, 65536\n\tbuffer_load_dwordx4 %3, %4, %0 offen offset:0 sc1 lds\n\ts_add_u32 m0, %1, 12288\n\ts_add_u32 %0, %2, 98304\n\tbuffer_load_dwordx4 %3, %4, %0 offen offset:0 sc1 lds\n\ts_add_u32 m0, %1, 16384\n\ts_add_u32 %0, %5, 131072\n\tbuffer_load_dwordx4 %3, %4, %0 offen offset:0 sc1 lds"
      : "=&s"(sXt) : "s"(ldsX), "s"(sXa), "v"(vXN), "s"(rsW), "s"(sXb) : "memory", "scc"); SB();
  STAMP(7);
  asm volatile("buffer_load_dwordx4 %0, %10, %11, %12 offen offset:0 sc1\n\tbuffer_load_dwordx4 %1, %10, %11, %13 offen offset:0 sc1\n\tbuffer_load_dwordx4 %2, %10, %11, %14 offen offset:0 sc1\n\tbuffer_load_dwordx4 %3, %10, %11, %12 offen offset:128 sc1\n\tbuffer_load_dwordx4 %4, %10, %11, %13 offen offset:128 sc1\n\tbuffer_load_dwordx4 %5, %10, %11, %14 offen offset:128 sc1\n\ts_add_u32 m0, %15, 20480\n\ts_add_u32 %6, %16, 16512\n\tbuffer_load_dwordx4 %17, %11, %6 offen offset:0 sc1 lds\n\ts_add_u32 m0, %15, 24576\n\ts_add_u32 %6, %16, 49280\n\tbuffer_load_dwordx4 %17, %11, %6 offen offset:0 sc1 lds\n\ts_add_u32 m0, %15, 28672\n\ts_add_u32 %6, %16, 82048\n\tbuffer_load_dwordx4 %17, %11, %6 offen offset:0 sc1 lds\n\ts_add_u32 m0, %15, 32768\n\ts_add_u32 %6, %16, 114816\n\tbuffer_load_dwordx4 %17, %11, %6 offen offset:0 sc1 lds\n\tbuffer_load_dwordx4 %7, %10, %11, %12 offen offset:256 sc1\n\tbuffer_load_dwordx4 %8, %10, %11, %13 offen offset:256 sc1\n\tbuffer_load_dwordx4 %9, %10, %11, %14 offen offset:256 sc1"
      : "=&a"(wq[0][0]), "=&a"(wq[0][1]), "=&a"(wq[0][2]), "=&a"(wq[1][0]), "=&a"(wq[1][1]), "=&a"(wq[1][2]), "=&s"(sXt), "=&a"(wq[2][0]), "=&a"(wq[2][1]), "=&a"(wq[2][2]) : "v"(vQ), "s"(rsW), "s"(sq[0]), "s"(sq[2]), "s"(sq[4]), "s"(ldsX), "s"(sXa), "v"(vXN) : "memory", "scc"); SB();
  f32x4 aq[8], ak[9], av[9]; bf16x8 xf[14][9]; u32x2 sg[27];
  asm volatile("s_waitcnt vmcnt(13)"
      :  :  : "memory"); SB();
  BAR0();
  STAMP(8);
  asm volatile("ds_read_b128 %0, %9 offset:0\n\tds_read_b128 %1, %9 offset:2048\n\tds_read_b128 %2, %9 offset:4096\n\tds_read_b128 %3, %9 offset:6144\n\tds_read_b128 %4, %9 offset:8192\n\tds_read_b128 %5, %9 offset:10240\n\tds_read_b128 %6, %9 offset:12288\n\tds_read_b128 %7, %9 offset:14336\n\tds_read_b128 %8, %9 offset:16384"
      : "=&v"(xf[0][0]), "=&v"(xf[0][1]), "=&v"(xf[0][2]), "=&v"(xf[0][3]), "=&v"(xf[0][4]), "=&v"(xf[0][5]), "=&v"(xf[0][6]), "=&v"(xf[0][7]), "=&v"(xf[0][8]) : "v"(xr0) : "memory"); SB();
  WAITVB(10);
  WAITL(8);
  SB();
  asm volatile("v_mfma_f32_16x16x32_bf16 %0, %4, %5, 0\n\ts_add_u32 m0, %6, 36864\n\ts_add_u32 %1, %7, 256\n\tv_mfma_f32_16x16x32_bf16 %2, %8, %5, 0\n\tds_read_b128 %3, %9 offset:0"
      : "=&v"(ak[0]), "=&s"(sXt), "=&v"(av[0]), "=&v"(xf[1][0]) : "a"(wq[0][1]), "v"(xf[0][0]), "s"(ldsX), "s"(sXa), "a"(wq[0][2]), "v"(xr1) : "memory", "scc"); SB();
  WAITL(8);
  SB();
  asm volatile("v_mfma_f32_16x16x32_bf16 %0, %4, %5, 0\n\tbuffer_load_dwordx4 %6, %7, %8 offen offset:0 sc1 lds\n\tv_mfma_f32_16x16x32_bf16 %1, %9, %5, 0\n\tds_read_b128 %2, %10 offset:2048\n\tv_mfma_f32_16x16x32_bf16 %3, %11, %5, 0"
      : "=&v"(ak[1]), "=&v"(av[1]), "=&v"(xf[1][1]), "=&v"(aq[0]) : "a"(wq[0][1]), "v"(xf[0][1]), "v"(vXN), "s"(rsW), "s"(sXt), "a"(wq[0][2]), "v"(xr1), "a"(wq[0][0]) : "memory"); SB();
  WAITL(8);
  SB();
  asm volatile("v_mfma_f32_16x16x32_bf16 %0, %6, %7, 0\n\tds_read_b128 %1, %8 offset:4096\n\tv_mfma_f32_16x16x32_bf16 %2, %9, %7, 0\n\tbuffer_load_dwordx4 %3, %10, %11, %12 offen offset:384 sc1\n\tv_mfma_f32_16x16x32_bf16 %4, %13, %7, 0\n\tds_read_b128 %5, %8 offset:6144"
      : "=&v"(ak[2]), "=&v"(xf[1][2]), "=&v"(av[2]), "=&a"(wq[3][0]), "=&v"(aq[1]), "=&v"(xf[1][3]) : "a"(wq[0][1]), "v"(xf[0][2]), "v"(xr1), "a"(wq[0][2]), "v"(vQ), "s"(rsW), "s"(sq[0]), "a"(wq[0][0]) : "memory"); SB();
  WAITL(9);
  SB();
  asm volatile("v_mfma_f32_16x16x32_bf16 %0, %5, %6, 0\n\ts_add_u32 m0, %7, 40960\n\ts_add_u32 %1, %8, 33024\n\tv_mfma_f32_16x16x32_bf16 %2, %9, %6, 0\n\tds_read_b128 %3, %10 offset:8192\n\tv_mfma_f32_16x16x32_bf16 %4, %11, %6, 0"
      : "=&v"(ak[3]), "=&s"(sXt), "=&v"(av[3]), "=&v"(xf[1][4]), "=&v"(aq[2]) : "a"(wq[0][1]), "v"(xf[0][3]), "s"(ldsX), "s"(sXa), "a"(wq[0][2]), "v"(xr1), "a"(wq[0][0]) : "memory", "scc"); SB();
  WAITL(9);
  SB();
  asm volatile("v_mfma_f32_16x16x32_bf16 %0, %5, %6, 0\n\tds_read_b128 %1, %7 offset:10240\n\tv_mfma_f32_16x16x32_bf16 %2, %8, %6, 0\n\tbuffer_load_dwordx4 %9, %10, %11 offen offset:0 sc1 lds\n\tv_mfma_f32_16x16x32_bf16 %3, %12, %6, 0\n\tds_read_b128 %4, %7 offset:12288"
      : "=&v"(ak[4]), "=&v"(xf[1][5]), "=&v"(av[4]), "=&v"(aq[3]), "=&v"(xf[1][6]) : "a"(wq[0][1]), "v"(xf[0][4]), "v"(xr1), "a"(wq[0][2]), "v"(vXN), "s"(rsW), "s"(sXt), "a"(wq[0][0]) : "memory"); SB();
  WAITL(10);
  SB();
  asm volatile("v_mfma_f32_16x16x32_bf16 %0, %5, %6, 0\n\tbuffer_load_dwordx4 %1, %7, %8, %9 offen offset:384 sc1\n\tv_mfma_f32_16x16x32_bf16 %2, %10, %6, 0\n\tds_read_b128 %3, %11 offset:14336\n\tv_mfma_f32_16x16x32_bf16 %4, %12, %6, 0"
      : "=&v"(ak[5]), "=&a"(wq[3][1]), "=&v"(av[5]), "=&v"(xf[1][7]), "=&v"(aq[4]) : "a"(wq[0][1]), "v"(xf[0][5]), "v"(vQ), "s"(rsW), "s"(sq[2]), "a"(wq[0][2]), "v"(xr1), "a"(wq[0][0]) : "memory"); SB();
  WAITL(10);
  SB();
  asm volatile("v_mfma_f32_16x16x32_bf16 %0, %5, %6, 0\n\tds_read_b128 %1, %7 offset:16384\n\tv_mfma_f32_16x16x32_bf16 %2, %8, %6, 0\n\ts_add_u32 m0, %9, 45056\n\ts_add_u32 %3, %10, 65792\n\tv_mfma_f32_16x16x32_bf16 %4, %11, %6, 0"
      : "=&v"(ak[6]), "=&v"(xf[1][8]), "=&v"(av[6]), "=&s"(sXt), "=&v"(aq[5]) : "a"(wq[0][1]), "v"(xf[0][6]), "v"(xr1), "a"(wq[0][2]), "s"(ldsX), "s"(sXa), "a"(wq[0][0]) : "memory", "scc"); SB();
  WAITL(10);
  SB();
  asm volatile("v_mfma_f32_16x16x32_bf16 %0, %4, %5, 0\n\tbuffer_load_dwordx4 %6, %7, %8 offen offset:0 sc1 lds\n\tv_mfma_f32_16x16x32_bf16 %1, %9, %5, 0\n\tbuffer_load_dwordx4 %2, %10, %7, %11 offen offset:384 sc1\n\tv_mfma_f32_16x16x32_bf16 %3, %12, %5, 0"
      : "=&v"(ak[7]), "=&v"(av[7]), "=&a"(wq[3][2]), "=&v"(aq[6]) : "a"(wq[0][1]), "v"(xf[0][7]), "v"(vXN), "s"(rsW), "s"(sXt), "a"(wq[0][2]), "v"(vQ), "s"(sq[4]), "a"(wq[0][0]) : "memory"); SB();
  WAITL(9);
  SB();
  asm volatile("v_mfma_f32_16x16x32_bf16 %0, %4, %5, 0\n\tbuffer_load_dwordx4 %1, %6, %7, %8 offen offset:512 sc1\n\tv_mfma_f32_16x16x32_bf16 %2, %9, %5, 0\n\tv_mfma_f32_16x16x32_bf16 %3, %10, %5, 0"
      : "=&v"(ak[8]), "=&a"(wq[4][0]), "=&v"(av[8]), "=&v"(aq[7]) : "a"(wq[0][1]), "v"(xf[0][8]), "v"(vQ), "s"(rsW), "s"(sq[0]), "a"(wq[0][2]), "a"(wq[0][0]) : "memory"); SB();
  WAITVB(14);
  WAITL(3);
  SB();
  asm volatile("v_mfma_f32_16x16x32_bf16 %0, %1, %2, %0\n\ts_waitcnt vmcnt(10)"
      : "+v"(ak[0]) : "a"(wq[1][1]), "v"(xf[1][0]) : "memory"); SB();
  BAR0();
  SB();
  asm volatile("v_mfma_f32_16x16x32_bf16 %0, %11, %12, %0\n\tds_read_b128 %1, %13 offset:18432\n\tv_mfma_f32_16x16x32_bf16 %2, %14, %15, %2\n\ts_add_u32 m0, %16, 49152\n\ts_add_u32 %3, %17, 98560\n\tv_mfma_f32_16x16x32_bf16 %4, %11, %15, %4\n\tds_read_b128 %5, %13 offset:20480\n\tv_mfma_f32_16x16x32_bf16 %6, %18, %15, %6\n\tbuffer_load_dwordx4 %19, %20, %3 offen offset:0 sc1 lds\n\tv_mfma_f32_16x16x32_bf16 %7, %14, %21, %7\n\tds_read_b128 %8, %13 offset:22528\n\tv_mfma_f32_16x16x32_bf16 %9, %11, %21, %9\n\tbuffer_load_dwordx4 %10, %22, %20, %23 offen offset:512 sc1"
      : "+v"(av[0]), "=&v"(xf[2][0]), "+v"(ak[1]), "=&s"(sXt), "+v"(av[1]), "=&v"(xf[2][1]), "+v"(aq[0]), "+v"(ak[2]), "=&v"(xf[2][2]), "+v"(av[2]), "=&a"(wq[4][1]) : "a"(wq[1][2]), "v"(xf[1][0]), "v"(xr0), "a"(wq[1][1]), "v"(xf[1][1]), "s"(ldsX), "s"(sXa), "a"(wq[1][0]), "v"(vXN), "s"(rsW), "v"(xf[1][2]), "v"(vQ), "s"(sq[2]) : "memory", "scc"); SB();
  asm volatile("v_mfma_f32_16x16x32_bf16 %0, %11, %12, %0\n\tds_read_b128 %1, %13 offset:24576\n\tv_mfma_f32_16x16x32_bf16 %2, %14, %15, %2\n\ts_add_u32 m0, %16, 53248\n\ts_add_u32 %3, %17, 131328\n\tv_mfma_f32_16x16x32_bf16 %4, %18, %15, %4\n\tds_read_b128 %5, %13 offset:26624\n\tv_mfma_f32_16x16x32_bf16 %6, %11, %15, %6\n\tbuffer_load_dwordx4 %19, %20, %3 offen offset:0 sc1 lds\n\tv_mfma_f32_16x16x32_bf16 %7, %14, %21, %7\n\tds_read_b128 %8, %13 offset:28672\n\tv_mfma_f32_16x16x32_bf16 %9, %18, %21, %9\n\tbuffer_load_dwordx4 %10, %22, %20, %23 offen offset:512 sc1"
      : "+v"(aq[1]), "=&v"(xf[2][3]), "+v"(ak[3]), "=&s"(sXt), "+v"(av[3]), "=&v"(xf[2][4]), "+v"(aq[2]), "+v"(ak[4]), "=&v"(xf[2][5]), "+v"(av[4]), "=&a"(wq[4][2]) : "a"(wq[1][0]), "v"(xf[1][2]), "v"(xr0), "a"(wq[1][1]), "v"(xf[1][3]), "s"(ldsX), "s"(sXb), "a"(wq[1][2]), "v"(vXN), "s"(rsW), "v"(xf[1][4]), "v"(vQ), "s"(sq[4]) : "memory", "scc"); SB();
  asm volatile("v_mfma_f32_16x16x32_bf16 %0, %7, %8, %0\n\tds_read_b128 %1, %9 offset:30720\n\tv_mfma_f32_16x16x32_bf16 %2, %10, %11, %2\n\ts_add_u32 m0, %12, 57344\n\ts_add_u32 %3, %13, 16768\n\tv_mfma_f32_16x16x32_bf16 %4, %14, %11, %4\n\tds_read_b128 %5, %9 offset:32768\n\tv_mfma_f32_16x16x32_bf16 %6, %7, %11, %6"
      : "+v"(aq[3]), "=&v"(xf[2][6]), "+v"(ak[5]), "=&s"(sXt), "+v"(av[5]), "=&v"(xf[2][7]), "+v"(aq[4]) : "a"(wq[1][0]), "v"(xf[1][4]), "v"(xr0), "a"(wq[1][1]), "v"(xf[1][5]), "s"(ldsX), "s"(sXa), "a"(wq[1][2]) : "memory", "scc"); SB();
  WAITL(8);
  SB();
  asm volatile("v_mfma_f32_16x16x32_bf16 %0, %10, %11, %0\n\tds_read_b128 %1, %12 offset:34816\n\tv_mfma_f32_16x16x32_bf16 %2, %13, %11, %2\n\tbuffer_load_dwordx4 %14, %15, %16 offen offset:0 sc1 lds\n\tv_mfma_f32_16x16x32_bf16 %3, %17, %11, %3\n\tbuffer_load_dwordx4 %4, %18, %15, %19 offen offset:640 sc1\n\tv_mfma_f32_16x16x32_bf16 %5, %10, %20, %5\n\tbuffer_load_dwordx4 %6, %18, %15, %21 offen offset:640 sc1\n\tv_mfma_f32_16x16x32_bf16 %7, %13, %20, %7\n\tv_mfma_f32_16x16x32_bf16 %8, %17, %20, %8\n\tv_mfma_f32_16x16x32_bf16 %9, %10, %22, %9"
      : "+v"(ak[6]), "=&v"(xf[2][8]), "+v"(av[6]), "+v"(aq[5]), "=&a"(wq[5][0]), "+v"(ak[7]), "=&a"(wq[5][1]), "+v"(av[7]), "+v"(aq[6]), "+v"(ak[8]) : "a"(wq[1][1]), "v"(xf[1][6]), "v"(xr0), "a"(wq[1][2]), "v"(vXN), "s"(rsW), "s"(sXt), "a"(wq[1][0]), "v"(vQ), "s"(sq[0]), "v"(xf[1][7]), "s"(sq[2]), "v"(xf[1][8]) : "memory"); SB();
  asm volatile("v_mfma_f32_16x16x32_bf16 %0, %2, %3, %0\n\tv_mfma_f32_16x16x32_bf16 %1, %4, %3, %1"
      : "+v"(av[8]), "+v"(aq[7]) : "a"(wq[1][2]), "v"(xf[1][8]), "a"(wq[1][0])); SB();
  STAMP(30);
  WAITVB(14);
  WAITL(3);
  SB();
  asm volatile("v_mfma_f32_16x16x32_bf16 %0, %11, %12, %0\n\ts_add_u32 m0, %13, 61440\n\ts_add_u32 %1, %14, 49536\n\tv_mfma_f32_16x16x32_bf16 %2, %15, %12, %2\n\tds_read_b128 %3, %16 offset:18432\n\tv_mfma_f32_16x16x32_bf16 %4, %11, %17, %4\n\tbuffer_load_dwordx4 %18, %19, %1 offen offset:0 sc1 lds\n\tv_mfma_f32_16x16x32_bf16 %5, %15, %17, %5\n\tds_read_b128 %6, %16 offset:20480\n\tv_mfma_f32_16x16x32_bf16 %7, %20, %17, %7\n\tbuffer_load_dwordx4 %8, %21, %19, %22 offen offset:640 sc1\n\tv_mfma_f32_16x16x32_bf16 %9, %11, %23, %9\n\tds_read_b128 %10, %16 offset:22528"
      : "+v"(ak[0]), "=&s"(sXt), "+v"(av[0]), "=&v"(xf[3][0]), "+v"(ak[1]), "+v"(av[1]), "=&v"(xf[3][1]), "+v"(aq[0]), "=&a"(wq[5][2]), "+v"(ak[2]), "=&v"(xf[3][2]) : "a"(wq[2][1]), "v"(xf[2][0]), "s"(ldsX), "s"(sXa), "a"(wq[2][2]), "v"(xr1), "v"(xf[2][1]), "v"(vXN), "s"(rsW), "a"(wq[2][0]), "v"(vQ), "s"(sq[4]), "v"(xf[2][2]) : "memory", "scc"); SB();
  asm volatile("v_mfma_f32_16x16x32_bf16 %0, %11, %12, %0\n\ts_add_u32 m0, %13, 65536\n\ts_add_u32 %1, %14, 82304\n\tv_mfma_f32_16x16x32_bf16 %2, %15, %12, %2\n\tds_read_b128 %3, %16 offset:24576\n\tv_mfma_f32_16x16x32_bf16 %4, %17, %18, %4\n\tbuffer_load_dwordx4 %19, %20, %1 offen offset:0 sc1 lds\n\tv_mfma_f32_16x16x32_bf16 %5, %11, %18, %5\n\tds_read_b128 %6, %16 offset:26624\n\tv_mfma_f32_16x16x32_bf16 %7, %15, %18, %7\n\tbuffer_load_dwordx4 %8, %21, %20, %22 offen offset:768 sc1\n\tv_mfma_f32_16x16x32_bf16 %9, %17, %23, %9\n\tds_read_b128 %10, %16 offset:28672"
      : "+v"(av[2]), "=&s"(sXt), "+v"(aq[1]), "=&v"(xf[3][3]), "+v"(ak[3]), "+v"(av[3]), "=&v"(xf[3][4]), "+v"(aq[2]), "=&a"(wq[6][0]), "+v"(ak[4]), "=&v"(xf[3][5]) : "a"(wq[2][2]), "v"(xf[2][2]), "s"(ldsX), "s"(sXa), "a"(wq[2][0]), "v"(xr1), "a"(wq[2][1]), "v"(xf[2][3]), "v"(vXN), "s"(rsW), "v"(vQ), "s"(sq[0]), "v"(xf[2][4]) : "memory", "scc"); SB();
  asm volatile("v_mfma_f32_16x16x32_bf16 %0, %8, %9, %0\n\tbuffer_load_dwordx4 %1, %10, %11, %12 offen offset:768 sc1\n\tv_mfma_f32_16x16x32_bf16 %2, %13, %9, %2\n\tds_read_b128 %3, %14 offset:30720\n\tv_mfma_f32_16x16x32_bf16 %4, %15, %16, %4\n\tv_mfma_f32_16x16x32_bf16 %5, %8, %16, %5\n\tds_read_b128 %6, %14 offset:32768\n\tv_mfma_f32_16x16x32_bf16 %7, %13, %16, %7"
      : "+v"(av[4]), "=&a"(wq[6][1]), "+v"(aq[3]), "=&v"(xf[3][6]), "+v"(ak[5]), "+v"(av[5]), "=&v"(xf[3][7]), "+v"(aq[4]) : "a"(wq[2][2]), "v"(xf[2][4]), "v"(vQ), "s"(rsW), "s"(sq[2]), "a"(wq[2][0]), "v"(xr1), "a"(wq[2][1]), "v"(xf[2][5]) : "memory"); SB();
  WAITL(8);
  SB();
  asm volatile("v_mfma_f32_16x16x32_bf16 %0, %10, %11, %0\n\tds_read_b128 %1, %12 offset:34816\n\tv_mfma_f32_16x16x32_bf16 %2, %13, %11, %2\n\tv_mfma_f32_16x16x32_bf16 %3, %14, %11, %3\n\tv_mfma_f32_16x16x32_bf16 %4, %10, %15, %4\n\tv_mfma_f32_16x16x32_bf16 %5, %13, %15, %5\n\tv_mfma_f32_16x16x32_bf16 %6, %14, %15, %6\n\tv_mfma_f32_16x16x32_bf16 %7, %10, %16, %7\n\tv_mfma_f32_16x16x32_bf16 %8, %13, %16, %8\n\tv_mfma_f32_16x16x32_bf16 %9, %14, %16, %9"
      : "+v"(ak[6]), "=&v"(xf[3][8]), "+v"(av[6]), "+v"(aq[5]), "+v"(ak[7]), "+v"(av[7]), "+v"(aq[6]), "+v"(ak[8]), "+v"(av[8]), "+v"(aq[7]) : "a"(wq[2][1]), "v"(xf[2][6]), "v"(xr1), "a"(wq[2][2]), "a"(wq[2][0]), "v"(xf[2][7]), "v"(xf[2][8]) : "memory"); SB();
  WAITVB(13);
  WAITL(3);
  SB();
  asm volatile("v_mfma_f32_16x16x32_bf16 %0, %1, %2, %0\n\ts_waitcnt vmcnt(9)"
      : "+v"(ak[0]) : "a"(wq[3][1]), "v"(xf[3][0]) : "memory"); SB();
  BAR0();
  SB();
  asm volatile("v_mfma_f32_16x16x32_bf16 %0, %11, %12, %0\n\tds_read_b128 %1, %13 offset:36864\n\tv_mfma_f32_16x16x32_bf16 %2, %14, %15, %2\n\ts_add_u32 m0, %16, 69632\n\ts_add_u32 %3, %17, 115072\n\tv_mfma_f32_16x16x32_bf16 %4, %11, %15, %4\n\tds_read_b128 %5, %13 offset:38912\n\tv_mfma_f32_16x16x32_bf16 %6, %18, %15, %6\n\tbuffer_load_dwordx4 %19, %20, %3 offen offset:0 sc1 lds\n\tv_mfma_f32_16x16x32_bf16 %7, %14, %21, %7\n\tds_read_b128 %8, %13 offset:40960\n\tv_mfma_f32_16x16x32_bf16 %9, %11, %21, %9\n\tbuffer_load_dwordx4 %10, %22, %20, %23 offen offset:768 sc1"
      : "+v"(av[0]), "=&v"(xf[4][0]), "+v"(ak[1]), "=&s"(sXt), "+v"(av[1]), "=&v"(xf[4][1]), "+v"(aq[0]), "+v"(ak[2]), "=&v"(xf[4][2]), "+v"(av[2]), "=&a"(wq[6][2]) : "a"(wq[3][2]), "v"(xf[3][0]), "v"(xr0), "a"(wq[3][1]), "v"(xf[3][1]), "s"(ldsX), "s"(sXa), "a"(wq[3][0]), "v"(vXN), "s"(rsW), "v"(xf[3][2]), "v"(vQ), "s"(sq[4]) : "memory", "scc"); SB();
  asm volatile("v_mfma_f32_16x16x32_bf16 %0, %11, %12, %0\n\tds_read_b128 %1, %13 offset:43008\n\tv_mfma_f32_16x16x32_bf16 %2, %14, %15, %2\n\ts_add_u32 m0, %16, 73728\n\ts_add_u32 %3, %17, 512\n\tv_mfma_f32_16x16x32_bf16 %4, %18, %15, %4\n\tds_read_b128 %5, %13 offset:45056\n\tv_mfma_f32_16x16x32_bf16 %6, %11, %15, %6\n\tbuffer_load_dwordx4 %19, %20, %3 offen offset:0 sc1 lds\n\tv_mfma_f32_16x16x32_bf16 %7, %14, %21, %7\n\tds_read_b128 %8, %13 offset:47104\n\tv_mfma_f32_16x16x32_bf16 %9, %18, %21, %9\n\tbuffer_load_dwordx4 %10, %22, %20, %23 offen offset:0 sc1"
      : "+v"(aq[1]), "=&v"(xf[4][3]), "+v"(ak[3]), "=&s"(sXt), "+v"(av[3]), "=&v"(xf[4][4]), "+v"(aq[2]), "+v"(ak[4]), "=&v"(xf[4][5]), "+v"(av[4]), "=&a"(wq[7][0]) : "a"(wq[3][0]), "v"(xf[3][2]), "v"(xr0), "a"(wq[3][1]), "v"(xf[3][3]), "s"(ldsX), "s"(sXa), "a"(wq[3][2]), "v"(vXN), "s"(rsW), "v"(xf[3][4]), "v"(vQ), "s"(sq[1]) : "memory", "scc"); SB();
  asm volatile("v_mfma_f32_16x16x32_bf16 %0, %7, %8, %0\n\tds_read_b128 %1, %9 offset:49152\n\tv_mfma_f32_16x16x32_bf16 %2, %10, %11, %2\n\ts_add_u32 m0, %12, 77824\n\ts_add_u32 %3, %13, 33280\n\tv_mfma_f32_16x16x32_bf16 %4, %14, %11, %4\n\tds_read_b128 %5, %9 offset:51200\n\tv_mfma_f32_16x16x32_bf16 %6, %7, %11, %6"
      : "+v"(aq[3]), "=&v"(xf[4][6]), "+v"(ak[5]), "=&s"(sXt), "+v"(av[5]), "=&v"(xf[4][7]), "+v"(aq[4]) : "a"(wq[3][0]), "v"(xf[3][4]), "v"(xr0), "a"(wq[3][1]), "v"(xf[3][5]), "s"(ldsX), "s"(sXa), "a"(wq[3][2]) : "memory", "scc"); SB();
  WAITL(8);
  SB();
  asm volatile("v_mfma_f32_16x16x32_bf16 %0, %10, %11, %0\n\tds_read_b128 %1, %12 offset:53248\n\tv_mfma_f32_16x16x32_bf16 %2, %13, %11, %2\n\tbuffer_load_dwordx4 %14, %15, %16 offen offset:0 sc1 lds\n\tv_mfma_f32_16x16x32_bf16 %3, %17, %11, %3\n\tbuffer_load_dwordx4 %4, %18, %15, %19 offen offset:0 sc1\n\tv_mfma_f32_16x16x32_bf16 %5, %10, %20, %5\n\tbuffer_load_dwordx4 %6, %18, %15, %21 offen offset:0 sc1\n\tv_mfma_f32_16x16x32_bf16 %7, %13, %20, %7\n\tv_mfma_f32_16x16x32_bf16 %8, %17, %20, %8\n\tv_mfma_f32_16x16x32_bf16 %9, %10, %22, %9"
      : "+v"(ak[6]), "=&v"(xf[4][8]), "+v"(av[6]), "+v"(aq[5]), "=&a"(wq[7][1]), "+v"(ak[7]), "=&a"(wq[7][2]), "+v"(av[7]), "+v"(aq[6]), "+v"(ak[8]) : "a"(wq[3][1]), "v"(xf[3][6]), "v"(xr0), "a"(wq[3][2]), "v"(vXN), "s"(rsW), "s"(sXt), "a"(wq[3][0]), "v"(vQ), "s"(sq[3]), "v"(xf[3][7]), "s"(sq[5]), "v"(xf[3][8]) : "memory"); SB();
  asm volatile("v_mfma_f32_16x16x32_bf16 %0, %2, %3, %0\n\tv_mfma_f32_16x16x32_bf16 %1, %4, %3, %1"
      : "+v"(av[8]), "+v"(aq[7]) : "a"(wq[3][2]), "v"(xf[3][8]), "a"(wq[3][0])); SB();
  WAITVB(15);
  WAITL(3);
  SB();
  asm volatile("v_mfma_f32_16x16x32_bf16 %0, %11, %12, %0\n\ts_add_u32 m0, %13, 81920\n\ts_add_u32 %1, %14, 66048\n\tv_mfma_f32_16x16x32_bf16 %2, %15, %12, %2\n\tds_read_b128 %3, %16 offset:36864\n\tv_xor_b32 %4, 0, %17\n\tv_mfma_f32_16x16x32_bf16 %5, %11, %18, %5\n\tbuffer_load_dwordx4 %19, %20, %1 offen offset:0 sc1 lds\n\tv_mfma_f32_16x16x32_bf16 %6, %15, %18, %6\n\tds_read_b128 %7, %16 offset:38912\n\tv_xor_b32 %8, 32, %17\n\tv_mfma_f32_16x16x32_bf16 %9, %21, %18, %9\n\tbuffer_load_dwordx4 %10, %22, %20, %23 offen offset:128 sc1"
      : "+v"(ak[0]), "=&s"(sXt), "+v"(av[0]), "=&v"(xf[5][0]), "=&v"(xta[0]), "+v"(ak[1]), "+v"(av[1]), "=&v"(xf[5][1]), "=&v"(xta[1]), "+v"(aq[0]), "=&a"(wq[8][0]) : "a"(wq[4][1]), "v"(xf[4][0]), "s"(ldsX), "s"(sXa), "a"(wq[4][2]), "v"(xr1), "v"(xs), "v"(xf[4][1]), "v"(vXN), "s"(rsW), "a"(wq[4][0]), "v"(vQ), "s"(sq[1]) : "memory", "scc"); SB();
  asm volatile("v_mfma_f32_16x16x32_bf16 %0, %12, %13, %0\n\tds_read_b128 %1, %14 offset:40960\n\tv_xor_b32 %2, 64, %15\n\tv_mfma_f32_16x16x32_bf16 %3, %16, %13, %3\n\ts_add_u32 m0, %17, 86016\n\ts_add_u32 %4, %18, 98816\n\tv_mfma_f32_16x16x32_bf16 %5, %19, %13, %5\n\tds_read_b128 %6, %14 offset:43008\n\tv_xor_b32 %7, 96, %15\n\tv_mfma_f32_16x16x32_bf16 %8, %12, %20, %8\n\tbuffer_load_dwordx4 %21, %22, %4 offen offset:0 sc1 lds\n\tv_mfma_f32_16x16x32_bf16 %9, %16, %20, %9\n\tds_read_b128 %10, %14 offset:45056\n\tds_read_b64 %11, %23 offset:0"
      : "+v"(ak[2]), "=&v"(xf[5][2]), "=&v"(xta[2]), "+v"(av[2]), "=&s"(sXt), "+v"(aq[1]), "=&v"(xf[5][3]), "=&v"(xta[3]), "+v"(ak[3]), "+v"(av[3]), "=&v"(xf[5][4]), "=&v"(xv[0]) : "a"(wq[4][1]), "v"(xf[4][2]), "v"(xr1), "v"(xs), "a"(wq[4][2]), "s"(ldsX), "s"(sXa), "a"(wq[4][0]), "v"(xf[4][3]), "v"(vXN), "s"(rsW), "v"(xta[0]) : "memory", "scc"); SB();
  asm volatile("v_mfma_f32_16x16x32_bf16 %0, %11, %12, %0\n\tbuffer_load_dwordx4 %1, %13, %14, %15 offen offset:128 sc1\n\tv_mfma_f32_16x16x32_bf16 %2, %16, %17, %2\n\tds_read_b128 %3, %18 offset:47104\n\tds_read_b64 %4, %19 offset:0\n\tv_mfma_f32_16x16x32_bf16 %5, %20, %17, %5\n\tbuffer_load_dwordx4 %6, %13, %14, %21 offen offset:128 sc1\n\tv_mfma_f32_16x16x32_bf16 %7, %11, %17, %7\n\tds_read_b128 %8, %18 offset:49152\n\tds_read_b64 %9, %22 offset:0\n\tv_mfma_f32_16x16x32_bf16 %10, %16, %23, %10"
      : "+v"(aq[2]), "=&a"(wq[8][1]), "+v"(ak[4]), "=&v"(xf[5][5]), "=&v"(xv[1]), "+v"(av[4]), "=&a"(wq[8][2]), "+v"(aq[3]), "=&v"(xf[5][6]), "=&v"(xv[2]), "+v"(ak[5]) : "a"(wq[4][0]), "v"(xf[4][3]), "v"(vQ), "s"(rsW), "s"(sq[3]), "a"(wq[4][1]), "v"(xf[4][4]), "v"(xr1), "v"(xta[1]), "a"(wq[4][2]), "s"(sq[5]), "v"(xta[2]), "v"(xf[4][5]) : "memory"); SB();
  asm volatile("buffer_load_dwordx4 %0, %5, %6, %7 offen offset:256 sc1\n\tv_mfma_f32_16x16x32_bf16 %1, %8, %9, %1\n\tds_read_b128 %2, %10 offset:51200\n\tds_read_b64 %3, %11 offset:0\n\tv_mfma_f32_16x16x32_bf16 %4, %12, %9, %4"
      : "=&a"(wq[9][0]), "+v"(av[5]), "=&v"(xf[5][7]), "=&v"(xv[3]), "+v"(aq[4]) : "v"(vQ), "s"(rsW), "s"(sq[1]), "a"(wq[4][2]), "v"(xf[4][5]), "v"(xr1), "v"(xta[3]), "a"(wq[4][0]) : "memory"); SB();
  WAITL(12);
  SB();
  asm volatile("v_mfma_f32_16x16x32_bf16 %0, %10, %11, %0\n\tds_read_b128 %1, %12 offset:53248\n\tv_mfma_f32_16x16x32_bf16 %2, %13, %11, %2\n\tv_mfma_f32_16x16x32_bf16 %3, %14, %11, %3\n\tv_mfma_f32_16x16x32_bf16 %4, %10, %15, %4\n\tv_mfma_f32_16x16x32_bf16 %5, %13, %15, %5\n\tv_mfma_f32_16x16x32_bf16 %6, %14, %15, %6\n\tv_mfma_f32_16x16x32_bf16 %7, %10, %16, %7\n\tv_mfma_f32_16x16x32_bf16 %8, %13, %16, %8\n\tv_mfma_f32_16x16x32_bf16 %9, %14, %16, %9"
      : "+v"(ak[6]), "=&v"(xf[5][8]), "+v"(av[6]), "+v"(aq[5]), "+v"(ak[7]), "+v"(av[7]), "+v"(aq[6]), "+v"(ak[8]), "+v"(av[8]), "+v"(aq[7]) : "a"(wq[4][1]), "v"(xf[4][6]), "v"(xr1), "a"(wq[4][2]), "a"(wq[4][0]), "v"(xf[4][7]), "v"(xf[4][8]) : "memory"); SB();
  WAITVB(16);
  WAITL(6);
  SB();
  asm volatile("v_mfma_f32_16x16x32_bf16 %0, %3, %4, %0\n\tv_add_u32 %1, 55296, %1\n\tv_mfma_f32_16x16x32_bf16 %2, %5, %4, %2\n\ts_waitcnt vmcnt(12)"
      : "+v"(ak[0]), "+v"(xr0), "+v"(av[0]) : "a"(wq[5][1]), "v"(xf[5][0]), "a"(wq[5][2]) : "memory"); SB();
  BAR0();
  SB();
  asm volatile("v_mfma_f32_16x16x32_bf16 %0, %12, %13, %0\n\tds_read_b128 %1, %14 offset:0\n\tv_mfma_f32_16x16x32_bf16 %2, %15, %13, %2\n\tds_read_b128 %3, %14 offset:2048\n\tv_mfma_f32_16x16x32_bf16 %4, %16, %13, %4\n\ts_add_u32 m0, %17, 90112\n\ts_add_u32 %5, %18, 131584\n\tv_mfma_f32_16x16x32_bf16 %6, %12, %19, %6\n\tds_read_b128 %7, %14 offset:4096\n\tv_mfma_f32_16x16x32_bf16 %8, %15, %19, %8\n\tbuffer_load_dwordx4 %20, %21, %5 offen offset:0 sc1 lds\n\tv_mfma_f32_16x16x32_bf16 %9, %16, %19, %9\n\tds_read_b128 %10, %14 offset:6144\n\tv_mfma_f32_16x16x32_bf16 %11, %12, %22, %11"
      : "+v"(ak[1]), "=&v"(xf[6][0]), "+v"(av[1]), "=&v"(xf[6][1]), "+v"(aq[0]), "=&s"(sXt), "+v"(ak[2]), "=&v"(xf[6][2]), "+v"(av[2]), "+v"(aq[1]), "=&v"(xf[6][3]), "+v"(ak[3]) : "a"(wq[5][1]), "v"(xf[5][1]), "v"(xr0), "a"(wq[5][2]), "a"(wq[5][0]), "s"(ldsX), "s"(sXb), "v"(xf[5][2]), "v"(vXN), "s"(rsW), "v"(xf[5][3]) : "memory", "scc"); SB();
  asm volatile("buffer_load_dwordx4 %0, %11, %12, %13 offen offset:256 sc1\n\tv_mfma_f32_16x16x32_bf16 %1, %14, %15, %1\n\tds_read_b128 %2, %16 offset:8192\n\tv_mfma_f32_16x16x32_bf16 %3, %17, %15, %3\n\ts_add_u32 m0, %18, 94208\n\ts_add_u32 %4, %19, 17024\n\tv_mfma_f32_16x16x32_bf16 %5, %20, %21, %5\n\tds_read_b128 %6, %16 offset:10240\n\tv_mfma_f32_16x16x32_bf16 %7, %14, %21, %7\n\tbuffer_load_dwordx4 %22, %12, %4 offen offset:0 sc1 lds\n\tv_mfma_f32_16x16x32_bf16 %8, %17, %21, %8\n\tds_read_b128 %9, %16 offset:12288\n\tv_mfma_f32_16x16x32_bf16 %10, %20, %23, %10"
      : "=&a"(wq[9][1]), "+v"(av[3]), "=&v"(xf[6][4]), "+v"(aq[2]), "=&s"(sXt), "+v"(ak[4]), "=&v"(xf[6][5]), "+v"(av[4]), "+v"(aq[3]), "=&v"(xf[6][6]), "+v"(ak[5]) : "v"(vQ), "s"(rsW), "s"(sq[3]), "a"(wq[5][2]), "v"(xf[5][3]), "v"(xr0), "a"(wq[5][0]), "s"(ldsX), "s"(sXa), "a"(wq[5][1]), "v"(xf[5][4]), "v"(vXN), "v"(xf[5][5]) : "memory", "scc"); SB();
  asm volatile("buffer_load_dwordx4 %0, %4, %5, %6 offen offset:256 sc1\n\tv_mfma_f32_16x16x32_bf16 %1, %7, %8, %1\n\tds_read_b128 %2, %9 offset:14336\n\tv_mfma_f32_16x16x32_bf16 %3, %10, %8, %3"
      : "=&a"(wq[9][2]), "+v"(av[5]), "=&v"(xf[6][7]), "+v"(aq[4]) : "v"(vQ), "s"(rsW), "s"(sq[5]), "a"(wq[5][2]), "v"(xf[5][5]), "v"(xr0), "a"(wq[5][0]) : "memory"); SB();
  WAITL(8);
  SB();
  asm volatile("v_mfma_f32_16x16x32_bf16 %0, %10, %11, %0\n\tds_read_b128 %1, %12 offset:16384\n\tv_mfma_f32_16x16x32_bf16 %2, %13, %11, %2\n\ts_add_u32 m0, %14, 98304\n\ts_add_u32 %3, %15, 49792\n\tv_mfma_f32_16x16x32_bf16 %4, %16, %11, %4\n\tbuffer_load_dwordx4 %17, %18, %3 offen offset:0 sc1 lds\n\tv_mfma_f32_16x16x32_bf16 %5, %10, %19, %5\n\tbuffer_load_dwordx4 %6, %20, %18, %21 offen offset:384 sc1\n\tv_mfma_f32_16x16x32_bf16 %7, %13, %19, %7\n\tv_mfma_f32_16x16x32_bf16 %8, %16, %19, %8\n\tv_mfma_f32_16x16x32_bf16 %9, %10, %22, %9"
      : "+v"(ak[6]), "=&v"(xf[6][8]), "+v"(av[6]), "=&s"(sXt), "+v"(aq[5]), "+v"(ak[7]), "=&a"(wq[10][0]), "+v"(av[7]), "+v"(aq[6]), "+v"(ak[8]) : "a"(wq[5][1]), "v"(xf[5][6]), "v"(xr0), "a"(wq[5][2]), "s"(ldsX), "s"(sXa), "a"(wq[5][0]), "v"(vXN), "s"(rsW), "v"(xf[5][7]), "v"(vQ), "s"(sq[1]), "v"(xf[5][8]) : "memory", "scc"); SB();
  asm volatile("v_mfma_f32_16x16x32_bf16 %0, %2, %3, %0\n\tv_mfma_f32_16x16x32_bf16 %1, %4, %3, %1"
      : "+v"(av[8]), "+v"(aq[7]) : "a"(wq[5][2]), "v"(xf[5][8]), "a"(wq[5][0])); SB();
  STAMP(31);
  WAITVB(17);
  WAITL(3);
  SB();
  asm volatile("v_mfma_f32_16x16x32_bf16 %0, %12, %13, %0\n\tv_add_u32 %1, 55296, %1\n\tv_mfma_f32_16x16x32_bf16 %2, %14, %13, %2\n\tds_read_b128 %3, %1 offset:0\n\tv_mfma_f32_16x16x32_bf16 %4, %12, %15, %4\n\ts_add_u32 m0, %16, 102400\n\ts_add_u32 %5, %17, 82560\n\tv_mfma_f32_16x16x32_bf16 %6, %14, %15, %6\n\tds_read_b128 %7, %1 offset:2048\n\tv_mfma_f32_16x16x32_bf16 %8, %18, %15, %8\n\tbuffer_load_dwordx4 %19, %20, %5 offen offset:0 sc1 lds\n\tv_mfma_f32_16x16x32_bf16 %9, %12, %21, %9\n\tds_read_b128 %10, %1 offset:4096\n\tv_mfma_f32_16x16x32_bf16 %11, %14, %21, %11"
      : "+v"(ak[0]), "+v"(xr1), "+v"(av[0]), "=&v"(xf[7][0]), "+v"(ak[1]), "=&s"(sXt), "+v"(av[1]), "=&v"(xf[7][1]), "+v"(aq[0]), "+v"(ak[2]), "=&v"(xf[7][2]), "+v"(av[2]) : "a"(wq[6][1]), "v"(xf[6][0]), "a"(wq[6][2]), "v"(xf[6][1]), "s"(ldsX), "s"(sXa), "a"(wq[6][0]), "v"(vXN), "s"(rsW), "v"(xf[6][2]) : "memory", "scc"); SB();
  asm volatile("buffer_load_dwordx4 %0, %11, %12, %13 offen offset:384 sc1\n\tv_mfma_f32_16x16x32_bf16 %1, %14, %15, %1\n\tds_read_b128 %2, %16 offset:6144\n\tv_mfma_f32_16x16x32_bf16 %3, %17, %18, %3\n\ts_add_u32 m0, %19, 106496\n\ts_add_u32 %4, %20, 115328\n\tv_mfma_f32_16x16x32_bf16 %5, %21, %18, %5\n\tds_read_b128 %6, %16 offset:8192\n\tv_mfma_f32_16x16x32_bf16 %7, %14, %18, %7\n\tbuffer_load_dwordx4 %22, %12, %4 offen offset:0 sc1 lds\n\tv_mfma_f32_16x16x32_bf16 %8, %17, %23, %8\n\tds_read_b128 %9, %16 offset:10240\n\tv_mfma_f32_16x16x32_bf16 %10, %21, %23, %10"
      : "=&a"(wq[10][1]), "+v"(aq[1]), "=&v"(xf[7][3]), "+v"(ak[3]), "=&s"(sXt), "+v"(av[3]), "=&v"(xf[7][4]), "+v"(aq[2]), "+v"(ak[4]), "=&v"(xf[7][5]), "+v"(av[4]) : "v"(vQ), "s"(rsW), "s"(sq[3]), "a"(wq[6][0]), "v"(xf[6][2]), "v"(xr1), "a"(wq[6][1]), "v"(xf[6][3]), "s"(ldsX), "s"(sXa), "a"(wq[6][2]), "v"(vXN), "v"(xf[6][4]) : "memory", "scc"); SB();
  asm volatile("buffer_load_dwordx4 %0, %8, %9, %10 offen offset:384 sc1\n\tv_mfma_f32_16x16x32_bf16 %1, %11, %12, %1\n\tds_read_b128 %2, %13 offset:12288\n\tv_mfma_f32_16x16x32_bf16 %3, %14, %15, %3\n\tbuffer_load_dwordx4 %4, %8, %9, %16 offen offset:512 sc1\n\tv_mfma_f32_16x16x32_bf16 %5, %17, %15, %5\n\tds_read_b128 %6, %13 offset:14336\n\tv_mfma_f32_16x16x32_bf16 %7, %11, %15, %7"
      : "=&a"(wq[10][2]), "+v"(aq[3]), "=&v"(xf[7][6]), "+v"(ak[5]), "=&a"(wq[11][0]), "+v"(av[5]), "=&v"(xf[7][7]), "+v"(aq[4]) : "v"(vQ), "s"(rsW), "s"(sq[5]), "a"(wq[6][0]), "v"(xf[6][4]), "v"(xr1), "a"(wq[6][1]), "v"(xf[6][5]), "s"(sq[1]), "a"(wq[6][2]) : "memory"); SB();
  WAITL(8);
  SB();
  asm volatile("v_mfma_f32_16x16x32_bf16 %0, %11, %12, %0\n\tds_read_b128 %1, %13 offset:16384\n\tv_mfma_f32_16x16x32_bf16 %2, %14, %12, %2\n\tbuffer_load_dwordx4 %3, %15, %16, %17 offen offset:512 sc1\n\tv_mfma_f32_16x16x32_bf16 %4, %18, %12, %4\n\tv_mfma_f32_16x16x32_bf16 %5, %11, %19, %5\n\tv_mfma_f32_16x16x32_bf16 %6, %14, %19, %6\n\tv_mfma_f32_16x16x32_bf16 %7, %18, %19, %7\n\tv_mfma_f32_16x16x32_bf16 %8, %11, %20, %8\n\tv_mfma_f32_16x16x32_bf16 %9, %14, %20, %9\n\tv_mfma_f32_16x16x32_bf16 %10, %18, %20, %10"
      : "+v"(ak[6]), "=&v"(xf[7][8]), "+v"(av[6]), "=&a"(wq[11][1]), "+v"(aq[5]), "+v"(ak[7]), "+v"(av[7]), "+v"(aq[6]), "+v"(ak[8]), "+v"(av[8]), "+v"(aq[7]) : "a"(wq[6][1]), "v"(xf[6][6]), "v"(xr1), "a"(wq[6][2]), "v"(vQ), "s"(rsW), "s"(sq[3]), "a"(wq[6][0]), "v"(xf[6][7]), "v"(xf[6][8]) : "memory"); SB();
  WAITVB(18);
  WAITL(3);
  SB();
  asm volatile("v_mfma_f32_16x16x32_bf16 %0, %1, %2, %0\n\ts_waitcnt vmcnt(11)"
      : "+v"(ak[0]) : "a"(wq[7][1]), "v"(xf[7][0]) : "memory"); SB();
  BAR0();
  SB();
  asm volatile("v_mfma_f32_16x16x32_bf16 %0, %11, %12, %0\n\tds_read_b128 %1, %13 offset:18432\n\tv_mfma_f32_16x16x32_bf16 %2, %14, %15, %2\n\ts_add_u32 m0, %16, 110592\n\ts_add_u32 %3, %17, 768\n\tv_mfma_f32_16x16x32_bf16 %4, %11, %15, %4\n\tds_read_b128 %5, %13 offset:20480\n\tv_mfma_f32_16x16x32_bf16 %6, %18, %15, %6\n\tbuffer_load_dwordx4 %19, %20, %3 offen offset:0 sc1 lds\n\tv_mfma_f32_16x16x32_bf16 %7, %14, %21, %7\n\tds_read_b128 %8, %13 offset:22528\n\tv_mfma_f32_16x16x32_bf16 %9, %11, %21, %9\n\tbuffer_load_dwordx4 %10, %22, %20, %23 offen offset:512 sc1"
      : "+v"(av[0]), "=&v"(xf[8][0]), "+v"(ak[1]), "=&s"(sXt), "+v"(av[1]), "=&v"(xf[8][1]), "+v"(aq[0]), "+v"(ak[2]), "=&v"(xf[8][2]), "+v"(av[2]), "=&a"(wq[11][2]) : "a"(wq[7][2]), "v"(xf[7][0]), "v"(xr0), "a"(wq[7][1]), "v"(xf[7][1]), "s"(ldsX), "s"(sXa), "a"(wq[7][0]), "v"(vXN), "s"(rsW), "v"(xf[7][2]), "v"(vQ), "s"(sq[5]) : "memory", "scc"); SB();
  asm volatile("v_mfma_f32_16x16x32_bf16 %0, %11, %12, %0\n\tds_read_b128 %1, %13 offset:24576\n\tv_mfma_f32_16x16x32_bf16 %2, %14, %15, %2\n\ts_add_u32 m0, %16, 114688\n\ts_add_u32 %3, %17, 33536\n\tv_mfma_f32_16x16x32_bf16 %4, %18, %15, %4\n\tds_read_b128 %5, %13 offset:26624\n\tv_mfma_f32_16x16x32_bf16 %6, %11, %15, %6\n\tbuffer_load_dwordx4 %19, %20, %3 offen offset:0 sc1 lds\n\tv_mfma_f32_16x16x32_bf16 %7, %14, %21, %7\n\tds_read_b128 %8, %13 offset:28672\n\tv_mfma_f32_16x16x32_bf16 %9, %18, %21, %9\n\tbuffer_load_dwordx4 %10, %22, %20, %23 offen offset:640 sc1"
      : "+v"(aq[1]), "=&v"(xf[8][3]), "+v"(ak[3]), "=&s"(sXt), "+v"(av[3]), "=&v"(xf[8][4]), "+v"(aq[2]), "+v"(ak[4]), "=&v"(xf[8][5]), "+v"(av[4]), "=&a"(wq[12][0]) : "a"(wq[7][0]), "v"(xf[7][2]), "v"(xr0), "a"(wq[7][1]), "v"(xf[7][3]), "s"(ldsX), "s"(sXa), "a"(wq[7][2]), "v"(vXN), "s"(rsW), "v"(xf[7][4]), "v"(vQ), "s"(sq[1]) : "memory", "scc"); SB();
  asm volatile("v_mfma_f32_16x16x32_bf16 %0, %7, %8, %0\n\tds_read_b128 %1, %9 offset:30720\n\tv_mfma_f32_16x16x32_bf16 %2, %10, %11, %2\n\ts_add_u32 m0, %12, 118784\n\ts_add_u32 %3, %13, 66304\n\tv_mfma_f32_16x16x32_bf16 %4, %14, %11, %4\n\tds_read_b128 %5, %9 offset:32768\n\tv_mfma_f32_16x16x32_bf16 %6, %7, %11, %6"
      : "+v"(aq[3]), "=&v"(xf[8][6]), "+v"(ak[5]), "=&s"(sXt), "+v"(av[5]), "=&v"(xf[8][7]), "+v"(aq[4]) : "a"(wq[7][0]), "v"(xf[7][4]), "v"(xr0), "a"(wq[7][1]), "v"(xf[7][5]), "s"(ldsX), "s"(sXa), "a"(wq[7][2]) : "memory", "scc"); SB();
  WAITL(8);
  SB();
  asm volatile("v_mfma_f32_16x16x32_bf16 %0, %10, %11, %0\n\tds_read_b128 %1, %12 offset:34816\n\tv_mfma_f32_16x16x32_bf16 %2, %13, %11, %2\n\tbuffer_load_dwordx4 %14, %15, %16 offen offset:0 sc1 lds\n\tv_mfma_f32_16x16x32_bf16 %3, %17, %11, %3\n\tbuffer_load_dwordx4 %4, %18, %15, %19 offen offset:640 sc1\n\tv_mfma_f32_16x16x32_bf16 %5, %10, %20, %5\n\tbuffer_load_dwordx4 %6, %18, %15, %21 offen offset:640 sc1\n\tv_mfma_f32_16x16x32_bf16 %7, %13, %20, %7\n\tbuffer_load_dword %8, %22, %15, 0 offen sc1\n\tv_mfma_f32_16x16x32_bf16 %9, %17, %20, %9"
      : "+v"(ak[6]), "=&v"(xf[8][8]), "+v"(av[6]), "+v"(aq[5]), "=&a"(wq[12][1]), "+v"(ak[7]), "=&a"(wq[12][2]), "+v"(av[7]), "=&v"(fq), "+v"(aq[6]) : "a"(wq[7][1]), "v"(xf[7][6]), "v"(xr0), "a"(wq[7][2]), "v"(vXN), "s"(rsW), "s"(sXt), "a"(wq[7][0]), "v"(vQ), "s"(sq[3]), "v"(xf[7][7]), "s"(sq[5]), "v"(vFQ) : "memory"); SB();
  asm volatile("v_mfma_f32_16x16x32_bf16 %0, %3, %4, %0\n\tv_mfma_f32_16x16x32_bf16 %1, %5, %4, %1\n\tv_mfma_f32_16x16x32_bf16 %2, %6, %4, %2"
      : "+v"(ak[8]), "+v"(av[8]), "+v"(aq[7]) : "a"(wq[7][1]), "v"(xf[7][8]), "a"(wq[7][2]), "a"(wq[7][0])); SB();
  WAITVB(21);
  WAITL(3);
  SB();
  asm volatile("v_mfma_f32_16x16x32_bf16 %0, %11, %12, %0\n\ts_add_u32 m0, %13, 122880\n\ts_add_u32 %1, %14, 99072\n\tv_mfma_f32_16x16x32_bf16 %2, %15, %12, %2\n\tds_read_b128 %3, %16 offset:18432\n\tv_mfma_f32_16x16x32_bf16 %4, %11, %17, %4\n\tbuffer_load_dwordx4 %18, %19, %1 offen offset:0 sc1 lds\n\tv_mfma_f32_16x16x32_bf16 %5, %15, %17, %5\n\tds_read_b128 %6, %16 offset:20480\n\tv_mfma_f32_16x16x32_bf16 %7, %20, %17, %7\n\tbuffer_load_dwordx4 %8, %21, %19, %22 offen offset:768 sc1\n\tv_mfma_f32_16x16x32_bf16 %9, %11, %23, %9\n\tds_read_b128 %10, %16 offset:22528"
      : "+v"(ak[0]), "=&s"(sXt), "+v"(av[0]), "=&v"(xf[9][0]), "+v"(ak[1]), "+v"(av[1]), "=&v"(xf[9][1]), "+v"(aq[0]), "=&a"(wq[13][0]), "+v"(ak[2]), "=&v"(xf[9][2]) : "a"(wq[8][1]), "v"(xf[8][0]), "s"(ldsX), "s"(sXa), "a"(wq[8][2]), "v"(xr1), "v"(xf[8][1]), "v"(vXN), "s"(rsW), "a"(wq[8][0]), "v"(vQ), "s"(sq[1]), "v"(xf[8][2]) : "memory", "scc"); SB();
  asm volatile("v_mfma_f32_16x16x32_bf16 %0, %11, %12, %0\n\ts_add_u32 m0, %13, 126976\n\ts_add_u32 %1, %14, 131840\n\tv_mfma_f32_16x16x32_bf16 %2, %15, %12, %2\n\tds_read_b128 %3, %16 offset:24576\n\tv_mfma_f32_16x16x32_bf16 %4, %17, %18, %4\n\tbuffer_load_dwordx4 %19, %20, %1 offen offset:0 sc1 lds\n\tv_mfma_f32_16x16x32_bf16 %5, %11, %18, %5\n\tds_read_b128 %6, %16 offset:26624\n\tv_mfma_f32_16x16x32_bf16 %7, %15, %18, %7\n\tbuffer_load_dwordx4 %8, %21, %20, %22 offen offset:768 sc1\n\tv_mfma_f32_16x16x32_bf16 %9, %17, %23, %9\n\tds_read_b128 %10, %16 offset:28672"
      : "+v"(av[2]), "=&s"(sXt), "+v"(aq[1]), "=&v"(xf[9][3]), "+v"(ak[3]), "+v"(av[3]), "=&v"(xf[9][4]), "+v"(aq[2]), "=&a"(wq[13][1]), "+v"(ak[4]), "=&v"(xf[9][5]) : "a"(wq[8][2]), "v"(xf[8][2]), "s"(ldsX), "s"(sXc), "a"(wq[8][0]), "v"(xr1), "a"(wq[8][1]), "v"(xf[8][3]), "v"(vXN), "s"(rsW), "v"(vQ), "s"(sq[3]), "v"(xf[8][4]) : "memory", "scc"); SB();
  asm volatile("v_mfma_f32_16x16x32_bf16 %0, %8, %9, %0\n\tbuffer_load_dwordx4 %1, %10, %11, %12 offen offset:768 sc1\n\tv_mfma_f32_16x16x32_bf16 %2, %13, %9, %2\n\tds_read_b128 %3, %14 offset:30720\n\tv_mfma_f32_16x16x32_bf16 %4, %15, %16, %4\n\tv_mfma_f32_16x16x32_bf16 %5, %8, %16, %5\n\tds_read_b128 %6, %14 offset:32768\n\tv_mfma_f32_16x16x32_bf16 %7, %13, %16, %7"
      : "+v"(av[4]), "=&a"(wq[13][2]), "+v"(aq[3]), "=&v"(xf[9][6]), "+v"(ak[5]), "+v"(av[5]), "=&v"(xf[9][7]), "+v"(aq[4]) : "a"(wq[8][2]), "v"(xf[8][4]), "v"(vQ), "s"(rsW), "s"(sq[5]), "a"(wq[8][0]), "v"(xr1), "a"(wq[8][1]), "v"(xf[8][5]) : "memory"); SB();
  WAITL(8);
  SB();
  asm volatile("v_mfma_f32_16x16x32_bf16 %0, %10, %11, %0\n\tds_read_b128 %1, %12 offset:34816\n\tv_mfma_f32_16x16x32_bf16 %2, %13, %11, %2\n\tv_mfma_f32_16x16x32_bf16 %3, %14, %11, %3\n\tv_mfma_f32_16x16x32_bf16 %4, %10, %15, %4\n\tv_mfma_f32_16x16x32_bf16 %5, %13, %15, %5\n\tv_mfma_f32_16x16x32_bf16 %6, %14, %15, %6\n\tv_mfma_f32_16x16x32_bf16 %7, %10, %16, %7\n\tv_mfma_f32_16x16x32_bf16 %8, %13, %16, %8\n\tv_mfma_f32_16x16x32_bf16 %9, %14, %16, %9"
      : "+v"(ak[6]), "=&v"(xf[9][8]), "+v"(av[6]), "+v"(aq[5]), "+v"(ak[7]), "+v"(av[7]), "+v"(aq[6]), "+v"(ak[8]), "+v"(av[8]), "+v"(aq[7]) : "a"(wq[8][1]), "v"(xf[8][6]), "v"(xr1), "a"(wq[8][2]), "a"(wq[8][0]), "v"(xf[8][7]), "v"(xf[8][8]) : "memory"); SB();
  WAITVB(21);
  WAITL(3);
  SB();
  asm volatile("v_mfma_f32_16x16x32_bf16 %0, %3, %4, %0\n\ts_add_u32 m0, %5, 18432\n\tv_add_u32 %1, 36864, %1\n\tv_mfma_f32_16x16x32_bf16 %2, %6, %4, %2\n\ts_waitcnt vmcnt(16)"
      : "+v"(ak[0]), "+v"(xr0), "+v"(av[0]) : "a"(wq[9][1]), "v"(xf[9][0]), "s"(ldsS), "a"(wq[9][2]) : "memory", "scc"); SB();
  BAR0();
  SB();
  asm volatile("v_mfma_f32_16x16x32_bf16 %0, %11, %12, %0\n\tds_read_b128 %1, %13 offset:0\n\tv_mfma_f32_16x16x32_bf16 %2, %14, %12, %2\n\tds_read_b128 %3, %13 offset:2048\n\tv_mfma_f32_16x16x32_bf16 %4, %15, %12, %4\n\tbuffer_load_dwordx4 %16, %17, %18 offen offset:0  lds\n\tv_mfma_f32_16x16x32_bf16 %5, %11, %19, %5\n\tds_read_b128 %6, %13 offset:4096\n\tv_mfma_f32_16x16x32_bf16 %7, %14, %19, %7\n\tbuffer_load_dwordx4 %20, %17, %18 offen offset:1024  lds\n\tv_mfma_f32_16x16x32_bf16 %8, %15, %19, %8\n\tds_read_b128 %9, %13 offset:6144\n\tv_mfma_f32_16x16x32_bf16 %10, %11, %21, %10\n\tbuffer_load_dwordx4 %22, %17, %18 offen offset:2048  lds"
      : "+v"(ak[1]), "=&v"(xf[10][0]), "+v"(av[1]), "=&v"(xf[10][1]), "+v"(aq[0]), "+v"(ak[2]), "=&v"(xf[10][2]), "+v"(av[2]), "+v"(aq[1]), "=&v"(xf[10][3]), "+v"(ak[3]) : "a"(wq[9][1]), "v"(xf[9][1]), "v"(xr0), "a"(wq[9][2]), "a"(wq[9][0]), "v"(vW2[0]), "s"(rs2), "s"(sw[0]), "v"(xf[9][2]), "v"(vW2[1]), "v"(xf[9][3]), "v"(vW2[2]) : "memory"); SB();
  asm volatile("v_mfma_f32_16x16x32_bf16 %0, %9, %10, %0\n\tds_read_b128 %1, %11 offset:8192\n\tv_mfma_f32_16x16x32_bf16 %2, %12, %10, %2\n\tbuffer_load_dwordx4 %13, %14, %15 offen offset:3072  lds\n\tv_mfma_f32_16x16x32_bf16 %3, %16, %17, %3\n\tds_read_b128 %4, %11 offset:10240\n\ts_add_u32 m0, %18, 34816\n\tv_mfma_f32_16x16x32_bf16 %5, %9, %17, %5\n\tbuffer_load_dwordx4 %19, %14, %20 offen offset:0  lds\n\tv_mfma_f32_16x16x32_bf16 %6, %12, %17, %6\n\tds_read_b128 %7, %11 offset:12288\n\tv_mfma_f32_16x16x32_bf16 %8, %16, %21, %8\n\tbuffer_load_dwordx4 %22, %14, %20 offen offset:1024  lds"
      : "+v"(av[3]), "=&v"(xf[10][4]), "+v"(aq[2]), "+v"(ak[4]), "=&v"(xf[10][5]), "+v"(av[4]), "+v"(aq[3]), "=&v"(xf[10][6]), "+v"(ak[5]) : "a"(wq[9][2]), "v"(xf[9][3]), "v"(xr0), "a"(wq[9][0]), "v"(vW2[3]), "s"(rs2), "s"(sw[0]), "a"(wq[9][1]), "v"(xf[9][4]), "s"(ldsS), "v"(vW2[0]), "s"(sw[1]), "v"(xf[9][5]), "v"(vW2[1]) : "memory", "scc"); SB();
  asm volatile("v_mfma_f32_16x16x32_bf16 %0, %3, %4, %0\n\tds_read_b128 %1, %5 offset:14336\n\tv_mfma_f32_16x16x32_bf16 %2, %6, %4, %2"
      : "+v"(av[5]), "=&v"(xf[10][7]), "+v"(aq[4]) : "a"(wq[9][2]), "v"(xf[9][5]), "v"(xr0), "a"(wq[9][0]) : "memory"); SB();
  WAITL(8);
  SB();
  asm volatile("v_mfma_f32_16x16x32_bf16 %0, %10, %11, %0\n\tds_read_b128 %1, %12 offset:16384\n\tv_mfma_f32_16x16x32_bf16 %2, %13, %11, %2\n\tbuffer_load_dwordx4 %14, %15, %16 offen offset:2048  lds\n\tv_mfma_f32_16x16x32_bf16 %3, %17, %11, %3\n\tbuffer_load_dwordx4 %18, %15, %16 offen offset:3072  lds\n\tv_mfma_f32_16x16x32_bf16 %4, %10, %19, %4\n\tv_mfma_f32_16x16x32_bf16 %5, %13, %19, %5\n\tv_mfma_f32_16x16x32_bf16 %6, %17, %19, %6\n\tv_mfma_f32_16x16x32_bf16 %7, %10, %20, %7\n\tv_mfma_f32_16x16x32_bf16 %8, %13, %20, %8\n\tv_mfma_f32_16x16x32_bf16 %9, %17, %20, %9"
      : "+v"(ak[6]), "=&v"(xf[10][8]), "+v"(av[6]), "+v"(aq[5]), "+v"(ak[7]), "+v"(av[7]), "+v"(aq[6]), "+v"(ak[8]), "+v"(av[8]), "+v"(aq[7]) : "a"(wq[9][1]), "v"(xf[9][6]), "v"(xr0), "a"(wq[9][2]), "v"(vW2[2]), "s"(rs2), "s"(sw[1]), "a"(wq[9][0]), "v"(vW2[3]), "v"(xf[9][7]), "v"(xf[9][8]) : "memory"); SB();
  STAMP(32);
  WAITVB(23);
  WAITL(3);
  SB();
  asm volatile("v_mfma_f32_16x16x32_bf16 %0, %13, %14, %0\n\tv_add_u32 %1, 36864, %1\n\tv_mfma_f32_16x16x32_bf16 %2, %15, %14, %2\n\tds_read_b128 %3, %1 offset:0\n\tv_mfma_f32_16x16x32_bf16 %4, %13, %16, %4\n\tv_mfma_f32_16x16x32_bf16 %5, %15, %16, %5\n\tds_read_b128 %6, %1 offset:2048\n\tv_mfma_f32_16x16x32_bf16 %7, %17, %16, %7\n\tv_mfma_f32_16x16x32_bf16 %8, %13, %18, %8\n\tds_read_b128 %9, %1 offset:4096\n\tv_mfma_f32_16x16x32_bf16 %10, %15, %18, %10\n\tv_mfma_f32_16x16x32_bf16 %11, %17, %18, %11\n\tds_read_b128 %12, %1 offset:6144"
      : "+v"(ak[0]), "+v"(xr1), "+v"(av[0]), "=&v"(xf[11][0]), "+v"(ak[1]), "+v"(av[1]), "=&v"(xf[11][1]), "+v"(aq[0]), "+v"(ak[2]), "=&v"(xf[11][2]), "+v"(av[2]), "+v"(aq[1]), "=&v"(xf[11][3]) : "a"(wq[10][1]), "v"(xf[10][0]), "a"(wq[10][2]), "v"(xf[10][1]), "a"(wq[10][0]), "v"(xf[10][2]) : "memory"); SB();
  asm volatile("v_mfma_f32_16x16x32_bf16 %0, %13, %14, %0\n\tv_mfma_f32_16x16x32_bf16 %1, %15, %14, %1\n\tds_read_b128 %2, %16 offset:8192\n\tv_mfma_f32_16x16x32_bf16 %3, %17, %14, %3\n\tv_mfma_f32_16x16x32_bf16 %4, %13, %18, %4\n\tds_read_b128 %5, %16 offset:10240\n\tv_mfma_f32_16x16x32_bf16 %6, %15, %18, %6\n\tv_mfma_f32_16x16x32_bf16 %7, %17, %18, %7\n\tds_read_b128 %8, %16 offset:12288\n\tv_mfma_f32_16x16x32_bf16 %9, %13, %19, %9\n\tv_mfma_f32_16x16x32_bf16 %10, %15, %19, %10\n\tds_read_b128 %11, %16 offset:14336\n\tv_mfma_f32_16x16x32_bf16 %12, %17, %19, %12"
      : "+v"(ak[3]), "+v"(av[3]), "=&v"(xf[11][4]), "+v"(aq[2]), "+v"(ak[4]), "=&v"(xf[11][5]), "+v"(av[4]), "+v"(aq[3]), "=&v"(xf[11][6]), "+v"(ak[5]), "+v"(av[5]), "=&v"(xf[11][7]), "+v"(aq[4]) : "a"(wq[10][1]), "v"(xf[10][3]), "a"(wq[10][2]), "v"(xr1), "a"(wq[10][0]), "v"(xf[10][4]), "v"(xf[10][5]) : "memory"); SB();
  WAITL(8);
  SB();
  asm volatile("v_mfma_f32_16x16x32_bf16 %0, %3, %4, %0\n\tds_read_b128 %1, %5 offset:16384\n\tv_mfma_f32_16x16x32_bf16 %2, %6, %4, %2\n\ts_waitcnt vmcnt(10)"
      : "+v"(ak[6]), "=&v"(xf[11][8]), "+v"(av[6]) : "a"(wq[10][1]), "v"(xf[10][6]), "v"(xr1), "a"(wq[10][2]) : "memory"); SB();
  BAR0();
  SB();
  asm volatile("v_mfma_f32_16x16x32_bf16 %0, %9, %10, %0\n\tds_read_b128 %1, %11 offset:18432\n\tds_read_b128 %2, %12 offset:18432\n\tv_mfma_f32_16x16x32_bf16 %3, %13, %14, %3\n\tv_mfma_f32_16x16x32_bf16 %4, %15, %14, %4\n\tv_mfma_f32_16x16x32_bf16 %5, %9, %14, %5\n\tv_mfma_f32_16x16x32_bf16 %6, %13, %16, %6\n\tv_mfma_f32_16x16x32_bf16 %7, %15, %16, %7\n\tv_mfma_f32_16x16x32_bf16 %8, %9, %16, %8"
      : "+v"(aq[5]), "=&v"(xf[12][0]), "=&v"(xf[13][0]), "+v"(ak[7]), "+v"(av[7]), "+v"(aq[6]), "+v"(ak[8]), "+v"(av[8]), "+v"(aq[7]) : "a"(wq[10][0]), "v"(xf[10][6]), "v"(xr0), "v"(xr1), "a"(wq[10][1]), "v"(xf[10][7]), "a"(wq[10][2]), "v"(xf[10][8]) : "memory"); SB();
  WAITV1(13, fq);
  if (__builtin_expect(__ballot(fq != nonce) != 0ull, 0)) poll1<4>(rsW, vFQ, nonce);
  STAMP(13);
  u32x4 wor[14];
  WAITVB(19);
  WAITL(0);
  SB();
  asm volatile("v_mfma_f32_16x16x32_bf16 %0, %1, %2, %0"
      : "+v"(ak[0]) : "a"(wq[11][1]), "v"(xf[11][0])); SB();
  TOUCH(vT2, rs2, 0u, 0);
  SB();
  asm volatile("v_mfma_f32_16x16x32_bf16 %0, %2, %3, %0\n\tds_read_b128 %1, %4 offset:20480"
      : "+v"(av[0]), "=&v"(xf[12][1]) : "a"(wq[11][2]), "v"(xf[11][0]), "v"(xr0) : "memory"); SB();
  WAITVB(15);
  SB();
  asm volatile("v_mfma_f32_16x16x32_bf16 %0, %3, %4, %0\n\tds_read_b128 %1, %5 offset:20480\n\tv_mfma_f32_16x16x32_bf16 %2, %6, %4, %2"
      : "+v"(ak[0]), "=&v"(xf[13][1]), "+v"(av[0]) : "a"(wq[12][1]), "v"(xf[12][0]), "v"(xr1), "a"(wq[12][2]) : "memory"); SB();
  WAITVB(9);
  SB();
  asm volatile("v_mfma_f32_16x16x32_bf16 %0, %2, %3, %0\n\tv_mfma_f32_16x16x32_bf16 %1, %4, %3, %1"
      : "+v"(ak[0]), "+v"(av[0]) : "a"(wq[13][1]), "v"(xf[13][0]), "a"(wq[13][2])); SB();
  WAITL(0);
  SB();
  asm volatile("v_mfma_f32_16x16x32_bf16 %0, %9, %10, %0\n\tds_read_b128 %1, %11 offset:22528\n\tds_read_b128 %2, %12 offset:22528\n\tv_mfma_f32_16x16x32_bf16 %3, %13, %10, %3\n\tv_mfma_f32_16x16x32_bf16 %4, %14, %10, %4\n\tv_mfma_f32_16x16x32_bf16 %0, %15, %16, %0\n\tv_mfma_f32_16x16x32_bf16 %3, %17, %16, %3\n\tv_cvt_pk_bf16_f32 %5, %18, %19\n\tv_cvt_pk_bf16_f32 %6, %20, %21\n\tv_mfma_f32_16x16x32_bf16 %4, %22, %16, %4\n\tv_cvt_pk_bf16_f32 %7, %23, %24\n\tv_cvt_pk_bf16_f32 %8, %25, %26"
      : "+v"(ak[1]), "=&v"(xf[12][2]), "=&v"(xf[13][2]), "+v"(av[1]), "+v"(aq[0]), "=&v"(sg[0][0]), "=&v"(sg[0][1]), "=&v"(sg[1][0]), "=&v"(sg[1][1]) : "a"(wq[11][1]), "v"(xf[11][1]), "v"(xr0), "v"(xr1), "a"(wq[11][2]), "a"(wq[11][0]), "a"(wq[12][1]), "v"(xf[12][1]), "a"(wq[12][2]), "v"(ak[0][0]), "v"(ak[0][1]), "v"(ak[0][2]), "v"(ak[0][3]), "a"(wq[12][0]), "v"(av[0][0]), "v"(av[0][1]), "v"(av[0][2]), "v"(av[0][3]) : "memory"); SB();
  asm volatile("v_mfma_f32_16x16x32_bf16 %0, %3, %4, %0\n\tv_mfma_f32_16x16x32_bf16 %1, %5, %4, %1\n\tv_mfma_f32_16x16x32_bf16 %2, %6, %4, %2"
      : "+v"(ak[1]), "+v"(av[1]), "+v"(aq[0]) : "a"(wq[13][1]), "v"(xf[13][1]), "a"(wq[13][2]), "a"(wq[13][0])); SB();
  WAITL(0);
  SB();
  asm volatile("v_mfma_f32_16x16x32_bf16 %0, %7, %8, %0\n\tds_read_b128 %1, %9 offset:24576\n\tds_read_b128 %2, %10 offset:24576\n\tv_mfma_f32_16x16x32_bf16 %3, %11, %8, %3\n\tds_write_b64 %12, %13 offset:18432\n\tv_mfma_f32_16x16x32_bf16 %4, %14, %8, %4\n\tv_mfma_f32_16x16x32_bf16 %0, %15, %16, %0\n\tv_mfma_f32_16x16x32_bf16 %3, %17, %16, %3\n\tds_write_b64 %18, %19 offset:0\n\tv_mfma_f32_16x16x32_bf16 %4, %20, %16, %4\n\tv_cvt_pk_bf16_f32 %5, %21, %22\n\tv_cvt_pk_bf16_f32 %6, %23, %24\n\tv_mfma_f32_16x16x32_bf16 %0, %25, %26, %0"
      : "+v"(ak[2]), "=&v"(xf[12][3]), "=&v"(xf[13][3]), "+v"(av[2]), "+v"(aq[1]), "=&v"(sg[3][0]), "=&v"(sg[3][1]) : "a"(wq[11][1]), "v"(xf[11][2]), "v"(xr0), "v"(xr1), "a"(wq[11][2]), "v"(sk0), "v"(sg[0]), "a"(wq[11][0]), "a"(wq[12][1]), "v"(xf[12][2]), "a"(wq[12][2]), "v"(sv), "v"(sg[1]), "a"(wq[12][0]), "v"(ak[1][0]), "v"(ak[1][1]), "v"(ak[1][2]), "v"(ak[1][3]), "a"(wq[13][1]), "v"(xf[13][2]) : "memory"); SB();
  asm volatile("v_cvt_pk_bf16_f32 %0, %7, %8\n\tv_cvt_pk_bf16_f32 %1, %9, %10\n\tv_mfma_f32_16x16x32_bf16 %2, %11, %12, %2\n\tbuffer_load_dwordx4 %3, %13, %14, %15 offen offset:0 sc1\n\tv_cvt_pk_bf16_f32 %4, %16, %17\n\tv_cvt_pk_bf16_f32 %5, %18, %19\n\tv_mfma_f32_16x16x32_bf16 %6, %20, %12, %6"
      : "=&v"(sg[4][0]), "=&v"(sg[4][1]), "+v"(av[2]), "=&a"(wor[0]), "=&v"(sg[5][0]), "=&v"(sg[5][1]), "+v"(aq[1]) : "v"(av[1][0]), "v"(av[1][1]), "v"(av[1][2]), "v"(av[1][3]), "a"(wq[13][2]), "v"(xf[13][2]), "v"(vWo), "s"(rsW), "s"(sWq[0]), "v"(aq[0][0]), "v"(aq[0][1]), "v"(aq[0][2]), "v"(aq[0][3]), "a"(wq[13][0]) : "memory"); SB();
  WAITL(2);
  SB();
  asm volatile("v_mfma_f32_16x16x32_bf16 %0, %7, %8, %0\n\tds_read_b128 %1, %9 offset:26624\n\tds_read_b128 %2, %10 offset:26624\n\tv_mfma_f32_16x16x32_bf16 %3, %11, %8, %3\n\tds_write_b64 %12, %13 offset:18944\n\tv_mfma_f32_16x16x32_bf16 %4, %14, %8, %4\n\tbuffer_load_dwordx4 %5, %15, %16, %17 offen offset:1024 sc1\n\tv_mfma_f32_16x16x32_bf16 %0, %18, %19, %0\n\tv_mfma_f32_16x16x32_bf16 %3, %20, %19, %3\n\tds_write_b64 %21, %22 offset:256\n\tv_mfma_f32_16x16x32_bf16 %4, %23, %19, %4\n\tv_cvt_pk_bf16_f32 %6, %24, %25"
      : "+v"(ak[3]), "=&v"(xf[12][4]), "=&v"(xf[13][4]), "+v"(av[3]), "+v"(aq[2]), "=&a"(wor[1]), "=&v"(sg[6][0]) : "a"(wq[11][1]), "v"(xf[11][3]), "v"(xr0), "v"(xr1), "a"(wq[11][2]), "v"(sk1), "v"(sg[3]), "a"(wq[11][0]), "v"(vWo), "s"(rsW), "s"(sWq[0]), "a"(wq[12][1]), "v"(xf[12][3]), "a"(wq[12][2]), "v"(sv), "v"(sg[4]), "a"(wq[12][0]), "v"(ak[2][0]), "v"(ak[2][1]) : "memory"); SB();
  asm volatile("v_cvt_pk_bf16_f32 %0, %7, %8\n\tv_mfma_f32_16x16x32_bf16 %1, %9, %10, %1\n\tbuffer_load_dwordx4 %2, %11, %12, %13 offen offset:2048 sc1\n\tv_cvt_pk_bf16_f32 %3, %14, %15\n\tv_cvt_pk_bf16_f32 %4, %16, %17\n\tv_mfma_f32_16x16x32_bf16 %5, %18, %10, %5\n\tds_write_b64 %19, %20 offset:36864\n\tv_mfma_f32_16x16x32_bf16 %6, %21, %10, %6"
      : "=&v"(sg[6][1]), "+v"(ak[3]), "=&a"(wor[2]), "=&v"(sg[7][0]), "=&v"(sg[7][1]), "+v"(av[3]), "+v"(aq[2]) : "v"(ak[2][2]), "v"(ak[2][3]), "a"(wq[13][1]), "v"(xf[13][3]), "v"(vWo), "s"(rsW), "s"(sWq[0]), "v"(av[2][0]), "v"(av[2][1]), "v"(av[2][2]), "v"(av[2][3]), "a"(wq[13][2]), "v"(sk0), "v"(sg[5]), "a"(wq[13][0]) : "memory"); SB();
  WAITL(3);
  SB();
  asm volatile("v_mfma_f32_16x16x32_bf16 %0, %8, %9, %0\n\tds_read_b128 %1, %10 offset:28672\n\tds_read_b128 %2, %11 offset:28672\n\tv_mfma_f32_16x16x32_bf16 %3, %12, %9, %3\n\tbuffer_load_dwordx4 %4, %13, %14, %15 offen offset:3072 sc1\n\tds_write_b64 %16, %17 offset:19456\n\tv_cvt_pk_bf16_f32 %5, %18, %19\n\tv_cvt_pk_bf16_f32 %6, %20, %21\n\tv_mfma_f32_16x16x32_bf16 %7, %22, %9, %7\n\tv_mfma_f32_16x16x32_bf16 %0, %23, %24, %0\n\tv_mfma_f32_16x16x32_bf16 %3, %25, %24, %3"
      : "+v"(ak[4]), "=&v"(xf[12][5]), "=&v"(xf[13][5]), "+v"(av[4]), "=&a"(wor[3]), "=&v"(sg[8][0]), "=&v"(sg[8][1]), "+v"(aq[3]) : "a"(wq[11][1]), "v"(xf[11][4]), "v"(xr0), "v"(xr1), "a"(wq[11][2]), "v"(vWo), "s"(rsW), "s"(sWq[0]), "v"(sk0), "v"(sg[6]), "v"(aq[1][0]), "v"(aq[1][1]), "v"(aq[1][2]), "v"(aq[1][3]), "a"(wq[11][0]), "a"(wq[12][1]), "v"(xf[12][4]), "a"(wq[12][2]) : "memory"); SB();
  asm volatile("ds_write_b64 %8, %9 offset:512\n\tv_mfma_f32_16x16x32_bf16 %0, %10, %11, %0\n\tbuffer_load_dwordx4 %1, %12, %13, %14 offen offset:0 sc1\n\tv_cvt_pk_bf16_f32 %2, %15, %16\n\tv_cvt_pk_bf16_f32 %3, %17, %18\n\tv_mfma_f32_16x16x32_bf16 %4, %19, %20, %4\n\tv_cvt_pk_bf16_f32 %5, %21, %22\n\tv_cvt_pk_bf16_f32 %6, %23, %24\n\tv_mfma_f32_16x16x32_bf16 %7, %25, %20, %7"
      : "+v"(aq[3]), "=&a"(wor[4]), "=&v"(sg[9][0]), "=&v"(sg[9][1]), "+v"(ak[4]), "=&v"(sg[10][0]), "=&v"(sg[10][1]), "+v"(av[4]) : "v"(sv), "v"(sg[7]), "a"(wq[12][0]), "v"(xf[12][4]), "v"(vWo), "s"(rsW), "s"(sWq[1]), "v"(ak[3][0]), "v"(ak[3][1]), "v"(ak[3][2]), "v"(ak[3][3]), "a"(wq[13][1]), "v"(xf[13][4]), "v"(av[3][0]), "v"(av[3][1]), "v"(av[3][2]), "v"(av[3][3]), "a"(wq[13][2]) : "memory"); SB();
  asm volatile("ds_write_b64 %1, %2 offset:37376\n\tv_mfma_f32_16x16x32_bf16 %0, %3, %4, %0"
      : "+v"(aq[3]) : "v"(sk1), "v"(sg[8]), "a"(wq[13][0]), "v"(xf[13][4]) : "memory"); SB();
  WAITL(3);
  SB();
  asm volatile("v_mfma_f32_16x16x32_bf16 %0, %9, %10, %0\n\tbuffer_load_dwordx4 %1, %11, %12, %13 offen offset:1024 sc1\n\tds_read_b128 %2, %14 offset:30720\n\tds_read_b128 %3, %15 offset:30720\n\tv_mfma_f32_16x16x32_bf16 %4, %16, %10, %4\n\tds_write_b64 %17, %18 offset:19968\n\tv_cvt_pk_bf16_f32 %5, %19, %20\n\tv_cvt_pk_bf16_f32 %6, %21, %22\n\tv_mfma_f32_16x16x32_bf16 %7, %23, %10, %7\n\tv_mfma_f32_16x16x32_bf16 %0, %24, %25, %0\n\tv_mfma_f32_16x16x32_bf16 %4, %26, %25, %4\n\tbuffer_load_dwordx4 %8, %11, %12, %13 offen offset:2048 sc1"
      : "+v"(ak[5]), "=&a"(wor[5]), "=&v"(xf[12][6]), "=&v"(xf[13][6]), "+v"(av[5]), "=&v"(sg[11][0]), "=&v"(sg[11][1]), "+v"(aq[4]), "=&a"(wor[6]) : "a"(wq[11][1]), "v"(xf[11][5]), "v"(vWo), "s"(rsW), "s"(sWq[1]), "v"(xr0), "v"(xr1), "a"(wq[11][2]), "v"(sk1), "v"(sg[9]), "v"(aq[2][0]), "v"(aq[2][1]), "v"(aq[2][2]), "v"(aq[2][3]), "a"(wq[11][0]), "a"(wq[12][1]), "v"(xf[12][5]), "a"(wq[12][2]) : "memory"); SB();
  asm volatile("ds_write_b64 %7, %8 offset:768\n\tv_mfma_f32_16x16x32_bf16 %0, %9, %10, %0\n\tv_cvt_pk_bf16_f32 %1, %11, %12\n\tv_cvt_pk_bf16_f32 %2, %13, %14\n\tv_mfma_f32_16x16x32_bf16 %3, %15, %16, %3\n\tv_cvt_pk_bf16_f32 %4, %17, %18\n\tv_cvt_pk_bf16_f32 %5, %19, %20\n\tv_mfma_f32_16x16x32_bf16 %6, %21, %16, %6\n\tds_write_b64 %22, %23 offset:37888\n\tv_mfma_f32_16x16x32_bf16 %0, %24, %16, %0"
      : "+v"(aq[4]), "=&v"(sg[12][0]), "=&v"(sg[12][1]), "+v"(ak[5]), "=&v"(sg[13][0]), "=&v"(sg[13][1]), "+v"(av[5]) : "v"(sv), "v"(sg[10]), "a"(wq[12][0]), "v"(xf[12][5]), "v"(ak[4][0]), "v"(ak[4][1]), "v"(ak[4][2]), "v"(ak[4][3]), "a"(wq[13][1]), "v"(xf[13][5]), "v"(av[4][0]), "v"(av[4][1]), "v"(av[4][2]), "v"(av[4][3]), "a"(wq[13][2]), "v"(sk0), "v"(sg[11]), "a"(wq[13][0]) : "memory"); SB();
  asm volatile("buffer_load_dwordx4 %0, %1, %2, %3 offen offset:3072 sc1"
      : "=&a"(wor[7]) : "v"(vWo), "s"(rsW), "s"(sWq[1]) : "memory"); SB();
  WAITL(3);
  SB();
  asm volatile("v_mfma_f32_16x16x32_bf16 %0, %8, %9, %0\n\tds_read_b128 %1, %10 offset:32768\n\tds_read_b128 %2, %11 offset:32768\n\tv_mfma_f32_16x16x32_bf16 %3, %12, %9, %3\n\tds_write_b64 %13, %14 offset:20480\n\tv_cvt_pk_bf16_f32 %4, %15, %16\n\tv_cvt_pk_bf16_f32 %5, %17, %18\n\tv_mfma_f32_16x16x32_bf16 %6, %19, %9, %6\n\tv_mfma_f32_16x16x32_bf16 %0, %20, %21, %0\n\tbuffer_load_dwordx4 %7, %22, %23, %24 offen offset:0 sc1\n\tv_mfma_f32_16x16x32_bf16 %3, %25, %21, %3"
      : "+v"(ak[6]), "=&v"(xf[12][7]), "=&v"(xf[13][7]), "+v"(av[6]), "=&v"(sg[14][0]), "=&v"(sg[14][1]), "+v"(aq[5]), "=&a"(wor[8]) : "a"(wq[11][1]), "v"(xf[11][6]), "v"(xr0), "v"(xr1), "a"(wq[11][2]), "v"(sk0), "v"(sg[12]), "v"(aq[3][0]), "v"(aq[3][1]), "v"(aq[3][2]), "v"(aq[3][3]), "a"(wq[11][0]), "a"(wq[12][1]), "v"(xf[12][6]), "v"(vWo), "s"(rsW), "s"(sWq[2]), "a"(wq[12][2]) : "memory"); SB();
  asm volatile("ds_write_b64 %8, %9 offset:1024\n\tv_mfma_f32_16x16x32_bf16 %0, %10, %11, %0\n\tv_cvt_pk_bf16_f32 %1, %12, %13\n\tv_cvt_pk_bf16_f32 %2, %14, %15\n\tv_mfma_f32_16x16x32_bf16 %3, %16, %17, %3\n\tv_cvt_pk_bf16_f32 %4, %18, %19\n\tv_cvt_pk_bf16_f32 %5, %20, %21\n\tv_mfma_f32_16x16x32_bf16 %6, %22, %17, %6\n\tbuffer_load_dwordx4 %7, %23, %24, %25 offen offset:1024 sc1"
      : "+v"(aq[5]), "=&v"(sg[15][0]), "=&v"(sg[15][1]), "+v"(ak[6]), "=&v"(sg[16][0]), "=&v"(sg[16][1]), "+v"(av[6]), "=&a"(wor[9]) : "v"(sv), "v"(sg[13]), "a"(wq[12][0]), "v"(xf[12][6]), "v"(ak[5][0]), "v"(ak[5][1]), "v"(ak[5][2]), "v"(ak[5][3]), "a"(wq[13][1]), "v"(xf[13][6]), "v"(av[5][0]), "v"(av[5][1]), "v"(av[5][2]), "v"(av[5][3]), "a"(wq[13][2]), "v"(vWo), "s"(rsW), "s"(sWq[2]) : "memory"); SB();
  asm volatile("ds_write_b64 %1, %2 offset:38400\n\tv_mfma_f32_16x16x32_bf16 %0, %3, %4, %0"
      : "+v"(aq[5]) : "v"(sk1), "v"(sg[14]), "a"(wq[13][0]), "v"(xf[13][6]) : "memory"); SB();
  WAITL(3);
  SB();
  asm volatile("v_mfma_f32_16x16x32_bf16 %0, %8, %9, %0\n\tds_read_b128 %1, %10 offset:34816\n\tds_read_b128 %2, %11 offset:34816\n\tv_mfma_f32_16x16x32_bf16 %3, %12, %9, %3\n\tds_write_b64 %13, %14 offset:20992\n\tv_cvt_pk_bf16_f32 %4, %15, %16\n\tv_cvt_pk_bf16_f32 %5, %17, %18\n\tv_mfma_f32_16x16x32_bf16 %6, %19, %9, %6\n\tbuffer_load_dwordx4 %7, %20, %21, %22 offen offset:2048 sc1\n\tv_mfma_f32_16x16x32_bf16 %0, %23, %24, %0\n\tv_mfma_f32_16x16x32_bf16 %3, %25, %24, %3"
      : "+v"(ak[7]), "=&v"(xf[12][8]), "=&v"(xf[13][8]), "+v"(av[7]), "=&v"(sg[17][0]), "=&v"(sg[17][1]), "+v"(aq[6]), "=&a"(wor[10]) : "a"(wq[11][1]), "v"(xf[11][7]), "v"(xr0), "v"(xr1), "a"(wq[11][2]), "v"(sk1), "v"(sg[15]), "v"(aq[4][0]), "v"(aq[4][1]), "v"(aq[4][2]), "v"(aq[4][3]), "a"(wq[11][0]), "v"(vWo), "s"(rsW), "s"(sWq[2]), "a"(wq[12][1]), "v"(xf[12][7]), "a"(wq[12][2]) : "memory"); SB();
  asm volatile("ds_write_b64 %8, %9 offset:1280\n\tv_mfma_f32_16x16x32_bf16 %0, %10, %11, %0\n\tv_cvt_pk_bf16_f32 %1, %12, %13\n\tv_cvt_pk_bf16_f32 %2, %14, %15\n\tv_mfma_f32_16x16x32_bf16 %3, %16, %17, %3\n\tbuffer_load_dwordx4 %4, %18, %19, %20 offen offset:3072 sc1\n\tv_cvt_pk_bf16_f32 %5, %21, %22\n\tv_cvt_pk_bf16_f32 %6, %23, %24\n\tv_mfma_f32_16x16x32_bf16 %7, %25, %17, %7"
      : "+v"(aq[6]), "=&v"(sg[18][0]), "=&v"(sg[18][1]), "+v"(ak[7]), "=&a"(wor[11]), "=&v"(sg[19][0]), "=&v"(sg[19][1]), "+v"(av[7]) : "v"(sv), "v"(sg[16]), "a"(wq[12][0]), "v"(xf[12][7]), "v"(ak[6][0]), "v"(ak[6][1]), "v"(ak[6][2]), "v"(ak[6][3]), "a"(wq[13][1]), "v"(xf[13][7]), "v"(vWo), "s"(rsW), "s"(sWq[2]), "v"(av[6][0]), "v"(av[6][1]), "v"(av[6][2]), "v"(av[6][3]), "a"(wq[13][2]) : "memory"); SB();
  asm volatile("ds_write_b64 %1, %2 offset:38912\n\tv_mfma_f32_16x16x32_bf16 %0, %3, %4, %0"
      : "+v"(aq[6]) : "v"(sk0), "v"(sg[17]), "a"(wq[13][0]), "v"(xf[13][7]) : "memory"); SB();
  WAITL(3);
  SB();
  asm volatile("v_mfma_f32_16x16x32_bf16 %0, %7, %8, %0\n\tv_cvt_pk_bf16_f32 %1, %9, %10\n\tv_cvt_pk_bf16_f32 %2, %11, %12\n\tv_mfma_f32_16x16x32_bf16 %3, %13, %8, %3\n\tbuffer_load_dwordx4 %4, %14, %15, %16 offen offset:0 sc1\n\tds_write_b64 %17, %18 offset:21504\n\tv_mfma_f32_16x16x32_bf16 %5, %19, %8, %5\n\tv_mfma_f32_16x16x32_bf16 %0, %20, %21, %0\n\tv_mfma_f32_16x16x32_bf16 %3, %22, %21, %3\n\tds_write_b64 %23, %24 offset:1536\n\tv_mfma_f32_16x16x32_bf16 %5, %25, %21, %5\n\tbuffer_load_dwordx4 %6, %14, %15, %16 offen offset:1024 sc1"
      : "+v"(ak[8]), "=&v"(sg[20][0]), "=&v"(sg[20][1]), "+v"(av[8]), "=&a"(wor[12]), "+v"(aq[7]), "=&a"(wor[13]) : "a"(wq[11][1]), "v"(xf[11][8]), "v"(aq[5][0]), "v"(aq[5][1]), "v"(aq[5][2]), "v"(aq[5][3]), "a"(wq[11][2]), "v"(vWo), "s"(rsW), "s"(sWq[3]), "v"(sk0), "v"(sg[18]), "a"(wq[11][0]), "a"(wq[12][1]), "v"(xf[12][8]), "a"(wq[12][2]), "v"(sv), "v"(sg[19]), "a"(wq[12][0]) : "memory"); SB();
  asm volatile("v_cvt_pk_bf16_f32 %0, %9, %10\n\tv_cvt_pk_bf16_f32 %1, %11, %12\n\tv_mfma_f32_16x16x32_bf16 %2, %13, %14, %2\n\tv_cvt_pk_bf16_f32 %3, %15, %16\n\tv_cvt_pk_bf16_f32 %4, %17, %18\n\tv_mfma_f32_16x16x32_bf16 %5, %19, %14, %5\n\tds_write_b64 %20, %21 offset:39424\n\tv_mfma_f32_16x16x32_bf16 %6, %22, %14, %6\n\tv_cvt_pk_bf16_f32 %7, %23, %24\n\tv_cvt_pk_bf16_f32 %8, %25, %26\n\ts_nop 5"
      : "=&v"(sg[21][0]), "=&v"(sg[21][1]), "+v"(ak[8]), "=&v"(sg[22][0]), "=&v"(sg[22][1]), "+v"(av[8]), "+v"(aq[7]), "=&v"(sg[23][0]), "=&v"(sg[23][1]) : "v"(ak[7][0]), "v"(ak[7][1]), "v"(ak[7][2]), "v"(ak[7][3]), "a"(wq[13][1]), "v"(xf[13][8]), "v"(av[7][0]), "v"(av[7][1]), "v"(av[7][2]), "v"(av[7][3]), "a"(wq[13][2]), "v"(sk1), "v"(sg[20]), "a"(wq[13][0]), "v"(aq[6][0]), "v"(aq[6][1]), "v"(aq[6][2]), "v"(aq[6][3]) : "memory"); SB();
  asm volatile("ds_write_b64 %6, %7 offset:22016\n\tds_write_b64 %8, %9 offset:1792\n\tds_write_b64 %10, %11 offset:39936\n\tv_cvt_pk_bf16_f32 %0, %12, %13\n\tv_cvt_pk_bf16_f32 %1, %14, %15\n\tv_cvt_pk_bf16_f32 %2, %16, %17\n\tv_cvt_pk_bf16_f32 %3, %18, %19\n\tv_cvt_pk_bf16_f32 %4, %20, %21\n\tv_cvt_pk_bf16_f32 %5, %22, %23"
      : "=&v"(sg[24][0]), "=&v"(sg[24][1]), "=&v"(sg[25][0]), "=&v"(sg[25][1]), "=&v"(sg[26][0]), "=&v"(sg[26][1]) : "v"(sk1), "v"(sg[21]), "v"(sv), "v"(sg[22]), "v"(sk0), "v"(sg[23]), "v"(ak[8][0]), "v"(ak[8][1]), "v"(ak[8][2]), "v"(ak[8][3]), "v"(av[8][0]), "v"(av[8][1]), "v"(av[8][2]), "v"(av[8][3]), "v"(aq[7][0]), "v"(aq[7][1]), "v"(aq[7][2]), "v"(aq[7][3]) : "memory"); SB();
  asm volatile("ds_write_b64 %0, %1 offset:22528\n\tds_write_b64 %2, %3 offset:2048\n\tds_write_b64 %4, %5 offset:40448"
      :  : "v"(sk0), "v"(sg[24]), "v"(sv), "v"(sg[25]), "v"(sk1), "v"(sg[26]) : "memory"); SB();
  BAR();
  STAMP(14);
  unsigned fu; u32x4 qf[2][2]; bf16x8 kf[2][2][2], vf[2][2][2];
  qf[0][0] = *(LDS_AS const u32x4*)(rk0 + SQ + 512 + 0); kf[0][0][0] = *(LDS_AS const bf16x8*)(rk0 + SK + 0); kf[0][0][1] = *(LDS_AS const bf16x8*)(rk0 + SK + 1024);
  qf[0][1] = *(LDS_AS const u32x4*)(rk0 + SQ + 512 + 9216); kf[0][1][0] = *(LDS_AS const bf16x8*)(rk0 + SK + 9216); kf[0][1][1] = *(LDS_AS const bf16x8*)(rk0 + SK + 10240);
  qf[1][0] = *(LDS_AS const u32x4*)(rk1 + SQ + 512 + 0); kf[1][0][0] = *(LDS_AS const bf16x8*)(rk1 + SK + 0); kf[1][0][1] = *(LDS_AS const bf16x8*)(rk1 + SK + 1024);
  qf[1][1] = *(LDS_AS const u32x4*)(rk1 + SQ + 512 + 9216); kf[1][1][0] = *(LDS_AS const bf16x8*)(rk1 + SK + 9216); kf[1][1][1] = *(LDS_AS const bf16x8*)(rk1 + SK + 10240);
  SB();
  asm volatile("s_add_u32 m0, %0, 0\n\ts_nop 0\n\tbuffer_load_dwordx4 %1, %2, %3 offen offset:0  lds\n\tbuffer_load_dwordx4 %4, %2, %3 offen offset:1024  lds\n\tbuffer_load_dwordx4 %5, %2, %3 offen offset:2048  lds\n\tbuffer_load_dwordx4 %6, %2, %3 offen offset:3072  lds"
      :  : "s"(ldsO), "v"(vW2[0]), "s"(rs2), "s"(sw[2]), "v"(vW2[1]), "v"(vW2[2]), "v"(vW2[3]) : "memory", "scc"); SB();
  f32x4 sc[2][2][2];
  asm volatile("v_mfma_f32_16x16x32_bf16 %0, %2, %3, 0\n\tv_mfma_f32_16x16x32_bf16 %1, %4, %3, 0"
      : "=&v"(sc[0][0][0]), "=&v"(sc[0][0][1]) : "v"(kf[0][0][0]), "v"(qf[0][0]), "v"(kf[0][0][1])); SB();
  vf[0][0][0] = tr8u(rv0 + 0, 512);
  vf[0][0][1] = tr8u(rv0 + 4608, 512);
  SB();
  asm volatile("v_mfma_f32_16x16x32_bf16 %0, %2, %3, 0\n\tv_mfma_f32_16x16x32_bf16 %1, %4, %3, 0"
      : "=&v"(sc[0][1][0]), "=&v"(sc[0][1][1]) : "v"(kf[0][1][0]), "v"(qf[0][1]), "v"(kf[0][1][1])); SB();
  vf[0][1][0] = tr8u(rv0 + 9216, 512);
  vf[0][1][1] = tr8u(rv0 + 13824, 512);
  SB();
  asm volatile("v_mfma_f32_16x16x32_bf16 %0, %2, %3, 0\n\tv_mfma_f32_16x16x32_bf16 %1, %4, %3, 0"
      : "=&v"(sc[1][0][0]), "=&v"(sc[1][0][1]) : "v"(kf[1][0][0]), "v"(qf[1][0]), "v"(kf[1][0][1])); SB();
  vf[1][0][0] = tr8u(rv1 + 0, 512);
  vf[1][0][1] = tr8u(rv1 + 4608, 512);
  SB();
  asm volatile("v_mfma_f32_16x16x32_bf16 %0, %2, %3, 0\n\tv_mfma_f32_16x16x32_bf16 %1, %4, %3, 0"
      : "=&v"(sc[1][1][0]), "=&v"(sc[1][1][1]) : "v"(kf[1][1][0]), "v"(qf[1][1]), "v"(kf[1][1][1])); SB();
  vf[1][1][0] = tr8u(rv1 + 9216, 512);
  vf[1][1][1] = tr8u(rv1 + 13824, 512);
  SB();
  SB();
  asm volatile("s_add_u32 m0, %0, 4096\n\ts_nop 0\n\tbuffer_load_dwordx4 %1, %2, %3 offen offset:0  lds\n\tbuffer_load_dwordx4 %4, %2, %3 offen offset:1024  lds\n\tbuffer_load_dwordx4 %5, %2, %3 offen offset:2048  lds\n\tbuffer_load_dwordx4 %6, %2, %3 offen offset:3072  lds"
      :  : "s"(ldsO), "v"(vW2[0]), "s"(rs2), "s"(sw[3]), "v"(vW2[1]), "v"(vW2[2]), "v"(vW2[3]) : "memory", "scc"); SB();
  f32x2_t lg[2][2][2], mx[2], rs[2]; u32x4 pf[2][2]; f32x4 oo[2][2][2];
  const f32x2_t scale = {0.18898223650461362f, 0.18898223650461362f};
#define SEL(p, h, i) __uint_as_float((m1[i] & __float_as_uint(sc[p][h][1][i])) | (~m1[i] & __float_as_uint(sc[p][h][0][i])))
  lg[0][0][0] = rbf2(__builtin_elementwise_fma(rbf2((f32x2_t){SEL(0, 0, 0), SEL(0, 0, 1)}), scale, (f32x2_t){cm[0], cm[1]}));
  lg[0][0][1] = rbf2(__builtin_elementwise_fma(rbf2((f32x2_t){SEL(0, 0, 2), SEL(0, 0, 3)}), scale, (f32x2_t){cm[2], cm[3]}));
  OPV(lg[0][0][0]); OPV(lg[0][0][1]);
  SB();
  asm volatile("s_add_u32 m0, %0, 0\n\ts_nop 0\n\tbuffer_load_dwordx4 %1, %2, %3 offen offset:0  lds\n\tbuffer_load_dwordx4 %4, %2, %3 offen offset:1024  lds\n\tbuffer_load_dwordx4 %5, %2, %3 offen offset:2048  lds\n\tbuffer_load_dwordx4 %6, %2, %3 offen offset:3072  lds"
      :  : "s"(ldsS), "v"(vW2[0]), "s"(rs2), "s"(sw[4]), "v"(vW2[1]), "v"(vW2[2]), "v"(vW2[3]) : "memory", "scc"); SB();
  lg[0][1][0] = rbf2(__builtin_elementwise_fma(rbf2((f32x2_t){SEL(0, 1, 0), SEL(0, 1, 1)}), scale, (f32x2_t){cm[0], cm[1]}));
  lg[0][1][1] = rbf2(__builtin_elementwise_fma(rbf2((f32x2_t){SEL(0, 1, 2), SEL(0, 1, 3)}), scale, (f32x2_t){cm[2], cm[3]}));
  OPV(lg[0][1][0]); OPV(lg[0][1][1]);
  SB();
  asm volatile("s_waitcnt vmcnt(31)"
      :  :  : "memory"); SB();
  fw2[0][0] = tr8u(tS[0] + 18432, 512);
  fw2[0][1] = tr8u(tS[1] + 18432, 512);
  fw2[0][2] = tr8u(tS[2] + 18432, 512);
  fw2[0][3] = tr8u(tS[3] + 18432, 512);
  SB();
  lg[1][0][0] = rbf2(__builtin_elementwise_fma(rbf2((f32x2_t){SEL(1, 0, 0), SEL(1, 0, 1)}), scale, (f32x2_t){cm[0], cm[1]}));
  lg[1][0][1] = rbf2(__builtin_elementwise_fma(rbf2((f32x2_t){SEL(1, 0, 2), SEL(1, 0, 3)}), scale, (f32x2_t){cm[2], cm[3]}));
  OPV(lg[1][0][0]); OPV(lg[1][0][1]);
  SB();
  WAITL(0);
  asm volatile("s_add_u32 m0, %0, 18432\n\ts_nop 0\n\tbuffer_load_dwordx4 %1, %2, %3 offen offset:0  lds\n\tbuffer_load_dwordx4 %4, %2, %3 offen offset:1024  lds\n\tbuffer_load_dwordx4 %5, %2, %3 offen offset:2048  lds\n\tbuffer_load_dwordx4 %6, %2, %3 offen offset:3072  lds"
      :  : "s"(ldsS), "v"(vW2[0]), "s"(rs2), "s"(sw[5]), "v"(vW2[1]), "v"(vW2[2]), "v"(vW2[3]) : "memory", "scc"); SB();
  lg[1][1][0] = rbf2(__builtin_elementwise_fma(rbf2((f32x2_t){SEL(1, 1, 0), SEL(1, 1, 1)}), scale, (f32x2_t){cm[0], cm[1]}));
  lg[1][1][1] = rbf2(__builtin_elementwise_fma(rbf2((f32x2_t){SEL(1, 1, 2), SEL(1, 1, 3)}), scale, (f32x2_t){cm[2], cm[3]}));
  OPV(lg[1][1][0]); OPV(lg[1][1][1]);
  SB();
  asm volatile("s_waitcnt vmcnt(31)"
      :  :  : "memory"); SB();
  fw2[1][0] = tr8u(tS[0] + 34816, 512);
  fw2[1][1] = tr8u(tS[1] + 34816, 512);
  fw2[1][2] = tr8u(tS[2] + 34816, 512);
  fw2[1][3] = tr8u(tS[3] + 34816, 512);
  SB();
  #pragma unroll
  for (int p = 0; p < 2; ++p)
    mx[p] = xbcast2(xmax2(fmx(fmx(lg[p][0][0][0], lg[p][0][0][1]), fmx(lg[p][0][1][0], lg[p][0][1][1])), fmx(fmx(lg[p][1][0][0], lg[p][1][0][1]), fmx(lg[p][1][1][0], lg[p][1][1][1]))));
  OPV(mx[0]); OPV(mx[1]);
  SB();
  WAITL(0);
  asm volatile("s_add_u32 m0, %0, 34816\n\ts_nop 0\n\tbuffer_load_dwordx4 %1, %2, %3 offen offset:0  lds\n\tbuffer_load_dwordx4 %4, %2, %3 offen offset:1024  lds\n\tbuffer_load_dwordx4 %5, %2, %3 offen offset:2048  lds\n\tbuffer_load_dwordx4 %6, %2, %3 offen offset:3072  lds\n\ts_waitcnt vmcnt(16)"
      :  : "s"(ldsS), "v"(vW2[0]), "s"(rs2), "s"(sw[6]), "v"(vW2[1]), "v"(vW2[2]), "v"(vW2[3]) : "memory", "scc"); SB();
  fw2[2][0] = tr8u(tO[0] + 0, 512);
  fw2[2][1] = tr8u(tO[1] + 0, 512);
  fw2[2][2] = tr8u(tO[2] + 0, 512);
  fw2[2][3] = tr8u(tO[3] + 0, 512);
  SB();
  #pragma unroll
  for (int j = 0; j < 2; ++j) {
    const f32x2_t z = (lg[0][0][j] - (f32x2_t){mx[0][0], mx[0][0]}) * (f32x2_t){1.442695041f, 1.442695041f};
    lg[0][0][j] = (f32x2_t){__builtin_amdgcn_exp2f(z[0]), __builtin_amdgcn_exp2f(z[1])}; OPV(lg[0][0][j]);
  }
  SB();
  WAITL(0);
  asm volatile("s_add_u32 m0, %0, 0\n\ts_nop 0\n\tbuffer_load_dwordx4 %1, %2, %3 offen offset:0  lds\n\tbuffer_load_dwordx4 %4, %2, %3 offen offset:1024  lds\n\tbuffer_load_dwordx4 %5, %2, %3 offen offset:2048  lds\n\tbuffer_load_dwordx4 %6, %2, %3 offen offset:3072  lds"
      :  : "s"(ldsO), "v"(vW2[0]), "s"(rs2), "s"(sw[7]), "v"(vW2[1]), "v"(vW2[2]), "v"(vW2[3]) : "memory", "scc"); SB();
  #pragma unroll
  for (int j = 0; j < 2; ++j) {
    const f32x2_t z = (lg[0][1][j] - (f32x2_t){mx[0][1], mx[0][1]}) * (f32x2_t){1.442695041f, 1.442695041f};
    lg[0][1][j] = (f32x2_t){__builtin_amdgcn_exp2f(z[0]), __builtin_amdgcn_exp2f(z[1])}; OPV(lg[0][1][j]);
  }
  SB();
  asm volatile("s_waitcnt vmcnt(16)"
      :  :  : "memory"); SB();
  fw2[3][0] = tr8u(tO[0] + 4096, 512);
  fw2[3][1] = tr8u(tO[1] + 4096, 512);
  fw2[3][2] = tr8u(tO[2] + 4096, 512);
  fw2[3][3] = tr8u(tO[3] + 4096, 512);
  SB();
  #pragma unroll
  for (int j = 0; j < 2; ++j) {
    const f32x2_t z = (lg[1][0][j] - (f32x2_t){mx[1][0], mx[1][0]}) * (f32x2_t){1.442695041f, 1.442695041f};
    lg[1][0][j] = (f32x2_t){__builtin_amdgcn_exp2f(z[0]), __builtin_amdgcn_exp2f(z[1])}; OPV(lg[1][0][j]);
  }
  SB();
  WAITL(0);
  asm volatile("s_add_u32 m0, %0, 4096\n\ts_nop 0\n\tbuffer_load_dwordx4 %1, %2, %3 offen offset:0  lds\n\tbuffer_load_dwordx4 %4, %2, %3 offen offset:1024  lds\n\tbuffer_load_dwordx4 %5, %2, %3 offen offset:2048  lds\n\tbuffer_load_dwordx4 %6, %2, %3 offen offset:3072  lds"
      :  : "s"(ldsO), "v"(vW2[0]), "s"(rs2), "s"(sw[8]), "v"(vW2[1]), "v"(vW2[2]), "v"(vW2[3]) : "memory", "scc"); SB();
  #pragma unroll
  for (int j = 0; j < 2; ++j) {
    const f32x2_t z = (lg[1][1][j] - (f32x2_t){mx[1][1], mx[1][1]}) * (f32x2_t){1.442695041f, 1.442695041f};
    lg[1][1][j] = (f32x2_t){__builtin_amdgcn_exp2f(z[0]), __builtin_amdgcn_exp2f(z[1])}; OPV(lg[1][1][j]);
  }
  SB();
  asm volatile("s_waitcnt vmcnt(16)"
      :  :  : "memory"); SB();
  fw2[4][0] = tr8u(tS[0] + 0, 512);
  fw2[4][1] = tr8u(tS[1] + 0, 512);
  fw2[4][2] = tr8u(tS[2] + 0, 512);
  fw2[4][3] = tr8u(tS[3] + 0, 512);
  SB();
  #pragma unroll
  for (int p = 0; p < 2; ++p) {
    const f32x2_t e0 = lg[p][0][0] + lg[p][0][1], e1 = lg[p][1][0] + lg[p][1][1];
    rs[p] = xbcast2(__builtin_amdgcn_rcpf(xsum2(e0[0] + e0[1], e1[0] + e1[1])));
  }
  OPV(rs[0]); OPV(rs[1]);
  SB();
  WAITL(0);
  asm volatile("s_add_u32 m0, %0, 0\n\ts_nop 0\n\tbuffer_load_dwordx4 %1, %2, %3 offen offset:0  lds\n\tbuffer_load_dwordx4 %4, %2, %3 offen offset:1024  lds\n\tbuffer_load_dwordx4 %5, %2, %3 offen offset:2048  lds\n\tbuffer_load_dwordx4 %6, %2, %3 offen offset:3072  lds\n\ts_waitcnt vmcnt(16)"
      :  : "s"(ldsS), "v"(vW2[0]), "s"(rs2), "s"(sw[9]), "v"(vW2[1]), "v"(vW2[2]), "v"(vW2[3]) : "memory", "scc"); SB();
  fw2[5][0] = tr8u(tS[0] + 18432, 512);
  fw2[5][1] = tr8u(tS[1] + 18432, 512);
  fw2[5][2] = tr8u(tS[2] + 18432, 512);
  fw2[5][3] = tr8u(tS[3] + 18432, 512);
  SB();
  { const f32x2_t pa = lg[0][0][0] * (f32x2_t){rs[0][0], rs[0][0]}, pb = lg[0][0][1] * (f32x2_t){rs[0][0], rs[0][0]};
    const unsigned wa = pack2(pa[0], pa[1]), wb = pack2(pb[0], pb[1]);
    pf[0][0] = (u32x4){wa & ~mp[0], wb & ~mp[1], wa & mp[0], wb & mp[1]}; OPV(pf[0][0]); }
  { const f32x2_t pa = lg[0][1][0] * (f32x2_t){rs[0][1], rs[0][1]}, pb = lg[0][1][1] * (f32x2_t){rs[0][1], rs[0][1]};
    const unsigned wa = pack2(pa[0], pa[1]), wb = pack2(pb[0], pb[1]);
    pf[0][1] = (u32x4){wa & ~mp[0], wb & ~mp[1], wa & mp[0], wb & mp[1]}; OPV(pf[0][1]); }
  SB();
  WAITL(0);
  asm volatile("s_add_u32 m0, %4, 18432\n\ts_nop 0\n\tbuffer_load_dwordx4 %5, %6, %7 offen offset:0  lds\n\tbuffer_load_dwordx4 %8, %6, %7 offen offset:1024  lds\n\tbuffer_load_dwordx4 %9, %6, %7 offen offset:2048  lds\n\tbuffer_load_dwordx4 %10, %6, %7 offen offset:3072  lds\n\ts_nop 1\n\tv_mfma_f32_16x16x32_bf16 %0, %11, %12, 0\n\tv_mfma_f32_16x16x32_bf16 %1, %13, %14, 0\n\tv_mfma_f32_16x16x32_bf16 %2, %15, %12, 0\n\tv_mfma_f32_16x16x32_bf16 %3, %16, %14, 0"
      : "=&v"(oo[0][0][0]), "=&v"(oo[0][1][0]), "=&v"(oo[0][0][1]), "=&v"(oo[0][1][1]) : "s"(ldsS), "v"(vW2[0]), "s"(rs2), "s"(sw[10]), "v"(vW2[1]), "v"(vW2[2]), "v"(vW2[3]), "v"(vf[0][0][0]), "v"(pf[0][0]), "v"(vf[0][1][0]), "v"(pf[0][1]), "v"(vf[0][0][1]), "v"(vf[0][1][1]) : "memory", "scc"); SB();
  { const f32x2_t pa = lg[1][0][0] * (f32x2_t){rs[1][0], rs[1][0]}, pb = lg[1][0][1] * (f32x2_t){rs[1][0], rs[1][0]};
    const unsigned wa = pack2(pa[0], pa[1]), wb = pack2(pb[0], pb[1]);
    pf[1][0] = (u32x4){wa & ~mp[0], wb & ~mp[1], wa & mp[0], wb & mp[1]}; OPV(pf[1][0]); }
  { const f32x2_t pa = lg[1][1][0] * (f32x2_t){rs[1][1], rs[1][1]}, pb = lg[1][1][1] * (f32x2_t){rs[1][1], rs[1][1]};
    const unsigned wa = pack2(pa[0], pa[1]), wb = pack2(pb[0], pb[1]);
    pf[1][1] = (u32x4){wa & ~mp[0], wb & ~mp[1], wa & mp[0], wb & mp[1]}; OPV(pf[1][1]); }
  SB();
  asm volatile("s_waitcnt vmcnt(16)"
      :  :  : "memory"); SB();
  fw2[6][0] = tr8u(tS[0] + 34816, 512);
  fw2[6][1] = tr8u(tS[1] + 34816, 512);
  fw2[6][2] = tr8u(tS[2] + 34816, 512);
  fw2[6][3] = tr8u(tS[3] + 34816, 512);
  SB();
  asm volatile("s_nop 1\n\tv_mfma_f32_16x16x32_bf16 %0, %4, %5, 0\n\tv_mfma_f32_16x16x32_bf16 %1, %6, %7, 0\n\tv_mfma_f32_16x16x32_bf16 %2, %8, %5, 0\n\tv_mfma_f32_16x16x32_bf16 %3, %9, %7, 0"
      : "=&v"(oo[1][0][0]), "=&v"(oo[1][1][0]), "=&v"(oo[1][0][1]), "=&v"(oo[1][1][1]) : "v"(vf[1][0][0]), "v"(pf[1][0]), "v"(vf[1][1][0]), "v"(pf[1][1]), "v"(vf[1][0][1]), "v"(vf[1][1][1])); SB();
  HS16(vS, sO, 0, ((u32x4){pack2(oo[0][0][0][0], oo[0][0][0][1]), pack2(oo[0][0][0][2], oo[0][0][0][3]), pack2(oo[0][1][0][0], oo[0][1][0][1]), pack2(oo[0][1][0][2], oo[0][1][0][3])})); SB();
  HS16H(0xffff, vS, sO, 128, ((u32x4){pack2(oo[0][0][1][0], oo[0][0][1][1]), pack2(oo[0][0][1][2], oo[0][0][1][3]), pack2(oo[0][1][1][0], oo[0][1][1][1]), pack2(oo[0][1][1][2], oo[0][1][1][3])})); SB();
  HS16(vS, sO, 1024, ((u32x4){pack2(oo[1][0][0][0], oo[1][0][0][1]), pack2(oo[1][0][0][2], oo[1][0][0][3]), pack2(oo[1][1][0][0], oo[1][1][0][1]), pack2(oo[1][1][0][2], oo[1][1][0][3])})); SB();
  HS16H(0xffff, vS, sO, 1152, ((u32x4){pack2(oo[1][0][1][0], oo[1][0][1][1]), pack2(oo[1][0][1][2], oo[1][0][1][3]), pack2(oo[1][1][1][0], oo[1][1][1][1]), pack2(oo[1][1][1][2], oo[1][1][1][3])})); SB();
  STAMP(15);
  asm volatile("s_waitcnt vmcnt(16)"
      :  :  : "memory"); SB();
  fw2[7][0] = tr8u(tO[0] + 0, 512);
  fw2[7][1] = tr8u(tO[1] + 0, 512);
  fw2[7][2] = tr8u(tO[2] + 0, 512);
  fw2[7][3] = tr8u(tO[3] + 0, 512);
  WAITL(8);
  asm volatile("s_add_u32 m0, %0, 34816\n\ts_nop 0\n\tbuffer_load_dwordx4 %1, %2, %3 offen offset:0  lds\n\tbuffer_load_dwordx4 %4, %2, %3 offen offset:1024  lds\n\tbuffer_load_dwordx4 %5, %2, %3 offen offset:2048  lds\n\tbuffer_load_dwordx4 %6, %2, %3 offen offset:3072  lds\n\ts_waitcnt vmcnt(16)"
      :  : "s"(ldsS), "v"(vW2[0]), "s"(rs2), "s"(sw[11]), "v"(vW2[1]), "v"(vW2[2]), "v"(vW2[3]) : "memory", "scc"); SB();
  fw2[8][0] = tr8u(tO[0] + 4096, 512);
  fw2[8][1] = tr8u(tO[1] + 4096, 512);
  fw2[8][2] = tr8u(tO[2] + 4096, 512);
  fw2[8][3] = tr8u(tO[3] + 4096, 512);
  WAITL(8);
  asm volatile("s_add_u32 m0, %1, 0\n\ts_nop 0\n\tbuffer_load_dwordx4 %2, %3, %4 offen offset:0  lds\n\tbuffer_load_dwordx4 %5, %3, %4 offen offset:1024  lds\n\tbuffer_load_dwordx4 %6, %3, %4 offen offset:2048  lds\n\tbuffer_load_dwordx4 %7, %3, %4 offen offset:3072  lds\n\tbuffer_load_dword %0, %8, %9, 0 offen sc1\n\ts_waitcnt vmcnt(17)"
      : "=&v"(fu) : "s"(ldsO), "v"(vW2[0]), "s"(rs2), "s"(sw[12]), "v"(vW2[1]), "v"(vW2[2]), "v"(vW2[3]), "v"(vFU1), "s"(rsW) : "memory", "scc"); SB();
  fw2[9][0] = tr8u(tS[0] + 0, 512);
  fw2[9][1] = tr8u(tS[1] + 0, 512);
  fw2[9][2] = tr8u(tS[2] + 0, 512);
  fw2[9][3] = tr8u(tS[3] + 0, 512);
  WAITL(8);
  asm volatile("s_add_u32 m0, %0, 4096\n\ts_nop 0\n\tbuffer_load_dwordx4 %1, %2, %3 offen offset:0  lds\n\tbuffer_load_dwordx4 %4, %2, %3 offen offset:1024  lds\n\tbuffer_load_dwordx4 %5, %2, %3 offen offset:2048  lds\n\tbuffer_load_dwordx4 %6, %2, %3 offen offset:3072  lds"
      :  : "s"(ldsO), "v"(vW2[0]), "s"(rs2), "s"(sw[13]), "v"(vW2[1]), "v"(vW2[2]), "v"(vW2[3]) : "memory", "scc"); SB();
  STAMP(16);
  {
  bf16x8 pu[14], fwo[56];
  f32x4 ac[2][4], as[4], py[4][4];
  float ye[4], ze[4], ge[16], xe[16], bias;
  u32x2 ov[4]; unsigned fo;
  WAITV1(4, fu);
  STFO(sFO);
  if (__ballot(fu != nonce) != 0ull) poll1<4>(rsW, vFU1, nonce);
  STAMP(17);
  asm volatile("buffer_load_dwordx4 %0, %10, %11, %12 offen offset:0 sc1\n\tbuffer_load_dwordx4 %1, %10, %11, %13 offen offset:0 sc1\n\tbuffer_load_dwordx4 %2, %10, %11, %14 offen offset:0 sc1\n\tbuffer_load_dwordx4 %3, %10, %11, %15 offen offset:0 sc1\n\tbuffer_load_dwordx4 %4, %10, %11, %16 offen offset:0 sc1\n\tbuffer_load_dwordx4 %5, %10, %11, %17 offen offset:0 sc1\n\tbuffer_load_dwordx4 %6, %10, %11, %18 offen offset:0 sc1\n\tbuffer_load_dwordx4 %7, %10, %11, %19 offen offset:0 sc1\n\tbuffer_load_dwordx4 %8, %10, %11, %20 offen offset:0 sc1\n\tbuffer_load_dwordx4 %9, %10, %11, %21 offen offset:0 sc1\n\ts_waitcnt vmcnt(28)"
      : "=&v"(pu[0]), "=&v"(pu[1]), "=&v"(pu[2]), "=&v"(pu[3]), "=&v"(pu[4]), "=&v"(pu[5]), "=&v"(pu[6]), "=&v"(pu[7]), "=&v"(pu[8]), "=&v"(pu[9]) : "v"(vFj[0]), "s"(rsW), "s"(su[0]), "s"(su[1]), "s"(su[2]), "s"(su[3]), "s"(su[4]), "s"(su[5]), "s"(su[6]), "s"(su[7]), "s"(su[8]), "s"(su[9]) : "memory"); SB();
  fw2[10][0] = tr8u(tS[0] + 18432, 512);
  fw2[10][1] = tr8u(tS[1] + 18432, 512);
  fw2[10][2] = tr8u(tS[2] + 18432, 512);
  fw2[10][3] = tr8u(tS[3] + 18432, 512);
  asm volatile("s_waitcnt vmcnt(20)"
      :  :  : "memory"); SB();
  fw2[11][0] = tr8u(tS[0] + 34816, 512);
  fw2[11][1] = tr8u(tS[1] + 34816, 512);
  fw2[11][2] = tr8u(tS[2] + 34816, 512);
  fw2[11][3] = tr8u(tS[3] + 34816, 512);
  asm volatile("s_waitcnt vmcnt(16)"
      :  :  : "memory"); SB();
  fw2[12][0] = tr8u(tO[0] + 0, 512);
  fw2[12][1] = tr8u(tO[1] + 0, 512);
  fw2[12][2] = tr8u(tO[2] + 0, 512);
  fw2[12][3] = tr8u(tO[3] + 0, 512);
  asm volatile("s_waitcnt vmcnt(11)"
      :  :  : "memory"); SB();
  fw2[13][0] = tr8u(tO[0] + 4096, 512);
  fw2[13][1] = tr8u(tO[1] + 4096, 512);
  fw2[13][2] = tr8u(tO[2] + 4096, 512);
  fw2[13][3] = tr8u(tO[3] + 4096, 512);
  asm volatile("ds_write_b128 %0, %1 offset:8192\n\tds_write_b128 %0, %2 offset:9216\n\tds_write_b128 %0, %3 offset:10240\n\tds_write_b128 %0, %4 offset:11264\n\tds_write_b128 %0, %5 offset:12288\n\tds_write_b128 %0, %6 offset:13312\n\tds_write_b128 %7, %8 offset:0\n\tds_write_b128 %7, %9 offset:1024\n\tds_write_b128 %7, %10 offset:2048\n\tds_write_b128 %7, %11 offset:3072\n\tds_write_b128 %7, %12 offset:4096\n\tds_write_b128 %7, %13 offset:5120\n\tds_write_b128 %7, %14 offset:6144\n\tds_write_b128 %7, %15 offset:7168"
      :  : "v"(wwb), "a"(wor[8]), "a"(wor[9]), "a"(wor[10]), "a"(wor[11]), "a"(wor[12]), "a"(wor[13]), "v"(wwa), "a"(wor[0]), "a"(wor[1]), "a"(wor[2]), "a"(wor[3]), "a"(wor[4]), "a"(wor[5]), "a"(wor[6]), "a"(wor[7]) : "memory"); SB();
  STAMP(39);
  WAITVB(9);
  SB();
  asm volatile("v_mfma_f32_16x16x32_bf16 %0, %9, %10, 0\n\tbuffer_load_dwordx4 %1, %11, %12, %13 offen offset:0 sc1\n\tv_mfma_f32_16x16x32_bf16 %2, %9, %14, 0\n\tv_lshlrev_b32 %3, 16, %15\n\tv_xor_b32 %4, 0, %16\n\tv_mfma_f32_16x16x32_bf16 %5, %9, %17, 0\n\tbuffer_load_dwordx4 %6, %11, %12, %18 offen offset:0 sc1\n\tv_mfma_f32_16x16x32_bf16 %7, %9, %19, 0\n\tv_xor_b32 %8, 32, %16"
      : "=&v"(ac[0][0]), "=&v"(pu[10]), "=&v"(ac[0][1]), "=&v"(bias), "=&v"(xta[0]), "=&v"(ac[0][2]), "=&v"(pu[11]), "=&v"(ac[0][3]), "=&v"(xta[1]) : "v"(pu[0]), "a"(fw2[0][0]), "v"(vFj[0]), "s"(rsW), "s"(su[10]), "a"(fw2[0][1]), "v"(braw), "v"(xs), "a"(fw2[0][2]), "s"(su[11]), "a"(fw2[0][3]) : "memory"); SB();
  WAITVB(10);
  SB();
  asm volatile("v_mfma_f32_16x16x32_bf16 %0, %9, %10, %0\n\tbuffer_load_dwordx4 %1, %11, %12, %13 offen offset:0 sc1\n\tv_mfma_f32_16x16x32_bf16 %2, %9, %14, %2\n\tv_xor_b32 %3, 64, %15\n\tv_xor_b32 %4, 96, %15\n\tv_mfma_f32_16x16x32_bf16 %5, %9, %16, %5\n\tbuffer_load_dwordx4 %6, %11, %12, %17 offen offset:0 sc1\n\tv_mfma_f32_16x16x32_bf16 %7, %9, %18, %7\n\tv_lshlrev_b32 %8, 16, %19"
      : "+v"(ac[0][0]), "=&v"(pu[12]), "+v"(ac[0][1]), "=&v"(xta[2]), "=&v"(xta[3]), "+v"(ac[0][2]), "=&v"(pu[13]), "+v"(ac[0][3]), "=&v"(xe[0]) : "v"(pu[1]), "a"(fw2[1][0]), "v"(vFj[0]), "s"(rsW), "s"(su[12]), "a"(fw2[1][1]), "v"(xs), "a"(fw2[1][2]), "s"(su[13]), "a"(fw2[1][3]), "v"(xv[0][0]) : "memory"); SB();
  WAITVB(11);
  SB();
  asm volatile("v_mfma_f32_16x16x32_bf16 %0, %9, %10, %0\n\tbuffer_load_dwordx4 %1, %11, %12, %13 offen offset:0 sc1\n\tv_mfma_f32_16x16x32_bf16 %2, %9, %14, %2\n\tv_and_b32 %3, 0xffff0000, %15\n\tv_lshlrev_b32 %4, 16, %16\n\tv_mfma_f32_16x16x32_bf16 %5, %9, %17, %5\n\tbuffer_load_dwordx4 %6, %11, %12, %18 offen offset:0 sc1\n\tv_mfma_f32_16x16x32_bf16 %7, %9, %19, %7\n\tv_and_b32 %8, 0xffff0000, %16"
      : "+v"(ac[0][0]), "=&v"(pu[0]), "+v"(ac[0][1]), "=&v"(xe[1]), "=&v"(xe[2]), "+v"(ac[0][2]), "=&v"(pu[1]), "+v"(ac[0][3]), "=&v"(xe[3]) : "v"(pu[2]), "a"(fw2[2][0]), "v"(vFj[1]), "s"(rsW), "s"(su[0]), "a"(fw2[2][1]), "v"(xv[0][0]), "v"(xv[0][1]), "a"(fw2[2][2]), "s"(su[1]), "a"(fw2[2][3]) : "memory"); SB();
  WAITVB(12);
  SB();
  asm volatile("v_mfma_f32_16x16x32_bf16 %0, %10, %11, %0\n\tbuffer_load_dwordx4 %1, %12, %13, %14 offen offset:0 sc1\n\tv_mfma_f32_16x16x32_bf16 %2, %10, %15, %2\n\tv_lshlrev_b32 %3, 16, %16\n\tv_and_b32 %4, 0xffff0000, %16\n\tv_mfma_f32_16x16x32_bf16 %5, %10, %17, %5\n\tv_lshlrev_b32 %6, 16, %18\n\tv_and_b32 %7, 0xffff0000, %18\n\tv_mfma_f32_16x16x32_bf16 %8, %10, %19, %8\n\tv_lshlrev_b32 %9, 16, %20"
      : "+v"(ac[0][0]), "=&v"(pu[2]), "+v"(ac[0][1]), "=&v"(xe[4]), "=&v"(xe[5]), "+v"(ac[0][2]), "=&v"(xe[6]), "=&v"(xe[7]), "+v"(ac[0][3]), "=&v"(xe[8]) : "v"(pu[3]), "a"(fw2[3][0]), "v"(vFj[1]), "s"(rsW), "s"(su[2]), "a"(fw2[3][1]), "v"(xv[1][0]), "a"(fw2[3][2]), "v"(xv[1][1]), "a"(fw2[3][3]), "v"(xv[2][0]) : "memory"); SB();
  WAITVB(12);
  SB();
  asm volatile("v_mfma_f32_16x16x32_bf16 %0, %10, %11, %0\n\tbuffer_load_dwordx4 %1, %12, %13, %14 offen offset:0 sc1\n\tv_mfma_f32_16x16x32_bf16 %2, %10, %15, %2\n\tv_and_b32 %3, 0xffff0000, %16\n\tv_lshlrev_b32 %4, 16, %17\n\tv_mfma_f32_16x16x32_bf16 %5, %10, %18, %5\n\tv_and_b32 %6, 0xffff0000, %17\n\tv_lshlrev_b32 %7, 16, %19\n\tv_mfma_f32_16x16x32_bf16 %8, %10, %20, %8\n\tv_and_b32 %9, 0xffff0000, %19"
      : "+v"(ac[0][0]), "=&v"(pu[3]), "+v"(ac[0][1]), "=&v"(xe[9]), "=&v"(xe[10]), "+v"(ac[0][2]), "=&v"(xe[11]), "=&v"(xe[12]), "+v"(ac[0][3]), "=&v"(xe[13]) : "v"(pu[4]), "a"(fw2[4][0]), "v"(vFj[1]), "s"(rsW), "s"(su[3]), "a"(fw2[4][1]), "v"(xv[2][0]), "v"(xv[2][1]), "a"(fw2[4][2]), "v"(xv[3][0]), "a"(fw2[4][3]) : "memory"); SB();
  WAITVB(12);
  SB();
  asm volatile("v_mfma_f32_16x16x32_bf16 %0, %7, %8, %0\n\tbuffer_load_dwordx4 %1, %9, %10, %11 offen offset:0 sc1\n\tv_mfma_f32_16x16x32_bf16 %2, %7, %12, %2\n\tv_lshlrev_b32 %3, 16, %13\n\tv_and_b32 %4, 0xffff0000, %13\n\tv_mfma_f32_16x16x32_bf16 %5, %7, %14, %5\n\tv_mfma_f32_16x16x32_bf16 %6, %7, %15, %6"
      : "+v"(ac[0][0]), "=&v"(pu[4]), "+v"(ac[0][1]), "=&v"(xe[14]), "=&v"(xe[15]), "+v"(ac[0][2]), "+v"(ac[0][3]) : "v"(pu[5]), "a"(fw2[5][0]), "v"(vFj[1]), "s"(rsW), "s"(su[4]), "a"(fw2[5][1]), "v"(xv[3][1]), "a"(fw2[5][2]), "a"(fw2[5][3]) : "memory"); SB();
  WAITVB(12);
  SB();
  asm volatile("v_mfma_f32_16x16x32_bf16 %0, %6, %7, %0\n\tbuffer_load_dwordx4 %1, %8, %9, %10 offen offset:0 sc1\n\tv_mfma_f32_16x16x32_bf16 %2, %6, %11, %2\n\tbuffer_load_dword %3, %12, %9, 0 offen sc1\n\tv_mfma_f32_16x16x32_bf16 %4, %6, %13, %4\n\tv_mfma_f32_16x16x32_bf16 %5, %6, %14, %5"
      : "+v"(ac[0][0]), "=&v"(pu[5]), "+v"(ac[0][1]), "=&v"(fu2), "+v"(ac[0][2]), "+v"(ac[0][3]) : "v"(pu[6]), "a"(fw2[6][0]), "v"(vFj[1]), "s"(rsW), "s"(su[5]), "a"(fw2[6][1]), "v"(vFU), "a"(fw2[6][2]), "a"(fw2[6][3]) : "memory"); SB();
  WAITVB(13);
  SB();
  asm volatile("v_mfma_f32_16x16x32_bf16 %0, %5, %6, %0\n\tbuffer_load_dwordx4 %1, %7, %8, %9 offen offset:0 sc1\n\tv_mfma_f32_16x16x32_bf16 %2, %5, %10, %2\n\tv_mfma_f32_16x16x32_bf16 %3, %5, %11, %3\n\tv_mfma_f32_16x16x32_bf16 %4, %5, %12, %4"
      : "+v"(ac[0][0]), "=&v"(pu[6]), "+v"(ac[0][1]), "+v"(ac[0][2]), "+v"(ac[0][3]) : "v"(pu[7]), "a"(fw2[7][0]), "v"(vFj[1]), "s"(rsW), "s"(su[6]), "a"(fw2[7][1]), "a"(fw2[7][2]), "a"(fw2[7][3]) : "memory"); SB();
  WAITVB(13);
  SB();
  asm volatile("v_mfma_f32_16x16x32_bf16 %0, %5, %6, %0\n\tbuffer_load_dwordx4 %1, %7, %8, %9 offen offset:0 sc1\n\tv_mfma_f32_16x16x32_bf16 %2, %5, %10, %2\n\tv_mfma_f32_16x16x32_bf16 %3, %5, %11, %3\n\tv_mfma_f32_16x16x32_bf16 %4, %5, %12, %4"
      : "+v"(ac[0][0]), "=&v"(pu[7]), "+v"(ac[0][1]), "+v"(ac[0][2]), "+v"(ac[0][3]) : "v"(pu[8]), "a"(fw2[8][0]), "v"(vFj[1]), "s"(rsW), "s"(su[7]), "a"(fw2[8][1]), "a"(fw2[8][2]), "a"(fw2[8][3]) : "memory"); SB();
  WAITVB(13);
  SB();
  asm volatile("v_mfma_f32_16x16x32_bf16 %0, %5, %6, %0\n\tbuffer_load_dwordx4 %1, %7, %8, %9 offen offset:0 sc1\n\tv_mfma_f32_16x16x32_bf16 %2, %5, %10, %2\n\tv_mfma_f32_16x16x32_bf16 %3, %5, %11, %3\n\tv_mfma_f32_16x16x32_bf16 %4, %5, %12, %4"
      : "+v"(ac[0][0]), "=&v"(pu[8]), "+v"(ac[0][1]), "+v"(ac[0][2]), "+v"(ac[0][3]) : "v"(pu[9]), "v"(fw2[9][0]), "v"(vFj[1]), "s"(rsW), "s"(su[8]), "v"(fw2[9][1]), "v"(fw2[9][2]), "v"(fw2[9][3]) : "memory"); SB();
  WAITVB(13);
  SB();
  asm volatile("v_mfma_f32_16x16x32_bf16 %0, %5, %6, %0\n\tbuffer_load_dwordx4 %1, %7, %8, %9 offen offset:0 sc1\n\tv_mfma_f32_16x16x32_bf16 %2, %5, %10, %2\n\tv_mfma_f32_16x16x32_bf16 %3, %5, %11, %3\n\tv_mfma_f32_16x16x32_bf16 %4, %5, %12, %4"
      : "+v"(ac[0][0]), "=&v"(pu[9]), "+v"(ac[0][1]), "+v"(ac[0][2]), "+v"(ac[0][3]) : "v"(pu[10]), "v"(fw2[10][0]), "v"(vFj[1]), "s"(rsW), "s"(su[9]), "v"(fw2[10][1]), "v"(fw2[10][2]), "v"(fw2[10][3]) : "memory"); SB();
  WAITVB(13);
  SB();
  asm volatile("v_mfma_f32_16x16x32_bf16 %0, %5, %6, %0\n\tbuffer_load_dwordx4 %1, %7, %8, %9 offen offset:0 sc1\n\tv_mfma_f32_16x16x32_bf16 %2, %5, %10, %2\n\tv_mfma_f32_16x16x32_bf16 %3, %5, %11, %3\n\tv_mfma_f32_16x16x32_bf16 %4, %5, %12, %4"
      : "+v"(ac[0][0]), "=&v"(pu[10]), "+v"(ac[0][1]), "+v"(ac[0][2]), "+v"(ac[0][3]) : "v"(pu[11]), "v"(fw2[11][0]), "v"(vFj[1]), "s"(rsW), "s"(su[10]), "v"(fw2[11][1]), "v"(fw2[11][2]), "v"(fw2[11][3]) : "memory"); SB();
  WAITVB(13);
  SB();
  asm volatile("v_mfma_f32_16x16x32_bf16 %0, %4, %5, %0\n\tbuffer_load_dwordx4 %1, %6, %7, %8 offen offset:0 sc1\n\tv_mfma_f32_16x16x32_bf16 %2, %4, %9, %2\n\tv_mfma_f32_16x16x32_bf16 %3, %4, %10, %3"
      : "+v"(ac[0][0]), "=&v"(pu[11]), "+v"(ac[0][1]), "+v"(ac[0][2]) : "v"(pu[12]), "v"(fw2[12][0]), "v"(vFj[1]), "s"(rsW), "s"(su[11]), "v"(fw2[12][1]), "v"(fw2[12][2]) : "memory"); SB();
  u32x4 rsO_ = srd(out, 2048u * 896u); OPQ(rsO_);
  SB();
  asm volatile("v_mfma_f32_16x16x32_bf16 %0, %1, %2, %0"
      : "+v"(ac[0][3]) : "v"(pu[12]), "v"(fw2[12][3])); SB();
  WAITVB(13);
  SB();
  asm volatile("v_mfma_f32_16x16x32_bf16 %0, %5, %6, %0\n\tbuffer_load_dwordx4 %1, %7, %8, %9 offen offset:0 sc1\n\tv_mfma_f32_16x16x32_bf16 %2, %5, %10, %2\n\tv_mfma_f32_16x16x32_bf16 %3, %5, %11, %3\n\tv_mfma_f32_16x16x32_bf16 %4, %5, %12, %4"
      : "+v"(ac[0][0]), "=&v"(pu[12]), "+v"(ac[0][1]), "+v"(ac[0][2]), "+v"(ac[0][3]) : "v"(pu[13]), "v"(fw2[13][0]), "v"(vFj[1]), "s"(rsW), "s"(su[12]), "v"(fw2[13][1]), "v"(fw2[13][2]), "v"(fw2[13][3]) : "memory"); SB();
  STAMP(40);
  WAITVB(13);
  SB();
  asm volatile("v_mfma_f32_16x16x32_bf16 %0, %5, %6, 0\n\tbuffer_load_dwordx4 %1, %7, %8, %9 offen offset:0 sc1\n\tv_mfma_f32_16x16x32_bf16 %2, %5, %10, 0\n\tv_mfma_f32_16x16x32_bf16 %3, %5, %11, 0\n\tv_mfma_f32_16x16x32_bf16 %4, %5, %12, 0"
      : "=&v"(ac[1][0]), "=&v"(pu[13]), "=&v"(ac[1][1]), "=&v"(ac[1][2]), "=&v"(ac[1][3]) : "v"(pu[0]), "a"(fw2[0][0]), "v"(vFj[1]), "s"(rsW), "s"(su[13]), "a"(fw2[0][1]), "a"(fw2[0][2]), "a"(fw2[0][3]) : "memory"); SB();
  WAITVB(13);
  SB();
  asm volatile("v_mfma_f32_16x16x32_bf16 %0, %1, %2, %0"
      : "+v"(ac[1][0]) : "v"(pu[1]), "a"(fw2[1][0])); SB();
  WAITV1(8, fu2);
  if (__builtin_expect(__ballot(fu2 != nonce) != 0ull, 0)) poll1<4>(rsW, vFU, nonce);
  STAMP(19);
  asm volatile("buffer_load_dwordx4 %0, %4, %5, %6 offen offset:0 sc1\n\tv_mfma_f32_16x16x32_bf16 %1, %7, %8, %1\n\tds_write_b128 %9, %10 offset:0\n\tv_mfma_f32_16x16x32_bf16 %2, %7, %11, %2\n\tv_mfma_f32_16x16x32_bf16 %3, %7, %12, %3"
      : "=&v"(pu[0]), "+v"(ac[1][1]), "+v"(ac[1][2]), "+v"(ac[1][3]) : "v"(vFj[2]), "s"(rsW), "s"(su[0]), "v"(pu[1]), "a"(fw2[1][1]), "v"(rwd), "v"(ac[0][0]), "a"(fw2[1][2]), "a"(fw2[1][3]) : "memory"); SB();
  WAITVB(13);
  SB();
  asm volatile("v_mfma_f32_16x16x32_bf16 %0, %5, %6, %0\n\tbuffer_load_dwordx4 %1, %7, %8, %9 offen offset:0 sc1\n\tv_mfma_f32_16x16x32_bf16 %2, %5, %10, %2\n\tds_write_b128 %11, %12 offset:1024\n\tv_mfma_f32_16x16x32_bf16 %3, %5, %13, %3\n\tv_mfma_f32_16x16x32_bf16 %4, %5, %14, %4"
      : "+v"(ac[1][0]), "=&v"(pu[1]), "+v"(ac[1][1]), "+v"(ac[1][2]), "+v"(ac[1][3]) : "v"(pu[2]), "a"(fw2[2][0]), "v"(vFj[2]), "s"(rsW), "s"(su[1]), "a"(fw2[2][1]), "v"(rwd), "v"(ac[0][1]), "a"(fw2[2][2]), "a"(fw2[2][3]) : "memory"); SB();
  WAITVB(13);
  SB();
  asm volatile("v_mfma_f32_16x16x32_bf16 %0, %5, %6, %0\n\tbuffer_load_dwordx4 %1, %7, %8, %9 offen offset:0 sc1\n\tv_mfma_f32_16x16x32_bf16 %2, %5, %10, %2\n\tds_write_b128 %11, %12 offset:2048\n\tv_mfma_f32_16x16x32_bf16 %3, %5, %13, %3\n\tv_mfma_f32_16x16x32_bf16 %4, %5, %14, %4"
      : "+v"(ac[1][0]), "=&v"(pu[2]), "+v"(ac[1][1]), "+v"(ac[1][2]), "+v"(ac[1][3]) : "v"(pu[3]), "a"(fw2[3][0]), "v"(vFj[2]), "s"(rsW), "s"(su[2]), "a"(fw2[3][1]), "v"(rwd), "v"(ac[0][2]), "a"(fw2[3][2]), "a"(fw2[3][3]) : "memory"); SB();
  WAITVB(13);
  SB();
  asm volatile("v_mfma_f32_16x16x32_bf16 %0, %5, %6, %0\n\tbuffer_load_dwordx4 %1, %7, %8, %9 offen offset:0 sc1\n\tv_mfma_f32_16x16x32_bf16 %2, %5, %10, %2\n\tds_write_b128 %11, %12 offset:3072\n\tv_mfma_f32_16x16x32_bf16 %3, %5, %13, %3\n\tv_mfma_f32_16x16x32_bf16 %4, %5, %14, %4"
      : "+v"(ac[1][0]), "=&v"(pu[3]), "+v"(ac[1][1]), "+v"(ac[1][2]), "+v"(ac[1][3]) : "v"(pu[4]), "a"(fw2[4][0]), "v"(vFj[2]), "s"(rsW), "s"(su[3]), "a"(fw2[4][1]), "v"(rwd), "v"(ac[0][3]), "a"(fw2[4][2]), "a"(fw2[4][3]) : "memory"); SB();
  WAITVB(13);
  SB();
  asm volatile("v_mfma_f32_16x16x32_bf16 %0, %3, %4, %0\n\tbuffer_load_dwordx4 %1, %5, %6, %7 offen offset:0 sc1\n\tv_mfma_f32_16x16x32_bf16 %2, %3, %8, %2"
      : "+v"(ac[1][0]), "=&v"(pu[4]), "+v"(ac[1][1]) : "v"(pu[5]), "a"(fw2[5][0]), "v"(vFj[2]), "s"(rsW), "s"(su[4]), "a"(fw2[5][1]) : "memory"); SB();
  WAITL(0);
  BAR0();
  SB();
  asm volatile("v_mfma_f32_16x16x32_bf16 %0, %5, %6, %0\n\tds_read_b128 %1, %7 offset:0\n\tds_read_b128 %2, %7 offset:4096\n\tv_mfma_f32_16x16x32_bf16 %3, %5, %8, %3\n\tds_read_b128 %4, %7 offset:8192"
      : "+v"(ac[1][2]), "=&v"(py[0][0]), "=&v"(py[0][1]), "+v"(ac[1][3]), "=&v"(py[0][2]) : "v"(pu[5]), "a"(fw2[5][2]), "v"(rr), "a"(fw2[5][3]) : "memory"); SB();
  WAITVB(12);
  SB();
  asm volatile("v_mfma_f32_16x16x32_bf16 %0, %8, %9, %0\n\tbuffer_load_dwordx4 %1, %10, %11, %12 offen offset:0 sc1\n\tv_mfma_f32_16x16x32_bf16 %2, %8, %13, %2\n\tds_read_b128 %3, %14 offset:12288\n\tds_read_b128 %4, %15 offset:0\n\tv_mfma_f32_16x16x32_bf16 %5, %8, %16, %5\n\tv_mfma_f32_16x16x32_bf16 %6, %8, %17, %6\n\tds_read_b128 %7, %15 offset:8192"
      : "+v"(ac[1][0]), "=&v"(pu[5]), "+v"(ac[1][1]), "=&v"(py[0][3]), "=&a"(fwo[0]), "+v"(ac[1][2]), "+v"(ac[1][3]), "=&a"(fwo[1]) : "v"(pu[6]), "a"(fw2[6][0]), "v"(vFj[2]), "s"(rsW), "s"(su[5]), "a"(fw2[6][1]), "v"(rr), "v"(ro), "a"(fw2[6][2]), "a"(fw2[6][3]) : "memory"); SB();
  WAITVB(12);
  SB();
  asm volatile("v_mfma_f32_16x16x32_bf16 %0, %5, %6, %0\n\tbuffer_load_dwordx4 %1, %7, %8, %9 offen offset:0 sc1\n\tv_mfma_f32_16x16x32_bf16 %2, %5, %10, %2\n\tds_read_b128 %3, %11 offset:16384\n\tv_mfma_f32_16x16x32_bf16 %4, %5, %12, %4"
      : "+v"(ac[1][0]), "=&v"(pu[6]), "+v"(ac[1][1]), "=&a"(fwo[2]), "+v"(ac[1][2]) : "v"(pu[7]), "a"(fw2[7][0]), "v"(vFj[2]), "s"(rsW), "s"(su[6]), "a"(fw2[7][1]), "v"(ro), "a"(fw2[7][2]) : "memory"); SB();
  WAITL(3);
  asm volatile("v_add_f32 %0, %3, %4\n\tv_mfma_f32_16x16x32_bf16 %1, %5, %6, %1\n\tv_add_f32 %2, %7, %4"
      : "=&v"(ye[0]), "+v"(ac[1][3]), "=&v"(ye[1]) : "v"(py[0][0][0]), "v"(bias), "v"(pu[7]), "a"(fw2[7][3]), "v"(py[0][0][1])); SB();
  WAITVB(12);
  SB();
  asm volatile("v_mfma_f32_16x16x32_bf16 %0, %9, %10, %0\n\tbuffer_load_dwordx4 %1, %11, %12, %13 offen offset:0 sc1\n\tv_mfma_f32_16x16x32_bf16 %2, %9, %14, %2\n\tv_add_f32 %3, %15, %16\n\tv_add_f32 %4, %17, %16\n\tv_mfma_f32_16x16x32_bf16 %5, %9, %18, %5\n\tv_add_f32 %6, %19, %20\n\tv_add_f32 %7, %21, %22\n\tv_mfma_f32_16x16x32_bf16 %8, %9, %23, %8"
      : "+v"(ac[1][0]), "=&v"(pu[7]), "+v"(ac[1][1]), "=&v"(ye[2]), "=&v"(ye[3]), "+v"(ac[1][2]), "=&v"(ze[0]), "=&v"(ze[1]), "+v"(ac[1][3]) : "v"(pu[8]), "a"(fw2[8][0]), "v"(vFj[2]), "s"(rsW), "s"(su[7]), "a"(fw2[8][1]), "v"(py[0][0][2]), "v"(bias), "v"(py[0][0][3]), "a"(fw2[8][2]), "v"(py[0][1][0]), "v"(py[0][2][0]), "v"(py[0][1][1]), "v"(py[0][2][1]), "a"(fw2[8][3]) : "memory"); SB();
  asm volatile("v_add_f32 %0, %1, %2"
      : "=&v"(ze[2]) : "v"(py[0][1][2]), "v"(py[0][2][2])); SB();
  WAITVB(12);
  SB();
  asm volatile("v_mfma_f32_16x16x32_bf16 %0, %9, %10, %0\n\tbuffer_load_dwordx4 %1, %11, %12, %13 offen offset:0 sc1\n\tv_mfma_f32_16x16x32_bf16 %2, %9, %14, %2\n\tv_add_f32 %3, %15, %16\n\tv_add_f32 %4, %4, %17\n\tv_mfma_f32_16x16x32_bf16 %5, %9, %18, %5\n\tv_add_f32 %6, %6, %19\n\tv_add_f32 %7, %7, %20\n\tv_mfma_f32_16x16x32_bf16 %8, %9, %21, %8"
      : "+v"(ac[1][0]), "=&v"(pu[8]), "+v"(ac[1][1]), "=&v"(ze[3]), "+v"(ye[0]), "+v"(ac[1][2]), "+v"(ye[1]), "+v"(ye[2]), "+v"(ac[1][3]) : "v"(pu[9]), "v"(fw2[9][0]), "v"(vFj[2]), "s"(rsW), "s"(su[8]), "v"(fw2[9][1]), "v"(py[0][1][3]), "v"(py[0][2][3]), "v"(py[0][3][0]), "v"(fw2[9][2]), "v"(py[0][3][1]), "v"(py[0][3][2]), "v"(fw2[9][3]) : "memory"); SB();
  asm volatile("v_add_f32 %0, %0, %1"
      : "+v"(ye[3]) : "v"(py[0][3][3])); SB();
  WAITVB(12);
  SB();
  asm volatile("v_mfma_f32_16x16x32_bf16 %0, %10, %11, %0\n\tbuffer_load_dwordx4 %1, %12, %13, %14 offen offset:0 sc1\n\tv_mfma_f32_16x16x32_bf16 %2, %10, %15, %2\n\tv_add_f32 %3, %3, %4\n\tv_add_f32 %5, %5, %16\n\tv_mfma_f32_16x16x32_bf16 %6, %10, %17, %6\n\tv_add_f32 %7, %7, %18\n\tv_add_f32 %8, %8, %19\n\tv_mfma_f32_16x16x32_bf16 %9, %10, %20, %9\n\tv_mul_f32 %4, 0xbfb8aa3b, %3"
      : "+v"(ac[1][0]), "=&v"(pu[9]), "+v"(ac[1][1]), "+v"(ye[0]), "+v"(ze[0]), "+v"(ye[1]), "+v"(ac[1][2]), "+v"(ye[2]), "+v"(ye[3]), "+v"(ac[1][3]) : "v"(pu[10]), "v"(fw2[10][0]), "v"(vFj[2]), "s"(rsW), "s"(su[9]), "v"(fw2[10][1]), "v"(ze[1]), "v"(fw2[10][2]), "v"(ze[2]), "v"(ze[3]), "v"(fw2[10][3]) : "memory"); SB();
  WAITVB(12);
  SB();
  asm volatile("v_mfma_f32_16x16x32_bf16 %0, %9, %10, %0\n\tbuffer_load_dwordx4 %1, %11, %12, %13 offen offset:0 sc1\n\tv_mfma_f32_16x16x32_bf16 %2, %9, %14, %2\n\tv_mul_f32 %3, 0xbfb8aa3b, %15\n\tv_mul_f32 %4, 0xbfb8aa3b, %16\n\tv_mfma_f32_16x16x32_bf16 %5, %9, %17, %5\n\tds_read_b128 %6, %18 offset:24576\n\tv_mul_f32 %7, 0xbfb8aa3b, %19\n\tv_mfma_f32_16x16x32_bf16 %8, %9, %20, %8"
      : "+v"(ac[1][0]), "=&v"(pu[10]), "+v"(ac[1][1]), "=&v"(ze[1]), "=&v"(ze[2]), "+v"(ac[1][2]), "=&a"(fwo[3]), "=&v"(ze[3]), "+v"(ac[1][3]) : "v"(pu[11]), "v"(fw2[11][0]), "v"(vFj[2]), "s"(rsW), "s"(su[10]), "v"(fw2[11][1]), "v"(ye[1]), "v"(ye[2]), "v"(fw2[11][2]), "v"(ro), "v"(ye[3]), "v"(fw2[11][3]) : "memory"); SB();
  WAITVB(12);
  SB();
  asm volatile("v_mfma_f32_16x16x32_bf16 %0, %8, %9, %0\n\tbuffer_load_dwordx4 %1, %10, %11, %12 offen offset:0 sc1\n\tv_mfma_f32_16x16x32_bf16 %2, %8, %13, %2\n\tv_exp_f32 %3, %3\n\tv_mfma_f32_16x16x32_bf16 %4, %8, %14, %4\n\tv_exp_f32 %5, %5\n\tv_mfma_f32_16x16x32_bf16 %6, %8, %15, %6\n\tds_read_b128 %7, %16 offset:1024"
      : "+v"(ac[1][0]), "=&v"(pu[11]), "+v"(ac[1][1]), "+v"(ze[0]), "+v"(ac[1][2]), "+v"(ze[1]), "+v"(ac[1][3]), "=&a"(fwo[4]) : "v"(pu[12]), "v"(fw2[12][0]), "v"(vFj[2]), "s"(rsW), "s"(su[11]), "v"(fw2[12][1]), "v"(fw2[12][2]), "v"(fw2[12][3]), "v"(ro) : "memory"); SB();
  WAITVB(12);
  SB();
  asm volatile("v_mfma_f32_16x16x32_bf16 %0, %9, %10, %0\n\tbuffer_load_dwordx4 %1, %11, %12, %13 offen offset:0 sc1\n\tv_mfma_f32_16x16x32_bf16 %2, %9, %14, %2\n\tv_exp_f32 %3, %3\n\tv_mfma_f32_16x16x32_bf16 %4, %9, %15, %4\n\tv_exp_f32 %5, %5\n\tv_mfma_f32_16x16x32_bf16 %6, %9, %16, %6\n\tv_add_f32 %7, 1.0, %7\n\tv_add_f32 %8, 1.0, %8"
      : "+v"(ac[1][0]), "=&v"(pu[12]), "+v"(ac[1][1]), "+v"(ze[2]), "+v"(ac[1][2]), "+v"(ze[3]), "+v"(ac[1][3]), "+v"(ze[0]), "+v"(ze[1]) : "v"(pu[13]), "v"(fw2[13][0]), "v"(vFj[2]), "s"(rsW), "s"(su[12]), "v"(fw2[13][1]), "v"(fw2[13][2]), "v"(fw2[13][3]) : "memory"); SB();
  STAMP(41);
  WAITVB(12);
  SB();
  asm volatile("v_mfma_f32_16x16x32_bf16 %0, %9, %10, 0\n\tbuffer_load_dwordx4 %1, %11, %12, %13 offen offset:0 sc1\n\tv_mfma_f32_16x16x32_bf16 %2, %9, %14, 0\n\tbuffer_load_dword %3, %15, %12, 0 offen sc1\n\tv_mfma_f32_16x16x32_bf16 %4, %9, %16, 0\n\tv_add_f32 %5, 1.0, %5\n\tv_add_f32 %6, 1.0, %6\n\tv_mfma_f32_16x16x32_bf16 %7, %9, %17, 0\n\tds_read_b128 %8, %18 offset:9216"
      : "=&v"(ac[0][0]), "=&v"(pu[13]), "=&v"(ac[0][1]), "=&v"(fo), "=&v"(ac[0][2]), "+v"(ze[2]), "+v"(ze[3]), "=&v"(ac[0][3]), "=&a"(fwo[5]) : "v"(pu[0]), "a"(fw2[0][0]), "v"(vFj[2]), "s"(rsW), "s"(su[13]), "a"(fw2[0][1]), "v"(vFO), "a"(fw2[0][2]), "a"(fw2[0][3]), "v"(ro) : "memory"); SB();
  WAITVB(13);
  SB();
  asm volatile("v_mfma_f32_16x16x32_bf16 %0, %7, %8, %0\n\tbuffer_load_dwordx4 %1, %9, %10, %11 offen offset:0 sc1\n\tv_mfma_f32_16x16x32_bf16 %2, %7, %12, %2\n\tds_write_b128 %13, %14 offset:18432\n\tv_mfma_f32_16x16x32_bf16 %3, %7, %15, %3\n\tv_rcp_f32 %4, %4\n\tv_mfma_f32_16x16x32_bf16 %5, %7, %16, %5\n\tds_read_b128 %6, %17 offset:17408"
      : "+v"(ac[0][0]), "=&v"(pu[0]), "+v"(ac[0][1]), "+v"(ac[0][2]), "+v"(ze[0]), "+v"(ac[0][3]), "=&a"(fwo[6]) : "v"(pu[1]), "a"(fw2[1][0]), "v"(vFj[3]), "s"(rsW), "s"(su[0]), "a"(fw2[1][1]), "v"(rwd), "v"(ac[1][0]), "a"(fw2[1][2]), "a"(fw2[1][3]), "v"(ro) : "memory"); SB();
  WAITVB(13);
  SB();
  asm volatile("v_mfma_f32_16x16x32_bf16 %0, %7, %8, %0\n\tbuffer_load_dwordx4 %1, %9, %10, %11 offen offset:0 sc1\n\tv_mfma_f32_16x16x32_bf16 %2, %7, %12, %2\n\tds_write_b128 %13, %14 offset:19456\n\tv_mfma_f32_16x16x32_bf16 %3, %7, %15, %3\n\tv_rcp_f32 %4, %4\n\tv_mfma_f32_16x16x32_bf16 %5, %7, %16, %5\n\tds_read_b128 %6, %17 offset:25600"
      : "+v"(ac[0][0]), "=&v"(pu[1]), "+v"(ac[0][1]), "+v"(ac[0][2]), "+v"(ze[1]), "+v"(ac[0][3]), "=&a"(fwo[7]) : "v"(pu[2]), "a"(fw2[2][0]), "v"(vFj[3]), "s"(rsW), "s"(su[1]), "a"(fw2[2][1]), "v"(rwd), "v"(ac[1][1]), "a"(fw2[2][2]), "a"(fw2[2][3]), "v"(ro) : "memory"); SB();
  WAITVB(13);
  SB();
  asm volatile("v_mfma_f32_16x16x32_bf16 %0, %7, %8, %0\n\tbuffer_load_dwordx4 %1, %9, %10, %11 offen offset:0 sc1\n\tv_mfma_f32_16x16x32_bf16 %2, %7, %12, %2\n\tds_write_b128 %13, %14 offset:20480\n\tv_mfma_f32_16x16x32_bf16 %3, %7, %15, %3\n\tv_rcp_f32 %4, %4\n\tv_mfma_f32_16x16x32_bf16 %5, %7, %16, %5\n\tds_read_b128 %6, %17 offset:2048"
      : "+v"(ac[0][0]), "=&v"(pu[2]), "+v"(ac[0][1]), "+v"(ac[0][2]), "+v"(ze[2]), "+v"(ac[0][3]), "=&a"(fwo[8]) : "v"(pu[3]), "a"(fw2[3][0]), "v"(vFj[3]), "s"(rsW), "s"(su[2]), "a"(fw2[3][1]), "v"(rwd), "v"(ac[1][2]), "a"(fw2[3][2]), "a"(fw2[3][3]), "v"(ro) : "memory"); SB();
  WAITVB(13);
  SB();
  asm volatile("v_mfma_f32_16x16x32_bf16 %0, %7, %8, %0\n\tbuffer_load_dwordx4 %1, %9, %10, %11 offen offset:0 sc1\n\tv_mfma_f32_16x16x32_bf16 %2, %7, %12, %2\n\tds_write_b128 %13, %14 offset:21504\n\tv_mfma_f32_16x16x32_bf16 %3, %7, %15, %3\n\tv_rcp_f32 %4, %4\n\tv_mfma_f32_16x16x32_bf16 %5, %7, %16, %5\n\tv_mul_f32 %6, %17, %18"
      : "+v"(ac[0][0]), "=&v"(pu[3]), "+v"(ac[0][1]), "+v"(ac[0][2]), "+v"(ze[3]), "+v"(ac[0][3]), "=&v"(ge[0]) : "v"(pu[4]), "a"(fw2[4][0]), "v"(vFj[3]), "s"(rsW), "s"(su[3]), "a"(fw2[4][1]), "v"(rwd), "v"(ac[1][3]), "a"(fw2[4][2]), "a"(fw2[4][3]), "v"(ye[0]), "v"(ze[0]) : "memory"); SB();
  WAITVB(13);
  SB();
  asm volatile("v_mfma_f32_16x16x32_bf16 %0, %3, %4, %0\n\tbuffer_load_dwordx4 %1, %5, %6, %7 offen offset:0 sc1\n\tv_mfma_f32_16x16x32_bf16 %2, %3, %8, %2"
      : "+v"(ac[0][0]), "=&v"(pu[4]), "+v"(ac[0][1]) : "v"(pu[5]), "a"(fw2[5][0]), "v"(vFj[3]), "s"(rsW), "s"(su[4]), "a"(fw2[5][1]) : "memory"); SB();
  WAITL(0);
  BAR0();
  SB();
  asm volatile("v_mfma_f32_16x16x32_bf16 %0, %5, %6, %0\n\tds_read_b128 %1, %7 offset:18432\n\tds_read_b128 %2, %7 offset:22528\n\tv_mfma_f32_16x16x32_bf16 %3, %5, %8, %3\n\tds_read_b128 %4, %7 offset:26624"
      : "+v"(ac[0][2]), "=&v"(py[1][0]), "=&v"(py[1][1]), "+v"(ac[0][3]), "=&v"(py[1][2]) : "v"(pu[5]), "a"(fw2[5][2]), "v"(rr), "a"(fw2[5][3]) : "memory"); SB();
  WAITVB(13);
  SB();
  asm volatile("v_mfma_f32_16x16x32_bf16 %0, %10, %11, %0\n\tbuffer_load_dwordx4 %1, %12, %13, %14 offen offset:0 sc1\n\tv_mfma_f32_16x16x32_bf16 %2, %10, %15, %2\n\tds_read_b128 %3, %16 offset:30720\n\tv_mul_f32 %4, %17, %18\n\tv_mfma_f32_16x16x32_bf16 %5, %10, %19, %5\n\tv_mul_f32 %6, %20, %21\n\tv_mul_f32 %7, %22, %23\n\tv_mfma_f32_16x16x32_bf16 %8, %10, %24, %8\n\tds_read_b128 %9, %25 offset:10240"
      : "+v"(ac[0][0]), "=&v"(pu[5]), "+v"(ac[0][1]), "=&v"(py[1][3]), "=&v"(ge[1]), "+v"(ac[0][2]), "=&v"(ge[2]), "=&v"(ge[3]), "+v"(ac[0][3]), "=&a"(fwo[9]) : "v"(pu[6]), "a"(fw2[6][0]), "v"(vFj[3]), "s"(rsW), "s"(su[5]), "a"(fw2[6][1]), "v"(rr), "v"(ye[1]), "v"(ze[1]), "a"(fw2[6][2]), "v"(ye[2]), "v"(ze[2]), "v"(ye[3]), "v"(ze[3]), "a"(fw2[6][3]), "v"(ro) : "memory"); SB();
  WAITVB(13);
  SB();
  asm volatile("v_mfma_f32_16x16x32_bf16 %0, %5, %6, %0\n\tbuffer_load_dwordx4 %1, %7, %8, %9 offen offset:0 sc1\n\tv_mfma_f32_16x16x32_bf16 %2, %5, %10, %2\n\tds_read_b128 %3, %11 offset:18432\n\tv_mfma_f32_16x16x32_bf16 %4, %5, %12, %4"
      : "+v"(ac[0][0]), "=&v"(pu[6]), "+v"(ac[0][1]), "=&a"(fwo[10]), "+v"(ac[0][2]) : "v"(pu[7]), "a"(fw2[7][0]), "v"(vFj[3]), "s"(rsW), "s"(su[6]), "a"(fw2[7][1]), "v"(ro), "a"(fw2[7][2]) : "memory"); SB();
  WAITL(2);
  asm volatile("v_add_f32 %0, %3, %4\n\tv_mfma_f32_16x16x32_bf16 %1, %5, %6, %1\n\tv_add_f32 %2, %7, %4"
      : "=&v"(ye[0]), "+v"(ac[0][3]), "=&v"(ye[1]) : "v"(py[1][0][0]), "v"(bias), "v"(pu[7]), "a"(fw2[7][3]), "v"(py[1][0][1])); SB();
  WAITVB(13);
  SB();
  asm volatile("v_mfma_f32_16x16x32_bf16 %0, %9, %10, %0\n\tbuffer_load_dwordx4 %1, %11, %12, %13 offen offset:0 sc1\n\tv_mfma_f32_16x16x32_bf16 %2, %9, %14, %2\n\tv_add_f32 %3, %15, %16\n\tv_add_f32 %4, %17, %16\n\tv_mfma_f32_16x16x32_bf16 %5, %9, %18, %5\n\tv_add_f32 %6, %19, %20\n\tv_add_f32 %7, %21, %22\n\tv_mfma_f32_16x16x32_bf16 %8, %9, %23, %8"
      : "+v"(ac[0][0]), "=&v"(pu[7]), "+v"(ac[0][1]), "=&v"(ye[2]), "=&v"(ye[3]), "+v"(ac[0][2]), "=&v"(ze[0]), "=&v"(ze[1]), "+v"(ac[0][3]) : "v"(pu[8]), "a"(fw2[8][0]), "v"(vFj[3]), "s"(rsW), "s"(su[7]), "a"(fw2[8][1]), "v"(py[1][0][2]), "v"(bias), "v"(py[1][0][3]), "a"(fw2[8][2]), "v"(py[1][1][0]), "v"(py[1][2][0]), "v"(py[1][1][1]), "v"(py[1][2][1]), "a"(fw2[8][3]) : "memory"); SB();
  asm volatile("v_add_f32 %0, %1, %2"
      : "=&v"(ze[2]) : "v"(py[1][1][2]), "v"(py[1][2][2])); SB();
  WAITVB(13);
  SB();
  asm volatile("v_mfma_f32_16x16x32_bf16 %0, %9, %10, %0\n\tbuffer_load_dwordx4 %1, %11, %12, %13 offen offset:0 sc1\n\tv_mfma_f32_16x16x32_bf16 %2, %9, %14, %2\n\tv_add_f32 %3, %15, %16\n\tv_add_f32 %4, %4, %17\n\tv_mfma_f32_16x16x32_bf16 %5, %9, %18, %5\n\tv_add_f32 %6, %6, %19\n\tv_add_f32 %7, %7, %20\n\tv_mfma_f32_16x16x32_bf16 %8, %9, %21, %8"
      : "+v"(ac[0][0]), "=&v"(pu[8]), "+v"(ac[0][1]), "=&v"(ze[3]), "+v"(ye[0]), "+v"(ac[0][2]), "+v"(ye[1]), "+v"(ye[2]), "+v"(ac[0][3]) : "v"(pu[9]), "v"(fw2[9][0]), "v"(vFj[3]), "s"(rsW), "s"(su[8]), "v"(fw2[9][1]), "v"(py[1][1][3]), "v"(py[1][2][3]), "v"(py[1][3][0]), "v"(fw2[9][2]), "v"(py[1][3][1]), "v"(py[1][3][2]), "v"(fw2[9][3]) : "memory"); SB();
  asm volatile("v_add_f32 %0, %0, %1"
      : "+v"(ye[3]) : "v"(py[1][3][3])); SB();
  WAITVB(13);
  SB();
  asm volatile("v_mfma_f32_16x16x32_bf16 %0, %10, %11, %0\n\tbuffer_load_dwordx4 %1, %12, %13, %14 offen offset:0 sc1\n\tv_mfma_f32_16x16x32_bf16 %2, %10, %15, %2\n\tv_add_f32 %3, %3, %4\n\tv_add_f32 %5, %5, %16\n\tv_mfma_f32_16x16x32_bf16 %6, %10, %17, %6\n\tv_add_f32 %7, %7, %18\n\tv_add_f32 %8, %8, %19\n\tv_mfma_f32_16x16x32_bf16 %9, %10, %20, %9\n\tv_mul_f32 %4, 0xbfb8aa3b, %3"
      : "+v"(ac[0][0]), "=&v"(pu[9]), "+v"(ac[0][1]), "+v"(ye[0]), "+v"(ze[0]), "+v"(ye[1]), "+v"(ac[0][2]), "+v"(ye[2]), "+v"(ye[3]), "+v"(ac[0][3]) : "v"(pu[10]), "v"(fw2[10][0]), "v"(vFj[3]), "s"(rsW), "s"(su[9]), "v"(fw2[10][1]), "v"(ze[1]), "v"(fw2[10][2]), "v"(ze[2]), "v"(ze[3]), "v"(fw2[10][3]) : "memory"); SB();
  WAITVB(13);
  SB();
  asm volatile("v_mfma_f32_16x16x32_bf16 %0, %9, %10, %0\n\tbuffer_load_dwordx4 %1, %11, %12, %13 offen offset:0 sc1\n\tv_mfma_f32_16x16x32_bf16 %2, %9, %14, %2\n\tv_mul_f32 %3, 0xbfb8aa3b, %15\n\tv_mul_f32 %4, 0xbfb8aa3b, %16\n\tv_mfma_f32_16x16x32_bf16 %5, %9, %17, %5\n\tds_read_b128 %6, %18 offset:26624\n\tv_mul_f32 %7, 0xbfb8aa3b, %19\n\tv_mfma_f32_16x16x32_bf16 %8, %9, %20, %8"
      : "+v"(ac[0][0]), "=&v"(pu[10]), "+v"(ac[0][1]), "=&v"(ze[1]), "=&v"(ze[2]), "+v"(ac[0][2]), "=&a"(fwo[11]), "=&v"(ze[3]), "+v"(ac[0][3]) : "v"(pu[11]), "v"(fw2[11][0]), "v"(vFj[3]), "s"(rsW), "s"(su[10]), "v"(fw2[11][1]), "v"(ye[1]), "v"(ye[2]), "v"(fw2[11][2]), "v"(ro), "v"(ye[3]), "v"(fw2[11][3]) : "memory"); SB();
  WAITVB(13);
  SB();
  asm volatile("v_mfma_f32_16x16x32_bf16 %0, %8, %9, %0\n\tbuffer_load_dwordx4 %1, %10, %11, %12 offen offset:0 sc1\n\tv_mfma_f32_16x16x32_bf16 %2, %8, %13, %2\n\tv_exp_f32 %3, %3\n\tv_mfma_f32_16x16x32_bf16 %4, %8, %14, %4\n\tv_exp_f32 %5, %5\n\tv_mfma_f32_16x16x32_bf16 %6, %8, %15, %6\n\tds_read_b128 %7, %16 offset:3072"
      : "+v"(ac[0][0]), "=&v"(pu[11]), "+v"(ac[0][1]), "+v"(ze[0]), "+v"(ac[0][2]), "+v"(ze[1]), "+v"(ac[0][3]), "=&a"(fwo[12]) : "v"(pu[12]), "v"(fw2[12][0]), "v"(vFj[3]), "s"(rsW), "s"(su[11]), "v"(fw2[12][1]), "v"(fw2[12][2]), "v"(fw2[12][3]), "v"(ro) : "memory"); SB();
  WAITVB(13);
  SB();
  asm volatile("v_mfma_f32_16x16x32_bf16 %0, %9, %10, %0\n\tbuffer_load_dwordx4 %1, %11, %12, %13 offen offset:0 sc1\n\tv_mfma_f32_16x16x32_bf16 %2, %9, %14, %2\n\tv_exp_f32 %3, %3\n\tv_mfma_f32_16x16x32_bf16 %4, %9, %15, %4\n\tv_exp_f32 %5, %5\n\tv_mfma_f32_16x16x32_bf16 %6, %9, %16, %6\n\tv_add_f32 %7, 1.0, %7\n\tv_add_f32 %8, 1.0, %8"
      : "+v"(ac[0][0]), "=&v"(pu[12]), "+v"(ac[0][1]), "+v"(ze[2]), "+v"(ac[0][2]), "+v"(ze[3]), "+v"(ac[0][3]), "+v"(ze[0]), "+v"(ze[1]) : "v"(pu[13]), "v"(fw2[13][0]), "v"(vFj[3]), "s"(rsW), "s"(su[12]), "v"(fw2[13][1]), "v"(fw2[13][2]), "v"(fw2[13][3]) : "memory"); SB();
  STAMP(42);
  WAITVB(12);
  SB();
  asm volatile("v_mfma_f32_16x16x32_bf16 %0, %9, %10, 0\n\tbuffer_load_dwordx4 %1, %11, %12, %13 offen offset:0 sc1\n\tv_mfma_f32_16x16x32_bf16 %2, %9, %14, 0\n\tv_add_f32 %3, 1.0, %3\n\tv_add_f32 %4, 1.0, %4\n\tv_mfma_f32_16x16x32_bf16 %5, %9, %15, 0\n\tv_rcp_f32 %6, %6\n\tv_mfma_f32_16x16x32_bf16 %7, %9, %16, 0\n\tds_read_b128 %8, %17 offset:11264"
      : "=&v"(ac[1][0]), "=&v"(pu[13]), "=&v"(ac[1][1]), "+v"(ze[2]), "+v"(ze[3]), "=&v"(ac[1][2]), "+v"(ze[0]), "=&v"(ac[1][3]), "=&a"(fwo[13]) : "v"(pu[0]), "a"(fw2[0][0]), "v"(vFj[3]), "s"(rsW), "s"(su[13]), "a"(fw2[0][1]), "a"(fw2[0][2]), "a"(fw2[0][3]), "v"(ro) : "memory"); SB();
  WAITVB(12);
  SB();
  asm volatile("v_mfma_f32_16x16x32_bf16 %0, %1, %2, %0"
      : "+v"(ac[1][0]) : "v"(pu[1]), "a"(fw2[1][0])); SB();
  WAITV1(14, fo);
  if (__builtin_expect(__ballot(fo != nonce) != 0ull, 0)) poll1<4>(rsW, vFO, nonce);
  STAMP(18);
  asm volatile("buffer_load_dwordx4 %0, %6, %7, %8 offen offset:0 sc1\n\tv_mfma_f32_16x16x32_bf16 %1, %9, %10, %1\n\tds_write_b128 %11, %12 offset:0\n\tv_mfma_f32_16x16x32_bf16 %2, %9, %13, %2\n\tv_rcp_f32 %3, %3\n\tv_mfma_f32_16x16x32_bf16 %4, %9, %14, %4\n\tds_read_b128 %5, %15 offset:19456"
      : "=&v"(pu[0]), "+v"(ac[1][1]), "+v"(ac[1][2]), "+v"(ze[1]), "+v"(ac[1][3]), "=&a"(fwo[14]) : "v"(vF), "s"(rsW), "s"(so0), "v"(pu[1]), "a"(fw2[1][1]), "v"(rwd), "v"(ac[0][0]), "a"(fw2[1][2]), "a"(fw2[1][3]), "v"(ro) : "memory"); SB();
  WAITVB(12);
  SB();
  asm volatile("v_mfma_f32_16x16x32_bf16 %0, %7, %8, %0\n\tbuffer_load_dwordx4 %1, %9, %10, %11 offen offset:128 sc1\n\tv_mfma_f32_16x16x32_bf16 %2, %7, %12, %2\n\tds_write_b128 %13, %14 offset:1024\n\tv_mfma_f32_16x16x32_bf16 %3, %7, %15, %3\n\tv_rcp_f32 %4, %4\n\tv_mfma_f32_16x16x32_bf16 %5, %7, %16, %5\n\tds_read_b128 %6, %17 offset:27648"
      : "+v"(ac[1][0]), "=&v"(pu[1]), "+v"(ac[1][1]), "+v"(ac[1][2]), "+v"(ze[2]), "+v"(ac[1][3]), "=&a"(fwo[15]) : "v"(pu[2]), "a"(fw2[2][0]), "v"(vF), "s"(rsW), "s"(so0), "a"(fw2[2][1]), "v"(rwd), "v"(ac[0][1]), "a"(fw2[2][2]), "a"(fw2[2][3]), "v"(ro) : "memory"); SB();
  WAITVB(12);
  SB();
  asm volatile("v_mfma_f32_16x16x32_bf16 %0, %7, %8, %0\n\tbuffer_load_dwordx4 %1, %9, %10, %11 offen offset:256 sc1\n\tv_mfma_f32_16x16x32_bf16 %2, %7, %12, %2\n\tds_write_b128 %13, %14 offset:2048\n\tv_mfma_f32_16x16x32_bf16 %3, %7, %15, %3\n\tv_rcp_f32 %4, %4\n\tv_mfma_f32_16x16x32_bf16 %5, %7, %16, %5\n\tv_mul_f32 %6, %17, %18"
      : "+v"(ac[1][0]), "=&v"(pu[2]), "+v"(ac[1][1]), "+v"(ac[1][2]), "+v"(ze[3]), "+v"(ac[1][3]), "=&v"(ge[4]) : "v"(pu[3]), "a"(fw2[3][0]), "v"(vF), "s"(rsW), "s"(so0), "a"(fw2[3][1]), "v"(rwd), "v"(ac[0][2]), "a"(fw2[3][2]), "a"(fw2[3][3]), "v"(ye[0]), "v"(ze[0]) : "memory"); SB();
  WAITVB(12);
  SB();
  asm volatile("v_mfma_f32_16x16x32_bf16 %0, %8, %9, %0\n\tbuffer_load_dwordx4 %1, %10, %11, %12 offen offset:384 sc1\n\tv_mfma_f32_16x16x32_bf16 %2, %8, %13, %2\n\tds_write_b128 %14, %15 offset:3072\n\tv_mfma_f32_16x16x32_bf16 %3, %8, %16, %3\n\tv_mul_f32 %4, %17, %18\n\tv_mul_f32 %5, %19, %20\n\tv_mfma_f32_16x16x32_bf16 %6, %8, %21, %6\n\tv_mul_f32 %7, %22, %23"
      : "+v"(ac[1][0]), "=&v"(pu[3]), "+v"(ac[1][1]), "+v"(ac[1][2]), "=&v"(ge[5]), "=&v"(ge[6]), "+v"(ac[1][3]), "=&v"(ge[7]) : "v"(pu[4]), "a"(fw2[4][0]), "v"(vF), "s"(rsW), "s"(so0), "a"(fw2[4][1]), "v"(rwd), "v"(ac[0][3]), "a"(fw2[4][2]), "v"(ye[1]), "v"(ze[1]), "v"(ye[2]), "v"(ze[2]), "a"(fw2[4][3]), "v"(ye[3]), "v"(ze[3]) : "memory"); SB();
  WAITVB(12);
  SB();
  asm volatile("v_mfma_f32_16x16x32_bf16 %0, %3, %4, %0\n\tbuffer_load_dwordx4 %1, %5, %6, %7 offen offset:512 sc1\n\tv_mfma_f32_16x16x32_bf16 %2, %3, %8, %2"
      : "+v"(ac[1][0]), "=&v"(pu[4]), "+v"(ac[1][1]) : "v"(pu[5]), "a"(fw2[5][0]), "v"(vF), "s"(rsW), "s"(so0), "a"(fw2[5][1]) : "memory"); SB();
  WAITL(0);
  BAR0();
  SB();
  asm volatile("v_mfma_f32_16x16x32_bf16 %0, %5, %6, %0\n\tds_read_b128 %1, %7 offset:0\n\tds_read_b128 %2, %7 offset:4096\n\tv_mfma_f32_16x16x32_bf16 %3, %5, %8, %3\n\tds_read_b128 %4, %7 offset:8192"
      : "+v"(ac[1][2]), "=&v"(py[2][0]), "=&v"(py[2][1]), "+v"(ac[1][3]), "=&v"(py[2][2]) : "v"(pu[5]), "a"(fw2[5][2]), "v"(rr), "a"(fw2[5][3]) : "memory"); SB();
  WAITVB(12);
  SB();
  asm volatile("v_mfma_f32_16x16x32_bf16 %0, %8, %9, %0\n\tbuffer_load_dwordx4 %1, %10, %11, %12 offen offset:640 sc1\n\tv_mfma_f32_16x16x32_bf16 %2, %8, %13, %2\n\tds_read_b128 %3, %14 offset:12288\n\tds_read_b128 %4, %15 offset:4096\n\tv_mfma_f32_16x16x32_bf16 %5, %8, %16, %5\n\tv_mfma_f32_16x16x32_bf16 %6, %8, %17, %6\n\tds_read_b128 %7, %15 offset:12288"
      : "+v"(ac[1][0]), "=&v"(pu[5]), "+v"(ac[1][1]), "=&v"(py[2][3]), "=&a"(fwo[16]), "+v"(ac[1][2]), "+v"(ac[1][3]), "=&a"(fwo[17]) : "v"(pu[6]), "a"(fw2[6][0]), "v"(vF), "s"(rsW), "s"(so0), "a"(fw2[6][1]), "v"(rr), "v"(ro), "a"(fw2[6][2]), "a"(fw2[6][3]) : "memory"); SB();
  WAITVB(12);
  SB();
  asm volatile("v_mfma_f32_16x16x32_bf16 %0, %5, %6, %0\n\tbuffer_load_dwordx4 %1, %7, %8, %9 offen offset:768 sc1\n\tv_mfma_f32_16x16x32_bf16 %2, %5, %10, %2\n\tds_read_b128 %3, %11 offset:20480\n\tv_mfma_f32_16x16x32_bf16 %4, %5, %12, %4"
      : "+v"(ac[1][0]), "=&v"(pu[6]), "+v"(ac[1][1]), "=&a"(fwo[18]), "+v"(ac[1][2]) : "v"(pu[7]), "a"(fw2[7][0]), "v"(vF), "s"(rsW), "s"(so0), "a"(fw2[7][1]), "v"(ro), "a"(fw2[7][2]) : "memory"); SB();
  WAITL(3);
  asm volatile("v_add_f32 %0, %3, %4\n\tv_mfma_f32_16x16x32_bf16 %1, %5, %6, %1\n\tv_add_f32 %2, %7, %4"
      : "=&v"(ye[0]), "+v"(ac[1][3]), "=&v"(ye[1]) : "v"(py[2][0][0]), "v"(bias), "v"(pu[7]), "a"(fw2[7][3]), "v"(py[2][0][1])); SB();
  WAITVB(12);
  SB();
  asm volatile("v_mfma_f32_16x16x32_bf16 %0, %9, %10, %0\n\tbuffer_load_dwordx4 %1, %11, %12, %13 offen offset:0 sc1\n\tv_mfma_f32_16x16x32_bf16 %2, %9, %14, %2\n\tv_add_f32 %3, %15, %16\n\tv_add_f32 %4, %17, %16\n\tv_mfma_f32_16x16x32_bf16 %5, %9, %18, %5\n\tv_add_f32 %6, %19, %20\n\tv_add_f32 %7, %21, %22\n\tv_mfma_f32_16x16x32_bf16 %8, %9, %23, %8"
      : "+v"(ac[1][0]), "=&v"(pu[7]), "+v"(ac[1][1]), "=&v"(ye[2]), "=&v"(ye[3]), "+v"(ac[1][2]), "=&v"(ze[0]), "=&v"(ze[1]), "+v"(ac[1][3]) : "v"(pu[8]), "a"(fw2[8][0]), "v"(vF), "s"(rsW), "s"(so1), "a"(fw2[8][1]), "v"(py[2][0][2]), "v"(bias), "v"(py[2][0][3]), "a"(fw2[8][2]), "v"(py[2][1][0]), "v"(py[2][2][0]), "v"(py[2][1][1]), "v"(py[2][2][1]), "a"(fw2[8][3]) : "memory"); SB();
  asm volatile("v_add_f32 %0, %1, %2"
      : "=&v"(ze[2]) : "v"(py[2][1][2]), "v"(py[2][2][2])); SB();
  WAITVB(12);
  SB();
  asm volatile("v_mfma_f32_16x16x32_bf16 %0, %9, %10, %0\n\tbuffer_load_dwordx4 %1, %11, %12, %13 offen offset:128 sc1\n\tv_mfma_f32_16x16x32_bf16 %2, %9, %14, %2\n\tv_add_f32 %3, %15, %16\n\tv_add_f32 %4, %4, %17\n\tv_mfma_f32_16x16x32_bf16 %5, %9, %18, %5\n\tv_add_f32 %6, %6, %19\n\tv_add_f32 %7, %7, %20\n\tv_mfma_f32_16x16x32_bf16 %8, %9, %21, %8"
      : "+v"(ac[1][0]), "=&v"(pu[8]), "+v"(ac[1][1]), "=&v"(ze[3]), "+v"(ye[0]), "+v"(ac[1][2]), "+v"(ye[1]), "+v"(ye[2]), "+v"(ac[1][3]) : "v"(pu[9]), "v"(fw2[9][0]), "v"(vF), "s"(rsW), "s"(so1), "v"(fw2[9][1]), "v"(py[2][1][3]), "v"(py[2][2][3]), "v"(py[2][3][0]), "v"(fw2[9][2]), "v"(py[2][3][1]), "v"(py[2][3][2]), "v"(fw2[9][3]) : "memory"); SB();
  asm volatile("v_add_f32 %0, %0, %1"
      : "+v"(ye[3]) : "v"(py[2][3][3])); SB();
  WAITVB(12);
  SB();
  asm volatile("v_mfma_f32_16x16x32_bf16 %0, %10, %11, %0\n\tbuffer_load_dwordx4 %1, %12, %13, %14 offen offset:256 sc1\n\tv_mfma_f32_16x16x32_bf16 %2, %10, %15, %2\n\tv_add_f32 %3, %3, %4\n\tv_add_f32 %5, %5, %16\n\tv_mfma_f32_16x16x32_bf16 %6, %10, %17, %6\n\tv_add_f32 %7, %7, %18\n\tv_add_f32 %8, %8, %19\n\tv_mfma_f32_16x16x32_bf16 %9, %10, %20, %9\n\tv_mul_f32 %4, 0xbfb8aa3b, %3"
      : "+v"(ac[1][0]), "=&v"(pu[9]), "+v"(ac[1][1]), "+v"(ye[0]), "+v"(ze[0]), "+v"(ye[1]), "+v"(ac[1][2]), "+v"(ye[2]), "+v"(ye[3]), "+v"(ac[1][3]) : "v"(pu[10]), "v"(fw2[10][0]), "v"(vF), "s"(rsW), "s"(so1), "v"(fw2[10][1]), "v"(ze[1]), "v"(fw2[10][2]), "v"(ze[2]), "v"(ze[3]), "v"(fw2[10][3]) : "memory"); SB();
  WAITVB(12);
  SB();
  asm volatile("v_mfma_f32_16x16x32_bf16 %0, %9, %10, %0\n\tbuffer_load_dwordx4 %1, %11, %12, %13 offen offset:384 sc1\n\tv_mfma_f32_16x16x32_bf16 %2, %9, %14, %2\n\tv_mul_f32 %3, 0xbfb8aa3b, %15\n\tv_mul_f32 %4, 0xbfb8aa3b, %16\n\tv_mfma_f32_16x16x32_bf16 %5, %9, %17, %5\n\tds_read_b128 %6, %18 offset:28672\n\tv_mul_f32 %7, 0xbfb8aa3b, %19\n\tv_mfma_f32_16x16x32_bf16 %8, %9, %20, %8"
      : "+v"(ac[1][0]), "=&v"(pu[10]), "+v"(ac[1][1]), "=&v"(ze[1]), "=&v"(ze[2]), "+v"(ac[1][2]), "=&a"(fwo[19]), "=&v"(ze[3]), "+v"(ac[1][3]) : "v"(pu[11]), "v"(fw2[11][0]), "v"(vF), "s"(rsW), "s"(so1), "v"(fw2[11][1]), "v"(ye[1]), "v"(ye[2]), "v"(fw2[11][2]), "v"(ro), "v"(ye[3]), "v"(fw2[11][3]) : "memory"); SB();
  WAITVB(12);
  SB();
  asm volatile("v_mfma_f32_16x16x32_bf16 %0, %8, %9, %0\n\tbuffer_load_dwordx4 %1, %10, %11, %12 offen offset:512 sc1\n\tv_mfma_f32_16x16x32_bf16 %2, %8, %13, %2\n\tv_exp_f32 %3, %3\n\tv_mfma_f32_16x16x32_bf16 %4, %8, %14, %4\n\tv_exp_f32 %5, %5\n\tv_mfma_f32_16x16x32_bf16 %6, %8, %15, %6\n\tds_read_b128 %7, %16 offset:5120"
      : "+v"(ac[1][0]), "=&v"(pu[11]), "+v"(ac[1][1]), "+v"(ze[0]), "+v"(ac[1][2]), "+v"(ze[1]), "+v"(ac[1][3]), "=&a"(fwo[20]) : "v"(pu[12]), "v"(fw2[12][0]), "v"(vF), "s"(rsW), "s"(so1), "v"(fw2[12][1]), "v"(fw2[12][2]), "v"(fw2[12][3]), "v"(ro) : "memory"); SB();
  WAITVB(12);
  SB();
  asm volatile("v_mfma_f32_16x16x32_bf16 %0, %9, %10, %0\n\tbuffer_load_dwordx4 %1, %11, %12, %13 offen offset:640 sc1\n\tv_mfma_f32_16x16x32_bf16 %2, %9, %14, %2\n\tv_exp_f32 %3, %3\n\tv_mfma_f32_16x16x32_bf16 %4, %9, %15, %4\n\tv_exp_f32 %5, %5\n\tv_mfma_f32_16x16x32_bf16 %6, %9, %16, %6\n\tv_add_f32 %7, 1.0, %7\n\tv_add_f32 %8, 1.0, %8"
      : "+v"(ac[1][0]), "=&v"(pu[12]), "+v"(ac[1][1]), "+v"(ze[2]), "+v"(ac[1][2]), "+v"(ze[3]), "+v"(ac[1][3]), "+v"(ze[0]), "+v"(ze[1]) : "v"(pu[13]), "v"(fw2[13][0]), "v"(vF), "s"(rsW), "s"(so1), "v"(fw2[13][1]), "v"(fw2[13][2]), "v"(fw2[13][3]) : "memory"); SB();
  STAMP(43);
  WAITVB(12);
  SB();
  asm volatile("v_mfma_f32_16x16x32_bf16 %0, %9, %10, 0\n\tbuffer_load_dwordx4 %1, %11, %12, %13 offen offset:768 sc1\n\tv_mfma_f32_16x16x32_bf16 %2, %14, %10, 0\n\tv_add_f32 %3, 1.0, %3\n\tv_add_f32 %4, 1.0, %4\n\tv_mfma_f32_16x16x32_bf16 %5, %15, %10, 0\n\tv_rcp_f32 %6, %6\n\tv_mfma_f32_16x16x32_bf16 %7, %16, %10, 0\n\tds_read_b128 %8, %17 offset:13312"
      : "=&v"(as[0]), "=&v"(pu[13]), "=&v"(as[1]), "+v"(ze[2]), "+v"(ze[3]), "=&v"(as[2]), "+v"(ze[0]), "=&v"(as[3]), "=&a"(fwo[21]) : "a"(fwo[0]), "v"(pu[0]), "v"(vF), "s"(rsW), "s"(so1), "a"(fwo[1]), "a"(fwo[2]), "a"(fwo[3]), "v"(ro) : "memory"); SB();
  WAITVB(12);
  SB();
  asm volatile("v_mfma_f32_16x16x32_bf16 %0, %7, %8, %0\n\tds_write_b128 %9, %10 offset:18432\n\tv_mfma_f32_16x16x32_bf16 %1, %11, %8, %1\n\tv_rcp_f32 %2, %2\n\tv_mfma_f32_16x16x32_bf16 %3, %12, %8, %3\n\tv_rcp_f32 %4, %4\n\tv_mfma_f32_16x16x32_bf16 %5, %13, %8, %5\n\tds_read_b128 %6, %14 offset:21504"
      : "+v"(as[0]), "+v"(as[1]), "+v"(ze[1]), "+v"(as[2]), "+v"(ze[2]), "+v"(as[3]), "=&a"(fwo[22]) : "a"(fwo[4]), "v"(pu[1]), "v"(rwd), "v"(ac[1][0]), "a"(fwo[5]), "a"(fwo[6]), "a"(fwo[7]), "v"(ro) : "memory"); SB();
  WAITVB(11);
  SB();
  asm volatile("v_mfma_f32_16x16x32_bf16 %0, %8, %9, %0\n\tds_write_b128 %10, %11 offset:19456\n\tv_mfma_f32_16x16x32_bf16 %1, %12, %9, %1\n\tv_rcp_f32 %2, %2\n\tv_mfma_f32_16x16x32_bf16 %3, %13, %9, %3\n\tv_mul_f32 %4, %14, %15\n\tv_mul_f32 %5, %16, %17\n\tv_mfma_f32_16x16x32_bf16 %6, %18, %9, %6\n\tv_mul_f32 %7, %19, %20"
      : "+v"(as[0]), "+v"(as[1]), "+v"(ze[3]), "+v"(as[2]), "=&v"(ge[8]), "=&v"(ge[9]), "+v"(as[3]), "=&v"(ge[10]) : "a"(fwo[8]), "v"(pu[2]), "v"(rwd), "v"(ac[1][1]), "a"(fwo[9]), "a"(fwo[10]), "v"(ye[0]), "v"(ze[0]), "v"(ye[1]), "v"(ze[1]), "a"(fwo[11]), "v"(ye[2]), "v"(ze[2]) : "memory"); SB();
  WAITVB(10);
  SB();
  asm volatile("v_mfma_f32_16x16x32_bf16 %0, %6, %7, %0\n\tds_write_b128 %8, %9 offset:20480\n\tv_mfma_f32_16x16x32_bf16 %1, %10, %7, %1\n\tds_read_b128 %2, %11 offset:29696\n\tv_mul_f32 %3, %12, %13\n\tv_mfma_f32_16x16x32_bf16 %4, %14, %7, %4\n\tv_mfma_f32_16x16x32_bf16 %5, %15, %7, %5"
      : "+v"(as[0]), "+v"(as[1]), "=&a"(fwo[23]), "=&v"(ge[11]), "+v"(as[2]), "+v"(as[3]) : "a"(fwo[12]), "v"(pu[3]), "v"(rwd), "v"(ac[1][2]), "a"(fwo[13]), "v"(ro), "v"(ye[3]), "v"(ze[3]), "a"(fwo[14]), "a"(fwo[15]) : "memory"); SB();
  WAITVB(9);
  asm volatile("ds_read_b128 %0, %6 offset:6144\n\tds_read_b128 %1, %6 offset:14336\n\tds_read_b128 %2, %6 offset:22528\n\tds_read_b128 %3, %6 offset:30720\n\tds_read_b128 %4, %6 offset:7168\n\tds_read_b128 %5, %6 offset:15360"
      : "=&a"(fwo[24]), "=&a"(fwo[25]), "=&a"(fwo[26]), "=&a"(fwo[27]), "=&a"(fwo[28]), "=&a"(fwo[29]) : "v"(ro) : "memory"); SB();
  WAITL(6);
  SB();
  asm volatile("v_mfma_f32_16x16x32_bf16 %0, %6, %7, %0\n\tds_write_b128 %8, %9 offset:21504\n\tv_mfma_f32_16x16x32_bf16 %1, %10, %7, %1\n\tds_read_b128 %2, %11 offset:23552\n\tv_mfma_f32_16x16x32_bf16 %3, %12, %7, %3\n\tv_mfma_f32_16x16x32_bf16 %4, %13, %7, %4\n\tds_read_b128 %5, %11 offset:31744"
      : "+v"(as[0]), "+v"(as[1]), "=&a"(fwo[30]), "+v"(as[2]), "+v"(as[3]), "=&a"(fwo[31]) : "a"(fwo[16]), "v"(pu[4]), "v"(rwd), "v"(ac[1][3]), "a"(fwo[17]), "v"(ro), "a"(fwo[18]), "a"(fwo[19]) : "memory"); SB();
  WAITVB(8);
  SB();
  asm volatile("v_mfma_f32_16x16x32_bf16 %0, %1, %2, %0"
      : "+v"(as[0]) : "a"(fwo[20]), "v"(pu[5])); SB();
  WAITL(2);
  BAR0();
  SB();
  asm volatile("v_mfma_f32_16x16x32_bf16 %0, %7, %8, %0\n\tds_read_b128 %1, %9 offset:18432\n\tds_read_b128 %2, %9 offset:22528\n\tv_mfma_f32_16x16x32_bf16 %3, %10, %8, %3\n\tds_read_b128 %4, %9 offset:26624\n\tds_read_b128 %5, %9 offset:30720\n\tv_mfma_f32_16x16x32_bf16 %6, %11, %8, %6"
      : "+v"(as[1]), "=&v"(py[3][0]), "=&v"(py[3][1]), "+v"(as[2]), "=&v"(py[3][2]), "=&v"(py[3][3]), "+v"(as[3]) : "a"(fwo[21]), "v"(pu[5]), "v"(rr), "a"(fwo[22]), "a"(fwo[23]) : "memory"); SB();
  WAITVB(7);
  asm volatile("ds_read_b128 %0, %6 offset:32768\n\tds_read_b128 %1, %6 offset:38912\n\tds_read_b128 %2, %6 offset:45056\n\tds_read_b128 %3, %6 offset:51200\n\tds_read_b128 %4, %6 offset:33792\n\tds_read_b128 %5, %6 offset:39936"
      : "=&a"(fwo[32]), "=&a"(fwo[33]), "=&a"(fwo[34]), "=&a"(fwo[35]), "=&a"(fwo[36]), "=&a"(fwo[37]) : "v"(ro) : "memory"); SB();
  WAITL(10);
  SB();
  asm volatile("v_mfma_f32_16x16x32_bf16 %0, %6, %7, %0\n\tds_read_b128 %1, %8 offset:46080\n\tv_mfma_f32_16x16x32_bf16 %2, %9, %7, %2\n\tv_mfma_f32_16x16x32_bf16 %3, %10, %7, %3\n\tds_read_b128 %4, %8 offset:52224\n\tv_mfma_f32_16x16x32_bf16 %5, %11, %7, %5"
      : "+v"(as[0]), "=&a"(fwo[38]), "+v"(as[1]), "+v"(as[2]), "=&a"(fwo[39]), "+v"(as[3]) : "a"(fwo[24]), "v"(pu[6]), "v"(ro), "a"(fwo[25]), "a"(fwo[26]), "a"(fwo[27]) : "memory"); SB();
  WAITL(8);
  SB();
  WAITVB(6);
  SB();
  asm volatile("v_mfma_f32_16x16x32_bf16 %0, %10, %11, %0\n\tv_add_f32 %1, %12, %13\n\tv_add_f32 %2, %14, %13\n\tv_mfma_f32_16x16x32_bf16 %3, %15, %11, %3\n\tv_add_f32 %4, %16, %13\n\tv_add_f32 %5, %17, %13\n\tv_mfma_f32_16x16x32_bf16 %6, %18, %11, %6\n\tv_add_f32 %7, %19, %20\n\tv_add_f32 %8, %21, %22\n\tv_mfma_f32_16x16x32_bf16 %9, %23, %11, %9"
      : "+v"(as[0]), "=&v"(ye[0]), "=&v"(ye[1]), "+v"(as[1]), "=&v"(ye[2]), "=&v"(ye[3]), "+v"(as[2]), "=&v"(ze[0]), "=&v"(ze[1]), "+v"(as[3]) : "a"(fwo[28]), "v"(pu[7]), "v"(py[3][0][0]), "v"(bias), "v"(py[3][0][1]), "a"(fwo[29]), "v"(py[3][0][2]), "v"(py[3][0][3]), "a"(fwo[30]), "v"(py[3][1][0]), "v"(py[3][2][0]), "v"(py[3][1][1]), "v"(py[3][2][1]), "a"(fwo[31])); SB();
  WAITVB(5);
  asm volatile("ds_read_b128 %0, %6 offset:34816\n\tds_read_b128 %1, %6 offset:40960\n\tds_read_b128 %2, %6 offset:47104\n\tds_read_b128 %3, %6 offset:53248\n\tds_read_b128 %4, %6 offset:35840\n\tds_read_b128 %5, %6 offset:41984"
      : "=&a"(fwo[40]), "=&a"(fwo[41]), "=&a"(fwo[42]), "=&a"(fwo[43]), "=&a"(fwo[44]), "=&a"(fwo[45]) : "v"(ro) : "memory"); SB();
  WAITL(6);
  SB();
  asm volatile("v_mfma_f32_16x16x32_bf16 %0, %9, %10, %0\n\tv_add_f32 %1, %11, %12\n\tv_add_f32 %2, %13, %14\n\tv_mfma_f32_16x16x32_bf16 %3, %15, %10, %3\n\tv_add_f32 %4, %4, %16\n\tv_add_f32 %5, %5, %17\n\tv_mfma_f32_16x16x32_bf16 %6, %18, %10, %6\n\tv_add_f32 %7, %7, %19\n\tv_add_f32 %8, %8, %20"
      : "+v"(as[0]), "=&v"(ze[2]), "=&v"(ze[3]), "+v"(as[1]), "+v"(ye[0]), "+v"(ye[1]), "+v"(as[2]), "+v"(ye[2]), "+v"(ye[3]) : "a"(fwo[32]), "v"(pu[8]), "v"(py[3][1][2]), "v"(py[3][2][2]), "v"(py[3][1][3]), "v"(py[3][2][3]), "a"(fwo[33]), "v"(py[3][3][0]), "v"(py[3][3][1]), "a"(fwo[34]), "v"(py[3][3][2]), "v"(py[3][3][3])); SB();
  asm volatile("v_mfma_f32_16x16x32_bf16 %0, %2, %3, %0\n\tv_add_f32 %1, %1, %4"
      : "+v"(as[3]), "+v"(ye[0]) : "a"(fwo[35]), "v"(pu[8]), "v"(ze[0])); SB();
  WAITVB(4);
  SB();
  asm volatile("v_mfma_f32_16x16x32_bf16 %0, %10, %11, %0\n\tv_add_f32 %1, %1, %2\n\tv_add_f32 %3, %3, %4\n\tv_mfma_f32_16x16x32_bf16 %5, %12, %11, %5\n\tv_add_f32 %6, %6, %13\n\tv_mul_f32 %7, 0xbfb8aa3b, %14\n\tv_mfma_f32_16x16x32_bf16 %8, %15, %11, %8\n\tv_mul_f32 %2, 0xbfb8aa3b, %1\n\tv_mul_f32 %4, 0xbfb8aa3b, %3\n\tv_mfma_f32_16x16x32_bf16 %9, %16, %11, %9"
      : "+v"(as[0]), "+v"(ye[1]), "+v"(ze[1]), "+v"(ye[2]), "+v"(ze[2]), "+v"(as[1]), "+v"(ye[3]), "=&v"(ze[0]), "+v"(as[2]), "+v"(as[3]) : "a"(fwo[36]), "v"(pu[9]), "a"(fwo[37]), "v"(ze[3]), "v"(ye[0]), "a"(fwo[38]), "a"(fwo[39])); SB();
  WAITVB(3);
  asm volatile("ds_read_b128 %0, %8 offset:48128\n\tds_read_b128 %1, %8 offset:54272\n\tds_read_b128 %2, %8 offset:36864\n\tds_read_b128 %3, %8 offset:43008\n\tds_read_b128 %4, %8 offset:49152\n\tds_read_b128 %5, %8 offset:55296\n\tds_read_b128 %6, %8 offset:37888\n\tds_read_b128 %7, %8 offset:44032"
      : "=&a"(fwo[46]), "=&a"(fwo[47]), "=&a"(fwo[48]), "=&a"(fwo[49]), "=&a"(fwo[50]), "=&a"(fwo[51]), "=&a"(fwo[52]), "=&a"(fwo[53]) : "v"(ro) : "memory"); SB();
  WAITL(6);
  SB();
  asm volatile("v_mfma_f32_16x16x32_bf16 %0, %9, %10, %0\n\tds_read_b128 %1, %11 offset:50176\n\tv_mul_f32 %2, 0xbfb8aa3b, %12\n\tv_mfma_f32_16x16x32_bf16 %3, %13, %10, %3\n\tv_exp_f32 %4, %4\n\tv_mfma_f32_16x16x32_bf16 %5, %14, %10, %5\n\tv_exp_f32 %6, %6\n\tv_mfma_f32_16x16x32_bf16 %7, %15, %10, %7\n\tds_read_b128 %8, %11 offset:56320"
      : "+v"(as[0]), "=&a"(fwo[54]), "=&v"(ze[3]), "+v"(as[1]), "+v"(ze[0]), "+v"(as[2]), "+v"(ze[1]), "+v"(as[3]), "=&a"(fwo[55]) : "a"(fwo[40]), "v"(pu[10]), "v"(ro), "v"(ye[3]), "a"(fwo[41]), "a"(fwo[42]), "a"(fwo[43]) : "memory"); SB();
  WAITL(0);
  WAITVB(0);
  SB();
  asm volatile("v_mfma_f32_16x16x32_bf16 %0, %9, %10, %0\n\tv_exp_f32 %1, %1\n\tv_mfma_f32_16x16x32_bf16 %0, %11, %12, %0\n\tv_exp_f32 %2, %2\n\tv_mfma_f32_16x16x32_bf16 %0, %13, %14, %0\n\tv_add_f32 %3, 1.0, %3\n\tv_add_f32 %4, 1.0, %4\n\tv_mfma_f32_16x16x32_bf16 %5, %15, %10, %5\n\tv_add_f32 %1, 1.0, %1\n\tv_add_f32 %2, 1.0, %2\n\tv_mfma_f32_16x16x32_bf16 %5, %16, %12, %5\n\tv_rcp_f32 %3, %3\n\tv_mfma_f32_16x16x32_bf16 %5, %17, %14, %5\n\tv_rcp_f32 %4, %4\n\tv_mfma_f32_16x16x32_bf16 %6, %18, %10, %6\n\tv_rcp_f32 %1, %1\n\tv_mfma_f32_16x16x32_bf16 %6, %19, %12, %6\n\tv_rcp_f32 %2, %2\n\tv_mfma_f32_16x16x32_bf16 %6, %20, %14, %6\n\tv_mul_f32 %7, %21, %3\n\tv_mul_f32 %8, %22, %4"
      : "+v"(as[0]), "+v"(ze[2]), "+v"(ze[3]), "+v"(ze[0]), "+v"(ze[1]), "+v"(as[1]), "+v"(as[2]), "=&v"(ge[12]), "=&v"(ge[13]) : "a"(fwo[44]), "v"(pu[11]), "a"(fwo[48]), "v"(pu[12]), "a"(fwo[52]), "v"(pu[13]), "a"(fwo[45]), "a"(fwo[49]), "a"(fwo[53]), "a"(fwo[46]), "a"(fwo[50]), "a"(fwo[54]), "v"(ye[0]), "v"(ye[1])); SB();
  asm volatile("v_mfma_f32_16x16x32_bf16 %0, %7, %8, %0\n\tv_mul_f32 %1, %9, %10\n\tv_mul_f32 %2, %11, %12\n\tv_mfma_f32_16x16x32_bf16 %0, %13, %14, %0\n\tv_fma_f32 %3, %15, %16, %3\n\tv_fma_f32 %4, %17, %18, %4\n\tv_mfma_f32_16x16x32_bf16 %0, %19, %20, %0\n\tv_fma_f32 %5, %21, %22, %5\n\tv_fma_f32 %6, %23, %24, %6"
      : "+v"(as[3]), "=&v"(ge[14]), "=&v"(ge[15]), "+v"(xe[0]), "+v"(xe[1]), "+v"(xe[2]), "+v"(xe[3]) : "a"(fwo[47]), "v"(pu[11]), "v"(ye[2]), "v"(ze[2]), "v"(ye[3]), "v"(ze[3]), "a"(fwo[51]), "v"(pu[12]), "v"(as[0][0]), "v"(ge[0]), "v"(as[0][1]), "v"(ge[1]), "a"(fwo[55]), "v"(pu[13]), "v"(as[0][2]), "v"(ge[2]), "v"(as[0][3]), "v"(ge[3])); SB();
  STAMP(44);
  asm volatile("v_cvt_pk_bf16_f32 %0, %8, %9\n\tv_cvt_pk_bf16_f32 %1, %10, %11\n\tv_fma_f32 %2, %12, %13, %2\n\tv_fma_f32 %3, %14, %15, %3\n\tv_fma_f32 %4, %16, %17, %4\n\tv_fma_f32 %5, %18, %19, %5\n\tv_cvt_pk_bf16_f32 %6, %2, %3\n\tv_cvt_pk_bf16_f32 %7, %4, %5"
      : "=&v"(ov[0][0]), "=&v"(ov[0][1]), "+v"(xe[4]), "+v"(xe[5]), "+v"(xe[6]), "+v"(xe[7]), "=&v"(ov[1][0]), "=&v"(ov[1][1]) : "v"(xe[0]), "v"(xe[1]), "v"(xe[2]), "v"(xe[3]), "v"(as[1][0]), "v"(ge[4]), "v"(as[1][1]), "v"(ge[5]), "v"(as[1][2]), "v"(ge[6]), "v"(as[1][3]), "v"(ge[7])); SB();
  asm volatile("ds_write_b64 %8, %9 offset:0\n\tv_fma_f32 %0, %10, %11, %0\n\tv_fma_f32 %1, %12, %13, %1\n\tv_fma_f32 %2, %14, %15, %2\n\tv_fma_f32 %3, %16, %17, %3\n\tv_cvt_pk_bf16_f32 %4, %0, %1\n\tv_cvt_pk_bf16_f32 %5, %2, %3\n\tds_write_b64 %18, %19 offset:0\n\tv_fma_f32 %6, %20, %21, %6\n\tv_fma_f32 %7, %22, %23, %7"
      : "+v"(xe[8]), "+v"(xe[9]), "+v"(xe[10]), "+v"(xe[11]), "=&v"(ov[2][0]), "=&v"(ov[2][1]), "+v"(xe[12]), "+v"(xe[13]) : "v"(xta[0]), "v"(ov[0]), "v"(as[2][0]), "v"(ge[8]), "v"(as[2][1]), "v"(ge[9]), "v"(as[2][2]), "v"(ge[10]), "v"(as[2][3]), "v"(ge[11]), "v"(xta[1]), "v"(ov[1]), "v"(as[3][0]), "v"(ge[12]), "v"(as[3][1]), "v"(ge[13]) : "memory"); SB();
  asm volatile("v_fma_f32 %0, %4, %5, %0\n\tv_fma_f32 %1, %6, %7, %1\n\tv_cvt_pk_bf16_f32 %2, %8, %9\n\tv_cvt_pk_bf16_f32 %3, %0, %1\n\tds_write_b64 %10, %11 offset:0"
      : "+v"(xe[14]), "+v"(xe[15]), "=&v"(ov[3][0]), "=&v"(ov[3][1]) : "v"(as[3][2]), "v"(ge[14]), "v"(as[3][3]), "v"(ge[15]), "v"(xe[12]), "v"(xe[13]), "v"(xta[2]), "v"(ov[2]) : "memory"); SB();
  asm volatile("ds_write_b64 %0, %1 offset:0"
      :  : "v"(xta[3]), "v"(ov[3]) : "memory"); SB();
  { u32x4 t0_, t1_; DSR16(t0_, xo, 0); DSR16(t1_, xo, 1024); WAITLB(1);
    BS16(vXa, rsO_, 0, t0_, "sc1"); WAITLB(0); BS16(vXb, rsO_, 1024, t1_, "sc1"); }
  STAMP(23);

  STAMP(12);
  STAMP_DUMP();
  }
  } else if (jb < 28) {
  asm volatile("; NEED B");
  u32x4 rs1 = srd(w1, 2048u * 3584u);
  STAMP(1);
  unsigned nonce; asm volatile("s_load_dword %0, %1, 0x0" : "=&s"(nonce) : "s"(ws) : "memory");
  const unsigned trb = (8 * g4 + q4) * 128 + pq * 8, trf = ((q4 >> 1) | ((g4 & 1) << 1)) * 32;
  const unsigned vF = r8 * 1024 + (lane & 7) * 16;
  unsigned tX[4];
#pragma unroll
  for (int i = 0; i < 4; ++i) { tX[i] = ldsR + trb + ((i * 32 ^ w * 32) ^ trf); OPV(tX[i]); }
  bf16x8 fw1[16][4], p[14]; u32x2 bv; unsigned sh[16], sh0 = WS_HT + d4 * 524288; OPQ(sh0);
  asm volatile("s_waitcnt lgkmcnt(0)" : "+s"(nonce) :: "memory"); nonce += 1u;
  asm volatile("s_waitcnt vmcnt(8)"
      :  :  : "memory"); SB();
  fw1[0][0] = tr8u(tX[0] + 0, 512);
  fw1[0][1] = tr8u(tX[1] + 0, 512);
  fw1[0][2] = tr8u(tX[2] + 0, 512);
  fw1[0][3] = tr8u(tX[3] + 0, 512);
  STAMP(50);
  { int k_ = KS1(3); OPQ(k_); DMA4(vW1a, vW1b, rs1, k_ * 114688, 55296, ldsR, 12288); sh[3] = sh0 + k_ * 8192; OPQ(sh[3]); }
  SB();
  { const unsigned vB = (ft * 64 + w * 16 + g4 * 4) * 2;
    LD8(bv, vB, b1, 0); }
  u32x4 rsW = srd((char*)(((size_t)ws + 8191) & ~(size_t)4095) + 512, WS_END); OPQ(rsW);
  #pragma unroll
  for (int c = 0; c < 3; ++c) { sh[c] = sh0 + KS1(c) * 8192; OPQ(sh[c]); }
  unsigned fh, vFH = WS_FA + (d4 * 128 + w * 32 + (lane & 31)) * (4 * 16);
  unsigned rw = lds0 + w * SCW + vL16, rr1 = lds0 + (w ^ 1) * SCW + vL16, rr2 = lds0 + (w ^ 2) * SCW + vL16, rr3 = lds0 + (w ^ 3) * SCW + vL16;
  unsigned vU = (g4 & 1) * 128 + ((g4 >> 1) * 2 + (n16 >> 3)) * 1024 + (n16 & 7) * 16, sU, sFU = WS_FU + (d4 * 128 + ft * 4 + w) * (4 * 4);
  sU = WS_UT + (d4 * 56 + 2 * ft) * 8192 + w * 4096;
  const int sp1 = (fi & 1);
  unsigned vHa = vF + (d4 == 1 ? 128 : 0) + 128 * sp1, vHc = vF + (d4 == 1 ? 128 : 0) + 128 - 128 * sp1, vHb = vF + (d4 == 1 ? 0 : 768), vU6 = vU + (d4 == 1 ? 0 : 768); vU = (vU ^ (128 * sp1)) + (d4 == 1 ? 128 : 0); OPV(vHa); OPV(vHc); OPV(vHb); OPV(vU6);
  float gc = 0.10294324f;
  asm volatile("" : "+v"(vFH), "+v"(rw), "+v"(rr1), "+v"(rr2), "+v"(rr3), "+v"(vU), "+v"(gc)); asm volatile("" : "+s"(sU), "+s"(sFU));
  const int r2 = (3 - (int)((size_t)w2 >> 7)) & 7;
  const int st = 8 * xq + ((r2 + 1 + fi) & 7), dt = d4, db0 = 16 + 3 * dt;
#undef BLK
#define BLK(j) (db0 + ((j) == 2 ? 2 - 2 * (dt & 1) : ((j) ^ 0) + (dt & 1)))
  SB();
  asm volatile("s_waitcnt vmcnt(9)"
      :  :  : "memory"); SB();
  fw1[1][0] = tr8u(tX[0] + 4096, 512);
  fw1[1][1] = tr8u(tX[1] + 4096, 512);
  fw1[1][2] = tr8u(tX[2] + 4096, 512);
  fw1[1][3] = tr8u(tX[3] + 4096, 512);
  { int k_ = KS1(4); OPQ(k_); DMA4(vW1a, vW1b, rs1, k_ * 114688, 55296, ldsR, 16384); sh[4] = sh0 + k_ * 8192; OPQ(sh[4]); }
  SB();
  u32x4 rs2 = srd(w2, 1792u * 4096u); OPQ(rs2);
  unsigned vW2[4];
#pragma unroll
  for (int i = 0; i < 4; ++i) vW2[i] = (8 * (r8 & 3) + 4 * (r8 >> 2)) * 4096 + st * 128 + ((lane & 7) ^ (2 * i)) * 16 + i * (4096 - 1024);
  int rot2 = 4 * dt + 2 * (st & 7) + 2; rot2 -= 14 * ((rot2 * 37) >> 9);
  bf16x8 fw2[14][4];
  unsigned ldsS = lds0 + S3B + w * 4096, ldsO = lds0 + WOB + w * 8192, ldsP = lds0 + WOB + 24576 + w * 6144, sWo, vWo = r8 * 16384 + (lane & 7) * 16, sWt;
  { const int k_ = BLK(w), k7 = (k_ * 37) >> 8; sWo = WS_WO + k7 * (131072 - 896) + k_ * 128; }
  unsigned fq, vFQ = WS_FQ + (lane < 56 ? lane : 55) * (4 * 4);
  asm volatile("" : "+v"(vW2[0]), "+v"(vW2[1]), "+v"(vW2[2]), "+v"(vW2[3]), "+v"(vFQ), "+v"(vWo)); asm volatile("" : "+s"(rot2), "+s"(ldsS), "+s"(ldsO), "+s"(ldsP), "+s"(sWo));
  if (w == 3) vWo = 0x40000000u; OPV(vWo);
  unsigned tS[4];
#pragma unroll
  for (int i = 0; i < 4; ++i) { tS[i] = ldsS + q4 * 1024 + g4 * 128 + pq * 8 + ((i ^ q4) * 32); OPV(tS[i]); }
  unsigned tO[4];
#pragma unroll
  for (int i = 0; i < 4; ++i) { tO[i] = ldsO + q4 * 1024 + g4 * 128 + pq * 8 + ((i ^ q4) * 32); OPV(tO[i]); }
  unsigned su[14], sw[14], sc_ = 14 * w + rot2; OPQ(sc_);
  SB();
  asm volatile("s_waitcnt vmcnt(9)"
      :  :  : "memory"); SB();
  fw1[2][0] = tr8u(tX[0] + 8192, 512);
  fw1[2][1] = tr8u(tX[1] + 8192, 512);
  fw1[2][2] = tr8u(tX[2] + 8192, 512);
  fw1[2][3] = tr8u(tX[3] + 8192, 512);
  { int k_ = KS1(5); OPQ(k_); DMA4(vW1a, vW1b, rs1, k_ * 114688, 55296, ldsR, 20480); sh[5] = sh0 + k_ * 8192; OPQ(sh[5]); }
  SB();
  unsigned vFU, vFO;
  { const int da = (db0 * 37) >> 8, db = ((db0 + 2) * 37) >> 8, l_ = lane < 56 ? lane : 55;
    vFU = WS_FU + ((l_ < 28 ? da * 128 + l_ : db * 128 + l_ - 28) + w * 28) * (4 * 4);
    vFO = WS_FO + ((2 * st + (w >> 1)) * 8 + (lane & 7)) * (4 * 16);
    asm volatile("" : "+v"(vFU), "+v"(vFO)); }
  unsigned vFU1 = WS_FU + (512 + ((db0 * 37) >> 8) * 128 + w * 28 + (lane < 28 ? lane : 27)) * (4 * 4), fu2; OPV(vFU1);
  unsigned vFj[4], so0 = WS_OT + (4 * st + w) * 16384, so1 = so0 + 8192;
#pragma unroll
  for (int j = 0; j < 3; ++j) { const int k_ = BLK(j), k7 = (k_ * 37) >> 8; vFj[j] = vF + (WS_UT + k7 * (56 * 8192 - 896) + k_ * 128); OPV(vFj[j]); }
  asm volatile("" : "+s"(so0), "+s"(so1));
  SB();
  asm volatile("s_waitcnt vmcnt(9)"
      :  :  : "memory"); SB();
  fw1[3][0] = tr8u(tX[0] + 12288, 512);
  fw1[3][1] = tr8u(tX[1] + 12288, 512);
  fw1[3][2] = tr8u(tX[2] + 12288, 512);
  fw1[3][3] = tr8u(tX[3] + 12288, 512);
  STAMP(51);
  { int k_ = KS1(6); OPQ(k_); DMA4(vW1a, vW1b, rs1, k_ * 114688, 55296, ldsR, 24576); sh[6] = sh0 + k_ * 8192; OPQ(sh[6]); }
  SB();
  const int tau = 2 * (n16 & 7) + (n16 >> 3);
  unsigned rwd = ldsS + vL16, rr = lds0 + S3B + w * 1024 + (16 * g4 + tau) * 16, ro = lds0 + WOB + vL16;
  unsigned xo = ldsS + 34816 + vL16, braw, xt = ldsS + 34816 + tau * 128 + (((g4 >> 1) ^ (n16 & 7)) * 16) + (g4 & 1) * 8, vB2 = (64 * st + 16 * w + tau) * 2;
  unsigned xs = xt ^ (32 * 0);
  asm volatile("" : "+v"(rwd), "+v"(rr), "+v"(ro), "+v"(xt), "+v"(xs), "+v"(xo), "+v"(vB2));
  SB();
  asm volatile("s_waitcnt vmcnt(8)"
      :  :  : "memory"); SB();
  fw1[4][0] = tr8u(tX[0] + 16384, 512);
  fw1[4][1] = tr8u(tX[1] + 16384, 512);
  fw1[4][2] = tr8u(tX[2] + 16384, 512);
  fw1[4][3] = tr8u(tX[3] + 16384, 512);
  WAITL(8);
  { int k_ = KS1(7); OPQ(k_); DMA4(vW1a, vW1b, rs1, k_ * 114688, 55296, ldsR, 0); sh[7] = sh0 + k_ * 8192; OPQ(sh[7]); }
  SB();
  unsigned vXa, vXb;
  { const int ga = (lane & 7) ^ (r8 >> 1), gb = ga ^ 4, r_ = (64 * st + 16 * w + r8) * 896;
    vXa = ga < 4 ? r_ + (db0 + (dt & 1)) * 32 + ga * 16 : ga < 6 ? r_ + BLK(2) * 32 + (ga - 4) * 16 : 0x40000000;
    vXb = (gb < 4 ? r_ + (db0 + (dt & 1)) * 32 + gb * 16 : gb < 6 ? r_ + BLK(2) * 32 + (gb - 4) * 16 : 0x40000000) + (8 * 896 - 1024); OPV(vXa); OPV(vXb); }
  SB();
  asm volatile("s_waitcnt vmcnt(8)"
      :  :  : "memory"); SB();
  fw1[5][0] = tr8u(tX[0] + 20480, 512);
  fw1[5][1] = tr8u(tX[1] + 20480, 512);
  fw1[5][2] = tr8u(tX[2] + 20480, 512);
  fw1[5][3] = tr8u(tX[3] + 20480, 512);
  WAITL(8);
  { int k_ = KS1(8); OPQ(k_); DMA4(vW1a, vW1b, rs1, k_ * 114688, 55296, ldsR, 4096); sh[8] = sh0 + k_ * 8192; OPQ(sh[8]); }
  SB();
  { unsigned k_ = sc_ + (rot2 >= 14 ? -14 : 0); OPQ(k_); su[0] = k_ << 13; sw[0] = k_ << 17; OPQ(su[0]); OPQ(sw[0]); }
  { unsigned k_ = sc_ + (rot2 >= 13 ? -13 : 1); OPQ(k_); su[1] = k_ << 13; sw[1] = k_ << 17; OPQ(su[1]); OPQ(sw[1]); }
  { unsigned k_ = sc_ + (rot2 >= 12 ? -12 : 2); OPQ(k_); su[2] = k_ << 13; sw[2] = k_ << 17; OPQ(su[2]); OPQ(sw[2]); }
  SB();
  asm volatile("s_waitcnt vmcnt(8)"
      :  :  : "memory"); SB();
  fw1[6][0] = tr8u(tX[0] + 24576, 512);
  fw1[6][1] = tr8u(tX[1] + 24576, 512);
  fw1[6][2] = tr8u(tX[2] + 24576, 512);
  fw1[6][3] = tr8u(tX[3] + 24576, 512);
  WAITV1(20, bv);
  f32x4 bb = {lo2f(bv[0]), hi2f(bv[0]), lo2f(bv[1]), hi2f(bv[1])}; OPV(bb);
  WAITL(8);
  { int k_ = KS1(9); OPQ(k_); DMA4(vW1a, vW1b, rs1, k_ * 114688, 55296, ldsR, 8192); sh[9] = sh0 + k_ * 8192; OPQ(sh[9]); }
  SB();
  { unsigned k_ = sc_ + (rot2 >= 11 ? -11 : 3); OPQ(k_); su[3] = k_ << 13; sw[3] = k_ << 17; OPQ(su[3]); OPQ(sw[3]); }
  { unsigned k_ = sc_ + (rot2 >= 10 ? -10 : 4); OPQ(k_); su[4] = k_ << 13; sw[4] = k_ << 17; OPQ(su[4]); OPQ(sw[4]); }
  { unsigned k_ = sc_ + (rot2 >= 9 ? -9 : 5); OPQ(k_); su[5] = k_ << 13; sw[5] = k_ << 17; OPQ(su[5]); OPQ(sw[5]); }
  SB();
  asm volatile("s_waitcnt vmcnt(8)"
      :  :  : "memory"); SB();
  fw1[7][0] = tr8u(tX[0] + 0, 512);
  fw1[7][1] = tr8u(tX[1] + 0, 512);
  fw1[7][2] = tr8u(tX[2] + 0, 512);
  fw1[7][3] = tr8u(tX[3] + 0, 512);
  STAMP(52);
  WAITL(8);
  { int k_ = KS1(10); OPQ(k_); DMA4(vW1a, vW1b, rs1, k_ * 114688, 55296, ldsR, 12288); sh[10] = sh0 + k_ * 8192; OPQ(sh[10]); }
  SB();
  { unsigned k_ = sc_ + (rot2 >= 8 ? -8 : 6); OPQ(k_); su[6] = k_ << 13; sw[6] = k_ << 17; OPQ(su[6]); OPQ(sw[6]); }
  { unsigned k_ = sc_ + (rot2 >= 7 ? -7 : 7); OPQ(k_); su[7] = k_ << 13; sw[7] = k_ << 17; OPQ(su[7]); OPQ(sw[7]); }
  { unsigned k_ = sc_ + (rot2 >= 6 ? -6 : 8); OPQ(k_); su[8] = k_ << 13; sw[8] = k_ << 17; OPQ(su[8]); OPQ(sw[8]); }
  SB();
  asm volatile("s_waitcnt vmcnt(8)"
      :  :  : "memory"); SB();
  fw1[8][0] = tr8u(tX[0] + 4096, 512);
  fw1[8][1] = tr8u(tX[1] + 4096, 512);
  fw1[8][2] = tr8u(tX[2] + 4096, 512);
  fw1[8][3] = tr8u(tX[3] + 4096, 512);
  WAITL(8);
  { int k_ = KS1(11); OPQ(k_); DMA4(vW1a, vW1b, rs1, k_ * 114688, 55296, ldsR, 16384); sh[11] = sh0 + k_ * 8192; OPQ(sh[11]); }
  SB();
  { unsigned k_ = sc_ + (rot2 >= 5 ? -5 : 9); OPQ(k_); su[9] = k_ << 13; sw[9] = k_ << 17; OPQ(su[9]); OPQ(sw[9]); }
  { unsigned k_ = sc_ + (rot2 >= 4 ? -4 : 10); OPQ(k_); su[10] = k_ << 13; sw[10] = k_ << 17; OPQ(su[10]); OPQ(sw[10]); }
  { unsigned k_ = sc_ + (rot2 >= 3 ? -3 : 11); OPQ(k_); su[11] = k_ << 13; sw[11] = k_ << 17; OPQ(su[11]); OPQ(sw[11]); }
  SB();
  asm volatile("s_waitcnt vmcnt(8)"
      :  :  : "memory"); SB();
  fw1[9][0] = tr8u(tX[0] + 8192, 512);
  fw1[9][1] = tr8u(tX[1] + 8192, 512);
  fw1[9][2] = tr8u(tX[2] + 8192, 512);
  fw1[9][3] = tr8u(tX[3] + 8192, 512);
  WAITL(8);
  { int k_ = KS1(12); OPQ(k_); DMA4(vW1a, vW1b, rs1, k_ * 114688, 55296, ldsR, 20480); sh[12] = sh0 + k_ * 8192; OPQ(sh[12]); }
  SB();
  { unsigned k_ = sc_ + (rot2 >= 2 ? -2 : 12); OPQ(k_); su[12] = k_ << 13; sw[12] = k_ << 17; OPQ(su[12]); OPQ(sw[12]); }
  { unsigned k_ = sc_ + (rot2 >= 1 ? -1 : 13); OPQ(k_); su[13] = k_ << 13; sw[13] = k_ << 17; OPQ(su[13]); OPQ(sw[13]); }
  SB();
  asm volatile("s_waitcnt vmcnt(8)"
      :  :  : "memory"); SB();
  fw1[10][0] = tr8u(tX[0] + 12288, 512);
  fw1[10][1] = tr8u(tX[1] + 12288, 512);
  fw1[10][2] = tr8u(tX[2] + 12288, 512);
  fw1[10][3] = tr8u(tX[3] + 12288, 512);
  WAITL(8);
  { int k_ = KS1(13); OPQ(k_); DMA4(vW1a, vW1b, rs1, k_ * 114688, 55296, ldsR, 24576); sh[13] = sh0 + k_ * 8192; OPQ(sh[13]); }
  asm volatile("s_waitcnt vmcnt(8)"
      :  :  : "memory"); SB();
  fw1[11][0] = tr8u(tX[0] + 16384, 512);
  fw1[11][1] = tr8u(tX[1] + 16384, 512);
  fw1[11][2] = tr8u(tX[2] + 16384, 512);
  fw1[11][3] = tr8u(tX[3] + 16384, 512);
  STAMP(53);
  WAITL(8);
  { int k_ = KS1(14); OPQ(k_); DMA4(vW1a, vW1b, rs1, k_ * 114688, 55296, ldsR, 0); sh[14] = sh0 + k_ * 8192; OPQ(sh[14]); }
  asm volatile("s_waitcnt vmcnt(8)"
      :  :  : "memory"); SB();
  fw1[12][0] = tr8u(tX[0] + 20480, 512);
  fw1[12][1] = tr8u(tX[1] + 20480, 512);
  fw1[12][2] = tr8u(tX[2] + 20480, 512);
  fw1[12][3] = tr8u(tX[3] + 20480, 512);
  WAITL(8);
  { int k_ = KS1(15); OPQ(k_); DMA4(vW1a, vW1b, rs1, k_ * 114688, 55296, ldsR, 4096); sh[15] = sh0 + k_ * 8192; OPQ(sh[15]); }
  asm volatile("buffer_load_dword %0, %1, %2, 0 offen sc1\n\ts_waitcnt vmcnt(9)"
      : "=&v"(fh) : "v"(vFH), "s"(rsW) : "memory"); SB();
  fw1[13][0] = tr8u(tX[0] + 24576, 512);
  fw1[13][1] = tr8u(tX[1] + 24576, 512);
  fw1[13][2] = tr8u(tX[2] + 24576, 512);
  fw1[13][3] = tr8u(tX[3] + 24576, 512);
  WAITV1(0, fh);
  if (__ballot(fh != nonce) != 0ull) poll1<4>(rsW, vFH, nonce);
  STAMP(6);
  asm volatile("buffer_load_dwordx4 %0, %4, %5, %6 offen offset:0 sc1\n\tbuffer_load_dwordx4 %1, %4, %5, %7 offen offset:0 sc1\n\tbuffer_load_dwordx4 %2, %4, %5, %8 offen offset:0 sc1\n\tbuffer_load_dwordx4 %3, %4, %5, %9 offen offset:0 sc1\n\ts_waitcnt vmcnt(9)"
      : "=&v"(p[0]), "=&v"(p[1]), "=&v"(p[2]), "=&v"(p[3]) : "v"(vHa), "s"(rsW), "s"(sh[0]), "s"(sh[1]), "s"(sh[2]), "s"(sh[3]) : "memory"); SB();
  fw1[14][0] = tr8u(tX[0] + 0, 512);
  fw1[14][1] = tr8u(tX[1] + 0, 512);
  fw1[14][2] = tr8u(tX[2] + 0, 512);
  fw1[14][3] = tr8u(tX[3] + 0, 512);
  asm volatile("buffer_load_dwordx4 %0, %4, %5, %6 offen offset:0 sc1\n\tbuffer_load_dwordx4 %1, %4, %5, %7 offen offset:0 sc1\n\tbuffer_load_dwordx4 %2, %4, %5, %8 offen offset:0 sc1\n\tbuffer_load_dwordx4 %3, %4, %5, %9 offen offset:0 sc1\n\ts_waitcnt vmcnt(9)"
      : "=&v"(p[4]), "=&v"(p[5]), "=&v"(p[6]), "=&v"(p[7]) : "v"(vHa), "s"(rsW), "s"(sh[4]), "s"(sh[5]), "s"(sh[6]), "s"(sh[7]) : "memory"); SB();
  fw1[15][0] = tr8u(tX[0] + 4096, 512);
  fw1[15][1] = tr8u(tX[1] + 4096, 512);
  fw1[15][2] = tr8u(tX[2] + 4096, 512);
  fw1[15][3] = tr8u(tX[3] + 4096, 512);
  STAMP(5);
  asm volatile("buffer_load_dwordx4 %0, %4, %5, %6 offen offset:0 sc1\n\tbuffer_load_dwordx4 %1, %4, %5, %7 offen offset:0 sc1\n\tbuffer_load_dwordx4 %2, %4, %5, %8 offen offset:0 sc1\n\tbuffer_load_dwordx4 %3, %4, %5, %9 offen offset:0 sc1"
      : "=&v"(p[8]), "=&v"(p[9]), "=&v"(p[10]), "=&v"(p[11]) : "v"(vHa), "s"(rsW), "s"(sh[8]), "s"(sh[9]), "s"(sh[10]), "s"(sh[11]) : "memory"); SB();
  f32x4 acc[7][4], pm[7][3]; float gm[4], gz[4]; unsigned uv[8][2];
  WAITVB(11);
  SB();
  asm volatile("v_mfma_f32_16x16x32_bf16 %0, %6, %7, 0\n\tbuffer_load_dwordx4 %1, %8, %9, %10 offen offset:0 sc1\n\tv_mfma_f32_16x16x32_bf16 %2, %11, %7, 0\n\tv_mfma_f32_16x16x32_bf16 %3, %12, %7, 0\n\tbuffer_load_dwordx4 %4, %8, %9, %13 offen offset:0 sc1\n\tv_mfma_f32_16x16x32_bf16 %5, %14, %7, %15"
      : "=&v"(acc[0][1]), "=&v"(p[12]), "=&v"(acc[0][2]), "=&v"(acc[0][3]), "=&v"(p[13]), "=&v"(acc[0][0]) : "a"(fw1[0][1]), "v"(p[0]), "v"(vHa), "s"(rsW), "s"(sh[12]), "a"(fw1[0][2]), "a"(fw1[0][3]), "s"(sh[13]), "a"(fw1[0][0]), "v"(bb) : "memory"); SB();
  WAITVB(12);
  SB();
  asm volatile("v_mfma_f32_16x16x32_bf16 %0, %5, %6, %0\n\tbuffer_load_dwordx4 %1, %7, %8, %9 offen offset:0 sc1\n\tv_mfma_f32_16x16x32_bf16 %2, %10, %6, %2\n\tv_mfma_f32_16x16x32_bf16 %3, %11, %6, %3\n\tv_mfma_f32_16x16x32_bf16 %4, %12, %6, %4"
      : "+v"(acc[0][1]), "=&v"(p[0]), "+v"(acc[0][2]), "+v"(acc[0][3]), "+v"(acc[0][0]) : "a"(fw1[1][1]), "v"(p[1]), "v"(vHa), "s"(rsW), "s"(sh[14]), "a"(fw1[1][2]), "a"(fw1[1][3]), "a"(fw1[1][0]) : "memory"); SB();
  WAITVB(12);
  SB();
  asm volatile("v_mfma_f32_16x16x32_bf16 %0, %5, %6, %0\n\tbuffer_load_dwordx4 %1, %7, %8, %9 offen offset:0 sc1\n\tv_mfma_f32_16x16x32_bf16 %2, %10, %6, %2\n\tv_mfma_f32_16x16x32_bf16 %3, %11, %6, %3\n\tv_mfma_f32_16x16x32_bf16 %4, %12, %6, %4"
      : "+v"(acc[0][1]), "=&v"(p[1]), "+v"(acc[0][2]), "+v"(acc[0][3]), "+v"(acc[0][0]) : "a"(fw1[2][1]), "v"(p[2]), "v"(vHa), "s"(rsW), "s"(sh[15]), "a"(fw1[2][2]), "a"(fw1[2][3]), "a"(fw1[2][0]) : "memory"); SB();
  WAITVB(12);
  SB();
  asm volatile("v_mfma_f32_16x16x32_bf16 %0, %5, %6, %0\n\tbuffer_load_dwordx4 %1, %7, %8, %9 offen offset:0 sc1\n\tv_mfma_f32_16x16x32_bf16 %2, %10, %6, %2\n\tv_mfma_f32_16x16x32_bf16 %3, %11, %6, %3\n\tv_mfma_f32_16x16x32_bf16 %4, %12, %6, %4"
      : "+v"(acc[0][1]), "=&v"(p[2]), "+v"(acc[0][2]), "+v"(acc[0][3]), "+v"(acc[0][0]) : "a"(fw1[3][1]), "v"(p[3]), "v"(vHc), "s"(rsW), "s"(sh[0]), "a"(fw1[3][2]), "a"(fw1[3][3]), "a"(fw1[3][0]) : "memory"); SB();
  WAITVB(12);
  SB();
  asm volatile("v_mfma_f32_16x16x32_bf16 %0, %5, %6, %0\n\tbuffer_load_dwordx4 %1, %7, %8, %9 offen offset:0 sc1\n\tv_mfma_f32_16x16x32_bf16 %2, %10, %6, %2\n\tv_mfma_f32_16x16x32_bf16 %3, %11, %6, %3\n\tv_mfma_f32_16x16x32_bf16 %4, %12, %6, %4"
      : "+v"(acc[0][1]), "=&v"(p[3]), "+v"(acc[0][2]), "+v"(acc[0][3]), "+v"(acc[0][0]) : "a"(fw1[4][1]), "v"(p[4]), "v"(vHc), "s"(rsW), "s"(sh[1]), "a"(fw1[4][2]), "a"(fw1[4][3]), "a"(fw1[4][0]) : "memory"); SB();
  WAITVB(12);
  SB();
  asm volatile("v_mfma_f32_16x16x32_bf16 %0, %5, %6, %0\n\tbuffer_load_dwordx4 %1, %7, %8, %9 offen offset:0 sc1\n\tv_mfma_f32_16x16x32_bf16 %2, %10, %6, %2\n\tv_mfma_f32_16x16x32_bf16 %3, %11, %6, %3\n\tv_mfma_f32_16x16x32_bf16 %4, %12, %6, %4"
      : "+v"(acc[0][1]), "=&v"(p[4]), "+v"(acc[0][2]), "+v"(acc[0][3]), "+v"(acc[0][0]) : "a"(fw1[5][1]), "v"(p[5]), "v"(vHc), "s"(rsW), "s"(sh[2]), "a"(fw1[5][2]), "a"(fw1[5][3]), "a"(fw1[5][0]) : "memory"); SB();
  WAITVB(12);
  SB();
  asm volatile("v_mfma_f32_16x16x32_bf16 %0, %5, %6, %0\n\tbuffer_load_dwordx4 %1, %7, %8, %9 offen offset:0 sc1\n\tv_mfma_f32_16x16x32_bf16 %2, %10, %6, %2\n\tv_mfma_f32_16x16x32_bf16 %3, %11, %6, %3\n\tv_mfma_f32_16x16x32_bf16 %4, %12, %6, %4"
      : "+v"(acc[0][1]), "=&v"(p[5]), "+v"(acc[0][2]), "+v"(acc[0][3]), "+v"(acc[0][0]) : "a"(fw1[6][1]), "v"(p[6]), "v"(vHc), "s"(rsW), "s"(sh[3]), "a"(fw1[6][2]), "a"(fw1[6][3]), "a"(fw1[6][0]) : "memory"); SB();
  WAITVB(12);
  SB();
  asm volatile("v_mfma_f32_16x16x32_bf16 %0, %5, %6, %0\n\tbuffer_load_dwordx4 %1, %7, %8, %9 offen offset:0 sc1\n\tv_mfma_f32_16x16x32_bf16 %2, %10, %6, %2\n\tv_mfma_f32_16x16x32_bf16 %3, %11, %6, %3\n\tv_mfma_f32_16x16x32_bf16 %4, %12, %6, %4"
      : "+v"(acc[0][1]), "=&v"(p[6]), "+v"(acc[0][2]), "+v"(acc[0][3]), "+v"(acc[0][0]) : "a"(fw1[7][1]), "v"(p[7]), "v"(vHc), "s"(rsW), "s"(sh[4]), "a"(fw1[7][2]), "a"(fw1[7][3]), "a"(fw1[7][0]) : "memory"); SB();
  WAITVB(12);
  SB();
  asm volatile("v_mfma_f32_16x16x32_bf16 %0, %5, %6, %0\n\tbuffer_load_dwordx4 %1, %7, %8, %9 offen offset:0 sc1\n\tv_mfma_f32_16x16x32_bf16 %2, %10, %6, %2\n\tv_mfma_f32_16x16x32_bf16 %3, %11, %6, %3\n\tv_mfma_f32_16x16x32_bf16 %4, %12, %6, %4"
      : "+v"(acc[0][1]), "=&v"(p[7]), "+v"(acc[0][2]), "+v"(acc[0][3]), "+v"(acc[0][0]) : "a"(fw1[8][1]), "v"(p[8]), "v"(vHc), "s"(rsW), "s"(sh[5]), "a"(fw1[8][2]), "a"(fw1[8][3]), "a"(fw1[8][0]) : "memory"); SB();
  WAITVB(12);
  SB();
  asm volatile("v_mfma_f32_16x16x32_bf16 %0, %5, %6, %0\n\tbuffer_load_dwordx4 %1, %7, %8, %9 offen offset:0 sc1\n\tv_mfma_f32_16x16x32_bf16 %2, %10, %6, %2\n\tv_mfma_f32_16x16x32_bf16 %3, %11, %6, %3\n\tv_mfma_f32_16x16x32_bf16 %4, %12, %6, %4"
      : "+v"(acc[0][1]), "=&v"(p[8]), "+v"(acc[0][2]), "+v"(acc[0][3]), "+v"(acc[0][0]) : "a"(fw1[9][1]), "v"(p[9]), "v"(vHc), "s"(rsW), "s"(sh[6]), "a"(fw1[9][2]), "a"(fw1[9][3]), "a"(fw1[9][0]) : "memory"); SB();
  WAITVB(12);
  SB();
  asm volatile("v_mfma_f32_16x16x32_bf16 %0, %5, %6, %0\n\tbuffer_load_dwordx4 %1, %7, %8, %9 offen offset:0 sc1\n\tv_mfma_f32_16x16x32_bf16 %2, %10, %6, %2\n\tv_mfma_f32_16x16x32_bf16 %3, %11, %6, %3\n\tv_mfma_f32_16x16x32_bf16 %4, %12, %6, %4"
      : "+v"(acc[0][1]), "=&v"(p[9]), "+v"(acc[0][2]), "+v"(acc[0][3]), "+v"(acc[0][0]) : "a"(fw1[10][1]), "v"(p[10]), "v"(vHc), "s"(rsW), "s"(sh[7]), "a"(fw1[10][2]), "a"(fw1[10][3]), "a"(fw1[10][0]) : "memory"); SB();
  WAITVB(12);
  SB();
  asm volatile("v_mfma_f32_16x16x32_bf16 %0, %5, %6, %0\n\tbuffer_load_dwordx4 %1, %7, %8, %9 offen offset:0 sc1\n\tv_mfma_f32_16x16x32_bf16 %2, %10, %6, %2\n\tv_mfma_f32_16x16x32_bf16 %3, %11, %6, %3\n\tv_mfma_f32_16x16x32_bf16 %4, %12, %6, %4"
      : "+v"(acc[0][1]), "=&v"(p[10]), "+v"(acc[0][2]), "+v"(acc[0][3]), "+v"(acc[0][0]) : "a"(fw1[11][1]), "v"(p[11]), "v"(vHc), "s"(rsW), "s"(sh[8]), "a"(fw1[11][2]), "a"(fw1[11][3]), "a"(fw1[11][0]) : "memory"); SB();
  WAITVB(12);
  SB();
  asm volatile("v_mfma_f32_16x16x32_bf16 %0, %5, %6, %0\n\tbuffer_load_dwordx4 %1, %7, %8, %9 offen offset:0 sc1\n\tv_mfma_f32_16x16x32_bf16 %2, %10, %6, %2\n\tv_mfma_f32_16x16x32_bf16 %3, %11, %6, %3\n\tv_mfma_f32_16x16x32_bf16 %4, %12, %6, %4"
      : "+v"(acc[0][1]), "=&v"(p[11]), "+v"(acc[0][2]), "+v"(acc[0][3]), "+v"(acc[0][0]) : "a"(fw1[12][1]), "v"(p[12]), "v"(vHc), "s"(rsW), "s"(sh[9]), "a"(fw1[12][2]), "a"(fw1[12][3]), "a"(fw1[12][0]) : "memory"); SB();
  WAITVB(12);
  SB();
  asm volatile("v_mfma_f32_16x16x32_bf16 %0, %5, %6, %0\n\tbuffer_load_dwordx4 %1, %7, %8, %9 offen offset:0 sc1\n\tv_mfma_f32_16x16x32_bf16 %2, %10, %6, %2\n\tv_mfma_f32_16x16x32_bf16 %3, %11, %6, %3\n\tv_mfma_f32_16x16x32_bf16 %4, %12, %6, %4"
      : "+v"(acc[0][1]), "=&v"(p[12]), "+v"(acc[0][2]), "+v"(acc[0][3]), "+v"(acc[0][0]) : "a"(fw1[13][1]), "v"(p[13]), "v"(vHc), "s"(rsW), "s"(sh[10]), "a"(fw1[13][2]), "a"(fw1[13][3]), "a"(fw1[13][0]) : "memory"); SB();
  WAITVB(12);
  SB();
  asm volatile("v_mfma_f32_16x16x32_bf16 %0, %5, %6, %0\n\tbuffer_load_dwordx4 %1, %7, %8, %9 offen offset:0 sc1\n\tv_mfma_f32_16x16x32_bf16 %2, %10, %6, %2\n\tv_mfma_f32_16x16x32_bf16 %3, %11, %6, %3\n\tv_mfma_f32_16x16x32_bf16 %4, %12, %6, %4"
      : "+v"(acc[0][1]), "=&v"(p[13]), "+v"(acc[0][2]), "+v"(acc[0][3]), "+v"(acc[0][0]) : "a"(fw1[14][1]), "v"(p[0]), "v"(vHc), "s"(rsW), "s"(sh[11]), "a"(fw1[14][2]), "a"(fw1[14][3]), "a"(fw1[14][0]) : "memory"); SB();
  WAITVB(12);
  SB();
  asm volatile("v_mfma_f32_16x16x32_bf16 %0, %5, %6, %0\n\tbuffer_load_dwordx4 %1, %7, %8, %9 offen offset:0 sc1\n\tv_mfma_f32_16x16x32_bf16 %2, %10, %6, %2\n\tv_mfma_f32_16x16x32_bf16 %3, %11, %6, %3\n\tv_mfma_f32_16x16x32_bf16 %4, %12, %6, %4"
      : "+v"(acc[0][1]), "=&v"(p[0]), "+v"(acc[0][2]), "+v"(acc[0][3]), "+v"(acc[0][0]) : "v"(fw1[15][1]), "v"(p[1]), "v"(vHc), "s"(rsW), "s"(sh[12]), "v"(fw1[15][2]), "v"(fw1[15][3]), "v"(fw1[15][0]) : "memory"); SB();
  STAMP(7);
  WAITVB(12);
  SB();
  asm volatile("v_mfma_f32_16x16x32_bf16 %0, %5, %6, 0\n\tbuffer_load_dwordx4 %1, %7, %8, %9 offen offset:0 sc1\n\tv_mfma_f32_16x16x32_bf16 %2, %10, %6, 0\n\tv_mfma_f32_16x16x32_bf16 %3, %11, %6, 0\n\tv_mfma_f32_16x16x32_bf16 %4, %12, %6, %13"
      : "=&v"(acc[1][1]), "=&v"(p[1]), "=&v"(acc[1][2]), "=&v"(acc[1][3]), "=&v"(acc[1][0]) : "a"(fw1[0][1]), "v"(p[2]), "v"(vHc), "s"(rsW), "s"(sh[13]), "a"(fw1[0][2]), "a"(fw1[0][3]), "a"(fw1[0][0]), "v"(bb) : "memory"); SB();
  WAITVB(12);
  SB();
  asm volatile("v_mfma_f32_16x16x32_bf16 %0, %5, %6, %0\n\tbuffer_load_dwordx4 %1, %7, %8, %9 offen offset:0 sc1\n\tv_mfma_f32_16x16x32_bf16 %2, %10, %6, %2\n\tds_write_b128 %11, %12 offset:1024\n\tv_mfma_f32_16x16x32_bf16 %3, %13, %6, %3\n\tv_mfma_f32_16x16x32_bf16 %4, %14, %6, %4"
      : "+v"(acc[1][1]), "=&v"(p[2]), "+v"(acc[1][2]), "+v"(acc[1][3]), "+v"(acc[1][0]) : "a"(fw1[1][1]), "v"(p[3]), "v"(vHc), "s"(rsW), "s"(sh[14]), "a"(fw1[1][2]), "v"(rw), "v"(acc[0][1]), "a"(fw1[1][3]), "a"(fw1[1][0]) : "memory"); SB();
  WAITVB(12);
  SB();
  asm volatile("v_mfma_f32_16x16x32_bf16 %0, %5, %6, %0\n\tbuffer_load_dwordx4 %1, %7, %8, %9 offen offset:0 sc1\n\tv_mfma_f32_16x16x32_bf16 %2, %10, %6, %2\n\tds_write_b128 %11, %12 offset:2048\n\tv_mfma_f32_16x16x32_bf16 %3, %13, %6, %3\n\tv_mfma_f32_16x16x32_bf16 %4, %14, %6, %4"
      : "+v"(acc[1][1]), "=&v"(p[3]), "+v"(acc[1][2]), "+v"(acc[1][3]), "+v"(acc[1][0]) : "a"(fw1[2][1]), "v"(p[4]), "v"(vHc), "s"(rsW), "s"(sh[15]), "a"(fw1[2][2]), "v"(rw), "v"(acc[0][2]), "a"(fw1[2][3]), "a"(fw1[2][0]) : "memory"); SB();
  WAITVB(12);
  SB();
  asm volatile("v_mfma_f32_16x16x32_bf16 %0, %5, %6, %0\n\tbuffer_load_dwordx4 %1, %7, %8, %9 offen offset:512 sc1\n\tv_mfma_f32_16x16x32_bf16 %2, %10, %6, %2\n\tds_write_b128 %11, %12 offset:3072\n\tv_mfma_f32_16x16x32_bf16 %3, %13, %6, %3\n\tv_mfma_f32_16x16x32_bf16 %4, %14, %6, %4"
      : "+v"(acc[1][1]), "=&v"(p[4]), "+v"(acc[1][2]), "+v"(acc[1][3]), "+v"(acc[1][0]) : "a"(fw1[3][1]), "v"(p[5]), "v"(vHa), "s"(rsW), "s"(sh[0]), "a"(fw1[3][2]), "v"(rw), "v"(acc[0][3]), "a"(fw1[3][3]), "a"(fw1[3][0]) : "memory"); SB();
  WAITVB(12);
  SB();
  asm volatile("v_mfma_f32_16x16x32_bf16 %0, %3, %4, %0\n\tbuffer_load_dwordx4 %1, %5, %6, %7 offen offset:512 sc1\n\tv_mfma_f32_16x16x32_bf16 %2, %8, %4, %2"
      : "+v"(acc[1][1]), "=&v"(p[5]), "+v"(acc[1][2]) : "a"(fw1[4][1]), "v"(p[6]), "v"(vHa), "s"(rsW), "s"(sh[1]), "a"(fw1[4][2]) : "memory"); SB();
  BAR();
  SB();
  asm volatile("v_mfma_f32_16x16x32_bf16 %0, %5, %6, %0\n\tds_read_b128 %1, %7 offset:1024\n\tds_read_b128 %2, %8 offset:2048\n\tv_mfma_f32_16x16x32_bf16 %3, %9, %6, %3\n\tds_read_b128 %4, %10 offset:3072"
      : "+v"(acc[1][3]), "=&v"(pm[0][0]), "=&v"(pm[0][1]), "+v"(acc[1][0]), "=&v"(pm[0][2]) : "a"(fw1[4][3]), "v"(p[6]), "v"(rr1), "v"(rr2), "a"(fw1[4][0]), "v"(rr3) : "memory"); SB();
  WAITVB(12);
  SB();
  asm volatile("v_mfma_f32_16x16x32_bf16 %0, %5, %6, %0\n\tbuffer_load_dwordx4 %1, %7, %8, %9 offen offset:512 sc1\n\tv_mfma_f32_16x16x32_bf16 %2, %10, %6, %2\n\tv_mfma_f32_16x16x32_bf16 %3, %11, %6, %3\n\tv_mfma_f32_16x16x32_bf16 %4, %12, %6, %4"
      : "+v"(acc[1][1]), "=&v"(p[6]), "+v"(acc[1][2]), "+v"(acc[1][3]), "+v"(acc[1][0]) : "a"(fw1[5][1]), "v"(p[7]), "v"(vHa), "s"(rsW), "s"(sh[2]), "a"(fw1[5][2]), "a"(fw1[5][3]), "a"(fw1[5][0]) : "memory"); SB();
  WAITVB(12);
  SB();
  asm volatile("v_mfma_f32_16x16x32_bf16 %0, %3, %4, %0\n\tbuffer_load_dwordx4 %1, %5, %6, %7 offen offset:512 sc1\n\tv_mfma_f32_16x16x32_bf16 %2, %8, %4, %2"
      : "+v"(acc[1][1]), "=&v"(p[7]), "+v"(acc[1][2]) : "a"(fw1[6][1]), "v"(p[8]), "v"(vHa), "s"(rsW), "s"(sh[3]), "a"(fw1[6][2]) : "memory"); SB();
  WAITL(0);
  asm volatile("v_add_f32 %0, %6, %7\n\tv_mfma_f32_16x16x32_bf16 %1, %8, %9, %1\n\tv_add_f32 %2, %10, %11\n\tv_add_f32 %3, %12, %13\n\tv_mfma_f32_16x16x32_bf16 %4, %14, %9, %4\n\tv_add_f32 %5, %15, %16"
      : "=&v"(gm[0]), "+v"(acc[1][3]), "=&v"(gm[1]), "=&v"(gm[2]), "+v"(acc[1][0]), "=&v"(gm[3]) : "v"(acc[0][0][0]), "v"(pm[0][0][0]), "a"(fw1[6][3]), "v"(p[8]), "v"(acc[0][0][1]), "v"(pm[0][0][1]), "v"(acc[0][0][2]), "v"(pm[0][0][2]), "a"(fw1[6][0]), "v"(acc[0][0][3]), "v"(pm[0][0][3])); SB();
  WAITVB(12);
  SB();
  asm volatile("v_mfma_f32_16x16x32_bf16 %0, %9, %10, %0\n\tbuffer_load_dwordx4 %1, %11, %12, %13 offen offset:512 sc1\n\tv_mfma_f32_16x16x32_bf16 %2, %14, %10, %2\n\tv_add_f32 %3, %15, %16\n\tv_add_f32 %4, %17, %18\n\tv_mfma_f32_16x16x32_bf16 %5, %19, %10, %5\n\tv_add_f32 %6, %20, %21\n\tv_add_f32 %7, %22, %23\n\tv_mfma_f32_16x16x32_bf16 %8, %24, %10, %8"
      : "+v"(acc[1][1]), "=&v"(p[8]), "+v"(acc[1][2]), "=&v"(gz[0]), "=&v"(gz[1]), "+v"(acc[1][3]), "=&v"(gz[2]), "=&v"(gz[3]), "+v"(acc[1][0]) : "a"(fw1[7][1]), "v"(p[9]), "v"(vHa), "s"(rsW), "s"(sh[4]), "a"(fw1[7][2]), "v"(pm[0][1][0]), "v"(pm[0][2][0]), "v"(pm[0][1][1]), "v"(pm[0][2][1]), "a"(fw1[7][3]), "v"(pm[0][1][2]), "v"(pm[0][2][2]), "v"(pm[0][1][3]), "v"(pm[0][2][3]), "a"(fw1[7][0]) : "memory"); SB();
  asm volatile("v_add_f32 %0, %0, %1"
      : "+v"(gm[0]) : "v"(gz[0])); SB();
  WAITVB(12);
  SB();
  asm volatile("v_mfma_f32_16x16x32_bf16 %0, %10, %11, %0\n\tbuffer_load_dwordx4 %1, %12, %13, %14 offen offset:512 sc1\n\tv_mfma_f32_16x16x32_bf16 %2, %15, %11, %2\n\tv_add_f32 %3, %3, %4\n\tv_add_f32 %5, %5, %16\n\tv_mfma_f32_16x16x32_bf16 %6, %17, %11, %6\n\tv_add_f32 %7, %7, %18\n\tv_mul_f32 %8, %19, %19\n\tv_mfma_f32_16x16x32_bf16 %9, %20, %11, %9\n\tv_mul_f32 %4, %3, %3"
      : "+v"(acc[1][1]), "=&v"(p[9]), "+v"(acc[1][2]), "+v"(gm[1]), "+v"(gz[1]), "+v"(gm[2]), "+v"(acc[1][3]), "+v"(gm[3]), "=&v"(gz[0]), "+v"(acc[1][0]) : "a"(fw1[8][1]), "v"(p[10]), "v"(vHa), "s"(rsW), "s"(sh[5]), "a"(fw1[8][2]), "v"(gz[2]), "a"(fw1[8][3]), "v"(gz[3]), "v"(gm[0]), "a"(fw1[8][0]) : "memory"); SB();
  WAITVB(12);
  SB();
  asm volatile("v_mfma_f32_16x16x32_bf16 %0, %9, %10, %0\n\tbuffer_load_dwordx4 %1, %11, %12, %13 offen offset:512 sc1\n\tv_mfma_f32_16x16x32_bf16 %2, %14, %10, %2\n\tv_mul_f32 %3, %15, %15\n\tv_mul_f32 %4, %16, %16\n\tv_mfma_f32_16x16x32_bf16 %5, %17, %10, %5\n\tv_fmaak_f32 %6, %18, %6, 0x40135761\n\tv_fmaak_f32 %7, %18, %7, 0x40135761\n\tv_mfma_f32_16x16x32_bf16 %8, %19, %10, %8\n\tv_fmaak_f32 %3, %18, %3, 0x40135761"
      : "+v"(acc[1][1]), "=&v"(p[10]), "+v"(acc[1][2]), "=&v"(gz[2]), "=&v"(gz[3]), "+v"(acc[1][3]), "+v"(gz[0]), "+v"(gz[1]), "+v"(acc[1][0]) : "a"(fw1[9][1]), "v"(p[11]), "v"(vHa), "s"(rsW), "s"(sh[6]), "a"(fw1[9][2]), "v"(gm[2]), "v"(gm[3]), "a"(fw1[9][3]), "v"(gc), "a"(fw1[9][0]) : "memory"); SB();
  WAITVB(12);
  SB();
  asm volatile("v_mfma_f32_16x16x32_bf16 %0, %9, %10, %0\n\tbuffer_load_dwordx4 %1, %11, %12, %13 offen offset:512 sc1\n\tv_mfma_f32_16x16x32_bf16 %2, %14, %10, %2\n\tv_fmaak_f32 %3, %15, %3, 0x40135761\n\tv_mul_f32 %4, %4, %16\n\tv_mfma_f32_16x16x32_bf16 %5, %17, %10, %5\n\tv_mul_f32 %6, %6, %18\n\tv_mul_f32 %7, %7, %19\n\tv_mfma_f32_16x16x32_bf16 %8, %20, %10, %8\n\tv_mul_f32 %3, %3, %21"
      : "+v"(acc[1][1]), "=&v"(p[11]), "+v"(acc[1][2]), "+v"(gz[3]), "+v"(gz[0]), "+v"(acc[1][3]), "+v"(gz[1]), "+v"(gz[2]), "+v"(acc[1][0]) : "a"(fw1[10][1]), "v"(p[12]), "v"(vHa), "s"(rsW), "s"(sh[7]), "a"(fw1[10][2]), "v"(gc), "v"(gm[0]), "a"(fw1[10][3]), "v"(gm[1]), "v"(gm[2]), "a"(fw1[10][0]), "v"(gm[3]) : "memory"); SB();
  WAITVB(12);
  SB();
  asm volatile("v_mfma_f32_16x16x32_bf16 %0, %7, %8, %0\n\tbuffer_load_dwordx4 %1, %9, %10, %11 offen offset:512 sc1\n\tv_mfma_f32_16x16x32_bf16 %2, %12, %8, %2\n\tv_exp_f32 %3, %3\n\tv_mfma_f32_16x16x32_bf16 %4, %13, %8, %4\n\tv_exp_f32 %5, %5\n\tv_mfma_f32_16x16x32_bf16 %6, %14, %8, %6"
      : "+v"(acc[1][1]), "=&v"(p[12]), "+v"(acc[1][2]), "+v"(gz[0]), "+v"(acc[1][3]), "+v"(gz[1]), "+v"(acc[1][0]) : "a"(fw1[11][1]), "v"(p[13]), "v"(vHa), "s"(rsW), "s"(sh[8]), "a"(fw1[11][2]), "a"(fw1[11][3]), "a"(fw1[11][0]) : "memory"); SB();
  WAITVB(12);
  SB();
  asm volatile("v_mfma_f32_16x16x32_bf16 %0, %8, %9, %0\n\tbuffer_load_dwordx4 %1, %10, %11, %12 offen offset:512 sc1\n\tv_mfma_f32_16x16x32_bf16 %2, %13, %9, %2\n\tv_exp_f32 %3, %3\n\tv_mfma_f32_16x16x32_bf16 %4, %14, %9, %4\n\tv_exp_f32 %5, %5\n\tv_mfma_f32_16x16x32_bf16 %6, %15, %9, %6\n\tv_add_f32 %7, 1.0, %7"
      : "+v"(acc[1][1]), "=&v"(p[13]), "+v"(acc[1][2]), "+v"(gz[2]), "+v"(acc[1][3]), "+v"(gz[3]), "+v"(acc[1][0]), "+v"(gz[0]) : "a"(fw1[12][1]), "v"(p[0]), "v"(vHa), "s"(rsW), "s"(sh[9]), "a"(fw1[12][2]), "a"(fw1[12][3]), "a"(fw1[12][0]) : "memory"); SB();
  WAITVB(12);
  SB();
  asm volatile("v_mfma_f32_16x16x32_bf16 %0, %8, %9, %0\n\tbuffer_load_dwordx4 %1, %10, %11, %12 offen offset:512 sc1\n\tv_mfma_f32_16x16x32_bf16 %2, %13, %9, %2\n\tv_add_f32 %3, 1.0, %3\n\tv_add_f32 %4, 1.0, %4\n\tv_mfma_f32_16x16x32_bf16 %5, %14, %9, %5\n\tv_add_f32 %6, 1.0, %6\n\tv_mfma_f32_16x16x32_bf16 %7, %15, %9, %7"
      : "+v"(acc[1][1]), "=&v"(p[0]), "+v"(acc[1][2]), "+v"(gz[1]), "+v"(gz[2]), "+v"(acc[1][3]), "+v"(gz[3]), "+v"(acc[1][0]) : "a"(fw1[13][1]), "v"(p[1]), "v"(vHa), "s"(rsW), "s"(sh[10]), "a"(fw1[13][2]), "a"(fw1[13][3]), "a"(fw1[13][0]) : "memory"); SB();
  WAITVB(12);
  SB();
  asm volatile("v_mfma_f32_16x16x32_bf16 %0, %7, %8, %0\n\tbuffer_load_dwordx4 %1, %9, %10, %11 offen offset:512 sc1\n\tv_mfma_f32_16x16x32_bf16 %2, %12, %8, %2\n\tv_rcp_f32 %3, %3\n\tv_mfma_f32_16x16x32_bf16 %4, %13, %8, %4\n\tv_rcp_f32 %5, %5\n\tv_mfma_f32_16x16x32_bf16 %6, %14, %8, %6"
      : "+v"(acc[1][1]), "=&v"(p[1]), "+v"(acc[1][2]), "+v"(gz[0]), "+v"(acc[1][3]), "+v"(gz[1]), "+v"(acc[1][0]) : "a"(fw1[14][1]), "v"(p[2]), "v"(vHa), "s"(rsW), "s"(sh[11]), "a"(fw1[14][2]), "a"(fw1[14][3]), "a"(fw1[14][0]) : "memory"); SB();
  WAITVB(12);
  SB();
  asm volatile("v_mfma_f32_16x16x32_bf16 %0, %9, %10, %0\n\tbuffer_load_dwordx4 %1, %11, %12, %13 offen offset:512 sc1\n\tv_mfma_f32_16x16x32_bf16 %2, %14, %10, %2\n\tv_rcp_f32 %3, %3\n\tv_mfma_f32_16x16x32_bf16 %4, %15, %10, %4\n\tv_rcp_f32 %5, %5\n\tv_mfma_f32_16x16x32_bf16 %6, %16, %10, %6\n\tv_fma_f32 %7, -%17, %7, %17\n\tv_fma_f32 %8, -%18, %8, %18"
      : "+v"(acc[1][1]), "=&v"(p[2]), "+v"(acc[1][2]), "+v"(gz[2]), "+v"(acc[1][3]), "+v"(gz[3]), "+v"(acc[1][0]), "+v"(gz[0]), "+v"(gz[1]) : "v"(fw1[15][1]), "v"(p[3]), "v"(vHa), "s"(rsW), "s"(sh[12]), "v"(fw1[15][2]), "v"(fw1[15][3]), "v"(fw1[15][0]), "v"(gm[0]), "v"(gm[1]) : "memory"); SB();
  WAITVB(12);
  SB();
  asm volatile("v_mfma_f32_16x16x32_bf16 %0, %9, %10, 0\n\tbuffer_load_dwordx4 %1, %11, %12, %13 offen offset:512 sc1\n\tv_mfma_f32_16x16x32_bf16 %2, %14, %10, 0\n\tv_fma_f32 %3, -%15, %3, %15\n\tv_fma_f32 %4, -%16, %4, %16\n\tv_mfma_f32_16x16x32_bf16 %5, %17, %10, 0\n\tv_cvt_pk_bf16_f32 %6, %18, %19\n\tv_cvt_pk_bf16_f32 %7, %3, %4\n\tv_mfma_f32_16x16x32_bf16 %8, %20, %10, %21"
      : "=&v"(acc[2][1]), "=&v"(p[3]), "=&v"(acc[2][2]), "+v"(gz[2]), "+v"(gz[3]), "=&v"(acc[2][3]), "=&v"(uv[0][0]), "=&v"(uv[0][1]), "=&v"(acc[2][0]) : "a"(fw1[0][1]), "v"(p[4]), "v"(vHa), "s"(rsW), "s"(sh[13]), "a"(fw1[0][2]), "v"(gm[2]), "v"(gm[3]), "a"(fw1[0][3]), "v"(gz[0]), "v"(gz[1]), "a"(fw1[0][0]), "v"(bb) : "memory"); SB();
  WAITVB(12);
  SB();
  asm volatile("v_mfma_f32_16x16x32_bf16 %0, %5, %6, %0\n\tbuffer_load_dwordx4 %1, %7, %8, %9 offen offset:512 sc1\n\tv_mfma_f32_16x16x32_bf16 %2, %10, %6, %2\n\tds_write_b128 %11, %12 offset:5120\n\tv_mfma_f32_16x16x32_bf16 %3, %13, %6, %3\n\tv_mfma_f32_16x16x32_bf16 %4, %14, %6, %4"
      : "+v"(acc[2][1]), "=&v"(p[4]), "+v"(acc[2][2]), "+v"(acc[2][3]), "+v"(acc[2][0]) : "a"(fw1[1][1]), "v"(p[5]), "v"(vHa), "s"(rsW), "s"(sh[14]), "a"(fw1[1][2]), "v"(rw), "v"(acc[1][1]), "a"(fw1[1][3]), "a"(fw1[1][0]) : "memory"); SB();
  WAITVB(12);
  SB();
  asm volatile("v_mfma_f32_16x16x32_bf16 %0, %5, %6, %0\n\tbuffer_load_dwordx4 %1, %7, %8, %9 offen offset:512 sc1\n\tv_mfma_f32_16x16x32_bf16 %2, %10, %6, %2\n\tds_write_b128 %11, %12 offset:6144\n\tv_mfma_f32_16x16x32_bf16 %3, %13, %6, %3\n\tv_mfma_f32_16x16x32_bf16 %4, %14, %6, %4"
      : "+v"(acc[2][1]), "=&v"(p[5]), "+v"(acc[2][2]), "+v"(acc[2][3]), "+v"(acc[2][0]) : "a"(fw1[2][1]), "v"(p[6]), "v"(vHa), "s"(rsW), "s"(sh[15]), "a"(fw1[2][2]), "v"(rw), "v"(acc[1][2]), "a"(fw1[2][3]), "a"(fw1[2][0]) : "memory"); SB();
  WAITVB(12);
  SB();
  asm volatile("v_mfma_f32_16x16x32_bf16 %0, %5, %6, %0\n\tbuffer_load_dwordx4 %1, %7, %8, %9 offen offset:512 sc1\n\tv_mfma_f32_16x16x32_bf16 %2, %10, %6, %2\n\tds_write_b128 %11, %12 offset:7168\n\tv_mfma_f32_16x16x32_bf16 %3, %13, %6, %3\n\tv_mfma_f32_16x16x32_bf16 %4, %14, %6, %4"
      : "+v"(acc[2][1]), "=&v"(p[6]), "+v"(acc[2][2]), "+v"(acc[2][3]), "+v"(acc[2][0]) : "a"(fw1[3][1]), "v"(p[7]), "v"(vHc), "s"(rsW), "s"(sh[0]), "a"(fw1[3][2]), "v"(rw), "v"(acc[1][3]), "a"(fw1[3][3]), "a"(fw1[3][0]) : "memory"); SB();
  WAITVB(12);
  SB();
  asm volatile("v_mfma_f32_16x16x32_bf16 %0, %3, %4, %0\n\tbuffer_load_dwordx4 %1, %5, %6, %7 offen offset:512 sc1\n\tv_mfma_f32_16x16x32_bf16 %2, %8, %4, %2"
      : "+v"(acc[2][1]), "=&v"(p[7]), "+v"(acc[2][2]) : "a"(fw1[4][1]), "v"(p[8]), "v"(vHc), "s"(rsW), "s"(sh[1]), "a"(fw1[4][2]) : "memory"); SB();
  BAR();
  SB();
  asm volatile("v_mfma_f32_16x16x32_bf16 %0, %5, %6, %0\n\tds_read_b128 %1, %7 offset:5120\n\tds_read_b128 %2, %8 offset:6144\n\tv_mfma_f32_16x16x32_bf16 %3, %9, %6, %3\n\tds_read_b128 %4, %10 offset:7168"
      : "+v"(acc[2][3]), "=&v"(pm[1][0]), "=&v"(pm[1][1]), "+v"(acc[2][0]), "=&v"(pm[1][2]) : "a"(fw1[4][3]), "v"(p[8]), "v"(rr1), "v"(rr2), "a"(fw1[4][0]), "v"(rr3) : "memory"); SB();
  WAITVB(12);
  SB();
  asm volatile("v_mfma_f32_16x16x32_bf16 %0, %5, %6, %0\n\tbuffer_load_dwordx4 %1, %7, %8, %9 offen offset:512 sc1\n\tv_mfma_f32_16x16x32_bf16 %2, %10, %6, %2\n\tv_mfma_f32_16x16x32_bf16 %3, %11, %6, %3\n\tv_mfma_f32_16x16x32_bf16 %4, %12, %6, %4"
      : "+v"(acc[2][1]), "=&v"(p[8]), "+v"(acc[2][2]), "+v"(acc[2][3]), "+v"(acc[2][0]) : "a"(fw1[5][1]), "v"(p[9]), "v"(vHc), "s"(rsW), "s"(sh[2]), "a"(fw1[5][2]), "a"(fw1[5][3]), "a"(fw1[5][0]) : "memory"); SB();
  WAITVB(12);
  SB();
  asm volatile("v_mfma_f32_16x16x32_bf16 %0, %3, %4, %0\n\tbuffer_load_dwordx4 %1, %5, %6, %7 offen offset:512 sc1\n\tv_mfma_f32_16x16x32_bf16 %2, %8, %4, %2"
      : "+v"(acc[2][1]), "=&v"(p[9]), "+v"(acc[2][2]) : "a"(fw1[6][1]), "v"(p[10]), "v"(vHc), "s"(rsW), "s"(sh[3]), "a"(fw1[6][2]) : "memory"); SB();
  WAITL(0);
  asm volatile("v_add_f32 %0, %6, %7\n\tv_mfma_f32_16x16x32_bf16 %1, %8, %9, %1\n\tv_add_f32 %2, %10, %11\n\tv_add_f32 %3, %12, %13\n\tv_mfma_f32_16x16x32_bf16 %4, %14, %9, %4\n\tv_add_f32 %5, %15, %16"
      : "=&v"(gm[0]), "+v"(acc[2][3]), "=&v"(gm[1]), "=&v"(gm[2]), "+v"(acc[2][0]), "=&v"(gm[3]) : "v"(acc[1][0][0]), "v"(pm[1][0][0]), "a"(fw1[6][3]), "v"(p[10]), "v"(acc[1][0][1]), "v"(pm[1][0][1]), "v"(acc[1][0][2]), "v"(pm[1][0][2]), "a"(fw1[6][0]), "v"(acc[1][0][3]), "v"(pm[1][0][3])); SB();
  WAITVB(12);
  SB();
  asm volatile("v_mfma_f32_16x16x32_bf16 %0, %9, %10, %0\n\tbuffer_load_dwordx4 %1, %11, %12, %13 offen offset:512 sc1\n\tv_mfma_f32_16x16x32_bf16 %2, %14, %10, %2\n\tv_add_f32 %3, %15, %16\n\tv_add_f32 %4, %17, %18\n\tv_mfma_f32_16x16x32_bf16 %5, %19, %10, %5\n\tv_add_f32 %6, %20, %21\n\tv_add_f32 %7, %22, %23\n\tv_mfma_f32_16x16x32_bf16 %8, %24, %10, %8"
      : "+v"(acc[2][1]), "=&v"(p[10]), "+v"(acc[2][2]), "=&v"(gz[0]), "=&v"(gz[1]), "+v"(acc[2][3]), "=&v"(gz[2]), "=&v"(gz[3]), "+v"(acc[2][0]) : "a"(fw1[7][1]), "v"(p[11]), "v"(vHc), "s"(rsW), "s"(sh[4]), "a"(fw1[7][2]), "v"(pm[1][1][0]), "v"(pm[1][2][0]), "v"(pm[1][1][1]), "v"(pm[1][2][1]), "a"(fw1[7][3]), "v"(pm[1][1][2]), "v"(pm[1][2][2]), "v"(pm[1][1][3]), "v"(pm[1][2][3]), "a"(fw1[7][0]) : "memory"); SB();
  asm volatile("v_add_f32 %0, %0, %1"
      : "+v"(gm[0]) : "v"(gz[0])); SB();
  WAITVB(12);
  SB();
  asm volatile("v_mfma_f32_16x16x32_bf16 %0, %10, %11, %0\n\tbuffer_load_dwordx4 %1, %12, %13, %14 offen offset:512 sc1\n\tv_mfma_f32_16x16x32_bf16 %2, %15, %11, %2\n\tv_add_f32 %3, %3, %4\n\tv_add_f32 %5, %5, %16\n\tv_mfma_f32_16x16x32_bf16 %6, %17, %11, %6\n\tv_add_f32 %7, %7, %18\n\tv_mul_f32 %8, %19, %19\n\tv_mfma_f32_16x16x32_bf16 %9, %20, %11, %9\n\tv_mul_f32 %4, %3, %3"
      : "+v"(acc[2][1]), "=&v"(p[11]), "+v"(acc[2][2]), "+v"(gm[1]), "+v"(gz[1]), "+v"(gm[2]), "+v"(acc[2][3]), "+v"(gm[3]), "=&v"(gz[0]), "+v"(acc[2][0]) : "a"(fw1[8][1]), "v"(p[12]), "v"(vHc), "s"(rsW), "s"(sh[5]), "a"(fw1[8][2]), "v"(gz[2]), "a"(fw1[8][3]), "v"(gz[3]), "v"(gm[0]), "a"(fw1[8][0]) : "memory"); SB();
  WAITVB(12);
  SB();
  asm volatile("v_mfma_f32_16x16x32_bf16 %0, %9, %10, %0\n\tbuffer_load_dwordx4 %1, %11, %12, %13 offen offset:512 sc1\n\tv_mfma_f32_16x16x32_bf16 %2, %14, %10, %2\n\tv_mul_f32 %3, %15, %15\n\tv_mul_f32 %4, %16, %16\n\tv_mfma_f32_16x16x32_bf16 %5, %17, %10, %5\n\tv_fmaak_f32 %6, %18, %6, 0x40135761\n\tv_fmaak_f32 %7, %18, %7, 0x40135761\n\tv_mfma_f32_16x16x32_bf16 %8, %19, %10, %8\n\tv_fmaak_f32 %3, %18, %3, 0x40135761"
      : "+v"(acc[2][1]), "=&v"(p[12]), "+v"(acc[2][2]), "=&v"(gz[2]), "=&v"(gz[3]), "+v"(acc[2][3]), "+v"(gz[0]), "+v"(gz[1]), "+v"(acc[2][0]) : "a"(fw1[9][1]), "v"(p[13]), "v"(vHc), "s"(rsW), "s"(sh[6]), "a"(fw1[9][2]), "v"(gm[2]), "v"(gm[3]), "a"(fw1[9][3]), "v"(gc), "a"(fw1[9][0]) : "memory"); SB();
  WAITVB(12);
  SB();
  asm volatile("v_mfma_f32_16x16x32_bf16 %0, %9, %10, %0\n\tbuffer_load_dwordx4 %1, %11, %12, %13 offen offset:512 sc1\n\tv_mfma_f32_16x16x32_bf16 %2, %14, %10, %2\n\tv_fmaak_f32 %3, %15, %3, 0x40135761\n\tv_mul_f32 %4, %4, %16\n\tv_mfma_f32_16x16x32_bf16 %5, %17, %10, %5\n\tv_mul_f32 %6, %6, %18\n\tv_mul_f32 %7, %7, %19\n\tv_mfma_f32_16x16x32_bf16 %8, %20, %10, %8\n\tv_mul_f32 %3, %3, %21"
      : "+v"(acc[2][1]), "=&v"(p[13]), "+v"(acc[2][2]), "+v"(gz[3]), "+v"(gz[0]), "+v"(acc[2][3]), "+v"(gz[1]), "+v"(gz[2]), "+v"(acc[2][0]) : "a"(fw1[10][1]), "v"(p[0]), "v"(vHc), "s"(rsW), "s"(sh[7]), "a"(fw1[10][2]), "v"(gc), "v"(gm[0]), "a"(fw1[10][3]), "v"(gm[1]), "v"(gm[2]), "a"(fw1[10][0]), "v"(gm[3]) : "memory"); SB();
  WAITVB(12);
  SB();
  asm volatile("v_mfma_f32_16x16x32_bf16 %0, %7, %8, %0\n\tbuffer_load_dwordx4 %1, %9, %10, %11 offen offset:512 sc1\n\tv_mfma_f32_16x16x32_bf16 %2, %12, %8, %2\n\tv_exp_f32 %3, %3\n\tv_mfma_f32_16x16x32_bf16 %4, %13, %8, %4\n\tv_exp_f32 %5, %5\n\tv_mfma_f32_16x16x32_bf16 %6, %14, %8, %6"
      : "+v"(acc[2][1]), "=&v"(p[0]), "+v"(acc[2][2]), "+v"(gz[0]), "+v"(acc[2][3]), "+v"(gz[1]), "+v"(acc[2][0]) : "a"(fw1[11][1]), "v"(p[1]), "v"(vHc), "s"(rsW), "s"(sh[8]), "a"(fw1[11][2]), "a"(fw1[11][3]), "a"(fw1[11][0]) : "memory"); SB();
  WAITVB(12);
  SB();
  asm volatile("v_mfma_f32_16x16x32_bf16 %0, %8, %9, %0\n\tbuffer_load_dwordx4 %1, %10, %11, %12 offen offset:512 sc1\n\tv_mfma_f32_16x16x32_bf16 %2, %13, %9, %2\n\tv_exp_f32 %3, %3\n\tv_mfma_f32_16x16x32_bf16 %4, %14, %9, %4\n\tv_exp_f32 %5, %5\n\tv_mfma_f32_16x16x32_bf16 %6, %15, %9, %6\n\tv_add_f32 %7, 1.0, %7"
      : "+v"(acc[2][1]), "=&v"(p[1]), "+v"(acc[2][2]), "+v"(gz[2]), "+v"(acc[2][3]), "+v"(gz[3]), "+v"(acc[2][0]), "+v"(gz[0]) : "a"(fw1[12][1]), "v"(p[2]), "v"(vHc), "s"(rsW), "s"(sh[9]), "a"(fw1[12][2]), "a"(fw1[12][3]), "a"(fw1[12][0]) : "memory"); SB();
  WAITVB(12);
  SB();
  asm volatile("v_mfma_f32_16x16x32_bf16 %0, %8, %9, %0\n\tbuffer_load_dwordx4 %1, %10, %11, %12 offen offset:512 sc1\n\tv_mfma_f32_16x16x32_bf16 %2, %13, %9, %2\n\tv_add_f32 %3, 1.0, %3\n\tv_add_f32 %4, 1.0, %4\n\tv_mfma_f32_16x16x32_bf16 %5, %14, %9, %5\n\tv_add_f32 %6, 1.0, %6\n\tv_mfma_f32_16x16x32_bf16 %7, %15, %9, %7"
      : "+v"(acc[2][1]), "=&v"(p[2]), "+v"(acc[2][2]), "+v"(gz[1]), "+v"(gz[2]), "+v"(acc[2][3]), "+v"(gz[3]), "+v"(acc[2][0]) : "a"(fw1[13][1]), "v"(p[3]), "v"(vHc), "s"(rsW), "s"(sh[10]), "a"(fw1[13][2]), "a"(fw1[13][3]), "a"(fw1[13][0]) : "memory"); SB();
  WAITVB(12);
  SB();
  asm volatile("v_mfma_f32_16x16x32_bf16 %0, %7, %8, %0\n\tbuffer_load_dwordx4 %1, %9, %10, %11 offen offset:512 sc1\n\tv_mfma_f32_16x16x32_bf16 %2, %12, %8, %2\n\tv_rcp_f32 %3, %3\n\tv_mfma_f32_16x16x32_bf16 %4, %13, %8, %4\n\tv_rcp_f32 %5, %5\n\tv_mfma_f32_16x16x32_bf16 %6, %14, %8, %6"
      : "+v"(acc[2][1]), "=&v"(p[3]), "+v"(acc[2][2]), "+v"(gz[0]), "+v"(acc[2][3]), "+v"(gz[1]), "+v"(acc[2][0]) : "a"(fw1[14][1]), "v"(p[4]), "v"(vHc), "s"(rsW), "s"(sh[11]), "a"(fw1[14][2]), "a"(fw1[14][3]), "a"(fw1[14][0]) : "memory"); SB();
  WAITVB(12);
  SB();
  asm volatile("v_mfma_f32_16x16x32_bf16 %0, %9, %10, %0\n\tbuffer_load_dwordx4 %1, %11, %12, %13 offen offset:512 sc1\n\tv_mfma_f32_16x16x32_bf16 %2, %14, %10, %2\n\tv_rcp_f32 %3, %3\n\tv_mfma_f32_16x16x32_bf16 %4, %15, %10, %4\n\tv_rcp_f32 %5, %5\n\tv_mfma_f32_16x16x32_bf16 %6, %16, %10, %6\n\tv_fma_f32 %7, -%17, %7, %17\n\tv_fma_f32 %8, -%18, %8, %18"
      : "+v"(acc[2][1]), "=&v"(p[4]), "+v"(acc[2][2]), "+v"(gz[2]), "+v"(acc[2][3]), "+v"(gz[3]), "+v"(acc[2][0]), "+v"(gz[0]), "+v"(gz[1]) : "v"(fw1[15][1]), "v"(p[5]), "v"(vHc), "s"(rsW), "s"(sh[12]), "v"(fw1[15][2]), "v"(fw1[15][3]), "v"(fw1[15][0]), "v"(gm[0]), "v"(gm[1]) : "memory"); SB();
  WAITVB(12);
  SB();
  asm volatile("v_mfma_f32_16x16x32_bf16 %0, %9, %10, 0\n\tbuffer_load_dwordx4 %1, %11, %12, %13 offen offset:512 sc1\n\tv_mfma_f32_16x16x32_bf16 %2, %14, %10, 0\n\tv_fma_f32 %3, -%15, %3, %15\n\tv_fma_f32 %4, -%16, %4, %16\n\tv_mfma_f32_16x16x32_bf16 %5, %17, %10, 0\n\tv_cvt_pk_bf16_f32 %6, %18, %19\n\tv_cvt_pk_bf16_f32 %7, %3, %4\n\tv_mfma_f32_16x16x32_bf16 %8, %20, %10, %21"
      : "=&v"(acc[3][1]), "=&v"(p[5]), "=&v"(acc[3][2]), "+v"(gz[2]), "+v"(gz[3]), "=&v"(acc[3][3]), "=&v"(uv[1][0]), "=&v"(uv[1][1]), "=&v"(acc[3][0]) : "a"(fw1[0][1]), "v"(p[6]), "v"(vHc), "s"(rsW), "s"(sh[13]), "a"(fw1[0][2]), "v"(gm[2]), "v"(gm[3]), "a"(fw1[0][3]), "v"(gz[0]), "v"(gz[1]), "a"(fw1[0][0]), "v"(bb) : "memory"); SB();
  WAITVB(12);
  SB();
  asm volatile("v_mfma_f32_16x16x32_bf16 %0, %6, %7, %0\n\tbuffer_load_dwordx4 %1, %8, %9, %10 offen offset:512 sc1\n\tv_mfma_f32_16x16x32_bf16 %2, %11, %7, %2\n\tds_write_b128 %12, %13 offset:1024\n\tv_mfma_f32_16x16x32_bf16 %3, %14, %7, %3\n\tbuffer_load_dword %4, %15, %9, 0 offen sc1\n\tv_mfma_f32_16x16x32_bf16 %5, %16, %7, %5"
      : "+v"(acc[3][1]), "=&v"(p[6]), "+v"(acc[3][2]), "+v"(acc[3][3]), "=&v"(fq), "+v"(acc[3][0]) : "a"(fw1[1][1]), "v"(p[7]), "v"(vHc), "s"(rsW), "s"(sh[14]), "a"(fw1[1][2]), "v"(rw), "v"(acc[2][1]), "a"(fw1[1][3]), "v"(vFQ), "a"(fw1[1][0]) : "memory"); SB();
  WAITVB(13);
  SB();
  asm volatile("v_mfma_f32_16x16x32_bf16 %0, %5, %6, %0\n\tbuffer_load_dwordx4 %1, %7, %8, %9 offen offset:512 sc1\n\tv_mfma_f32_16x16x32_bf16 %2, %10, %6, %2\n\tds_write_b128 %11, %12 offset:2048\n\tv_mfma_f32_16x16x32_bf16 %3, %13, %6, %3\n\tv_mfma_f32_16x16x32_bf16 %4, %14, %6, %4"
      : "+v"(acc[3][1]), "=&v"(p[7]), "+v"(acc[3][2]), "+v"(acc[3][3]), "+v"(acc[3][0]) : "a"(fw1[2][1]), "v"(p[8]), "v"(vHc), "s"(rsW), "s"(sh[15]), "a"(fw1[2][2]), "v"(rw), "v"(acc[2][2]), "a"(fw1[2][3]), "a"(fw1[2][0]) : "memory"); SB();
  WAITVB(13);
  SB();
  asm volatile("v_mfma_f32_16x16x32_bf16 %0, %5, %6, %0\n\tbuffer_load_dwordx4 %1, %7, %8, %9 offen offset:256 sc1\n\tv_mfma_f32_16x16x32_bf16 %2, %10, %6, %2\n\tds_write_b128 %11, %12 offset:3072\n\tv_mfma_f32_16x16x32_bf16 %3, %13, %6, %3\n\tv_mfma_f32_16x16x32_bf16 %4, %14, %6, %4"
      : "+v"(acc[3][1]), "=&v"(p[8]), "+v"(acc[3][2]), "+v"(acc[3][3]), "+v"(acc[3][0]) : "a"(fw1[3][1]), "v"(p[9]), "v"(vHa), "s"(rsW), "s"(sh[0]), "a"(fw1[3][2]), "v"(rw), "v"(acc[2][3]), "a"(fw1[3][3]), "a"(fw1[3][0]) : "memory"); SB();
  WAITVB(13);
  SB();
  asm volatile("v_mfma_f32_16x16x32_bf16 %0, %3, %4, %0\n\tbuffer_load_dwordx4 %1, %5, %6, %7 offen offset:256 sc1\n\tv_mfma_f32_16x16x32_bf16 %2, %8, %4, %2"
      : "+v"(acc[3][1]), "=&v"(p[9]), "+v"(acc[3][2]) : "a"(fw1[4][1]), "v"(p[10]), "v"(vHa), "s"(rsW), "s"(sh[1]), "a"(fw1[4][2]) : "memory"); SB();
  BAR();
  SB();
  asm volatile("v_mfma_f32_16x16x32_bf16 %0, %5, %6, %0\n\tds_read_b128 %1, %7 offset:1024\n\tds_read_b128 %2, %8 offset:2048\n\tv_mfma_f32_16x16x32_bf16 %3, %9, %6, %3\n\tds_read_b128 %4, %10 offset:3072"
      : "+v"(acc[3][3]), "=&v"(pm[2][0]), "=&v"(pm[2][1]), "+v"(acc[3][0]), "=&v"(pm[2][2]) : "a"(fw1[4][3]), "v"(p[10]), "v"(rr1), "v"(rr2), "a"(fw1[4][0]), "v"(rr3) : "memory"); SB();
  WAITVB(13);
  SB();
  asm volatile("v_mfma_f32_16x16x32_bf16 %0, %5, %6, %0\n\tbuffer_load_dwordx4 %1, %7, %8, %9 offen offset:256 sc1\n\tv_mfma_f32_16x16x32_bf16 %2, %10, %6, %2\n\tv_mfma_f32_16x16x32_bf16 %3, %11, %6, %3\n\tv_mfma_f32_16x16x32_bf16 %4, %12, %6, %4"
      : "+v"(acc[3][1]), "=&v"(p[10]), "+v"(acc[3][2]), "+v"(acc[3][3]), "+v"(acc[3][0]) : "a"(fw1[5][1]), "v"(p[11]), "v"(vHa), "s"(rsW), "s"(sh[2]), "a"(fw1[5][2]), "a"(fw1[5][3]), "a"(fw1[5][0]) : "memory"); SB();
  WAITVB(13);
  SB();
  asm volatile("v_mfma_f32_16x16x32_bf16 %0, %3, %4, %0\n\tbuffer_load_dwordx4 %1, %5, %6, %7 offen offset:256 sc1\n\tv_mfma_f32_16x16x32_bf16 %2, %8, %4, %2"
      : "+v"(acc[3][1]), "=&v"(p[11]), "+v"(acc[3][2]) : "a"(fw1[6][1]), "v"(p[12]), "v"(vHa), "s"(rsW), "s"(sh[3]), "a"(fw1[6][2]) : "memory"); SB();
  WAITL(0);
  asm volatile("v_add_f32 %0, %6, %7\n\tv_mfma_f32_16x16x32_bf16 %1, %8, %9, %1\n\tv_add_f32 %2, %10, %11\n\tv_add_f32 %3, %12, %13\n\tv_mfma_f32_16x16x32_bf16 %4, %14, %9, %4\n\tv_add_f32 %5, %15, %16"
      : "=&v"(gm[0]), "+v"(acc[3][3]), "=&v"(gm[1]), "=&v"(gm[2]), "+v"(acc[3][0]), "=&v"(gm[3]) : "v"(acc[2][0][0]), "v"(pm[2][0][0]), "a"(fw1[6][3]), "v"(p[12]), "v"(acc[2][0][1]), "v"(pm[2][0][1]), "v"(acc[2][0][2]), "v"(pm[2][0][2]), "a"(fw1[6][0]), "v"(acc[2][0][3]), "v"(pm[2][0][3])); SB();
  WAITVB(13);
  SB();
  asm volatile("v_mfma_f32_16x16x32_bf16 %0, %9, %10, %0\n\tbuffer_load_dwordx4 %1, %11, %12, %13 offen offset:256 sc1\n\tv_mfma_f32_16x16x32_bf16 %2, %14, %10, %2\n\tv_add_f32 %3, %15, %16\n\tv_add_f32 %4, %17, %18\n\tv_mfma_f32_16x16x32_bf16 %5, %19, %10, %5\n\tv_add_f32 %6, %20, %21\n\tv_add_f32 %7, %22, %23\n\tv_mfma_f32_16x16x32_bf16 %8, %24, %10, %8"
      : "+v"(acc[3][1]), "=&v"(p[12]), "+v"(acc[3][2]), "=&v"(gz[0]), "=&v"(gz[1]), "+v"(acc[3][3]), "=&v"(gz[2]), "=&v"(gz[3]), "+v"(acc[3][0]) : "a"(fw1[7][1]), "v"(p[13]), "v"(vHa), "s"(rsW), "s"(sh[4]), "a"(fw1[7][2]), "v"(pm[2][1][0]), "v"(pm[2][2][0]), "v"(pm[2][1][1]), "v"(pm[2][2][1]), "a"(fw1[7][3]), "v"(pm[2][1][2]), "v"(pm[2][2][2]), "v"(pm[2][1][3]), "v"(pm[2][2][3]), "a"(fw1[7][0]) : "memory"); SB();
  asm volatile("v_add_f32 %0, %0, %1"
      : "+v"(gm[0]) : "v"(gz[0])); SB();
  WAITVB(13);
  SB();
  asm volatile("v_mfma_f32_16x16x32_bf16 %0, %10, %11, %0\n\tbuffer_load_dwordx4 %1, %12, %13, %14 offen offset:256 sc1\n\tv_mfma_f32_16x16x32_bf16 %2, %15, %11, %2\n\tv_add_f32 %3, %3, %4\n\tv_add_f32 %5, %5, %16\n\tv_mfma_f32_16x16x32_bf16 %6, %17, %11, %6\n\tv_add_f32 %7, %7, %18\n\tv_mul_f32 %8, %19, %19\n\tv_mfma_f32_16x16x32_bf16 %9, %20, %11, %9\n\tv_mul_f32 %4, %3, %3"
      : "+v"(acc[3][1]), "=&v"(p[13]), "+v"(acc[3][2]), "+v"(gm[1]), "+v"(gz[1]), "+v"(gm[2]), "+v"(acc[3][3]), "+v"(gm[3]), "=&v"(gz[0]), "+v"(acc[3][0]) : "a"(fw1[8][1]), "v"(p[0]), "v"(vHa), "s"(rsW), "s"(sh[5]), "a"(fw1[8][2]), "v"(gz[2]), "a"(fw1[8][3]), "v"(gz[3]), "v"(gm[0]), "a"(fw1[8][0]) : "memory"); SB();
  WAITVB(13);
  SB();
  asm volatile("v_mfma_f32_16x16x32_bf16 %0, %9, %10, %0\n\tbuffer_load_dwordx4 %1, %11, %12, %13 offen offset:256 sc1\n\tv_mfma_f32_16x16x32_bf16 %2, %14, %10, %2\n\tv_mul_f32 %3, %15, %15\n\tv_mul_f32 %4, %16, %16\n\tv_mfma_f32_16x16x32_bf16 %5, %17, %10, %5\n\tv_fmaak_f32 %6, %18, %6, 0x40135761\n\tv_fmaak_f32 %7, %18, %7, 0x40135761\n\tv_mfma_f32_16x16x32_bf16 %8, %19, %10, %8\n\tv_fmaak_f32 %3, %18, %3, 0x40135761"
      : "+v"(acc[3][1]), "=&v"(p[0]), "+v"(acc[3][2]), "=&v"(gz[2]), "=&v"(gz[3]), "+v"(acc[3][3]), "+v"(gz[0]), "+v"(gz[1]), "+v"(acc[3][0]) : "a"(fw1[9][1]), "v"(p[1]), "v"(vHa), "s"(rsW), "s"(sh[6]), "a"(fw1[9][2]), "v"(gm[2]), "v"(gm[3]), "a"(fw1[9][3]), "v"(gc), "a"(fw1[9][0]) : "memory"); SB();
  WAITVB(13);
  SB();
  asm volatile("v_mfma_f32_16x16x32_bf16 %0, %9, %10, %0\n\tbuffer_load_dwordx4 %1, %11, %12, %13 offen offset:256 sc1\n\tv_mfma_f32_16x16x32_bf16 %2, %14, %10, %2\n\tv_fmaak_f32 %3, %15, %3, 0x40135761\n\tv_mul_f32 %4, %4, %16\n\tv_mfma_f32_16x16x32_bf16 %5, %17, %10, %5\n\tv_mul_f32 %6, %6, %18\n\tv_mul_f32 %7, %7, %19\n\tv_mfma_f32_16x16x32_bf16 %8, %20, %10, %8\n\tv_mul_f32 %3, %3, %21"
      : "+v"(acc[3][1]), "=&v"(p[1]), "+v"(acc[3][2]), "+v"(gz[3]), "+v"(gz[0]), "+v"(acc[3][3]), "+v"(gz[1]), "+v"(gz[2]), "+v"(acc[3][0]) : "a"(fw1[10][1]), "v"(p[2]), "v"(vHa), "s"(rsW), "s"(sh[7]), "a"(fw1[10][2]), "v"(gc), "v"(gm[0]), "a"(fw1[10][3]), "v"(gm[1]), "v"(gm[2]), "a"(fw1[10][0]), "v"(gm[3]) : "memory"); SB();
  WAITVB(13);
  SB();
  asm volatile("v_mfma_f32_16x16x32_bf16 %0, %7, %8, %0\n\tbuffer_load_dwordx4 %1, %9, %10, %11 offen offset:256 sc1\n\tv_mfma_f32_16x16x32_bf16 %2, %12, %8, %2\n\tv_exp_f32 %3, %3\n\tv_mfma_f32_16x16x32_bf16 %4, %13, %8, %4\n\tv_exp_f32 %5, %5\n\tv_mfma_f32_16x16x32_bf16 %6, %14, %8, %6"
      : "+v"(acc[3][1]), "=&v"(p[2]), "+v"(acc[3][2]), "+v"(gz[0]), "+v"(acc[3][3]), "+v"(gz[1]), "+v"(acc[3][0]) : "a"(fw1[11][1]), "v"(p[3]), "v"(vHa), "s"(rsW), "s"(sh[8]), "a"(fw1[11][2]), "a"(fw1[11][3]), "a"(fw1[11][0]) : "memory"); SB();
  WAITVB(13);
  SB();
  asm volatile("v_mfma_f32_16x16x32_bf16 %0, %8, %9, %0\n\tbuffer_load_dwordx4 %1, %10, %11, %12 offen offset:256 sc1\n\tv_mfma_f32_16x16x32_bf16 %2, %13, %9, %2\n\tv_exp_f32 %3, %3\n\tv_mfma_f32_16x16x32_bf16 %4, %14, %9, %4\n\tv_exp_f32 %5, %5\n\tv_mfma_f32_16x16x32_bf16 %6, %15, %9, %6\n\tv_add_f32 %7, 1.0, %7"
      : "+v"(acc[3][1]), "=&v"(p[3]), "+v"(acc[3][2]), "+v"(gz[2]), "+v"(acc[3][3]), "+v"(gz[3]), "+v"(acc[3][0]), "+v"(gz[0]) : "a"(fw1[12][1]), "v"(p[4]), "v"(vHa), "s"(rsW), "s"(sh[9]), "a"(fw1[12][2]), "a"(fw1[12][3]), "a"(fw1[12][0]) : "memory"); SB();
  WAITVB(13);
  SB();
  asm volatile("v_mfma_f32_16x16x32_bf16 %0, %8, %9, %0\n\tbuffer_load_dwordx4 %1, %10, %11, %12 offen offset:256 sc1\n\tv_mfma_f32_16x16x32_bf16 %2, %13, %9, %2\n\tv_add_f32 %3, 1.0, %3\n\tv_add_f32 %4, 1.0, %4\n\tv_mfma_f32_16x16x32_bf16 %5, %14, %9, %5\n\tv_add_f32 %6, 1.0, %6\n\tv_mfma_f32_16x16x32_bf16 %7, %15, %9, %7"
      : "+v"(acc[3][1]), "=&v"(p[4]), "+v"(acc[3][2]), "+v"(gz[1]), "+v"(gz[2]), "+v"(acc[3][3]), "+v"(gz[3]), "+v"(acc[3][0]) : "a"(fw1[13][1]), "v"(p[5]), "v"(vHa), "s"(rsW), "s"(sh[10]), "a"(fw1[13][2]), "a"(fw1[13][3]), "a"(fw1[13][0]) : "memory"); SB();
  WAITVB(13);
  SB();
  asm volatile("v_mfma_f32_16x16x32_bf16 %0, %7, %8, %0\n\tbuffer_load_dwordx4 %1, %9, %10, %11 offen offset:256 sc1\n\tv_mfma_f32_16x16x32_bf16 %2, %12, %8, %2\n\tv_rcp_f32 %3, %3\n\tv_mfma_f32_16x16x32_bf16 %4, %13, %8, %4\n\tv_rcp_f32 %5, %5\n\tv_mfma_f32_16x16x32_bf16 %6, %14, %8, %6"
      : "+v"(acc[3][1]), "=&v"(p[5]), "+v"(acc[3][2]), "+v"(gz[0]), "+v"(acc[3][3]), "+v"(gz[1]), "+v"(acc[3][0]) : "a"(fw1[14][1]), "v"(p[6]), "v"(vHa), "s"(rsW), "s"(sh[11]), "a"(fw1[14][2]), "a"(fw1[14][3]), "a"(fw1[14][0]) : "memory"); SB();
  WAITVB(12);
  SB();
  asm volatile("v_mfma_f32_16x16x32_bf16 %0, %9, %10, %0\n\tbuffer_load_dwordx4 %1, %11, %12, %13 offen offset:256 sc1\n\tv_mfma_f32_16x16x32_bf16 %2, %14, %10, %2\n\tv_rcp_f32 %3, %3\n\tv_mfma_f32_16x16x32_bf16 %4, %15, %10, %4\n\tv_rcp_f32 %5, %5\n\tv_mfma_f32_16x16x32_bf16 %6, %16, %10, %6\n\tv_fma_f32 %7, -%17, %7, %17\n\tv_fma_f32 %8, -%18, %8, %18"
      : "+v"(acc[3][1]), "=&v"(p[6]), "+v"(acc[3][2]), "+v"(gz[2]), "+v"(acc[3][3]), "+v"(gz[3]), "+v"(acc[3][0]), "+v"(gz[0]), "+v"(gz[1]) : "v"(fw1[15][1]), "v"(p[7]), "v"(vHa), "s"(rsW), "s"(sh[12]), "v"(fw1[15][2]), "v"(fw1[15][3]), "v"(fw1[15][0]), "v"(gm[0]), "v"(gm[1]) : "memory"); SB();
  STAMP(9);
  WAITVB(12);
  SB();
  asm volatile("v_mfma_f32_16x16x32_bf16 %0, %9, %10, 0\n\tbuffer_load_dwordx4 %1, %11, %12, %13 offen offset:256 sc1\n\tv_mfma_f32_16x16x32_bf16 %2, %14, %10, 0\n\tv_fma_f32 %3, -%15, %3, %15\n\tv_fma_f32 %4, -%16, %4, %16\n\tv_mfma_f32_16x16x32_bf16 %5, %17, %10, 0\n\tv_cvt_pk_bf16_f32 %6, %18, %19\n\tv_cvt_pk_bf16_f32 %7, %3, %4\n\tv_mfma_f32_16x16x32_bf16 %8, %20, %10, %21"
      : "=&v"(acc[4][1]), "=&v"(p[7]), "=&v"(acc[4][2]), "+v"(gz[2]), "+v"(gz[3]), "=&v"(acc[4][3]), "=&v"(uv[2][0]), "=&v"(uv[2][1]), "=&v"(acc[4][0]) : "a"(fw1[0][1]), "v"(p[8]), "v"(vHa), "s"(rsW), "s"(sh[13]), "a"(fw1[0][2]), "v"(gm[2]), "v"(gm[3]), "a"(fw1[0][3]), "v"(gz[0]), "v"(gz[1]), "a"(fw1[0][0]), "v"(bb) : "memory"); SB();
  WAITVB(12);
  SB();
  asm volatile("v_mfma_f32_16x16x32_bf16 %0, %5, %6, %0\n\tbuffer_load_dwordx4 %1, %7, %8, %9 offen offset:256 sc1\n\tv_mfma_f32_16x16x32_bf16 %2, %10, %6, %2\n\tds_write_b128 %11, %12 offset:5120\n\tv_mfma_f32_16x16x32_bf16 %3, %13, %6, %3\n\tv_mfma_f32_16x16x32_bf16 %4, %14, %6, %4"
      : "+v"(acc[4][1]), "=&v"(p[8]), "+v"(acc[4][2]), "+v"(acc[4][3]), "+v"(acc[4][0]) : "a"(fw1[1][1]), "v"(p[9]), "v"(vHa), "s"(rsW), "s"(sh[14]), "a"(fw1[1][2]), "v"(rw), "v"(acc[3][1]), "a"(fw1[1][3]), "a"(fw1[1][0]) : "memory"); SB();
  WAITVB(12);
  SB();
  asm volatile("v_mfma_f32_16x16x32_bf16 %0, %5, %6, %0\n\tbuffer_load_dwordx4 %1, %7, %8, %9 offen offset:256 sc1\n\tv_mfma_f32_16x16x32_bf16 %2, %10, %6, %2\n\tds_write_b128 %11, %12 offset:6144\n\tv_mfma_f32_16x16x32_bf16 %3, %13, %6, %3\n\tv_mfma_f32_16x16x32_bf16 %4, %14, %6, %4"
      : "+v"(acc[4][1]), "=&v"(p[9]), "+v"(acc[4][2]), "+v"(acc[4][3]), "+v"(acc[4][0]) : "a"(fw1[2][1]), "v"(p[10]), "v"(vHa), "s"(rsW), "s"(sh[15]), "a"(fw1[2][2]), "v"(rw), "v"(acc[3][2]), "a"(fw1[2][3]), "a"(fw1[2][0]) : "memory"); SB();
  WAITVB(12);
  SB();
  asm volatile("v_mfma_f32_16x16x32_bf16 %0, %5, %6, %0\n\tbuffer_load_dwordx4 %1, %7, %8, %9 offen offset:256 sc1\n\tv_mfma_f32_16x16x32_bf16 %2, %10, %6, %2\n\tds_write_b128 %11, %12 offset:7168\n\tv_mfma_f32_16x16x32_bf16 %3, %13, %6, %3\n\tv_mfma_f32_16x16x32_bf16 %4, %14, %6, %4"
      : "+v"(acc[4][1]), "=&v"(p[10]), "+v"(acc[4][2]), "+v"(acc[4][3]), "+v"(acc[4][0]) : "a"(fw1[3][1]), "v"(p[11]), "v"(vHc), "s"(rsW), "s"(sh[0]), "a"(fw1[3][2]), "v"(rw), "v"(acc[3][3]), "a"(fw1[3][3]), "a"(fw1[3][0]) : "memory"); SB();
  WAITVB(12);
  SB();
  asm volatile("v_mfma_f32_16x16x32_bf16 %0, %3, %4, %0\n\tbuffer_load_dwordx4 %1, %5, %6, %7 offen offset:256 sc1\n\tv_mfma_f32_16x16x32_bf16 %2, %8, %4, %2"
      : "+v"(acc[4][1]), "=&v"(p[11]), "+v"(acc[4][2]) : "a"(fw1[4][1]), "v"(p[12]), "v"(vHc), "s"(rsW), "s"(sh[1]), "a"(fw1[4][2]) : "memory"); SB();
  BAR();
  SB();
  asm volatile("v_mfma_f32_16x16x32_bf16 %0, %5, %6, %0\n\tds_read_b128 %1, %7 offset:5120\n\tds_read_b128 %2, %8 offset:6144\n\tv_mfma_f32_16x16x32_bf16 %3, %9, %6, %3\n\tds_read_b128 %4, %10 offset:7168"
      : "+v"(acc[4][3]), "=&v"(pm[3][0]), "=&v"(pm[3][1]), "+v"(acc[4][0]), "=&v"(pm[3][2]) : "a"(fw1[4][3]), "v"(p[12]), "v"(rr1), "v"(rr2), "a"(fw1[4][0]), "v"(rr3) : "memory"); SB();
  WAITVB(12);
  SB();
  asm volatile("v_mfma_f32_16x16x32_bf16 %0, %5, %6, %0\n\tbuffer_load_dwordx4 %1, %7, %8, %9 offen offset:256 sc1\n\tv_mfma_f32_16x16x32_bf16 %2, %10, %6, %2\n\tv_mfma_f32_16x16x32_bf16 %3, %11, %6, %3\n\tv_mfma_f32_16x16x32_bf16 %4, %12, %6, %4"
      : "+v"(acc[4][1]), "=&v"(p[12]), "+v"(acc[4][2]), "+v"(acc[4][3]), "+v"(acc[4][0]) : "a"(fw1[5][1]), "v"(p[13]), "v"(vHc), "s"(rsW), "s"(sh[2]), "a"(fw1[5][2]), "a"(fw1[5][3]), "a"(fw1[5][0]) : "memory"); SB();
  WAITVB(12);
  SB();
  asm volatile("v_mfma_f32_16x16x32_bf16 %0, %3, %4, %0\n\tbuffer_load_dwordx4 %1, %5, %6, %7 offen offset:256 sc1\n\tv_mfma_f32_16x16x32_bf16 %2, %8, %4, %2"
      : "+v"(acc[4][1]), "=&v"(p[13]), "+v"(acc[4][2]) : "a"(fw1[6][1]), "v"(p[0]), "v"(vHc), "s"(rsW), "s"(sh[3]), "a"(fw1[6][2]) : "memory"); SB();
  WAITL(0);
  asm volatile("v_add_f32 %0, %6, %7\n\tv_mfma_f32_16x16x32_bf16 %1, %8, %9, %1\n\tv_add_f32 %2, %10, %11\n\tv_add_f32 %3, %12, %13\n\tv_mfma_f32_16x16x32_bf16 %4, %14, %9, %4\n\tv_add_f32 %5, %15, %16"
      : "=&v"(gm[0]), "+v"(acc[4][3]), "=&v"(gm[1]), "=&v"(gm[2]), "+v"(acc[4][0]), "=&v"(gm[3]) : "v"(acc[3][0][0]), "v"(pm[3][0][0]), "a"(fw1[6][3]), "v"(p[0]), "v"(acc[3][0][1]), "v"(pm[3][0][1]), "v"(acc[3][0][2]), "v"(pm[3][0][2]), "a"(fw1[6][0]), "v"(acc[3][0][3]), "v"(pm[3][0][3])); SB();
  WAITVB(12);
  SB();
  asm volatile("v_mfma_f32_16x16x32_bf16 %0, %9, %10, %0\n\tbuffer_load_dwordx4 %1, %11, %12, %13 offen offset:256 sc1\n\tv_mfma_f32_16x16x32_bf16 %2, %14, %10, %2\n\tv_add_f32 %3, %15, %16\n\tv_add_f32 %4, %17, %18\n\tv_mfma_f32_16x16x32_bf16 %5, %19, %10, %5\n\tv_add_f32 %6, %20, %21\n\tv_add_f32 %7, %22, %23\n\tv_mfma_f32_16x16x32_bf16 %8, %24, %10, %8"
      : "+v"(acc[4][1]), "=&v"(p[0]), "+v"(acc[4][2]), "=&v"(gz[0]), "=&v"(gz[1]), "+v"(acc[4][3]), "=&v"(gz[2]), "=&v"(gz[3]), "+v"(acc[4][0]) : "a"(fw1[7][1]), "v"(p[1]), "v"(vHc), "s"(rsW), "s"(sh[4]), "a"(fw1[7][2]), "v"(pm[3][1][0]), "v"(pm[3][2][0]), "v"(pm[3][1][1]), "v"(pm[3][2][1]), "a"(fw1[7][3]), "v"(pm[3][1][2]), "v"(pm[3][2][2]), "v"(pm[3][1][3]), "v"(pm[3][2][3]), "a"(fw1[7][0]) : "memory"); SB();
  asm volatile("v_add_f32 %0, %0, %1"
      : "+v"(gm[0]) : "v"(gz[0])); SB();
  WAITVB(12);
  SB();
  asm volatile("v_mfma_f32_16x16x32_bf16 %0, %10, %11, %0\n\tbuffer_load_dwordx4 %1, %12, %13, %14 offen offset:256 sc1\n\tv_mfma_f32_16x16x32_bf16 %2, %15, %11, %2\n\tv_add_f32 %3, %3, %4\n\tv_add_f32 %5, %5, %16\n\tv_mfma_f32_16x16x32_bf16 %6, %17, %11, %6\n\tv_add_f32 %7, %7, %18\n\tv_mul_f32 %8, %19, %19\n\tv_mfma_f32_16x16x32_bf16 %9, %20, %11, %9\n\tv_mul_f32 %4, %3, %3"
      : "+v"(acc[4][1]), "=&v"(p[1]), "+v"(acc[4][2]), "+v"(gm[1]), "+v"(gz[1]), "+v"(gm[2]), "+v"(acc[4][3]), "+v"(gm[3]), "=&v"(gz[0]), "+v"(acc[4][0]) : "a"(fw1[8][1]), "v"(p[2]), "v"(vHc), "s"(rsW), "s"(sh[5]), "a"(fw1[8][2]), "v"(gz[2]), "a"(fw1[8][3]), "v"(gz[3]), "v"(gm[0]), "a"(fw1[8][0]) : "memory"); SB();
  WAITVB(12);
  SB();
  asm volatile("v_mfma_f32_16x16x32_bf16 %0, %9, %10, %0\n\tbuffer_load_dwordx4 %1, %11, %12, %13 offen offset:256 sc1\n\tv_mfma_f32_16x16x32_bf16 %2, %14, %10, %2\n\tv_mul_f32 %3, %15, %15\n\tv_mul_f32 %4, %16, %16\n\tv_mfma_f32_16x16x32_bf16 %5, %17, %10, %5\n\tv_fmaak_f32 %6, %18, %6, 0x40135761\n\tv_fmaak_f32 %7, %18, %7, 0x40135761\n\tv_mfma_f32_16x16x32_bf16 %8, %19, %10, %8\n\tv_fmaak_f32 %3, %18, %3, 0x40135761"
      : "+v"(acc[4][1]), "=&v"(p[2]), "+v"(acc[4][2]), "=&v"(gz[2]), "=&v"(gz[3]), "+v"(acc[4][3]), "+v"(gz[0]), "+v"(gz[1]), "+v"(acc[4][0]) : "a"(fw1[9][1]), "v"(p[3]), "v"(vHc), "s"(rsW), "s"(sh[6]), "a"(fw1[9][2]), "v"(gm[2]), "v"(gm[3]), "a"(fw1[9][3]), "v"(gc), "a"(fw1[9][0]) : "memory"); SB();
  WAITVB(12);
  SB();
  asm volatile("v_mfma_f32_16x16x32_bf16 %0, %9, %10, %0\n\tbuffer_load_dwordx4 %1, %11, %12, %13 offen offset:256 sc1\n\tv_mfma_f32_16x16x32_bf16 %2, %14, %10, %2\n\tv_fmaak_f32 %3, %15, %3, 0x40135761\n\tv_mul_f32 %4, %4, %16\n\tv_mfma_f32_16x16x32_bf16 %5, %17, %10, %5\n\tv_mul_f32 %6, %6, %18\n\tv_mul_f32 %7, %7, %19\n\tv_mfma_f32_16x16x32_bf16 %8, %20, %10, %8\n\tv_mul_f32 %3, %3, %21"
      : "+v"(acc[4][1]), "=&v"(p[3]), "+v"(acc[4][2]), "+v"(gz[3]), "+v"(gz[0]), "+v"(acc[4][3]), "+v"(gz[1]), "+v"(gz[2]), "+v"(acc[4][0]) : "a"(fw1[10][1]), "v"(p[4]), "v"(vHc), "s"(rsW), "s"(sh[7]), "a"(fw1[10][2]), "v"(gc), "v"(gm[0]), "a"(fw1[10][3]), "v"(gm[1]), "v"(gm[2]), "a"(fw1[10][0]), "v"(gm[3]) : "memory"); SB();
  WAITVB(12);
  SB();
  asm volatile("v_mfma_f32_16x16x32_bf16 %0, %7, %8, %0\n\tbuffer_load_dwordx4 %1, %9, %10, %11 offen offset:256 sc1\n\tv_mfma_f32_16x16x32_bf16 %2, %12, %8, %2\n\tv_exp_f32 %3, %3\n\tv_mfma_f32_16x16x32_bf16 %4, %13, %8, %4\n\tv_exp_f32 %5, %5\n\tv_mfma_f32_16x16x32_bf16 %6, %14, %8, %6"
      : "+v"(acc[4][1]), "=&v"(p[4]), "+v"(acc[4][2]), "+v"(gz[0]), "+v"(acc[4][3]), "+v"(gz[1]), "+v"(acc[4][0]) : "a"(fw1[11][1]), "v"(p[5]), "v"(vHc), "s"(rsW), "s"(sh[8]), "a"(fw1[11][2]), "a"(fw1[11][3]), "a"(fw1[11][0]) : "memory"); SB();
  WAITVB(12);
  SB();
  asm volatile("v_mfma_f32_16x16x32_bf16 %0, %8, %9, %0\n\tbuffer_load_dwordx4 %1, %10, %11, %12 offen offset:256 sc1\n\tv_mfma_f32_16x16x32_bf16 %2, %13, %9, %2\n\tv_exp_f32 %3, %3\n\tv_mfma_f32_16x16x32_bf16 %4, %14, %9, %4\n\tv_exp_f32 %5, %5\n\tv_mfma_f32_16x16x32_bf16 %6, %15, %9, %6\n\tv_add_f32 %7, 1.0, %7"
      : "+v"(acc[4][1]), "=&v"(p[5]), "+v"(acc[4][2]), "+v"(gz[2]), "+v"(acc[4][3]), "+v"(gz[3]), "+v"(acc[4][0]), "+v"(gz[0]) : "a"(fw1[12][1]), "v"(p[6]), "v"(vHc), "s"(rsW), "s"(sh[9]), "a"(fw1[12][2]), "a"(fw1[12][3]), "a"(fw1[12][0]) : "memory"); SB();
  WAITVB(12);
  SB();
  asm volatile("v_mfma_f32_16x16x32_bf16 %0, %8, %9, %0\n\tbuffer_load_dwordx4 %1, %10, %11, %12 offen offset:256 sc1\n\tv_mfma_f32_16x16x32_bf16 %2, %13, %9, %2\n\tv_add_f32 %3, 1.0, %3\n\tv_add_f32 %4, 1.0, %4\n\tv_mfma_f32_16x16x32_bf16 %5, %14, %9, %5\n\tv_add_f32 %6, 1.0, %6\n\tv_mfma_f32_16x16x32_bf16 %7, %15, %9, %7"
      : "+v"(acc[4][1]), "=&v"(p[6]), "+v"(acc[4][2]), "+v"(gz[1]), "+v"(gz[2]), "+v"(acc[4][3]), "+v"(gz[3]), "+v"(acc[4][0]) : "a"(fw1[13][1]), "v"(p[7]), "v"(vHc), "s"(rsW), "s"(sh[10]), "a"(fw1[13][2]), "a"(fw1[13][3]), "a"(fw1[13][0]) : "memory"); SB();
  WAITVB(12);
  SB();
  asm volatile("v_mfma_f32_16x16x32_bf16 %0, %7, %8, %0\n\tbuffer_load_dwordx4 %1, %9, %10, %11 offen offset:256 sc1\n\tv_mfma_f32_16x16x32_bf16 %2, %12, %8, %2\n\tv_rcp_f32 %3, %3\n\tv_mfma_f32_16x16x32_bf16 %4, %13, %8, %4\n\tv_rcp_f32 %5, %5\n\tv_mfma_f32_16x16x32_bf16 %6, %14, %8, %6"
      : "+v"(acc[4][1]), "=&v"(p[7]), "+v"(acc[4][2]), "+v"(gz[0]), "+v"(acc[4][3]), "+v"(gz[1]), "+v"(acc[4][0]) : "a"(fw1[14][1]), "v"(p[8]), "v"(vHc), "s"(rsW), "s"(sh[11]), "a"(fw1[14][2]), "a"(fw1[14][3]), "a"(fw1[14][0]) : "memory"); SB();
  WAITVB(12);
  SB();
  asm volatile("v_mfma_f32_16x16x32_bf16 %0, %9, %10, %0\n\tbuffer_load_dwordx4 %1, %11, %12, %13 offen offset:256 sc1\n\tv_mfma_f32_16x16x32_bf16 %2, %14, %10, %2\n\tv_rcp_f32 %3, %3\n\tv_mfma_f32_16x16x32_bf16 %4, %15, %10, %4\n\tv_rcp_f32 %5, %5\n\tv_mfma_f32_16x16x32_bf16 %6, %16, %10, %6\n\tv_fma_f32 %7, -%17, %7, %17\n\tv_fma_f32 %8, -%18, %8, %18"
      : "+v"(acc[4][1]), "=&v"(p[8]), "+v"(acc[4][2]), "+v"(gz[2]), "+v"(acc[4][3]), "+v"(gz[3]), "+v"(acc[4][0]), "+v"(gz[0]), "+v"(gz[1]) : "v"(fw1[15][1]), "v"(p[9]), "v"(vHc), "s"(rsW), "s"(sh[12]), "v"(fw1[15][2]), "v"(fw1[15][3]), "v"(fw1[15][0]), "v"(gm[0]), "v"(gm[1]) : "memory"); SB();
  WAITVB(12);
  SB();
  asm volatile("v_mfma_f32_16x16x32_bf16 %0, %9, %10, 0\n\tbuffer_load_dwordx4 %1, %11, %12, %13 offen offset:256 sc1\n\tv_mfma_f32_16x16x32_bf16 %2, %14, %10, 0\n\tv_fma_f32 %3, -%15, %3, %15\n\tv_fma_f32 %4, -%16, %4, %16\n\tv_mfma_f32_16x16x32_bf16 %5, %17, %10, 0\n\tv_cvt_pk_bf16_f32 %6, %18, %19\n\tv_cvt_pk_bf16_f32 %7, %3, %4\n\tv_mfma_f32_16x16x32_bf16 %8, %20, %10, %21"
      : "=&v"(acc[5][1]), "=&v"(p[9]), "=&v"(acc[5][2]), "+v"(gz[2]), "+v"(gz[3]), "=&v"(acc[5][3]), "=&v"(uv[3][0]), "=&v"(uv[3][1]), "=&v"(acc[5][0]) : "a"(fw1[0][1]), "v"(p[10]), "v"(vHc), "s"(rsW), "s"(sh[13]), "a"(fw1[0][2]), "v"(gm[2]), "v"(gm[3]), "a"(fw1[0][3]), "v"(gz[0]), "v"(gz[1]), "a"(fw1[0][0]), "v"(bb) : "memory"); SB();
  WAITVB(12);
  SB();
  asm volatile("v_mfma_f32_16x16x32_bf16 %0, %5, %6, %0\n\tbuffer_load_dwordx4 %1, %7, %8, %9 offen offset:256 sc1\n\tv_mfma_f32_16x16x32_bf16 %2, %10, %6, %2\n\tds_write_b128 %11, %12 offset:1024\n\tv_mfma_f32_16x16x32_bf16 %3, %13, %6, %3\n\tv_mfma_f32_16x16x32_bf16 %4, %14, %6, %4"
      : "+v"(acc[5][1]), "=&v"(p[10]), "+v"(acc[5][2]), "+v"(acc[5][3]), "+v"(acc[5][0]) : "a"(fw1[1][1]), "v"(p[11]), "v"(vHc), "s"(rsW), "s"(sh[14]), "a"(fw1[1][2]), "v"(rw), "v"(acc[4][1]), "a"(fw1[1][3]), "a"(fw1[1][0]) : "memory"); SB();
  WAITVB(12);
  SB();
  asm volatile("v_mfma_f32_16x16x32_bf16 %0, %5, %6, %0\n\tbuffer_load_dwordx4 %1, %7, %8, %9 offen offset:256 sc1\n\tv_mfma_f32_16x16x32_bf16 %2, %10, %6, %2\n\tds_write_b128 %11, %12 offset:2048\n\tv_mfma_f32_16x16x32_bf16 %3, %13, %6, %3\n\tv_mfma_f32_16x16x32_bf16 %4, %14, %6, %4"
      : "+v"(acc[5][1]), "=&v"(p[11]), "+v"(acc[5][2]), "+v"(acc[5][3]), "+v"(acc[5][0]) : "a"(fw1[2][1]), "v"(p[12]), "v"(vHc), "s"(rsW), "s"(sh[15]), "a"(fw1[2][2]), "v"(rw), "v"(acc[4][2]), "a"(fw1[2][3]), "a"(fw1[2][0]) : "memory"); SB();
  WAITVB(12);
  SB();
  asm volatile("v_mfma_f32_16x16x32_bf16 %0, %5, %6, %0\n\tbuffer_load_dwordx4 %1, %7, %8, %9 offen offset:0 sc1\n\tv_mfma_f32_16x16x32_bf16 %2, %10, %6, %2\n\tds_write_b128 %11, %12 offset:3072\n\tv_mfma_f32_16x16x32_bf16 %3, %13, %6, %3\n\tv_mfma_f32_16x16x32_bf16 %4, %14, %6, %4"
      : "+v"(acc[5][1]), "=&v"(p[12]), "+v"(acc[5][2]), "+v"(acc[5][3]), "+v"(acc[5][0]) : "a"(fw1[3][1]), "v"(p[13]), "v"(vHb), "s"(rsW), "s"(sh[0]), "a"(fw1[3][2]), "v"(rw), "v"(acc[4][3]), "a"(fw1[3][3]), "a"(fw1[3][0]) : "memory"); SB();
  WAITVB(12);
  SB();
  asm volatile("v_mfma_f32_16x16x32_bf16 %0, %3, %4, %0\n\tbuffer_load_dwordx4 %1, %5, %6, %7 offen offset:0 sc1\n\tv_mfma_f32_16x16x32_bf16 %2, %8, %4, %2"
      : "+v"(acc[5][1]), "=&v"(p[13]), "+v"(acc[5][2]) : "a"(fw1[4][1]), "v"(p[0]), "v"(vHb), "s"(rsW), "s"(sh[1]), "a"(fw1[4][2]) : "memory"); SB();
  BAR();
  SB();
  asm volatile("v_mfma_f32_16x16x32_bf16 %0, %5, %6, %0\n\tds_read_b128 %1, %7 offset:1024\n\tds_read_b128 %2, %8 offset:2048\n\tv_mfma_f32_16x16x32_bf16 %3, %9, %6, %3\n\tds_read_b128 %4, %10 offset:3072"
      : "+v"(acc[5][3]), "=&v"(pm[4][0]), "=&v"(pm[4][1]), "+v"(acc[5][0]), "=&v"(pm[4][2]) : "a"(fw1[4][3]), "v"(p[0]), "v"(rr1), "v"(rr2), "a"(fw1[4][0]), "v"(rr3) : "memory"); SB();
  WAITVB(12);
  SB();
  asm volatile("v_mfma_f32_16x16x32_bf16 %0, %5, %6, %0\n\tbuffer_load_dwordx4 %1, %7, %8, %9 offen offset:0 sc1\n\tv_mfma_f32_16x16x32_bf16 %2, %10, %6, %2\n\tv_mfma_f32_16x16x32_bf16 %3, %11, %6, %3\n\tv_mfma_f32_16x16x32_bf16 %4, %12, %6, %4"
      : "+v"(acc[5][1]), "=&v"(p[0]), "+v"(acc[5][2]), "+v"(acc[5][3]), "+v"(acc[5][0]) : "a"(fw1[5][1]), "v"(p[1]), "v"(vHb), "s"(rsW), "s"(sh[2]), "a"(fw1[5][2]), "a"(fw1[5][3]), "a"(fw1[5][0]) : "memory"); SB();
  WAITVB(12);
  SB();
  asm volatile("v_mfma_f32_16x16x32_bf16 %0, %3, %4, %0\n\tbuffer_load_dwordx4 %1, %5, %6, %7 offen offset:0 sc1\n\tv_mfma_f32_16x16x32_bf16 %2, %8, %4, %2"
      : "+v"(acc[5][1]), "=&v"(p[1]), "+v"(acc[5][2]) : "a"(fw1[6][1]), "v"(p[2]), "v"(vHb), "s"(rsW), "s"(sh[3]), "a"(fw1[6][2]) : "memory"); SB();
  WAITL(0);
  asm volatile("v_add_f32 %0, %6, %7\n\tv_mfma_f32_16x16x32_bf16 %1, %8, %9, %1\n\tv_add_f32 %2, %10, %11\n\tv_add_f32 %3, %12, %13\n\tv_mfma_f32_16x16x32_bf16 %4, %14, %9, %4\n\tv_add_f32 %5, %15, %16"
      : "=&v"(gm[0]), "+v"(acc[5][3]), "=&v"(gm[1]), "=&v"(gm[2]), "+v"(acc[5][0]), "=&v"(gm[3]) : "v"(acc[4][0][0]), "v"(pm[4][0][0]), "a"(fw1[6][3]), "v"(p[2]), "v"(acc[4][0][1]), "v"(pm[4][0][1]), "v"(acc[4][0][2]), "v"(pm[4][0][2]), "a"(fw1[6][0]), "v"(acc[4][0][3]), "v"(pm[4][0][3])); SB();
  WAITVB(12);
  SB();
  asm volatile("v_mfma_f32_16x16x32_bf16 %0, %9, %10, %0\n\tbuffer_load_dwordx4 %1, %11, %12, %13 offen offset:0 sc1\n\tv_mfma_f32_16x16x32_bf16 %2, %14, %10, %2\n\tv_add_f32 %3, %15, %16\n\tv_add_f32 %4, %17, %18\n\tv_mfma_f32_16x16x32_bf16 %5, %19, %10, %5\n\tv_add_f32 %6, %20, %21\n\tv_add_f32 %7, %22, %23\n\tv_mfma_f32_16x16x32_bf16 %8, %24, %10, %8"
      : "+v"(acc[5][1]), "=&v"(p[2]), "+v"(acc[5][2]), "=&v"(gz[0]), "=&v"(gz[1]), "+v"(acc[5][3]), "=&v"(gz[2]), "=&v"(gz[3]), "+v"(acc[5][0]) : "a"(fw1[7][1]), "v"(p[3]), "v"(vHb), "s"(rsW), "s"(sh[4]), "a"(fw1[7][2]), "v"(pm[4][1][0]), "v"(pm[4][2][0]), "v"(pm[4][1][1]), "v"(pm[4][2][1]), "a"(fw1[7][3]), "v"(pm[4][1][2]), "v"(pm[4][2][2]), "v"(pm[4][1][3]), "v"(pm[4][2][3]), "a"(fw1[7][0]) : "memory"); SB();
  asm volatile("v_add_f32 %0, %0, %1"
      : "+v"(gm[0]) : "v"(gz[0])); SB();
  WAITVB(12);
  SB();
  asm volatile("v_mfma_f32_16x16x32_bf16 %0, %10, %11, %0\n\tbuffer_load_dwordx4 %1, %12, %13, %14 offen offset:0 sc1\n\tv_mfma_f32_16x16x32_bf16 %2, %15, %11, %2\n\tv_add_f32 %3, %3, %4\n\tv_add_f32 %5, %5, %16\n\tv_mfma_f32_16x16x32_bf16 %6, %17, %11, %6\n\tv_add_f32 %7, %7, %18\n\tv_mul_f32 %8, %19, %19\n\tv_mfma_f32_16x16x32_bf16 %9, %20, %11, %9\n\tv_mul_f32 %4, %3, %3"
      : "+v"(acc[5][1]), "=&v"(p[3]), "+v"(acc[5][2]), "+v"(gm[1]), "+v"(gz[1]), "+v"(gm[2]), "+v"(acc[5][3]), "+v"(gm[3]), "=&v"(gz[0]), "+v"(acc[5][0]) : "a"(fw1[8][1]), "v"(p[4]), "v"(vHb), "s"(rsW), "s"(sh[5]), "a"(fw1[8][2]), "v"(gz[2]), "a"(fw1[8][3]), "v"(gz[3]), "v"(gm[0]), "a"(fw1[8][0]) : "memory"); SB();
  WAITVB(12);
  SB();
  asm volatile("v_mfma_f32_16x16x32_bf16 %0, %9, %10, %0\n\tbuffer_load_dwordx4 %1, %11, %12, %13 offen offset:0 sc1\n\tv_mfma_f32_16x16x32_bf16 %2, %14, %10, %2\n\tv_mul_f32 %3, %15, %15\n\tv_mul_f32 %4, %16, %16\n\tv_mfma_f32_16x16x32_bf16 %5, %17, %10, %5\n\tv_fmaak_f32 %6, %18, %6, 0x40135761\n\tv_fmaak_f32 %7, %18, %7, 0x40135761\n\tv_mfma_f32_16x16x32_bf16 %8, %19, %10, %8\n\tv_fmaak_f32 %3, %18, %3, 0x40135761"
      : "+v"(acc[5][1]), "=&v"(p[4]), "+v"(acc[5][2]), "=&v"(gz[2]), "=&v"(gz[3]), "+v"(acc[5][3]), "+v"(gz[0]), "+v"(gz[1]), "+v"(acc[5][0]) : "a"(fw1[9][1]), "v"(p[5]), "v"(vHb), "s"(rsW), "s"(sh[6]), "a"(fw1[9][2]), "v"(gm[2]), "v"(gm[3]), "a"(fw1[9][3]), "v"(gc), "a"(fw1[9][0]) : "memory"); SB();
  WAITVB(12);
  SB();
  asm volatile("v_mfma_f32_16x16x32_bf16 %0, %9, %10, %0\n\tbuffer_load_dwordx4 %1, %11, %12, %13 offen offset:0 sc1\n\tv_mfma_f32_16x16x32_bf16 %2, %14, %10, %2\n\tv_fmaak_f32 %3, %15, %3, 0x40135761\n\tv_mul_f32 %4, %4, %16\n\tv_mfma_f32_16x16x32_bf16 %5, %17, %10, %5\n\tv_mul_f32 %6, %6, %18\n\tv_mul_f32 %7, %7, %19\n\tv_mfma_f32_16x16x32_bf16 %8, %20, %10, %8\n\tv_mul_f32 %3, %3, %21"
      : "+v"(acc[5][1]), "=&v"(p[5]), "+v"(acc[5][2]), "+v"(gz[3]), "+v"(gz[0]), "+v"(acc[5][3]), "+v"(gz[1]), "+v"(gz[2]), "+v"(acc[5][0]) : "a"(fw1[10][1]), "v"(p[6]), "v"(vHb), "s"(rsW), "s"(sh[7]), "a"(fw1[10][2]), "v"(gc), "v"(gm[0]), "a"(fw1[10][3]), "v"(gm[1]), "v"(gm[2]), "a"(fw1[10][0]), "v"(gm[3]) : "memory"); SB();
  WAITVB(12);
  SB();
  asm volatile("v_mfma_f32_16x16x32_bf16 %0, %7, %8, %0\n\tbuffer_load_dwordx4 %1, %9, %10, %11 offen offset:0 sc1\n\tv_mfma_f32_16x16x32_bf16 %2, %12, %8, %2\n\tv_exp_f32 %3, %3\n\tv_mfma_f32_16x16x32_bf16 %4, %13, %8, %4\n\tv_exp_f32 %5, %5\n\tv_mfma_f32_16x16x32_bf16 %6, %14, %8, %6"
      : "+v"(acc[5][1]), "=&v"(p[6]), "+v"(acc[5][2]), "+v"(gz[0]), "+v"(acc[5][3]), "+v"(gz[1]), "+v"(acc[5][0]) : "a"(fw1[11][1]), "v"(p[7]), "v"(vHb), "s"(rsW), "s"(sh[8]), "a"(fw1[11][2]), "a"(fw1[11][3]), "a"(fw1[11][0]) : "memory"); SB();
  WAITVB(12);
  SB();
  asm volatile("v_mfma_f32_16x16x32_bf16 %0, %8, %9, %0\n\tbuffer_load_dwordx4 %1, %10, %11, %12 offen offset:0 sc1\n\tv_mfma_f32_16x16x32_bf16 %2, %13, %9, %2\n\tv_exp_f32 %3, %3\n\tv_mfma_f32_16x16x32_bf16 %4, %14, %9, %4\n\tv_exp_f32 %5, %5\n\tv_mfma_f32_16x16x32_bf16 %6, %15, %9, %6\n\tv_add_f32 %7, 1.0, %7"
      : "+v"(acc[5][1]), "=&v"(p[7]), "+v"(acc[5][2]), "+v"(gz[2]), "+v"(acc[5][3]), "+v"(gz[3]), "+v"(acc[5][0]), "+v"(gz[0]) : "a"(fw1[12][1]), "v"(p[8]), "v"(vHb), "s"(rsW), "s"(sh[9]), "a"(fw1[12][2]), "a"(fw1[12][3]), "a"(fw1[12][0]) : "memory"); SB();
  WAITVB(12);
  SB();
  asm volatile("v_mfma_f32_16x16x32_bf16 %0, %8, %9, %0\n\tbuffer_load_dwordx4 %1, %10, %11, %12 offen offset:0 sc1\n\tv_mfma_f32_16x16x32_bf16 %2, %13, %9, %2\n\tv_add_f32 %3, 1.0, %3\n\tv_add_f32 %4, 1.0, %4\n\tv_mfma_f32_16x16x32_bf16 %5, %14, %9, %5\n\tv_add_f32 %6, 1.0, %6\n\tv_mfma_f32_16x16x32_bf16 %7, %15, %9, %7"
      : "+v"(acc[5][1]), "=&v"(p[8]), "+v"(acc[5][2]), "+v"(gz[1]), "+v"(gz[2]), "+v"(acc[5][3]), "+v"(gz[3]), "+v"(acc[5][0]) : "a"(fw1[13][1]), "v"(p[9]), "v"(vHb), "s"(rsW), "s"(sh[10]), "a"(fw1[13][2]), "a"(fw1[13][3]), "a"(fw1[13][0]) : "memory"); SB();
  WAITVB(12);
  SB();
  asm volatile("v_mfma_f32_16x16x32_bf16 %0, %7, %8, %0\n\tbuffer_load_dwordx4 %1, %9, %10, %11 offen offset:0 sc1\n\tv_mfma_f32_16x16x32_bf16 %2, %12, %8, %2\n\tv_rcp_f32 %3, %3\n\tv_mfma_f32_16x16x32_bf16 %4, %13, %8, %4\n\tv_rcp_f32 %5, %5\n\tv_mfma_f32_16x16x32_bf16 %6, %14, %8, %6"
      : "+v"(acc[5][1]), "=&v"(p[9]), "+v"(acc[5][2]), "+v"(gz[0]), "+v"(acc[5][3]), "+v"(gz[1]), "+v"(acc[5][0]) : "a"(fw1[14][1]), "v"(p[10]), "v"(vHb), "s"(rsW), "s"(sh[11]), "a"(fw1[14][2]), "a"(fw1[14][3]), "a"(fw1[14][0]) : "memory"); SB();
  WAITVB(12);
  SB();
  asm volatile("v_mfma_f32_16x16x32_bf16 %0, %9, %10, %0\n\tbuffer_load_dwordx4 %1, %11, %12, %13 offen offset:0 sc1\n\tv_mfma_f32_16x16x32_bf16 %2, %14, %10, %2\n\tv_rcp_f32 %3, %3\n\tv_mfma_f32_16x16x32_bf16 %4, %15, %10, %4\n\tv_rcp_f32 %5, %5\n\tv_mfma_f32_16x16x32_bf16 %6, %16, %10, %6\n\tv_fma_f32 %7, -%17, %7, %17\n\tv_fma_f32 %8, -%18, %8, %18"
      : "+v"(acc[5][1]), "=&v"(p[10]), "+v"(acc[5][2]), "+v"(gz[2]), "+v"(acc[5][3]), "+v"(gz[3]), "+v"(acc[5][0]), "+v"(gz[0]), "+v"(gz[1]) : "v"(fw1[15][1]), "v"(p[11]), "v"(vHb), "s"(rsW), "s"(sh[12]), "v"(fw1[15][2]), "v"(fw1[15][3]), "v"(fw1[15][0]), "v"(gm[0]), "v"(gm[1]) : "memory"); SB();
  WAITVB(12);
  SB();
  asm volatile("v_mfma_f32_16x16x32_bf16 %0, %9, %10, 0\n\tbuffer_load_dwordx4 %1, %11, %12, %13 offen offset:0 sc1\n\tv_mfma_f32_16x16x32_bf16 %2, %14, %10, 0\n\tv_fma_f32 %3, -%15, %3, %15\n\tv_fma_f32 %4, -%16, %4, %16\n\tv_mfma_f32_16x16x32_bf16 %5, %17, %10, 0\n\tv_cvt_pk_bf16_f32 %6, %18, %19\n\tv_cvt_pk_bf16_f32 %7, %3, %4\n\tv_mfma_f32_16x16x32_bf16 %8, %20, %10, %21"
      : "=&v"(acc[6][1]), "=&v"(p[11]), "=&v"(acc[6][2]), "+v"(gz[2]), "+v"(gz[3]), "=&v"(acc[6][3]), "=&v"(uv[4][0]), "=&v"(uv[4][1]), "=&v"(acc[6][0]) : "a"(fw1[0][1]), "v"(p[12]), "v"(vHb), "s"(rsW), "s"(sh[13]), "a"(fw1[0][2]), "v"(gm[2]), "v"(gm[3]), "a"(fw1[0][3]), "v"(gz[0]), "v"(gz[1]), "a"(fw1[0][0]), "v"(bb) : "memory"); SB();
  WAITVB(12);
  SB();
  asm volatile("v_mfma_f32_16x16x32_bf16 %0, %5, %6, %0\n\tbuffer_load_dwordx4 %1, %7, %8, %9 offen offset:0 sc1\n\tv_mfma_f32_16x16x32_bf16 %2, %10, %6, %2\n\tds_write_b128 %11, %12 offset:5120\n\tv_mfma_f32_16x16x32_bf16 %3, %13, %6, %3\n\tv_mfma_f32_16x16x32_bf16 %4, %14, %6, %4"
      : "+v"(acc[6][1]), "=&v"(p[12]), "+v"(acc[6][2]), "+v"(acc[6][3]), "+v"(acc[6][0]) : "a"(fw1[1][1]), "v"(p[13]), "v"(vHb), "s"(rsW), "s"(sh[14]), "a"(fw1[1][2]), "v"(rw), "v"(acc[5][1]), "a"(fw1[1][3]), "a"(fw1[1][0]) : "memory"); SB();
  WAITVB(12);
  SB();
  asm volatile("v_mfma_f32_16x16x32_bf16 %0, %5, %6, %0\n\tbuffer_load_dwordx4 %1, %7, %8, %9 offen offset:0 sc1\n\tv_mfma_f32_16x16x32_bf16 %2, %10, %6, %2\n\tds_write_b128 %11, %12 offset:6144\n\tv_mfma_f32_16x16x32_bf16 %3, %13, %6, %3\n\tv_mfma_f32_16x16x32_bf16 %4, %14, %6, %4"
      : "+v"(acc[6][1]), "=&v"(p[13]), "+v"(acc[6][2]), "+v"(acc[6][3]), "+v"(acc[6][0]) : "a"(fw1[2][1]), "v"(p[0]), "v"(vHb), "s"(rsW), "s"(sh[15]), "a"(fw1[2][2]), "v"(rw), "v"(acc[5][2]), "a"(fw1[2][3]), "a"(fw1[2][0]) : "memory"); SB();
  WAITVB(12);
  SB();
  asm volatile("v_mfma_f32_16x16x32_bf16 %0, %5, %6, %0\n\tv_permlane16_swap_b32 %1, %2\n\tv_permlane16_swap_b32 %3, %4"
      : "+v"(acc[6][1]), "+v"(uv[0][0]), "+v"(uv[1][0]), "+v"(uv[0][1]), "+v"(uv[1][1]) : "a"(fw1[3][1]), "v"(p[1])); SB();
  HS16(vU, sU, 0, ((u32x4){uv[0][0], uv[0][1], uv[1][0], uv[1][1]}));
  SB();
  asm volatile("v_mfma_f32_16x16x32_bf16 %0, %6, %7, %0\n\tds_write_b128 %8, %9 offset:7168\n\tv_mfma_f32_16x16x32_bf16 %1, %10, %7, %1\n\tv_permlane16_swap_b32 %2, %3\n\tv_permlane16_swap_b32 %4, %5"
      : "+v"(acc[6][2]), "+v"(acc[6][3]), "+v"(uv[2][0]), "+v"(uv[3][0]), "+v"(uv[2][1]), "+v"(uv[3][1]) : "a"(fw1[3][2]), "v"(p[1]), "v"(rw), "v"(acc[5][3]), "a"(fw1[3][3]) : "memory"); SB();
  HS16(vU, sU, 512, ((u32x4){uv[2][0], uv[2][1], uv[3][0], uv[3][1]}));
  SB();
  asm volatile("v_mfma_f32_16x16x32_bf16 %0, %1, %2, %0"
      : "+v"(acc[6][0]) : "a"(fw1[3][0]), "v"(p[1])); SB();
  WAITVB(13);
  SB();
  asm volatile("v_mfma_f32_16x16x32_bf16 %0, %2, %3, %0\n\ts_add_u32 m0, %4, 0\n\ts_nop 0\n\tv_mfma_f32_16x16x32_bf16 %1, %5, %3, %1"
      : "+v"(acc[6][1]), "+v"(acc[6][2]) : "a"(fw1[4][1]), "v"(p[2]), "s"(ldsO), "a"(fw1[4][2]) : "memory", "scc"); SB();
  BAR();
  SB();
  asm volatile("v_mfma_f32_16x16x32_bf16 %0, %5, %6, %0\n\tds_read_b128 %1, %7 offset:5120\n\tds_read_b128 %2, %8 offset:6144\n\tv_mfma_f32_16x16x32_bf16 %3, %9, %6, %3\n\tds_read_b128 %4, %10 offset:7168"
      : "+v"(acc[6][3]), "=&v"(pm[5][0]), "=&v"(pm[5][1]), "+v"(acc[6][0]), "=&v"(pm[5][2]) : "a"(fw1[4][3]), "v"(p[2]), "v"(rr1), "v"(rr2), "a"(fw1[4][0]), "v"(rr3) : "memory"); SB();
  WAITVB(12);
  SB();
  asm volatile("v_mfma_f32_16x16x32_bf16 %0, %4, %5, %0\n\tbuffer_load_dwordx4 %6, %7, %8 offen offset:0  lds\n\tv_mfma_f32_16x16x32_bf16 %1, %9, %5, %1\n\tv_mfma_f32_16x16x32_bf16 %2, %10, %5, %2\n\tbuffer_load_dwordx4 %11, %7, %8 offen offset:1024  lds\n\tv_mfma_f32_16x16x32_bf16 %3, %12, %5, %3"
      : "+v"(acc[6][1]), "+v"(acc[6][2]), "+v"(acc[6][3]), "+v"(acc[6][0]) : "a"(fw1[5][1]), "v"(p[3]), "v"(vW2[0]), "s"(rs2), "s"(sw[0]), "a"(fw1[5][2]), "a"(fw1[5][3]), "v"(vW2[1]), "a"(fw1[5][0]) : "memory"); SB();
  WAITVB(13);
  SB();
  asm volatile("v_mfma_f32_16x16x32_bf16 %0, %1, %2, %0"
      : "+v"(acc[6][1]) : "a"(fw1[6][1]), "v"(p[4])); SB();
  WAITL(0);
  asm volatile("v_add_f32 %0, %9, %10\n\tv_mfma_f32_16x16x32_bf16 %1, %11, %12, %1\n\tv_add_f32 %2, %13, %14\n\tv_add_f32 %3, %15, %16\n\tv_mfma_f32_16x16x32_bf16 %4, %17, %12, %4\n\tv_add_f32 %5, %18, %19\n\tv_add_f32 %6, %20, %21\n\tv_mfma_f32_16x16x32_bf16 %7, %22, %12, %7\n\tv_add_f32 %8, %23, %24"
      : "=&v"(gm[0]), "+v"(acc[6][2]), "=&v"(gm[1]), "=&v"(gm[2]), "+v"(acc[6][3]), "=&v"(gm[3]), "=&v"(gz[0]), "+v"(acc[6][0]), "=&v"(gz[1]) : "v"(acc[5][0][0]), "v"(pm[5][0][0]), "a"(fw1[6][2]), "v"(p[4]), "v"(acc[5][0][1]), "v"(pm[5][0][1]), "v"(acc[5][0][2]), "v"(pm[5][0][2]), "a"(fw1[6][3]), "v"(acc[5][0][3]), "v"(pm[5][0][3]), "v"(pm[5][1][0]), "v"(pm[5][2][0]), "a"(fw1[6][0]), "v"(pm[5][1][1]), "v"(pm[5][2][1])); SB();
  WAITVB(12);
  SB();
  asm volatile("v_mfma_f32_16x16x32_bf16 %0, %11, %12, %0\n\tv_add_f32 %1, %13, %14\n\tv_add_f32 %2, %15, %16\n\tv_mfma_f32_16x16x32_bf16 %3, %17, %12, %3\n\tv_add_f32 %4, %4, %5\n\tv_add_f32 %6, %6, %18\n\tv_mfma_f32_16x16x32_bf16 %7, %19, %12, %7\n\tv_add_f32 %8, %8, %1\n\tv_add_f32 %9, %9, %2\n\tv_mfma_f32_16x16x32_bf16 %10, %20, %12, %10\n\tv_mul_f32 %5, %4, %4"
      : "+v"(acc[6][1]), "=&v"(gz[2]), "=&v"(gz[3]), "+v"(acc[6][2]), "+v"(gm[0]), "+v"(gz[0]), "+v"(gm[1]), "+v"(acc[6][3]), "+v"(gm[2]), "+v"(gm[3]), "+v"(acc[6][0]) : "a"(fw1[7][1]), "v"(p[5]), "v"(pm[5][1][2]), "v"(pm[5][2][2]), "v"(pm[5][1][3]), "v"(pm[5][2][3]), "a"(fw1[7][2]), "v"(gz[1]), "a"(fw1[7][3]), "a"(fw1[7][0])); SB();
  WAITVB(11);
  SB();
  asm volatile("v_mfma_f32_16x16x32_bf16 %0, %8, %9, %0\n\tv_mul_f32 %1, %10, %10\n\tv_mul_f32 %2, %11, %11\n\tv_mfma_f32_16x16x32_bf16 %3, %12, %9, %3\n\tv_mul_f32 %4, %13, %13\n\tv_fmaak_f32 %5, %14, %5, 0x40135761\n\tv_mfma_f32_16x16x32_bf16 %6, %15, %9, %6\n\tv_fmaak_f32 %1, %14, %1, 0x40135761\n\tv_fmaak_f32 %2, %14, %2, 0x40135761\n\tv_mfma_f32_16x16x32_bf16 %7, %16, %9, %7\n\tv_fmaak_f32 %4, %14, %4, 0x40135761"
      : "+v"(acc[6][1]), "=&v"(gz[1]), "=&v"(gz[2]), "+v"(acc[6][2]), "=&v"(gz[3]), "+v"(gz[0]), "+v"(acc[6][3]), "+v"(acc[6][0]) : "a"(fw1[8][1]), "v"(p[6]), "v"(gm[1]), "v"(gm[2]), "a"(fw1[8][2]), "v"(gm[3]), "v"(gc), "a"(fw1[8][3]), "a"(fw1[8][0])); SB();
  WAITVB(10);
  SB();
  asm volatile("v_mfma_f32_16x16x32_bf16 %0, %8, %9, %0\n\tv_mul_f32 %1, %1, %10\n\tv_mul_f32 %2, %2, %11\n\tv_mfma_f32_16x16x32_bf16 %3, %12, %9, %3\n\tv_mul_f32 %4, %4, %13\n\tv_mul_f32 %5, %5, %14\n\tv_mfma_f32_16x16x32_bf16 %6, %15, %9, %6\n\tv_exp_f32 %1, %1\n\tv_mfma_f32_16x16x32_bf16 %7, %16, %9, %7"
      : "+v"(acc[6][1]), "+v"(gz[0]), "+v"(gz[1]), "+v"(acc[6][2]), "+v"(gz[2]), "+v"(gz[3]), "+v"(acc[6][3]), "+v"(acc[6][0]) : "a"(fw1[9][1]), "v"(p[7]), "v"(gm[0]), "v"(gm[1]), "a"(fw1[9][2]), "v"(gm[2]), "v"(gm[3]), "a"(fw1[9][3]), "a"(fw1[9][0])); SB();
  WAITVB(9);
  SB();
  asm volatile("v_mfma_f32_16x16x32_bf16 %0, %8, %9, %0\n\tv_exp_f32 %1, %1\n\tv_mfma_f32_16x16x32_bf16 %2, %10, %9, %2\n\tv_exp_f32 %3, %3\n\tv_mfma_f32_16x16x32_bf16 %4, %11, %9, %4\n\tv_exp_f32 %5, %5\n\tv_mfma_f32_16x16x32_bf16 %6, %12, %9, %6\n\tv_add_f32 %7, 1.0, %7"
      : "+v"(acc[6][1]), "+v"(gz[1]), "+v"(acc[6][2]), "+v"(gz[2]), "+v"(acc[6][3]), "+v"(gz[3]), "+v"(acc[6][0]), "+v"(gz[0]) : "a"(fw1[10][1]), "v"(p[8]), "a"(fw1[10][2]), "a"(fw1[10][3]), "a"(fw1[10][0])); SB();
  WAITVB(8);
  SB();
  asm volatile("v_mfma_f32_16x16x32_bf16 %0, %8, %9, %0\n\tv_add_f32 %1, 1.0, %1\n\tv_add_f32 %2, 1.0, %2\n\tv_mfma_f32_16x16x32_bf16 %3, %10, %9, %3\n\tv_add_f32 %4, 1.0, %4\n\tv_mfma_f32_16x16x32_bf16 %5, %11, %9, %5\n\tv_rcp_f32 %6, %6\n\tv_mfma_f32_16x16x32_bf16 %7, %12, %9, %7"
      : "+v"(acc[6][1]), "+v"(gz[1]), "+v"(gz[2]), "+v"(acc[6][2]), "+v"(gz[3]), "+v"(acc[6][3]), "+v"(gz[0]), "+v"(acc[6][0]) : "a"(fw1[11][1]), "v"(p[9]), "a"(fw1[11][2]), "a"(fw1[11][3]), "a"(fw1[11][0])); SB();
  WAITVB(7);
  SB();
  asm volatile("v_mfma_f32_16x16x32_bf16 %0, %8, %9, %0\n\tv_rcp_f32 %1, %1\n\tv_mfma_f32_16x16x32_bf16 %2, %10, %9, %2\n\tv_rcp_f32 %3, %3\n\tv_mfma_f32_16x16x32_bf16 %4, %11, %9, %4\n\tv_rcp_f32 %5, %5\n\tv_mfma_f32_16x16x32_bf16 %6, %12, %9, %6\n\tv_fma_f32 %7, -%13, %7, %13"
      : "+v"(acc[6][1]), "+v"(gz[1]), "+v"(acc[6][2]), "+v"(gz[2]), "+v"(acc[6][3]), "+v"(gz[3]), "+v"(acc[6][0]), "+v"(gz[0]) : "a"(fw1[12][1]), "v"(p[10]), "a"(fw1[12][2]), "a"(fw1[12][3]), "a"(fw1[12][0]), "v"(gm[0])); SB();
  WAITVB(6);
  SB();
  asm volatile("v_mfma_f32_16x16x32_bf16 %0, %9, %10, %0\n\tv_fma_f32 %1, -%11, %1, %11\n\tv_fma_f32 %2, -%12, %2, %12\n\tv_mfma_f32_16x16x32_bf16 %3, %13, %10, %3\n\tv_fma_f32 %4, -%14, %4, %14\n\tv_cvt_pk_bf16_f32 %5, %15, %1\n\tv_mfma_f32_16x16x32_bf16 %6, %16, %10, %6\n\tv_cvt_pk_bf16_f32 %7, %2, %4\n\tv_mfma_f32_16x16x32_bf16 %8, %17, %10, %8"
      : "+v"(acc[6][1]), "+v"(gz[1]), "+v"(gz[2]), "+v"(acc[6][2]), "+v"(gz[3]), "=&v"(uv[5][0]), "+v"(acc[6][3]), "=&v"(uv[5][1]), "+v"(acc[6][0]) : "a"(fw1[13][1]), "v"(p[11]), "v"(gm[1]), "v"(gm[2]), "a"(fw1[13][2]), "v"(gm[3]), "v"(gz[0]), "a"(fw1[13][3]), "a"(fw1[13][0])); SB();
  WAITVB(5);
  SB();
  asm volatile("v_mfma_f32_16x16x32_bf16 %0, %5, %6, %0\n\tv_permlane16_swap_b32 %1, %2\n\tv_permlane16_swap_b32 %3, %4"
      : "+v"(acc[6][1]), "+v"(uv[4][0]), "+v"(uv[5][0]), "+v"(uv[4][1]), "+v"(uv[5][1]) : "a"(fw1[14][1]), "v"(p[12])); SB();
  HS16(vU, sU, 256, ((u32x4){uv[4][0], uv[4][1], uv[5][0], uv[5][1]}));
  SB();
  asm volatile("v_mfma_f32_16x16x32_bf16 %0, %3, %4, %0\n\tbuffer_load_dwordx4 %5, %6, %7 offen offset:2048  lds\n\tv_mfma_f32_16x16x32_bf16 %1, %8, %4, %1\n\tv_mfma_f32_16x16x32_bf16 %2, %9, %4, %2"
      : "+v"(acc[6][2]), "+v"(acc[6][3]), "+v"(acc[6][0]) : "a"(fw1[14][2]), "v"(p[12]), "v"(vW2[2]), "s"(rs2), "s"(sw[0]), "a"(fw1[14][3]), "a"(fw1[14][0]) : "memory"); SB();
  WAITVB(6);
  SB();
  asm volatile("v_mfma_f32_16x16x32_bf16 %0, %4, %5, %0\n\tbuffer_load_dwordx4 %6, %7, %8 offen offset:3072  lds\n\tv_mfma_f32_16x16x32_bf16 %1, %9, %5, %1\n\tv_mfma_f32_16x16x32_bf16 %2, %10, %5, %2\n\ts_add_u32 m0, %11, 4096\n\ts_nop 0\n\tv_mfma_f32_16x16x32_bf16 %3, %12, %5, %3"
      : "+v"(acc[6][1]), "+v"(acc[6][2]), "+v"(acc[6][3]), "+v"(acc[6][0]) : "v"(fw1[15][1]), "v"(p[13]), "v"(vW2[3]), "s"(rs2), "s"(sw[0]), "v"(fw1[15][2]), "v"(fw1[15][3]), "s"(ldsO), "v"(fw1[15][0]) : "memory", "scc"); SB();
  STAMP(8);
  asm volatile("s_nop 4\n\tds_write_b128 %0, %1 offset:1024\n\tds_write_b128 %0, %2 offset:2048\n\tds_write_b128 %0, %3 offset:3072\n\tbuffer_load_dwordx4 %4, %5, %6 offen offset:0  lds\n\tbuffer_load_dwordx4 %7, %5, %6 offen offset:1024  lds\n\tbuffer_load_dwordx4 %8, %5, %6 offen offset:2048  lds\n\tbuffer_load_dwordx4 %9, %5, %6 offen offset:3072  lds\n\ts_add_u32 m0, %10, 18432\n\ts_nop 0\n\tbuffer_load_dwordx4 %4, %5, %11 offen offset:0  lds\n\tbuffer_load_dwordx4 %7, %5, %11 offen offset:1024  lds\n\tbuffer_load_dwordx4 %8, %5, %11 offen offset:2048  lds\n\tbuffer_load_dwordx4 %9, %5, %11 offen offset:3072  lds\n\ts_waitcnt vmcnt(13)"
      :  : "v"(rw), "v"(acc[6][1]), "v"(acc[6][2]), "v"(acc[6][3]), "v"(vW2[0]), "s"(rs2), "s"(sw[1]), "v"(vW2[1]), "v"(vW2[2]), "v"(vW2[3]), "s"(ldsS), "s"(sw[2]) : "memory", "scc"); SB();
  STFU(sFU + 2048 * 4);
  BAR();
  asm volatile("ds_read_b128 %0, %3 offset:1024\n\tds_read_b128 %1, %4 offset:2048\n\tds_read_b128 %2, %5 offset:3072"
      : "=&v"(pm[6][0]), "=&v"(pm[6][1]), "=&v"(pm[6][2]) : "v"(rr1), "v"(rr2), "v"(rr3) : "memory"); SB();
  WAITL(0);
  asm volatile("v_add_f32 %0, %12, %13\n\tv_add_f32 %1, %14, %15\n\tv_add_f32 %2, %16, %17\n\tv_add_f32 %3, %18, %19\n\tv_add_f32 %4, %20, %21\n\tv_add_f32 %5, %22, %23\n\tv_add_f32 %6, %24, %25\n\tv_add_f32 %7, %26, %27\n\tv_add_f32 %0, %0, %4\n\tv_add_f32 %1, %1, %5\n\tv_add_f32 %2, %2, %6\n\tv_add_f32 %3, %3, %7\n\tv_mul_f32 %4, %0, %0\n\tv_mul_f32 %5, %1, %1\n\tv_mul_f32 %6, %2, %2\n\tv_mul_f32 %7, %3, %3\n\tv_fmaak_f32 %4, %28, %4, 0x40135761\n\tv_fmaak_f32 %5, %28, %5, 0x40135761\n\tv_fmaak_f32 %6, %28, %6, 0x40135761\n\tv_fmaak_f32 %7, %28, %7, 0x40135761\n\tv_mul_f32 %4, %4, %0\n\tv_mul_f32 %5, %5, %1\n\tv_mul_f32 %6, %6, %2\n\tv_mul_f32 %7, %7, %3\n\tv_exp_f32 %4, %4\n\tv_exp_f32 %5, %5\n\tv_exp_f32 %6, %6\n\tv_exp_f32 %7, %7\n\tv_add_f32 %4, 1.0, %4\n\tv_add_f32 %5, 1.0, %5\n\tv_add_f32 %6, 1.0, %6\n\tv_add_f32 %7, 1.0, %7\n\tv_rcp_f32 %4, %4\n\tv_rcp_f32 %5, %5\n\tv_rcp_f32 %6, %6\n\tv_rcp_f32 %7, %7\n\tv_fma_f32 %4, -%0, %4, %0\n\tv_fma_f32 %5, -%1, %5, %1\n\tv_fma_f32 %6, -%2, %6, %2\n\tv_fma_f32 %7, -%3, %7, %3\n\tv_cvt_pk_bf16_f32 %8, %4, %5\n\tv_cvt_pk_bf16_f32 %9, %6, %7\n\tv_cvt_pk_bf16_f32 %10, %4, %5\n\tv_cvt_pk_bf16_f32 %11, %6, %7\n\ts_nop 0\n\tv_permlane16_swap_b32 %8, %10\n\tv_permlane16_swap_b32 %9, %11"
      : "=&v"(gm[0]), "=&v"(gm[1]), "=&v"(gm[2]), "=&v"(gm[3]), "=&v"(gz[0]), "=&v"(gz[1]), "=&v"(gz[2]), "=&v"(gz[3]), "=&v"(uv[6][0]), "=&v"(uv[6][1]), "=&v"(uv[7][0]), "=&v"(uv[7][1]) : "v"(acc[6][0][0]), "v"(pm[6][0][0]), "v"(acc[6][0][1]), "v"(pm[6][0][1]), "v"(acc[6][0][2]), "v"(pm[6][0][2]), "v"(acc[6][0][3]), "v"(pm[6][0][3]), "v"(pm[6][1][0]), "v"(pm[6][2][0]), "v"(pm[6][1][1]), "v"(pm[6][2][1]), "v"(pm[6][1][2]), "v"(pm[6][2][2]), "v"(pm[6][1][3]), "v"(pm[6][2][3]), "v"(gc)); SB();
  HS16M(0xffff, 0xffff, vU6, sU, 0, ((u32x4){uv[6][0], uv[6][1], uv[7][0], uv[7][1]}));
  STAMP(10);
  asm volatile("s_add_u32 m0, %0, 34816\n\ts_nop 0\n\tbuffer_load_dwordx4 %1, %2, %3 offen offset:0  lds\n\tbuffer_load_dwordx4 %4, %2, %3 offen offset:1024  lds\n\tbuffer_load_dwordx4 %5, %2, %3 offen offset:2048  lds\n\tbuffer_load_dwordx4 %6, %2, %3 offen offset:3072  lds\n\ts_add_u32 m0, %0, 0\n\ts_nop 0\n\tbuffer_load_dwordx4 %1, %2, %7 offen offset:0  lds\n\tbuffer_load_dwordx4 %4, %2, %7 offen offset:1024  lds\n\tbuffer_load_dwordx4 %5, %2, %7 offen offset:2048  lds\n\tbuffer_load_dwordx4 %6, %2, %7 offen offset:3072  lds\n\ts_waitcnt vmcnt(18)"
      :  : "s"(ldsS), "v"(vW2[0]), "s"(rs2), "s"(sw[3]), "v"(vW2[1]), "v"(vW2[2]), "v"(vW2[3]), "s"(sw[4]) : "memory", "scc"); SB();
  fw2[0][0] = tr8u(tO[0] + 0, 512);
  fw2[0][1] = tr8u(tO[1] + 0, 512);
  fw2[0][2] = tr8u(tO[2] + 0, 512);
  fw2[0][3] = tr8u(tO[3] + 0, 512);
  WAITL(0);
  asm volatile("s_add_u32 m0, %0, 0\n\ts_nop 0\n\tbuffer_load_dwordx4 %1, %2, %3 offen offset:0  lds\n\tbuffer_load_dwordx4 %4, %2, %3 offen offset:1024  lds\n\tbuffer_load_dwordx4 %5, %2, %3 offen offset:2048  lds\n\tbuffer_load_dwordx4 %6, %2, %3 offen offset:3072  lds\n\ts_waitcnt vmcnt(18)"
      :  : "s"(ldsO), "v"(vW2[0]), "s"(rs2), "s"(sw[5]), "v"(vW2[1]), "v"(vW2[2]), "v"(vW2[3]) : "memory", "scc"); SB();
  fw2[1][0] = tr8u(tO[0] + 4096, 512);
  fw2[1][1] = tr8u(tO[1] + 4096, 512);
  fw2[1][2] = tr8u(tO[2] + 4096, 512);
  fw2[1][3] = tr8u(tO[3] + 4096, 512);
  asm volatile("s_waitcnt vmcnt(14)"
      :  :  : "memory"); SB();
  fw2[2][0] = tr8u(tS[0] + 18432, 512);
  fw2[2][1] = tr8u(tS[1] + 18432, 512);
  fw2[2][2] = tr8u(tS[2] + 18432, 512);
  fw2[2][3] = tr8u(tS[3] + 18432, 512);
  WAITV1(12, fq);
  STFU(sFU);
  STAMP(11);
  {
  if (__builtin_expect(__ballot(fq != nonce) != 0ull, 0)) poll1<4>(rsW, vFQ, nonce);
  unsigned fu;
  WAITL(8);
  asm volatile("s_add_u32 m0, %0, 4096\n\ts_nop 0\n\tbuffer_load_dwordx4 %1, %2, %3 offen offset:0  lds\n\tbuffer_load_dwordx4 %4, %2, %3 offen offset:1024  lds\n\tbuffer_load_dwordx4 %5, %2, %3 offen offset:2048  lds\n\tbuffer_load_dwordx4 %6, %2, %3 offen offset:3072  lds\n\ts_waitcnt vmcnt(13)"
      :  : "s"(ldsO), "v"(vW2[0]), "s"(rs2), "s"(sw[6]), "v"(vW2[1]), "v"(vW2[2]), "v"(vW2[3]) : "memory", "scc"); SB();
  fw2[3][0] = tr8u(tS[0] + 34816, 512);
  fw2[3][1] = tr8u(tS[1] + 34816, 512);
  fw2[3][2] = tr8u(tS[2] + 34816, 512);
  fw2[3][3] = tr8u(tS[3] + 34816, 512);
  WAITL(8);
  asm volatile("s_add_u32 m0, %0, 18432\n\ts_nop 0\n\tbuffer_load_dwordx4 %1, %2, %3 offen offset:0  lds\n\tbuffer_load_dwordx4 %4, %2, %3 offen offset:1024  lds\n\tbuffer_load_dwordx4 %5, %2, %3 offen offset:2048  lds\n\tbuffer_load_dwordx4 %6, %2, %3 offen offset:3072  lds\n\ts_waitcnt vmcnt(13)"
      :  : "s"(ldsS), "v"(vW2[0]), "s"(rs2), "s"(sw[7]), "v"(vW2[1]), "v"(vW2[2]), "v"(vW2[3]) : "memory", "scc"); SB();
  fw2[4][0] = tr8u(tS[0] + 0, 512);
  fw2[4][1] = tr8u(tS[1] + 0, 512);
  fw2[4][2] = tr8u(tS[2] + 0, 512);
  fw2[4][3] = tr8u(tS[3] + 0, 512);
  WAITL(8);
  asm volatile("s_add_u32 m0, %0, 34816\n\ts_nop 0\n\tbuffer_load_dwordx4 %1, %2, %3 offen offset:0  lds\n\tbuffer_load_dwordx4 %4, %2, %3 offen offset:1024  lds\n\tbuffer_load_dwordx4 %5, %2, %3 offen offset:2048  lds\n\tbuffer_load_dwordx4 %6, %2, %3 offen offset:3072  lds\n\ts_waitcnt vmcnt(13)"
      :  : "s"(ldsS), "v"(vW2[0]), "s"(rs2), "s"(sw[8]), "v"(vW2[1]), "v"(vW2[2]), "v"(vW2[3]) : "memory", "scc"); SB();
  fw2[5][0] = tr8u(tO[0] + 0, 512);
  fw2[5][1] = tr8u(tO[1] + 0, 512);
  fw2[5][2] = tr8u(tO[2] + 0, 512);
  fw2[5][3] = tr8u(tO[3] + 0, 512);
  WAITL(8);
  asm volatile("s_add_u32 m0, %0, 0\n\ts_nop 0\n\tbuffer_load_dwordx4 %1, %2, %3 offen offset:0  lds\n\tbuffer_load_dwordx4 %4, %2, %3 offen offset:1024  lds\n\tbuffer_load_dwordx4 %5, %2, %3 offen offset:2048  lds\n\tbuffer_load_dwordx4 %6, %2, %3 offen offset:3072  lds\n\ts_waitcnt vmcnt(12)"
      :  : "s"(ldsS), "v"(vW2[0]), "s"(rs2), "s"(sw[9]), "v"(vW2[1]), "v"(vW2[2]), "v"(vW2[3]) : "memory", "scc"); SB();
  fw2[6][0] = tr8u(tO[0] + 4096, 512);
  fw2[6][1] = tr8u(tO[1] + 4096, 512);
  fw2[6][2] = tr8u(tO[2] + 4096, 512);
  fw2[6][3] = tr8u(tO[3] + 4096, 512);
  WAITL(8);
  asm volatile("s_add_u32 m0, %0, 0\n\ts_nop 0\n\tbuffer_load_dwordx4 %1, %2, %3 offen offset:0  lds\n\tbuffer_load_dwordx4 %4, %2, %3 offen offset:1024  lds\n\tbuffer_load_dwordx4 %5, %2, %3 offen offset:2048  lds\n\tbuffer_load_dwordx4 %6, %2, %3 offen offset:3072  lds\n\ts_waitcnt vmcnt(12)"
      :  : "s"(ldsO), "v"(vW2[0]), "s"(rs2), "s"(sw[10]), "v"(vW2[1]), "v"(vW2[2]), "v"(vW2[3]) : "memory", "scc"); SB();
  fw2[7][0] = tr8u(tS[0] + 18432, 512);
  fw2[7][1] = tr8u(tS[1] + 18432, 512);
  fw2[7][2] = tr8u(tS[2] + 18432, 512);
  fw2[7][3] = tr8u(tS[3] + 18432, 512);
  WAITL(0);
  asm volatile("s_add_u32 m0, %0, 18432\n\ts_nop 0\n\tbuffer_load_dwordx4 %1, %2, %3 offen offset:0  lds\n\tbuffer_load_dwordx4 %4, %2, %3 offen offset:1024  lds\n\tbuffer_load_dwordx4 %5, %2, %3 offen offset:2048  lds\n\tbuffer_load_dwordx4 %6, %2, %3 offen offset:3072  lds\n\ts_waitcnt vmcnt(12)"
      :  : "s"(ldsS), "v"(vW2[0]), "s"(rs2), "s"(sw[11]), "v"(vW2[1]), "v"(vW2[2]), "v"(vW2[3]) : "memory", "scc"); SB();
  fw2[8][0] = tr8u(tS[0] + 34816, 512);
  fw2[8][1] = tr8u(tS[1] + 34816, 512);
  fw2[8][2] = tr8u(tS[2] + 34816, 512);
  fw2[8][3] = tr8u(tS[3] + 34816, 512);
  WAITL(0);
  asm volatile("s_add_u32 m0, %0, 34816\n\ts_nop 0\n\tbuffer_load_dwordx4 %1, %2, %3 offen offset:0  lds\n\tbuffer_load_dwordx4 %4, %2, %3 offen offset:1024  lds\n\tbuffer_load_dwordx4 %5, %2, %3 offen offset:2048  lds\n\tbuffer_load_dwordx4 %6, %2, %3 offen offset:3072  lds\n\ts_waitcnt vmcnt(12)"
      :  : "s"(ldsS), "v"(vW2[0]), "s"(rs2), "s"(sw[12]), "v"(vW2[1]), "v"(vW2[2]), "v"(vW2[3]) : "memory", "scc"); SB();
  fw2[9][0] = tr8u(tS[0] + 0, 512);
  fw2[9][1] = tr8u(tS[1] + 0, 512);
  fw2[9][2] = tr8u(tS[2] + 0, 512);
  fw2[9][3] = tr8u(tS[3] + 0, 512);
  WAITL(0);
  asm volatile("s_add_u32 m0, %1, 0\n\ts_nop 0\n\tbuffer_load_dwordx4 %2, %3, %4 offen offset:0  lds\n\tbuffer_load_dwordx4 %5, %3, %4 offen offset:1024  lds\n\tbuffer_load_dwordx4 %6, %3, %4 offen offset:2048  lds\n\tbuffer_load_dwordx4 %7, %3, %4 offen offset:3072  lds\n\ts_add_u32 m0, %8, 8192\n\ts_add_u32 %0, %9, 8192"
      : "=&s"(sWt) : "s"(ldsS), "v"(vW2[0]), "s"(rs2), "s"(sw[13]), "v"(vW2[1]), "v"(vW2[2]), "v"(vW2[3]), "s"(ldsP), "s"(sWo) : "memory", "scc"); SB();
  DMAW4(vWo, rsW, sWt);
  asm volatile("s_waitcnt vmcnt(16)"
      :  :  : "memory"); SB();
  fw2[10][0] = tr8u(tO[0] + 0, 512);
  fw2[10][1] = tr8u(tO[1] + 0, 512);
  fw2[10][2] = tr8u(tO[2] + 0, 512);
  fw2[10][3] = tr8u(tO[3] + 0, 512);
  asm volatile("buffer_load_dword %0, %2, %3, 0 offen sc1\n\ts_add_u32 m0, %4, 12288\n\ts_add_u32 %1, %5, 12288"
      : "=&v"(fu), "=&s"(sWt) : "v"(vFU), "s"(rsW), "s"(ldsP), "s"(sWo) : "memory", "scc"); SB();
  DMAW2(vWo, rsW, sWt);
  asm volatile("s_waitcnt vmcnt(15)"
      :  :  : "memory"); SB();
  fw2[11][0] = tr8u(tS[0] + 18432, 512);
  fw2[11][1] = tr8u(tS[1] + 18432, 512);
  fw2[11][2] = tr8u(tS[2] + 18432, 512);
  fw2[11][3] = tr8u(tS[3] + 18432, 512);
  WAITL(8);
  asm volatile("s_add_u32 m0, %1, 0\n\ts_add_u32 %0, %2, 0"
      : "=&s"(sWt) : "s"(ldsO), "s"(sWo) : "memory", "scc"); SB();
  DMAW4(vWo, rsW, sWt);
  asm volatile("s_waitcnt vmcnt(15)"
      :  :  : "memory"); SB();
  fw2[12][0] = tr8u(tS[0] + 34816, 512);
  fw2[12][1] = tr8u(tS[1] + 34816, 512);
  fw2[12][2] = tr8u(tS[2] + 34816, 512);
  fw2[12][3] = tr8u(tS[3] + 34816, 512);
  asm volatile("s_add_u32 m0, %1, 4096\n\ts_add_u32 %0, %2, 4096"
      : "=&s"(sWt) : "s"(ldsO), "s"(sWo) : "memory", "scc"); SB();
  DMAW4(vWo, rsW, sWt);
  u32x4 rsX = srd(x, 2048u * 896u); OPQ(rsX);
  WAITL(0);
  asm volatile("s_add_u32 m0, %1, 34816\n\ts_nop 0\n\tbuffer_load_dwordx4 %2, %3, %4 offen offset:0  lds\n\tbuffer_load_dwordx4 %5, %3, %4 offen offset:1024  lds\n\tglobal_load_ushort %0, %6, %7 offset:0"
      : "=&v"(braw) : "s"(ldsS), "v"(vXa), "s"(rsX), "s"(0u), "v"(vXb), "v"(vB2), "s"(b2) : "memory", "scc"); SB();
  STAMP(16);
  bf16x8 pu[14], fwo[56];
  f32x4 ac[2][4], as[4], py[4][4];
  float ye[4], ze[4], ge[16], xe[16], bias;
  u32x2 ov[4]; unsigned fo;
  u32x2 xv[4]; unsigned xta[4];
  WAITV1(13, fu);
  if (__ballot(fu != nonce) != 0ull) poll1<4>(rsW, vFU, nonce);
  STAMP(17);
  asm volatile("buffer_load_dwordx4 %0, %10, %11, %12 offen offset:0 sc1\n\tbuffer_load_dwordx4 %1, %10, %11, %13 offen offset:0 sc1\n\tbuffer_load_dwordx4 %2, %10, %11, %14 offen offset:0 sc1\n\tbuffer_load_dwordx4 %3, %10, %11, %15 offen offset:0 sc1\n\tbuffer_load_dwordx4 %4, %10, %11, %16 offen offset:0 sc1\n\tbuffer_load_dwordx4 %5, %10, %11, %17 offen offset:0 sc1\n\tbuffer_load_dwordx4 %6, %10, %11, %18 offen offset:0 sc1\n\tbuffer_load_dwordx4 %7, %10, %11, %19 offen offset:0 sc1\n\tbuffer_load_dwordx4 %8, %10, %11, %20 offen offset:0 sc1\n\tbuffer_load_dwordx4 %9, %10, %11, %21 offen offset:0 sc1"
      : "=&v"(pu[0]), "=&v"(pu[1]), "=&v"(pu[2]), "=&v"(pu[3]), "=&v"(pu[4]), "=&v"(pu[5]), "=&v"(pu[6]), "=&v"(pu[7]), "=&v"(pu[8]), "=&v"(pu[9]) : "v"(vFj[0]), "s"(rsW), "s"(su[0]), "s"(su[1]), "s"(su[2]), "s"(su[3]), "s"(su[4]), "s"(su[5]), "s"(su[6]), "s"(su[7]), "s"(su[8]), "s"(su[9]) : "memory"); SB();
  STAMP(39);
  WAITVB(9);
  SB();
  asm volatile("v_mfma_f32_16x16x32_bf16 %0, %6, %7, 0\n\tbuffer_load_dwordx4 %1, %8, %9, %10 offen offset:0 sc1\n\tv_mfma_f32_16x16x32_bf16 %2, %6, %11, 0\n\ts_waitcnt vmcnt(29)\n\tv_mfma_f32_16x16x32_bf16 %3, %6, %12, 0\n\tbuffer_load_dwordx4 %4, %8, %9, %13 offen offset:0 sc1\n\tv_mfma_f32_16x16x32_bf16 %5, %6, %14, 0"
      : "=&v"(ac[0][0]), "=&v"(pu[10]), "=&v"(ac[0][1]), "=&v"(ac[0][2]), "=&v"(pu[11]), "=&v"(ac[0][3]) : "v"(pu[0]), "a"(fw2[0][0]), "v"(vFj[0]), "s"(rsW), "s"(su[10]), "a"(fw2[0][1]), "a"(fw2[0][2]), "s"(su[11]), "a"(fw2[0][3]) : "memory"); SB();
  WAITVB(10);
  SB();
  asm volatile("v_mfma_f32_16x16x32_bf16 %0, %3, %4, %0\n\tbuffer_load_dwordx4 %1, %5, %6, %7 offen offset:0 sc1\n\tv_mfma_f32_16x16x32_bf16 %2, %3, %8, %2"
      : "+v"(ac[0][0]), "=&v"(pu[12]), "+v"(ac[0][1]) : "v"(pu[1]), "a"(fw2[1][0]), "v"(vFj[0]), "s"(rsW), "s"(su[12]), "a"(fw2[1][1]) : "memory"); SB();
  fw2[13][0] = tr8u(tS[0] + 0, 512);
  SB();
  asm volatile("v_mfma_f32_16x16x32_bf16 %0, %3, %4, %0\n\tbuffer_load_dwordx4 %1, %5, %6, %7 offen offset:0 sc1\n\tv_mfma_f32_16x16x32_bf16 %2, %3, %8, %2"
      : "+v"(ac[0][2]), "=&v"(pu[13]), "+v"(ac[0][3]) : "v"(pu[1]), "a"(fw2[1][2]), "v"(vFj[0]), "s"(rsW), "s"(su[13]), "a"(fw2[1][3]) : "memory"); SB();
  WAITVB(11);
  SB();
  asm volatile("v_mfma_f32_16x16x32_bf16 %0, %3, %4, %0\n\tbuffer_load_dwordx4 %1, %5, %6, %7 offen offset:0 sc1\n\tv_mfma_f32_16x16x32_bf16 %2, %3, %8, %2"
      : "+v"(ac[0][0]), "=&v"(pu[0]), "+v"(ac[0][1]) : "v"(pu[2]), "a"(fw2[2][0]), "v"(vFj[1]), "s"(rsW), "s"(su[0]), "a"(fw2[2][1]) : "memory"); SB();
  fw2[13][1] = tr8u(tS[1] + 0, 512);
  SB();
  asm volatile("v_mfma_f32_16x16x32_bf16 %0, %3, %4, %0\n\tbuffer_load_dwordx4 %1, %5, %6, %7 offen offset:0 sc1\n\tv_mfma_f32_16x16x32_bf16 %2, %3, %8, %2"
      : "+v"(ac[0][2]), "=&v"(pu[1]), "+v"(ac[0][3]) : "v"(pu[2]), "a"(fw2[2][2]), "v"(vFj[1]), "s"(rsW), "s"(su[1]), "a"(fw2[2][3]) : "memory"); SB();
  fw2[13][2] = tr8u(tS[2] + 0, 512);
  fw2[13][3] = tr8u(tS[3] + 0, 512);
  WAITVB(16);
  SB();
  WAITVB(12);
  SB();
  asm volatile("v_mfma_f32_16x16x32_bf16 %0, %10, %11, %0\n\tbuffer_load_dwordx4 %1, %12, %13, %14 offen offset:0 sc1\n\tv_mfma_f32_16x16x32_bf16 %2, %10, %15, %2\n\tv_lshlrev_b32 %3, 16, %16\n\tv_xor_b32 %4, 0, %17\n\tv_mfma_f32_16x16x32_bf16 %5, %10, %18, %5\n\tv_xor_b32 %6, 32, %17\n\tv_xor_b32 %7, 64, %19\n\tv_mfma_f32_16x16x32_bf16 %8, %10, %20, %8\n\tds_read_b64 %9, %4 offset:0"
      : "+v"(ac[0][0]), "=&v"(pu[2]), "+v"(ac[0][1]), "=&v"(bias), "=&v"(xta[0]), "+v"(ac[0][2]), "=&v"(xta[1]), "=&v"(xta[2]), "+v"(ac[0][3]), "=&v"(xv[0]) : "v"(pu[3]), "a"(fw2[3][0]), "v"(vFj[1]), "s"(rsW), "s"(su[2]), "a"(fw2[3][1]), "v"(braw), "v"(xs), "a"(fw2[3][2]), "v"(xt), "a"(fw2[3][3]) : "memory"); SB();
  WAITVB(12);
  SB();
  asm volatile("v_mfma_f32_16x16x32_bf16 %0, %7, %8, %0\n\tbuffer_load_dwordx4 %1, %9, %10, %11 offen offset:0 sc1\n\tv_mfma_f32_16x16x32_bf16 %2, %7, %12, %2\n\tds_read_b64 %3, %13 offset:0\n\tds_read_b64 %4, %14 offset:0\n\tv_mfma_f32_16x16x32_bf16 %5, %7, %15, %5\n\tv_mfma_f32_16x16x32_bf16 %6, %7, %16, %6"
      : "+v"(ac[0][0]), "=&v"(pu[3]), "+v"(ac[0][1]), "=&v"(xv[1]), "=&v"(xv[2]), "+v"(ac[0][2]), "+v"(ac[0][3]) : "v"(pu[4]), "a"(fw2[4][0]), "v"(vFj[1]), "s"(rsW), "s"(su[3]), "a"(fw2[4][1]), "v"(xta[1]), "v"(xta[2]), "a"(fw2[4][2]), "a"(fw2[4][3]) : "memory"); SB();
  WAITVB(12);
  SB();
  asm volatile("v_mfma_f32_16x16x32_bf16 %0, %4, %5, %0\n\tbuffer_load_dwordx4 %1, %6, %7, %8 offen offset:0 sc1\n\tv_mfma_f32_16x16x32_bf16 %2, %4, %9, %2\n\tv_mfma_f32_16x16x32_bf16 %3, %4, %10, %3"
      : "+v"(ac[0][0]), "=&v"(pu[4]), "+v"(ac[0][1]), "+v"(ac[0][2]) : "v"(pu[5]), "a"(fw2[5][0]), "v"(vFj[1]), "s"(rsW), "s"(su[4]), "a"(fw2[5][1]), "a"(fw2[5][2]) : "memory"); SB();
  WAITL(0);
  asm volatile("v_lshlrev_b32 %0, 16, %3\n\tv_mfma_f32_16x16x32_bf16 %1, %4, %5, %1\n\tv_and_b32 %2, 0xffff0000, %3"
      : "=&v"(xe[0]), "+v"(ac[0][3]), "=&v"(xe[1]) : "v"(xv[0][0]), "v"(pu[5]), "a"(fw2[5][3])); SB();
  WAITVB(12);
  SB();
  asm volatile("v_mfma_f32_16x16x32_bf16 %0, %10, %11, %0\n\tbuffer_load_dwordx4 %1, %12, %13, %14 offen offset:0 sc1\n\tv_mfma_f32_16x16x32_bf16 %2, %10, %15, %2\n\tv_lshlrev_b32 %3, 16, %16\n\tv_and_b32 %4, 0xffff0000, %16\n\tv_mfma_f32_16x16x32_bf16 %5, %10, %17, %5\n\tv_lshlrev_b32 %6, 16, %18\n\tv_and_b32 %7, 0xffff0000, %18\n\tv_mfma_f32_16x16x32_bf16 %8, %10, %19, %8\n\tv_lshlrev_b32 %9, 16, %20"
      : "+v"(ac[0][0]), "=&v"(pu[5]), "+v"(ac[0][1]), "=&v"(xe[2]), "=&v"(xe[3]), "+v"(ac[0][2]), "=&v"(xe[4]), "=&v"(xe[5]), "+v"(ac[0][3]), "=&v"(xe[6]) : "v"(pu[6]), "a"(fw2[6][0]), "v"(vFj[1]), "s"(rsW), "s"(su[5]), "a"(fw2[6][1]), "v"(xv[0][1]), "a"(fw2[6][2]), "v"(xv[1][0]), "a"(fw2[6][3]), "v"(xv[1][1]) : "memory"); SB();
  WAITVB(12);
  SB();
  asm volatile("v_mfma_f32_16x16x32_bf16 %0, %10, %11, %0\n\tbuffer_load_dwordx4 %1, %12, %13, %14 offen offset:0 sc1\n\tv_mfma_f32_16x16x32_bf16 %2, %10, %15, %2\n\tv_and_b32 %3, 0xffff0000, %16\n\tv_lshlrev_b32 %4, 16, %17\n\tv_mfma_f32_16x16x32_bf16 %5, %10, %18, %5\n\tv_and_b32 %6, 0xffff0000, %17\n\tv_lshlrev_b32 %7, 16, %19\n\tv_mfma_f32_16x16x32_bf16 %8, %10, %20, %8\n\tv_and_b32 %9, 0xffff0000, %19"
      : "+v"(ac[0][0]), "=&v"(pu[6]), "+v"(ac[0][1]), "=&v"(xe[7]), "=&v"(xe[8]), "+v"(ac[0][2]), "=&v"(xe[9]), "=&v"(xe[10]), "+v"(ac[0][3]), "=&v"(xe[11]) : "v"(pu[7]), "a"(fw2[7][0]), "v"(vFj[1]), "s"(rsW), "s"(su[6]), "a"(fw2[7][1]), "v"(xv[1][1]), "v"(xv[2][0]), "a"(fw2[7][2]), "v"(xv[2][1]), "a"(fw2[7][3]) : "memory"); SB();
  WAITVB(12);
  SB();
  asm volatile("v_mfma_f32_16x16x32_bf16 %0, %5, %6, %0\n\tbuffer_load_dwordx4 %1, %7, %8, %9 offen offset:0 sc1\n\tv_mfma_f32_16x16x32_bf16 %2, %5, %10, %2\n\tv_mfma_f32_16x16x32_bf16 %3, %5, %11, %3\n\tv_mfma_f32_16x16x32_bf16 %4, %5, %12, %4"
      : "+v"(ac[0][0]), "=&v"(pu[7]), "+v"(ac[0][1]), "+v"(ac[0][2]), "+v"(ac[0][3]) : "v"(pu[8]), "a"(fw2[8][0]), "v"(vFj[1]), "s"(rsW), "s"(su[7]), "a"(fw2[8][1]), "a"(fw2[8][2]), "a"(fw2[8][3]) : "memory"); SB();
  WAITVB(12);
  SB();
  asm volatile("v_mfma_f32_16x16x32_bf16 %0, %5, %6, %0\n\tbuffer_load_dwordx4 %1, %7, %8, %9 offen offset:0 sc1\n\tv_mfma_f32_16x16x32_bf16 %2, %5, %10, %2\n\tv_mfma_f32_16x16x32_bf16 %3, %5, %11, %3\n\tv_mfma_f32_16x16x32_bf16 %4, %5, %12, %4"
      : "+v"(ac[0][0]), "=&v"(pu[8]), "+v"(ac[0][1]), "+v"(ac[0][2]), "+v"(ac[0][3]) : "v"(pu[9]), "v"(fw2[9][0]), "v"(vFj[1]), "s"(rsW), "s"(su[8]), "v"(fw2[9][1]), "v"(fw2[9][2]), "v"(fw2[9][3]) : "memory"); SB();
  WAITVB(12);
  SB();
  asm volatile("v_mfma_f32_16x16x32_bf16 %0, %5, %6, %0\n\tbuffer_load_dwordx4 %1, %7, %8, %9 offen offset:0 sc1\n\tv_mfma_f32_16x16x32_bf16 %2, %5, %10, %2\n\tv_mfma_f32_16x16x32_bf16 %3, %5, %11, %3\n\tv_mfma_f32_16x16x32_bf16 %4, %5, %12, %4"
      : "+v"(ac[0][0]), "=&v"(pu[9]), "+v"(ac[0][1]), "+v"(ac[0][2]), "+v"(ac[0][3]) : "v"(pu[10]), "v"(fw2[10][0]), "v"(vFj[1]), "s"(rsW), "s"(su[9]), "v"(fw2[10][1]), "v"(fw2[10][2]), "v"(fw2[10][3]) : "memory"); SB();
  WAITVB(12);
  SB();
  asm volatile("v_mfma_f32_16x16x32_bf16 %0, %5, %6, %0\n\tbuffer_load_dwordx4 %1, %7, %8, %9 offen offset:0 sc1\n\tv_mfma_f32_16x16x32_bf16 %2, %5, %10, %2\n\tv_mfma_f32_16x16x32_bf16 %3, %5, %11, %3\n\tv_mfma_f32_16x16x32_bf16 %4, %5, %12, %4"
      : "+v"(ac[0][0]), "=&v"(pu[10]), "+v"(ac[0][1]), "+v"(ac[0][2]), "+v"(ac[0][3]) : "v"(pu[11]), "v"(fw2[11][0]), "v"(vFj[1]), "s"(rsW), "s"(su[10]), "v"(fw2[11][1]), "v"(fw2[11][2]), "v"(fw2[11][3]) : "memory"); SB();
  WAITVB(12);
  SB();
  asm volatile("v_mfma_f32_16x16x32_bf16 %0, %4, %5, %0\n\tbuffer_load_dwordx4 %1, %6, %7, %8 offen offset:0 sc1\n\tv_mfma_f32_16x16x32_bf16 %2, %4, %9, %2\n\tv_mfma_f32_16x16x32_bf16 %3, %4, %10, %3"
      : "+v"(ac[0][0]), "=&v"(pu[11]), "+v"(ac[0][1]), "+v"(ac[0][2]) : "v"(pu[12]), "v"(fw2[12][0]), "v"(vFj[1]), "s"(rsW), "s"(su[11]), "v"(fw2[12][1]), "v"(fw2[12][2]) : "memory"); SB();
  u32x4 rsO_ = srd(out, 2048u * 896u); OPQ(rsO_);
  SB();
  asm volatile("v_mfma_f32_16x16x32_bf16 %0, %1, %2, %0"
      : "+v"(ac[0][3]) : "v"(pu[12]), "v"(fw2[12][3])); SB();
  WAITVB(12);
  SB();
  asm volatile("v_mfma_f32_16x16x32_bf16 %0, %5, %6, %0\n\tbuffer_load_dwordx4 %1, %7, %8, %9 offen offset:0 sc1\n\tv_mfma_f32_16x16x32_bf16 %2, %5, %10, %2\n\tv_mfma_f32_16x16x32_bf16 %3, %5, %11, %3\n\tv_mfma_f32_16x16x32_bf16 %4, %5, %12, %4"
      : "+v"(ac[0][0]), "=&v"(pu[12]), "+v"(ac[0][1]), "+v"(ac[0][2]), "+v"(ac[0][3]) : "v"(pu[13]), "v"(fw2[13][0]), "v"(vFj[1]), "s"(rsW), "s"(su[12]), "v"(fw2[13][1]), "v"(fw2[13][2]), "v"(fw2[13][3]) : "memory"); SB();
  STAMP(40);
  WAITVB(12);
  SB();
  asm volatile("v_mfma_f32_16x16x32_bf16 %0, %6, %7, 0\n\tbuffer_load_dwordx4 %1, %8, %9, %10 offen offset:0 sc1\n\tv_mfma_f32_16x16x32_bf16 %2, %6, %11, 0\n\tbuffer_load_dword %3, %12, %9, 0 offen sc1\n\tv_mfma_f32_16x16x32_bf16 %4, %6, %13, 0\n\tv_mfma_f32_16x16x32_bf16 %5, %6, %14, 0"
      : "=&v"(ac[1][0]), "=&v"(pu[13]), "=&v"(ac[1][1]), "=&v"(fo), "=&v"(ac[1][2]), "=&v"(ac[1][3]) : "v"(pu[0]), "a"(fw2[0][0]), "v"(vFj[1]), "s"(rsW), "s"(su[13]), "a"(fw2[0][1]), "v"(vFO), "a"(fw2[0][2]), "a"(fw2[0][3]) : "memory"); SB();
  WAITVB(13);
  SB();
  asm volatile("v_mfma_f32_16x16x32_bf16 %0, %5, %6, %0\n\tbuffer_load_dwordx4 %1, %7, %8, %9 offen offset:0 sc1\n\tv_mfma_f32_16x16x32_bf16 %2, %5, %10, %2\n\tds_write_b128 %11, %12 offset:0\n\tv_mfma_f32_16x16x32_bf16 %3, %5, %13, %3\n\tv_mfma_f32_16x16x32_bf16 %4, %5, %14, %4"
      : "+v"(ac[1][0]), "=&v"(pu[0]), "+v"(ac[1][1]), "+v"(ac[1][2]), "+v"(ac[1][3]) : "v"(pu[1]), "a"(fw2[1][0]), "v"(vFj[2]), "s"(rsW), "s"(su[0]), "a"(fw2[1][1]), "v"(rwd), "v"(ac[0][0]), "a"(fw2[1][2]), "a"(fw2[1][3]) : "memory"); SB();
  WAITVB(13);
  SB();
  asm volatile("v_mfma_f32_16x16x32_bf16 %0, %5, %6, %0\n\tbuffer_load_dwordx4 %1, %7, %8, %9 offen offset:0 sc1\n\tv_mfma_f32_16x16x32_bf16 %2, %5, %10, %2\n\tds_write_b128 %11, %12 offset:1024\n\tv_mfma_f32_16x16x32_bf16 %3, %5, %13, %3\n\tv_mfma_f32_16x16x32_bf16 %4, %5, %14, %4"
      : "+v"(ac[1][0]), "=&v"(pu[1]), "+v"(ac[1][1]), "+v"(ac[1][2]), "+v"(ac[1][3]) : "v"(pu[2]), "a"(fw2[2][0]), "v"(vFj[2]), "s"(rsW), "s"(su[1]), "a"(fw2[2][1]), "v"(rwd), "v"(ac[0][1]), "a"(fw2[2][2]), "a"(fw2[2][3]) : "memory"); SB();
  WAITVB(13);
  SB();
  asm volatile("v_mfma_f32_16x16x32_bf16 %0, %5, %6, %0\n\tbuffer_load_dwordx4 %1, %7, %8, %9 offen offset:0 sc1\n\tv_mfma_f32_16x16x32_bf16 %2, %5, %10, %2\n\tds_write_b128 %11, %12 offset:2048\n\tv_mfma_f32_16x16x32_bf16 %3, %5, %13, %3\n\tv_mfma_f32_16x16x32_bf16 %4, %5, %14, %4"
      : "+v"(ac[1][0]), "=&v"(pu[2]), "+v"(ac[1][1]), "+v"(ac[1][2]), "+v"(ac[1][3]) : "v"(pu[3]), "a"(fw2[3][0]), "v"(vFj[2]), "s"(rsW), "s"(su[2]), "a"(fw2[3][1]), "v"(rwd), "v"(ac[0][2]), "a"(fw2[3][2]), "a"(fw2[3][3]) : "memory"); SB();
  WAITVB(13);
  SB();
  asm volatile("v_mfma_f32_16x16x32_bf16 %0, %5, %6, %0\n\tbuffer_load_dwordx4 %1, %7, %8, %9 offen offset:0 sc1\n\tv_mfma_f32_16x16x32_bf16 %2, %5, %10, %2\n\tds_write_b128 %11, %12 offset:3072\n\tv_mfma_f32_16x16x32_bf16 %3, %5, %13, %3\n\tv_mfma_f32_16x16x32_bf16 %4, %5, %14, %4"
      : "+v"(ac[1][0]), "=&v"(pu[3]), "+v"(ac[1][1]), "+v"(ac[1][2]), "+v"(ac[1][3]) : "v"(pu[4]), "a"(fw2[4][0]), "v"(vFj[2]), "s"(rsW), "s"(su[3]), "a"(fw2[4][1]), "v"(rwd), "v"(ac[0][3]), "a"(fw2[4][2]), "a"(fw2[4][3]) : "memory"); SB();
  WAITVB(13);
  SB();
  asm volatile("v_mfma_f32_16x16x32_bf16 %0, %3, %4, %0\n\tbuffer_load_dwordx4 %1, %5, %6, %7 offen offset:0 sc1\n\tv_mfma_f32_16x16x32_bf16 %2, %3, %8, %2\n\ts_waitcnt vmcnt(37)"
      : "+v"(ac[1][0]), "=&v"(pu[4]), "+v"(ac[1][1]) : "v"(pu[5]), "a"(fw2[5][0]), "v"(vFj[2]), "s"(rsW), "s"(su[4]), "a"(fw2[5][1]) : "memory"); SB();
  WAITL(0);
  BAR0();
  SB();
  asm volatile("v_mfma_f32_16x16x32_bf16 %0, %5, %6, %0\n\tds_read_b128 %1, %7 offset:0\n\tds_read_b128 %2, %7 offset:4096\n\tv_mfma_f32_16x16x32_bf16 %3, %5, %8, %3\n\tds_read_b128 %4, %7 offset:8192"
      : "+v"(ac[1][2]), "=&v"(py[0][0]), "=&v"(py[0][1]), "+v"(ac[1][3]), "=&v"(py[0][2]) : "v"(pu[5]), "a"(fw2[5][2]), "v"(rr), "a"(fw2[5][3]) : "memory"); SB();
  WAITVB(13);
  SB();
  asm volatile("v_mfma_f32_16x16x32_bf16 %0, %8, %9, %0\n\tbuffer_load_dwordx4 %1, %10, %11, %12 offen offset:0 sc1\n\tv_mfma_f32_16x16x32_bf16 %2, %8, %13, %2\n\tds_read_b128 %3, %14 offset:12288\n\tds_read_b128 %4, %15 offset:0\n\tv_mfma_f32_16x16x32_bf16 %5, %8, %16, %5\n\tv_mfma_f32_16x16x32_bf16 %6, %8, %17, %6\n\tds_read_b128 %7, %15 offset:8192"
      : "+v"(ac[1][0]), "=&v"(pu[5]), "+v"(ac[1][1]), "=&v"(py[0][3]), "=&a"(fwo[0]), "+v"(ac[1][2]), "+v"(ac[1][3]), "=&a"(fwo[1]) : "v"(pu[6]), "a"(fw2[6][0]), "v"(vFj[2]), "s"(rsW), "s"(su[5]), "a"(fw2[6][1]), "v"(rr), "v"(ro), "a"(fw2[6][2]), "a"(fw2[6][3]) : "memory"); SB();
  WAITVB(13);
  SB();
  asm volatile("v_mfma_f32_16x16x32_bf16 %0, %5, %6, %0\n\tbuffer_load_dwordx4 %1, %7, %8, %9 offen offset:0 sc1\n\tv_mfma_f32_16x16x32_bf16 %2, %5, %10, %2\n\tds_read_b128 %3, %11 offset:16384\n\tv_mfma_f32_16x16x32_bf16 %4, %5, %12, %4"
      : "+v"(ac[1][0]), "=&v"(pu[6]), "+v"(ac[1][1]), "=&a"(fwo[2]), "+v"(ac[1][2]) : "v"(pu[7]), "a"(fw2[7][0]), "v"(vFj[2]), "s"(rsW), "s"(su[6]), "a"(fw2[7][1]), "v"(ro), "a"(fw2[7][2]) : "memory"); SB();
  WAITL(3);
  asm volatile("v_add_f32 %0, %3, %4\n\tv_mfma_f32_16x16x32_bf16 %1, %5, %6, %1\n\tv_add_f32 %2, %7, %4"
      : "=&v"(ye[0]), "+v"(ac[1][3]), "=&v"(ye[1]) : "v"(py[0][0][0]), "v"(bias), "v"(pu[7]), "a"(fw2[7][3]), "v"(py[0][0][1])); SB();
  WAITVB(13);
  SB();
  asm volatile("v_mfma_f32_16x16x32_bf16 %0, %9, %10, %0\n\tbuffer_load_dwordx4 %1, %11, %12, %13 offen offset:0 sc1\n\tv_mfma_f32_16x16x32_bf16 %2, %9, %14, %2\n\tv_add_f32 %3, %15, %16\n\tv_add_f32 %4, %17, %16\n\tv_mfma_f32_16x16x32_bf16 %5, %9, %18, %5\n\tv_add_f32 %6, %19, %20\n\tv_add_f32 %7, %21, %22\n\tv_mfma_f32_16x16x32_bf16 %8, %9, %23, %8"
      : "+v"(ac[1][0]), "=&v"(pu[7]), "+v"(ac[1][1]), "=&v"(ye[2]), "=&v"(ye[3]), "+v"(ac[1][2]), "=&v"(ze[0]), "=&v"(ze[1]), "+v"(ac[1][3]) : "v"(pu[8]), "a"(fw2[8][0]), "v"(vFj[2]), "s"(rsW), "s"(su[7]), "a"(fw2[8][1]), "v"(py[0][0][2]), "v"(bias), "v"(py[0][0][3]), "a"(fw2[8][2]), "v"(py[0][1][0]), "v"(py[0][2][0]), "v"(py[0][1][1]), "v"(py[0][2][1]), "a"(fw2[8][3]) : "memory"); SB();
  asm volatile("v_add_f32 %0, %1, %2"
      : "=&v"(ze[2]) : "v"(py[0][1][2]), "v"(py[0][2][2])); SB();
  WAITVB(13);
  SB();
  asm volatile("v_mfma_f32_16x16x32_bf16 %0, %9, %10, %0\n\tbuffer_load_dwordx4 %1, %11, %12, %13 offen offset:0 sc1\n\tv_mfma_f32_16x16x32_bf16 %2, %9, %14, %2\n\tv_add_f32 %3, %15, %16\n\tv_add_f32 %4, %4, %17\n\tv_mfma_f32_16x16x32_bf16 %5, %9, %18, %5\n\tv_add_f32 %6, %6, %19\n\tv_add_f32 %7, %7, %20\n\tv_mfma_f32_16x16x32_bf16 %8, %9, %21, %8"
      : "+v"(ac[1][0]), "=&v"(pu[8]), "+v"(ac[1][1]), "=&v"(ze[3]), "+v"(ye[0]), "+v"(ac[1][2]), "+v"(ye[1]), "+v"(ye[2]), "+v"(ac[1][3]) : "v"(pu[9]), "v"(fw2[9][0]), "v"(vFj[2]), "s"(rsW), "s"(su[8]), "v"(fw2[9][1]), "v"(py[0][1][3]), "v"(py[0][2][3]), "v"(py[0][3][0]), "v"(fw2[9][2]), "v"(py[0][3][1]), "v"(py[0][3][2]), "v"(fw2[9][3]) : "memory"); SB();
  asm volatile("v_add_f32 %0, %0, %1"
      : "+v"(ye[3]) : "v"(py[0][3][3])); SB();
  WAITVB(13);
  SB();
  asm volatile("v_mfma_f32_16x16x32_bf16 %0, %10, %11, %0\n\tbuffer_load_dwordx4 %1, %12, %13, %14 offen offset:0 sc1\n\tv_mfma_f32_16x16x32_bf16 %2, %10, %15, %2\n\tv_add_f32 %3, %3, %4\n\tv_add_f32 %5, %5, %16\n\tv_mfma_f32_16x16x32_bf16 %6, %10, %17, %6\n\tv_add_f32 %7, %7, %18\n\tv_add_f32 %8, %8, %19\n\tv_mfma_f32_16x16x32_bf16 %9, %10, %20, %9\n\tv_mul_f32 %4, 0xbfb8aa3b, %3"
      : "+v"(ac[1][0]), "=&v"(pu[9]), "+v"(ac[1][1]), "+v"(ye[0]), "+v"(ze[0]), "+v"(ye[1]), "+v"(ac[1][2]), "+v"(ye[2]), "+v"(ye[3]), "+v"(ac[1][3]) : "v"(pu[10]), "v"(fw2[10][0]), "v"(vFj[2]), "s"(rsW), "s"(su[9]), "v"(fw2[10][1]), "v"(ze[1]), "v"(fw2[10][2]), "v"(ze[2]), "v"(ze[3]), "v"(fw2[10][3]) : "memory"); SB();
  WAITVB(13);
  SB();
  asm volatile("v_mfma_f32_16x16x32_bf16 %0, %9, %10, %0\n\tbuffer_load_dwordx4 %1, %11, %12, %13 offen offset:0 sc1\n\tv_mfma_f32_16x16x32_bf16 %2, %9, %14, %2\n\tv_mul_f32 %3, 0xbfb8aa3b, %15\n\tv_mul_f32 %4, 0xbfb8aa3b, %16\n\tv_mfma_f32_16x16x32_bf16 %5, %9, %17, %5\n\tds_read_b128 %6, %18 offset:1024\n\tv_mul_f32 %7, 0xbfb8aa3b, %19\n\tv_mfma_f32_16x16x32_bf16 %8, %9, %20, %8"
      : "+v"(ac[1][0]), "=&v"(pu[10]), "+v"(ac[1][1]), "=&v"(ze[1]), "=&v"(ze[2]), "+v"(ac[1][2]), "=&a"(fwo[3]), "=&v"(ze[3]), "+v"(ac[1][3]) : "v"(pu[11]), "v"(fw2[11][0]), "v"(vFj[2]), "s"(rsW), "s"(su[10]), "v"(fw2[11][1]), "v"(ye[1]), "v"(ye[2]), "v"(fw2[11][2]), "v"(ro), "v"(ye[3]), "v"(fw2[11][3]) : "memory"); SB();
  WAITVB(13);
  SB();
  asm volatile("v_mfma_f32_16x16x32_bf16 %0, %8, %9, %0\n\tbuffer_load_dwordx4 %1, %10, %11, %12 offen offset:0 sc1\n\tv_mfma_f32_16x16x32_bf16 %2, %8, %13, %2\n\tv_exp_f32 %3, %3\n\tv_mfma_f32_16x16x32_bf16 %4, %8, %14, %4\n\tv_exp_f32 %5, %5\n\tv_mfma_f32_16x16x32_bf16 %6, %8, %15, %6\n\tds_read_b128 %7, %16 offset:9216"
      : "+v"(ac[1][0]), "=&v"(pu[11]), "+v"(ac[1][1]), "+v"(ze[0]), "+v"(ac[1][2]), "+v"(ze[1]), "+v"(ac[1][3]), "=&a"(fwo[4]) : "v"(pu[12]), "v"(fw2[12][0]), "v"(vFj[2]), "s"(rsW), "s"(su[11]), "v"(fw2[12][1]), "v"(fw2[12][2]), "v"(fw2[12][3]), "v"(ro) : "memory"); SB();
  WAITVB(13);
  SB();
  asm volatile("v_mfma_f32_16x16x32_bf16 %0, %9, %10, %0\n\tbuffer_load_dwordx4 %1, %11, %12, %13 offen offset:0 sc1\n\tv_mfma_f32_16x16x32_bf16 %2, %9, %14, %2\n\tv_exp_f32 %3, %3\n\tv_mfma_f32_16x16x32_bf16 %4, %9, %15, %4\n\tv_exp_f32 %5, %5\n\tv_mfma_f32_16x16x32_bf16 %6, %9, %16, %6\n\tv_add_f32 %7, 1.0, %7\n\tv_add_f32 %8, 1.0, %8"
      : "+v"(ac[1][0]), "=&v"(pu[12]), "+v"(ac[1][1]), "+v"(ze[2]), "+v"(ac[1][2]), "+v"(ze[3]), "+v"(ac[1][3]), "+v"(ze[0]), "+v"(ze[1]) : "v"(pu[13]), "v"(fw2[13][0]), "v"(vFj[2]), "s"(rsW), "s"(su[12]), "v"(fw2[13][1]), "v"(fw2[13][2]), "v"(fw2[13][3]) : "memory"); SB();
  STAMP(41);
  WAITVB(12);
  SB();
  asm volatile("v_mfma_f32_16x16x32_bf16 %0, %9, %10, 0\n\tbuffer_load_dwordx4 %1, %11, %12, %13 offen offset:0 sc1\n\tv_mfma_f32_16x16x32_bf16 %2, %9, %14, 0\n\tv_add_f32 %3, 1.0, %3\n\tv_add_f32 %4, 1.0, %4\n\tv_mfma_f32_16x16x32_bf16 %5, %9, %15, 0\n\tv_rcp_f32 %6, %6\n\tv_mfma_f32_16x16x32_bf16 %7, %9, %16, 0\n\tds_read_b128 %8, %17 offset:17408"
      : "=&v"(ac[0][0]), "=&v"(pu[13]), "=&v"(ac[0][1]), "+v"(ze[2]), "+v"(ze[3]), "=&v"(ac[0][2]), "+v"(ze[0]), "=&v"(ac[0][3]), "=&a"(fwo[5]) : "v"(pu[0]), "a"(fw2[0][0]), "v"(vFj[2]), "s"(rsW), "s"(su[13]), "a"(fw2[0][1]), "a"(fw2[0][2]), "a"(fw2[0][3]), "v"(ro) : "memory"); SB();
  WAITVB(12);
  SB();
  asm volatile("v_mfma_f32_16x16x32_bf16 %0, %1, %2, %0"
      : "+v"(ac[0][0]) : "v"(pu[1]), "a"(fw2[1][0])); SB();
  WAITV1(14, fo);
  if (__builtin_expect(__ballot(fo != nonce) != 0ull, 0)) poll1<4>(rsW, vFO, nonce);
  STAMP(18);
  asm volatile("buffer_load_dwordx4 %0, %6, %7, %8 offen offset:0 sc1\n\tv_mfma_f32_16x16x32_bf16 %1, %9, %10, %1\n\tds_write_b128 %11, %12 offset:18432\n\tv_mfma_f32_16x16x32_bf16 %2, %9, %13, %2\n\tv_rcp_f32 %3, %3\n\tv_mfma_f32_16x16x32_bf16 %4, %9, %14, %4\n\tds_read_b128 %5, %15 offset:2048"
      : "=&v"(pu[0]), "+v"(ac[0][1]), "+v"(ac[0][2]), "+v"(ze[1]), "+v"(ac[0][3]), "=&a"(fwo[6]) : "v"(vF), "s"(rsW), "s"(so0), "v"(pu[1]), "a"(fw2[1][1]), "v"(rwd), "v"(ac[1][0]), "a"(fw2[1][2]), "a"(fw2[1][3]), "v"(ro) : "memory"); SB();
  WAITVB(12);
  SB();
  asm volatile("v_mfma_f32_16x16x32_bf16 %0, %7, %8, %0\n\tbuffer_load_dwordx4 %1, %9, %10, %11 offen offset:128 sc1\n\tv_mfma_f32_16x16x32_bf16 %2, %7, %12, %2\n\tds_write_b128 %13, %14 offset:19456\n\tv_mfma_f32_16x16x32_bf16 %3, %7, %15, %3\n\tv_rcp_f32 %4, %4\n\tv_mfma_f32_16x16x32_bf16 %5, %7, %16, %5\n\tds_read_b128 %6, %17 offset:10240"
      : "+v"(ac[0][0]), "=&v"(pu[1]), "+v"(ac[0][1]), "+v"(ac[0][2]), "+v"(ze[2]), "+v"(ac[0][3]), "=&a"(fwo[7]) : "v"(pu[2]), "a"(fw2[2][0]), "v"(vF), "s"(rsW), "s"(so0), "a"(fw2[2][1]), "v"(rwd), "v"(ac[1][1]), "a"(fw2[2][2]), "a"(fw2[2][3]), "v"(ro) : "memory"); SB();
  WAITVB(12);
  SB();
  asm volatile("v_mfma_f32_16x16x32_bf16 %0, %7, %8, %0\n\tbuffer_load_dwordx4 %1, %9, %10, %11 offen offset:256 sc1\n\tv_mfma_f32_16x16x32_bf16 %2, %7, %12, %2\n\tds_write_b128 %13, %14 offset:20480\n\tv_mfma_f32_16x16x32_bf16 %3, %7, %15, %3\n\tv_rcp_f32 %4, %4\n\tv_mfma_f32_16x16x32_bf16 %5, %7, %16, %5\n\tv_mul_f32 %6, %17, %18"
      : "+v"(ac[0][0]), "=&v"(pu[2]), "+v"(ac[0][1]), "+v"(ac[0][2]), "+v"(ze[3]), "+v"(ac[0][3]), "=&v"(ge[0]) : "v"(pu[3]), "a"(fw2[3][0]), "v"(vF), "s"(rsW), "s"(so0), "a"(fw2[3][1]), "v"(rwd), "v"(ac[1][2]), "a"(fw2[3][2]), "a"(fw2[3][3]), "v"(ye[0]), "v"(ze[0]) : "memory"); SB();
  WAITVB(12);
  SB();
  asm volatile("v_mfma_f32_16x16x32_bf16 %0, %8, %9, %0\n\tbuffer_load_dwordx4 %1, %10, %11, %12 offen offset:384 sc1\n\tv_mfma_f32_16x16x32_bf16 %2, %8, %13, %2\n\tds_write_b128 %14, %15 offset:21504\n\tv_mfma_f32_16x16x32_bf16 %3, %8, %16, %3\n\tv_mul_f32 %4, %17, %18\n\tv_mul_f32 %5, %19, %20\n\tv_mfma_f32_16x16x32_bf16 %6, %8, %21, %6\n\tv_mul_f32 %7, %22, %23"
      : "+v"(ac[0][0]), "=&v"(pu[3]), "+v"(ac[0][1]), "+v"(ac[0][2]), "=&v"(ge[1]), "=&v"(ge[2]), "+v"(ac[0][3]), "=&v"(ge[3]) : "v"(pu[4]), "a"(fw2[4][0]), "v"(vF), "s"(rsW), "s"(so0), "a"(fw2[4][1]), "v"(rwd), "v"(ac[1][3]), "a"(fw2[4][2]), "v"(ye[1]), "v"(ze[1]), "v"(ye[2]), "v"(ze[2]), "a"(fw2[4][3]), "v"(ye[3]), "v"(ze[3]) : "memory"); SB();
  WAITVB(12);
  SB();
  asm volatile("v_mfma_f32_16x16x32_bf16 %0, %3, %4, %0\n\tbuffer_load_dwordx4 %1, %5, %6, %7 offen offset:512 sc1\n\tv_mfma_f32_16x16x32_bf16 %2, %3, %8, %2"
      : "+v"(ac[0][0]), "=&v"(pu[4]), "+v"(ac[0][1]) : "v"(pu[5]), "a"(fw2[5][0]), "v"(vF), "s"(rsW), "s"(so0), "a"(fw2[5][1]) : "memory"); SB();
  WAITL(0);
  BAR0();
  SB();
  asm volatile("v_mfma_f32_16x16x32_bf16 %0, %5, %6, %0\n\tds_read_b128 %1, %7 offset:18432\n\tds_read_b128 %2, %7 offset:22528\n\tv_mfma_f32_16x16x32_bf16 %3, %5, %8, %3\n\tds_read_b128 %4, %7 offset:26624"
      : "+v"(ac[0][2]), "=&v"(py[1][0]), "=&v"(py[1][1]), "+v"(ac[0][3]), "=&v"(py[1][2]) : "v"(pu[5]), "a"(fw2[5][2]), "v"(rr), "a"(fw2[5][3]) : "memory"); SB();
  WAITVB(12);
  SB();
  asm volatile("v_mfma_f32_16x16x32_bf16 %0, %8, %9, %0\n\tbuffer_load_dwordx4 %1, %10, %11, %12 offen offset:640 sc1\n\tv_mfma_f32_16x16x32_bf16 %2, %8, %13, %2\n\tds_read_b128 %3, %14 offset:30720\n\tds_read_b128 %4, %15 offset:18432\n\tv_mfma_f32_16x16x32_bf16 %5, %8, %16, %5\n\tv_mfma_f32_16x16x32_bf16 %6, %8, %17, %6\n\tds_read_b128 %7, %15 offset:3072"
      : "+v"(ac[0][0]), "=&v"(pu[5]), "+v"(ac[0][1]), "=&v"(py[1][3]), "=&a"(fwo[8]), "+v"(ac[0][2]), "+v"(ac[0][3]), "=&a"(fwo[9]) : "v"(pu[6]), "a"(fw2[6][0]), "v"(vF), "s"(rsW), "s"(so0), "a"(fw2[6][1]), "v"(rr), "v"(ro), "a"(fw2[6][2]), "a"(fw2[6][3]) : "memory"); SB();
  WAITVB(12);
  SB();
  asm volatile("v_mfma_f32_16x16x32_bf16 %0, %5, %6, %0\n\tbuffer_load_dwordx4 %1, %7, %8, %9 offen offset:768 sc1\n\tv_mfma_f32_16x16x32_bf16 %2, %5, %10, %2\n\tds_read_b128 %3, %11 offset:11264\n\tv_mfma_f32_16x16x32_bf16 %4, %5, %12, %4"
      : "+v"(ac[0][0]), "=&v"(pu[6]), "+v"(ac[0][1]), "=&a"(fwo[10]), "+v"(ac[0][2]) : "v"(pu[7]), "a"(fw2[7][0]), "v"(vF), "s"(rsW), "s"(so0), "a"(fw2[7][1]), "v"(ro), "a"(fw2[7][2]) : "memory"); SB();
  WAITL(3);
  asm volatile("v_add_f32 %0, %3, %4\n\tv_mfma_f32_16x16x32_bf16 %1, %5, %6, %1\n\tv_add_f32 %2, %7, %4"
      : "=&v"(ye[0]), "+v"(ac[0][3]), "=&v"(ye[1]) : "v"(py[1][0][0]), "v"(bias), "v"(pu[7]), "a"(fw2[7][3]), "v"(py[1][0][1])); SB();
  WAITVB(12);
  SB();
  asm volatile("v_mfma_f32_16x16x32_bf16 %0, %9, %10, %0\n\tbuffer_load_dwordx4 %1, %11, %12, %13 offen offset:0 sc1\n\tv_mfma_f32_16x16x32_bf16 %2, %9, %14, %2\n\tv_add_f32 %3, %15, %16\n\tv_add_f32 %4, %17, %16\n\tv_mfma_f32_16x16x32_bf16 %5, %9, %18, %5\n\tv_add_f32 %6, %19, %20\n\tv_add_f32 %7, %21, %22\n\tv_mfma_f32_16x16x32_bf16 %8, %9, %23, %8"
      : "+v"(ac[0][0]), "=&v"(pu[7]), "+v"(ac[0][1]), "=&v"(ye[2]), "=&v"(ye[3]), "+v"(ac[0][2]), "=&v"(ze[0]), "=&v"(ze[1]), "+v"(ac[0][3]) : "v"(pu[8]), "a"(fw2[8][0]), "v"(vF), "s"(rsW), "s"(so1), "a"(fw2[8][1]), "v"(py[1][0][2]), "v"(bias), "v"(py[1][0][3]), "a"(fw2[8][2]), "v"(py[1][1][0]), "v"(py[1][2][0]), "v"(py[1][1][1]), "v"(py[1][2][1]), "a"(fw2[8][3]) : "memory"); SB();
  asm volatile("v_add_f32 %0, %1, %2"
      : "=&v"(ze[2]) : "v"(py[1][1][2]), "v"(py[1][2][2])); SB();
  WAITVB(12);
  SB();
  asm volatile("v_mfma_f32_16x16x32_bf16 %0, %9, %10, %0\n\tbuffer_load_dwordx4 %1, %11, %12, %13 offen offset:128 sc1\n\tv_mfma_f32_16x16x32_bf16 %2, %9, %14, %2\n\tv_add_f32 %3, %15, %16\n\tv_add_f32 %4, %4, %17\n\tv_mfma_f32_16x16x32_bf16 %5, %9, %18, %5\n\tv_add_f32 %6, %6, %19\n\tv_add_f32 %7, %7, %20\n\tv_mfma_f32_16x16x32_bf16 %8, %9, %21, %8"
      : "+v"(ac[0][0]), "=&v"(pu[8]), "+v"(ac[0][1]), "=&v"(ze[3]), "+v"(ye[0]), "+v"(ac[0][2]), "+v"(ye[1]), "+v"(ye[2]), "+v"(ac[0][3]) : "v"(pu[9]), "v"(fw2[9][0]), "v"(vF), "s"(rsW), "s"(so1), "v"(fw2[9][1]), "v"(py[1][1][3]), "v"(py[1][2][3]), "v"(py[1][3][0]), "v"(fw2[9][2]), "v"(py[1][3][1]), "v"(py[1][3][2]), "v"(fw2[9][3]) : "memory"); SB();
  asm volatile("v_add_f32 %0, %0, %1"
      : "+v"(ye[3]) : "v"(py[1][3][3])); SB();
  WAITVB(12);
  SB();
  asm volatile("v_mfma_f32_16x16x32_bf16 %0, %10, %11, %0\n\tbuffer_load_dwordx4 %1, %12, %13, %14 offen offset:256 sc1\n\tv_mfma_f32_16x16x32_bf16 %2, %10, %15, %2\n\tv_add_f32 %3, %3, %4\n\tv_add_f32 %5, %5, %16\n\tv_mfma_f32_16x16x32_bf16 %6, %10, %17, %6\n\tv_add_f32 %7, %7, %18\n\tv_add_f32 %8, %8, %19\n\tv_mfma_f32_16x16x32_bf16 %9, %10, %20, %9\n\tv_mul_f32 %4, 0xbfb8aa3b, %3"
      : "+v"(ac[0][0]), "=&v"(pu[9]), "+v"(ac[0][1]), "+v"(ye[0]), "+v"(ze[0]), "+v"(ye[1]), "+v"(ac[0][2]), "+v"(ye[2]), "+v"(ye[3]), "+v"(ac[0][3]) : "v"(pu[10]), "v"(fw2[10][0]), "v"(vF), "s"(rsW), "s"(so1), "v"(fw2[10][1]), "v"(ze[1]), "v"(fw2[10][2]), "v"(ze[2]), "v"(ze[3]), "v"(fw2[10][3]) : "memory"); SB();
  WAITVB(12);
  SB();
  asm volatile("v_mfma_f32_16x16x32_bf16 %0, %9, %10, %0\n\tbuffer_load_dwordx4 %1, %11, %12, %13 offen offset:384 sc1\n\tv_mfma_f32_16x16x32_bf16 %2, %9, %14, %2\n\tv_mul_f32 %3, 0xbfb8aa3b, %15\n\tv_mul_f32 %4, 0xbfb8aa3b, %16\n\tv_mfma_f32_16x16x32_bf16 %5, %9, %17, %5\n\tds_read_b128 %6, %18 offset:19456\n\tv_mul_f32 %7, 0xbfb8aa3b, %19\n\tv_mfma_f32_16x16x32_bf16 %8, %9, %20, %8"
      : "+v"(ac[0][0]), "=&v"(pu[10]), "+v"(ac[0][1]), "=&v"(ze[1]), "=&v"(ze[2]), "+v"(ac[0][2]), "=&a"(fwo[11]), "=&v"(ze[3]), "+v"(ac[0][3]) : "v"(pu[11]), "v"(fw2[11][0]), "v"(vF), "s"(rsW), "s"(so1), "v"(fw2[11][1]), "v"(ye[1]), "v"(ye[2]), "v"(fw2[11][2]), "v"(ro), "v"(ye[3]), "v"(fw2[11][3]) : "memory"); SB();
  WAITVB(12);
  SB();
  asm volatile("v_mfma_f32_16x16x32_bf16 %0, %8, %9, %0\n\tbuffer_load_dwordx4 %1, %10, %11, %12 offen offset:512 sc1\n\tv_mfma_f32_16x16x32_bf16 %2, %8, %13, %2\n\tv_exp_f32 %3, %3\n\tv_mfma_f32_16x16x32_bf16 %4, %8, %14, %4\n\tv_exp_f32 %5, %5\n\tv_mfma_f32_16x16x32_bf16 %6, %8, %15, %6\n\tds_read_b128 %7, %16 offset:4096"
      : "+v"(ac[0][0]), "=&v"(pu[11]), "+v"(ac[0][1]), "+v"(ze[0]), "+v"(ac[0][2]), "+v"(ze[1]), "+v"(ac[0][3]), "=&a"(fwo[12]) : "v"(pu[12]), "v"(fw2[12][0]), "v"(vF), "s"(rsW), "s"(so1), "v"(fw2[12][1]), "v"(fw2[12][2]), "v"(fw2[12][3]), "v"(ro) : "memory"); SB();
  WAITVB(12);
  SB();
  asm volatile("v_mfma_f32_16x16x32_bf16 %0, %9, %10, %0\n\tbuffer_load_dwordx4 %1, %11, %12, %13 offen offset:640 sc1\n\tv_mfma_f32_16x16x32_bf16 %2, %9, %14, %2\n\tv_exp_f32 %3, %3\n\tv_mfma_f32_16x16x32_bf16 %4, %9, %15, %4\n\tv_exp_f32 %5, %5\n\tv_mfma_f32_16x16x32_bf16 %6, %9, %16, %6\n\tv_add_f32 %7, 1.0, %7\n\tv_add_f32 %8, 1.0, %8"
      : "+v"(ac[0][0]), "=&v"(pu[12]), "+v"(ac[0][1]), "+v"(ze[2]), "+v"(ac[0][2]), "+v"(ze[3]), "+v"(ac[0][3]), "+v"(ze[0]), "+v"(ze[1]) : "v"(pu[13]), "v"(fw2[13][0]), "v"(vF), "s"(rsW), "s"(so1), "v"(fw2[13][1]), "v"(fw2[13][2]), "v"(fw2[13][3]) : "memory"); SB();
  STAMP(42);
  WAITVB(12);
  asm volatile("ds_read_b128 %0, %8 offset:12288\n\tv_mfma_f32_16x16x32_bf16 %1, %9, %10, 0\n\tbuffer_load_dwordx4 %2, %11, %12, %13 offen offset:768 sc1\n\tv_mfma_f32_16x16x32_bf16 %3, %14, %10, 0\n\tv_add_f32 %4, 1.0, %4\n\tv_add_f32 %5, 1.0, %5\n\tv_mfma_f32_16x16x32_bf16 %6, %15, %10, 0\n\tds_read_b128 %7, %8 offset:20480"
      : "=&a"(fwo[13]), "=&v"(as[0]), "=&v"(pu[13]), "=&v"(as[1]), "+v"(ze[2]), "+v"(ze[3]), "=&v"(as[2]), "=&a"(fwo[14]) : "v"(ro), "a"(fwo[0]), "v"(pu[0]), "v"(vF), "s"(rsW), "s"(so1), "a"(fwo[1]), "a"(fwo[2]) : "memory"); SB();
  WAITVB(12);
  SB();
  asm volatile("v_mfma_f32_16x16x32_bf16 %0, %5, %6, %0\n\tds_write_b128 %7, %8 offset:0\n\tv_mfma_f32_16x16x32_bf16 %1, %9, %6, %1\n\tv_rcp_f32 %2, %2\n\tv_mfma_f32_16x16x32_bf16 %3, %10, %6, %3\n\tds_read_b128 %4, %11 offset:5120"
      : "+v"(as[0]), "+v"(as[1]), "+v"(ze[0]), "+v"(as[2]), "=&a"(fwo[15]) : "a"(fwo[3]), "v"(pu[1]), "v"(rwd), "v"(ac[0][0]), "a"(fwo[4]), "a"(fwo[5]), "v"(ro) : "memory"); SB();
  WAITVB(11);
  asm volatile("ds_read_b128 %0, %4 offset:13312\n\tds_read_b128 %1, %4 offset:21504\n\tds_read_b128 %2, %4 offset:6144\n\tds_read_b128 %3, %4 offset:14336"
      : "=&a"(fwo[16]), "=&a"(fwo[17]), "=&a"(fwo[18]), "=&a"(fwo[19]) : "v"(ro) : "memory"); SB();
  WAITL(9);
  SB();
  asm volatile("v_mfma_f32_16x16x32_bf16 %0, %5, %6, %0\n\tds_write_b128 %7, %8 offset:1024\n\tv_mfma_f32_16x16x32_bf16 %1, %9, %6, %1\n\tv_rcp_f32 %2, %2\n\tv_mfma_f32_16x16x32_bf16 %3, %10, %6, %3\n\tds_read_b128 %4, %11 offset:22528"
      : "+v"(as[0]), "+v"(as[1]), "+v"(ze[1]), "+v"(as[2]), "=&a"(fwo[20]) : "a"(fwo[6]), "v"(pu[2]), "v"(rwd), "v"(ac[0][1]), "a"(fwo[7]), "a"(fwo[8]), "v"(ro) : "memory"); SB();
  WAITVB(10);
  SB();
  asm volatile("v_mfma_f32_16x16x32_bf16 %0, %5, %6, %0\n\tds_write_b128 %7, %8 offset:2048\n\tv_mfma_f32_16x16x32_bf16 %1, %9, %6, %1\n\tv_rcp_f32 %2, %2\n\tv_mfma_f32_16x16x32_bf16 %3, %10, %6, %3\n\tds_read_b128 %4, %11 offset:7168"
      : "+v"(as[0]), "+v"(as[1]), "+v"(ze[2]), "+v"(as[2]), "=&a"(fwo[21]) : "a"(fwo[9]), "v"(pu[3]), "v"(rwd), "v"(ac[0][2]), "a"(fwo[10]), "a"(fwo[11]), "v"(ro) : "memory"); SB();
  WAITVB(9);
  asm volatile("ds_read_b128 %0, %4 offset:15360\n\tds_read_b128 %1, %4 offset:23552\n\tds_read_b128 %2, %4 offset:32768\n\tds_read_b128 %3, %4 offset:38912"
      : "=&a"(fwo[22]), "=&a"(fwo[23]), "=&a"(fwo[24]), "=&a"(fwo[25]) : "v"(ro) : "memory"); SB();
  WAITL(10);
  SB();
  asm volatile("v_mfma_f32_16x16x32_bf16 %0, %5, %6, %0\n\tds_write_b128 %7, %8 offset:3072\n\tv_mfma_f32_16x16x32_bf16 %1, %9, %6, %1\n\tv_rcp_f32 %2, %2\n\tv_mfma_f32_16x16x32_bf16 %3, %10, %6, %3\n\tv_mul_f32 %4, %11, %12"
      : "+v"(as[0]), "+v"(as[1]), "+v"(ze[3]), "+v"(as[2]), "=&v"(ge[4]) : "a"(fwo[12]), "v"(pu[4]), "v"(rwd), "v"(ac[0][3]), "a"(fwo[13]), "a"(fwo[14]), "v"(ye[0]), "v"(ze[0]) : "memory"); SB();
  WAITVB(8);
  SB();
  asm volatile("v_mfma_f32_16x16x32_bf16 %0, %4, %5, %0\n\tv_mul_f32 %1, %6, %7\n\tv_mul_f32 %2, %8, %9\n\tv_mfma_f32_16x16x32_bf16 %3, %10, %5, %3"
      : "+v"(as[0]), "=&v"(ge[5]), "=&v"(ge[6]), "+v"(as[1]) : "a"(fwo[15]), "v"(pu[5]), "v"(ye[1]), "v"(ze[1]), "v"(ye[2]), "v"(ze[2]), "a"(fwo[16])); SB();
  WAITL(0);
  BAR0();
  SB();
  asm volatile("v_mfma_f32_16x16x32_bf16 %0, %1, %2, %0"
      : "+v"(as[2]) : "a"(fwo[17]), "v"(pu[5])); SB();
  WAITVB(7);
  asm volatile("ds_read_b128 %0, %14 offset:45056\n\tds_read_b128 %1, %14 offset:33792\n\tds_read_b128 %2, %14 offset:39936\n\tds_read_b128 %3, %14 offset:46080\n\tds_read_b128 %4, %14 offset:34816\n\tds_read_b128 %5, %14 offset:40960\n\tv_mfma_f32_16x16x32_bf16 %6, %15, %16, %6\n\tds_read_b128 %7, %17 offset:0\n\tds_read_b128 %8, %17 offset:4096\n\tv_mfma_f32_16x16x32_bf16 %9, %18, %16, %9\n\tds_read_b128 %10, %17 offset:8192\n\tds_read_b128 %11, %17 offset:12288\n\tv_mfma_f32_16x16x32_bf16 %12, %19, %16, %12\n\tv_mul_f32 %13, %20, %21"
      : "=&a"(fwo[26]), "=&a"(fwo[27]), "=&a"(fwo[28]), "=&a"(fwo[29]), "=&a"(fwo[30]), "=&a"(fwo[31]), "+v"(as[0]), "=&v"(py[2][0]), "=&v"(py[2][1]), "+v"(as[1]), "=&v"(py[2][2]), "=&v"(py[2][3]), "+v"(as[2]), "=&v"(ge[7]) : "v"(ro), "a"(fwo[18]), "v"(pu[6]), "v"(rr), "a"(fwo[19]), "a"(fwo[20]), "v"(ye[3]), "v"(ze[3]) : "memory"); SB();
  WAITVB(6);
  SB();
  asm volatile("v_mfma_f32_16x16x32_bf16 %0, %4, %5, %0\n\tds_read_b128 %1, %6 offset:47104\n\tv_mfma_f32_16x16x32_bf16 %2, %7, %5, %2\n\tv_mfma_f32_16x16x32_bf16 %3, %8, %5, %3"
      : "+v"(as[0]), "=&a"(fwo[32]), "+v"(as[1]), "+v"(as[2]) : "a"(fwo[21]), "v"(pu[7]), "v"(ro), "a"(fwo[22]), "a"(fwo[23]) : "memory"); SB();
  WAITVB(5);
  asm volatile("ds_read_b128 %0, %5 offset:35840\n\tds_read_b128 %1, %5 offset:41984\n\tds_read_b128 %2, %5 offset:48128\n\tds_read_b128 %3, %5 offset:36864\n\tds_read_b128 %4, %5 offset:43008"
      : "=&a"(fwo[33]), "=&a"(fwo[34]), "=&a"(fwo[35]), "=&a"(fwo[36]), "=&a"(fwo[37]) : "v"(ro) : "memory"); SB();
  WAITL(12);
  SB();
  asm volatile("v_mfma_f32_16x16x32_bf16 %0, %1, %2, %0"
      : "+v"(as[0]) : "a"(fwo[24]), "v"(pu[8])); SB();
  WAITL(6);
  asm volatile("v_add_f32 %0, %6, %7\n\tv_mfma_f32_16x16x32_bf16 %1, %8, %9, %1\n\tv_add_f32 %2, %10, %7\n\tv_add_f32 %3, %11, %7\n\tv_mfma_f32_16x16x32_bf16 %4, %12, %9, %4\n\tv_add_f32 %5, %13, %7"
      : "=&v"(ye[0]), "+v"(as[1]), "=&v"(ye[1]), "=&v"(ye[2]), "+v"(as[2]), "=&v"(ye[3]) : "v"(py[2][0][0]), "v"(bias), "a"(fwo[25]), "v"(pu[8]), "v"(py[2][0][1]), "v"(py[2][0][2]), "a"(fwo[26]), "v"(py[2][0][3])); SB();
  WAITVB(4);
  SB();
  asm volatile("v_mfma_f32_16x16x32_bf16 %0, %7, %8, %0\n\tv_add_f32 %1, %9, %10\n\tv_add_f32 %2, %11, %12\n\tv_mfma_f32_16x16x32_bf16 %3, %13, %8, %3\n\tv_add_f32 %4, %14, %15\n\tv_add_f32 %5, %16, %17\n\tv_mfma_f32_16x16x32_bf16 %6, %18, %8, %6"
      : "+v"(as[0]), "=&v"(ze[0]), "=&v"(ze[1]), "+v"(as[1]), "=&v"(ze[2]), "=&v"(ze[3]), "+v"(as[2]) : "a"(fwo[27]), "v"(pu[9]), "v"(py[2][1][0]), "v"(py[2][2][0]), "v"(py[2][1][1]), "v"(py[2][2][1]), "a"(fwo[28]), "v"(py[2][1][2]), "v"(py[2][2][2]), "v"(py[2][1][3]), "v"(py[2][2][3]), "a"(fwo[29])); SB();
  WAITVB(3);
  asm volatile("ds_read_b128 %0, %4 offset:49152\n\tds_read_b128 %1, %4 offset:37888\n\tds_read_b128 %2, %4 offset:44032\n\tds_read_b128 %3, %4 offset:50176"
      : "=&a"(fwo[38]), "=&a"(fwo[39]), "=&a"(fwo[40]), "=&a"(fwo[41]) : "v"(ro) : "memory"); SB();
  WAITL(6);
  SB();
  asm volatile("v_mfma_f32_16x16x32_bf16 %0, %7, %8, %0\n\tv_add_f32 %1, %1, %9\n\tv_add_f32 %2, %2, %10\n\tv_mfma_f32_16x16x32_bf16 %3, %11, %8, %3\n\tv_add_f32 %4, %4, %12\n\tv_add_f32 %5, %5, %13\n\tv_mfma_f32_16x16x32_bf16 %6, %14, %8, %6"
      : "+v"(as[0]), "+v"(ye[0]), "+v"(ye[1]), "+v"(as[1]), "+v"(ye[2]), "+v"(ye[3]), "+v"(as[2]) : "a"(fwo[30]), "v"(pu[10]), "v"(py[2][3][0]), "v"(py[2][3][1]), "a"(fwo[31]), "v"(py[2][3][2]), "v"(py[2][3][3]), "a"(fwo[32])); SB();
  WAITL(0);
  WAITVB(0);
  SB();
  asm volatile("v_mfma_f32_16x16x32_bf16 %0, %10, %11, %0\n\tv_add_f32 %1, %1, %2\n\tv_add_f32 %3, %3, %4\n\tv_mfma_f32_16x16x32_bf16 %0, %12, %13, %0\n\tv_add_f32 %5, %5, %6\n\tv_add_f32 %7, %7, %8\n\tv_mfma_f32_16x16x32_bf16 %0, %14, %15, %0\n\tv_mul_f32 %2, 0xbfb8aa3b, %1\n\tv_mul_f32 %4, 0xbfb8aa3b, %3\n\tv_mfma_f32_16x16x32_bf16 %9, %16, %11, %9\n\tv_mul_f32 %6, 0xbfb8aa3b, %5\n\tv_mul_f32 %8, 0xbfb8aa3b, %7\n\tv_mfma_f32_16x16x32_bf16 %9, %17, %13, %9\n\tv_exp_f32 %2, %2\n\tv_mfma_f32_16x16x32_bf16 %9, %18, %15, %9\n\tv_exp_f32 %4, %4"
      : "+v"(as[0]), "+v"(ye[0]), "+v"(ze[0]), "+v"(ye[1]), "+v"(ze[1]), "+v"(ye[2]), "+v"(ze[2]), "+v"(ye[3]), "+v"(ze[3]), "+v"(as[1]) : "a"(fwo[33]), "v"(pu[11]), "a"(fwo[36]), "v"(pu[12]), "a"(fwo[39]), "v"(pu[13]), "a"(fwo[34]), "a"(fwo[37]), "a"(fwo[40])); SB();
  asm volatile("v_mfma_f32_16x16x32_bf16 %0, %5, %6, %0\n\tv_exp_f32 %1, %1\n\tv_mfma_f32_16x16x32_bf16 %0, %7, %8, %0\n\tv_exp_f32 %2, %2\n\tv_mfma_f32_16x16x32_bf16 %0, %9, %10, %0\n\tv_add_f32 %3, 1.0, %3\n\tv_add_f32 %4, 1.0, %4"
      : "+v"(as[2]), "+v"(ze[2]), "+v"(ze[3]), "+v"(ze[0]), "+v"(ze[1]) : "a"(fwo[35]), "v"(pu[11]), "a"(fwo[38]), "v"(pu[12]), "a"(fwo[41]), "v"(pu[13])); SB();
  STAMP(44);
  asm volatile("v_add_f32 %0, 1.0, %0\n\tv_add_f32 %1, 1.0, %1\n\tv_rcp_f32 %2, %2\n\tv_rcp_f32 %3, %3\n\tv_rcp_f32 %0, %0\n\tv_rcp_f32 %1, %1\n\tv_mul_f32 %4, %11, %2\n\tv_mul_f32 %5, %12, %3\n\tv_mul_f32 %6, %13, %0\n\tv_mul_f32 %7, %14, %1\n\tv_fma_f32 %8, %15, %16, %8\n\tv_fma_f32 %9, %17, %18, %9\n\tv_fma_f32 %10, %19, %20, %10"
      : "+v"(ze[2]), "+v"(ze[3]), "+v"(ze[0]), "+v"(ze[1]), "=&v"(ge[8]), "=&v"(ge[9]), "=&v"(ge[10]), "=&v"(ge[11]), "+v"(xe[0]), "+v"(xe[1]), "+v"(xe[2]) : "v"(ye[0]), "v"(ye[1]), "v"(ye[2]), "v"(ye[3]), "v"(as[0][0]), "v"(ge[0]), "v"(as[0][1]), "v"(ge[1]), "v"(as[0][2]), "v"(ge[2])); SB();
  asm volatile("v_fma_f32 %0, %9, %10, %0\n\tv_cvt_pk_bf16_f32 %1, %11, %12\n\tv_cvt_pk_bf16_f32 %2, %13, %0\n\tv_fma_f32 %3, %14, %15, %3\n\tv_fma_f32 %4, %16, %17, %4\n\tv_fma_f32 %5, %18, %19, %5\n\tv_fma_f32 %6, %20, %21, %6\n\tv_cvt_pk_bf16_f32 %7, %3, %4\n\tv_cvt_pk_bf16_f32 %8, %5, %6"
      : "+v"(xe[3]), "=&v"(ov[0][0]), "=&v"(ov[0][1]), "+v"(xe[4]), "+v"(xe[5]), "+v"(xe[6]), "+v"(xe[7]), "=&v"(ov[1][0]), "=&v"(ov[1][1]) : "v"(as[0][3]), "v"(ge[3]), "v"(xe[0]), "v"(xe[1]), "v"(xe[2]), "v"(as[1][0]), "v"(ge[4]), "v"(as[1][1]), "v"(ge[5]), "v"(as[1][2]), "v"(ge[6]), "v"(as[1][3]), "v"(ge[7])); SB();
  asm volatile("ds_write_b64 %6, %7 offset:0\n\tv_fma_f32 %0, %8, %9, %0\n\tv_fma_f32 %1, %10, %11, %1\n\tv_fma_f32 %2, %12, %13, %2\n\tv_fma_f32 %3, %14, %15, %3\n\tv_cvt_pk_bf16_f32 %4, %0, %1\n\tv_cvt_pk_bf16_f32 %5, %2, %3\n\tds_write_b64 %16, %17 offset:0"
      : "+v"(xe[8]), "+v"(xe[9]), "+v"(xe[10]), "+v"(xe[11]), "=&v"(ov[2][0]), "=&v"(ov[2][1]) : "v"(xta[0]), "v"(ov[0]), "v"(as[2][0]), "v"(ge[8]), "v"(as[2][1]), "v"(ge[9]), "v"(as[2][2]), "v"(ge[10]), "v"(as[2][3]), "v"(ge[11]), "v"(xta[1]), "v"(ov[1]) : "memory"); SB();
  asm volatile("ds_write_b64 %0, %1 offset:0"
      :  : "v"(xta[2]), "v"(ov[2]) : "memory"); SB();
  { u32x4 t0_, t1_; DSR16(t0_, xo, 0); DSR16(t1_, xo, 1024); WAITLB(1);
    BS16(vXa, rsO_, 0, t0_, ""); WAITLB(0); BS16(vXb, rsO_, 1024, t1_, ""); }
  STAMP(23);

  STAMP(12);
  STAMP_DUMP();
  }
  } else {
  asm volatile("; NEED B");
  unsigned nonce; asm volatile("s_load_dword %0, %1, 0x0" : "=&s"(nonce) : "s"(ws) : "memory");
  const int iw = (4 * xq + jb - 28) * 4 + w, ih = (iw * 37) >> 8;
  const unsigned ldsI = lds0 + w * SCW;
  { u32x4 rsO = srd(wo, 448u * 896u);
    const unsigned vO_ = (28 * (r8 >> 2) + (r8 & 3)) * 896 + ((lane & 7) ^ ((r8 & 2) << 1)) * 16, sO_ = (4 * iw + 28 * ih) * 896;
    DMA1(vO_, rsO, sO_, ldsI, 0, 0, "");
    DMA1(vO_, rsO, sO_, ldsI, 896, 128, "");
    DMA1(vO_, rsO, sO_, ldsI, 1792, 256, "");
    DMA1(vO_, rsO, sO_, ldsI, 2688, 384, "");
    DMA1(vO_, rsO, sO_, ldsI, 3584, 512, "");
    DMA1(vO_, rsO, sO_, ldsI, 4480, 640, "");
    DMA1(vO_, rsO, sO_, ldsI, 5376, 768, "");
  }
  u32x4 rsW = srd((char*)(((size_t)ws + 8191) & ~(size_t)4095) + 512, WS_END); OPQ(rsW);
  const unsigned trb = (8 * g4 + q4) * 128 + pq * 8, trf = ((q4 >> 1) | ((g4 & 1) << 1)) * 32;
  const unsigned vF = r8 * 1024 + (lane & 7) * 16;
  const int r2 = (3 - (int)((size_t)w2 >> 7)) & 7;
  const int st = 8 * xq + r2, dt = jb - 28, db0 = 16 + 3 * dt;
#undef BLK
#define BLK(j) (db0 + ((j) == 2 ? 2 - 2 * (dt & 1) : ((j) ^ 0) + (dt & 1)))
  unsigned to = ldsI + q4 * 128 + ((g4 ^ (q4 & 2)) * 32) + pq * 8, vo[7], so_ = iw < 56 ? WS_WO + (iw >> 2) * 1024 + (iw & 3) * 32768 : 0x80000000u; OPQ(so_);
#pragma unroll
  for (int t = 0; t < 7; ++t) { const int k_ = 4 * t + g4, k7 = (k_ * 37) >> 8; vo[t] = k7 * (131072 - 896) + k_ * 128 + (n16 >> 3) * 16384 + (lane & 7) * 16; }
  u32x4 rs2 = srd(w2, 1792u * 4096u); OPQ(rs2);
  unsigned vW2[4];
#pragma unroll
  for (int i = 0; i < 4; ++i) vW2[i] = (8 * (r8 & 3) + 4 * (r8 >> 2)) * 4096 + st * 128 + ((lane & 7) ^ (2 * i)) * 16 + i * (4096 - 1024);
  int rot2 = 4 * dt + 2 * (st & 7) + 2; rot2 -= 14 * ((rot2 * 37) >> 9);
  bf16x8 fw2[14][4];
  unsigned ldsS = lds0 + S3B + w * 4096, ldsO = lds0 + WOB + w * 8192, ldsP = lds0 + WOB + 24576 + w * 6144, sWo, vWo = r8 * 16384 + (lane & 7) * 16, sWt;
  { const int k_ = BLK(w), k7 = (k_ * 37) >> 8; sWo = WS_WO + k7 * (131072 - 896) + k_ * 128; }
  unsigned fq, vFQ = WS_FQ + (lane < 56 ? lane : 55) * (4 * 4);
  asm volatile("" : "+v"(vW2[0]), "+v"(vW2[1]), "+v"(vW2[2]), "+v"(vW2[3]), "+v"(vFQ), "+v"(vWo)); asm volatile("" : "+s"(rot2), "+s"(ldsS), "+s"(ldsO), "+s"(ldsP), "+s"(sWo));
  if (w == 3) vWo = 0x40000000u; OPV(vWo);
  unsigned tS[4];
#pragma unroll
  for (int i = 0; i < 4; ++i) { tS[i] = ldsS + q4 * 1024 + g4 * 128 + pq * 8 + ((i ^ q4) * 32); OPV(tS[i]); }
  unsigned tO[4];
#pragma unroll
  for (int i = 0; i < 4; ++i) { tO[i] = ldsO + q4 * 1024 + g4 * 128 + pq * 8 + ((i ^ q4) * 32); OPV(tO[i]); }
  unsigned su[14], sw[14], sc_ = 14 * w + rot2; OPQ(sc_);
  { unsigned k_ = sc_ + (rot2 >= 14 ? -14 : 0); OPQ(k_); su[0] = k_ << 13; sw[0] = k_ << 17; OPQ(su[0]); OPQ(sw[0]); }
  { unsigned k_ = sc_ + (rot2 >= 13 ? -13 : 1); OPQ(k_); su[1] = k_ << 13; sw[1] = k_ << 17; OPQ(su[1]); OPQ(sw[1]); }
  { unsigned k_ = sc_ + (rot2 >= 12 ? -12 : 2); OPQ(k_); su[2] = k_ << 13; sw[2] = k_ << 17; OPQ(su[2]); OPQ(sw[2]); }
  { unsigned k_ = sc_ + (rot2 >= 11 ? -11 : 3); OPQ(k_); su[3] = k_ << 13; sw[3] = k_ << 17; OPQ(su[3]); OPQ(sw[3]); }
  { unsigned k_ = sc_ + (rot2 >= 10 ? -10 : 4); OPQ(k_); su[4] = k_ << 13; sw[4] = k_ << 17; OPQ(su[4]); OPQ(sw[4]); }
  { unsigned k_ = sc_ + (rot2 >= 9 ? -9 : 5); OPQ(k_); su[5] = k_ << 13; sw[5] = k_ << 17; OPQ(su[5]); OPQ(sw[5]); }
  { unsigned k_ = sc_ + (rot2 >= 8 ? -8 : 6); OPQ(k_); su[6] = k_ << 13; sw[6] = k_ << 17; OPQ(su[6]); OPQ(sw[6]); }
  { unsigned k_ = sc_ + (rot2 >= 7 ? -7 : 7); OPQ(k_); su[7] = k_ << 13; sw[7] = k_ << 17; OPQ(su[7]); OPQ(sw[7]); }
  { unsigned k_ = sc_ + (rot2 >= 6 ? -6 : 8); OPQ(k_); su[8] = k_ << 13; sw[8] = k_ << 17; OPQ(su[8]); OPQ(sw[8]); }
  { unsigned k_ = sc_ + (rot2 >= 5 ? -5 : 9); OPQ(k_); su[9] = k_ << 13; sw[9] = k_ << 17; OPQ(su[9]); OPQ(sw[9]); }
  { unsigned k_ = sc_ + (rot2 >= 4 ? -4 : 10); OPQ(k_); su[10] = k_ << 13; sw[10] = k_ << 17; OPQ(su[10]); OPQ(sw[10]); }
  { unsigned k_ = sc_ + (rot2 >= 3 ? -3 : 11); OPQ(k_); su[11] = k_ << 13; sw[11] = k_ << 17; OPQ(su[11]); OPQ(sw[11]); }
  { unsigned k_ = sc_ + (rot2 >= 2 ? -2 : 12); OPQ(k_); su[12] = k_ << 13; sw[12] = k_ << 17; OPQ(su[12]); OPQ(sw[12]); }
  { unsigned k_ = sc_ + (rot2 >= 1 ? -1 : 13); OPQ(k_); su[13] = k_ << 13; sw[13] = k_ << 17; OPQ(su[13]); OPQ(sw[13]); }
  unsigned vFU, vFO;
  { const int da = (db0 * 37) >> 8, db = ((db0 + 2) * 37) >> 8, l_ = lane < 56 ? lane : 55;
    vFU = WS_FU + ((l_ < 28 ? da * 128 + l_ : db * 128 + l_ - 28) + w * 28) * (4 * 4);
    vFO = WS_FO + ((2 * st + (w >> 1)) * 8 + (lane & 7)) * (4 * 16);
    asm volatile("" : "+v"(vFU), "+v"(vFO)); }
  unsigned vFU1 = WS_FU + (512 + ((db0 * 37) >> 8) * 128 + w * 28 + (lane < 28 ? lane : 27)) * (4 * 4), fu2; OPV(vFU1);
  unsigned vFj[4], so0 = WS_OT + (4 * st + w) * 16384, so1 = so0 + 8192;
#pragma unroll
  for (int j = 0; j < 3; ++j) { const int k_ = BLK(j), k7 = (k_ * 37) >> 8; vFj[j] = vF + (WS_UT + k7 * (56 * 8192 - 896) + k_ * 128); OPV(vFj[j]); }
  asm volatile("" : "+s"(so0), "+s"(so1));
  const int tau = 2 * (n16 & 7) + (n16 >> 3);
  unsigned rwd = ldsS + vL16, rr = lds0 + S3B + w * 1024 + (16 * g4 + tau) * 16, ro = lds0 + WOB + vL16;
  unsigned xo = ldsS + 34816 + vL16, braw, xt = ldsS + 34816 + tau * 128 + (((g4 >> 1) ^ (n16 & 7)) * 16) + (g4 & 1) * 8, vB2 = (64 * st + 16 * w + tau) * 2;
  unsigned xs = xt ^ (32 * 0);
  asm volatile("" : "+v"(rwd), "+v"(rr), "+v"(ro), "+v"(xt), "+v"(xs), "+v"(xo), "+v"(vB2));
  unsigned vXa, vXb;
  { const int ga = (lane & 7) ^ (r8 >> 1), gb = ga ^ 4, r_ = (64 * st + 16 * w + r8) * 896;
    vXa = ga < 4 ? r_ + (db0 + (dt & 1)) * 32 + ga * 16 : ga < 6 ? r_ + BLK(2) * 32 + (ga - 4) * 16 : 0x40000000;
    vXb = (gb < 4 ? r_ + (db0 + (dt & 1)) * 32 + gb * 16 : gb < 6 ? r_ + BLK(2) * 32 + (gb - 4) * 16 : 0x40000000) + (8 * 896 - 1024); OPV(vXa); OPV(vXb); }
  asm volatile("s_waitcnt lgkmcnt(0)" : "+s"(nonce) :: "memory"); nonce += 1u;
  asm volatile("s_waitcnt vmcnt(6)"
      :  :  : "memory"); SB();
  { const bf16x8 q_ = tr8u(to + 0, 512);
    HS16(vo[0], so_, 0, __builtin_bit_cast(u32x4, q_)); }
  asm volatile("s_waitcnt vmcnt(6)"
      :  :  : "memory"); SB();
  { const bf16x8 q_ = tr8u(to + 1024, 512);
    HS16(vo[1], so_, 0, __builtin_bit_cast(u32x4, q_)); }
  asm volatile("s_waitcnt vmcnt(6)"
      :  :  : "memory"); SB();
  { const bf16x8 q_ = tr8u(to + 2048, 512);
    HS16(vo[2], so_, 0, __builtin_bit_cast(u32x4, q_)); }
  asm volatile("s_waitcnt vmcnt(6)"
      :  :  : "memory"); SB();
  { const bf16x8 q_ = tr8u(to + 3072, 512);
    HS16(vo[3], so_, 0, __builtin_bit_cast(u32x4, q_)); }
  asm volatile("s_waitcnt vmcnt(6)"
      :  :  : "memory"); SB();
  { const bf16x8 q_ = tr8u(to + 4096, 512);
    HS16(vo[4], so_, 0, __builtin_bit_cast(u32x4, q_)); }
  asm volatile("s_waitcnt vmcnt(6)"
      :  :  : "memory"); SB();
  { const bf16x8 q_ = tr8u(to + 5120, 512);
    HS16(vo[5], so_, 0, __builtin_bit_cast(u32x4, q_)); }
  asm volatile("s_waitcnt vmcnt(6)"
      :  :  : "memory"); SB();
  { const bf16x8 q_ = tr8u(to + 6144, 512);
    HS16(vo[6], so_, 0, __builtin_bit_cast(u32x4, q_)); }
  for (int k = 0; k < 0; ++k) __builtin_amdgcn_s_sleep(4);
  const unsigned vT = (lane & 7) == r2 ? 0x40000000u : (448 * w + 8 * dt + r8) * 4096 + (8 * xq + (lane & 7)) * 128; const int k0 = (2 * (lane & 7)) % 14;
  { const unsigned v_ = vT + (k0 + 0 >= 14 ? k0 + 0 - 14 : k0 + 0) * 131072; TOUCH(v_, rs2, 0u, 0); }
  { const unsigned v_ = vT + (k0 + 4 >= 14 ? k0 + 4 - 14 : k0 + 4) * 131072; TOUCH(v_, rs2, 0u, 0); }
  asm volatile("s_waitcnt vmcnt(1)"
      :  :  : "memory"); SB();
  { const unsigned v_ = vT + (k0 + 8 >= 14 ? k0 + 8 - 14 : k0 + 8) * 131072; TOUCH(v_, rs2, 0u, 0); }
  asm volatile("s_waitcnt vmcnt(3)"
      :  :  : "memory"); SB();
  STFQ(WS_FQ + iw * (4 * 4));
  STAMP(4);
  asm volatile("s_waitcnt vmcnt(1)"
      :  :  : "memory"); SB();
  { const unsigned v_ = vT + (k0 + 12 >= 14 ? k0 + 12 - 14 : k0 + 12) * 131072; TOUCH(v_, rs2, 0u, 0); }
  asm volatile("s_waitcnt vmcnt(1)"
      :  :  : "memory"); SB();
  { const unsigned v_ = vT + (k0 + 1 >= 14 ? k0 + 1 - 14 : k0 + 1) * 131072; TOUCH(v_, rs2, 0u, 0); }
  asm volatile("s_waitcnt vmcnt(1)"
      :  :  : "memory"); SB();
  { const unsigned v_ = vT + (k0 + 5 >= 14 ? k0 + 5 - 14 : k0 + 5) * 131072; TOUCH(v_, rs2, 0u, 0); }
  asm volatile("s_waitcnt vmcnt(1)"
      :  :  : "memory"); SB();
  { const unsigned v_ = vT + (k0 + 9 >= 14 ? k0 + 9 - 14 : k0 + 9) * 131072; TOUCH(v_, rs2, 0u, 0); }
  asm volatile("s_waitcnt vmcnt(1)"
      :  :  : "memory"); SB();
  { const unsigned v_ = vT + (k0 + 13 >= 14 ? k0 + 13 - 14 : k0 + 13) * 131072; TOUCH(v_, rs2, 0u, 0); }
  asm volatile("s_waitcnt vmcnt(1)"
      :  :  : "memory"); SB();
  { const unsigned v_ = vT + (k0 + 2 >= 14 ? k0 + 2 - 14 : k0 + 2) * 131072; TOUCH(v_, rs2, 0u, 0); }
  asm volatile("s_waitcnt vmcnt(1)"
      :  :  : "memory"); SB();
  { const unsigned v_ = vT + (k0 + 6 >= 14 ? k0 + 6 - 14 : k0 + 6) * 131072; TOUCH(v_, rs2, 0u, 0); }
  STAMP(3);
  asm volatile("s_add_u32 m0, %0, 0\n\ts_nop 0\n\tbuffer_load_dwordx4 %1, %2, %3 offen offset:0  lds\n\tbuffer_load_dwordx4 %4, %2, %3 offen offset:1024  lds\n\tbuffer_load_dwordx4 %5, %2, %3 offen offset:2048  lds\n\tbuffer_load_dwordx4 %6, %2, %3 offen offset:3072  lds\n\ts_add_u32 m0, %0, 4096\n\ts_nop 0\n\tbuffer_load_dwordx4 %1, %2, %7 offen offset:0  lds\n\tbuffer_load_dwordx4 %4, %2, %7 offen offset:1024  lds\n\tbuffer_load_dwordx4 %5, %2, %7 offen offset:2048  lds\n\tbuffer_load_dwordx4 %6, %2, %7 offen offset:3072  lds\n\ts_add_u32 m0, %8, 18432\n\ts_nop 0\n\tbuffer_load_dwordx4 %1, %2, %9 offen offset:0  lds\n\tbuffer_load_dwordx4 %4, %2, %9 offen offset:1024  lds\n\tbuffer_load_dwordx4 %5, %2, %9 offen offset:2048  lds\n\tbuffer_load_dwordx4 %6, %2, %9 offen offset:3072  lds\n\ts_add_u32 m0, %8, 34816\n\ts_nop 0\n\tbuffer_load_dwordx4 %1, %2, %10 offen offset:0  lds\n\tbuffer_load_dwordx4 %4, %2, %10 offen offset:1024  lds\n\tbuffer_load_dwordx4 %5, %2, %10 offen offset:2048  lds\n\tbuffer_load_dwordx4 %6, %2, %10 offen offset:3072  lds\n\ts_waitcnt vmcnt(12)"
      :  : "s"(ldsO), "v"(vW2[0]), "s"(rs2), "s"(sw[0]), "v"(vW2[1]), "v"(vW2[2]), "v"(vW2[3]), "s"(sw[1]), "s"(ldsS), "s"(sw[2]), "s"(sw[3]) : "memory", "scc"); SB();
  fw2[0][0] = tr8u(tO[0] + 0, 512);
  fw2[0][1] = tr8u(tO[1] + 0, 512);
  fw2[0][2] = tr8u(tO[2] + 0, 512);
  fw2[0][3] = tr8u(tO[3] + 0, 512);
  asm volatile("s_add_u32 m0, %0, 0\n\ts_nop 0\n\tbuffer_load_dwordx4 %1, %2, %3 offen offset:0  lds\n\tbuffer_load_dwordx4 %4, %2, %3 offen offset:1024  lds\n\tbuffer_load_dwordx4 %5, %2, %3 offen offset:2048  lds\n\tbuffer_load_dwordx4 %6, %2, %3 offen offset:3072  lds\n\ts_waitcnt vmcnt(12)"
      :  : "s"(ldsS), "v"(vW2[0]), "s"(rs2), "s"(sw[4]), "v"(vW2[1]), "v"(vW2[2]), "v"(vW2[3]) : "memory", "scc"); SB();
  fw2[1][0] = tr8u(tO[0] + 4096, 512);
  fw2[1][1] = tr8u(tO[1] + 4096, 512);
  fw2[1][2] = tr8u(tO[2] + 4096, 512);
  fw2[1][3] = tr8u(tO[3] + 4096, 512);
  WAITL(8);
  asm volatile("s_add_u32 m0, %0, 0\n\ts_nop 0\n\tbuffer_load_dwordx4 %1, %2, %3 offen offset:0  lds\n\tbuffer_load_dwordx4 %4, %2, %3 offen offset:1024  lds\n\tbuffer_load_dwordx4 %5, %2, %3 offen offset:2048  lds\n\tbuffer_load_dwordx4 %6, %2, %3 offen offset:3072  lds\n\ts_waitcnt vmcnt(12)"
      :  : "s"(ldsO), "v"(vW2[0]), "s"(rs2), "s"(sw[5]), "v"(vW2[1]), "v"(vW2[2]), "v"(vW2[3]) : "memory", "scc"); SB();
  fw2[2][0] = tr8u(tS[0] + 18432, 512);
  fw2[2][1] = tr8u(tS[1] + 18432, 512);
  fw2[2][2] = tr8u(tS[2] + 18432, 512);
  fw2[2][3] = tr8u(tS[3] + 18432, 512);
  WAITL(8);
  asm volatile("s_add_u32 m0, %0, 4096\n\ts_nop 0\n\tbuffer_load_dwordx4 %1, %2, %3 offen offset:0  lds\n\tbuffer_load_dwordx4 %4, %2, %3 offen offset:1024  lds\n\tbuffer_load_dwordx4 %5, %2, %3 offen offset:2048  lds\n\tbuffer_load_dwordx4 %6, %2, %3 offen offset:3072  lds\n\ts_waitcnt vmcnt(12)"
      :  : "s"(ldsO), "v"(vW2[0]), "s"(rs2), "s"(sw[6]), "v"(vW2[1]), "v"(vW2[2]), "v"(vW2[3]) : "memory", "scc"); SB();
  fw2[3][0] = tr8u(tS[0] + 34816, 512);
  fw2[3][1] = tr8u(tS[1] + 34816, 512);
  fw2[3][2] = tr8u(tS[2] + 34816, 512);
  fw2[3][3] = tr8u(tS[3] + 34816, 512);
  WAITL(8);
  asm volatile("s_add_u32 m0, %0, 18432\n\ts_nop 0\n\tbuffer_load_dwordx4 %1, %2, %3 offen offset:0  lds\n\tbuffer_load_dwordx4 %4, %2, %3 offen offset:1024  lds\n\tbuffer_load_dwordx4 %5, %2, %3 offen offset:2048  lds\n\tbuffer_load_dwordx4 %6, %2, %3 offen offset:3072  lds\n\ts_waitcnt vmcnt(12)"
      :  : "s"(ldsS), "v"(vW2[0]), "s"(rs2), "s"(sw[7]), "v"(vW2[1]), "v"(vW2[2]), "v"(vW2[3]) : "memory", "scc"); SB();
  fw2[4][0] = tr8u(tS[0] + 0, 512);
  fw2[4][1] = tr8u(tS[1] + 0, 512);
  fw2[4][2] = tr8u(tS[2] + 0, 512);
  fw2[4][3] = tr8u(tS[3] + 0, 512);
  WAITL(8);
  asm volatile("s_add_u32 m0, %0, 34816\n\ts_nop 0\n\tbuffer_load_dwordx4 %1, %2, %3 offen offset:0  lds\n\tbuffer_load_dwordx4 %4, %2, %3 offen offset:1024  lds\n\tbuffer_load_dwordx4 %5, %2, %3 offen offset:2048  lds\n\tbuffer_load_dwordx4 %6, %2, %3 offen offset:3072  lds\n\ts_waitcnt vmcnt(12)"
      :  : "s"(ldsS), "v"(vW2[0]), "s"(rs2), "s"(sw[8]), "v"(vW2[1]), "v"(vW2[2]), "v"(vW2[3]) : "memory", "scc"); SB();
  fw2[5][0] = tr8u(tO[0] + 0, 512);
  fw2[5][1] = tr8u(tO[1] + 0, 512);
  fw2[5][2] = tr8u(tO[2] + 0, 512);
  fw2[5][3] = tr8u(tO[3] + 0, 512);
  WAITL(8);
  asm volatile("s_add_u32 m0, %0, 0\n\ts_nop 0\n\tbuffer_load_dwordx4 %1, %2, %3 offen offset:0  lds\n\tbuffer_load_dwordx4 %4, %2, %3 offen offset:1024  lds\n\tbuffer_load_dwordx4 %5, %2, %3 offen offset:2048  lds\n\tbuffer_load_dwordx4 %6, %2, %3 offen offset:3072  lds\n\ts_waitcnt vmcnt(12)"
      :  : "s"(ldsS), "v"(vW2[0]), "s"(rs2), "s"(sw[9]), "v"(vW2[1]), "v"(vW2[2]), "v"(vW2[3]) : "memory", "scc"); SB();
  fw2[6][0] = tr8u(tO[0] + 4096, 512);
  fw2[6][1] = tr8u(tO[1] + 4096, 512);
  fw2[6][2] = tr8u(tO[2] + 4096, 512);
  fw2[6][3] = tr8u(tO[3] + 4096, 512);
  WAITL(8);
  asm volatile("s_add_u32 m0, %0, 0\n\ts_nop 0\n\tbuffer_load_dwordx4 %1, %2, %3 offen offset:0  lds\n\tbuffer_load_dwordx4 %4, %2, %3 offen offset:1024  lds\n\tbuffer_load_dwordx4 %5, %2, %3 offen offset:2048  lds\n\tbuffer_load_dwordx4 %6, %2, %3 offen offset:3072  lds\n\ts_waitcnt vmcnt(12)"
      :  : "s"(ldsO), "v"(vW2[0]), "s"(rs2), "s"(sw[10]), "v"(vW2[1]), "v"(vW2[2]), "v"(vW2[3]) : "memory", "scc"); SB();
  fw2[7][0] = tr8u(tS[0] + 18432, 512);
  fw2[7][1] = tr8u(tS[1] + 18432, 512);
  fw2[7][2] = tr8u(tS[2] + 18432, 512);
  fw2[7][3] = tr8u(tS[3] + 18432, 512);
  WAITL(0);
  asm volatile("s_add_u32 m0, %0, 18432\n\ts_nop 0\n\tbuffer_load_dwordx4 %1, %2, %3 offen offset:0  lds\n\tbuffer_load_dwordx4 %4, %2, %3 offen offset:1024  lds\n\tbuffer_load_dwordx4 %5, %2, %3 offen offset:2048  lds\n\tbuffer_load_dwordx4 %6, %2, %3 offen offset:3072  lds\n\ts_waitcnt vmcnt(12)"
      :  : "s"(ldsS), "v"(vW2[0]), "s"(rs2), "s"(sw[11]), "v"(vW2[1]), "v"(vW2[2]), "v"(vW2[3]) : "memory", "scc"); SB();
  fw2[8][0] = tr8u(tS[0] + 34816, 512);
  fw2[8][1] = tr8u(tS[1] + 34816, 512);
  fw2[8][2] = tr8u(tS[2] + 34816, 512);
  fw2[8][3] = tr8u(tS[3] + 34816, 512);
  WAITL(0);
  asm volatile("s_add_u32 m0, %0, 34816\n\ts_nop 0\n\tbuffer_load_dwordx4 %1, %2, %3 offen offset:0  lds\n\tbuffer_load_dwordx4 %4, %2, %3 offen offset:1024  lds\n\tbuffer_load_dwordx4 %5, %2, %3 offen offset:2048  lds\n\tbuffer_load_dwordx4 %6, %2, %3 offen offset:3072  lds\n\ts_waitcnt vmcnt(12)"
      :  : "s"(ldsS), "v"(vW2[0]), "s"(rs2), "s"(sw[12]), "v"(vW2[1]), "v"(vW2[2]), "v"(vW2[3]) : "memory", "scc"); SB();
  fw2[9][0] = tr8u(tS[0] + 0, 512);
  fw2[9][1] = tr8u(tS[1] + 0, 512);
  fw2[9][2] = tr8u(tS[2] + 0, 512);
  fw2[9][3] = tr8u(tS[3] + 0, 512);
  WAITL(0);
  asm volatile("s_add_u32 m0, %0, 0\n\ts_nop 0\n\tbuffer_load_dwordx4 %1, %2, %3 offen offset:0  lds\n\tbuffer_load_dwordx4 %4, %2, %3 offen offset:1024  lds\n\tbuffer_load_dwordx4 %5, %2, %3 offen offset:2048  lds\n\tbuffer_load_dwordx4 %6, %2, %3 offen offset:3072  lds\n\ts_waitcnt vmcnt(12)"
      :  : "s"(ldsS), "v"(vW2[0]), "s"(rs2), "s"(sw[13]), "v"(vW2[1]), "v"(vW2[2]), "v"(vW2[3]) : "memory", "scc"); SB();
  fw2[10][0] = tr8u(tO[0] + 0, 512);
  fw2[10][1] = tr8u(tO[1] + 0, 512);
  fw2[10][2] = tr8u(tO[2] + 0, 512);
  fw2[10][3] = tr8u(tO[3] + 0, 512);
  asm volatile("s_waitcnt vmcnt(8)"
      :  :  : "memory"); SB();
  fw2[11][0] = tr8u(tS[0] + 18432, 512);
  fw2[11][1] = tr8u(tS[1] + 18432, 512);
  fw2[11][2] = tr8u(tS[2] + 18432, 512);
  fw2[11][3] = tr8u(tS[3] + 18432, 512);
  asm volatile("s_waitcnt vmcnt(4)"
      :  :  : "memory"); SB();
  fw2[12][0] = tr8u(tS[0] + 34816, 512);
  fw2[12][1] = tr8u(tS[1] + 34816, 512);
  fw2[12][2] = tr8u(tS[2] + 34816, 512);
  fw2[12][3] = tr8u(tS[3] + 34816, 512);
  asm volatile("s_waitcnt vmcnt(0)"
      :  :  : "memory"); SB();
  fw2[13][0] = tr8u(tS[0] + 0, 512);
  fw2[13][1] = tr8u(tS[1] + 0, 512);
  fw2[13][2] = tr8u(tS[2] + 0, 512);
  fw2[13][3] = tr8u(tS[3] + 0, 512);
  poll0<16>(rsW, vFQ, nonce);
  asm volatile("s_add_u32 m0, %1, 8192\n\ts_add_u32 %0, %2, 8192"
      : "=&s"(sWt) : "s"(ldsP), "s"(sWo) : "memory", "scc"); SB();
  DMAW4(vWo, rsW, sWt);
  u32x4 rsX = srd(x, 2048u * 896u); OPQ(rsX);
  WAITL(8);
  asm volatile("s_add_u32 m0, %2, 34816\n\ts_nop 0\n\tbuffer_load_dwordx4 %3, %4, %5 offen offset:0  lds\n\tbuffer_load_dwordx4 %6, %4, %5 offen offset:1024  lds\n\tglobal_load_ushort %0, %7, %8 offset:0\n\ts_add_u32 m0, %9, 12288\n\ts_add_u32 %1, %10, 12288"
      : "=&v"(braw), "=&s"(sWt) : "s"(ldsS), "v"(vXa), "s"(rsX), "s"(0u), "v"(vXb), "v"(vB2), "s"(b2), "s"(ldsP), "s"(sWo) : "memory", "scc"); SB();
  DMAW2(vWo, rsW, sWt);
  asm volatile("s_add_u32 m0, %1, 0\n\ts_add_u32 %0, %2, 0"
      : "=&s"(sWt) : "s"(ldsO), "s"(sWo) : "memory", "scc"); SB();
  DMAW4(vWo, rsW, sWt);
  asm volatile("s_add_u32 m0, %1, 4096\n\ts_add_u32 %0, %2, 4096"
      : "=&s"(sWt) : "s"(ldsO), "s"(sWo) : "memory", "scc"); SB();
  DMAW4(vWo, rsW, sWt);
  STAMP(16);
  asm volatile("s_waitcnt vmcnt(0)"
      :  :  : "memory"); SB();
  BAR();
  STAMP(5);
  for (int k = 0; k < 0; ++k) __builtin_amdgcn_s_sleep(4);
  {
  unsigned fz[3], vZ0 = WS_FU + (w * 128 + (lane < 56 ? lane : 55)) * (4 * 4), vZ1 = vZ0 + 56 * (4 * 4), vZ2 = WS_FQ + lane * (4 * 4); OPV(vZ0); OPV(vZ1); OPV(vZ2);
  bf16x8 pu[14], fwo[56];
  f32x4 ac[2][4], as[4], py[4][4];
  float ye[4], ze[4], ge[16], xe[16], bias;
  u32x2 ov[4]; unsigned fo;
  u32x2 xv[4]; unsigned xta[4];
  if (w != xq) poll0<16>(rsW, vFU, nonce);
  BAR0();
  if (w == xq) poll0<16>(rsW, vFU, nonce);
  STAMP(17);
  asm volatile("buffer_load_dwordx4 %0, %10, %11, %12 offen offset:0 sc1\n\tbuffer_load_dwordx4 %1, %10, %11, %13 offen offset:0 sc1\n\tbuffer_load_dwordx4 %2, %10, %11, %14 offen offset:0 sc1\n\tbuffer_load_dwordx4 %3, %10, %11, %15 offen offset:0 sc1\n\tbuffer_load_dwordx4 %4, %10, %11, %16 offen offset:0 sc1\n\tbuffer_load_dwordx4 %5, %10, %11, %17 offen offset:0 sc1\n\tbuffer_load_dwordx4 %6, %10, %11, %18 offen offset:0 sc1\n\tbuffer_load_dwordx4 %7, %10, %11, %19 offen offset:0 sc1\n\tbuffer_load_dwordx4 %8, %10, %11, %20 offen offset:0 sc1\n\tbuffer_load_dwordx4 %9, %10, %11, %21 offen offset:0 sc1"
      : "=&v"(pu[0]), "=&v"(pu[1]), "=&v"(pu[2]), "=&v"(pu[3]), "=&v"(pu[4]), "=&v"(pu[5]), "=&v"(pu[6]), "=&v"(pu[7]), "=&v"(pu[8]), "=&v"(pu[9]) : "v"(vFj[0]), "s"(rsW), "s"(su[0]), "s"(su[1]), "s"(su[2]), "s"(su[3]), "s"(su[4]), "s"(su[5]), "s"(su[6]), "s"(su[7]), "s"(su[8]), "s"(su[9]) : "memory"); SB();
  STAMP(39);
  WAITVB(9);
  SB();
  asm volatile("v_mfma_f32_16x16x32_bf16 %0, %8, %9, 0\n\tbuffer_load_dwordx4 %1, %10, %11, %12 offen offset:0 sc1\n\tv_mfma_f32_16x16x32_bf16 %2, %8, %13, 0\n\tds_read_b128 %3, %14 offset:0\n\tv_mfma_f32_16x16x32_bf16 %4, %8, %15, 0\n\tbuffer_load_dwordx4 %5, %10, %11, %16 offen offset:0 sc1\n\tv_mfma_f32_16x16x32_bf16 %6, %8, %17, 0\n\tds_read_b128 %7, %14 offset:8192"
      : "=&v"(ac[0][0]), "=&v"(pu[10]), "=&v"(ac[0][1]), "=&a"(fwo[0]), "=&v"(ac[0][2]), "=&v"(pu[11]), "=&v"(ac[0][3]), "=&a"(fwo[1]) : "v"(pu[0]), "a"(fw2[0][0]), "v"(vFj[0]), "s"(rsW), "s"(su[10]), "a"(fw2[0][1]), "v"(ro), "a"(fw2[0][2]), "s"(su[11]), "a"(fw2[0][3]) : "memory"); SB();
  WAITVB(10);
  SB();
  asm volatile("v_mfma_f32_16x16x32_bf16 %0, %8, %9, %0\n\tbuffer_load_dwordx4 %1, %10, %11, %12 offen offset:0 sc1\n\tv_mfma_f32_16x16x32_bf16 %2, %8, %13, %2\n\tds_read_b128 %3, %14 offset:16384\n\tv_mfma_f32_16x16x32_bf16 %4, %8, %15, %4\n\tbuffer_load_dwordx4 %5, %10, %11, %16 offen offset:0 sc1\n\tv_mfma_f32_16x16x32_bf16 %6, %8, %17, %6\n\tds_read_b128 %7, %14 offset:1024"
      : "+v"(ac[0][0]), "=&v"(pu[12]), "+v"(ac[0][1]), "=&a"(fwo[2]), "+v"(ac[0][2]), "=&v"(pu[13]), "+v"(ac[0][3]), "=&a"(fwo[3]) : "v"(pu[1]), "a"(fw2[1][0]), "v"(vFj[0]), "s"(rsW), "s"(su[12]), "a"(fw2[1][1]), "v"(ro), "a"(fw2[1][2]), "s"(su[13]), "a"(fw2[1][3]) : "memory"); SB();
  WAITVB(11);
  SB();
  asm volatile("v_mfma_f32_16x16x32_bf16 %0, %3, %4, %0\n\tbuffer_load_dwordx4 %1, %5, %6, %7 offen offset:0 sc1\n\tv_mfma_f32_16x16x32_bf16 %2, %3, %8, %2"
      : "+v"(ac[0][0]), "=&v"(pu[0]), "+v"(ac[0][1]) : "v"(pu[2]), "a"(fw2[2][0]), "v"(vFj[1]), "s"(rsW), "s"(su[0]), "a"(fw2[2][1]) : "memory"); SB();
  WAITVB(25);
  asm volatile("v_lshlrev_b32 %0, 16, %5\n\tv_mfma_f32_16x16x32_bf16 %1, %6, %7, %1\n\tbuffer_load_dwordx4 %2, %8, %9, %10 offen offset:0 sc1\n\tv_mfma_f32_16x16x32_bf16 %3, %6, %11, %3\n\tv_xor_b32 %4, 0, %12"
      : "=&v"(bias), "+v"(ac[0][2]), "=&v"(pu[1]), "+v"(ac[0][3]), "=&v"(xta[0]) : "v"(braw), "v"(pu[2]), "a"(fw2[2][2]), "v"(vFj[1]), "s"(rsW), "s"(su[1]), "a"(fw2[2][3]), "v"(xs) : "memory"); SB();
  WAITVB(12);
  SB();
  asm volatile("v_mfma_f32_16x16x32_bf16 %0, %10, %11, %0\n\tbuffer_load_dwordx4 %1, %12, %13, %14 offen offset:0 sc1\n\tv_mfma_f32_16x16x32_bf16 %2, %10, %15, %2\n\tv_xor_b32 %3, 32, %16\n\tv_xor_b32 %4, 64, %17\n\tv_mfma_f32_16x16x32_bf16 %5, %10, %18, %5\n\tds_read_b64 %6, %19 offset:0\n\tds_read_b64 %7, %3 offset:0\n\tv_mfma_f32_16x16x32_bf16 %8, %10, %20, %8\n\tds_read_b64 %9, %4 offset:0"
      : "+v"(ac[0][0]), "=&v"(pu[2]), "+v"(ac[0][1]), "=&v"(xta[1]), "=&v"(xta[2]), "+v"(ac[0][2]), "=&v"(xv[0]), "=&v"(xv[1]), "+v"(ac[0][3]), "=&v"(xv[2]) : "v"(pu[3]), "a"(fw2[3][0]), "v"(vFj[1]), "s"(rsW), "s"(su[2]), "a"(fw2[3][1]), "v"(xs), "v"(xt), "a"(fw2[3][2]), "v"(xta[0]), "a"(fw2[3][3]) : "memory"); SB();
  WAITVB(12);
  SB();
  asm volatile("v_mfma_f32_16x16x32_bf16 %0, %7, %8, %0\n\tbuffer_load_dwordx4 %1, %9, %10, %11 offen offset:0 sc1\n\tv_mfma_f32_16x16x32_bf16 %2, %7, %12, %2\n\tds_read_b128 %3, %13 offset:9216\n\tv_mfma_f32_16x16x32_bf16 %4, %7, %14, %4\n\tv_mfma_f32_16x16x32_bf16 %5, %7, %15, %5\n\tds_read_b128 %6, %13 offset:17408"
      : "+v"(ac[0][0]), "=&v"(pu[3]), "+v"(ac[0][1]), "=&a"(fwo[4]), "+v"(ac[0][2]), "+v"(ac[0][3]), "=&a"(fwo[5]) : "v"(pu[4]), "a"(fw2[4][0]), "v"(vFj[1]), "s"(rsW), "s"(su[3]), "a"(fw2[4][1]), "v"(ro), "a"(fw2[4][2]), "a"(fw2[4][3]) : "memory"); SB();
  WAITVB(12);
  SB();
  asm volatile("v_mfma_f32_16x16x32_bf16 %0, %3, %4, %0\n\tbuffer_load_dwordx4 %1, %5, %6, %7 offen offset:0 sc1\n\tv_mfma_f32_16x16x32_bf16 %2, %3, %8, %2"
      : "+v"(ac[0][0]), "=&v"(pu[4]), "+v"(ac[0][1]) : "v"(pu[5]), "a"(fw2[5][0]), "v"(vFj[1]), "s"(rsW), "s"(su[4]), "a"(fw2[5][1]) : "memory"); SB();
  WAITL(2);
  asm volatile("v_lshlrev_b32 %0, 16, %6\n\tv_mfma_f32_16x16x32_bf16 %1, %7, %8, %1\n\tv_and_b32 %2, 0xffff0000, %6\n\tv_lshlrev_b32 %3, 16, %9\n\tv_mfma_f32_16x16x32_bf16 %4, %7, %10, %4\n\tv_and_b32 %5, 0xffff0000, %9"
      : "=&v"(xe[0]), "+v"(ac[0][2]), "=&v"(xe[1]), "=&v"(xe[2]), "+v"(ac[0][3]), "=&v"(xe[3]) : "v"(xv[0][0]), "v"(pu[5]), "a"(fw2[5][2]), "v"(xv[0][1]), "a"(fw2[5][3])); SB();
  WAITVB(12);
  SB();
  asm volatile("v_mfma_f32_16x16x32_bf16 %0, %10, %11, %0\n\tbuffer_load_dwordx4 %1, %12, %13, %14 offen offset:0 sc1\n\tv_mfma_f32_16x16x32_bf16 %2, %10, %15, %2\n\tv_lshlrev_b32 %3, 16, %16\n\tv_and_b32 %4, 0xffff0000, %16\n\tv_mfma_f32_16x16x32_bf16 %5, %10, %17, %5\n\tv_lshlrev_b32 %6, 16, %18\n\tv_and_b32 %7, 0xffff0000, %18\n\tv_mfma_f32_16x16x32_bf16 %8, %10, %19, %8\n\tv_lshlrev_b32 %9, 16, %20"
      : "+v"(ac[0][0]), "=&v"(pu[5]), "+v"(ac[0][1]), "=&v"(xe[4]), "=&v"(xe[5]), "+v"(ac[0][2]), "=&v"(xe[6]), "=&v"(xe[7]), "+v"(ac[0][3]), "=&v"(xe[8]) : "v"(pu[6]), "a"(fw2[6][0]), "v"(vFj[1]), "s"(rsW), "s"(su[5]), "a"(fw2[6][1]), "v"(xv[1][0]), "a"(fw2[6][2]), "v"(xv[1][1]), "a"(fw2[6][3]), "v"(xv[2][0]) : "memory"); SB();
  WAITVB(12);
  SB();
  asm volatile("v_mfma_f32_16x16x32_bf16 %0, %9, %10, %0\n\tbuffer_load_dwordx4 %1, %11, %12, %13 offen offset:0 sc1\n\tv_mfma_f32_16x16x32_bf16 %2, %9, %14, %2\n\tv_and_b32 %3, 0xffff0000, %15\n\tv_lshlrev_b32 %4, 16, %16\n\tv_mfma_f32_16x16x32_bf16 %5, %9, %17, %5\n\tds_read_b128 %6, %18 offset:2048\n\tv_and_b32 %7, 0xffff0000, %16\n\tv_mfma_f32_16x16x32_bf16 %8, %9, %19, %8"
      : "+v"(ac[0][0]), "=&v"(pu[6]), "+v"(ac[0][1]), "=&v"(xe[9]), "=&v"(xe[10]), "+v"(ac[0][2]), "=&a"(fwo[6]), "=&v"(xe[11]), "+v"(ac[0][3]) : "v"(pu[7]), "a"(fw2[7][0]), "v"(vFj[1]), "s"(rsW), "s"(su[6]), "a"(fw2[7][1]), "v"(xv[2][0]), "v"(xv[2][1]), "a"(fw2[7][2]), "v"(ro), "a"(fw2[7][3]) : "memory"); SB();
  WAITVB(12);
  SB();
  asm volatile("v_mfma_f32_16x16x32_bf16 %0, %7, %8, %0\n\tbuffer_load_dwordx4 %1, %9, %10, %11 offen offset:0 sc1\n\tv_mfma_f32_16x16x32_bf16 %2, %7, %12, %2\n\tds_read_b128 %3, %13 offset:10240\n\tv_mfma_f32_16x16x32_bf16 %4, %7, %14, %4\n\tv_mfma_f32_16x16x32_bf16 %5, %7, %15, %5\n\tds_read_b128 %6, %13 offset:18432"
      : "+v"(ac[0][0]), "=&v"(pu[7]), "+v"(ac[0][1]), "=&a"(fwo[7]), "+v"(ac[0][2]), "+v"(ac[0][3]), "=&a"(fwo[8]) : "v"(pu[8]), "a"(fw2[8][0]), "v"(vFj[1]), "s"(rsW), "s"(su[7]), "a"(fw2[8][1]), "v"(ro), "a"(fw2[8][2]), "a"(fw2[8][3]) : "memory"); SB();
  WAITVB(12);
  SB();
  asm volatile("v_mfma_f32_16x16x32_bf16 %0, %7, %8, %0\n\tbuffer_load_dwordx4 %1, %9, %10, %11 offen offset:0 sc1\n\tv_mfma_f32_16x16x32_bf16 %2, %7, %12, %2\n\tds_read_b128 %3, %13 offset:3072\n\tv_mfma_f32_16x16x32_bf16 %4, %7, %14, %4\n\tv_mfma_f32_16x16x32_bf16 %5, %7, %15, %5\n\tds_read_b128 %6, %13 offset:11264"
      : "+v"(ac[0][0]), "=&v"(pu[8]), "+v"(ac[0][1]), "=&a"(fwo[9]), "+v"(ac[0][2]), "+v"(ac[0][3]), "=&a"(fwo[10]) : "v"(pu[9]), "v"(fw2[9][0]), "v"(vFj[1]), "s"(rsW), "s"(su[8]), "v"(fw2[9][1]), "v"(ro), "v"(fw2[9][2]), "v"(fw2[9][3]) : "memory"); SB();
  WAITVB(12);
  SB();
  asm volatile("v_mfma_f32_16x16x32_bf16 %0, %7, %8, %0\n\tbuffer_load_dwordx4 %1, %9, %10, %11 offen offset:0 sc1\n\tv_mfma_f32_16x16x32_bf16 %2, %7, %12, %2\n\tds_read_b128 %3, %13 offset:19456\n\tv_mfma_f32_16x16x32_bf16 %4, %7, %14, %4\n\tv_mfma_f32_16x16x32_bf16 %5, %7, %15, %5\n\tds_read_b128 %6, %13 offset:4096"
      : "+v"(ac[0][0]), "=&v"(pu[9]), "+v"(ac[0][1]), "=&a"(fwo[11]), "+v"(ac[0][2]), "+v"(ac[0][3]), "=&a"(fwo[12]) : "v"(pu[10]), "v"(fw2[10][0]), "v"(vFj[1]), "s"(rsW), "s"(su[9]), "v"(fw2[10][1]), "v"(ro), "v"(fw2[10][2]), "v"(fw2[10][3]) : "memory"); SB();
  WAITVB(12);
  SB();
  asm volatile("v_mfma_f32_16x16x32_bf16 %0, %7, %8, %0\n\tbuffer_load_dwordx4 %1, %9, %10, %11 offen offset:0 sc1\n\tv_mfma_f32_16x16x32_bf16 %2, %7, %12, %2\n\tds_read_b128 %3, %13 offset:12288\n\tv_mfma_f32_16x16x32_bf16 %4, %7, %14, %4\n\tv_mfma_f32_16x16x32_bf16 %5, %7, %15, %5\n\tds_read_b128 %6, %13 offset:20480"
      : "+v"(ac[0][0]), "=&v"(pu[10]), "+v"(ac[0][1]), "=&a"(fwo[13]), "+v"(ac[0][2]), "+v"(ac[0][3]), "=&a"(fwo[14]) : "v"(pu[11]), "v"(fw2[11][0]), "v"(vFj[1]), "s"(rsW), "s"(su[10]), "v"(fw2[11][1]), "v"(ro), "v"(fw2[11][2]), "v"(fw2[11][3]) : "memory"); SB();
  WAITVB(12);
  SB();
  asm volatile("v_mfma_f32_16x16x32_bf16 %0, %5, %6, %0\n\tbuffer_load_dwordx4 %1, %7, %8, %9 offen offset:0 sc1\n\tv_mfma_f32_16x16x32_bf16 %2, %5, %10, %2\n\tds_read_b128 %3, %11 offset:5120\n\tv_mfma_f32_16x16x32_bf16 %4, %5, %12, %4"
      : "+v"(ac[0][0]), "=&v"(pu[11]), "+v"(ac[0][1]), "=&a"(fwo[15]), "+v"(ac[0][2]) : "v"(pu[12]), "v"(fw2[12][0]), "v"(vFj[1]), "s"(rsW), "s"(su[11]), "v"(fw2[12][1]), "v"(ro), "v"(fw2[12][2]) : "memory"); SB();
  u32x4 rsO_ = srd(out, 2048u * 896u); OPQ(rsO_);
  SB();
  asm volatile("v_mfma_f32_16x16x32_bf16 %0, %2, %3, %0\n\tds_read_b128 %1, %4 offset:13312"
      : "+v"(ac[0][3]), "=&a"(fwo[16]) : "v"(pu[12]), "v"(fw2[12][3]), "v"(ro) : "memory"); SB();
  WAITVB(12);
  SB();
  asm volatile("v_mfma_f32_16x16x32_bf16 %0, %7, %8, %0\n\tbuffer_load_dwordx4 %1, %9, %10, %11 offen offset:0 sc1\n\tv_mfma_f32_16x16x32_bf16 %2, %7, %12, %2\n\tds_read_b128 %3, %13 offset:21504\n\tv_mfma_f32_16x16x32_bf16 %4, %7, %14, %4\n\tv_mfma_f32_16x16x32_bf16 %5, %7, %15, %5\n\tds_read_b128 %6, %13 offset:6144"
      : "+v"(ac[0][0]), "=&v"(pu[12]), "+v"(ac[0][1]), "=&a"(fwo[17]), "+v"(ac[0][2]), "+v"(ac[0][3]), "=&a"(fwo[18]) : "v"(pu[13]), "v"(fw2[13][0]), "v"(vFj[1]), "s"(rsW), "s"(su[12]), "v"(fw2[13][1]), "v"(ro), "v"(fw2[13][2]), "v"(fw2[13][3]) : "memory"); SB();
  STAMP(40);
  WAITVB(12);
  SB();
  asm volatile("v_mfma_f32_16x16x32_bf16 %0, %7, %8, 0\n\tbuffer_load_dwordx4 %1, %9, %10, %11 offen offset:0 sc1\n\tv_mfma_f32_16x16x32_bf16 %2, %7, %12, 0\n\tbuffer_load_dword %3, %13, %10, 0 offen sc1\n\tv_mfma_f32_16x16x32_bf16 %4, %7, %14, 0\n\tds_read_b128 %5, %15 offset:14336\n\tv_mfma_f32_16x16x32_bf16 %6, %7, %16, 0"
      : "=&v"(ac[1][0]), "=&v"(pu[13]), "=&v"(ac[1][1]), "=&v"(fo), "=&v"(ac[1][2]), "=&a"(fwo[19]), "=&v"(ac[1][3]) : "v"(pu[0]), "a"(fw2[0][0]), "v"(vFj[1]), "s"(rsW), "s"(su[13]), "a"(fw2[0][1]), "v"(vFO), "a"(fw2[0][2]), "v"(ro), "a"(fw2[0][3]) : "memory"); SB();
  WAITVB(13);
  SB();
  asm volatile("v_mfma_f32_16x16x32_bf16 %0, %5, %6, %0\n\tbuffer_load_dwordx4 %1, %7, %8, %9 offen offset:0 sc1\n\tv_mfma_f32_16x16x32_bf16 %2, %5, %10, %2\n\tds_write_b128 %11, %12 offset:0\n\tv_mfma_f32_16x16x32_bf16 %3, %5, %13, %3\n\tv_mfma_f32_16x16x32_bf16 %4, %5, %14, %4"
      : "+v"(ac[1][0]), "=&v"(pu[0]), "+v"(ac[1][1]), "+v"(ac[1][2]), "+v"(ac[1][3]) : "v"(pu[1]), "a"(fw2[1][0]), "v"(vFj[2]), "s"(rsW), "s"(su[0]), "a"(fw2[1][1]), "v"(rwd), "v"(ac[0][0]), "a"(fw2[1][2]), "a"(fw2[1][3]) : "memory"); SB();
  WAITVB(13);
  SB();
  asm volatile("v_mfma_f32_16x16x32_bf16 %0, %5, %6, %0\n\tbuffer_load_dwordx4 %1, %7, %8, %9 offen offset:0 sc1\n\tv_mfma_f32_16x16x32_bf16 %2, %5, %10, %2\n\tds_write_b128 %11, %12 offset:1024\n\tv_mfma_f32_16x16x32_bf16 %3, %5, %13, %3\n\tv_mfma_f32_16x16x32_bf16 %4, %5, %14, %4"
      : "+v"(ac[1][0]), "=&v"(pu[1]), "+v"(ac[1][1]), "+v"(ac[1][2]), "+v"(ac[1][3]) : "v"(pu[2]), "a"(fw2[2][0]), "v"(vFj[2]), "s"(rsW), "s"(su[1]), "a"(fw2[2][1]), "v"(rwd), "v"(ac[0][1]), "a"(fw2[2][2]), "a"(fw2[2][3]) : "memory"); SB();
  WAITVB(13);
  SB();
  asm volatile("v_mfma_f32_16x16x32_bf16 %0, %5, %6, %0\n\tbuffer_load_dwordx4 %1, %7, %8, %9 offen offset:0 sc1\n\tv_mfma_f32_16x16x32_bf16 %2, %5, %10, %2\n\tds_write_b128 %11, %12 offset:2048\n\tv_mfma_f32_16x16x32_bf16 %3, %5, %13, %3\n\tv_mfma_f32_16x16x32_bf16 %4, %5, %14, %4"
      : "+v"(ac[1][0]), "=&v"(pu[2]), "+v"(ac[1][1]), "+v"(ac[1][2]), "+v"(ac[1][3]) : "v"(pu[3]), "a"(fw2[3][0]), "v"(vFj[2]), "s"(rsW), "s"(su[2]), "a"(fw2[3][1]), "v"(rwd), "v"(ac[0][2]), "a"(fw2[3][2]), "a"(fw2[3][3]) : "memory"); SB();
  WAITVB(13);
  SB();
  asm volatile("v_mfma_f32_16x16x32_bf16 %0, %5, %6, %0\n\tbuffer_load_dwordx4 %1, %7, %8, %9 offen offset:0 sc1\n\tv_mfma_f32_16x16x32_bf16 %2, %5, %10, %2\n\tds_write_b128 %11, %12 offset:3072\n\tv_mfma_f32_16x16x32_bf16 %3, %5, %13, %3\n\tv_mfma_f32_16x16x32_bf16 %4, %5, %14, %4"
      : "+v"(ac[1][0]), "=&v"(pu[3]), "+v"(ac[1][1]), "+v"(ac[1][2]), "+v"(ac[1][3]) : "v"(pu[4]), "a"(fw2[4][0]), "v"(vFj[2]), "s"(rsW), "s"(su[3]), "a"(fw2[4][1]), "v"(rwd), "v"(ac[0][3]), "a"(fw2[4][2]), "a"(fw2[4][3]) : "memory"); SB();
  WAITVB(13);
  SB();
  asm volatile("v_mfma_f32_16x16x32_bf16 %0, %3, %4, %0\n\tbuffer_load_dwordx4 %1, %5, %6, %7 offen offset:0 sc1\n\tv_mfma_f32_16x16x32_bf16 %2, %3, %8, %2"
      : "+v"(ac[1][0]), "=&v"(pu[4]), "+v"(ac[1][1]) : "v"(pu[5]), "a"(fw2[5][0]), "v"(vFj[2]), "s"(rsW), "s"(su[4]), "a"(fw2[5][1]) : "memory"); SB();
  WAITL(0);
  BAR0();
  SB();
  asm volatile("v_mfma_f32_16x16x32_bf16 %0, %5, %6, %0\n\tds_read_b128 %1, %7 offset:0\n\tds_read_b128 %2, %7 offset:4096\n\tv_mfma_f32_16x16x32_bf16 %3, %5, %8, %3\n\tds_read_b128 %4, %7 offset:8192"
      : "+v"(ac[1][2]), "=&v"(py[0][0]), "=&v"(py[0][1]), "+v"(ac[1][3]), "=&v"(py[0][2]) : "v"(pu[5]), "a"(fw2[5][2]), "v"(rr), "a"(fw2[5][3]) : "memory"); SB();
  WAITVB(13);
  SB();
  asm volatile("v_mfma_f32_16x16x32_bf16 %0, %9, %10, %0\n\tbuffer_load_dwordx4 %1, %11, %12, %13 offen offset:0 sc1\n\tv_mfma_f32_16x16x32_bf16 %2, %9, %14, %2\n\tbuffer_load_dword %3, %15, %12, 0 offen sc1\n\tbuffer_load_dword %4, %16, %12, 0 offen sc1\n\tbuffer_load_dword %5, %17, %12, 0 offen sc1\n\tv_mfma_f32_16x16x32_bf16 %6, %9, %18, %6\n\tds_read_b128 %7, %19 offset:12288\n\tv_mfma_f32_16x16x32_bf16 %8, %9, %20, %8"
      : "+v"(ac[1][0]), "=&v"(pu[5]), "+v"(ac[1][1]), "=&v"(fz[0]), "=&v"(fz[1]), "=&v"(fz[2]), "+v"(ac[1][2]), "=&v"(py[0][3]), "+v"(ac[1][3]) : "v"(pu[6]), "a"(fw2[6][0]), "v"(vFj[2]), "s"(rsW), "s"(su[5]), "a"(fw2[6][1]), "v"(vZ0), "v"(vZ1), "v"(vZ2), "a"(fw2[6][2]), "v"(rr), "a"(fw2[6][3]) : "memory"); SB();
  WAITVB(16);
  SB();
  asm volatile("v_mfma_f32_16x16x32_bf16 %0, %5, %6, %0\n\tbuffer_load_dwordx4 %1, %7, %8, %9 offen offset:0 sc1\n\tv_mfma_f32_16x16x32_bf16 %2, %5, %10, %2\n\tv_mfma_f32_16x16x32_bf16 %3, %5, %11, %3\n\tv_mfma_f32_16x16x32_bf16 %4, %5, %12, %4"
      : "+v"(ac[1][0]), "=&v"(pu[6]), "+v"(ac[1][1]), "+v"(ac[1][2]), "+v"(ac[1][3]) : "v"(pu[7]), "a"(fw2[7][0]), "v"(vFj[2]), "s"(rsW), "s"(su[6]), "a"(fw2[7][1]), "a"(fw2[7][2]), "a"(fw2[7][3]) : "memory"); SB();
  WAITL(0);
  SB();
  WAITVB(16);
  SB();
  asm volatile("v_mfma_f32_16x16x32_bf16 %0, %10, %11, %0\n\tbuffer_load_dwordx4 %1, %12, %13, %14 offen offset:0 sc1\n\tv_mfma_f32_16x16x32_bf16 %2, %10, %15, %2\n\tv_add_f32 %3, %16, %17\n\tv_add_f32 %4, %18, %17\n\tv_mfma_f32_16x16x32_bf16 %5, %10, %19, %5\n\tv_add_f32 %6, %20, %17\n\tv_add_f32 %7, %21, %17\n\tv_mfma_f32_16x16x32_bf16 %8, %10, %22, %8\n\tv_add_f32 %9, %23, %24"
      : "+v"(ac[1][0]), "=&v"(pu[7]), "+v"(ac[1][1]), "=&v"(ye[0]), "=&v"(ye[1]), "+v"(ac[1][2]), "=&v"(ye[2]), "=&v"(ye[3]), "+v"(ac[1][3]), "=&v"(ze[0]) : "v"(pu[8]), "a"(fw2[8][0]), "v"(vFj[2]), "s"(rsW), "s"(su[7]), "a"(fw2[8][1]), "v"(py[0][0][0]), "v"(bias), "v"(py[0][0][1]), "a"(fw2[8][2]), "v"(py[0][0][2]), "v"(py[0][0][3]), "a"(fw2[8][3]), "v"(py[0][1][0]), "v"(py[0][2][0]) : "memory"); SB();
  WAITVB(16);
  SB();
  asm volatile("v_mfma_f32_16x16x32_bf16 %0, %9, %10, %0\n\tbuffer_load_dwordx4 %1, %11, %12, %13 offen offset:0 sc1\n\tv_mfma_f32_16x16x32_bf16 %2, %9, %14, %2\n\tv_add_f32 %3, %15, %16\n\tv_add_f32 %4, %17, %18\n\tv_mfma_f32_16x16x32_bf16 %5, %9, %19, %5\n\tv_add_f32 %6, %20, %21\n\tv_add_f32 %7, %7, %22\n\tv_mfma_f32_16x16x32_bf16 %8, %9, %23, %8"
      : "+v"(ac[1][0]), "=&v"(pu[8]), "+v"(ac[1][1]), "=&v"(ze[1]), "=&v"(ze[2]), "+v"(ac[1][2]), "=&v"(ze[3]), "+v"(ye[0]), "+v"(ac[1][3]) : "v"(pu[9]), "v"(fw2[9][0]), "v"(vFj[2]), "s"(rsW), "s"(su[8]), "v"(fw2[9][1]), "v"(py[0][1][1]), "v"(py[0][2][1]), "v"(py[0][1][2]), "v"(py[0][2][2]), "v"(fw2[9][2]), "v"(py[0][1][3]), "v"(py[0][2][3]), "v"(py[0][3][0]), "v"(fw2[9][3]) : "memory"); SB();
  asm volatile("v_add_f32 %0, %0, %1"
      : "+v"(ye[1]) : "v"(py[0][3][1])); SB();
  WAITVB(16);
  SB();
  asm volatile("v_mfma_f32_16x16x32_bf16 %0, %9, %10, %0\n\tbuffer_load_dwordx4 %1, %11, %12, %13 offen offset:0 sc1\n\tv_mfma_f32_16x16x32_bf16 %2, %9, %14, %2\n\tv_add_f32 %3, %3, %15\n\tv_add_f32 %4, %4, %16\n\tv_mfma_f32_16x16x32_bf16 %5, %9, %17, %5\n\tv_add_f32 %6, %6, %18\n\tv_add_f32 %7, %7, %19\n\tv_mfma_f32_16x16x32_bf16 %8, %9, %20, %8\n\tv_add_f32 %3, %3, %21"
      : "+v"(ac[1][0]), "=&v"(pu[9]), "+v"(ac[1][1]), "+v"(ye[2]), "+v"(ye[3]), "+v"(ac[1][2]), "+v"(ye[0]), "+v"(ye[1]), "+v"(ac[1][3]) : "v"(pu[10]), "v"(fw2[10][0]), "v"(vFj[2]), "s"(rsW), "s"(su[9]), "v"(fw2[10][1]), "v"(py[0][3][2]), "v"(py[0][3][3]), "v"(fw2[10][2]), "v"(ze[0]), "v"(ze[1]), "v"(fw2[10][3]), "v"(ze[2]) : "memory"); SB();
  WAITVB(16);
  SB();
  asm volatile("v_mfma_f32_16x16x32_bf16 %0, %10, %11, %0\n\tbuffer_load_dwordx4 %1, %12, %13, %14 offen offset:0 sc1\n\tv_mfma_f32_16x16x32_bf16 %2, %10, %15, %2\n\tv_add_f32 %3, %3, %4\n\tv_mul_f32 %5, 0xbfb8aa3b, %16\n\tv_mfma_f32_16x16x32_bf16 %6, %10, %17, %6\n\tv_mul_f32 %7, 0xbfb8aa3b, %18\n\tv_mul_f32 %8, 0xbfb8aa3b, %19\n\tv_mfma_f32_16x16x32_bf16 %9, %10, %20, %9\n\tv_mul_f32 %4, 0xbfb8aa3b, %3"
      : "+v"(ac[1][0]), "=&v"(pu[10]), "+v"(ac[1][1]), "+v"(ye[3]), "+v"(ze[3]), "=&v"(ze[0]), "+v"(ac[1][2]), "=&v"(ze[1]), "=&v"(ze[2]), "+v"(ac[1][3]) : "v"(pu[11]), "v"(fw2[11][0]), "v"(vFj[2]), "s"(rsW), "s"(su[10]), "v"(fw2[11][1]), "v"(ye[0]), "v"(fw2[11][2]), "v"(ye[1]), "v"(ye[2]), "v"(fw2[11][3]) : "memory"); SB();
  WAITVB(16);
  SB();
  asm volatile("v_mfma_f32_16x16x32_bf16 %0, %7, %8, %0\n\tbuffer_load_dwordx4 %1, %9, %10, %11 offen offset:0 sc1\n\tv_mfma_f32_16x16x32_bf16 %2, %7, %12, %2\n\tv_exp_f32 %3, %3\n\tv_mfma_f32_16x16x32_bf16 %4, %7, %13, %4\n\tv_exp_f32 %5, %5\n\tv_mfma_f32_16x16x32_bf16 %6, %7, %14, %6"
      : "+v"(ac[1][0]), "=&v"(pu[11]), "+v"(ac[1][1]), "+v"(ze[0]), "+v"(ac[1][2]), "+v"(ze[1]), "+v"(ac[1][3]) : "v"(pu[12]), "v"(fw2[12][0]), "v"(vFj[2]), "s"(rsW), "s"(su[11]), "v"(fw2[12][1]), "v"(fw2[12][2]), "v"(fw2[12][3]) : "memory"); SB();
  WAITVB(16);
  SB();
  asm volatile("v_mfma_f32_16x16x32_bf16 %0, %9, %10, %0\n\tbuffer_load_dwordx4 %1, %11, %12, %13 offen offset:0 sc1\n\tv_mfma_f32_16x16x32_bf16 %2, %9, %14, %2\n\tv_exp_f32 %3, %3\n\tv_mfma_f32_16x16x32_bf16 %4, %9, %15, %4\n\tv_exp_f32 %5, %5\n\tv_mfma_f32_16x16x32_bf16 %6, %9, %16, %6\n\tv_add_f32 %7, 1.0, %7\n\tv_add_f32 %8, 1.0, %8"
      : "+v"(ac[1][0]), "=&v"(pu[12]), "+v"(ac[1][1]), "+v"(ze[2]), "+v"(ac[1][2]), "+v"(ze[3]), "+v"(ac[1][3]), "+v"(ze[0]), "+v"(ze[1]) : "v"(pu[13]), "v"(fw2[13][0]), "v"(vFj[2]), "s"(rsW), "s"(su[12]), "v"(fw2[13][1]), "v"(fw2[13][2]), "v"(fw2[13][3]) : "memory"); SB();
  STAMP(41);
  WAITVB(15);
  SB();
  asm volatile("v_mfma_f32_16x16x32_bf16 %0, %8, %9, 0\n\tbuffer_load_dwordx4 %1, %10, %11, %12 offen offset:0 sc1\n\tv_mfma_f32_16x16x32_bf16 %2, %8, %13, 0\n\tv_add_f32 %3, 1.0, %3\n\tv_add_f32 %4, 1.0, %4\n\tv_mfma_f32_16x16x32_bf16 %5, %8, %14, 0\n\tv_rcp_f32 %6, %6\n\tv_mfma_f32_16x16x32_bf16 %7, %8, %15, 0"
      : "=&v"(ac[0][0]), "=&v"(pu[13]), "=&v"(ac[0][1]), "+v"(ze[2]), "+v"(ze[3]), "=&v"(ac[0][2]), "+v"(ze[0]), "=&v"(ac[0][3]) : "v"(pu[0]), "a"(fw2[0][0]), "v"(vFj[2]), "s"(rsW), "s"(su[13]), "a"(fw2[0][1]), "a"(fw2[0][2]), "a"(fw2[0][3]) : "memory"); SB();
  WAITVB(15);
  SB();
  asm volatile("v_mfma_f32_16x16x32_bf16 %0, %1, %2, %0"
      : "+v"(ac[0][0]) : "v"(pu[1]), "a"(fw2[1][0])); SB();
  WAITV1(17, fo);
  if (__builtin_expect(__ballot(fo != nonce) != 0ull, 0)) poll1<4>(rsW, vFO, nonce);
  STAMP(18);
  asm volatile("buffer_load_dwordx4 %0, %6, %7, %8 offen offset:0 sc1\n\tv_mfma_f32_16x16x32_bf16 %1, %9, %10, %1\n\tds_write_b128 %11, %12 offset:18432\n\tv_mfma_f32_16x16x32_bf16 %2, %9, %13, %2\n\tv_rcp_f32 %3, %3\n\tv_mfma_f32_16x16x32_bf16 %4, %9, %14, %4\n\tds_read_b128 %5, %15 offset:22528"
      : "=&v"(pu[0]), "+v"(ac[0][1]), "+v"(ac[0][2]), "+v"(ze[1]), "+v"(ac[0][3]), "=&a"(fwo[20]) : "v"(vF), "s"(rsW), "s"(so0), "v"(pu[1]), "a"(fw2[1][1]), "v"(rwd), "v"(ac[1][0]), "a"(fw2[1][2]), "a"(fw2[1][3]), "v"(ro) : "memory"); SB();
  WAITVB(15);
  SB();
  asm volatile("v_mfma_f32_16x16x32_bf16 %0, %7, %8, %0\n\tbuffer_load_dwordx4 %1, %9, %10, %11 offen offset:128 sc1\n\tv_mfma_f32_16x16x32_bf16 %2, %7, %12, %2\n\tds_write_b128 %13, %14 offset:19456\n\tv_mfma_f32_16x16x32_bf16 %3, %7, %15, %3\n\tv_rcp_f32 %4, %4\n\tv_mfma_f32_16x16x32_bf16 %5, %7, %16, %5\n\tds_read_b128 %6, %17 offset:7168"
      : "+v"(ac[0][0]), "=&v"(pu[1]), "+v"(ac[0][1]), "+v"(ac[0][2]), "+v"(ze[2]), "+v"(ac[0][3]), "=&a"(fwo[21]) : "v"(pu[2]), "a"(fw2[2][0]), "v"(vF), "s"(rsW), "s"(so0), "a"(fw2[2][1]), "v"(rwd), "v"(ac[1][1]), "a"(fw2[2][2]), "a"(fw2[2][3]), "v"(ro) : "memory"); SB();
  WAITVB(15);
  SB();
  asm volatile("v_mfma_f32_16x16x32_bf16 %0, %7, %8, %0\n\tbuffer_load_dwordx4 %1, %9, %10, %11 offen offset:256 sc1\n\tv_mfma_f32_16x16x32_bf16 %2, %7, %12, %2\n\tds_write_b128 %13, %14 offset:20480\n\tv_mfma_f32_16x16x32_bf16 %3, %7, %15, %3\n\tv_rcp_f32 %4, %4\n\tv_mfma_f32_16x16x32_bf16 %5, %7, %16, %5\n\tv_mul_f32 %6, %17, %18"
      : "+v"(ac[0][0]), "=&v"(pu[2]), "+v"(ac[0][1]), "+v"(ac[0][2]), "+v"(ze[3]), "+v"(ac[0][3]), "=&v"(ge[0]) : "v"(pu[3]), "a"(fw2[3][0]), "v"(vF), "s"(rsW), "s"(so0), "a"(fw2[3][1]), "v"(rwd), "v"(ac[1][2]), "a"(fw2[3][2]), "a"(fw2[3][3]), "v"(ye[0]), "v"(ze[0]) : "memory"); SB();
  WAITVB(15);
  SB();
  asm volatile("v_mfma_f32_16x16x32_bf16 %0, %8, %9, %0\n\tbuffer_load_dwordx4 %1, %10, %11, %12 offen offset:384 sc1\n\tv_mfma_f32_16x16x32_bf16 %2, %8, %13, %2\n\tds_write_b128 %14, %15 offset:21504\n\tv_mfma_f32_16x16x32_bf16 %3, %8, %16, %3\n\tv_mul_f32 %4, %17, %18\n\tv_mul_f32 %5, %19, %20\n\tv_mfma_f32_16x16x32_bf16 %6, %8, %21, %6\n\tv_mul_f32 %7, %22, %23"
      : "+v"(ac[0][0]), "=&v"(pu[3]), "+v"(ac[0][1]), "+v"(ac[0][2]), "=&v"(ge[1]), "=&v"(ge[2]), "+v"(ac[0][3]), "=&v"(ge[3]) : "v"(pu[4]), "a"(fw2[4][0]), "v"(vF), "s"(rsW), "s"(so0), "a"(fw2[4][1]), "v"(rwd), "v"(ac[1][3]), "a"(fw2[4][2]), "v"(ye[1]), "v"(ze[1]), "v"(ye[2]), "v"(ze[2]), "a"(fw2[4][3]), "v"(ye[3]), "v"(ze[3]) : "memory"); SB();
  WAITVB(15);
  SB();
  asm volatile("v_mfma_f32_16x16x32_bf16 %0, %3, %4, %0\n\tbuffer_load_dwordx4 %1, %5, %6, %7 offen offset:512 sc1\n\tv_mfma_f32_16x16x32_bf16 %2, %3, %8, %2"
      : "+v"(ac[0][0]), "=&v"(pu[4]), "+v"(ac[0][1]) : "v"(pu[5]), "a"(fw2[5][0]), "v"(vF), "s"(rsW), "s"(so0), "a"(fw2[5][1]) : "memory"); SB();
  WAITL(0);
  BAR0();
  SB();
  asm volatile("v_mfma_f32_16x16x32_bf16 %0, %5, %6, %0\n\tds_read_b128 %1, %7 offset:18432\n\tds_read_b128 %2, %7 offset:22528\n\tv_mfma_f32_16x16x32_bf16 %3, %5, %8, %3\n\tds_read_b128 %4, %7 offset:26624"
      : "+v"(ac[0][2]), "=&v"(py[1][0]), "=&v"(py[1][1]), "+v"(ac[0][3]), "=&v"(py[1][2]) : "v"(pu[5]), "a"(fw2[5][2]), "v"(rr), "a"(fw2[5][3]) : "memory"); SB();
  WAITVB(12);
  SB();
  asm volatile("v_mfma_f32_16x16x32_bf16 %0, %8, %9, %0\n\tbuffer_load_dwordx4 %1, %10, %11, %12 offen offset:640 sc1\n\tv_mfma_f32_16x16x32_bf16 %2, %8, %13, %2\n\tds_read_b128 %3, %14 offset:30720\n\tds_read_b128 %4, %15 offset:15360\n\tv_mfma_f32_16x16x32_bf16 %5, %8, %16, %5\n\tv_mfma_f32_16x16x32_bf16 %6, %8, %17, %6\n\tds_read_b128 %7, %15 offset:23552"
      : "+v"(ac[0][0]), "=&v"(pu[5]), "+v"(ac[0][1]), "=&v"(py[1][3]), "=&a"(fwo[22]), "+v"(ac[0][2]), "+v"(ac[0][3]), "=&a"(fwo[23]) : "v"(pu[6]), "a"(fw2[6][0]), "v"(vF), "s"(rsW), "s"(so0), "a"(fw2[6][1]), "v"(rr), "v"(ro), "a"(fw2[6][2]), "a"(fw2[6][3]) : "memory"); SB();
  WAITVB(12);
  SB();
  asm volatile("v_mfma_f32_16x16x32_bf16 %0, %5, %6, %0\n\tbuffer_load_dwordx4 %1, %7, %8, %9 offen offset:768 sc1\n\tv_mfma_f32_16x16x32_bf16 %2, %5, %10, %2\n\tds_read_b128 %3, %11 offset:32768\n\tv_mfma_f32_16x16x32_bf16 %4, %5, %12, %4"
      : "+v"(ac[0][0]), "=&v"(pu[6]), "+v"(ac[0][1]), "=&a"(fwo[24]), "+v"(ac[0][2]) : "v"(pu[7]), "a"(fw2[7][0]), "v"(vF), "s"(rsW), "s"(so0), "a"(fw2[7][1]), "v"(ro), "a"(fw2[7][2]) : "memory"); SB();
  WAITL(3);
  asm volatile("v_add_f32 %0, %3, %4\n\tv_mfma_f32_16x16x32_bf16 %1, %5, %6, %1\n\tv_add_f32 %2, %7, %4"
      : "=&v"(ye[0]), "+v"(ac[0][3]), "=&v"(ye[1]) : "v"(py[1][0][0]), "v"(bias), "v"(pu[7]), "a"(fw2[7][3]), "v"(py[1][0][1])); SB();
  WAITVB(12);
  SB();
  asm volatile("v_mfma_f32_16x16x32_bf16 %0, %9, %10, %0\n\tbuffer_load_dwordx4 %1, %11, %12, %13 offen offset:0 sc1\n\tv_mfma_f32_16x16x32_bf16 %2, %9, %14, %2\n\tv_add_f32 %3, %15, %16\n\tv_add_f32 %4, %17, %16\n\tv_mfma_f32_16x16x32_bf16 %5, %9, %18, %5\n\tv_add_f32 %6, %19, %20\n\tv_add_f32 %7, %21, %22\n\tv_mfma_f32_16x16x32_bf16 %8, %9, %23, %8"
      : "+v"(ac[0][0]), "=&v"(pu[7]), "+v"(ac[0][1]), "=&v"(ye[2]), "=&v"(ye[3]), "+v"(ac[0][2]), "=&v"(ze[0]), "=&v"(ze[1]), "+v"(ac[0][3]) : "v"(pu[8]), "a"(fw2[8][0]), "v"(vF), "s"(rsW), "s"(so1), "a"(fw2[8][1]), "v"(py[1][0][2]), "v"(bias), "v"(py[1][0][3]), "a"(fw2[8][2]), "v"(py[1][1][0]), "v"(py[1][2][0]), "v"(py[1][1][1]), "v"(py[1][2][1]), "a"(fw2[8][3]) : "memory"); SB();
  asm volatile("v_add_f32 %0, %1, %2"
      : "=&v"(ze[2]) : "v"(py[1][1][2]), "v"(py[1][2][2])); SB();
  WAITVB(12);
  SB();
  asm volatile("v_mfma_f32_16x16x32_bf16 %0, %9, %10, %0\n\tbuffer_load_dwordx4 %1, %11, %12, %13 offen offset:128 sc1\n\tv_mfma_f32_16x16x32_bf16 %2, %9, %14, %2\n\tv_add_f32 %3, %15, %16\n\tv_add_f32 %4, %4, %17\n\tv_mfma_f32_16x16x32_bf16 %5, %9, %18, %5\n\tv_add_f32 %6, %6, %19\n\tv_add_f32 %7, %7, %20\n\tv_mfma_f32_16x16x32_bf16 %8, %9, %21, %8"
      : "+v"(ac[0][0]), "=&v"(pu[8]), "+v"(ac[0][1]), "=&v"(ze[3]), "+v"(ye[0]), "+v"(ac[0][2]), "+v"(ye[1]), "+v"(ye[2]), "+v"(ac[0][3]) : "v"(pu[9]), "v"(fw2[9][0]), "v"(vF), "s"(rsW), "s"(so1), "v"(fw2[9][1]), "v"(py[1][1][3]), "v"(py[1][2][3]), "v"(py[1][3][0]), "v"(fw2[9][2]), "v"(py[1][3][1]), "v"(py[1][3][2]), "v"(fw2[9][3]) : "memory"); SB();
  asm volatile("v_add_f32 %0, %0, %1"
      : "+v"(ye[3]) : "v"(py[1][3][3])); SB();
  WAITVB(12);
  SB();
  asm volatile("v_mfma_f32_16x16x32_bf16 %0, %10, %11, %0\n\tbuffer_load_dwordx4 %1, %12, %13, %14 offen offset:256 sc1\n\tv_mfma_f32_16x16x32_bf16 %2, %10, %15, %2\n\tv_add_f32 %3, %3, %4\n\tv_add_f32 %5, %5, %16\n\tv_mfma_f32_16x16x32_bf16 %6, %10, %17, %6\n\tv_add_f32 %7, %7, %18\n\tv_add_f32 %8, %8, %19\n\tv_mfma_f32_16x16x32_bf16 %9, %10, %20, %9\n\tv_mul_f32 %4, 0xbfb8aa3b, %3"
      : "+v"(ac[0][0]), "=&v"(pu[9]), "+v"(ac[0][1]), "+v"(ye[0]), "+v"(ze[0]), "+v"(ye[1]), "+v"(ac[0][2]), "+v"(ye[2]), "+v"(ye[3]), "+v"(ac[0][3]) : "v"(pu[10]), "v"(fw2[10][0]), "v"(vF), "s"(rsW), "s"(so1), "v"(fw2[10][1]), "v"(ze[1]), "v"(fw2[10][2]), "v"(ze[2]), "v"(ze[3]), "v"(fw2[10][3]) : "memory"); SB();
  WAITVB(12);
  SB();
  asm volatile("v_mfma_f32_16x16x32_bf16 %0, %9, %10, %0\n\tbuffer_load_dwordx4 %1, %11, %12, %13 offen offset:384 sc1\n\tv_mfma_f32_16x16x32_bf16 %2, %9, %14, %2\n\tv_mul_f32 %3, 0xbfb8aa3b, %15\n\tv_mul_f32 %4, 0xbfb8aa3b, %16\n\tv_mfma_f32_16x16x32_bf16 %5, %9, %17, %5\n\tds_read_b128 %6, %18 offset:38912\n\tv_mul_f32 %7, 0xbfb8aa3b, %19\n\tv_mfma_f32_16x16x32_bf16 %8, %9, %20, %8"
      : "+v"(ac[0][0]), "=&v"(pu[10]), "+v"(ac[0][1]), "=&v"(ze[1]), "=&v"(ze[2]), "+v"(ac[0][2]), "=&a"(fwo[25]), "=&v"(ze[3]), "+v"(ac[0][3]) : "v"(pu[11]), "v"(fw2[11][0]), "v"(vF), "s"(rsW), "s"(so1), "v"(fw2[11][1]), "v"(ye[1]), "v"(ye[2]), "v"(fw2[11][2]), "v"(ro), "v"(ye[3]), "v"(fw2[11][3]) : "memory"); SB();
  WAITVB(12);
  SB();
  asm volatile("v_mfma_f32_16x16x32_bf16 %0, %3, %4, %0\n\tbuffer_load_dwordx4 %1, %5, %6, %7 offen offset:512 sc1\n\tv_mfma_f32_16x16x32_bf16 %2, %3, %8, %2"
      : "+v"(ac[0][0]), "=&v"(pu[11]), "+v"(ac[0][1]) : "v"(pu[12]), "v"(fw2[12][0]), "v"(vF), "s"(rsW), "s"(so1), "v"(fw2[12][1]) : "memory"); SB();
  WAITV3(20, fz[0], fz[1], fz[2]);
  if (__builtin_expect(__ballot(((fz[0] ^ nonce) | (fz[1] ^ nonce) | (fz[2] ^ nonce)) != 0u) != 0ull, 0)) { poll0<16>(rsW, vZ0, nonce); poll0<16>(rsW, vZ1, nonce); poll0<16>(rsW, vZ2, nonce); }
  SB();
  asm volatile("v_mfma_f32_16x16x32_bf16 %0, %4, %5, %0\n\tv_exp_f32 %1, %1\n\tv_mfma_f32_16x16x32_bf16 %2, %4, %6, %2\n\tds_read_b128 %3, %7 offset:45056"
      : "+v"(ac[0][2]), "+v"(ze[0]), "+v"(ac[0][3]), "=&a"(fwo[26]) : "v"(pu[12]), "v"(fw2[12][2]), "v"(fw2[12][3]), "v"(ro) : "memory"); SB();
  WAITVB(12);
  SB();
  asm volatile("v_mfma_f32_16x16x32_bf16 %0, %8, %9, %0\n\tbuffer_load_dwordx4 %1, %10, %11, %12 offen offset:640 sc1\n\tv_mfma_f32_16x16x32_bf16 %2, %8, %13, %2\n\tv_exp_f32 %3, %3\n\tv_mfma_f32_16x16x32_bf16 %4, %8, %14, %4\n\tv_exp_f32 %5, %5\n\tv_mfma_f32_16x16x32_bf16 %6, %8, %15, %6\n\tv_exp_f32 %7, %7"
      : "+v"(ac[0][0]), "=&v"(pu[12]), "+v"(ac[0][1]), "+v"(ze[1]), "+v"(ac[0][2]), "+v"(ze[2]), "+v"(ac[0][3]), "+v"(ze[3]) : "v"(pu[13]), "v"(fw2[13][0]), "v"(vF), "s"(rsW), "s"(so1), "v"(fw2[13][1]), "v"(fw2[13][2]), "v"(fw2[13][3]) : "memory"); SB();
  STAMP(42);
  WAITVB(12);
  SB();
  asm volatile("v_mfma_f32_16x16x32_bf16 %0, %7, %8, 0\n\tbuffer_load_dwordx4 %1, %9, %10, %11 offen offset:768 sc1\n\tv_mfma_f32_16x16x32_bf16 %2, %12, %8, 0\n\tv_add_f32 %3, 1.0, %3\n\tv_add_f32 %4, 1.0, %4\n\tv_mfma_f32_16x16x32_bf16 %5, %13, %8, 0\n\tv_add_f32 %6, 1.0, %6"
      : "=&v"(as[0]), "=&v"(pu[13]), "=&v"(as[1]), "+v"(ze[0]), "+v"(ze[1]), "=&v"(as[2]), "+v"(ze[2]) : "a"(fwo[0]), "v"(pu[0]), "v"(vF), "s"(rsW), "s"(so1), "a"(fwo[1]), "a"(fwo[2]) : "memory"); SB();
  WAITVB(12);
  SB();
  asm volatile("v_mfma_f32_16x16x32_bf16 %0, %5, %6, %0\n\tds_write_b128 %7, %8 offset:0\n\tv_mfma_f32_16x16x32_bf16 %1, %9, %6, %1\n\tds_read_b128 %2, %10 offset:33792\n\tv_add_f32 %3, 1.0, %3\n\tv_mfma_f32_16x16x32_bf16 %4, %11, %6, %4"
      : "+v"(as[0]), "+v"(as[1]), "=&a"(fwo[27]), "+v"(ze[3]), "+v"(as[2]) : "a"(fwo[3]), "v"(pu[1]), "v"(rwd), "v"(ac[0][0]), "a"(fwo[4]), "v"(ro), "a"(fwo[5]) : "memory"); SB();
  WAITVB(11);
  SB();
  asm volatile("v_mfma_f32_16x16x32_bf16 %0, %5, %6, %0\n\tds_write_b128 %7, %8 offset:1024\n\tv_mfma_f32_16x16x32_bf16 %1, %9, %6, %1\n\tv_rcp_f32 %2, %2\n\tv_mfma_f32_16x16x32_bf16 %3, %10, %6, %3\n\tds_read_b128 %4, %11 offset:39936"
      : "+v"(as[0]), "+v"(as[1]), "+v"(ze[0]), "+v"(as[2]), "=&a"(fwo[28]) : "a"(fwo[6]), "v"(pu[2]), "v"(rwd), "v"(ac[0][1]), "a"(fwo[7]), "a"(fwo[8]), "v"(ro) : "memory"); SB();
  WAITVB(10);
  SB();
  asm volatile("v_mfma_f32_16x16x32_bf16 %0, %5, %6, %0\n\tds_write_b128 %7, %8 offset:2048\n\tv_mfma_f32_16x16x32_bf16 %1, %9, %6, %1\n\tv_rcp_f32 %2, %2\n\tv_mfma_f32_16x16x32_bf16 %3, %10, %6, %3\n\tds_read_b128 %4, %11 offset:46080"
      : "+v"(as[0]), "+v"(as[1]), "+v"(ze[1]), "+v"(as[2]), "=&a"(fwo[29]) : "a"(fwo[9]), "v"(pu[3]), "v"(rwd), "v"(ac[0][2]), "a"(fwo[10]), "a"(fwo[11]), "v"(ro) : "memory"); SB();
  WAITVB(9);
  SB();
  asm volatile("v_mfma_f32_16x16x32_bf16 %0, %5, %6, %0\n\tds_write_b128 %7, %8 offset:3072\n\tv_mfma_f32_16x16x32_bf16 %1, %9, %6, %1\n\tv_rcp_f32 %2, %2\n\tv_mfma_f32_16x16x32_bf16 %3, %10, %6, %3\n\tds_read_b128 %4, %11 offset:34816"
      : "+v"(as[0]), "+v"(as[1]), "+v"(ze[2]), "+v"(as[2]), "=&a"(fwo[30]) : "a"(fwo[12]), "v"(pu[4]), "v"(rwd), "v"(ac[0][3]), "a"(fwo[13]), "a"(fwo[14]), "v"(ro) : "memory"); SB();
  WAITVB(8);
  SB();
  asm volatile("v_mfma_f32_16x16x32_bf16 %0, %3, %4, %0\n\tv_rcp_f32 %1, %1\n\tv_mfma_f32_16x16x32_bf16 %2, %5, %4, %2"
      : "+v"(as[0]), "+v"(ze[3]), "+v"(as[1]) : "a"(fwo[15]), "v"(pu[5]), "a"(fwo[16])); SB();
  WAITL(1);
  BAR0();
  SB();
  asm volatile("v_mfma_f32_16x16x32_bf16 %0, %1, %2, %0"
      : "+v"(as[2]) : "a"(fwo[17]), "v"(pu[5])); SB();
  WAITVB(7);
  asm volatile("ds_read_b128 %0, %9 offset:40960\n\tv_mfma_f32_16x16x32_bf16 %1, %10, %11, %1\n\tds_read_b128 %2, %12 offset:0\n\tds_read_b128 %3, %12 offset:4096\n\tv_mfma_f32_16x16x32_bf16 %4, %13, %11, %4\n\tds_read_b128 %5, %12 offset:8192\n\tds_read_b128 %6, %12 offset:12288\n\tv_mfma_f32_16x16x32_bf16 %7, %14, %11, %7\n\tv_mul_f32 %8, %15, %16"
      : "=&a"(fwo[31]), "+v"(as[0]), "=&v"(py[2][0]), "=&v"(py[2][1]), "+v"(as[1]), "=&v"(py[2][2]), "=&v"(py[2][3]), "+v"(as[2]), "=&v"(ge[4]) : "v"(ro), "a"(fwo[18]), "v"(pu[6]), "v"(rr), "a"(fwo[19]), "a"(fwo[20]), "v"(ye[0]), "v"(ze[0]) : "memory"); SB();
  WAITVB(6);
  SB();
  asm volatile("v_mfma_f32_16x16x32_bf16 %0, %7, %8, %0\n\tv_mul_f32 %1, %9, %10\n\tv_mul_f32 %2, %11, %12\n\tv_mfma_f32_16x16x32_bf16 %3, %13, %8, %3\n\tds_read_b128 %4, %14 offset:47104\n\tv_mul_f32 %5, %15, %16\n\tv_mfma_f32_16x16x32_bf16 %6, %17, %8, %6"
      : "+v"(as[0]), "=&v"(ge[5]), "=&v"(ge[6]), "+v"(as[1]), "=&a"(fwo[32]), "=&v"(ge[7]), "+v"(as[2]) : "a"(fwo[21]), "v"(pu[7]), "v"(ye[1]), "v"(ze[1]), "v"(ye[2]), "v"(ze[2]), "a"(fwo[22]), "v"(ro), "v"(ye[3]), "v"(ze[3]), "a"(fwo[23]) : "memory"); SB();
  WAITVB(5);
  asm volatile("ds_read_b128 %0, %6 offset:35840\n\tds_read_b128 %1, %6 offset:41984\n\tds_read_b128 %2, %6 offset:48128\n\tds_read_b128 %3, %6 offset:36864\n\tds_read_b128 %4, %6 offset:43008\n\tv_mfma_f32_16x16x32_bf16 %5, %7, %8, %5"
      : "=&a"(fwo[33]), "=&a"(fwo[34]), "=&a"(fwo[35]), "=&a"(fwo[36]), "=&a"(fwo[37]), "+v"(as[0]) : "v"(ro), "a"(fwo[24]), "v"(pu[8]) : "memory"); SB();
  WAITL(6);
  asm volatile("v_add_f32 %0, %6, %7\n\tv_mfma_f32_16x16x32_bf16 %1, %8, %9, %1\n\tv_add_f32 %2, %10, %7\n\tv_add_f32 %3, %11, %7\n\tv_mfma_f32_16x16x32_bf16 %4, %12, %9, %4\n\tv_add_f32 %5, %13, %7"
      : "=&v"(ye[0]), "+v"(as[1]), "=&v"(ye[1]), "=&v"(ye[2]), "+v"(as[2]), "=&v"(ye[3]) : "v"(py[2][0][0]), "v"(bias), "a"(fwo[25]), "v"(pu[8]), "v"(py[2][0][1]), "v"(py[2][0][2]), "a"(fwo[26]), "v"(py[2][0][3])); SB();
  WAITVB(4);
  SB();
  asm volatile("v_mfma_f32_16x16x32_bf16 %0, %7, %8, %0\n\tv_add_f32 %1, %9, %10\n\tv_add_f32 %2, %11, %12\n\tv_mfma_f32_16x16x32_bf16 %3, %13, %8, %3\n\tv_add_f32 %4, %14, %15\n\tv_add_f32 %5, %16, %17\n\tv_mfma_f32_16x16x32_bf16 %6, %18, %8, %6"
      : "+v"(as[0]), "=&v"(ze[0]), "=&v"(ze[1]), "+v"(as[1]), "=&v"(ze[2]), "=&v"(ze[3]), "+v"(as[2]) : "a"(fwo[27]), "v"(pu[9]), "v"(py[2][1][0]), "v"(py[2][2][0]), "v"(py[2][1][1]), "v"(py[2][2][1]), "a"(fwo[28]), "v"(py[2][1][2]), "v"(py[2][2][2]), "v"(py[2][1][3]), "v"(py[2][2][3]), "a"(fwo[29])); SB();
  WAITVB(3);
  asm volatile("ds_read_b128 %0, %4 offset:49152\n\tds_read_b128 %1, %4 offset:37888\n\tds_read_b128 %2, %4 offset:44032\n\tds_read_b128 %3, %4 offset:50176"
      : "=&a"(fwo[38]), "=&a"(fwo[39]), "=&a"(fwo[40]), "=&a"(fwo[41]) : "v"(ro) : "memory"); SB();
  WAITL(6);
  SB();
  asm volatile("v_mfma_f32_16x16x32_bf16 %0, %7, %8, %0\n\tv_add_f32 %1, %1, %9\n\tv_add_f32 %2, %2, %10\n\tv_mfma_f32_16x16x32_bf16 %3, %11, %8, %3\n\tv_add_f32 %4, %4, %12\n\tv_add_f32 %5, %5, %13\n\tv_mfma_f32_16x16x32_bf16 %6, %14, %8, %6"
      : "+v"(as[0]), "+v"(ye[0]), "+v"(ye[1]), "+v"(as[1]), "+v"(ye[2]), "+v"(ye[3]), "+v"(as[2]) : "a"(fwo[30]), "v"(pu[10]), "v"(py[2][3][0]), "v"(py[2][3][1]), "a"(fwo[31]), "v"(py[2][3][2]), "v"(py[2][3][3]), "a"(fwo[32])); SB();
  WAITL(0);
  WAITVB(0);
  SB();
  asm volatile("v_mfma_f32_16x16x32_bf16 %0, %10, %11, %0\n\tv_add_f32 %1, %1, %2\n\tv_add_f32 %3, %3, %4\n\tv_mfma_f32_16x16x32_bf16 %0, %12, %13, %0\n\tv_add_f32 %5, %5, %6\n\tv_add_f32 %7, %7, %8\n\tv_mfma_f32_16x16x32_bf16 %0, %14, %15, %0\n\tv_mul_f32 %2, 0xbfb8aa3b, %1\n\tv_mul_f32 %4, 0xbfb8aa3b, %3\n\tv_mfma_f32_16x16x32_bf16 %9, %16, %11, %9\n\tv_mul_f32 %6, 0xbfb8aa3b, %5\n\tv_mul_f32 %8, 0xbfb8aa3b, %7\n\tv_mfma_f32_16x16x32_bf16 %9, %17, %13, %9\n\tv_exp_f32 %2, %2\n\tv_mfma_f32_16x16x32_bf16 %9, %18, %15, %9\n\tv_exp_f32 %4, %4"
      : "+v"(as[0]), "+v"(ye[0]), "+v"(ze[0]), "+v"(ye[1]), "+v"(ze[1]), "+v"(ye[2]), "+v"(ze[2]), "+v"(ye[3]), "+v"(ze[3]), "+v"(as[1]) : "a"(fwo[33]), "v"(pu[11]), "a"(fwo[36]), "v"(pu[12]), "a"(fwo[39]), "v"(pu[13]), "a"(fwo[34]), "a"(fwo[37]), "a"(fwo[40])); SB();
  asm volatile("v_mfma_f32_16x16x32_bf16 %0, %5, %6, %0\n\tv_exp_f32 %1, %1\n\tv_mfma_f32_16x16x32_bf16 %0, %7, %8, %0\n\tv_exp_f32 %2, %2\n\tv_mfma_f32_16x16x32_bf16 %0, %9, %10, %0\n\tv_add_f32 %3, 1.0, %3\n\tv_add_f32 %4, 1.0, %4"
      : "+v"(as[2]), "+v"(ze[2]), "+v"(ze[3]), "+v"(ze[0]), "+v"(ze[1]) : "a"(fwo[35]), "v"(pu[11]), "a"(fwo[38]), "v"(pu[12]), "a"(fwo[41]), "v"(pu[13])); SB();
  STAMP(44);
  asm volatile("v_add_f32 %0, 1.0, %0\n\tv_add_f32 %1, 1.0, %1\n\tv_rcp_f32 %2, %2\n\tv_rcp_f32 %3, %3\n\tv_rcp_f32 %0, %0\n\tv_rcp_f32 %1, %1\n\tv_mul_f32 %4, %11, %2\n\tv_mul_f32 %5, %12, %3\n\tv_mul_f32 %6, %13, %0\n\tv_mul_f32 %7, %14, %1\n\tv_fma_f32 %8, %15, %16, %8\n\tv_fma_f32 %9, %17, %18, %9\n\tv_fma_f32 %10, %19, %20, %10"
      : "+v"(ze[2]), "+v"(ze[3]), "+v"(ze[0]), "+v"(ze[1]), "=&v"(ge[8]), "=&v"(ge[9]), "=&v"(ge[10]), "=&v"(ge[11]), "+v"(xe[0]), "+v"(xe[1]), "+v"(xe[2]) : "v"(ye[0]), "v"(ye[1]), "v"(ye[2]), "v"(ye[3]), "v"(as[0][0]), "v"(ge[0]), "v"(as[0][1]), "v"(ge[1]), "v"(as[0][2]), "v"(ge[2])); SB();
  asm volatile("v_fma_f32 %0, %9, %10, %0\n\tv_cvt_pk_bf16_f32 %1, %11, %12\n\tv_cvt_pk_bf16_f32 %2, %13, %0\n\tv_fma_f32 %3, %14, %15, %3\n\tv_fma_f32 %4, %16, %17, %4\n\tv_fma_f32 %5, %18, %19, %5\n\tv_fma_f32 %6, %20, %21, %6\n\tv_cvt_pk_bf16_f32 %7, %3, %4\n\tv_cvt_pk_bf16_f32 %8, %5, %6"
      : "+v"(xe[3]), "=&v"(ov[0][0]), "=&v"(ov[0][1]), "+v"(xe[4]), "+v"(xe[5]), "+v"(xe[6]), "+v"(xe[7]), "=&v"(ov[1][0]), "=&v"(ov[1][1]) : "v"(as[0][3]), "v"(ge[3]), "v"(xe[0]), "v"(xe[1]), "v"(xe[2]), "v"(as[1][0]), "v"(ge[4]), "v"(as[1][1]), "v"(ge[5]), "v"(as[1][2]), "v"(ge[6]), "v"(as[1][3]), "v"(ge[7])); SB();
  asm volatile("ds_write_b64 %6, %7 offset:0\n\tv_fma_f32 %0, %8, %9, %0\n\tv_fma_f32 %1, %10, %11, %1\n\tv_fma_f32 %2, %12, %13, %2\n\tv_fma_f32 %3, %14, %15, %3\n\tv_cvt_pk_bf16_f32 %4, %0, %1\n\tv_cvt_pk_bf16_f32 %5, %2, %3\n\tds_write_b64 %16, %17 offset:0"
      : "+v"(xe[8]), "+v"(xe[9]), "+v"(xe[10]), "+v"(xe[11]), "=&v"(ov[2][0]), "=&v"(ov[2][1]) : "v"(xta[0]), "v"(ov[0]), "v"(as[2][0]), "v"(ge[8]), "v"(as[2][1]), "v"(ge[9]), "v"(as[2][2]), "v"(ge[10]), "v"(as[2][3]), "v"(ge[11]), "v"(xta[1]), "v"(ov[1]) : "memory"); SB();
  asm volatile("ds_write_b64 %0, %1 offset:0"
      :  : "v"(xta[2]), "v"(ov[2]) : "memory"); SB();
  { u32x4 t0_, t1_; DSR16(t0_, xo, 0); DSR16(t1_, xo, 1024); WAITLB(1);
    BS16(vXa, rsO_, 0, t0_, ""); WAITLB(0); BS16(vXb, rsO_, 1024, t1_, ""); }
  STAMP(23);
  if (b == 8 * 28 && w == 0) { const unsigned vZ = 0u; ST4(vZ, ws, nonce); }

  STAMP(12);
  STAMP_DUMP();
  }
  }
  asm volatile(".Lcpf_e:");
}

extern "C" void kernel_launch(void* const* d_in, const int* in_sizes, int n_in,
                              void* d_out, int out_size, void* d_ws, size_t ws_size,
                              hipStream_t stream) {
  fused_kernel<<<256, 256, 0, stream>>>((const char*)d_in[0], (const char*)d_in[1], (const char*)d_in[2], (const char*)d_in[4],
                                        (const char*)d_in[5], (const char*)d_in[6], (const char*)d_in[7], (char*)d_ws, (char*)d_out);
}
```

```cpp
#include <hip/hip_runtime.h>
#include <hip/hip_bf16.h>
#include <stdint.h>

typedef short bf16x8 __attribute__((ext_vector_type(8)));
typedef short s16x4 __attribute__((ext_vector_type(4)));
typedef float f32x4 __attribute__((ext_vector_type(4)));
typedef unsigned u32x4 __attribute__((ext_vector_type(4)));
typedef unsigned u32x2 __attribute__((ext_vector_type(2)));
typedef __bf16 bf16x2_t __attribute__((ext_vector_type(2)));
typedef float f32x2_t __attribute__((ext_vector_type(2)));
#define LDS_AS __attribute__((address_space(3)))
#define DEV __device__ __forceinline__

#define STAMP(i)
#define STAMP_DUMP()

#define SB() __builtin_amdgcn_sched_barrier(0)
#define WAITV(n) asm volatile("s_waitcnt vmcnt(" #n ")" ::: "memory")
#define WAITV1(n, a) asm volatile("s_waitcnt vmcnt(" #n ")" : "+v"(a) :: "memory")
#define WAITV2(n, a, b) asm volatile("s_waitcnt vmcnt(" #n ")" : "+v"(a), "+v"(b) :: "memory")
#define WAITV3(n, a, b, c) asm volatile("s_waitcnt vmcnt(" #n ")" : "+v"(a), "+v"(b), "+v"(c) :: "memory")
#define WAITV4(n, a, b, c, d) asm volatile("s_waitcnt vmcnt(" #n ")" : "+v"(a), "+v"(b), "+v"(c), "+v"(d) :: "memory")
#define WAITL(n) do { asm volatile("" ::: "memory"); __builtin_amdgcn_s_waitcnt(0xc07f | ((n) << 8)); asm volatile("" ::: "memory"); } while (0)
#define BAR() do { WAITL(0); __builtin_amdgcn_s_barrier(); asm volatile("" ::: "memory"); } while (0)
#define BAR0() do { asm volatile("" ::: "memory"); __builtin_amdgcn_s_barrier(); asm volatile("" ::: "memory"); } while (0)
#define OPQ(p) asm volatile("" : "+s"(p))
#define OPV(p) asm volatile("" : "+v"(p))
#define LD16(dst, voff, sb, imm) asm volatile("global_load_dwordx4 %0, %1, %2 offset:" #imm : "=&v"(dst) : "v"(voff), "s"(sb) : "memory")
#define LD8(dst, voff, sb, imm) asm volatile("global_load_dwordx2 %0, %1, %2 offset:" #imm : "=&v"(dst) : "v"(voff), "s"(sb) : "memory")
#define DMA1(v0, rs, so, lds, limm, imm, bits) \
  asm volatile("s_add_u32 m0, %3, %4\n\ts_nop 0\n\tbuffer_load_dwordx4 %0, %1, %2 offen offset:" #imm " " bits " lds" :: "v"(v0), "s"(rs), "s"(so), "s"(lds), "n"(limm) : "memory", "scc")
#define DMA4(v0, v1, rs, so, step, lds, limm) do { unsigned t_; \
  asm volatile("s_add_u32 m0, %5, %6\n\ts_add_u32 %0, %4, %7\n\tbuffer_load_dwordx4 %1, %3, %4 offen lds\n\tbuffer_load_dwordx4 %2, %3, %4 offen offset:1024 lds\n\t" \
               "buffer_load_dwordx4 %1, %3, %0 offen offset:2048 lds\n\tbuffer_load_dwordx4 %2, %3, %0 offen offset:3072 lds" \
               : "=&s"(t_) : "v"(v0), "v"(v1), "s"(rs), "s"(so), "s"(lds), "n"(limm), "n"(step) : "memory", "scc"); } while (0)
#define DMAW2(v0, rs, so) asm volatile("s_cmp_eq_u32 %3, 3\n\ts_cbranch_scc1 .LBBa%=\n\tbuffer_load_dwordx4 %0, %1, %2 offen offset:0 sc1 lds\n\tbuffer_load_dwordx4 %0, %1, %2 offen offset:1024 sc1 lds\n\ts_branch .LBBb%=\n.LBBa%=:\n\tbuffer_load_dword %0, %1, %2 offen lds\n\tbuffer_load_dword %0, %1, %2 offen lds\n.LBBb%=:" \
  :: "v"(v0), "s"(rs), "s"(so), "s"(w) : "memory", "scc")
#define DMAW4(v0, rs, so) asm volatile("s_cmp_eq_u32 %3, 3\n\ts_cbranch_scc1 .LBBa%=\n\tbuffer_load_dwordx4 %0, %1, %2 offen offset:0 sc1 lds\n\tbuffer_load_dwordx4 %0, %1, %2 offen offset:1024 sc1 lds\n\tbuffer_load_dwordx4 %0, %1, %2 offen offset:2048 sc1 lds\n\tbuffer_load_dwordx4 %0, %1, %2 offen offset:3072 sc1 lds\n\ts_branch .LBBb%=\n.LBBa%=:\n\tbuffer_load_dword %0, %1, %2 offen lds\n\tbuffer_load_dword %0, %1, %2 offen lds\n\tbuffer_load_dword %0, %1, %2 offen lds\n\tbuffer_load_dword %0, %1, %2 offen lds\n.LBBb%=:" \
  :: "v"(v0), "s"(rs), "s"(so), "s"(w) : "memory", "scc")
#define TOUCH(v0, rs, so, sadd) do { unsigned t_; \
  asm volatile("s_mov_b32 m0, %4\n\ts_add_u32 %0, %3, %5\n\tbuffer_load_dword %1, %2, %0 offen lds" : "=&s"(t_) : "v"(v0), "s"(rs), "s"(so), "s"(lds0 + DUMP), "n"(sadd) : "memory", "scc"); } while (0)
#define HS16(voff, so, imm, v) do { const u32x4 v_ = (v); \
  asm volatile("buffer_store_dwordx4 %0, %1, %2, %3 offen offset:" #imm " sc1\n\ts_nop 1" :: "v"(v_), "v"(voff), "s"(rsW), "s"(so) : "memory"); } while (0)
#define HS16M(lo, hi, voff, so, imm, v) do { const u32x4 v_ = (v); \
  asm volatile("s_mov_b32 exec_lo, " #lo "\n\ts_mov_b32 exec_hi, " #hi "\n\tbuffer_store_dwordx4 %0, %1, %2, %3 offen offset:" #imm " sc1\n\ts_mov_b64 exec, -1\n\ts_nop 0" \
               :: "v"(v_), "v"(voff), "s"(rsW), "s"(so) : "memory"); } while (0)
#define HS16H(hi, voff, so, imm, v) do { const u32x4 v_ = (v); \
  asm volatile("s_mov_b32 exec_hi, " #hi "\n\tbuffer_store_dwordx4 %0, %1, %2, %3 offen offset:" #imm " sc1\n\ts_mov_b32 exec_hi, -1\n\ts_nop 0" \
               :: "v"(v_), "v"(voff), "s"(rsW), "s"(so) : "memory"); } while (0)
#define BS16(voff, rs, imm, v, bits) do { const u32x4 v_ = (v); asm volatile("buffer_store_dwordx4 %0, %1, %2, 0 offen offset:" #imm " " bits "\n\ts_nop 1" :: "v"(v_), "v"(voff), "s"(rs) : "memory"); } while (0)
#define ST4(voff, sb, v) do { const unsigned v_ = (v); asm volatile("global_store_dword %0, %1, %2 sc1" :: "v"(voff), "v"(v_), "s"(sb) : "memory"); } while (0)
#define FLAG16(mask, so) do { const u32x4 v_ = {nonce, nonce, nonce, nonce}; \
  asm volatile("s_mov_b64 exec, " #mask "\n\tbuffer_store_dwordx4 %0, %1, %2, %3 offen sc1\n\ts_mov_b64 exec, -1\n\ts_nop 0" :: "v"(v_), "v"(vL16), "s"(rsW), "s"(so) : "memory"); } while (0)
#define STFA(so) FLAG16(15, so)
#define STFQ(so) FLAG16(1, so)
#define STFU(so) FLAG16(1, so)
#define STFO(so) FLAG16(15, so)
DEV u32x4 srd(const void* p, unsigned bytes) { return (u32x4){(unsigned)(size_t)p, (unsigned)((size_t)p >> 32), bytes, 0x20000u}; }
DEV float lo2f(unsigned v) { return __uint_as_float(v << 16); }
DEV float hi2f(unsigned v) { return __uint_as_float(v & 0xffff0000u); }
DEV unsigned pack2(float a, float b) { f32x2_t v = {a, b}; return __builtin_bit_cast(unsigned, __builtin_convertvector(v, bf16x2_t)); }
DEV float rbf(float a) { return __uint_as_float(pack2(0.f, a)); }
DEV f32x2_t rbf2(f32x2_t a) { return (f32x2_t){rbf(a[0]), rbf(a[1])}; }
DEV float dot2(unsigned a, unsigned b, float c) {
  return __builtin_amdgcn_fdot2_f32_bf16(__builtin_bit_cast(bf16x2_t, a), __builtin_bit_cast(bf16x2_t, b), c, false);
}
DEV bf16x8 cat8(s16x4 a, s16x4 b) { return __builtin_shufflevector(a, b, 0, 1, 2, 3, 4, 5, 6, 7); }
DEV bf16x8 tr8u(unsigned p, int second) {
  return cat8(__builtin_amdgcn_ds_read_tr16_b64_v4i16((LDS_AS s16x4*)p), __builtin_amdgcn_ds_read_tr16_b64_v4i16((LDS_AS s16x4*)(p + second)));
}
template <int CTRL> DEV float dpp_add(float v) {
  return v + __builtin_bit_cast(float, __builtin_amdgcn_update_dpp(0, __builtin_bit_cast(int, v), CTRL, 0xf, 0xf, true));
}
DEV float red16(float v) { return dpp_add<0x140>(dpp_add<0x141>(dpp_add<0x4E>(dpp_add<0xB1>(v)))); }
DEV unsigned ror8(unsigned v) { return (unsigned)__builtin_amdgcn_update_dpp(0, (int)v, 0x128, 0xf, 0xf, true); }
DEV float fmx(float a, float b) { return __builtin_elementwise_maximum(a, b); }
DEV float xmax2(float a, float b) {
  const auto r = __builtin_amdgcn_permlane16_swap(__float_as_uint(a), __float_as_uint(b), false, false);
  const float v = fmx(__uint_as_float(r[0]), __uint_as_float(r[1]));
  const auto s = __builtin_amdgcn_permlane32_swap(__float_as_uint(v), __float_as_uint(v), false, false);
  return fmx(__uint_as_float(s[0]), __uint_as_float(s[1]));
}
DEV float xsum2(float a, float b) {
  const auto r = __builtin_amdgcn_permlane16_swap(__float_as_uint(a), __float_as_uint(b), false, false);
  const float v = __uint_as_float(r[0]) + __uint_as_float(r[1]);
  const auto s = __builtin_amdgcn_permlane32_swap(__float_as_uint(v), __float_as_uint(v), false, false);
  return __uint_as_float(s[0]) + __uint_as_float(s[1]);
}
DEV f32x2_t xbcast2(float v) {
  const auto r = __builtin_amdgcn_permlane16_swap(__float_as_uint(v), __float_as_uint(v), false, false);
  return (f32x2_t){__uint_as_float(r[0]), __uint_as_float(r[1])};
}
#define DSR16(d, addr, off) asm volatile("ds_read_b128 %0, %1 offset:" #off : "=&v"(d) : "v"(addr) : "memory")
#define WAITVB(n) __builtin_amdgcn_s_waitcnt(0x0f70 | ((n) & 15) | (((n) >> 4) << 14))
#define WAITLB(n) __builtin_amdgcn_s_waitcnt(0xc07f | ((n) << 8))

constexpr int kSpinCap = 200000;
template <int SLEEP> DEV void poll1(const u32x4& rsW, unsigned voff, unsigned nonce) {
#pragma unroll 1
  for (int spin = 0; spin < kSpinCap; ++spin) {
    __builtin_amdgcn_s_sleep(SLEEP);
    unsigned v;
    asm volatile("buffer_load_dword %0, %1, %2, 0 offen sc1\n\ts_waitcnt vmcnt(0)" : "=&v"(v) : "v"(voff), "s"(rsW) : "memory");
    if (__ballot(v != nonce) == 0ull) return;
  }
}

template <int SLEEP> DEV void poll1(const u32x4& rsW, unsigned voff, unsigned voff2, unsigned nonce) {
#pragma unroll 1
  for (int spin = 0; spin < kSpinCap; ++spin) {
    __builtin_amdgcn_s_sleep(SLEEP);
    unsigned v, v2;
    asm volatile("buffer_load_dword %0, %2, %4, 0 offen sc1\n\tbuffer_load_dword %1, %3, %4, 0 offen sc1\n\ts_waitcnt vmcnt(0)" : "=&v"(v), "=&v"(v2) : "v"(voff), "v"(voff2), "s"(rsW) : "memory");
    if (__ballot(((v ^ nonce) | (v2 ^ nonce)) != 0u) == 0ull) return;
  }
}
template <int SLEEP> DEV void poll0(const u32x4& rsW, unsigned voff, unsigned nonce) {
  unsigned v;
  asm volatile("buffer_load_dword %0, %1, %2, 0 offen sc1\n\ts_waitcnt vmcnt(0)" : "=&v"(v) : "v"(voff), "s"(rsW) : "memory");
  if (__ballot(v != nonce) != 0ull) poll1<SLEEP>(rsW, voff, nonce);
}

DEV float ln_inv(float var) {
  const float sd = rbf(__builtin_amdgcn_sqrtf(rbf(rbf(fmaxf(var, 0.f)) + 1e-5f)));
  return __builtin_amdgcn_rcpf(sd);
}
DEV float ln_a(const unsigned (&p)[14], f32x2_t (&d)[14], float& dm) {
  float s0 = 0.f, s1 = 0.f;
#pragma unroll
  for (int i = 0; i < 14; i += 2) { s0 = dot2(p[i], 0x3f803f80u, s0); s1 = dot2(p[i + 1], 0x3f803f80u, s1); }
  const float mean = red16(s0 + s1) * (1.0f / 448.0f);
  const float mb = rbf(mean);
  dm = mean - mb;
  f32x2_t q = {0.f, 0.f}, q1 = {0.f, 0.f};
#pragma unroll
  for (int i = 0; i < 14; ++i) d[i] = (f32x2_t){lo2f(p[i]), hi2f(p[i])} - (f32x2_t){mb, mb};
#pragma unroll
  for (int i = 0; i < 14; i += 2) { q = d[i] * d[i] + q; q1 = d[i + 1] * d[i + 1] + q1; }
  q += q1;
  return q[0] + q[1];
}
DEV void ln_b(f32x2_t (&d)[14], float q, float dm, float& inv, float& a, float& c) {
  const float var = red16(q) * (1.0f / 448.0f) - dm * dm;
  inv = ln_inv(var);
  const float inv2 = ln_inv(var * inv * inv);
  a = inv * inv2; c = -rbf(dm * inv) * inv2;
#pragma unroll
  for (int i = 0; i < 14; ++i) { d[i] = rbf2(d[i]); asm volatile("" : "+v"(d[i])); }
}
DEV void ln_h(unsigned (&p)[14], const f32x2_t (&d)[14], float a, float c) {
#pragma unroll
  for (int i = 0; i < 14; ++i) {
    const f32x2_t r = __builtin_elementwise_fma(d[i], (f32x2_t){a, a}, (f32x2_t){c, c});
    p[i] = pack2(r[0], r[1]);
  }
}
DEV void ln_x(unsigned (&p)[14], const f32x2_t (&d)[14], float inv) {
#pragma unroll
  for (int i = 0; i < 14; ++i) { const f32x2_t r = d[i] * (f32x2_t){inv, inv}; p[i] = pack2(r[0], r[1]); }
}

constexpr unsigned WS_XN = 0;
constexpr unsigned WS_HT = WS_XN + 2048 * 1024;
constexpr unsigned WS_WQ = WS_HT + 4 * 64 * 8192;
constexpr unsigned WS_UT = WS_WQ + 8 * 4 * 6 * 8192;
constexpr unsigned WS_OT = WS_UT + 28 * 8 * 8192;
constexpr unsigned WS_WO = WS_OT + 128 * 2 * 8192;
constexpr unsigned WS_FA = WS_WO + 4 * 131072;
constexpr unsigned WS_FQ = WS_FA + 1536 * 4 * 16;
constexpr unsigned WS_FU = WS_FQ + 64 * 4 * 4;
constexpr unsigned WS_FO = WS_FU + 1024 * 4 * 4;
constexpr unsigned WS_END = WS_FO + 512 * 4 * 16;
constexpr int SCW = 10240;
constexpr int RB = 40960, RW = 30720;
constexpr int WOB = 55296;
constexpr int S3B = 112640;
constexpr int TP = 912;
constexpr int SK = 18432, SQ = 36864 - 512;
constexpr int DUMP = 130816;
constexpr int LDS_BYTES = 163840;

__global__ __launch_bounds__(256, 1) void fused_kernel(const char* __restrict__ x, const char* __restrict__ wqkv, const char* __restrict__ wo,
                                                       const char* __restrict__ w1, const char* __restrict__ b1,
                                                       const char* __restrict__ w2, const char* __restrict__ b2,
                                                       char* __restrict__ ws, char* __restrict__ out) {
  __shared__ __attribute__((aligned(16))) char smem[LDS_BYTES];
  const int lane = threadIdx.x & 63;
  const int w = __builtin_amdgcn_readfirstlane(threadIdx.x >> 6);
  const int b = blockIdx.x, xq = (b & 7) >> 1, jb = b >> 3;
  STAMP(0);
  { const char* pc_; unsigned vo_ = (threadIdx.x * 4 + (jb & 3)) * 128;
    asm volatile("s_getpc_b64 %0\n.Lcpf_b:\n\ts_mov_b32 m0, %2\n\tv_min_u32 %1, .Lcpf_e-.Lcpf_b-4, %1\n\tglobal_load_lds_dword %1, %0"
                 : "=&s"(pc_), "+v"(vo_) : "s"((unsigned)(size_t)(LDS_AS char*)smem + 40704) : "memory"); }
  const unsigned lds0 = (unsigned)(size_t)(LDS_AS char*)smem;
  const int n16 = lane & 15, g4 = lane >> 4, q4 = n16 >> 2, pq = lane & 3, r8 = lane >> 3;
  const unsigned vL16 = lane * 16;
  const unsigned wsw = ((lane & 7) ^ (r8 & 2)) * 16;
  const int th = xq >> 1, hq = xq & 1, pr_ = 64 * (1 - th) + 32 * hq + jb;
  unsigned vX = (4 * w + g4) * 896 + n16 * 16, vX3 = (4 * w + g4) * 896 + 768 + (n16 & 7) * 16 + (n16 >> 3) * 8, oX = pr_ * (16 * 896);
  asm volatile("" : "+v"(vX), "+v"(vX3)); asm volatile("" : "+s"(oX));
  u32x4 xa, xb, xc; u32x2 xd;
  const int d4 = (jb * 37) >> 8, fi = jb - 7 * d4, ft = 7 * xq + fi;
  const int rota = 4 * d4 + 2 * fi, rotg = 0 * fi;
#define KS1(c) ((((c) + rotg) & 0) + (((c) + rota) & 15) + 16 * w)
  const unsigned ldsR = lds0 + RB + w * RW;
  unsigned vW1a = r8 * 3584 + wsw + ft * 128, vW1b = (vW1a ^ 64) + (8 * 3584 - 1024);
  unsigned sp[3];
#pragma unroll
  for (int c = 0; c < 3; ++c) { sp[c] = KS1(c) * 114688; OPQ(sp[c]); }
  asm volatile("" : "+v"(vW1a), "+v"(vW1b) : "s"(ldsR));
  asm volatile("" :: "s"(x), "s"(wqkv), "s"(wo), "s"(w1), "s"(b1), "s"(w2), "s"(b2), "s"(ws), "s"(out));
  int cls = b & 1;
  if (cls) {
  asm volatile("; ROLE C");
  const char* sX = x + oX;
  LD16(xa, vX, sX, 0); LD16(xb, vX, sX, 256); LD16(xc, vX, sX, 512); LD8(xd, vX3, sX, 0);
  } else if (jb >= 28) {
    asm volatile("; ROLE B");
    const int b7 = (int)((size_t)w1 >> 7) + 7 * xq, f_ = ((3 - b7) & 3) + 4 * (jb & 1);
    const unsigned vT = (512 * w + 32 * ((4 * g4 + 2 * f_ + 2 + ((jb >> 1) & 1)) & 15) + 2 * n16 + (((b7 + f_) >> 2) & 1)) * 3584 + (7 * xq + f_) * 128;
    u32x4 rs1 = srd(w1, f_ < 7 ? 2048u * 3584u : 0u);
    TOUCH(vT, rs1, 0u, 0);
  } else {
  asm volatile("; ROLE B");
  u32x4 rs1 = srd(w1, 2048u * 3584u);
  DMA4(vW1a, vW1b, rs1, sp[0], 55296, ldsR, 0);
  DMA4(vW1a, vW1b, rs1, sp[1], 55296, ldsR, 4096);
  DMA4(vW1a, vW1b, rs1, sp[2], 55296, ldsR, 8192);
  }
  OPQ(cls);
  if (cls) {
  asm volatile("; NEED C");
  SB();
  const int kg = 32 * (1 - th) + jb, hpq = 4 * (1 - hq) + w;
  const unsigned ldsQ = lds0 + 16384 + w * 3072;
  unsigned vQ_ = r8 * 2688 + ((lane & 7) ^ ((r8 & 2) << 1)) * 16, sQ_ = kg * (8 * 2688) + hpq * 112;
  { u32x4 rsQ = srd(wqkv, 448u * 2688u);
    DMA1(vQ_, rsQ, sQ_, ldsQ, 0, 0, "");
    DMA1(vQ_, rsQ, sQ_, ldsQ, 128, 896, "");
    DMA1(vQ_, rsQ, sQ_, ldsQ, 256, 1792, "");
  }
  unsigned nonce0; asm volatile("s_load_dword %0, %1, 0x0" : "=&s"(nonce0) : "s"(ws) : "memory");
  const int tt = 8 * th + (jb >> 2), hp = 4 * hq + (jb & 3);
  const int st = 16 * (1 - th) + 8 * hq + (jb & 7), dt = jb >> 3, db0 = 4 * dt;
#define BLK(j) (db0 + ((j) ^ 0))
  u32x4 rsW = srd((char*)(((size_t)ws + 8191) & ~(size_t)4095) + 512, WS_END); OPQ(rsW);
  const unsigned trb = (8 * g4 + q4) * 128 + pq * 8, trf = ((q4 >> 1) | ((g4 & 1) << 1)) * 32;
  const unsigned vF = r8 * 1024 + (lane & 7) * 16;
  unsigned vN = (4 * w + g4) * 1024 + n16 * 16, sN = WS_XN + pr_ * 16384;
  unsigned hw = lds0 + (4 * w + g4) * TP + n16 * 16, hw3 = lds0 + (4 * w + g4) * TP + 768 + (n16 & 7) * 16 + (n16 >> 3) * 8;
  unsigned th_ = lds0 + q4 * TP + 224 * w + 64 * g4 + 32 * (pq >> 1) + 8 * (pq & 1), sH = WS_HT + w * 524288 + pr_ * 4096, sFA = WS_FA + (w * 128 + pr_) * (4 * 16);
  unsigned tq = ldsQ + q4 * 128 + ((g4 ^ (q4 & 2)) * 32) + pq * 8, vq = (r8 >> 1) * 49152 + (r8 & 1) * 1024 + (lane & 7) * 16, sq0, sq1, sq2;
  { const int ks = kg >> 2, k7 = ks >= 7; sq0 = WS_WQ + hpq * 196608 + k7 * (8192 - 896) + ks * 128 + (kg & 3) * 2048 + (((55 - kg) >> 31) & 0x80000000u); sq1 = sq0 + 16384; sq2 = sq0 + 32768; }
  asm volatile("" : "+v"(vN), "+v"(hw), "+v"(hw3), "+v"(th_), "+v"(tq), "+v"(vq)); asm volatile("" : "+s"(sN), "+s"(sH), "+s"(sFA), "+s"(sq0), "+s"(sq1), "+s"(sq2));
  WAITV4(3, xa, xb, xc, xd);
  STAMP(1);
  unsigned pr[14] = {xa[0], xa[1], xa[2], xa[3], xb[0], xb[1], xb[2], xb[3], xc[0], xc[1], xc[2], xc[3], xd[0], xd[1]};
  f32x2_t dd[14]; float dm, inv, la, lc;
  float qs_ = ln_a(pr, dd, dm); OPV(qs_);
  SB();
  ln_b(dd, qs_, dm, inv, la, lc); OPV(la);
  SB();
  ln_h(pr, dd, la, lc);
  *(LDS_AS u32x4*)hw = (u32x4){pr[0], pr[1], pr[2], pr[3]}; *(LDS_AS u32x4*)(hw + 256) = (u32x4){pr[4], pr[5], pr[6], pr[7]};
  *(LDS_AS u32x4*)(hw + 512) = (u32x4){pr[8], pr[9], pr[10], pr[11]}; *(LDS_AS u32x2*)hw3 = (u32x2){pr[12], pr[13]};
  SB();
  ln_x(pr, dd, inv);
  HS16(vN, sN, 0, ((u32x4){pr[0], pr[1], pr[2], pr[3]}));
  HS16(vN, sN, 256, ((u32x4){pr[4], pr[5], pr[6], pr[7]}));
  HS16(vN, sN, 512, ((u32x4){pr[8], pr[9], pr[10], pr[11]}));
  HS16M(0x00ff00ff, 0x00ff00ff, vN, sN, 768, ((u32x4){pr[12], pr[13], ror8(pr[12]), ror8(pr[13])}));
  unsigned nonce; asm volatile("s_waitcnt lgkmcnt(0)\n\ts_add_u32 %0, %1, 1" : "=s"(nonce) : "s"(nonce0) : "memory", "scc");
  asm volatile("s_waitcnt vmcnt(4)"
      :  :  : "memory"); SB();
  const bf16x8 q0 = tr8u(tq, 512), q1 = tr8u(tq + 1024, 512), q2 = tr8u(tq + 2048, 512);
  BAR();
  STAMP(2);
  { const bf16x8 h0 = tr8u(th_, 4 * TP), h1 = tr8u(th_ + 16, 4 * TP), h2 = tr8u(th_ + 8 * TP, 4 * TP), h3 = tr8u(th_ + 8 * TP + 16, 4 * TP);
    HS16H(0xffffff, vL16, sH, 0, __builtin_bit_cast(u32x4, h0));
    HS16H(0xffffff, vL16, sH, 1024, __builtin_bit_cast(u32x4, h1));
    HS16H(0xffffff, vL16, sH, 2048, __builtin_bit_cast(u32x4, h2));
    HS16H(0xffffff, vL16, sH, 3072, __builtin_bit_cast(u32x4, h3));
  }
  HS16H(0xffffff, vq, sq0, 0, __builtin_bit_cast(u32x4, q0));
  HS16H(0xffffff, vq, sq1, 0, __builtin_bit_cast(u32x4, q1));
  HS16H(0xffffff, vq, sq2, 0, __builtin_bit_cast(u32x4, q2));
  STAMP(3);
  unsigned fw, fa, vFW, vFA;
  { const int k_ = lane < 56 ? lane : 55; vFW = WS_FA + 0 + ((hp & 3) * 128 + 64 * (k_ >> 5) + 32 * (1 - hq) + (k_ & 31)) * (4 * 16); }
  { const int k_ = 8 * tt - 1 + (n16 < 9 ? n16 : 8); vFA = WS_FA + 0 + (g4 * 128 + (k_ < 0 ? 0 : k_)) * (4 * 16); }
  unsigned vQ = w == 3 && (r8 & 1) ? 0x40000000u : vF      , sq[6];
#pragma unroll
  for (int t = 0; t < 6; ++t) { sq[t] = WS_WQ + ((hp * 4 + w) * 6 + t) * 8192; OPQ(sq[t]); }
  unsigned vXN = r8 * 1024 + ((lane & 7) ^ (4 * (w & 1) + (r8 >> 1))) * 16, sXa = WS_XN + (128 * tt - 16 + 8 * w) * 1024, sXb = sXa + (w >= 2 ? 128 - 147456 : 0);
  unsigned sXc = w >= 2 ? 0x80000000u : sXa, ldsX = lds0 + w * 1024, sXt;
  unsigned xr0 = lds0 + n16 * 128 + ((g4 ^ (n16 >> 1)) * 16), xr1 = xr0 ^ 64;
  unsigned sk0, sk1, sv, rk0, rk1, rv0, rv1, vS, sO, sFO = WS_FO + ((tt * 4 + w) * 8 + hp) * (4 * 16);
  { const int c = 16 * w + 4 * g4;
    const bool ok = c < 56;
    const int hd = ok ? (c >= 28) : (g4 & 1), d = ok ? c - 28 * hd : 28, row = (n16 >> 1) + 72 * (n16 & 1);
    sk0 = lds0 + hd * 9216 + row * 64 + (((d >> 3) ^ (n16 >> 3) ^ (2 * (n16 & 1))) << 4) + ((d >> 2) & 1) * 8; sk1 = sk0 ^ 32;
    sv = lds0 + (2 * hd + (d >> 4)) * 4608 + row * 32 + ((((d >> 2) & 3) ^ (n16 >> 3) ^ (2 * (n16 & 1))) << 3);
    rk0 = lds0 + (16 * w + n16) * 64 + ((g4 ^ q4) << 4); rk1 = (rk0 ^ 32) + 4608;
    rv0 = lds0 + (16 * w + 4 * g4 + q4) * 32 + (pq ^ (g4 & 1)) * 8; rv1 = (rv0 ^ 16) + 2304;
    const int ga = 7 * (hp & 3) + g4;
    vS = (n16 >> 3) * 16384 + (ga & 3) * 2048 + (ga >> 2) * 128 + (n16 & 7) * 16; sO = WS_OT + (8 * tt + 2 * w) * 16384 + (hp >> 2) * 8192; }
  bf16x8 wq[14][3];
  asm volatile("" : "+v"(vFW), "+v"(vFA), "+v"(vQ), "+v"(vXN), "+v"(xr0), "+v"(xr1)); asm volatile("" : "+s"(sXa), "+s"(sXb), "+s"(sXc), "+s"(ldsX));
  asm volatile("" : "+v"(sk0), "+v"(sk1), "+v"(sv), "+v"(rk0), "+v"(rk1), "+v"(rv0), "+v"(rv1), "+v"(vS)); asm volatile("" : "+s"(sO), "+s"(sFO));
  u32x4 rs2 = srd(w2, 1792u * 4096u); OPQ(rs2);
  unsigned vW2[4];
#pragma unroll
  for (int i = 0; i < 4; ++i) vW2[i] = (8 * (r8 & 3) + 4 * (r8 >> 2)) * 4096 + st * 128 + ((lane & 7) ^ (2 * i)) * 16 + i * (4096 - 1024);
  int rot2 = 4 * dt + 2 * (st & 7) + 0 * (b & 1); rot2 -= 14 * ((rot2 * 37) >> 9);
  bf16x8 fw2[14][4];
  unsigned ldsS = lds0 + S3B + w * 4096, ldsO = lds0 + WOB + w * 8192, ldsP = lds0 + WOB + 24576 + w * 6144, sWo, vWo = r8 * 16384 + (lane & 7) * 16, sWt;
  { const int k_ = BLK(w), k7 = (k_ * 37) >> 8; sWo = WS_WO + k7 * (131072 - 896) + k_ * 128; }
  unsigned fq, vFQ = WS_FQ + (lane < 56 ? lane : 55) * (4 * 4);
  asm volatile("" : "+v"(vW2[0]), "+v"(vW2[1]), "+v"(vW2[2]), "+v"(vW2[3]), "+v"(vFQ), "+v"(vWo)); asm volatile("" : "+s"(rot2), "+s"(ldsS), "+s"(ldsO), "+s"(ldsP), "+s"(sWo));
  unsigned tS[4];
#pragma unroll
  for (int i = 0; i < 4; ++i) { tS[i] = ldsS + q4 * 1024 + g4 * 128 + pq * 8 + ((i ^ q4) * 32); OPV(tS[i]); }
  unsigned tO[4];
#pragma unroll
  for (int i = 0; i < 4; ++i) { tO[i] = ldsO + q4 * 1024 + g4 * 128 + pq * 8 + ((i ^ q4) * 32); OPV(tO[i]); }
  unsigned su[14], sw[14], sc_ = 14 * w + rot2; OPQ(sc_);
  unsigned ldsZ = lds0 + w * 4096; OPQ(ldsZ);
  unsigned tZ[4];
#pragma unroll
  for (int i = 0; i < 4; ++i) { tZ[i] = ldsZ + q4 * 1024 + g4 * 128 + pq * 8 + ((i ^ q4) * 32); OPV(tZ[i]); }
  const int dk = 4 * g4 - n16;
  f32x4 cm; unsigned m1[4], mp[2], mq_ = g4 == 3 ? 0u : ~0u;
  const float cz = tt + w == 0 && lane < 32 ? -998244352.f : 0.f;
#pragma unroll
  for (int i = 0; i < 4; ++i) { m1[i] = (unsigned)((dk + i + 7) >> 31); cm[i] = __uint_as_float(~m1[i] & __float_as_uint((unsigned)(dk + i - 1) < 8u ? cz : -__builtin_inff())); }
  mp[0] = (m1[0] & 0xffffu) | (m1[1] & 0xffff0000u); mp[1] = (m1[2] & 0xffffu) | (m1[3] & 0xffff0000u);
  asm volatile("" : "+v"(cm), "+v"(m1[0]), "+v"(m1[1]), "+v"(m1[2]), "+v"(m1[3]), "+v"(mp[0]), "+v"(mp[1]), "+v"(mq_));
  asm volatile("s_waitcnt vmcnt(0)"
      :  :  : "memory"); SB();
  STFA(sFA + 0);
  STAMP(4);
  { const unsigned vT = lane < 14 ? ((jb * 4 + w) * 14 + lane) * 4096 + ((st & ~7) + ((3 - (int)((size_t)w2 >> 7)) & 7)) * 128 : 0x40000000u; TOUCH(vT, rs2, 0u, 0); }
  unsigned vFU, vFO;
  { const int da = (db0 * 37) >> 8, db = ((db0 + 3) * 37) >> 8, l_ = lane < 56 ? lane : 55;
    vFU = WS_FU + ((l_ < 28 ? da * 128 + l_ : db * 128 + l_ - 28) + w * 28) * (4 * 4);
    vFO = WS_FO + ((2 * st + (w >> 1)) * 8 + (lane & 7)) * (4 * 16);
    asm volatile("" : "+v"(vFU), "+v"(vFO)); }
  unsigned vFU1 = WS_FU + (512 + ((db0 * 37) >> 8) * 128 + w * 28 + (lane < 28 ? lane : 27)) * (4 * 4), fu2; OPV(vFU1);
  unsigned vFj[4], so0 = WS_OT + (4 * st + w) * 16384, so1 = so0 + 8192;
#pragma unroll
  for (int j = 0; j < 4; ++j) { const int k_ = BLK(j), k7 = (k_ * 37) >> 8; vFj[j] = vF + (WS_UT + k7 * (56 * 8192 - 896) + k_ * 128); OPV(vFj[j]); }
  asm volatile("" : "+s"(so0), "+s"(so1));
  const int tau = 2 * (n16 & 7) + (n16 >> 3);
  unsigned rwd = ldsS + vL16, rr = lds0 + S3B + w * 1024 + (16 * g4 + tau) * 16, ro = lds0 + WOB + vL16;
  unsigned xo = ldsS + 34816 + vL16, braw, xt = ldsS + 34816 + tau * 128 + (((g4 >> 1) ^ (n16 & 7)) * 16) + (g4 & 1) * 8, vB2 = (64 * st + 16 * w + tau) * 2;
  unsigned xs = xt ^ (32 * 0);
  asm volatile("" : "+v"(rwd), "+v"(rr), "+v"(ro), "+v"(xt), "+v"(xs), "+v"(xo), "+v"(vB2));
  unsigned vXa = (64 * st + 16 * w + r8) * 896 + db0 * 32 + ((lane & 7) ^ (r8 >> 1)) * 16, vXb = (vXa ^ 64) + (8 * 896 - 1024); OPV(vXa); OPV(vXb);
  { unsigned k_ = sc_ + (rot2 >= 14 ? -14 : 0); OPQ(k_); su[0] = k_ << 13; sw[0] = k_ << 17; OPQ(su[0]); OPQ(sw[0]); }
  { unsigned k_ = sc_ + (rot2 >= 13 ? -13 : 1); OPQ(k_); su[1] = k_ << 13; sw[1] = k_ << 17; OPQ(su[1]); OPQ(sw[1]); }
  { unsigned k_ = sc_ + (rot2 >= 12 ? -12 : 2); OPQ(k_); su[2] = k_ << 13; sw[2] = k_ << 17; OPQ(su[2]); OPQ(sw[2]); }
  { unsigned k_ = sc_ + (rot2 >= 11 ? -11 : 3); OPQ(k_); su[3] = k_ << 13; sw[3] = k_ << 17; OPQ(su[3]); OPQ(sw[3]); }
  { unsigned k_ = sc_ + (rot2 >= 10 ? -10 : 4); OPQ(k_); su[4] = k_ << 13; sw[4] = k_ << 17; OPQ(su[4]); OPQ(sw[4]); }
  { unsigned k_ = sc_ + (rot2 >= 9 ? -9 : 5); OPQ(k_); su[5] = k_ << 13; sw[5] = k_ << 17; OPQ(su[5]); OPQ(sw[5]); }
  { unsigned k_ = sc_ + (rot2 >= 8 ? -8 : 6); OPQ(k_); su[6] = k_ << 13; sw[6] = k_ << 17; OPQ(su[6]); OPQ(sw[6]); }
  { unsigned k_ = sc_ + (rot2 >= 7 ? -7 : 7); OPQ(k_); su[7] = k_ << 13; sw[7] = k_ << 17; OPQ(su[7]); OPQ(sw[7]); }
  { unsigned k_ = sc_ + (rot2 >= 6 ? -6 : 8); OPQ(k_); su[8] = k_ << 13; sw[8] = k_ << 17; OPQ(su[8]); OPQ(sw[8]); }
  { unsigned k_ = sc_ + (rot2 >= 5 ? -5 : 9); OPQ(k_); su[9] = k_ << 13; sw[9] = k_ << 17; OPQ(su[9]); OPQ(sw[9]); }
  { unsigned k_ = sc_ + (rot2 >= 4 ? -4 : 10); OPQ(k_); su[10] = k_ << 13; sw[10] = k_ << 17; OPQ(su[10]); OPQ(sw[10]); }
  { unsigned k_ = sc_ + (rot2 >= 3 ? -3 : 11); OPQ(k_); su[11] = k_ << 13; sw[11] = k_ << 17; OPQ(su[11]); OPQ(sw[11]); }
  { unsigned k_ = sc_ + (rot2 >= 2 ? -2 : 12); OPQ(k_); su[12] = k_ << 13; sw[12] = k_ << 17; OPQ(su[12]); OPQ(sw[12]); }
  { unsigned k_ = sc_ + (rot2 >= 1 ? -1 : 13); OPQ(k_); su[13] = k_ << 13; sw[13] = k_ << 17; OPQ(su[13]); OPQ(sw[13]); }
  unsigned sWq[4] = {sWo, sWo + 4096, sWo + 8192, sWo + 12288}, wwa = ldsO + vL16, wwb = ldsP + vL16; OPQ(sWq[1]); OPQ(sWq[2]); OPQ(sWq[3]); OPV(wwa); OPV(wwb);
  unsigned vT2 = (lane & 31) * 4096 + st * 128 + (lane < 32 ? sw[2] : sw[3]); OPV(vT2);
  u32x2 xv[4]; unsigned xta[4];
  u32x4 rsX = srd(x, 2048u * 896u); OPQ(rsX);
  asm volatile("s_add_u32 m0, %1, 34816\n\ts_nop 0\n\tbuffer_load_dwordx4 %2, %3, %4 offen offset:0  lds\n\tbuffer_load_dwordx4 %5, %3, %4 offen offset:1024  lds\n\tglobal_load_ushort %0, %6, %7 offset:0"
      : "=&v"(braw) : "s"(ldsS), "v"(vXa), "s"(rsX), "s"(0u), "v"(vXb), "v"(vB2), "s"(b2) : "memory", "scc"); SB();
  STAMP(5);
  __builtin_amdgcn_s_sleep(2);
  __builtin_amdgcn_s_sleep(2);
  __builtin_amdgcn_s_sleep(2);
  __builtin_amdgcn_s_sleep(2);
  asm volatile("buffer_load_dword %0, %2, %3, 0 offen sc1\n\tbuffer_load_dword %1, %4, %3, 0 offen sc1"
      : "=&v"(fw), "=&v"(fa) : "v"(vFW), "s"(rsW), "v"(vFA) : "memory"); SB();
  BAR();
  WAITV2(0, fw, fa);
  fa = (fa ^ nonce) | (fw ^ nonce) ? ~nonce : nonce; if (__ballot(fa != nonce) != 0ull) poll1<1>(rsW, vFA, vFW, nonce);
  STAMP(6);
  asm volatile("s_nop 0\n\ts_add_u32 m0, %1, 0\n\ts_add_u32 %0, %2, 0\n\tbuffer_load_dwordx4 %3, %4, %0 offen offset:0 sc1 lds\n\ts_add_u32 m0, %1, 4096\n\ts_add_u32 %0, %2, 32768\n\tbuffer_load_dwordx4 %3, %4, %0 offen offset:0 sc1 lds\n\ts_add_u32 m0, %1, 8192\n\ts_add_u32 %0, %2, 65536\n\tbuffer_load_dwordx4 %3, %4, %0 offen offset:0 sc1 lds\n\ts_add_u32 m0, %1, 12288\n\ts_add_u32 %0, %2, 98304\n\tbuffer_load_dwordx4 %3, %4, %0 offen offset:0 sc1 lds\n\ts_add_u32 m0, %1, 16384\n\ts_add_u32 %0, %5, 131072\n\tbuffer_load_dwordx4 %3, %4, %0 offen offset:0 sc1 lds"
      : "=&s"(sXt) : "s"(ldsX), "s"(sXa), "v"(vXN), "s"(rsW), "s"(sXb) : "memory", "scc"); SB();
  STAMP(7);
  asm volatile("buffer_load_dwordx4 %0, %10, %11, %12 offen offset:0 sc1\n\tbuffer_load_dwordx4 %1, %10, %11, %13 offen offset:0 sc1\n\tbuffer_load_dwordx4 %2, %10, %11, %14 offen offset:0 sc1\n\tbuffer_load_dwordx4 %3, %10, %11, %12 offen offset:128 sc1\n\tbuffer_load_dwordx4 %4, %10, %11, %13 offen offset:128 sc1\n\tbuffer_load_dwordx4 %5, %10, %11, %14 offen offset:128 sc1\n\ts_add_u32 m0, %15, 20480\n\ts_add_u32 %6, %16, 16512\n\tbuffer_load_dwordx4 %17, %11, %6 offen offset:0 sc1 lds\n\ts_add_u32 m0, %15, 24576\n\ts_add_u32 %6, %16, 49280\n\tbuffer_load_dwordx4 %17, %11, %6 offen offset:0 sc1 lds\n\ts_add_u32 m0, %15, 28672\n\ts_add_u32 %6, %16, 82048\n\tbuffer_load_dwordx4 %17, %11, %6 offen offset:0 sc1 lds\n\ts_add_u32 m0, %15, 32768\n\ts_add_u32 %6, %16, 114816\n\tbuffer_load_dwordx4 %17, %11, %6 offen offset:0 sc1 lds\n\tbuffer_load_dwordx4 %7, %10, %11, %12 offen offset:256 sc1\n\tbuffer_load_dwordx4 %8, %10, %11, %13 offen offset:256 sc1\n\tbuffer_load_dwordx4 %9, %10, %11, %14 offen offset:256 sc1"
      : "=&a"(wq[0][0]), "=&a"(wq[0][1]), "=&a"(wq[0][2]), "=&a"(wq[1][0]), "=&a"(wq[1][1]), "=&a"(wq[1][2]), "=&s"(sXt), "=&a"(wq[2][0]), "=&a"(wq[2][1]), "=&a"(wq[2][2]) : "v"(vQ), "s"(rsW), "s"(sq[0]), "s"(sq[2]), "s"(sq[4]), "s"(ldsX), "s"(sXa), "v"(vXN) : "memory", "scc"); SB();
  f32x4 aq[8], ak[9], av[9]; bf16x8 xf[14][9]; u32x2 sg[27];
  asm volatile("s_waitcnt vmcnt(13)"
      :  :  : "memory"); SB();
  BAR0();
  STAMP(8);
  asm volatile("ds_read_b128 %0, %9 offset:0\n\tds_read_b128 %1, %9 offset:2048\n\tds_read_b128 %2, %9 offset:4096\n\tds_read_b128 %3, %9 offset:6144\n\tds_read_b128 %4, %9 offset:8192\n\tds_read_b128 %5, %9 offset:10240\n\tds_read_b128 %6, %9 offset:12288\n\tds_read_b128 %7, %9 offset:14336\n\tds_read_b128 %8, %9 offset:16384"
      : "=&v"(xf[0][0]), "=&v"(xf[0][1]), "=&v"(xf[0][2]), "=&v"(xf[0][3]), "=&v"(xf[0][4]), "=&v"(xf[0][5]), "=&v"(xf[0][6]), "=&v"(xf[0][7]), "=&v"(xf[0][8]) : "v"(xr0) : "memory"); SB();
  WAITVB(10);
  WAITL(8);
  SB();
  asm volatile("v_mfma_f32_16x16x32_bf16 %0, %4, %5, 0\n\ts_add_u32 m0, %6, 36864\n\ts_add_u32 %1, %7, 256\n\tv_mfma_f32_16x16x32_bf16 %2, %8, %5, 0\n\tds_read_b128 %3, %9 offset:0"
      : "=&v"(ak[0]), "=&s"(sXt), "=&v"(av[0]), "=&v"(xf[1][0]) : "a"(wq[0][1]), "v"(xf[0][0]), "s"(ldsX), "s"(sXa), "a"(wq[0][2]), "v"(xr1) : "memory", "scc"); SB();
  WAITL(8);
  SB();
  asm volatile("v_mfma_f32_16x16x32_bf16 %0, %4, %5, 0\n\tbuffer_load_dwordx4 %6, %7, %8 offen offset:0 sc1 lds\n\tv_mfma_f32_16x16x32_bf16 %1, %9, %5, 0\n\tds_read_b128 %2, %10 offset:2048\n\tv_mfma_f32_16x16x32_bf16 %3, %11, %5, 0"
      : "=&v"(ak[1]), "=&v"(av[1]), "=&v"(xf[1][1]), "=&v"(aq[0]) : "a"(wq[0][1]), "v"(xf[0][1]), "v"(vXN), "s"(rsW), "s"(sXt), "a"(wq[0][2]), "v"(xr1), "a"(wq[0][0]) : "memory"); SB();
  WAITL(8);
  SB();
  asm volatile("v_mfma_f32_16x16x32_bf16 %0, %6, %7, 0\n\tds_read_b128 %1, %8 offset:4096\n\tv_mfma_f32_16x16x32_bf16 %2, %9, %7, 0\n\tbuffer_load_dwordx4 %3, %10, %11, %12 offen offset:384 sc1\n\tv_mfma_f32_16x16x32_bf16 %4, %13, %7, 0\n\tds_read_b128 %5, %8 offset:6144"
      : "=&v"(ak[2]), "=&v"(xf[1][2]), "=&v"(av[2]), "=&a"(wq[3][0]), "=&v"(aq[1]), "=&v"(xf[1][3]) : "a"(wq[0][1]), "v"(xf[0][2]), "v"(xr1), "a"(wq[0][2]), "v"(vQ), "s"(rsW), "s"(sq[0]), "a"(wq[0][0]) : "memory"); SB();
  WAITL(9);
  SB();
  asm volatile("v_mfma_f32_16x16x32_bf16 %0, %5, %6, 0\n\ts_add_u32 m0, %7, 40960\n\ts_add_u32 %1, %8, 33024\n\tv_mfma_f32_16x16x32_bf16 %2, %9, %6, 0\n\tds_read_b128 %3, %10 offset:8192\n\tv_mfma_f32_16x16x32_bf16 %4, %11, %6, 0"
      : "=&v"(ak[3]), "=&s"(sXt), "=&v"(av[3]), "=&v"(xf[1][4]), "=&v"(aq[2]) : "a"(wq[0][1]), "v"(xf[0][3]), "s"(ldsX), "s"(sXa), "a"(wq[0][2]), "v"(xr1), "a"(wq[0][0]) : "memory", "scc"); SB();
  WAITL(9);
  SB();
  asm volatile("v_mfma_f32_16x16x32_bf16 %0, %5, %6, 0\n\tds_read_b128 %1, %7 offset:10240\n\tv_mfma_f32_16x16x32_bf16 %2, %8, %6, 0\n\tbuffer_load_dwordx4 %9, %10, %11 offen offset:0 sc1 lds\n\tv_mfma_f32_16x16x32_bf16 %3, %12, %6, 0\n\tds_read_b128 %4, %7 offset:12288"
      : "=&v"(ak[4]), "=&v"(xf[1][5]), "=&v"(av[4]), "=&v"(aq[3]), "=&v"(xf[1][6]) : "a"(wq[0][1]), "v"(xf[0][4]), "v"(xr1), "a"(wq[0][2]), "v"(vXN), "s"(rsW), "s"(sXt), "a"(wq[0][0]) : "memory"); SB();
  WAITL(10);
  SB();
  asm volatile("v_mfma_f32_16x16x32_bf16 %0, %5, %6, 0\n\tbuffer_load_dwordx4 %1, %7, %8, %9 offen offset:384 sc1\n\tv_mfma_f32_16x16x32_bf16 %2, %10, %6, 0\n\tds_read_b128 %3, %11 offset:14336\n\tv_mfma_f32_16x16x32_bf16 %4, %12, %6, 0"
      : "=&v"(ak[5]), "=&a"(wq[3][1]), "=&v"(av[5]), "=&v"(xf[1][7]), "=&v"(aq[4]) : "a"(wq[0][1]), "v"(xf[0][5]), "v"(vQ), "s"(rsW), "s"(sq[2]), "a"(wq[0][2]), "v"(xr1), "a"(wq[0][0]) : "memory"); SB();
  WAITL(10);
  SB();
  asm volatile("v_mfma_f32_16x16x32_bf16 %0, %5, %6, 0\n\tds_read_b128 %1, %7 offset:16384\n\tv_mfma_f32_16x16x32_bf16 %2, %8, %6, 0\n\ts_add_u32 m0, %9, 45056\n\ts_add_u32 %3, %10, 65792\n\tv_mfma_f32_16x16x32_bf16 %4, %11, %6, 0"
      : "=&v"(ak[6]), "=&v"(xf[1][8]), "=&v"(av[6]), "=&s"(sXt), "=&v"(aq[5]) : "a"(wq[0][1]), "v"(xf[0][6]), "v"(xr1), "a"(wq[0][2]), "s"(ldsX), "s"(sXa), "a"(wq[0][0]) : "memory", "scc"); SB();
  WAITL(10);
  SB();
  asm volatile("v_mfma_f32_16x16x32_bf16 %0, %4, %5, 0\n\tbuffer_load_dwordx4 %6, %7, %8 offen offset:0 sc1 lds\n\tv_mfma_f32_16x16x32_bf16 %1, %9, %5, 0\n\tbuffer_load_dwordx4 %2, %10, %7, %11 offen offset:384 sc1\n\tv_mfma_f32_16x16x32_bf16 %3, %12, %5, 0"
      : "=&v"(ak[7]), "=&v"(av[7]), "=&a"(wq[3][2]), "=&v"(aq[6]) : "a"(wq[0][1]), "v"(xf[0][7]), "v"(vXN), "s"(rsW), "s"(sXt), "a"(wq[0][2]), "v"(vQ), "s"(sq[4]), "a"(wq[0][0]) : "memory"); SB();
  WAITL(9);
  SB();
  asm volatile("v_mfma_f32_16x16x32_bf16 %0, %4, %5, 0\n\tbuffer_load_dwordx4 %1, %6, %7, %8 offen offset:512 sc1\n\tv_mfma_f32_16x16x32_bf16 %2, %9, %5, 0\n\tv_mfma_f32_16x16x32_bf16 %3, %10, %5, 0"
      : "=&v"(ak[8]), "=&a"(wq[4][0]), "=&v"(av[8]), "=&v"(aq[7]) : "a"(wq[0][1]), "v"(xf[0][8]), "v"(vQ), "s"(rsW), "s"(sq[0]), "a"(wq[0][2]), "a"(wq[0][0]) : "memory"); SB();
  WAITVB(14);
  WAITL(3);
  SB();
  asm volatile("v_mfma_f32_16x16x32_bf16 %0, %1, %2, %0\n\ts_waitcnt vmcnt(10)"
      : "+v"(ak[0]) : "a"(wq[1][1]), "v"(xf[1][0]) : "memory"); SB();
  BAR0();
  SB();
  asm volatile("v_mfma_f32_16x16x32_bf16 %0, %11, %12, %0\n\tds_read_b128 %1, %13 offset:18432\n\tv_mfma_f32_16x16x32_bf16 %2, %14, %15, %2\n\ts_add_u32 m0, %16, 49152\n\ts_add_u32 %3, %17, 98560\n\tv_mfma_f32_16x16x32_bf16 %4, %11, %15, %4\n\tds_read_b128 %5, %13 offset:20480\n\tv_mfma_f32_16x16x32_bf16 %6, %18, %15, %6\n\tbuffer_load_dwordx4 %19, %20, %3 offen offset:0 sc1 lds\n\tv_mfma_f32_16x16x32_bf16 %7, %14, %21, %7\n\tds_read_b128 %8, %13 offset:22528\n\tv_mfma_f32_16x16x32_bf16 %9, %11, %21, %9\n\tbuffer_load_dwordx4 %10, %22, %20, %23 offen offset:512 sc1"
      : "+v"(av[0]), "=&v"(xf[2][0]), "+v"(ak[1]), "=&s"(sXt), "+v"(av[1]), "=&v"(xf[2][1]), "+v"(aq[0]), "+v"(ak[2]), "=&v"(xf[2][2]), "+v"(av[2]), "=&a"(wq[4][1]) : "a"(wq[1][2]), "v"(xf[1][0]), "v"(xr0), "a"(wq[1][1]), "v"(xf[1][1]), "s"(ldsX), "s"(sXa), "a"(wq[1][0]), "v"(vXN), "s"(rsW), "v"(xf[1][2]), "v"(vQ), "s"(sq[2]) : "memory", "scc"); SB();
  asm volatile("v_mfma_f32_16x16x32_bf16 %0, %11, %12, %0\n\tds_read_b128 %1, %13 offset:24576\n\tv_mfma_f32_16x16x32_bf16 %2, %14, %15, %2\n\ts_add_u32 m0, %16, 53248\n\ts_add_u32 %3, %17, 131328\n\tv_mfma_f32_16x16x32_bf16 %4, %18, %15, %4\n\tds_read_b128 %5, %13 offset:26624\n\tv_mfma_f32_16x16x32_bf16 %6, %11, %15, %6\n\tbuffer_load_dwordx4 %19, %20, %3 offen offset:0 sc1 lds\n\tv_mfma_f32_16x16x32_bf16 %7, %14, %21, %7\n\tds_read_b128 %8, %13 offset:28672\n\tv_mfma_f32_16x16x32_bf16 %9, %18, %21, %9\n\tbuffer_load_dwordx4 %10, %22, %20, %23 offen offset:512 sc1"
      : "+v"(aq[1]), "=&v"(xf[2][3]), "+v"(ak[3]), "=&s"(sXt), "+v"(av[3]), "=&v"(xf[2][4]), "+v"(aq[2]), "+v"(ak[4]), "=&v"(xf[2][5]), "+v"(av[4]), "=&a"(wq[4][2]) : "a"(wq[1][0]), "v"(xf[1][2]), "v"(xr0), "a"(wq[1][1]), "v"(xf[1][3]), "s"(ldsX), "s"(sXb), "a"(wq[1][2]), "v"(vXN), "s"(rsW), "v"(xf[1][4]), "v"(vQ), "s"(sq[4]) : "memory", "scc"); SB();
  asm volatile("v_mfma_f32_16x16x32_bf16 %0, %7, %8, %0\n\tds_read_b128 %1, %9 offset:30720\n\tv_mfma_f32_16x16x32_bf16 %2, %10, %11, %2\n\ts_add_u32 m0, %12, 57344\n\ts_add_u32 %3, %13, 16768\n\tv_mfma_f32_16x16x32_bf16 %4, %14, %11, %4\n\tds_read_b128 %5, %9 offset:32768\n\tv_mfma_f32_16x16x32_bf16 %6, %7, %11, %6"
      : "+v"(aq[3]), "=&v"(xf[2][6]), "+v"(ak[5]), "=&s"(sXt), "+v"(av[5]), "=&v"(xf[2][7]), "+v"(aq[4]) : "a"(wq[1][0]), "v"(xf[1][4]), "v"(xr0), "a"(wq[1][1]), "v"(xf[1][5]), "s"(ldsX), "s"(sXa), "a"(wq[1][2]) : "memory", "scc"); SB();
  WAITL(8);
  SB();
  asm volatile("v_mfma_f32_16x16x32_bf16 %0, %10, %11, %0\n\tds_read_b128 %1, %12 offset:34816\n\tv_mfma_f32_16x16x32_bf16 %2, %13, %11, %2\n\tbuffer_load_dwordx4 %14, %15, %16 offen offset:0 sc1 lds\n\tv_mfma_f32_16x16x32_bf16 %3, %17, %11, %3\n\tbuffer_load_dwordx4 %4, %18, %15, %19 offen offset:640 sc1\n\tv_mfma_f32_16x16x32_bf16 %5, %10, %20, %5\n\tbuffer_load_dwordx4 %6, %18, %15, %21 offen offset:640 sc1\n\tv_mfma_f32_16x16x32_bf16 %7, %13, %20, %7\n\tv_mfma_f32_16x16x32_bf16 %8, %17, %20, %8\n\tv_mfma_f32_16x16x32_bf16 %9, %10, %22, %9"
      : "+v"(ak[6]), "=&v"(xf[2][8]), "+v"(av[6]), "+v"(aq[5]), "=&a"(wq[5][0]), "+v"(ak[7]), "=&a"(wq[5][1]), "+v"(av[7]), "+v"(aq[6]), "+v"(ak[8]) : "a"(wq[1][1]), "v"(xf[1][6]), "v"(xr0), "a"(wq[1][2]), "v"(vXN), "s"(rsW), "s"(sXt), "a"(wq[1][0]), "v"(vQ), "s"(sq[0]), "v"(xf[1][7]), "s"(sq[2]), "v"(xf[1][8]) : "memory"); SB();
  asm volatile("v_mfma_f32_16x16x32_bf16 %0, %2, %3, %0\n\tv_mfma_f32_16x16x32_bf16 %1, %4, %3, %1"
      : "+v"(av[8]), "+v"(aq[7]) : "a"(wq[1][2]), "v"(xf[1][8]), "a"(wq[1][0])); SB();
  STAMP(30);
  WAITVB(14);
  WAITL(3);
  SB();
  asm volatile("v_mfma_f32_16x16x32_bf16 %0, %11, %12, %0\n\ts_add_u32 m0, %13, 61440\n\ts_add_u32 %1, %14, 49536\n\tv_mfma_f32_16x16x32_bf16 %2, %15, %12, %2\n\tds_read_b128 %3, %16 offset:18432\n\tv_mfma_f32_16x16x32_bf16 %4, %11, %17, %4\n\tbuffer_load_dwordx4 %18, %19, %1 offen offset:0 sc1 lds\n\tv_mfma_f32_16x16x32_bf16 %5, %15, %17, %5\n\tds_read_b128 %6, %16 offset:20480\n\tv_mfma_f32_16x16x32_bf16 %7, %20, %17, %7\n\tbuffer_load_dwordx4 %8, %21, %19, %22 offen offset:640 sc1\n\tv_mfma_f32_16x16x32_bf16 %9, %11, %23, %9\n\tds_read_b128 %10, %16 offset:22528"
      : "+v"(ak[0]), "=&s"(sXt), "+v"(av[0]), "=&v"(xf[3][0]), "+v"(ak[1]), "+v"(av[1]), "=&v"(xf[3][1]), "+v"(aq[0]), "=&a"(wq[5][2]), "+v"(ak[2]), "=&v"(xf[3][2]) : "a"(wq[2][1]), "v"(xf[2][0]), "s"(ldsX), "s"(sXa), "a"(wq[2][2]), "v"(xr1), "v"(xf[2][1]), "v"(vXN), "s"(rsW), "a"(wq[2][0]), "v"(vQ), "s"(sq[4]), "v"(xf[2][2]) : "memory", "scc"); SB();
  asm volatile("v_mfma_f32_16x16x32_bf16 %0, %11, %12, %0\n\ts_add_u32 m0, %13, 65536\n\ts_add_u32 %1, %14, 82304\n\tv_mfma_f32_16x16x32_bf16 %2, %15, %12, %2\n\tds_read_b128 %3, %16 offset:24576\n\tv_mfma_f32_16x16x32_bf16 %4, %17, %18, %4\n\tbuffer_load_dwordx4 %19, %20, %1 offen offset:0 sc1 lds\n\tv_mfma_f32_16x16x32_bf16 %5, %11, %18, %5\n\tds_read_b128 %6, %16 offset:26624\n\tv_mfma_f32_16x16x32_bf16 %7, %15, %18, %7\n\tbuffer_load_dwordx4 %8, %21, %20, %22 offen offset:768 sc1\n\tv_mfma_f32_16x16x32_bf16 %9, %17, %23, %9\n\tds_read_b128 %10, %16 offset:28672"
      : "+v"(av[2]), "=&s"(sXt), "+v"(aq[1]), "=&v"(xf[3][3]), "+v"(ak[3]), "+v"(av[3]), "=&v"(xf[3][4]), "+v"(aq[2]), "=&a"(wq[6][0]), "+v"(ak[4]), "=&v"(xf[3][5]) : "a"(wq[2][2]), "v"(xf[2][2]), "s"(ldsX), "s"(sXa), "a"(wq[2][0]), "v"(xr1), "a"(wq[2][1]), "v"(xf[2][3]), "v"(vXN), "s"(rsW), "v"(vQ), "s"(sq[0]), "v"(xf[2][4]) : "memory", "scc"); SB();
  asm volatile("v_mfma_f32_16x16x32_bf16 %0, %8, %9, %0\n\tbuffer_load_dwordx4 %1, %10, %11, %12 offen offset:768 sc1\n\tv_mfma_f32_16x16x32_bf16 %2, %13, %9, %2\n\tds_read_b128 %3, %14 offset:30720\n\tv_mfma_f32_16x16x32_bf16 %4, %15, %16, %4\n\tv_mfma_f32_16x16x32_bf16 %5, %8, %16, %5\n\tds_read_b128 %6, %14 offset:32768\n\tv_mfma_f32_16x16x32_bf16 %7, %13, %16, %7"
      : "+v"(av[4]), "=&a"(wq[6][1]), "+v"(aq[3]), "=&v"(xf[3][6]), "+v"(ak[5]), "+v"(av[5]), "=&v"(xf[3][7]), "+v"(aq[4]) : "a"(wq[2][2]), "v"(xf[2][4]), "v"(vQ), "s"(rsW), "s"(sq[2]), "a"(wq[2][0]), "v"(xr1), "a"(wq[2][1]), "v"(xf[2][5]) : "memory"); SB();
  WAITL(8);
  SB();
  asm volatile("v_mfma_f32_16x16x32_bf16 %0, %10, %11, %0\n\tds_read_b128 %1, %12 offset:34816\n\tv_mfma_f32_16x16x32_bf16 %2, %13, %11, %2\n\tv_mfma_f32_16x16x32_bf16 %3, %14, %11, %3\n\tv_mfma_f32_16x16x32_bf16 %4, %10, %15, %4\n\tv_mfma_f32_16x16x32_bf16 %5, %13, %15, %5\n\tv_mfma_f32_16x16x32_bf16 %6, %14, %15, %6\n\tv_mfma_f32_16x16x32_bf16 %7, %10, %16, %7\n\tv_mfma_f32_16x16x32_bf16 %8, %13, %16, %8\n\tv_mfma_f32_16x16x32_bf16 %9, %14, %16, %9"
      : "+v"(ak[6]), "=&v"(xf[3][8]), "+v"(av[6]), "+v"(aq[5]), "+v"(ak[7]), "+v"(av[7]), "+v"(aq[6]), "+v"(ak[8]), "+v"(av[8]), "+v"(aq[7]) : "a"(wq[2][1]), "v"(xf[2][6]), "v"(xr1), "a"(wq[2][2]), "a"(wq[2][0]), "v"(xf[2][7]), "v"(xf[2][8]) : "memory"); SB();
  WAITVB(13);
  WAITL(3);
  SB();
  asm volatile("v_mfma_f32_16x16x32_bf16 %0, %1, %2, %0\n\ts_waitcnt vmcnt(9)"
      : "+v"(ak[0]) : "a"(wq[3][1]), "v"(xf[3][0]) : "memory"); SB();
  BAR0();
  SB();
  asm volatile("v_mfma_f32_16x16x32_bf16 %0, %11, %12, %0\n\tds_read_b128 %1, %13 offset:36864\n\tv_mfma_f32_16x16x32_bf16 %2, %14, %15, %2\n\ts_add_u32 m0, %16, 69632\n\ts_add_u32 %3, %17, 115072\n\tv_mfma_f32_16x16x32_bf16 %4, %11, %15, %4\n\tds_read_b128 %5, %13 offset:38912\n\tv_mfma_f32_16x16x32_bf16 %6, %18, %15, %6\n\tbuffer_load_dwordx4 %19, %20, %3 offen offset:0 sc1 lds\n\tv_mfma_f32_16x16x32_bf16 %7, %14, %21, %7\n\tds_read_b128 %8, %13 offset:40960\n\tv_mfma_f32_16x16x32_bf16 %9, %11, %21, %9\n\tbuffer_load_dwordx4 %10, %22, %20, %23 offen offset:768 sc1"
      : "+v"(av[0]), "=&v"(xf[4][0]), "+v"(ak[1]), "=&s"(sXt), "+v"(av[1]), "=&v"(xf[4][1]), "+v"(aq[0]), "+v"(ak[2]), "=&v"(xf[4][2]), "+v"(av[2]), "=&a"(wq[6][2]) : "a"(wq[3][2]), "v"(xf[3][0]), "v"(xr0), "a"(wq[3][1]), "v"(xf[3][1]), "s"(ldsX), "s"(sXa), "a"(wq[3][0]), "v"(vXN), "s"(rsW), "v"(xf[3][2]), "v"(vQ), "s"(sq[4]) : "memory", "scc"); SB();
  asm volatile("v_mfma_f32_16x16x32_bf16 %0, %11, %12, %0\n\tds_read_b128 %1, %13 offset:43008\n\tv_mfma_f32_16x16x32_bf16 %2, %14, %15, %2\n\ts_add_u32 m0, %16, 73728\n\ts_add_u32 %3, %17, 512\n\tv_mfma_f32_16x16x32_bf16 %4, %18, %15, %4\n\tds_read_b128 %5, %13 offset:45056\n\tv_mfma_f32_16x16x32_bf16 %6, %11, %15, %6\n\tbuffer_load_dwordx4 %19, %20, %3 offen offset:0 sc1 lds\n\tv_mfma_f32_16x16x32_bf16 %7, %14, %21, %7\n\tds_read_b128 %8, %13 offset:47104\n\tv_mfma_f32_16x16x32_bf16 %9, %18, %21, %9\n\tbuffer_load_dwordx4 %10, %22, %20, %23 offen offset:0 sc1"
      : "+v"(aq[1]), "=&v"(xf[4][3]), "+v"(ak[3]), "=&s"(sXt), "+v"(av[3]), "=&v"(xf[4][4]), "+v"(aq[2]), "+v"(ak[4]), "=&v"(xf[4][5]), "+v"(av[4]), "=&a"(wq[7][0]) : "a"(wq[3][0]), "v"(xf[3][2]), "v"(xr0), "a"(wq[3][1]), "v"(xf[3][3]), "s"(ldsX), "s"(sXa), "a"(wq[3][2]), "v"(vXN), "s"(rsW), "v"(xf[3][4]), "v"(vQ), "s"(sq[1]) : "memory", "scc"); SB();
  asm volatile("v_mfma_f32_16x16x32_bf16 %0, %7, %8, %0\n\tds_read_b128 %1, %9 offset:49152\n\tv_mfma_f32_16x16x32_bf16 %2, %10, %11, %2\n\ts_add_u32 m0, %12, 77824\n\ts_add_u32 %3, %13, 33280\n\tv_mfma_f32_16x16x32_bf16 %4, %14, %11, %4\n\tds_read_b128 %5, %9 offset:51200\n\tv_mfma_f32_16x16x32_bf16 %6, %7, %11, %6"
      : "+v"(aq[3]), "=&v"(xf[4][6]), "+v"(ak[5]), "=&s"(sXt), "+v"(av[5]), "=&v"(xf[4][7]), "+v"(aq[4]) : "a"(wq[3][0]), "v"(xf[3][4]), "v"(xr0), "a"(wq[3][1]), "v"(xf[3][5]), "s"(ldsX), "s"(sXa), "a"(wq[3][2]) : "memory", "scc"); SB();
  WAITL(8);
  SB();
  asm volatile("v_mfma_f32_16x16x32_bf16 %0, %10, %11, %0\n\tds_read_b128 %1, %12 offset:53248\n\tv_mfma_f32_16x16x32_bf16 %2, %13, %11, %2\n\tbuffer_load_dwordx4 %14, %15, %16 offen offset:0 sc1 lds\n\tv_mfma_f32_16x16x32_bf16 %3, %17, %11, %3\n\tbuffer_load_dwordx4 %4, %18, %15, %19 offen offset:0 sc1\n\tv_mfma_f32_16x16x32_bf16 %5, %10, %20, %5\n\tbuffer_load_dwordx4 %6, %18, %15, %21 offen offset:0 sc1\n\tv_mfma_f32_16x16x32_bf16 %7, %13, %20, %7\n\tv_mfma_f32_16x16x32_bf16 %8, %17, %20, %8\n\tv_mfma_f32_16x16x32_bf16 %9, %10, %22, %9"
      : "+v"(ak[6]), "=&v"(xf[4][8]), "+v"(av[6]), "+v"(aq[5]), "=&a"(wq[7][1]), "+v"(ak[7]), "=&a"(wq[7][2]), "+v"(av[7]), "+v"(aq[6]), "+v"(ak[8]) : "a"(wq[3][1]), "v"(xf[3][6]), "v"(xr0), "a"(wq[3][2]), "v"(vXN), "s"(rsW), "s"(sXt), "a"(wq[3][0]), "v"(vQ), "s"(sq[3]), "v"(xf[3][7]), "s"(sq[5]), "v"(xf[3][8]) : "memory"); SB();
  asm volatile("v_mfma_f32_16x16x32_bf16 %0, %2, %3, %0\n\tv_mfma_f32_16x16x32_bf16 %1, %4, %3, %1"
      : "+v"(av[8]), "+v"(aq[7]) : "a"(wq[3][2]), "v"(xf[3][8]), "a"(wq[3][0])); SB();
  WAITVB(15);
  WAITL(3);
  SB();
  asm volatile("v_mfma_f32_16x16x32_bf16 %0, %11, %12, %0\n\ts_add_u32 m0, %13, 81920\n\ts_add_u32 %1, %14, 66048\n\tv_mfma_f32_16x16x32_bf16 %2, %15, %12, %2\n\tds_read_b128 %3, %16 offset:36864\n\tv_xor_b32 %4, 0, %17\n\tv_mfma_f32_16x16x32_bf16 %5, %11, %18, %5\n\tbuffer_load_dwordx4 %19, %20, %1 offen offset:0 sc1 lds\n\tv_mfma_f32_16x16x32_bf16 %6, %15, %18, %6\n\tds_read_b128 %7, %16 offset:38912\n\tv_xor_b32 %8, 32, %17\n\tv_mfma_f32_16x16x32_bf16 %9, %21, %18, %9\n\tbuffer_load_dwordx4 %10, %22, %20, %23 offen offset:128 sc1"
      : "+v"(ak[0]), "=&s"(sXt), "+v"(av[0]), "=&v"(xf[5][0]), "=&v"(xta[0]), "+v"(ak[1]), "+v"(av[1]), "=&v"(xf[5][1]), "=&v"(xta[1]), "+v"(aq[0]), "=&a"(wq[8][0]) : "a"(wq[4][1]), "v"(xf[4][0]), "s"(ldsX), "s"(sXa), "a"(wq[4][2]), "v"(xr1), "v"(xs), "v"(xf[4][1]), "v"(vXN), "s"(rsW), "a"(wq[4][0]), "v"(vQ), "s"(sq[1]) : "memory", "scc"); SB();
  asm volatile("v_mfma_f32_16x16x32_bf16 %0, %12, %13, %0\n\tds_read_b128 %1, %14 offset:40960\n\tv_xor_b32 %2, 64, %15\n\tv_mfma_f32_16x16x32_bf16 %3, %16, %13, %3\n\ts_add_u32 m0, %17, 86016\n\ts_add_u32 %4, %18, 98816\n\tv_mfma_f32_16x16x32_bf16 %5, %19, %13, %5\n\tds_read_b128 %6, %14 offset:43008\n\tv_xor_b32 %7, 96, %15\n\tv_mfma_f32_16x16x32_bf16 %8, %12, %20, %8\n\tbuffer_load_dwordx4 %21, %22, %4 offen offset:0 sc1 lds\n\tv_mfma_f32_16x16x32_bf16 %9, %16, %20, %9\n\tds_read_b128 %10, %14 offset:45056\n\tds_read_b64 %11, %23 offset:0"
      : "+v"(ak[2]), "=&v"(xf[5][2]), "=&v"(xta[2]), "+v"(av[2]), "=&s"(sXt), "+v"(aq[1]), "=&v"(xf[5][3]), "=&v"(xta[3]), "+v"(ak[3]), "+v"(av[3]), "=&v"(xf[5][4]), "=&v"(xv[0]) : "a"(wq[4][1]), "v"(xf[4][2]), "v"(xr1), "v"(xs), "a"(wq[4][2]), "s"(ldsX), "s"(sXa), "a"(wq[4][0]), "v"(xf[4][3]), "v"(vXN), "s"(rsW), "v"(xta[0]) : "memory", "scc"); SB();
  asm volatile("v_mfma_f32_16x16x32_bf16 %0, %11, %12, %0\n\tbuffer_load_dwordx4 %1, %13, %14, %15 offen offset:128 sc1\n\tv_mfma_f32_16x16x32_bf16 %2, %16, %17, %2\n\tds_read_b128 %3, %18 offset:47104\n\tds_read_b64 %4, %19 offset:0\n\tv_mfma_f32_16x16x32_bf16 %5, %20, %17, %5\n\tbuffer_load_dwordx4 %6, %13, %14, %21 offen offset:128 sc1\n\tv_mfma_f32_16x16x32_bf16 %7, %11, %17, %7\n\tds_read_b128 %8, %18 offset:49152\n\tds_read_b64 %9, %22 offset:0\n\tv_mfma_f32_16x16x32_bf16 %10, %16, %23, %10"
      : "+v"(aq[2]), "=&a"(wq[8][1]), "+v"(ak[4]), "=&v"(xf[5][5]), "=&v"(xv[1]), "+v"(av[4]), "=&a"(wq[8][2]), "+v"(aq[3]), "=&v"(xf[5][6]), "=&v"(xv[2]), "+v"(ak[5]) : "a"(wq[4][0]), "v"(xf[4][3]), "v"(vQ), "s"(rsW), "s"(sq[3]), "a"(wq[4][1]), "v"(xf[4][4]), "v"(xr1), "v"(xta[1]), "a"(wq[4][2]), "s"(sq[5]), "v"(xta[2]), "v"(xf[4][5]) : "memory"); SB();
  asm volatile("buffer_load_dwordx4 %0, %5, %6, %7 offen offset:256 sc1\n\tv_mfma_f32_16x16x32_bf16 %1, %8, %9, %1\n\tds_read_b128 %2, %10 offset:51200\n\tds_read_b64 %3, %11 offset:0\n\tv_mfma_f32_16x16x32_bf16 %4, %12, %9, %4"
      : "=&a"(wq[9][0]), "+v"(av[5]), "=&v"(xf[5][7]), "=&v"(xv[3]), "+v"(aq[4]) : "v"(vQ), "s"(rsW), "s"(sq[1]), "a"(wq[4][2]), "v"(xf[4][5]), "v"(xr1), "v"(xta[3]), "a"(wq[4][0]) : "memory"); SB();
  WAITL(12);
  SB();
  asm volatile("v_mfma_f32_16x16x32_bf16 %0, %10, %11, %0\n\tds_read_b128 %1, %12 offset:53248\n\tv_mfma_f32_16x16x32_bf16 %2, %13, %11, %2\n\tv_mfma_f32_16x16x32_bf16 %3, %14, %11, %3\n\tv_mfma_f32_16x16x32_bf16 %4, %10, %15, %4\n\tv_mfma_f32_16x16x32_bf16 %5, %13, %15, %5\n\tv_mfma_f32_16x16x32_bf16 %6, %14, %15, %6\n\tv_mfma_f32_16x16x32_bf16 %7, %10, %16, %7\n\tv_mfma_f32_16x16x32_bf16 %8, %13, %16, %8\n\tv_mfma_f32_16x16x32_bf16 %9, %14, %16, %9"
      : "+v"(ak[6]), "=&v"(xf[5][8]), "+v"(av[6]), "+v"(aq[5]), "+v"(ak[7]), "+v"(av[7]), "+v"(aq[6]), "+v"(ak[8]), "+v"(av[8]), "+v"(aq[7]) : "a"(wq[4][1]), "v"(xf[4][6]), "v"(xr1), "a"(wq[4][2]), "a"(wq[4][0]), "v"(xf[4][7]), "v"(xf[4][8]) : "memory"); SB();
  WAITVB(16);
  WAITL(6);
  SB();
  asm volatile("v_mfma_f32_16x16x32_bf16 %0, %3, %4, %0\n\tv_add_u32 %1, 55296, %1\n\tv_mfma_f32_16x16x32_bf16 %2, %5, %4, %2\n\ts_waitcnt vmcnt(12)"
      : "+v"(ak[0]), "+v"(xr0), "+v"(av[0]) : "a"(wq[5][1]), "v"(xf[5][0]), "a"(wq[5][2]) : "memory"); SB();
  BAR0();
  SB();
  asm volatile("v_mfma_f32_16x16x32_bf16 %0, %12, %13, %0\n\tds_read_b128 %1, %14 offset:0\n\tv_mfma_f32_16x16x32_bf16 %2, %15, %13, %2\n\tds_read_b128 %3, %14 offset:2048\n\tv_mfma_f32_16x16x32_bf16 %4, %16, %13, %4\n\ts_add_u32 m0, %17, 90112\n\ts_add_u32 %5, %18, 131584\n\tv_mfma_f32_16x16x32_bf16 %6, %12, %19, %6\n\tds_read_b128 %7, %14 offset:4096\n\tv_mfma_f32_16x16x32_bf16 %8, %15, %19, %8\n\tbuffer_load_dwordx4 %20, %21, %5 offen offset:0 sc1 lds\n\tv_mfma_f32_16x16x32_bf16 %9, %16, %19, %9\n\tds_read_b128 %10, %14 offset:6144\n\tv_mfma_f32_16x16x32_bf16 %11, %12, %22, %11"
      : "+v"(ak[1]), "=&v"(xf[6][0]), "+v"(av[1]), "=&v"(xf[6][1]), "+v"(aq[0]), "=&s"(sXt), "+v"(ak[2]), "=&v"(xf[6][2]), "+v"(av[2]), "+v"(aq[1]), "=&v"(xf[6][3]), "+v"(ak[3]) : "a"(wq[5][1]), "v"(xf[5][1]), "v"(xr0), "a"(wq[5][2]), "a"(wq[5][0]), "s"(ldsX), "s"(sXb), "v"(xf[5][2]), "v"(vXN), "s"(rsW), "v"(xf[5][3]) : "memory", "scc"); SB();
  asm volatile("buffer_load_dwordx4 %0, %11, %12, %13 offen offset:256 sc1\n\tv_mfma_f32_16x16x32_bf16 %1, %14, %15, %1\n\tds_read_b128 %2, %16 offset:8192\n\tv_mfma_f32_16x16x32_bf16 %3, %17, %15, %3\n\ts_add_u32 m0, %18, 94208\n\ts_add_u32 %4, %19, 17024\n\tv_mfma_f32_16x16x32_bf16 %5, %20, %21, %5\n\tds_read_b128 %6, %16 offset:10240\n\tv_mfma_f32_16x16x32_bf16 %7, %14, %21, %7\n\tbuffer_load_dwordx4 %22, %12, %4 offen offset:0 sc1 lds\n\tv_mfma_f32_16x16x32_bf16 %8, %17, %21, %8\n\tds_read_b128 %9, %16 offset:12288\n\tv_mfma_f32_16x16x32_bf16 %10, %20, %23, %10"
      : "=&a"(wq[9][1]), "+v"(av[3]), "=&v"(xf[6][4]), "+v"(aq[2]), "=&s"(sXt), "+v"(ak[4]), "=&v"(xf[6][5]), "+v"(av[4]), "+v"(aq[3]), "=&v"(xf[6][6]), "+v"(ak[5]) : "v"(vQ), "s"(rsW), "s"(sq[3]), "a"(wq[5][2]), "v"(xf[5][3]), "v"(xr0), "a"(wq[5][0]), "s"(ldsX), "s"(sXa), "a"(wq[5][1]), "v"(xf[5][4]), "v"(vXN), "v"(xf[5][5]) : "memory", "scc"); SB();
  asm volatile("buffer_load_dwordx4 %0, %4, %5, %6 offen offset:256 sc1\n\tv_mfma_f32_16x16x32_bf16 %1, %7, %8, %1\n\tds_read_b128 %2, %9 offset:14336\n\tv_mfma_f32_16x16x32_bf16 %3, %10, %8, %3"
      : "=&a"(wq[9][2]), "+v"(av[5]), "=&v"(xf[6][7]), "+v"(aq[4]) : "v"(vQ), "s"(rsW), "s"(sq[5]), "a"(wq[5][2]), "v"(xf[5][5]), "v"(xr0), "a"(wq[5][0]) : "memory"); SB();
  WAITL(8);
  SB();
  asm volatile("v_mfma_f32_16x16x32_bf16 %0, %10, %11, %0\n\tds_read_b128 %1, %12 offset:16384\n\tv_mfma_f32_16x16x32_bf16 %2, %13, %11, %2\n\ts_add_u32 m0, %14, 98304\n\ts_add_u32 %3, %15, 49792\n\tv_mfma_f32_16x16x32_bf16 %4, %16, %11, %4\n\tbuffer_load_dwordx4 %17, %18, %3 offen offset:0 sc1 lds\n\tv_mfma_f32_16x16x32_bf16 %5, %10, %19, %5\n\tbuffer_load_dwordx4 %6, %20, %18, %21 offen offset:384 sc1\n\tv_mfma_f32_16x16x32_bf16 %7, %13, %19, %7\n\tv_mfma_f32_16x16x32_bf16 %8, %16, %19, %8\n\tv_mfma_f32_16x16x32_bf16 %9, %10, %22, %9"
      : "+v"(ak[6]), "=&v"(xf[6][8]), "+v"(av[6]), "=&s"(sXt), "+v"(aq[5]), "+v"(ak[7]), "=&a"(wq[10][0]), "+v"(av[7]), "+v"(aq[6]), "+v"(ak[8]) : "a"(wq[5][1]), "v"(xf[5][6]), "v"(xr0), "a"(wq[5][2]), "s"(ldsX), "s"(sXa), "a"(wq[5][0]), "v"(vXN), "s"(rsW), "v"(xf[5][7]), "v"(vQ), "s"(sq[1]), "v"(xf[5][8]) : "memory", "scc"); SB();
  asm volatile("v_mfma_f32_16x16x32_bf16 %0, %2, %3, %0\n\tv_mfma_f32_16x16x32_bf16 %1, %4, %3, %1"
      : "+v"(av[8]), "+v"(aq[7]) : "a"(wq[5][2]), "v"(xf[5][8]), "a"(wq[5][0])); SB();
  STAMP(31);
  WAITVB(17);
  WAITL(3);
  SB();
  asm volatile("v_mfma_f32_16x16x32_bf16 %0, %12, %13, %0\n\tv_add_u32 %1, 55296, %1\n\tv_mfma_f32_16x16x32_bf16 %2, %14, %13, %2\n\tds_read_b128 %3, %1 offset:0\n\tv_mfma_f32_16x16x32_bf16 %4, %12, %15, %4\n\ts_add_u32 m0, %16, 102400\n\ts_add_u32 %5, %17, 82560\n\tv_mfma_f32_16x16x32_bf16 %6, %14, %15, %6\n\tds_read_b128 %7, %1 offset:2048\n\tv_mfma_f32_16x16x32_bf16 %8, %18, %15, %8\n\tbuffer_load_dwordx4 %19, %20, %5 offen offset:0 sc1 lds\n\tv_mfma_f32_16x16x32_bf16 %9, %12, %21, %9\n\tds_read_b128 %10, %1 offset:4096\n\tv_mfma_f32_16x16x32_bf16 %11, %14, %21, %11"
      : "+v"(ak[0]), "+v"(xr1), "+v"(av[0]), "=&v"(xf[7][0]), "+v"(ak[1]), "=&s"(sXt), "+v"(av[1]), "=&v"(xf[7][1]), "+v"(aq[0]), "+v"(ak[2]), "=&v"(xf[7][2]), "+v"(av[2]) : "a"(wq[6][1]), "v"(xf[6][0]), "a"(wq[6][2]), "v"(xf[6][1]), "s"(ldsX), "s"(sXa), "a"(wq[6][0]), "v"(vXN), "s"(rsW), "v"(xf[6][2]) : "memory", "scc"); SB();
  asm volatile("buffer_load_dwordx4 %0, %11, %12, %13 offen offset:384 sc1\n\tv_mfma_f32_16x16x32_bf16 %1, %14, %15, %1\n\tds_read_b128 %2, %16 offset:6144\n\tv_mfma_f32_16x16x32_bf16 %3, %17, %18, %3\n\ts_add_u32 m0, %19, 106496\n\ts_add_u32 %4, %20, 115328\n\tv_mfma_f32_16x16x32_bf16 %5, %21, %18, %5\n\tds_read_b128 %6, %16 offset:8192\n\tv_mfma_f32_16x16x32_bf16 %7, %14, %18, %7\n\tbuffer_load_dwordx4 %22, %12, %4 offen offset:0 sc1 lds\n\tv_mfma_f32_16x16x32_bf16 %8, %17, %23, %8\n\tds_read_b128 %9, %16 offset:10240\n\tv_mfma_f32_16x16x32_bf16 %10, %21, %23, %10"
      : "=&a"(wq[10][1]), "+v"(aq[1]), "=&v"(xf[7][3]), "+v"(ak[3]), "=&s"(sXt), "+v"(av[3]), "=&v"(xf[7][4]), "+v"(aq[2]), "+v"(ak[4]), "=&v"(xf[7][5]), "+v"(av[4]) : "v"(vQ), "s"(rsW), "s"(sq[3]), "a"(wq[6][0]), "v"(xf[6][2]), "v"(xr1), "a"(wq[6][1]), "v"(xf[6][3]), "s"(ldsX), "s"(sXa), "a"(wq[6][2]), "v"(vXN), "v"(xf[6][4]) : "memory", "scc"); SB();
  asm volatile("buffer_load_dwordx4 %0, %8, %9, %10 offen offset:384 sc1\n\tv_mfma_f32_16x16x32_bf16 %1, %11, %12, %1\n\tds_read_b128 %2, %13 offset:12288\n\tv_mfma_f32_16x16x32_bf16 %3, %14, %15, %3\n\tbuffer_load_dwordx4 %4, %8, %9, %16 offen offset:512 sc1\n\tv_mfma_f32_16x16x32_bf16 %5, %17, %15, %5\n\tds_read_b128 %6, %13 offset:14336\n\tv_mfma_f32_16x16x32_bf16 %7, %11, %15, %7"
      : "=&a"(wq[10][2]), "+v"(aq[3]), "=&v"(xf[7][6]), "+v"(ak[5]), "=&a"(wq[11][0]), "+v"(av[5]), "=&v"(xf[7][7]), "+v"(aq[4]) : "v"(vQ), "s"(rsW), "s"(sq[5]), "a"(wq[6][0]), "v"(xf[6][4]), "v"(xr1), "a"(wq[6][1]), "v"(xf[6][5]), "s"(sq[1]), "a"(wq[6][2]) : "memory"); SB();
  WAITL(8);
  SB();
  asm volatile("v_mfma_f32_16x16x32_bf16 %0, %11, %12, %0\n\tds_read_b128 %1, %13 offset:16384\n\tv_mfma_f32_16x16x32_bf16 %2, %14, %12, %2\n\tbuffer_load_dwordx4 %3, %15, %16, %17 offen offset:512 sc1\n\tv_mfma_f32_16x16x32_bf16 %4, %18, %12, %4\n\tv_mfma_f32_16x16x32_bf16 %5, %11, %19, %5\n\tv_mfma_f32_16x16x32_bf16 %6, %14, %19, %6\n\tv_mfma_f32_16x16x32_bf16 %7, %18, %19, %7\n\tv_mfma_f32_16x16x32_bf16 %8, %11, %20, %8\n\tv_mfma_f32_16x16x32_bf16 %9, %14, %20, %9\n\tv_mfma_f32_16x16x32_bf16 %10, %18, %20, %10"
      : "+v"(ak[6]), "=&v"(xf[7][8]), "+v"(av[6]), "=&a"(wq[11][1]), "+v"(aq[5]), "+v"(ak[7]), "+v"(av[7]), "+v"(aq[6]), "+v"(ak[8]), "+v"(av[8]), "+v"(aq[7]) : "a"(wq[6][1]), "v"(xf[6][6]), "v"(xr1), "a"(wq[6][2]), "v"(vQ), "s"(rsW), "s"(sq[3]), "a"(wq[6][0]), "v"(xf[6][7]), "v"(xf[6][8]) : "memory"); SB();
  WAITVB(18);
  WAITL(3);
  SB();
  asm volatile("v_mfma_f32_16x16x32_bf16 %0, %1, %2, %0\n\ts_waitcnt vmcnt(11)"
      : "+v"(ak[0]) : "a"(wq[7][1]), "v"(xf[7][0]) : "memory"); SB();
  BAR0();
  SB();
  asm volatile("v_mfma_f32_16x16x32_bf16 %0, %11, %12, %0\n\tds_read_b128 %1, %13 offset:18432\n\tv_mfma_f32_16x16x32_bf16 %2, %14, %15, %2\n\ts_add_u32 m0, %16, 110592\n\ts_add_u32 %3, %17, 768\n\tv_mfma_f32_16x16x32_bf16 %4, %11, %15, %4\n\tds_read_b128 %5, %13 offset:20480\n\tv_mfma_f32_16x16x32_bf16 %6, %18, %15, %6\n\tbuffer_load_dwordx4 %19, %20, %3 offen offset:0 sc1 lds\n\tv_mfma_f32_16x16x32_bf16 %7, %14, %21, %7\n\tds_read_b128 %8, %13 offset:22528\n\tv_mfma_f32_16x16x32_bf16 %9, %11, %21, %9\n\tbuffer_load_dwordx4 %10, %22, %20, %23 offen offset:512 sc1"
      : "+v"(av[0]), "=&v"(xf[8][0]), "+v"(ak[1]), "=&s"(sXt), "+v"(av[1]), "=&v"(xf[8][1]), "+v"(aq[0]), "+v"(ak[2]), "=&v"(xf[8][2]), "+v"(av[2]), "=&a"(wq[11][2]) : "a"(wq[7][2]), "v"(xf[7][0]), "v"(xr0), "a"(wq[7][1]), "v"(xf[7][1]), "s"(ldsX), "s"(sXa), "a"(wq[7][0]), "v"(vXN), "s"(rsW), "v"(xf[7][2]), "v"(vQ), "s"(sq[5]) : "memory", "scc"); SB();
  asm volatile("v_mfma_f32_16x16x32_bf16 %0, %11, %12, %0\n\tds_read_b128 %1, %13 offset:24576\n\tv_mfma_f32_16x16x32_bf16 %2, %14, %15, %2\n\ts_add_u32 m0, %16, 114688\n\ts_add_u32 %3, %17, 33536\n\tv_mfma_f32_16x16x32_bf16 %4, %18, %15, %4\n\tds_read_b128 %5, %13 offset:26624\n\tv_mfma_f32_16x16x32_bf16 %6, %11, %15, %6\n\tbuffer_load_dwordx4 %19, %20, %3 offen offset:0 sc1 lds\n\tv_mfma_f32_16x16x32_bf16 %7, %14, %21, %7\n\tds_read_b128 %8, %13 offset:28672\n\tv_mfma_f32_16x16x32_bf16 %9, %18, %21, %9\n\tbuffer_load_dwordx4 %10, %22, %20, %23 offen offset:640 sc1"
      : "+v"(aq[1]), "=&v"(xf[8][3]), "+v"(ak[3]), "=&s"(sXt), "+v"(av[3]), "=&v"(xf[8][4]), "+v"(aq[2]), "+v"(ak[4]), "=&v"(xf[8][5]), "+v"(av[4]), "=&a"(wq[12][0]) : "a"(wq[7][0]), "v"(xf[7][2]), "v"(xr0), "a"(wq[7][1]), "v"(xf[7][3]), "s"(ldsX), "s"(sXa), "a"(wq[7][2]), "v"(vXN), "s"(rsW), "v"(xf[7][4]), "v"(vQ), "s"(sq[1]) : "memory", "scc"); SB();
  asm volatile("v_mfma_f32_16x16x32_bf16 %0, %7, %8, %0\n\tds_read_b128 %1, %9 offset:30720\n\tv_mfma_f32_16x16x32_bf16 %2, %10, %11, %2\n\ts_add_u32 m0, %12, 118784\n\ts_add_u32 %3, %13, 66304\n\tv_mfma_f32_16x16x32_bf16 %4, %14, %11, %4\n\tds_read_b128 %5, %9 offset:32768\n\tv_mfma_f32_16x16x32_bf16 %6, %7, %11, %6"
      : "+v"(aq[3]), "=&v"(xf[8][6]), "+v"(ak[5]), "=&s"(sXt), "+v"(av[5]), "=&v"(xf[8][7]), "+v"(aq[4]) : "a"(wq[7][0]), "v"(xf[7][4]), "v"(xr0), "a"(wq[7][1]), "v"(xf[7][5]), "s"(ldsX), "s"(sXa), "a"(wq[7][2]) : "memory", "scc"); SB();
  WAITL(8);
  SB();
  asm volatile("v_mfma_f32_16x16x32_bf16 %0, %10, %11, %0\n\tds_read_b128 %1, %12 offset:34816\n\tv_mfma_f32_16x16x32_bf16 %2, %13, %11, %2\n\tbuffer_load_dwordx4 %14, %15, %16 offen offset:0 sc1 lds\n\tv_mfma_f32_16x16x32_bf16 %3, %17, %11, %3\n\tbuffer_load_dwordx4 %4, %18, %15, %19 offen offset:640 sc1\n\tv_mfma_f32_16x16x32_bf16 %5, %10, %20, %5\n\tbuffer_load_dwordx4 %6, %18, %15, %21 offen offset:640 sc1\n\tv_mfma_f32_16x16x32_bf16 %7, %13, %20, %7\n\tbuffer_load_dword %8, %22, %15, 0 offen sc1\n\tv_mfma_f32_16x16x32_bf16 %9, %17, %20, %9"
      : "+v"(ak[6]), "=&v"(xf[8][8]), "+v"(av[6]), "+v"(aq[5]), "=&a"(wq[12][1]), "+v"(ak[7]), "=&a"(wq[12][2]), "+v"(av[7]), "=&v"(fq), "+v"(aq[6]) : "a"(wq[7][1]), "v"(xf[7][6]), "v"(xr0), "a"(wq[7][2]), "v"(vXN), "s"(rsW), "s"(sXt), "a"(wq[7][0]), "v"(vQ), "s"(sq[3]), "v"(xf[7][7]), "s"(sq[5]), "v"(vFQ) : "memory"); SB();
  asm volatile("v_mfma_f32_16x16x32_bf16 %0, %3, %4, %0\n\tv_mfma_f32_16x16x32_bf16 %1, %5, %4, %1\n\tv_mfma_f32_16x16x32_bf16 %2, %6, %4, %2"
      : "+v"(ak[8]), "+v"(av[8]), "+v"(aq[7]) : "a"(wq[7][1]), "v"(xf[7][8]), "a"(wq[7][2]), "a"(wq[7][0])); SB();
  WAITVB(21);
  WAITL(3);
  SB();
  asm volatile("v_mfma_f32_16x16x32_bf16 %0, %11, %12, %0\n\ts_add_u32 m0, %13, 122880\n\ts_add_u32 %1, %14, 99072\n\tv_mfma_f32_16x16x32_bf16 %2, %15, %12, %2\n\tds_read_b128 %3, %16 offset:18432\n\tv_mfma_f32_16x16x32_bf16 %4, %11, %17, %4\n\tbuffer_load_dwordx4 %18, %19, %1 offen offset:0 sc1 lds\n\tv_mfma_f32_16x16x32_bf16 %5, %15, %17, %5\n\tds_read_b128 %6, %16 offset:20480\n\tv_mfma_f32_16x16x32_bf16 %7, %20, %17, %7\n\tbuffer_load_dwordx4 %8, %21, %19, %22 offen offset:768 sc1\n\tv_mfma_f32_16x16x32_bf16 %9, %11, %23, %9\n\tds_read_b128 %10, %16 offset:22528"
      : "+v"(ak[0]), "=&s"(sXt), "+v"(av[0]), "=&v"(xf[9][0]), "+v"(ak[1]), "+v"(av[1]), "=&v"(xf[9][1]), "+v"(aq[0]), "=&a"(wq[13][0]), "+v"(ak[2]), "=&v"(xf[9][2]) : "a"(wq[8][1]), "v"(xf[8][0]), "s"(ldsX), "s"(sXa), "a"(wq[8][2]), "v"(xr1), "v"(xf[8][1]), "v"(vXN), "s"(rsW), "a"(wq[8][0]), "v"(vQ), "s"(sq[1]), "v"(xf[8][2]) : "memory", "scc"); SB();
  asm volatile("v_mfma_f32_16x16x32_bf16 %0, %11, %12, %0\n\ts_add_u32 m0, %13, 126976\n\ts_add_u32 %1, %14, 131840\n\tv_mfma_f32_16x16x32_bf16 %2, %15, %12, %2\n\tds_read_b128 %3, %16 offset:24576\n\tv_mfma_f32_16x16x32_bf16 %4, %17, %18, %4\n\tbuffer_load_dwordx4 %19, %20, %1 offen offset:0 sc1 lds\n\tv_mfma_f32_16x16x32_bf16 %5, %11, %18, %5\n\tds_read_b128 %6, %16 offset:26624\n\tv_mfma_f32_16x16x32_bf16 %7, %15, %18, %7\n\tbuffer_load_dwordx4 %8, %21, %20, %22 offen offset:768 sc1\n\tv_mfma_f32_16x16x32_bf16 %9, %17, %23, %9\n\tds_read_b128 %10, %16 offset:28672"
      : "+v"(av[2]), "=&s"(sXt), "+v"(aq[1]), "=&v"(xf[9][3]), "+v"(ak[3]), "+v"(av[3]), "=&v"(xf[9][4]), "+v"(aq[2]), "=&a"(wq[13][1]), "+v"(ak[4]), "=&v"(xf[9][5]) : "a"(wq[8][2]), "v"(xf[8][2]), "s"(ldsX), "s"(sXc), "a"(wq[8][0]), "v"(xr1), "a"(wq[8][1]), "v"(xf[8][3]), "v"(vXN), "s"(rsW), "v"(vQ), "s"(sq[3]), "v"(xf[8][4]) : "memory", "scc"); SB();
  asm volatile("v_mfma_f32_16x16x32_bf16 %0, %8, %9, %0\n\tbuffer_load_dwordx4 %1, %10, %11, %12 offen offset:768 sc1\n\tv_mfma_f32_16x16x32_bf16 %2, %13, %9, %2\n\tds_read_b128 %3, %14 offset:30720\n\tv_mfma_f32_16x16x32_bf16 %4, %15, %16, %4\n\tv_mfma_f32_16x16x32_bf16 %5, %8, %16, %5\n\tds_read_b128 %6, %14 offset:32768\n\tv_mfma_f32_16x16x32_bf16 %7, %13, %16, %7"
      : "+v"(av[4]), "=&a"(wq[13][2]), "+v"(aq[3]), "=&v"(xf[9][6]), "+v"(ak[5]), "+v"(av[5]), "=&v"(xf[9][7]), "+v"(aq[4]) : "a"(wq[8][2]), "v"(xf[8][4]), "v"(vQ), "s"(rsW), "s"(sq[5]), "a"(wq[8][0]), "v"(xr1), "a"(wq[8][1]), "v"(xf[8][5]) : "memory"); SB();
  WAITL(8);
  SB();
  asm volatile("v_mfma_f32_16x16x32_bf16 %0, %10, %11, %0\n\tds_read_b128 %1, %12 offset:34816\n\tv_mfma_f32_16x16x32_bf16 %2, %13, %11, %2\n\tv_mfma_f32_16x16x32_bf16 %3, %14, %11, %3\n\tv_mfma_f32_16x16x32_bf16 %4, %10, %15, %4\n\tv_mfma_f32_16x16x32_bf16 %5, %13, %15, %5\n\tv_mfma_f32_16x16x32_bf16 %6, %14, %15, %6\n\tv_mfma_f32_16x16x32_bf16 %7, %10, %16, %7\n\tv_mfma_f32_16x16x32_bf16 %8, %13, %16, %8\n\tv_mfma_f32_16x16x32_bf16 %9, %14, %16, %9"
      : "+v"(ak[6]), "=&v"(xf[9][8]), "+v"(av[6]), "+v"(aq[5]), "+v"(ak[7]), "+v"(av[7]), "+v"(aq[6]), "+v"(ak[8]), "+v"(av[8]), "+v"(aq[7]) : "a"(wq[8][1]), "v"(xf[8][6]), "v"(xr1), "a"(wq[8][2]), "a"(wq[8][0]), "v"(xf[8][7]), "v"(xf[8][8]) : "memory"); SB();
  WAITVB(21);
  WAITL(3);
  SB();
  asm volatile("v_mfma_f32_16x16x32_bf16 %0, %3, %4, %0\n\ts_add_u32 m0, %5, 18432\n\tv_add_u32 %1, 36864, %1\n\tv_mfma_f32_16x16x32_bf16 %2, %6, %4, %2\n\ts_waitcnt vmcnt(16)"
      : "+v"(ak[0]), "+v"(xr0), "+v"(av[0]) : "a"(wq[9][1]), "v"(xf[9][0]), "s"(ldsS), "a"(wq[9][2]) : "memory", "scc"); SB();
  BAR0();
  SB();
  asm volatile("v_mfma_f32_16x16x32_bf16 %0, %11, %12, %0\n\tds_read_b128 %1, %13 offset:0\n\tv_mfma_f32_16x16x32_bf16 %2, %14, %12, %2\n\tds_read_b128 %3, %13 offset:2048\n\tv_mfma_f32_16x16x32_bf16 %4, %15, %12, %4\n\tbuffer_load_dwordx4 %16, %17, %18 offen offset:0  lds\n\tv_mfma_f32_16x16x32_bf16 %5, %11, %19, %5\n\tds_read_b128 %6, %13 offset:4096\n\tv_mfma_f32_16x16x32_bf16 %7, %14, %19, %7\n\tbuffer_load_dwordx4 %20, %17, %18 offen offset:1024  lds\n\tv_mfma_f32_16x16x32_bf16 %8, %15, %19, %8\n\tds_read_b128 %9, %13 offset:6144\n\tv_mfma_f32_16x16x32_bf16 %10, %11, %21, %10\n\tbuffer_load_dwordx4 %22, %17, %18 offen offset:2048  lds"
      : "+v"(ak[1]), "=&v"(xf[10][0]), "+v"(av[1]), "=&v"(xf[10][1]), "+v"(aq[0]), "+v"(ak[2]), "=&v"(xf[10][2]), "+v"(av[2]), "+v"(aq[1]), "=&v"(xf[10][3]), "+v"(ak[3]) : "a"(wq[9][1]), "v"(xf[9][1]), "v"(xr0), "a"(wq[9][2]), "a"(wq[9][0]), "v"(vW2[0]), "s"(rs2), "s"(sw[0]), "v"(xf[9][2]), "v"(vW2[1]), "v"(xf[9][3]), "v"(vW2[2]) : "memory"); SB();
  asm volatile("v_mfma_f32_16x16x32_bf16 %0, %9, %10, %0\n\tds_read_b128 %1, %11 offset:8192\n\tv_mfma_f32_16x16x32_bf16 %2, %12, %10, %2\n\tbuffer_load_dwordx4 %13, %14, %15 offen offset:3072  lds\n\tv_mfma_f32_16x16x32_bf16 %3, %16, %17, %3\n\tds_read_b128 %4, %11 offset:10240\n\ts_add_u32 m0, %18, 34816\n\tv_mfma_f32_16x16x32_bf16 %5, %9, %17, %5\n\tbuffer_load_dwordx4 %19, %14, %20 offen offset:0  lds\n\tv_mfma_f32_16x16x32_bf16 %6, %12, %17, %6\n\tds_read_b128 %7, %11 offset:12288\n\tv_mfma_f32_16x16x32_bf16 %8, %16, %21, %8\n\tbuffer_load_dwordx4 %22, %14, %20 offen offset:1024  lds"
      : "+v"(av[3]), "=&v"(xf[10][4]), "+v"(aq[2]), "+v"(ak[4]), "=&v"(xf[10][5]), "+v"(av[4]), "+v"(aq[3]), "=&v"(xf[10][6]), "+v"(ak[5]) : "a"(wq[9][2]), "v"(xf[9][3]), "v"(xr0), "a"(wq[9][0]), "v"(vW2[3]), "s"(rs2), "s"(sw[0]), "a"(wq[9][1]), "v"(xf[9][4]), "s"(ldsS), "v"(vW2[0]), "s"(sw[1]), "v"(xf[9][5]), "v"(vW2[1]) : "memory", "scc"); SB();
  asm volatile("v_mfma_f32_16x16x32_bf16 %0, %3, %4, %0\n\tds_read_b128 %1, %5 offset:14336\n\tv_mfma_f32_16x16x32_bf16 %2, %6, %4, %2"
      : "+v"(av[5]), "=&v"(xf[10][7]), "+v"(aq[4]) : "a"(wq[9][2]), "v"(xf[9][5]), "v"(xr0), "a"(wq[9][0]) : "memory"); SB();
  WAITL(8);
  SB();
  asm volatile("v_mfma_f32_16x16x32_bf16 %0, %10, %11, %0\n\tds_read_b128 %1, %12 offset:16384\n\tv_mfma_f32_16x16x32_bf16 %2, %13, %11, %2\n\tbuffer_load_dwordx4 %14, %15, %16 offen offset:2048  lds\n\tv_mfma_f32_16x16x32_bf16 %3, %17, %11, %3\n\tbuffer_load_dwordx4 %18, %15, %16 offen offset:3072  lds\n\tv_mfma_f32_16x16x32_bf16 %4, %10, %19, %4\n\tv_mfma_f32_16x16x32_bf16 %5, %13, %19, %5\n\tv_mfma_f32_16x16x32_bf16 %6, %17, %19, %6\n\tv_mfma_f32_16x16x32_bf16 %7, %10, %20, %7\n\tv_mfma_f32_16x16x32_bf16 %8, %13, %20, %8\n\tv_mfma_f32_16x16x32_bf16 %9, %17, %20, %9"
      : "+v"(ak[6]), "=&v"(xf[10][8]), "+v"(av[6]), "+v"(aq[5]), "+v"(ak[7]), "+v"(av[7]), "+v"(aq[6]), "+v"(ak[8]), "+v"(av[8]), "+v"(aq[7]) : "a"(wq[9][1]), "v"(xf[9][6]), "v"(xr0), "a"(wq[9][2]), "v"(vW2[2]), "s"(rs2), "s"(sw[1]), "a"(wq[9][0]), "v"(vW2[3]), "v"(xf[9][7]), "v"(xf[9][8]) : "memory"); SB();
  STAMP(32);
  WAITVB(23);
  WAITL(3);
  SB();
  asm volatile("v_mfma_f32_16x16x32_bf16 %0, %13, %14, %0\n\tv_add_u32 %1, 36864, %1\n\tv_mfma_f32_16x16x32_bf16 %2, %15, %14, %2\n\tds_read_b128 %3, %1 offset:0\n\tv_mfma_f32_16x16x32_bf16 %4, %13, %16, %4\n\tv_mfma_f32_16x16x32_bf16 %5, %15, %16, %5\n\tds_read_b128 %6, %1 offset:2048\n\tv_mfma_f32_16x16x32_bf16 %7, %17, %16, %7\n\tv_mfma_f32_16x16x32_bf16 %8, %13, %18, %8\n\tds_read_b128 %9, %1 offset:4096\n\tv_mfma_f32_16x16x32_bf16 %10, %15, %18, %10\n\tv_mfma_f32_16x16x32_bf16 %11, %17, %18, %11\n\tds_read_b128 %12, %1 offset:6144"
      : "+v"(ak[0]), "+v"(xr1), "+v"(av[0]), "=&v"(xf[11][0]), "+v"(ak[1]), "+v"(av[1]), "=&v"(xf[11][1]), "+v"(aq[0]), "+v"(ak[2]), "=&v"(xf[11][2]), "+v"(av[2]), "+v"(aq[1]), "=&v"(xf[11][3]) : "a"(wq[10][1]), "v"(xf[10][0]), "a"(wq[10][2]), "v"(xf[10][1]), "a"(wq[10][0]), "v"(xf[10][2]) : "memory"); SB();
  asm volatile("v_mfma_f32_16x16x32_bf16 %0, %13, %14, %0\n\tv_mfma_f32_16x16x32_bf16 %1, %15, %14, %1\n\tds_read_b128 %2, %16 offset:8192\n\tv_mfma_f32_16x16x32_bf16 %3, %17, %14, %3\n\tv_mfma_f32_16x16x32_bf16 %4, %13, %18, %4\n\tds_read_b128 %5, %16 offset:10240\n\tv_mfma_f32_16x16x32_bf16 %6, %15, %18, %6\n\tv_mfma_f32_16x16x32_bf16 %7, %17, %18, %7\n\tds_read_b128 %8, %16 offset:12288\n\tv_mfma_f32_16x16x32_bf16 %9, %13, %19, %9\n\tv_mfma_f32_16x16x32_bf16 %10, %15, %19, %10\n\tds_read_b128 %11, %16 offset:14336\n\tv_mfma_f32_16x16x32_bf16 %12, %17, %19, %12"
      : "+v"(ak[3]), "+v"(av[3]), "=&v"(xf[11][4]), "+v"(aq[2]), "+v"(ak[4]), "=&v"(xf[11][5]), "+v"(av[4]), "+v"(aq[3]), "=&v"(xf[11][6]), "+v"(ak[5]), "+v"(av[5]), "=&v"(xf[11][7]), "+v"(aq[4]) : "a"(wq[10][1]), "v"(xf[10][3]), "a"(wq[10][2]), "v"(xr1), "a"(wq[10][0]), "v"(xf[10][4]), "v"(xf[10][5]) : "memory"); SB();
  WAITL(8);
  SB();
  asm volatile("v_mfma_f32_16x16x32_bf16 %0, %3, %4, %0\n\tds_read_b128 %1, %5 offset:16384\n\tv_mfma_f32_16x16x32_bf16 %2, %6, %4, %2\n\ts_waitcnt vmcnt(10)"
      : "+v"(ak[6]), "=&v"(xf[11][8]), "+v"(av[6]) : "a"(wq[10][1]), "v"(xf[10][6]), "v"(xr1), "a"(wq[10][2]) : "memory"); SB();
  BAR0();
  SB();
  asm volatile("v_mfma_f32_16x16x32_bf16 %0, %9, %10, %0\n\tds_read_b128 %1, %11 offset:18432\n\tds_read_b128 %2, %12 offset:18432\n\tv_mfma_f32_16x16x32_bf16 %3, %13, %14, %3\n\tv_mfma_f32_16x16x32_bf16 %4, %15, %14, %4\n\tv_mfma_f32_16x16x32_bf16 %5, %9, %14, %5\n\tv_mfma_f32_16x16x32_bf16 %6, %13, %16, %6\n\tv_mfma_f32_16x16x32_bf16 %7, %15, %16, %7\n\tv_mfma_f32_16x16x32_bf16 %8, %9, %16, %8"
      : "+v"(aq[5]), "=&v"(xf[12][0]), "=&v"(xf[13][0]), "+v"(ak[7]), "+v"(av[7]), "+v"(aq[6]), "+v"(ak[8]), "+v"(av[8]), "+v"(aq[7]) : "a"(wq[10][0]), "v"(xf[10][6]), "v"(xr0), "v"(xr1), "a"(wq[10][1]), "v"(xf[10][7]), "a"(wq[10][2]), "v"(xf[10][8]) : "memory"); SB();
  WAITV1(13, fq);
  if (__builtin_expect(__ballot(fq != nonce) != 0ull, 0)) poll1<4>(rsW, vFQ, nonce);
  STAMP(13);
  u32x4 wor[14];
  WAITVB(19);
  WAITL(0);
  SB();
  asm volatile("v_mfma_f32_16x16x32_bf16 %0, %1, %2, %0"
      : "+v"(ak[0]) : "a"(wq[11][1]), "v"(xf[11][0])); SB();
  TOUCH(vT2, rs2, 0u, 0);
  SB();
  asm volatile("v_mfma_f32_16x16x32_bf16 %0, %2, %3, %0\n\tds_read_b128 %1, %4 offset:20480"
      : "+v"(av[0]), "=&v"(xf[12][1]) : "a"(wq[11][2]), "v"(xf[11][0]), "v"(xr0) : "memory"); SB();
  WAITVB(15);
  SB();
  asm volatile("v_mfma_f32_16x16x32_bf16 %0, %3, %4, %0\n\tds_read_b128 %1, %5 offset:20480\n\tv_mfma_f32_16x16x32_bf16 %2, %6, %4, %2"
      : "+v"(ak[0]), "=&v"(xf[13][1]), "+v"(av[0]) : "a"(wq[12][1]), "v"(xf[12][0]), "v"(xr1), "a"(wq[12][2]) : "memory"); SB();
  WAITVB(9);
  SB();
  asm volatile("v_mfma_f32_16x16x32_bf16 %0, %2, %3, %0\n\tv_mfma_f32_16x16x32_bf16 %1, %4, %3, %1"
      : "+v"(ak[0]), "+v"(av[0]) : "a"(wq[13][1]), "v"(xf[13][0]), "a"(wq[13][2])); SB();
  WAITL(0);
  SB();
  asm volatile("v_mfma_f32_16x16x32_bf16 %0, %9, %10, %0\n\tds_read_b128 %1, %11 offset:22528\n\tds_read_b128 %2, %12 offset:22528\n\tv_mfma_f32_16x16x32_bf16 %3, %13, %10, %3\n\tv_mfma_f32_16x16x32_bf16 %4, %14, %10, %4\n\tv_mfma_f32_16x16x32_bf16 %0, %15, %16, %0\n\tv_mfma_f32_16x16x32_bf16 %3, %17, %16, %3\n\tv_cvt_pk_bf16_f32 %5, %18, %19\n\tv_cvt_pk_bf16_f32 %6, %20, %21\n\tv_mfma_f32_16x16x32_bf16 %4, %22, %16, %4\n\tv_cvt_pk_bf16_f32 %7, %23, %24\n\tv_cvt_pk_bf16_f32 %8, %25, %26"
      : "+v"(ak[1]), "=&v"(xf[12][2]), "=&v"(xf[13][2]), "+v"(av[1]), "+v"(aq[0]), "=&v"(sg[0][0]), "=&v"(sg[0][1]), "=&v"(sg[1][0]), "=&v"(sg[1][1]) : "a"(wq[11][1]), "v"(xf[11][1]), "v"(xr0), "v"(xr1), "a"(wq[11][2]), "a"(wq[11][0]), "a"(wq[12][1]), "v"(xf[12][1]), "a"(wq[12][2]), "v"(ak[0][0]), "v"(ak[0][1]), "v"(ak[0][2]), "v"(ak[0][3]), "a"(wq[12][0]), "v"(av[0][0]), "v"(av[0][1]), "v"(av[0][2]), "v"(av[0][3]) : "memory"); SB();
  asm volatile("v_mfma_f32_16x16x32_bf16 %0, %3, %4, %0\n\tv_mfma_f32_16x16x32_bf16 %1, %5, %4, %1\n\tv_mfma_f32_16x16x32_bf16 %2, %6, %4, %2"
      : "+v"(ak[1]), "+v"(av[1]), "+v"(aq[0]) : "a"(wq[13][1]), "v"(xf[13][1]), "a"(wq[13][2]), "a"(wq[13][0])); SB();
  WAITL(0);
  SB();
  asm volatile("v_mfma_f32_16x16x32_bf16 %0, %7, %8, %0\n\tds_read_b128 %1, %9 offset:24576\n\tds_read_b128 %2, %10 offset:24576\n\tv_mfma_f32_16x16x32_bf16 %3, %11, %8, %3\n\tds_write_b64 %12, %13 offset:18432\n\tv_mfma_f32_16x16x32_bf16 %4, %14, %8, %4\n\tv_mfma_f32_16x16x32_bf16 %0, %15, %16, %0\n\tv_mfma_f32_16x16x32_bf16 %3, %17, %16, %3\n\tds_write_b64 %18, %19 offset:0\n\tv_mfma_f32_16x16x32_bf16 %4, %20, %16, %4\n\tv_cvt_pk_bf16_f32 %5, %21, %22\n\tv_cvt_pk_bf16_f32 %6, %23, %24\n\tv_mfma_f32_16x16x32_bf16 %0, %25, %26, %0"
      : "+v"(ak[2]), "=&v"(xf[12][3]), "=&v"(xf[13][3]), "+v"(av[2]), "+v"(aq[1]), "=&v"(sg[3][0]), "=&v"(sg[3][1]) : "a"(wq[11][1]), "v"(xf[11][2]), "v"(xr0), "v"(xr1), "a"(wq[11][2]), "v"(sk0), "v"(sg[0]), "a"(wq[11][0]), "a"(wq[12][1]), "v"(xf[12][2]), "a"(wq[12][2]), "v"(sv), "v"(sg[1]), "a"(wq[12][0]), "v"(ak[1][0]), "v"(ak[1][1]), "v"(ak[1][2]), "v"(ak[1][3]), "a"(wq[13][1]), "v"(xf[13][2]) : "memory"); SB();
  asm volatile("v_cvt_pk_bf16_f32 %0, %7, %8\n\tv_cvt_pk_bf16_f32 %1, %9, %10\n\tv_mfma_f32_16x16x32_bf16 %2, %11, %12, %2\n\tbuffer_load_dwordx4 %3, %13, %14, %15 offen offset:0 sc1\n\tv_cvt_pk_bf16_f32 %4, %16, %17\n\tv_cvt_pk_bf16_f32 %5, %18, %19\n\tv_mfma_f32_16x16x32_bf16 %6, %20, %12, %6"
      : "=&v"(sg[4][0]), "=&v"(sg[4][1]), "+v"(av[2]), "=&a"(wor[0]), "=&v"(sg[5][0]), "=&v"(sg[5][1]), "+v"(aq[1]) : "v"(av[1][0]), "v"(av[1][1]), "v"(av[1][2]), "v"(av[1][3]), "a"(wq[13][2]), "v"(xf[13][2]), "v"(vWo), "s"(rsW), "s"(sWq[0]), "v"(aq[0][0]), "v"(aq[0][1]), "v"(aq[0][2]), "v"(aq[0][3]), "a"(wq[13][0]) : "memory"); SB();
  WAITL(2);
  SB();
  asm volatile("v_mfma_f32_16x16x32_bf16 %0, %7, %8, %0\n\tds_read_b128 %1, %9 offset:26624\n\tds_read_b128 %2, %10 offset:26624\n\tv_mfma_f32_16x16x32_bf16 %3, %11, %8, %3\n\tds_write_b64 %12, %13 offset:18944\n\tv_mfma_f32_16x16x32_bf16 %4, %14, %8, %4\n\tbuffer_load_dwordx4 %5, %15, %16, %17 offen offset:1024 sc1\n\tv_mfma_f32_16x16x32_bf16 %0, %18, %19, %0\n\tv_mfma_f32_16x16x32_bf16 %3, %20, %19, %3\n\tds_write_b64 %21, %22 offset:256\n\tv_mfma_f32_16x16x32_bf16 %4, %23, %19, %4\n\tv_cvt_pk_bf16_f32 %6, %24, %25"
      : "+v"(ak[3]), "=&v"(xf[12][4]), "=&v"(xf[13][4]), "+v"(av[3]), "+v"(aq[2]), "=&a"(wor[1]), "=&v"(sg[6][0]) : "a"(wq[11][1]), "v"(xf[11][3]), "v"(xr0), "v"(xr1), "a"(wq[11][2]), "v"(sk1), "v"(sg[3]), "a"(wq[11][0]), "v"(vWo), "s"(rsW), "s"(sWq[0]), "a"(wq[12][1]), "v"(xf[12][3]), "a"(wq[12][2]), "v"(sv), "v"(sg[4]), "a"(wq[12][0]), "v"(ak[2][0]), "v"(ak[2][1]) : "memory"); SB();
  asm volatile("v_cvt_pk_bf16_f32 %0, %7, %8\n\tv_mfma_f32_16x16x32_bf16 %1, %9, %10, %1\n\tbuffer_load_dwordx4 %2, %11, %12, %13 offen offset:2048 sc1\n\tv_cvt_pk_bf16_f32 %3, %14, %15\n\tv_cvt_pk_bf16_f32 %4, %16, %17\n\tv_mfma_f32_16x16x32_bf16 %5, %18, %10, %5\n\tds_write_b64 %19, %20 offset:36864\n\tv_mfma_f32_16x16x32_bf16 %6, %21, %10, %6"
      : "=&v"(sg[6][1]), "+v"(ak[3]), "=&a"(wor[2]), "=&v"(sg[7][0]), "=&v"(sg[7][1]), "+v"(av[3]), "+v"(aq[2]) : "v"(ak[2][2]), "v"(ak[2][3]), "a"(wq[13][1]), "v"(xf[13][3]), "v"(vWo), "s"(rsW), "s"(sWq[0]), "v"(av[2][0]), "v"(av[2][1]), "v"(av[2][2]), "v"(av[2][3]), "a"(wq[13][2]), "v"(sk0), "v"(sg[5]), "a"(wq[13][0]) : "memory"); SB();
  WAITL(3);
  SB();
  asm volatile("v_mfma_f32_16x16x32_bf16 %0, %8, %9, %0\n\tds_read_b128 %1, %10 offset:28672\n\tds_read_b128 %2, %11 offset:28672\n\tv_mfma_f32_16x16x32_bf16 %3, %12, %9, %3\n\tbuffer_load_dwordx4 %4, %13, %14, %15 offen offset:3072 sc1\n\tds_write_b64 %16, %17 offset:19456\n\tv_cvt_pk_bf16_f32 %5, %18, %19\n\tv_cvt_pk_bf16_f32 %6, %20, %21\n\tv_mfma_f32_16x16x32_bf16 %7, %22, %9, %7\n\tv_mfma_f32_16x16x32_bf16 %0, %23, %24, %0\n\tv_mfma_f32_16x16x32_bf16 %3, %25, %24, %3"
      : "+v"(ak[4]), "=&v"(xf[12][5]), "=&v"(xf[13][5]), "+v"(av[4]), "=&a"(wor[3]), "=&v"(sg[8][0]), "=&v"(sg[8][1]), "+v"(aq[3]) : "a"(wq[11][1]), "v"(xf[11][4]), "v"(xr0), "v"(xr1), "a"(wq[11][2]), "v"(vWo), "s"(rsW), "s"(sWq[0]), "v"(sk0), "v"(sg[6]), "v"(aq[1][0]), "v"(aq[1][1]), "v"(aq[1][2]), "v"(aq[1][3]), "a"(wq[11][0]), "a"(wq[12][1]), "v"(xf[12][4]), "a"(wq[12][2]) : "memory"); SB();
  asm volatile("ds_write_b64 %8, %9 offset:512\n\tv_mfma_f32_16x16x32_bf16 %0, %10, %11, %0\n\tbuffer_load_dwordx4 %1, %12, %13, %14 offen offset:0 sc1\n\tv_cvt_pk_bf16_f32 %2, %15, %16\n\tv_cvt_pk_bf16_f32 %3, %17, %18\n\tv_mfma_f32_16x16x32_bf16 %4, %19, %20, %4\n\tv_cvt_pk_bf16_f32 %5, %21, %22\n\tv_cvt_pk_bf16_f32 %6, %23, %24\n\tv_mfma_f32_16x16x32_bf16 %7, %25, %20, %7"
      : "+v"(aq[3]), "=&a"(wor[4]), "=&v"(sg[9][0]), "=&v"(sg[9][1]), "+v"(ak[4]), "=&v"(sg[10][0]), "=&v"(sg[10][1]), "+v"(av[4]) : "v"(sv), "v"(sg[7]), "a"(wq[12][0]), "v"(xf[12][4]), "v"(vWo), "s"(rsW), "s"(sWq[1]), "v"(ak[3][0]), "v"(ak[3][1]), "v"(ak[3][2]), "v"(ak[3][3]), "a"(wq[13][1]), "v"(xf[13][4]), "v"(av[3][0]), "v"(av[3][1]), "v"(av[3][2]), "v"(av[3][3]), "a"(wq[13][2]) : "memory"); SB();
  asm volatile("ds_write_b64 %1, %2 offset:37376\n\tv_mfma_f32_16x16x32_bf16 %0, %3, %4, %0"
      : "+v"(aq[3]) : "v"(sk1), "v"(sg[8]), "a"(wq[13][0]), "v"(xf[13][4]) : "memory"); SB();
  WAITL(3);
  SB();
  asm volatile("v_mfma_f32_16x16x32_bf16 %0, %9, %10, %0\n\tbuffer_load_dwordx4 %1, %11, %12, %13 offen offset:1024 sc1\n\tds_read_b128 %2, %14 offset:30720\n\tds_read_b128 %3, %15 offset:30720\n\tv_mfma_f32_16x16x32_bf16 %4, %16, %10, %4\n\tds_write_b64 %17, %18 offset:19968\n\tv_cvt_pk_bf16_f32 %5, %19, %20\n\tv_cvt_pk_bf16_f32 %6, %21, %22\n\tv_mfma_f32_16x16x32_bf16 %7, %23, %10, %7\n\tv_mfma_f32_16x16x32_bf16 %0, %24, %25, %0\n\tv_mfma_f32_16x16x32_bf16 %4, %26, %25, %4\n\tbuffer_load_dwordx4 %8, %11, %12, %13 offen offset:2048 sc1"
      : "+v"(ak[5]), "=&a"(wor[5]), "=&v"(xf[12][6]), "=&v"(xf[13][6]), "+v"(av[5]), "=&v"(sg[11][0]), "=&v"(sg[11][1]), "+v"(aq[4]), "=&a"(wor[6]) : "a"(wq[11][1]), "v"(xf[11][5]), "v"(vWo), "s"(rsW), "s"(sWq[1]), "v"(xr0), "v"(xr1), "a"(wq[11][2]), "v"(sk1), "v"(sg[9]), "v"(aq[2][0]), "v"(aq[2][1]), "v"(aq[2][2]), "v"(aq[2][3]), "a"(wq[11][0]), "a"(wq[12][1]), "v"(xf[12][5]), "a"(wq[12][2]) : "memory"); SB();
  asm volatile("ds_write_b64 %7, %8 offset:768\n\tv_mfma_f32_16x16x32_bf16 %0, %9, %10, %0\n\tv_cvt_pk_bf16_f32 %1, %11, %12\n\tv_cvt_pk_bf16_f32 %2, %13, %14\n\tv_mfma_f32_16x16x32_bf16 %3, %15, %16, %3\n\tv_cvt_pk_bf16_f32 %4, %17, %18\n\tv_cvt_pk_bf16_f32 %5, %19, %20\n\tv_mfma_f32_16x16x32_bf16 %6, %21, %16, %6\n\tds_write_b64 %22, %23 offset:37888\n\tv_mfma_f32_16x16x32_bf16 %0, %24, %16, %0"
      : "+v"(aq[4]), "=&v"(sg[12][0]), "=&v"(sg[12][1]), "+v"(ak[5]), "=&v"(sg[13][0]), "=&v"(sg[13][1]), "+v"(av[5]) : "v"(sv), "v"(sg[10]), "a"(wq[12][0]), "v"(xf[12][5]), "v"(ak[4][0]), "v"(ak[4][1]), "v"(ak[4][2]), "v"(ak[4][3]), "a"(wq[13][1]), "v"(xf[13][5]), "v"(av[4][0]), "v"(av[4][1]), "v"(av[4][2]), "v"(av[4][3]), "a"(wq[13][2]), "v"(sk0), "v"(sg[11]), "a"(wq[13][0]) : "memory"); SB();
  asm volatile("buffer_load_dwordx4 %0, %1, %2, %3 offen offset:3072 sc1"
      : "=&a"(wor[7]) : "v"(vWo), "s"(rsW), "s"(sWq[1]) : "memory"); SB();
  WAITL(3);
  SB();
  asm volatile("v_mfma_f32_16x16x32_bf16 %0, %8, %9, %0\n\tds_read_b128 %1, %10 offset:32768\n\tds_read_b128 %2, %11 offset:32768\n\tv_mfma_f32_16x16x32_bf16 %3, %12, %9, %3\n\tds_write_b64 %13, %14 offset:20480\n\tv_cvt_pk_bf16_f32 %4, %15, %16\n\tv_cvt_pk_bf16_f32 %5, %17, %18\n\tv_mfma_f32_16x16x32_bf16 %6, %19, %9, %6\n\tv_mfma_f32_16x16x32_bf16 %0, %20, %21, %0\n\tbuffer_load_dwordx4 %7, %22, %23, %24 offen offset:0 sc1\n\tv_mfma_f32_16x16x32_bf16 %3, %25, %21, %3"
      : "+v"(ak[6]), "=&v"(xf[12][7]), "=&v"(xf[13][7]), "+v"(av[6]), "=&v"(sg[14][0]), "=&v"(sg[14][1]), "+v"(aq[5]), "=&a"(wor[8]) : "a"(wq[11][1]), "v"(xf[11][6]), "v"(xr0), "v"(xr1), "a"(wq[11][2]), "v"(sk0), "v"(sg[12]), "v"(aq[3][0]), "v"(aq[3][1]), "v"(aq[3][2]), "v"(aq[3][3]), "a"(wq[11][0]), "a"(wq[12][1]), "v"(xf[12][6]), "v"(vWo), "s"(rsW), "s"(sWq[2]), "a"(wq[12][2]) : "memory"); SB();
  asm volatile("ds_write_b64 %8, %9 offset:1024\n\tv_mfma_f32_16x16x32_bf16 %0, %10, %11, %0\n\tv_cvt_pk_bf16_f32 %1, %12, %13\n\tv_cvt_pk_bf16_f32 %2, %14, %15\n\tv_mfma_f32_16x16x32_bf16 %3, %16, %17, %3\n\tv_cvt_pk_bf16_f32 %4, %18, %19\n\tv_cvt_pk_bf16_f32 %5, %20, %21\n\tv_mfma_f32_16x16x32_bf16 %6, %22, %17, %6\n\tbuffer_load_dwordx4 %7, %23, %24, %25 offen offset:1024 sc1"
      : "+v"(aq[5]), "=&v"(sg[15][0]), "=&v"(sg[15][1]), "+v"(ak[6]), "=&v"(sg[16][0]), "=&v"(sg[16][1]), "+v"(av[6]), "=&a"(wor[9]) : "v"(sv), "v"(sg[13]), "a"(wq[12][0]), "v"(xf[12][6]), "v"(ak[5][0]), "v"(ak[5][1]), "v"(ak[5][2]), "v"(ak[5][3]), "a"(wq[13][1]), "v"(xf[13][6]), "v"(av[5][0]), "v"(av[5][1]), "v"(av[5][2]), "v"(av[5][3]), "a"(wq[13][2]), "v"(vWo), "s"(rsW), "s"(sWq[2]) : "memory"); SB();
  asm volatile("ds_write_b64 %1, %2 offset:38400\n\tv_mfma_f32_16x16x32_bf16 %0, %3, %4, %0"
      : "+v"(aq[5]) : "v"(sk1), "v"(sg[14]), "a"(wq[13][0]), "v"(xf[13][6]) : "memory"); SB();
  WAITL(3);
  SB();
  asm volatile("v_mfma_f32_16x16x32_bf16 %0, %8, %9, %0\n\tds_read_b128 %1, %10 offset:34816\n\tds_read_b128 %2, %11 offset:34816\n\tv_mfma_f32_16x16x32_bf16 %3, %12, %9, %3\n\tds_write_b64 %13, %14 offset:20992\n\tv_cvt_pk_bf16_f32 %4, %15, %16\n\tv_cvt_pk_bf16_f32 %5, %17, %18\n\tv_mfma_f32_16x16x32_bf16 %6, %19, %9, %6\n\tbuffer_load_dwordx4 %7, %20, %21, %22 offen offset:2048 sc1\n\tv_mfma_f32_16x16x32_bf16 %0, %23, %24, %0\n\tv_mfma_f32_16x16x32_bf16 %3, %25, %24, %3"
      : "+v"(ak[7]), "=&v"(xf[12][8]), "=&v"(xf[13][8]), "+v"(av[7]), "=&v"(sg[17][0]), "=&v"(sg[17][1]), "+v"(aq[6]), "=&a"(wor[10]) : "a"(wq[11][1]), "v"(xf[11][7]), "v"(xr0), "v"(xr1), "a"(wq[11][2]), "v"(sk1), "v"(sg[15]), "v"(aq[4][0]), "v"(aq[4][1]), "v"(aq[4][2]), "v"(aq[4][3]), "a"(wq[11][0]), "v"(vWo), "s"(rsW), "s"(sWq[2]), "a"(wq[12][1]), "v"(xf[12][7]), "a"(wq[12][2]) : "memory"); SB();
  asm volatile("ds_write_b64 %8, %9 offset:1280\n\tv_mfma_f32_16x16x32_bf16 %0, %10, %11, %0\n\tv_cvt_pk_bf16_f32 %1, %12, %13\n\tv_cvt_pk_bf16_f32 %2, %14, %15\n\tv_mfma_f32_16x16x32_bf16 %3, %16, %17, %3\n\tbuffer_load_dwordx4 %4, %18, %19, %20 offen offset:3072 sc1\n\tv_cvt_pk_bf16_f32 %5, %21, %22\n\tv_cvt_pk_bf16_f32 %6, %23, %24\n\tv_mfma_f32_16x16x32_bf16 %7, %25, %17, %7"
      : "+v"(aq[6]), "=&v"(sg[18][0]), "=&v"(sg[18][1]), "+v"(ak[7]), "=&a"(wor[11]), "=&v"(sg[19][0]), "=&v"(sg[19][1]), "+v"(av[7]) : "v"(sv), "v"(sg[16]), "a"(wq[12][0]), "v"(xf[12][7]), "v"(ak[6][0]), "v"(ak[6][1]), "v"(ak[6][2]), "v"(ak[6][3]), "a"(wq[13][1]), "v"(xf[13][7]), "v"(vWo), "s"(rsW), "s"(sWq[2]), "v"(av[6][0]), "v"(av[6][1]), "v"(av[6][2]), "v"(av[6][3]), "a"(wq[13][2]) : "memory"); SB();
  asm volatile("ds_write_b64 %1, %2 offset:38912\n\tv_mfma_f32_16x16x32_bf16 %0, %3, %4, %0"
      : "+v"(aq[6]) : "v"(sk0), "v"(sg[17]), "a"(wq[13][0]), "v"(xf[13][7]) : "memory"); SB();
  WAITL(3);
  SB();
  asm volatile("v_mfma_f32_16x16x32_bf16 %0, %7, %8, %0\n\tv_cvt_pk_bf16_f32 %1, %9, %10\n\tv_cvt_pk_bf16_f32 %2, %11, %12\n\tv_mfma_f32_16x16x32_bf16 %3, %13, %8, %3\n\tbuffer_load_dwordx4 %4, %14, %15, %16 offen offset:0 sc1\n\tds_write_b64 %17, %18 offset:21504\n\tv_mfma_f32_16x16x32_bf16 %5, %19, %8, %5\n\tv_mfma_f32_16x16x32_bf16 %0, %20, %21, %0\n\tv_mfma_f32_16x16x32_bf16 %3, %22, %21, %3\n\tds_write_b64 %23, %24 offset:1536\n\tv_mfma_f32_16x16x32_bf16 %5, %25, %21, %5\n\tbuffer_load_dwordx4 %6, %14, %15, %16 offen offset:1024 sc1"
      : "+v"(ak[8]), "=&v"(sg[20][0]), "=&v"(sg[20][1]), "+v"(av[8]), "=&a"(wor[12]), "+v"(aq[7]), "=&a"(wor[13]) : "a"(wq[11][1]), "v"(xf[11][8]), "v"(aq[5][0]), "v"(aq[5][1]), "v"(aq[5][2]), "v"(aq[5][3]), "a"(wq[11][2]), "v"(vWo), "s"(rsW), "s"(sWq[3]), "v"(sk0), "v"(sg[18]), "a"(wq[11][0]), "a"(wq[12][1]), "v"(xf[12][8]), "a"(wq[12][2]), "v"(sv), "v"(sg[19]), "a"(wq[12][0]) : "memory"); SB();
  asm volatile("v_cvt_pk_bf16_f32 %0, %9, %10\n\tv_cvt_pk_bf16_f32 %1, %11, %12\n\tv_mfma_f32_16x16x32_bf16 %2, %13, %14, %2\n\tv_cvt_pk_bf16_f32 %3, %15, %16\n\tv_cvt_pk_bf16_f32 %4, %17, %18\n\tv_mfma_f32_16x16x32_bf16 %5, %19, %14, %5\n\tds_write_b64 %20, %21 offset:39424\n\tv_mfma_f32_16x16x32_bf16 %6, %22, %14, %6\n\tv_cvt_pk_bf16_f32 %7, %23, %24\n\tv_cvt_pk_bf16_f32 %8, %25, %26\n\ts_nop 5"
      : "=&v"(sg[21][0]), "=&v"(sg[21][1]), "+v"(ak[8]), "=&v"(sg[22][0]), "=&v"(sg[22][1]), "+v"(av[8]), "+v"(aq[7]), "=&v"(sg[23][0]), "=&v"(sg[23][1]) : "v"(ak[7][0]), "v"(ak[7][1]), "v"(ak[7][2]), "v"(ak[7][3]), "a"(wq[13][1]), "v"(xf[13][8]), "v"(av[7][0]), "v"(av[7][1]), "v"(av[7][2]), "v"(av[7][3]), "a"(wq[13][2]), "v"(sk1), "v"(sg[20]), "a"(wq[13][0]), "v"(aq[6][0]), "v"(aq[6][1]), "v"(aq[6][2]), "v"(aq[6][3]) : "memory"); SB();
  asm volatile("ds_write_b64 %6, %7 offset:22016\n\tds_write_b64 %8, %9 offset:1792\n\tds_write_b64 %10, %11 offset:39936\n\tv_cvt_pk_bf16_f32 %0, %12, %13\n\tv_cvt_pk_bf16_f32 %1, %14, %15\n\tv_cvt_pk_bf16_f32 %2, %16, %17\n\tv_cvt_pk_bf16_f32 %3, %18, %19\n\tv_cvt_pk_bf16_f32 %4, %20, %21\n\tv_cvt_pk_bf16_f32 %5, %22, %23"
      : "=&v"(sg[24][0]), "=&v"(sg[24][1]), "=&v"(sg[25][0]), "=&v"(sg[25][1]), "=&v"(sg[26][0]), "=&v"(sg[26][1]) : "v"(sk1), "v"(sg[21]), "v"(sv), "v"(sg[22]), "v"(sk0), "v"(sg[23]), "v"(ak[8][0]), "v"(ak[8][1]), "v"(ak[8][2]), "v"(ak[8][3]), "v"(av[8][0]), "v"(av[8][1]), "v"(av[8][2]), "v"(av[8][3]), "v"(aq[7][0]), "v"(aq[7][1]), "v"(aq[7][2]), "v"(aq[7][3]) : "memory"); SB();
  asm volatile("ds_write_b64 %0, %1 offset:22528\n\tds_write_b64 %2, %3 offset:2048\n\tds_write_b64 %4, %5 offset:40448"
      :  : "v"(sk0), "v"(sg[24]), "v"(sv), "v"(sg[25]), "v"(sk1), "v"(sg[26]) : "memory"); SB();
  BAR();
  STAMP(14);
  unsigned fu; u32x4 qf[2][2]; bf16x8 kf[2][2][2], vf[2][2][2];
  qf[0][0] = *(LDS_AS const u32x4*)(rk0 + SQ + 512 + 0); kf[0][0][0] = *(LDS_AS const bf16x8*)(rk0 + SK + 0); kf[0][0][1] = *(LDS_AS const bf16x8*)(rk0 + SK + 1024);
  qf[0][1] = *(LDS_AS const u32x4*)(rk0 + SQ + 512 + 9216); kf[0][1][0] = *(LDS_AS const bf16x8*)(rk0 + SK + 9216); kf[0][1][1] = *(LDS_AS const bf16x8*)(rk0 + SK + 10240);
  qf[1][0] = *(LDS_AS const u32x4*)(rk1 + SQ + 512 + 0); kf[1][0][0] = *(LDS_AS const bf16x8*)(rk1 + SK + 0); kf[1][0][1] = *(LDS_AS const bf16x8*)(rk1 + SK + 1024);
  qf[1][1] = *(LDS_AS const u32x4*)(rk1 + SQ + 512 + 9216); kf[1][1][0] = *(LDS_AS const bf16x8*)(rk1 + SK + 9216); kf[1][1][1] = *(LDS_AS const bf16x8*)(rk1 + SK + 10240);
  SB();
  asm volatile("s_add_u32 m0, %0, 0\n\ts_nop 0\n\tbuffer_load_dwordx4 %1, %2, %3 offen offset:0  lds\n\tbuffer_load_dwordx4 %4, %2, %3 offen offset:1024  lds\n\tbuffer_load_dwordx4 %5, %2, %3 offen offset:2048  lds\n\tbuffer_load_dwordx4 %6, %2, %3 offen offset:3072  lds"
      :  : "s"(ldsO), "v"(vW2[0]), "s"(rs2), "s"(sw[2]), "v"(vW2[1]), "v"(vW2[2]), "v"(vW2[3]) : "memory", "scc"); SB();
  f32x4 sc[2][2][2];
  asm volatile("v_mfma_f32_16x16x32_bf16 %0, %2, %3, 0\n\tv_mfma_f32_16x16x32_bf16 %1, %4, %3, 0"
      : "=&v"(sc[0][0][0]), "=&v"(sc[0][0][1]) : "v"(kf[0][0][0]), "v"(qf[0][0]), "v"(kf[0][0][1])); SB();
  vf[0][0][0] = tr8u(rv0 + 0, 512);
  vf[0][0][1] = tr8u(rv0 + 4608, 512);
  SB();
  asm volatile("v_mfma_f32_16x16x32_bf16 %0, %2, %3, 0\n\tv_mfma_f32_16x16x32_bf16 %1, %4, %3, 0"
      : "=&v"(sc[0][1][0]), "=&v"(sc[0][1][1]) : "v"(kf[0][1][0]), "v"(qf[0][1]), "v"(kf[0][1][1])); SB();
  vf[0][1][0] = tr8u(rv0 + 9216, 512);
  vf[0][1][1] = tr8u(rv0 + 13824, 512);
  SB();
  asm volatile("v_mfma_f32_16x16x32_bf16 %0, %2, %3, 0\n\tv_mfma_f32_16x16x32_bf16 %1, %4, %3, 0"
      : "=&v"(sc[1][0][0]), "=&v"(sc[1][0][1]) : "v"(kf[1][0][0]), "v"(qf[1][0]), "v"(kf[1][0][1])); SB();
  vf[1][0][0] = tr8u(rv1 + 0, 512);
  vf[1][0][1] = tr8u(rv1 + 4608, 512);
  SB();
  asm volatile("v_mfma_f32_16x16x32_bf16 %0, %2, %3, 0\n\tv_mfma_f32_16x16x32_bf16 %1, %4, %3, 0"
      : "=&v"(sc[1][1][0]), "=&v"(sc[1][1][1]) : "v"(kf[1][1][0]), "v"(qf[1][1]), "v"(kf[1][1][1])); SB();
  vf[1][1][0] = tr8u(rv1 + 9216, 512);
  vf[1][1][1] = tr8u(rv1 + 13824, 512);
  SB();
  asm volatile("s_add_u32 m0, %0, 4096\n\ts_nop 0\n\tbuffer_load_dwordx4 %1, %2, %3 offen offset:0  lds\n\tbuffer_load_dwordx4 %4, %2, %3 offen offset:1024  lds\n\tbuffer_load_dwordx4 %5, %2, %3 offen offset:2048  lds\n\tbuffer_load_dwordx4 %6, %2, %3 offen offset:3072  lds"
      :  : "s"(ldsO), "v"(vW2[0]), "s"(rs2), "s"(sw[3]), "v"(vW2[1]), "v"(vW2[2]), "v"(vW2[3]) : "memory", "scc"); SB();
  f32x2_t lg[2][2][2], mx[2], rs[2]; u32x4 pf[2][2]; f32x4 oo[2][2][2];
  const f32x2_t scale = {0.18898223650461362f, 0.18898223650461362f};
#define SEL(p, h, i) __uint_as_float((m1[i] & __float_as_uint(sc[p][h][1][i])) | (~m1[i] & __float_as_uint(sc[p][h][0][i])))
  lg[0][0][0] = rbf2(__builtin_elementwise_fma(rbf2((f32x2_t){SEL(0, 0, 0), SEL(0, 0, 1)}), scale, (f32x2_t){cm[0], cm[1]}));
  lg[0][0][1] = rbf2(__builtin_elementwise_fma(rbf2((f32x2_t){SEL(0, 0, 2), SEL(0, 0, 3)}), scale, (f32x2_t){cm[2], cm[3]}));
  OPV(lg[0][0][0]); OPV(lg[0][0][1]);
  SB();
  asm volatile("s_add_u32 m0, %0, 0\n\ts_nop 0\n\tbuffer_load_dwordx4 %1, %2, %3 offen offset:0  lds\n\tbuffer_load_dwordx4 %4, %2, %3 offen offset:1024  lds\n\tbuffer_load_dwordx4 %5, %2, %3 offen offset:2048  lds\n\tbuffer_load_dwordx4 %6, %2, %3 offen offset:3072  lds"
      :  : "s"(ldsS), "v"(vW2[0]), "s"(rs2), "s"(sw[4]), "v"(vW2[1]), "v"(vW2[2]), "v"(vW2[3]) : "memory", "scc"); SB();
  lg[0][1][0] = rbf2(__builtin_elementwise_fma(rbf2((f32x2_t){SEL(0, 1, 0), SEL(0, 1, 1)}), scale, (f32x2_t){cm[0], cm[1]}));
  lg[0][1][1] = rbf2(__builtin_elementwise_fma(rbf2((f32x2_t){SEL(0, 1, 2), SEL(0, 1, 3)}), scale, (f32x2_t){cm[2], cm[3]}));
  OPV(lg[0][1][0]); OPV(lg[0][1][1]);
  SB();
  asm volatile("s_waitcnt vmcnt(31)"
      :  :  : "memory"); SB();
  fw2[0][0] = tr8u(tS[0] + 18432, 512);
  fw2[0][1] = tr8u(tS[1] + 18432, 512);
  fw2[0][2] = tr8u(tS[2] + 18432, 512);
  fw2[0][3] = tr8u(tS[3] + 18432, 512);
  SB();
  lg[1][0][0] = rbf2(__builtin_elementwise_fma(rbf2((f32x2_t){SEL(1, 0, 0), SEL(1, 0, 1)}), scale, (f32x2_t){cm[0], cm[1]}));
  lg[1][0][1] = rbf2(__builtin_elementwise_fma(rbf2((f32x2_t){SEL(1, 0, 2), SEL(1, 0, 3)}), scale, (f32x2_t){cm[2], cm[3]}));
  OPV(lg[1][0][0]); OPV(lg[1][0][1]);
  SB();
  WAITL(0);
  asm volatile("s_add_u32 m0, %0, 18432\n\ts_nop 0\n\tbuffer_load_dwordx4 %1, %2, %3 offen offset:0  lds\n\tbuffer_load_dwordx4 %4, %2, %3 offen offset:1024  lds\n\tbuffer_load_dwordx4 %5, %2, %3 offen offset:2048  lds\n\tbuffer_load_dwordx4 %6, %2, %3 offen offset:3072  lds"
      :  : "s"(ldsS), "v"(vW2[0]), "s"(rs2), "s"(sw[5]), "v"(vW2[1]), "v"(vW2[2]), "v"(vW2[3]) : "memory", "scc"); SB();
  lg[1][1][0] = rbf2(__builtin_elementwise_fma(rbf2((f32x2_t){SEL(1, 1, 0), SEL(1, 1, 1)}), scale, (f32x2_t){cm[0], cm[1]}));
  lg[1][1][1] = rbf2(__builtin_elementwise_fma(rbf2((f32x2_t){SEL(1, 1, 2), SEL(1, 1, 3)}), scale, (f32x2_t){cm[2], cm[3]}));
  OPV(lg[1][1][0]); OPV(lg[1][1][1]);
  SB();
  asm volatile("s_waitcnt vmcnt(31)"
      :  :  : "memory"); SB();
  fw2[1][0] = tr8u(tS[0] + 34816, 512);
  fw2[1][1] = tr8u(tS[1] + 34816, 512);
  fw2[1][2] = tr8u(tS[2] + 34816, 512);
  fw2[1][3] = tr8u(tS[3] + 34816, 512);
  SB();
  #pragma unroll
  for (int p = 0; p < 2; ++p)
    mx[p] = xbcast2(xmax2(fmx(fmx(lg[p][0][0][0], lg[p][0][0][1]), fmx(lg[p][0][1][0], lg[p][0][1][1])), fmx(fmx(lg[p][1][0][0], lg[p][1][0][1]), fmx(lg[p][1][1][0], lg[p][1][1][1]))));
  OPV(mx[0]); OPV(mx[1]);
  SB();
  WAITL(0);
  asm volatile("s_add_u32 m0, %0, 34816\n\ts_nop 0\n\tbuffer_load_dwordx4 %1, %2, %3 offen offset:0  lds\n\tbuffer_load_dwordx4 %4, %2, %3 offen offset:1024  lds\n\tbuffer_load_dwordx4 %5, %2, %3 offen offset:2048  lds\n\tbuffer_load_dwordx4 %6, %2, %3 offen offset:3072  lds\n\ts_waitcnt vmcnt(16)"
      :  : "s"(ldsS), "v"(vW2[0]), "s"(rs2), "s"(sw[6]), "v"(vW2[1]), "v"(vW2[2]), "v"(vW2[3]) : "memory", "scc"); SB();
  fw2[2][0] = tr8u(tO[0] + 0, 512);
  fw2[2][1] = tr8u(tO[1] + 0, 512);
  fw2[2][2] = tr8u(tO[2] + 0, 512);
  fw2[2][3] = tr8u(tO[3] + 0, 512);
  SB();
  #pragma unroll
  for (int j = 0; j < 2; ++j) {
    const f32x2_t z = (lg[0][0][j] - (f32x2_t){mx[0][0], mx[0][0]}) * (f32x2_t){1.442695041f, 1.442695041f};
    lg[0][0][j] = (f32x2_t){__builtin_amdgcn_exp2f(z[0]), __builtin_amdgcn_exp2f(z[1])}; OPV(lg[0][0][j]);
  }
  SB();
  WAITL(0);
  asm volatile("s_add_u32 m0, %0, 0\n\ts_nop 0\n\tbuffer_load_dwordx4 %1, %2, %3 offen offset:0  lds\n\tbuffer_load_dwordx4 %4, %2, %3 offen offset:1024  lds\n\tbuffer_load_dwordx4 %5, %2, %3 offen offset:2048  lds\n\tbuffer_load_dwordx4 %6, %2, %3 offen offset:3072  lds"
      :  : "s"(ldsO), "v"(vW2[0]), "s"(rs2), "s"(sw[7]), "v"(vW2[1]), "v"(vW2[2]), "v"(vW2[3]) : "memory", "scc"); SB();
  #pragma unroll
  for (int j = 0; j < 2; ++j) {
    const f32x2_t z = (lg[0][1][j] - (f32x2_t){mx[0][1], mx[0][1]}) * (f32x2_t){1.442695041f, 1.442695041f};
    lg[0][1][j] = (f32x2_t){__builtin_amdgcn_exp2f(z[0]), __builtin_amdgcn_exp2f(z[1])}; OPV(lg[0][1][j]);
  }
  SB();
  asm volatile("s_waitcnt vmcnt(16)"
      :  :  : "memory"); SB();
  fw2[3][0] = tr8u(tO[0] + 4096, 512);
  fw2[3][1] = tr8u(tO[1] + 4096, 512);
  fw2[3][2] = tr8u(tO[2] + 4096, 512);
  fw2[3][3] = tr8u(tO[3] + 4096, 512);
  SB();
  #pragma unroll
  for (int j = 0; j < 2; ++j) {
    const f32x2_t z = (lg[1][0][j] - (f32x2_t){mx[1][0], mx[1][0]}) * (f32x2_t){1.442695041f, 1.442695041f};
    lg[1][0][j] = (f32x2_t){__builtin_amdgcn_exp2f(z[0]), __builtin_amdgcn_exp2f(z[1])}; OPV(lg[1][0][j]);
  }
  SB();
  WAITL(0);
  asm volatile("s_add_u32 m0, %0, 4096\n\ts_nop 0\n\tbuffer_load_dwordx4 %1, %2, %3 offen offset:0  lds\n\tbuffer_load_dwordx4 %4, %2, %3 offen offset:1024  lds\n\tbuffer_load_dwordx4 %5, %2, %3 offen offset:2048  lds\n\tbuffer_load_dwordx4 %6, %2, %3 offen offset:3072  lds"
      :  : "s"(ldsO), "v"(vW2[0]), "s"(rs2), "s"(sw[8]), "v"(vW2[1]), "v"(vW2[2]), "v"(vW2[3]) : "memory", "scc"); SB();
  #pragma unroll
  for (int j = 0; j < 2; ++j) {
    const f32x2_t z = (lg[1][1][j] - (f32x2_t){mx[1][1], mx[1][1]}) * (f32x2_t){1.442695041f, 1.442695041f};
    lg[1][1][j] = (f32x2_t){__builtin_amdgcn_exp2f(z[0]), __builtin_amdgcn_exp2f(z[1])}; OPV(lg[1][1][j]);
  }
  SB();
  asm volatile("s_waitcnt vmcnt(16)"
      :  :  : "memory"); SB();
  fw2[4][0] = tr8u(tS[0] + 0, 512);
  fw2[4][1] = tr8u(tS[1] + 0, 512);
  fw2[4][2] = tr8u(tS[2] + 0, 512);
  fw2[4][3] = tr8u(tS[3] + 0, 512);
  SB();
  #pragma unroll
  for (int p = 0; p < 2; ++p) {
    const f32x2_t e0 = lg[p][0][0] + lg[p][0][1], e1 = lg[p][1][0] + lg[p][1][1];
    rs[p] = xbcast2(__builtin_amdgcn_rcpf(xsum2(e0[0] + e0[1], e1[0] + e1[1])));
  }
  OPV(rs[0]); OPV(rs[1]);
  SB();
  WAITL(0);
  asm volatile("s_add_u32 m0, %0, 0\n\ts_nop 0\n\tbuffer_load_dwordx4 %1, %2, %3 offen offset:0  lds\n\tbuffer_load_dwordx4 %4, %2, %3 offen offset:1024  lds\n\tbuffer_load_dwordx4 %5, %2, %3 offen offset:2048  lds\n\tbuffer_load_dwordx4 %6, %2, %3 offen offset:3072  lds\n\ts_waitcnt vmcnt(16)"
      :  : "s"(ldsS), "v"(vW2[0]), "s"(rs2), "s"(sw[9]), "v"(vW2[1]), "v"(vW2[2]), "v"(vW2[3]) : "memory", "scc"); SB();
  fw2[5][0] = tr8u(tS[0] + 18432, 512);
  fw2[5][1] = tr8u(tS[1] + 18432, 512);
  fw2[5][2] = tr8u(tS[2] + 18432, 512);
  fw2[5][3] = tr8u(tS[3] + 18432, 512);
  SB();
  { const f32x2_t pa = lg[0][0][0] * (f32x2_t){rs[0][0], rs[0][0]}, pb = lg[0][0][1] * (f32x2_t){rs[0][0], rs[0][0]};
    const unsigned wa = pack2(pa[0], pa[1]), wb = pack2(pb[0], pb[1]);
    pf[0][0] = (u32x4){wa & ~mp[0], wb & ~mp[1], wa & mp[0], wb & mp[1]}; OPV(pf[0][0]); }
  { const f32x2_t pa = lg[0][1][0] * (f32x2_t){rs[0][1], rs[0][1]}, pb = lg[0][1][1] * (f32x2_t){rs[0][1], rs[0][1]};
    const unsigned wa = pack2(pa[0], pa[1]), wb = pack2(pb[0], pb[1]);
    pf[0][1] = (u32x4){wa & ~mp[0], wb & ~mp[1], wa & mp[0], wb & mp[1]}; OPV(pf[0][1]); }
  SB();
  WAITL(0);
  asm volatile("s_add_u32 m0, %4, 18432\n\ts_nop 0\n\tbuffer_load_dwordx4 %5, %6, %7 offen offset:0  lds\n\tbuffer_load_dwordx4 %8, %6, %7 offen offset:1024  lds\n\tbuffer_load_dwordx4 %9, %6, %7 offen offset:2048  lds\n\tbuffer_load_dwordx4 %10, %6, %7 offen offset:3072  lds\n\ts_nop 1\n\tv_mfma_f32_16x16x32_bf16 %0, %11, %12, 0\n\tv_mfma_f32_16x16x32_bf16 %1, %13, %14, 0\n\tv_mfma_f32_16x16x32_bf16 %2, %15, %12, 0\n\tv_mfma_f32_16x16x32_bf16 %3, %16, %14, 0"
      : "=&v"(oo[0][0][0]), "=&v"(oo[0][1][0]), "=&v"(oo[0][0][1]), "=&v"(oo[0][1][1]) : "s"(ldsS), "v"(vW2[0]), "s"(rs2), "s"(sw[10]), "v"(vW2[1]), "v"(vW2[2]), "v"(vW2[3]), "v"(vf[0][0][0]), "v"(pf[0][0]), "v"(vf[0][1][0]), "v"(pf[0][1]), "v"(vf[0][0][1]), "v"(vf[0][1][1]) : "memory", "scc"); SB();
  { const f32x2_t pa = lg[1][0][0] * (f32x2_t){rs[1][0], rs[1][0]}, pb = lg[1][0][1] * (f32x2_t){rs[1][0], rs[1][0]};
    const unsigned wa = pack2(pa[0], pa[1]), wb = pack2(pb[0], pb[1]);
    pf[1][0] = (u32x4){wa & ~mp[0], wb & ~mp[1], wa & mp[0], wb & mp[1]}; OPV(pf[1][0]); }
  { const f32x2_t pa = lg[1][1][0] * (f32x2_t){rs[1][1], rs[1][1]}, pb = lg[1][1][1] * (f32x2_t){rs[1][1], rs[1][1]};
    const unsigned wa = pack2(pa[0], pa[1]), wb = pack2(pb[0], pb[1]);
    pf[1][1] = (u32x4){wa & ~mp[0], wb & ~mp[1], wa & mp[0], wb & mp[1]}; OPV(pf[1][1]); }
  SB();
  asm volatile("s_waitcnt vmcnt(16)"
      :  :  : "memory"); SB();
  fw2[6][0] = tr8u(tS[0] + 34816, 512);
  fw2[6][1] = tr8u(tS[1] + 34816, 512);
  fw2[6][2] = tr8u(tS[2] + 34816, 512);
  fw2[6][3] = tr8u(tS[3] + 34816, 512);
  SB();
  asm volatile("s_nop 1\n\tv_mfma_f32_16x16x32_bf16 %0, %4, %5, 0\n\tv_mfma_f32_16x16x32_bf16 %1, %6, %7, 0\n\tv_mfma_f32_16x16x32_bf16 %2, %8, %5, 0\n\tv_mfma_f32_16x16x32_bf16 %3, %9, %7, 0"
      : "=&v"(oo[1][0][0]), "=&v"(oo[1][1][0]), "=&v"(oo[1][0][1]), "=&v"(oo[1][1][1]) : "v"(vf[1][0][0]), "v"(pf[1][0]), "v"(vf[1][1][0]), "v"(pf[1][1]), "v"(vf[1][0][1]), "v"(vf[1][1][1])); SB();
  HS16(vS, sO, 0, ((u32x4){pack2(oo[0][0][0][0], oo[0][0][0][1]), pack2(oo[0][0][0][2], oo[0][0][0][3]), pack2(oo[0][1][0][0], oo[0][1][0][1]), pack2(oo[0][1][0][2], oo[0][1][0][3])})); SB();
  HS16H(0xffff, vS, sO, 128, ((u32x4){pack2(oo[0][0][1][0], oo[0][0][1][1]), pack2(oo[0][0][1][2], oo[0][0][1][3]), pack2(oo[0][1][1][0], oo[0][1][1][1]), pack2(oo[0][1][1][2], oo[0][1][1][3])})); SB();
  HS16(vS, sO, 1024, ((u32x4){pack2(oo[1][0][0][0], oo[1][0][0][1]), pack2(oo[1][0][0][2], oo[1][0][0][3]), pack2(oo[1][1][0][0], oo[1][1][0][1]), pack2(oo[1][1][0][2], oo[1][1][0][3])})); SB();
  HS16H(0xffff, vS, sO, 1152, ((u32x4){pack2(oo[1][0][1][0], oo[1][0][1][1]), pack2(oo[1][0][1][2], oo[1][0][1][3]), pack2(oo[1][1][1][0], oo[1][1][1][1]), pack2(oo[1][1][1][2], oo[1][1][1][3])})); SB();
  STAMP(15);
  asm volatile("s_waitcnt vmcnt(16)"
      :  :  : "memory"); SB();
  fw2[7][0] = tr8u(tO[0] + 0, 512);
  fw2[7][1] = tr8u(tO[1] + 0, 512);
  fw2[7][2] = tr8u(tO[2] + 0, 512);
  fw2[7][3] = tr8u(tO[3] + 0, 512);
  WAITL(8);
  asm volatile("s_add_u32 m0, %0, 34816\n\ts_nop 0\n\tbuffer_load_dwordx4 %1, %2, %3 offen offset:0  lds\n\tbuffer_load_dwordx4 %4, %2, %3 offen offset:1024  lds\n\tbuffer_load_dwordx4 %5, %2, %3 offen offset:2048  lds\n\tbuffer_load_dwordx4 %6, %2, %3 offen offset:3072  lds\n\ts_waitcnt vmcnt(16)"
      :  : "s"(ldsS), "v"(vW2[0]), "s"(rs2), "s"(sw[11]), "v"(vW2[1]), "v"(vW2[2]), "v"(vW2[3]) : "memory", "scc"); SB();
  fw2[8][0] = tr8u(tO[0] + 4096, 512);
  fw2[8][1] = tr8u(tO[1] + 4096, 512);
  fw2[8][2] = tr8u(tO[2] + 4096, 512);
  fw2[8][3] = tr8u(tO[3] + 4096, 512);
  WAITL(8);
  asm volatile("s_add_u32 m0, %1, 0\n\ts_nop 0\n\tbuffer_load_dwordx4 %2, %3, %4 offen offset:0  lds\n\tbuffer_load_dwordx4 %5, %3, %4 offen offset:1024  lds\n\tbuffer_load_dwordx4 %6, %3, %4 offen offset:2048  lds\n\tbuffer_load_dwordx4 %7, %3, %4 offen offset:3072  lds\n\tbuffer_load_dword %0, %8, %9, 0 offen sc1\n\ts_waitcnt vmcnt(17)"
      : "=&v"(fu) : "s"(ldsO), "v"(vW2[0]), "s"(rs2), "s"(sw[12]), "v"(vW2[1]), "v"(vW2[2]), "v"(vW2[3]), "v"(vFU1), "s"(rsW) : "memory", "scc"); SB();
  fw2[9][0] = tr8u(tS[0] + 0, 512);
  fw2[9][1] = tr8u(tS[1] + 0, 512);
  fw2[9][2] = tr8u(tS[2] + 0, 512);
  fw2[9][3] = tr8u(tS[3] + 0, 512);
  WAITL(8);
  asm volatile("s_add_u32 m0, %0, 4096\n\ts_nop 0\n\tbuffer_load_dwordx4 %1, %2, %3 offen offset:0  lds\n\tbuffer_load_dwordx4 %4, %2, %3 offen offset:1024  lds\n\tbuffer_load_dwordx4 %5, %2, %3 offen offset:2048  lds\n\tbuffer_load_dwordx4 %6, %2, %3 offen offset:3072  lds"
      :  : "s"(ldsO), "v"(vW2[0]), "s"(rs2), "s"(sw[13]), "v"(vW2[1]), "v"(vW2[2]), "v"(vW2[3]) : "memory", "scc"); SB();
  STAMP(16);
  {
  bf16x8 pu[14], fwo[56];
  f32x4 ac[2][4], as[4], py[4][4];
  float ye[4], ze[4], ge[16], xe[16], bias;
  u32x2 ov[4]; unsigned fo;
  WAITV1(4, fu);
  STFO(sFO);
  if (__ballot(fu != nonce) != 0ull) poll1<4>(rsW, vFU1, nonce);
  STAMP(17);
  asm volatile("buffer_load_dwordx4 %0, %10, %11, %12 offen offset:0 sc1\n\tbuffer_load_dwordx4 %1, %10, %11, %13 offen offset:0 sc1\n\tbuffer_load_dwordx4 %2, %10, %11, %14 offen offset:0 sc1\n\tbuffer_load_dwordx4 %3, %10, %11, %15 offen offset:0 sc1\n\tbuffer_load_dwordx4 %4, %10, %11, %16 offen offset:0 sc1\n\tbuffer_load_dwordx4 %5, %10, %11, %17 offen offset:0 sc1\n\tbuffer_load_dwordx4 %6, %10, %11, %18 offen offset:0 sc1\n\tbuffer_load_dwordx4 %7, %10, %11, %19 offen offset:0 sc1\n\tbuffer_load_dwordx4 %8, %10, %11, %20 offen offset:0 sc1\n\tbuffer_load_dwordx4 %9, %10, %11, %21 offen offset:0 sc1\n\ts_waitcnt vmcnt(28)"
      : "=&v"(pu[0]), "=&v"(pu[1]), "=&v"(pu[2]), "=&v"(pu[3]), "=&v"(pu[4]), "=&v"(pu[5]), "=&v"(pu[6]), "=&v"(pu[7]), "=&v"(pu[8]), "=&v"(pu[9]) : "v"(vFj[0]), "s"(rsW), "s"(su[0]), "s"(su[1]), "s"(su[2]), "s"(su[3]), "s"(su[4]), "s"(su[5]), "s"(su[6]), "s"(su[7]), "s"(su[8]), "s"(su[9]) : "memory"); SB();
  fw2[10][0] = tr8u(tS[0] + 18432, 512);
  fw2[10][1] = tr8u(tS[1] + 18432, 512);
  fw2[10][2] = tr8u(tS[2] + 18432, 512);
  fw2[10][3] = tr8u(tS[3] + 18432, 512);
  asm volatile("s_waitcnt vmcnt(20)"
      :  :  : "memory"); SB();
  fw2[11][0] = tr8u(tS[0] + 34816, 512);
  fw2[11][1] = tr8u(tS[1] + 34816, 512);
  fw2[11][2] = tr8u(tS[2] + 34816, 512);
  fw2[11][3] = tr8u(tS[3] + 34816, 512);
  asm volatile("s_waitcnt vmcnt(16)"
      :  :  : "memory"); SB();
  fw2[12][0] = tr8u(tO[0] + 0, 512);
  fw2[12][1] = tr8u(tO[1] + 0, 512);
  fw2[12][2] = tr8u(tO[2] + 0, 512);
  fw2[12][3] = tr8u(tO[3] + 0, 512);
  asm volatile("s_waitcnt vmcnt(11)"
      :  :  : "memory"); SB();
  fw2[13][0] = tr8u(tO[0] + 4096, 512);
  fw2[13][1] = tr8u(tO[1] + 4096, 512);
  fw2[13][2] = tr8u(tO[2] + 4096, 512);
  fw2[13][3] = tr8u(tO[3] + 4096, 512);
  asm volatile("ds_write_b128 %0, %1 offset:8192\n\tds_write_b128 %0, %2 offset:9216\n\tds_write_b128 %0, %3 offset:10240\n\tds_write_b128 %0, %4 offset:11264\n\tds_write_b128 %0, %5 offset:12288\n\tds_write_b128 %0, %6 offset:13312\n\tds_write_b128 %7, %8 offset:0\n\tds_write_b128 %7, %9 offset:1024\n\tds_write_b128 %7, %10 offset:2048\n\tds_write_b128 %7, %11 offset:3072\n\tds_write_b128 %7, %12 offset:4096\n\tds_write_b128 %7, %13 offset:5120\n\tds_write_b128 %7, %14 offset:6144\n\tds_write_b128 %7, %15 offset:7168"
      :  : "v"(wwb), "a"(wor[8]), "a"(wor[9]), "a"(wor[10]), "a"(wor[11]), "a"(wor[12]), "a"(wor[13]), "v"(wwa), "a"(wor[0]), "a"(wor[1]), "a"(wor[2]), "a"(wor[3]), "a"(wor[4]), "a"(wor[5]), "a"(wor[6]), "a"(wor[7]) : "memory"); SB();
  STAMP(39);
  WAITVB(9);
  SB();
  asm volatile("v_mfma_f32_16x16x32_bf16 %0, %9, %10, 0\n\tbuffer_load_dwordx4 %1, %11, %12, %13 offen offset:0 sc1\n\tv_mfma_f32_16x16x32_bf16 %2, %9, %14, 0\n\tv_lshlrev_b32 %3, 16, %15\n\tv_xor_b32 %4, 0, %16\n\tv_mfma_f32_16x16x32_bf16 %5, %9, %17, 0\n\tbuffer_load_dwordx4 %6, %11, %12, %18 offen offset:0 sc1\n\tv_mfma_f32_16x16x32_bf16 %7, %9, %19, 0\n\tv_xor_b32 %8, 32, %16"
      : "=&v"(ac[0][0]), "=&v"(pu[10]), "=&v"(ac[0][1]), "=&v"(bias), "=&v"(xta[0]), "=&v"(ac[0][2]), "=&v"(pu[11]), "=&v"(ac[0][3]), "=&v"(xta[1]) : "v"(pu[0]), "a"(fw2[0][0]), "v"(vFj[0]), "s"(rsW), "s"(su[10]), "a"(fw2[0][1]), "v"(braw), "v"(xs), "a"(fw2[0][2]), "s"(su[11]), "a"(fw2[0][3]) : "memory"); SB();
  WAITVB(10);
  SB();
  asm volatile("v_mfma_f32_16x16x32_bf16 %0, %9, %10, %0\n\tbuffer_load_dwordx4 %1, %11, %12, %13 offen offset:0 sc1\n\tv_mfma_f32_16x16x32_bf16 %2, %9, %14, %2\n\tv_xor_b32 %3, 64, %15\n\tv_xor_b32 %4, 96, %15\n\tv_mfma_f32_16x16x32_bf16 %5, %9, %16, %5\n\tbuffer_load_dwordx4 %6, %11, %12, %17 offen offset:0 sc1\n\tv_mfma_f32_16x16x32_bf16 %7, %9, %18, %7\n\tv_lshlrev_b32 %8, 16, %19"
      : "+v"(ac[0][0]), "=&v"(pu[12]), "+v"(ac[0][1]), "=&v"(xta[2]), "=&v"(xta[3]), "+v"(ac[0][2]), "=&v"(pu[13]), "+v"(ac[0][3]), "=&v"(xe[0]) : "v"(pu[1]), "a"(fw2[1][0]), "v"(vFj[0]), "s"(rsW), "s"(su[12]), "a"(fw2[1][1]), "v"(xs), "a"(fw2[1][2]), "s"(su[13]), "a"(fw2[1][3]), "v"(xv[0][0]) : "memory"); SB();
  WAITVB(11);
  SB();
  asm volatile("v_mfma_f32_16x16x32_bf16 %0, %9, %10, %0\n\tbuffer_load_dwordx4 %1, %11, %12, %13 offen offset:0 sc1\n\tv_mfma_f32_16x16x32_bf16 %2, %9, %14, %2\n\tv_and_b32 %3, 0xffff0000, %15\n\tv_lshlrev_b32 %4, 16, %16\n\tv_mfma_f32_16x16x32_bf16 %5, %9, %17, %5\n\tbuffer_load_dwordx4 %6, %11, %12, %18 offen offset:0 sc1\n\tv_mfma_f32_16x16x32_bf16 %7, %9, %19, %7\n\tv_and_b32 %8, 0xffff0000, %16"
      : "+v"(ac[0][0]), "=&v"(pu[0]), "+v"(ac[0][1]), "=&v"(xe[1]), "=&v"(xe[2]), "+v"(ac[0][2]), "=&v"(pu[1]), "+v"(ac[0][3]), "=&v"(xe[3]) : "v"(pu[2]), "a"(fw2[2][0]), "v"(vFj[1]), "s"(rsW), "s"(su[0]), "a"(fw2[2][1]), "v"(xv[0][0]), "v"(xv[0][1]), "a"(fw2[2][2]), "s"(su[1]), "a"(fw2[2][3]) : "memory"); SB();
  WAITVB(12);
  SB();
  asm volatile("v_mfma_f32_16x16x32_bf16 %0, %10, %11, %0\n\tbuffer_load_dwordx4 %1, %12, %13, %14 offen offset:0 sc1\n\tv_mfma_f32_16x16x32_bf16 %2, %10, %15, %2\n\tv_lshlrev_b32 %3, 16, %16\n\tv_and_b32 %4, 0xffff0000, %16\n\tv_mfma_f32_16x16x32_bf16 %5, %10, %17, %5\n\tv_lshlrev_b32 %6, 16, %18\n\tv_and_b32 %7, 0xffff0000, %18\n\tv_mfma_f32_16x16x32_bf16 %8, %10, %19, %8\n\tv_lshlrev_b32 %9, 16, %20"
      : "+v"(ac[0][0]), "=&v"(pu[2]), "+v"(ac[0][1]), "=&v"(xe[4]), "=&v"(xe[5]), "+v"(ac[0][2]), "=&v"(xe[6]), "=&v"(xe[7]), "+v"(ac[0][3]), "=&v"(xe[8]) : "v"(pu[3]), "a"(fw2[3][0]), "v"(vFj[1]), "s"(rsW), "s"(su[2]), "a"(fw2[3][1]), "v"(xv[1][0]), "a"(fw2[3][2]), "v"(xv[1][1]), "a"(fw2[3][3]), "v"(xv[2][0]) : "memory"); SB();
  WAITVB(12);
  SB();
  asm volatile("v_mfma_f32_16x16x32_bf16 %0, %10, %11, %0\n\tbuffer_load_dwordx4 %1, %12, %13, %14 offen offset:0 sc1\n\tv_mfma_f32_16x16x32_bf16 %2, %10, %15, %2\n\tv_and_b32 %3, 0xffff0000, %16\n\tv_lshlrev_b32 %4, 16, %17\n\tv_mfma_f32_16x16x32_bf16 %5, %10, %18, %5\n\tv_and_b32 %6, 0xffff0000, %17\n\tv_lshlrev_b32 %7, 16, %19\n\tv_mfma_f32_16x16x32_bf16 %8, %10, %20, %8\n\tv_and_b32 %9, 0xffff0000, %19"
      : "+v"(ac[0][0]), "=&v"(pu[3]), "+v"(ac[0][1]), "=&v"(xe[9]), "=&v"(xe[10]), "+v"(ac[0][2]), "=&v"(xe[11]), "=&v"(xe[12]), "+v"(ac[0][3]), "=&v"(xe[13]) : "v"(pu[4]), "a"(fw2[4][0]), "v"(vFj[1]), "s"(rsW), "s"(su[3]), "a"(fw2[4][1]), "v"(xv[2][0]), "v"(xv[2][1]), "a"(fw2[4][2]), "v"(xv[3][0]), "a"(fw2[4][3]) : "memory"); SB();
  WAITVB(12);
  SB();
  asm volatile("v_mfma_f32_16x16x32_bf16 %0, %7, %8, %0\n\tbuffer_load_dwordx4 %1, %9, %10, %11 offen offset:0 sc1\n\tv_mfma_f32_16x16x32_bf16 %2, %7, %12, %2\n\tv_lshlrev_b32 %3, 16, %13\n\tv_and_b32 %4, 0xffff0000, %13\n\tv_mfma_f32_16x16x32_bf16 %5, %7, %14, %5\n\tv_mfma_f32_16x16x32_bf16 %6, %7, %15, %6"
      : "+v"(ac[0][0]), "=&v"(pu[4]), "+v"(ac[0][1]), "=&v"(xe[14]), "=&v"(xe[15]), "+v"(ac[0][2]), "+v"(ac[0][3]) : "v"(pu[5]), "a"(fw2[5][0]), "v"(vFj[1]), "s"(rsW), "s"(su[4]), "a"(fw2[5][1]), "v"(xv[3][1]), "a"(fw2[5][2]), "a"(fw2[5][3]) : "memory"); SB();
  WAITVB(12);
  SB();
  asm volatile("v_mfma_f32_16x16x32_bf16 %0, %6, %7, %0\n\tbuffer_load_dwordx4 %1, %8, %9, %10 offen offset:0 sc1\n\tv_mfma_f32_16x16x32_bf16 %2, %6, %11, %2\n\tbuffer_load_dword %3, %12, %9, 0 offen sc1\n\tv_mfma_f32_16x16x32_bf16 %4, %6, %13, %4\n\tv_mfma_f32_16x16x32_bf16 %5, %6, %14, %5"
      : "+v"(ac[0][0]), "=&v"(pu[5]), "+v"(ac[0][1]), "=&v"(fu2), "+v"(ac[0][2]), "+v"(ac[0][3]) : "v"(pu[6]), "a"(fw2[6][0]), "v"(vFj[1]), "s"(rsW), "s"(su[5]), "a"(fw2[6][1]), "v"(vFU), "a"(fw2[6][2]), "a"(fw2[6][3]) : "memory"); SB();
  WAITVB(13);
  SB();
  asm volatile("v_mfma_f32_16x16x32_bf16 %0, %5, %6, %0\n\tbuffer_load_dwordx4 %1, %7, %8, %9 offen offset:0 sc1\n\tv_mfma_f32_16x16x32_bf16 %2, %5, %10, %2\n\tv_mfma_f32_16x16x32_bf16 %3, %5, %11, %3\n\tv_mfma_f32_16x16x32_bf16 %4, %5, %12, %4"
      : "+v"(ac[0][0]), "=&v"(pu[6]), "+v"(ac[0][1]), "+v"(ac[0][2]), "+v"(ac[0][3]) : "v"(pu[7]), "a"(fw2[7][0]), "v"(vFj[1]), "s"(rsW), "s"(su[6]), "a"(fw2[7][1]), "a"(fw2[7][2]), "a"(fw2[7][3]) : "memory"); SB();
  WAITVB(13);
  SB();
  asm volatile("v_mfma_f32_16x16x32_bf16 %0, %5, %6, %0\n\tbuffer_load_dwordx4 %1, %7, %8, %9 offen offset:0 sc1\n\tv_mfma_f32_16x16x32_bf16 %2, %5, %10, %2\n\tv_mfma_f32_16x16x32_bf16 %3, %5, %11, %3\n\tv_mfma_f32_16x16x32_bf16 %4, %5, %12, %4"
      : "+v"(ac[0][0]), "=&v"(pu[7]), "+v"(ac[0][1]), "+v"(ac[0][2]), "+v"(ac[0][3]) : "v"(pu[8]), "a"(fw2[8][0]), "v"(vFj[1]), "s"(rsW), "s"(su[7]), "a"(fw2[8][1]), "a"(fw2[8][2]), "a"(fw2[8][3]) : "memory"); SB();
  WAITVB(13);
  SB();
  asm volatile("v_mfma_f32_16x16x32_bf16 %0, %5, %6, %0\n\tbuffer_load_dwordx4 %1, %7, %8, %9 offen offset:0 sc1\n\tv_mfma_f32_16x16x32_bf16 %2, %5, %10, %2\n\tv_mfma_f32_16x16x32_bf16 %3, %5, %11, %3\n\tv_mfma_f32_16x16x32_bf16 %4, %5, %12, %4"
      : "+v"(ac[0][0]), "=&v"(pu[8]), "+v"(ac[0][1]), "+v"(ac[0][2]), "+v"(ac[0][3]) : "v"(pu[9]), "v"(fw2[9][0]), "v"(vFj[1]), "s"(rsW), "s"(su[8]), "v"(fw2[9][1]), "v"(fw2[9][2]), "v"(fw2[9][3]) : "memory"); SB();
  WAITVB(13);
  SB();
  asm volatile("v_mfma_f32_16x16x32_bf16 %0, %5, %6, %0\n\tbuffer_load_dwordx4 %1, %7, %8, %9 offen offset:0 sc1\n\tv_mfma_f32_16x16x32_bf16 %2, %5, %10, %2\n\tv_mfma_f32_16x16x32_bf16 %3, %5, %11, %3\n\tv_mfma_f32_16x16x32_bf16 %4, %5, %12, %4"
      : "+v"(ac[0][0]), "=&v"(pu[9]), "+v"(ac[0][1]), "+v"(ac[0][2]), "+v"(ac[0][3]) : "v"(pu[10]), "v"(fw2[10][0]), "v"(vFj[1]), "s"(rsW), "s"(su[9]), "v"(fw2[10][1]), "v"(fw2[10][2]), "v"(fw2[10][3]) : "memory"); SB();
  WAITVB(13);
  SB();
  asm volatile("v_mfma_f32_16x16x32_bf16 %0, %5, %6, %0\n\tbuffer_load_dwordx4 %1, %7, %8, %9 offen offset:0 sc1\n\tv_mfma_f32_16x16x32_bf16 %2, %5, %10, %2\n\tv_mfma_f32_16x16x32_bf16 %3, %5, %11, %3\n\tv_mfma_f32_16x16x32_bf16 %4, %5, %12, %4"
      : "+v"(ac[0][0]), "=&v"(pu[10]), "+v"(ac[0][1]), "+v"(ac[0][2]), "+v"(ac[0][3]) : "v"(pu[11]), "v"(fw2[11][0]), "v"(vFj[1]), "s"(rsW), "s"(su[10]), "v"(fw2[11][1]), "v"(fw2[11][2]), "v"(fw2[11][3]) : "memory"); SB();
  WAITVB(13);
  SB();
  asm volatile("v_mfma_f32_16x16x32_bf16 %0, %4, %5, %0\n\tbuffer_load_dwordx4 %1, %6, %7, %8 offen offset:0 sc1\n\tv_mfma_f32_16x16x32_bf16 %2, %4, %9, %2\n\tv_mfma_f32_16x16x32_bf16 %3, %4, %10, %3"
      : "+v"(ac[0][0]), "=&v"(pu[11]), "+v"(ac[0][1]), "+v"(ac[0][2]) : "v"(pu[12]), "v"(fw2[12][0]), "v"(vFj[1]), "s"(rsW), "s"(su[11]), "v"(fw2[12][1]), "v"(fw2[12][2]) : "memory"); SB();
  u32x4 rsO_ = srd(out, 2048u * 896u); OPQ(rsO_);
  SB();
  asm volatile("v_mfma_f32_16x16x32_bf16 %0, %1, %2, %0"
      : "+v"(ac[0][3]) : "v"(pu[12]), "v"(fw2[12][3])); SB();
  WAITVB(13);
  SB();
  asm volatile("v_mfma_f32_16x16x32_bf16 %0, %5, %6, %0\n\tbuffer_load_dwordx4 %1, %7, %8, %9 offen offset:0 sc1\n\tv_mfma_f32_16x16x32_bf16 %2, %5, %10, %2\n\tv_mfma_f32_16x16x32_bf16 %3, %5, %11, %3\n\tv_mfma_f32_16x16x32_bf16 %4, %5, %12, %4"
      : "+v"(ac[0][0]), "=&v"(pu[12]), "+v"(ac[0][1]), "+v"(ac[0][2]), "+v"(ac[0][3]) : "v"(pu[13]), "v"(fw2[13][0]), "v"(vFj[1]), "s"(rsW), "s"(su[12]), "v"(fw2[13][1]), "v"(fw2[13][2]), "v"(fw2[13][3]) : "memory"); SB();
  STAMP(40);
  WAITVB(13);
  SB();
  asm volatile("v_mfma_f32_16x16x32_bf16 %0, %5, %6, 0\n\tbuffer_load_dwordx4 %1, %7, %8, %9 offen offset:0 sc1\n\tv_mfma_f32_16x16x32_bf16 %2, %5, %10, 0\n\tv_mfma_f32_16x16x32_bf16 %3, %5, %11, 0\n\tv_mfma_f32_16x16x32_bf16 %4, %5, %12, 0"
      : "=&v"(ac[1][0]), "=&v"(pu[13]), "=&v"(ac[1][1]), "=&v"(ac[1][2]), "=&v"(ac[1][3]) : "v"(pu[0]), "a"(fw2[0][0]), "v"(vFj[1]), "s"(rsW), "s"(su[13]), "a"(fw2[0][1]), "a"(fw2[0][2]), "a"(fw2[0][3]) : "memory"); SB();
  WAITVB(13);
  SB();
  asm volatile("v_mfma_f32_16x16x32_bf16 %0, %1, %2, %0"
      : "+v"(ac[1][0]) : "v"(pu[1]), "a"(fw2[1][0])); SB();
  WAITV1(8, fu2);
  if (__builtin_expect(__ballot(fu2 != nonce) != 0ull, 0)) poll1<4>(rsW, vFU, nonce);
  STAMP(19);
  asm volatile("buffer_load_dwordx4 %0, %4, %5, %6 offen offset:0 sc1\n\tv_mfma_f32_16x16x32_bf16 %1, %7, %8, %1\n\tds_write_b128 %9, %10 offset:0\n\tv_mfma_f32_16x16x32_bf16 %2, %7, %11, %2\n\tv_mfma_f32_16x16x32_bf16 %3, %7, %12, %3"
      : "=&v"(pu[0]), "+v"(ac[1][1]), "+v"(ac[1][2]), "+v"(ac[1][3]) : "v"(vFj[2]), "s"(rsW), "s"(su[0]), "v"(pu[1]), "a"(fw2[1][1]), "v"(rwd), "v"(ac[0][0]), "a"(fw2[1][2]), "a"(fw2[1][3]) : "memory"); SB();
  WAITVB(13);
  SB();
  asm volatile("v_mfma_f32_16x16x32_bf16 %0, %5, %6, %0\n\tbuffer_load_dwordx4 %1, %7, %8, %9 offen offset:0 sc1\n\tv_mfma_f32_16x16x32_bf16 %2, %5, %10, %2\n\tds_write_b128 %11, %12 offset:1024\n\tv_mfma_f32_16x16x32_bf16 %3, %5, %13, %3\n\tv_mfma_f32_16x16x32_bf16 %4, %5, %14, %4"
      : "+v"(ac[1][0]), "=&v"(pu[1]), "+v"(ac[1][1]), "+v"(ac[1][2]), "+v"(ac[1][3]) : "v"(pu[2]), "a"(fw2[2][0]), "v"(vFj[2]), "s"(rsW), "s"(su[1]), "a"(fw2[2][1]), "v"(rwd), "v"(ac[0][1]), "a"(fw2[2][2]), "a"(fw2[2][3]) : "memory"); SB();
  WAITVB(13);
  SB();
  asm volatile("v_mfma_f32_16x16x32_bf16 %0, %5, %6, %0\n\tbuffer_load_dwordx4 %1, %7, %8, %9 offen offset:0 sc1\n\tv_mfma_f32_16x16x32_bf16 %2, %5, %10, %2\n\tds_write_b128 %11, %12 offset:2048\n\tv_mfma_f32_16x16x32_bf16 %3, %5, %13, %3\n\tv_mfma_f32_16x16x32_bf16 %4, %5, %14, %4"
      : "+v"(ac[1][0]), "=&v"(pu[2]), "+v"(ac[1][1]), "+v"(ac[1][2]), "+v"(ac[1][3]) : "v"(pu[3]), "a"(fw2[3][0]), "v"(vFj[2]), "s"(rsW), "s"(su[2]), "a"(fw2[3][1]), "v"(rwd), "v"(ac[0][2]), "a"(fw2[3][2]), "a"(fw2[3][3]) : "memory"); SB();
  WAITVB(13);
  SB();
  asm volatile("v_mfma_f32_16x16x32_bf16 %0, %5, %6, %0\n\tbuffer_load_dwordx4 %1, %7, %8, %9 offen offset:0 sc1\n\tv_mfma_f32_16x16x32_bf16 %2, %5, %10, %2\n\tds_write_b128 %11, %12 offset:3072\n\tv_mfma_f32_16x16x32_bf16 %3, %5, %13, %3\n\tv_mfma_f32_16x16x32_bf16 %4, %5, %14, %4"
      : "+v"(ac[1][0]), "=&v"(pu[3]), "+v"(ac[1][1]), "+v"(ac[1][2]), "+v"(ac[1][3]) : "v"(pu[4]), "a"(fw2[4][0]), "v"(vFj[2]), "s"(rsW), "s"(su[3]), "a"(fw2[4][1]), "v"(rwd), "v"(ac[0][3]), "a"(fw2[4][2]), "a"(fw2[4][3]) : "memory"); SB();
  WAITVB(13);
  SB();
  asm volatile("v_mfma_f32_16x16x32_bf16 %0, %3, %4, %0\n\tbuffer_load_dwordx4 %1, %5, %6, %7 offen offset:0 sc1\n\tv_mfma_f32_16x16x32_bf16 %2, %3, %8, %2"
      : "+v"(ac[1][0]), "=&v"(pu[4]), "+v"(ac[1][1]) : "v"(pu[5]), "a"(fw2[5][0]), "v"(vFj[2]), "s"(rsW), "s"(su[4]), "a"(fw2[5][1]) : "memory"); SB();
  WAITL(0);
  BAR0();
  SB();
  asm volatile("v_mfma_f32_16x16x32_bf16 %0, %5, %6, %0\n\tds_read_b128 %1, %7 offset:0\n\tds_read_b128 %2, %7 offset:4096\n\tv_mfma_f32_16x16x32_bf16 %3, %5, %8, %3\n\tds_read_b128 %4, %7 offset:8192"
      : "+v"(ac[1][2]), "=&v"(py[0][0]), "=&v"(py[0][1]), "+v"(ac[1][3]), "=&v"(py[0][2]) : "v"(pu[5]), "a"(fw2[5][2]), "v"(rr), "a"(fw2[5][3]) : "memory"); SB();
  WAITVB(12);
  SB();
  asm volatile("v_mfma_f32_16x16x32_bf16 %0, %8, %9, %0\n\tbuffer_load_dwordx4 %1, %10, %11, %12 offen offset:0 sc1\n\tv_mfma_f32_16x16x32_bf16 %2, %8, %13, %2\n\tds_read_b128 %3, %14 offset:12288\n\tds_read_b128 %4, %15 offset:0\n\tv_mfma_f32_16x16x32_bf16 %5, %8, %16, %5\n\tv_mfma_f32_16x16x32_bf16 %6, %8, %17, %6\n\tds_read_b128 %7, %15 offset:8192"
      : "+v"(ac[1][0]), "=&v"(pu[5]), "+v"(ac[1][1]), "=&v"(py[0][3]), "=&a"(fwo[0]), "+v"(ac[1][2]), "+v"(ac[1][3]), "=&a"(fwo[1]) : "v"(pu[6]), "a"(fw2[6][0]), "v"(vFj[2]), "s"(rsW), "s"(su[5]), "a"(fw2[6][1]), "v"(rr), "v"(ro), "a"(fw2[6][2]), "a"(fw2[6][3]) : "memory"); SB();
  WAITVB(12);
  SB();
  asm volatile("v_mfma_f32_16x16x32_bf16 %0, %5, %6, %0\n\tbuffer_load_dwordx4 %1, %7, %8, %9 offen offset:0 sc1\n\tv_mfma_f32_16x16x32_bf16 %2, %5, %10, %2\n\tds_read_b128 %3, %11 offset:16384\n\tv_mfma_f32_16x16x32_bf16 %4, %5, %12, %4"
      : "+v"(ac[1][0]), "=&v"(pu[6]), "+v"(ac[1][1]), "=&a"(fwo[2]), "+v"(ac[1][2]) : "v"(pu[7]), "a"(fw2[7][0]), "v"(vFj[2]), "s"(rsW), "s"(su[6]), "a"(fw2[7][1]), "v"(ro), "a"(fw2[7][2]) : "memory"); SB();
  WAITL(3);
  asm volatile("v_add_f32 %0, %3, %4\n\tv_mfma_f32_16x16x32_bf16 %1, %5, %6, %1\n\tv_add_f32 %2, %7, %4"
      : "=&v"(ye[0]), "+v"(ac[1][3]), "=&v"(ye[1]) : "v"(py[0][0][0]), "v"(bias), "v"(pu[7]), "a"(fw2[7][3]), "v"(py[0][0][1])); SB();
  WAITVB(12);
  SB();
  asm volatile("v_mfma_f32_16x16x32_bf16 %0, %9, %10, %0\n\tbuffer_load_dwordx4 %1, %11, %12, %13 offen offset:0 sc1\n\tv_mfma_f32_16x16x32_bf16 %2, %9, %14, %2\n\tv_add_f32 %3, %15, %16\n\tv_add_f32 %4, %17, %16\n\tv_mfma_f32_16x16x32_bf16 %5, %9, %18, %5\n\tv_add_f32 %6, %19, %20\n\tv_add_f32 %7, %21, %22\n\tv_mfma_f32_16x16x32_bf16 %8, %9, %23, %8"
      : "+v"(ac[1][0]), "=&v"(pu[7]), "+v"(ac[1][1]), "=&v"(ye[2]), "=&v"(ye[3]), "+v"(ac[1][2]), "=&v"(ze[0]), "=&v"(ze[1]), "+v"(ac[1][3]) : "v"(pu[8]), "a"(fw2[8][0]), "v"(vFj[2]), "s"(rsW), "s"(su[7]), "a"(fw2[8][1]), "v"(py[0][0][2]), "v"(bias), "v"(py[0][0][3]), "a"(fw2[8][2]), "v"(py[0][1][0]), "v"(py[0][2][0]), "v"(py[0][1][1]), "v"(py[0][2][1]), "a"(fw2[8][3]) : "memory"); SB();
  asm volatile("v_add_f32 %0, %1, %2"
      : "=&v"(ze[2]) : "v"(py[0][1][2]), "v"(py[0][2][2])); SB();
  WAITVB(12);
  SB();
  asm volatile("v_mfma_f32_16x16x32_bf16 %0, %9, %10, %0\n\tbuffer_load_dwordx4 %1, %11, %12, %13 offen offset:0 sc1\n\tv_mfma_f32_16x16x32_bf16 %2, %9, %14, %2\n\tv_add_f32 %3, %15, %16\n\tv_add_f32 %4, %4, %17\n\tv_mfma_f32_16x16x32_bf16 %5, %9, %18, %5\n\tv_add_f32 %6, %6, %19\n\tv_add_f32 %7, %7, %20\n\tv_mfma_f32_16x16x32_bf16 %8, %9, %21, %8"
      : "+v"(ac[1][0]), "=&v"(pu[8]), "+v"(ac[1][1]), "=&v"(ze[3]), "+v"(ye[0]), "+v"(ac[1][2]), "+v"(ye[1]), "+v"(ye[2]), "+v"(ac[1][3]) : "v"(pu[9]), "v"(fw2[9][0]), "v"(vFj[2]), "s"(rsW), "s"(su[8]), "v"(fw2[9][1]), "v"(py[0][1][3]), "v"(py[0][2][3]), "v"(py[0][3][0]), "v"(fw2[9][2]), "v"(py[0][3][1]), "v"(py[0][3][2]), "v"(fw2[9][3]) : "memory"); SB();
  asm volatile("v_add_f32 %0, %0, %1"
      : "+v"(ye[3]) : "v"(py[0][3][3])); SB();
  WAITVB(12);
  SB();
  asm volatile("v_mfma_f32_16x16x32_bf16 %0, %10, %11, %0\n\tbuffer_load_dwordx4 %1, %12, %13, %14 offen offset:0 sc1\n\tv_mfma_f32_16x16x32_bf16 %2, %10, %15, %2\n\tv_add_f32 %3, %3, %4\n\tv_add_f32 %5, %5, %16\n\tv_mfma_f32_16x16x32_bf16 %6, %10, %17, %6\n\tv_add_f32 %7, %7, %18\n\tv_add_f32 %8, %8, %19\n\tv_mfma_f32_16x16x32_bf16 %9, %10, %20, %9\n\tv_mul_f32 %4, 0xbfb8aa3b, %3"
      : "+v"(ac[1][0]), "=&v"(pu[9]), "+v"(ac[1][1]), "+v"(ye[0]), "+v"(ze[0]), "+v"(ye[1]), "+v"(ac[1][2]), "+v"(ye[2]), "+v"(ye[3]), "+v"(ac[1][3]) : "v"(pu[10]), "v"(fw2[10][0]), "v"(vFj[2]), "s"(rsW), "s"(su[9]), "v"(fw2[10][1]), "v"(ze[1]), "v"(fw2[10][2]), "v"(ze[2]), "v"(ze[3]), "v"(fw2[10][3]) : "memory"); SB();
  WAITVB(12);
  SB();
  asm volatile("v_mfma_f32_16x16x32_bf16 %0, %9, %10, %0\n\tbuffer_load_dwordx4 %1, %11, %12, %13 offen offset:0 sc1\n\tv_mfma_f32_16x16x32_bf16 %2, %9, %14, %2\n\tv_mul_f32 %3, 0xbfb8aa3b, %15\n\tv_mul_f32 %4, 0xbfb8aa3b, %16\n\tv_mfma_f32_16x16x32_bf16 %5, %9, %17, %5\n\tds_read_b128 %6, %18 offset:24576\n\tv_mul_f32 %7, 0xbfb8aa3b, %19\n\tv_mfma_f32_16x16x32_bf16 %8, %9, %20, %8"
      : "+v"(ac[1][0]), "=&v"(pu[10]), "+v"(ac[1][1]), "=&v"(ze[1]), "=&v"(ze[2]), "+v"(ac[1][2]), "=&a"(fwo[3]), "=&v"(ze[3]), "+v"(ac[1][3]) : "v"(pu[11]), "v"(fw2[11][0]), "v"(vFj[2]), "s"(rsW), "s"(su[10]), "v"(fw2[11][1]), "v"(ye[1]), "v"(ye[2]), "v"(fw2[11][2]), "v"(ro), "v"(ye[3]), "v"(fw2[11][3]) : "memory"); SB();
  WAITVB(12);
  SB();
  asm volatile("v_mfma_f32_16x16x32_bf16 %0, %8, %9, %0\n\tbuffer_load_dwordx4 %1, %10, %11, %12 offen offset:0 sc1\n\tv_mfma_f32_16x16x32_bf16 %2, %8, %13, %2\n\tv_exp_f32 %3, %3\n\tv_mfma_f32_16x16x32_bf16 %4, %8, %14, %4\n\tv_exp_f32 %5, %5\n\tv_mfma_f32_16x16x32_bf16 %6, %8, %15, %6\n\tds_read_b128 %7, %16 offset:1024"
      : "+v"(ac[1][0]), "=&v"(pu[11]), "+v"(ac[1][1]), "+v"(ze[0]), "+v"(ac[1][2]), "+v"(ze[1]), "+v"(ac[1][3]), "=&a"(fwo[4]) : "v"(pu[12]), "v"(fw2[12][0]), "v"(vFj[2]), "s"(rsW), "s"(su[11]), "v"(fw2[12][1]), "v"(fw2[12][2]), "v"(fw2[12][3]), "v"(ro) : "memory"); SB();
  WAITVB(12);
  SB();
  asm volatile("v_mfma_f32_16x16x32_bf16 %0, %9, %10, %0\n\tbuffer_load_dwordx4 %1, %11, %12, %13 offen offset:0 sc1\n\tv_mfma_f32_16x16x32_bf16 %2, %9, %14, %2\n\tv_exp_f32 %3, %3\n\tv_mfma_f32_16x16x32_bf16 %4, %9, %15, %4\n\tv_exp_f32 %5, %5\n\tv_mfma_f32_16x16x32_bf16 %6, %9, %16, %6\n\tv_add_f32 %7, 1.0, %7\n\tv_add_f32 %8, 1.0, %8"
      : "+v"(ac[1][0]), "=&v"(pu[12]), "+v"(ac[1][1]), "+v"(ze[2]), "+v"(ac[1][2]), "+v"(ze[3]), "+v"(ac[1][3]), "+v"(ze[0]), "+v"(ze[1]) : "v"(pu[13]), "v"(fw2[13][0]), "v"(vFj[2]), "s"(rsW), "s"(su[12]), "v"(fw2[13][1]), "v"(fw2[13][2]), "v"(fw2[13][3]) : "memory"); SB();
  STAMP(41);
  WAITVB(12);
  SB();
  asm volatile("v_mfma_f32_16x16x32_bf16 %0, %9, %10, 0\n\tbuffer_load_dwordx4 %1, %11, %12, %13 offen offset:0 sc1\n\tv_mfma_f32_16x16x32_bf16 %2, %9, %14, 0\n\tbuffer_load_dword %3, %15, %12, 0 offen sc1\n\tv_mfma_f32_16x16x32_bf16 %4, %9, %16, 0\n\tv_add_f32 %5, 1.0, %5\n\tv_add_f32 %6, 1.0, %6\n\tv_mfma_f32_16x16x32_bf16 %7, %9, %17, 0\n\tds_read_b128 %8, %18 offset:9216"
      : "=&v"(ac[0][0]), "=&v"(pu[13]), "=&v"(ac[0][1]), "=&v"(fo), "=&v"(ac[0][2]), "+v"(ze[2]), "+v"(ze[3]), "=&v"(ac[0][3]), "=&a"(fwo[5]) : "v"(pu[0]), "a"(fw2[0][0]), "v"(vFj[2]), "s"(rsW), "s"(su[13]), "a"(fw2[0][1]), "v"(vFO), "a"(fw2[0][2]), "a"(fw2[0][3]), "v"(ro) : "memory"); SB();
  WAITVB(13);
  SB();
  asm volatile("v_mfma_f32_16x16x32_bf16 %0, %7, %8, %0\n\tbuffer_load_dwordx4 %1, %9, %10, %11 offen offset:0 sc1\n\tv_mfma_f32_16x16x32_bf16 %2, %7, %12, %2\n\tds_write_b128 %13, %14 offset:18432\n\tv_mfma_f32_16x16x32_bf16 %3, %7, %15, %3\n\tv_rcp_f32 %4, %4\n\tv_mfma_f32_16x16x32_bf16 %5, %7, %16, %5\n\tds_read_b128 %6, %17 offset:17408"
      : "+v"(ac[0][0]), "=&v"(pu[0]), "+v"(ac[0][1]), "+v"(ac[0][2]), "+v"(ze[0]), "+v"(ac[0][3]), "=&a"(fwo[6]) : "v"(pu[1]), "a"(fw2[1][0]), "v"(vFj[3]), "s"(rsW), "s"(su[0]), "a"(fw2[1][1]), "v"(rwd), "v"(ac[1][0]), "a"(fw2[1][2]), "a"(fw2[1][3]), "v"(ro) : "memory"); SB();
  WAITVB(13);
  SB();
  asm volatile("v_mfma_f32_16x16x32_bf16 %0, %7, %8, %0\n\tbuffer_load_dwordx4 %1, %9, %10, %11 offen offset:0 sc1\n\tv_mfma_f32_16x16x32_bf16 %2, %7, %12, %2\n\tds_write_b128 %13, %14 offset:19456\n\tv_mfma_f32_16x16x32_bf16 %3, %7, %15, %3\n\tv_rcp_f32 %4, %4\n\tv_mfma_f32_16x16x32_bf16 %5, %7, %16, %5\n\tds_read_b128 %6, %17 offset:25600"
      : "+v"(ac[0][0]), "=&v"(pu[1]), "+v"(ac[0][1]), "+v"(ac[0][2]), "+v"(ze[1]), "+v"(ac[0][3]), "=&a"(fwo[7]) : "v"(pu[2]), "a"(fw2[2][0]), "v"(vFj[3]), "s"(rsW), "s"(su[1]), "a"(fw2[2][1]), "v"(rwd), "v"(ac[1][1]), "a"(fw2[2][2]), "a"(fw2[2][3]), "v"(ro) : "memory"); SB();
  WAITVB(13);
  SB();
  asm volatile("v_mfma_f32_16x16x32_bf16 %0, %7, %8, %0\n\tbuffer_load_dwordx4 %1, %9, %10, %11 offen offset:0 sc1\n\tv_mfma_f32_16x16x32_bf16 %2, %7, %12, %2\n\tds_write_b128 %13, %14 offset:20480\n\tv_mfma_f32_16x16x32_bf16 %3, %7, %15, %3\n\tv_rcp_f32 %4, %4\n\tv_mfma_f32_16x16x32_bf16 %5, %7, %16, %5\n\tds_read_b128 %6, %17 offset:2048"
      : "+v"(ac[0][0]), "=&v"(pu[2]), "+v"(ac[0][1]), "+v"(ac[0][2]), "+v"(ze[2]), "+v"(ac[0][3]), "=&a"(fwo[8]) : "v"(pu[3]), "a"(fw2[3][0]), "v"(vFj[3]), "s"(rsW), "s"(su[2]), "a"(fw2[3][1]), "v"(rwd), "v"(ac[1][2]), "a"(fw2[3][2]), "a"(fw2[3][3]), "v"(ro) : "memory"); SB();
  WAITVB(13);
  SB();
  asm volatile("v_mfma_f32_16x16x32_bf16 %0, %7, %8, %0\n\tbuffer_load_dwordx4 %1, %9, %10, %11 offen offset:0 sc1\n\tv_mfma_f32_16x16x32_bf16 %2, %7, %12, %2\n\tds_write_b128 %13, %14 offset:21504\n\tv_mfma_f32_16x16x32_bf16 %3, %7, %15, %3\n\tv_rcp_f32 %4, %4\n\tv_mfma_f32_16x16x32_bf16 %5, %7, %16, %5\n\tv_mul_f32 %6, %17, %18"
      : "+v"(ac[0][0]), "=&v"(pu[3]), "+v"(ac[0][1]), "+v"(ac[0][2]), "+v"(ze[3]), "+v"(ac[0][3]), "=&v"(ge[0]) : "v"(pu[4]), "a"(fw2[4][0]), "v"(vFj[3]), "s"(rsW), "s"(su[3]), "a"(fw2[4][1]), "v"(rwd), "v"(ac[1][3]), "a"(fw2[4][2]), "a"(fw2[4][3]), "v"(ye[0]), "v"(ze[0]) : "memory"); SB();
  WAITVB(13);
  SB();
  asm volatile("v_mfma_f32_16x16x32_bf16 %0, %3, %4, %0\n\tbuffer_load_dwordx4 %1, %5, %6, %7 offen offset:0 sc1\n\tv_mfma_f32_16x16x32_bf16 %2, %3, %8, %2"
      : "+v"(ac[0][0]), "=&v"(pu[4]), "+v"(ac[0][1]) : "v"(pu[5]), "a"(fw2[5][0]), "v"(vFj[3]), "s"(rsW), "s"(su[4]), "a"(fw2[5][1]) : "memory"); SB();
  WAITL(0);
  BAR0();
  SB();
  asm volatile("v_mfma_f32_16x16x32_bf16 %0, %5, %6, %0\n\tds_read_b128 %1, %7 offset:18432\n\tds_read_b128 %2, %7 offset:22528\n\tv_mfma_f32_16x16x32_bf16 %3, %5, %8, %3\n\tds_read_b128 %4, %7 offset:26624"
      : "+v"(ac[0][2]), "=&v"(py[1][0]), "=&v"(py[1][1]), "+v"(ac[0][3]), "=&v"(py[1][2]) : "v"(pu[5]), "a"(fw2[5][2]), "v"(rr), "a"(fw2[5][3]) : "memory"); SB();
  WAITVB(13);
  SB();
  asm volatile("v_mfma_f32_16x16x32_bf16 %0, %10, %11, %0\n\tbuffer_load_dwordx4 %1, %12, %13, %14 offen offset:0 sc1\n\tv_mfma_f32_16x16x32_bf16 %2, %10, %15, %2\n\tds_read_b128 %3, %16 offset:30720\n\tv_mul_f32 %4, %17, %18\n\tv_mfma_f32_16x16x32_bf16 %5, %10, %19, %5\n\tv_mul_f32 %6, %20, %21\n\tv_mul_f32 %7, %22, %23\n\tv_mfma_f32_16x16x32_bf16 %8, %10, %24, %8\n\tds_read_b128 %9, %25 offset:10240"
      : "+v"(ac[0][0]), "=&v"(pu[5]), "+v"(ac[0][1]), "=&v"(py[1][3]), "=&v"(ge[1]), "+v"(ac[0][2]), "=&v"(ge[2]), "=&v"(ge[3]), "+v"(ac[0][3]), "=&a"(fwo[9]) : "v"(pu[6]), "a"(fw2[6][0]), "v"(vFj[3]), "s"(rsW), "s"(su[5]), "a"(fw2[6][1]), "v"(rr), "v"(ye[1]), "v"(ze[1]), "a"(fw2[6][2]), "v"(ye[2]), "v"(ze[2]), "v"(ye[3]), "v"(ze[3]), "a"(fw2[6][3]), "v"(ro) : "memory"); SB();
  WAITVB(13);
  SB();
  asm volatile("v_mfma_f32_16x16x32_bf16 %0, %5, %6, %0\n\tbuffer_load_dwordx4 %1, %7, %8, %9 offen offset:0 sc1\n\tv_mfma_f32_16x16x32_bf16 %2, %5, %10, %2\n\tds_read_b128 %3, %11 offset:18432\n\tv_mfma_f32_16x16x32_bf16 %4, %5, %12, %4"
      : "+v"(ac[0][0]), "=&v"(pu[6]), "+v"(ac[0][1]), "=&a"(fwo[10]), "+v"(ac[0][2]) : "v"(pu[7]), "a"(fw2[7][0]), "v"(vFj[3]), "s"(rsW), "s"(su[6]), "a"(fw2[7][1]), "v"(ro), "a"(fw2[7][2]) : "memory"); SB();
  WAITL(2);
  asm volatile("v_add_f32 %0, %3, %4\n\tv_mfma_f32_16x16x32_bf16 %1, %5, %6, %1\n\tv_add_f32 %2, %7, %4"
      : "=&v"(ye[0]), "+v"(ac[0][3]), "=&v"(ye[1]) : "v"(py[1][0][0]), "v"(bias), "v"(pu[7]), "a"(fw2[7][3]), "v"(py[1][0][1])); SB();
  WAITVB(13);
  SB();
  asm volatile("v_mfma_f32_16x16x32_bf16 %0, %9, %10, %0\n\tbuffer_load_dwordx4 %1, %11, %12, %13 offen offset:0 sc1\n\tv_mfma_f32_16x16x32_bf16 %2, %9, %14, %2\n\tv_add_f32 %3, %15, %16\n\tv_add_f32 %4, %17, %16\n\tv_mfma_f32_16x16x32_bf16 %5, %9, %18, %5\n\tv_add_f32 %6, %19, %20\n\tv_add_f32 %7, %21, %22\n\tv_mfma_f32_16x16x32_bf16 %8, %9, %23, %8"
      : "+v"(ac[0][0]), "=&v"(pu[7]), "+v"(ac[0][1]), "=&v"(ye[2]), "=&v"(ye[3]), "+v"(ac[0][2]), "=&v"(ze[0]), "=&v"(ze[1]), "+v"(ac[0][3]) : "v"(pu[8]), "a"(fw2[8][0]), "v"(vFj[3]), "s"(rsW), "s"(su[7]), "a"(fw2[8][1]), "v"(py[1][0][2]), "v"(bias), "v"(py[1][0][3]), "a"(fw2[8][2]), "v"(py[1][1][0]), "v"(py[1][2][0]), "v"(py[1][1][1]), "v"(py[1][2][1]), "a"(fw2[8][3]) : "memory"); SB();
  asm volatile("v_add_f32 %0, %1, %2"
      : "=&v"(ze[2]) : "v"(py[1][1][2]), "v"(py[1][2][2])); SB();
  WAITVB(13);
  SB();
  asm volatile("v_mfma_f32_16x16x32_bf16 %0, %9, %10, %0\n\tbuffer_load_dwordx4 %1, %11, %12, %13 offen offset:0 sc1\n\tv_mfma_f32_16x16x32_bf16 %2, %9, %14, %2\n\tv_add_f32 %3, %15, %16\n\tv_add_f32 %4, %4, %17\n\tv_mfma_f32_16x16x32_bf16 %5, %9, %18, %5\n\tv_add_f32 %6, %6, %19\n\tv_add_f32 %7, %7, %20\n\tv_mfma_f32_16x16x32_bf16 %8, %9, %21, %8"
      : "+v"(ac[0][0]), "=&v"(pu[8]), "+v"(ac[0][1]), "=&v"(ze[3]), "+v"(ye[0]), "+v"(ac[0][2]), "+v"(ye[1]), "+v"(ye[2]), "+v"(ac[0][3]) : "v"(pu[9]), "v"(fw2[9][0]), "v"(vFj[3]), "s"(rsW), "s"(su[8]), "v"(fw2[9][1]), "v"(py[1][1][3]), "v"(py[1][2][3]), "v"(py[1][3][0]), "v"(fw2[9][2]), "v"(py[1][3][1]), "v"(py[1][3][2]), "v"(fw2[9][3]) : "memory"); SB();
  asm volatile("v_add_f32 %0, %0, %1"
      : "+v"(ye[3]) : "v"(py[1][3][3])); SB();
  WAITVB(13);
  SB();
  asm volatile("v_mfma_f32_16x16x32_bf16 %0, %10, %11, %0\n\tbuffer_load_dwordx4 %1, %12, %13, %14 offen offset:0 sc1\n\tv_mfma_f32_16x16x32_bf16 %2, %10, %15, %2\n\tv_add_f32 %3, %3, %4\n\tv_add_f32 %5, %5, %16\n\tv_mfma_f32_16x16x32_bf16 %6, %10, %17, %6\n\tv_add_f32 %7, %7, %18\n\tv_add_f32 %8, %8, %19\n\tv_mfma_f32_16x16x32_bf16 %9, %10, %20, %9\n\tv_mul_f32 %4, 0xbfb8aa3b, %3"
      : "+v"(ac[0][0]), "=&v"(pu[9]), "+v"(ac[0][1]), "+v"(ye[0]), "+v"(ze[0]), "+v"(ye[1]), "+v"(ac[0][2]), "+v"(ye[2]), "+v"(ye[3]), "+v"(ac[0][3]) : "v"(pu[10]), "v"(fw2[10][0]), "v"(vFj[3]), "s"(rsW), "s"(su[9]), "v"(fw2[10][1]), "v"(ze[1]), "v"(fw2[10][2]), "v"(ze[2]), "v"(ze[3]), "v"(fw2[10][3]) : "memory"); SB();
  WAITVB(13);
  SB();
  asm volatile("v_mfma_f32_16x16x32_bf16 %0, %9, %10, %0\n\tbuffer_load_dwordx4 %1, %11, %12, %13 offen offset:0 sc1\n\tv_mfma_f32_16x16x32_bf16 %2, %9, %14, %2\n\tv_mul_f32 %3, 0xbfb8aa3b, %15\n\tv_mul_f32 %4, 0xbfb8aa3b, %16\n\tv_mfma_f32_16x16x32_bf16 %5, %9, %17, %5\n\tds_read_b128 %6, %18 offset:26624\n\tv_mul_f32 %7, 0xbfb8aa3b, %19\n\tv_mfma_f32_16x16x32_bf16 %8, %9, %20, %8"
      : "+v"(ac[0][0]), "=&v"(pu[10]), "+v"(ac[0][1]), "=&v"(ze[1]), "=&v"(ze[2]), "+v"(ac[0][2]), "=&a"(fwo[11]), "=&v"(ze[3]), "+v"(ac[0][3]) : "v"(pu[11]), "v"(fw2[11][0]), "v"(vFj[3]), "s"(rsW), "s"(su[10]), "v"(fw2[11][1]), "v"(ye[1]), "v"(ye[2]), "v"(fw2[11][2]), "v"(ro), "v"(ye[3]), "v"(fw2[11][3]) : "memory"); SB();
  WAITVB(13);
  SB();
  asm volatile("v_mfma_f32_16x16x32_bf16 %0, %8, %9, %0\n\tbuffer_load_dwordx4 %1, %10, %11, %12 offen offset:0 sc1\n\tv_mfma_f32_16x16x32_bf16 %2, %8, %13, %2\n\tv_exp_f32 %3, %3\n\tv_mfma_f32_16x16x32_bf16 %4, %8, %14, %4\n\tv_exp_f32 %5, %5\n\tv_mfma_f32_16x16x32_bf16 %6, %8, %15, %6\n\tds_read_b128 %7, %16 offset:3072"
      : "+v"(ac[0][0]), "=&v"(pu[11]), "+v"(ac[0][1]), "+v"(ze[0]), "+v"(ac[0][2]), "+v"(ze[1]), "+v"(ac[0][3]), "=&a"(fwo[12]) : "v"(pu[12]), "v"(fw2[12][0]), "v"(vFj[3]), "s"(rsW), "s"(su[11]), "v"(fw2[12][1]), "v"(fw2[12][2]), "v"(fw2[12][3]), "v"(ro) : "memory"); SB();
  WAITVB(13);
  SB();
  asm volatile("v_mfma_f32_16x16x32_bf16 %0, %9, %10, %0\n\tbuffer_load_dwordx4 %1, %11, %12, %13 offen offset:0 sc1\n\tv_mfma_f32_16x16x32_bf16 %2, %9, %14, %2\n\tv_exp_f32 %3, %3\n\tv_mfma_f32_16x16x32_bf16 %4, %9, %15, %4\n\tv_exp_f32 %5, %5\n\tv_mfma_f32_16x16x32_bf16 %6, %9, %16, %6\n\tv_add_f32 %7, 1.0, %7\n\tv_add_f32 %8, 1.0, %8"
      : "+v"(ac[0][0]), "=&v"(pu[12]), "+v"(ac[0][1]), "+v"(ze[2]), "+v"(ac[0][2]), "+v"(ze[3]), "+v"(ac[0][3]), "+v"(ze[0]), "+v"(ze[1]) : "v"(pu[13]), "v"(fw2[13][0]), "v"(vFj[3]), "s"(rsW), "s"(su[12]), "v"(fw2[13][1]), "v"(fw2[13][2]), "v"(fw2[13][3]) : "memory"); SB();
  STAMP(42);
  WAITVB(12);
  SB();
  asm volatile("v_mfma_f32_16x16x32_bf16 %0, %9, %10, 0\n\tbuffer_load_dwordx4 %1, %11, %12, %13 offen offset:0 sc1\n\tv_mfma_f32_16x16x32_bf16 %2, %9, %14, 0\n\tv_add_f32 %3, 1.0, %3\n\tv_add_f32 %4, 1.0, %4\n\tv_mfma_f32_16x16x32_bf16 %5, %9, %15, 0\n\tv_rcp_f32 %6, %6\n\tv_mfma_f32_16x16x32_bf16 %7, %9, %16, 0\n\tds_read_b128 %8, %17 offset:11264"
      : "=&v"(ac[1][0]), "=&v"(pu[13]), "=&v"(ac[1][1]), "+v"(ze[2]), "+v"(ze[3]), "=&v"(ac[1][2]), "+v"(ze[0]), "=&v"(ac[1][3]), "=&a"(fwo[13]) : "v"(pu[0]), "a"(fw2[0][0]), "v"(vFj[3]), "s"(rsW), "s"(su[13]), "a"(fw2[0][1]), "a"(fw2[0][2]), "a"(fw2[0][3]), "v"(ro) : "memory"); SB();
  WAITVB(12);
  SB();
  asm volatile("v_mfma_f32_16x16x32_bf16 %0, %1, %2, %0"
      : "+v"(ac[1][0]) : "v"(pu[1]), "a"(fw2[1][0])); SB();
  WAITV1(14, fo);
  if (__builtin_expect(__ballot(fo != nonce) != 0ull, 0)) poll1<4>(rsW, vFO, nonce);
  STAMP(18);
  asm volatile("buffer_load_dwordx4 %0, %6, %7, %8 offen offset:0 sc1\n\tv_mfma_f32_16x16x32_bf16 %1, %9, %10, %1\n\tds_write_b128 %11, %12 offset:0\n\tv_mfma_f32_16x16x32_bf16 %2, %9, %13, %2\n\tv_rcp_f32 %3, %3\n\tv_mfma_f32_16x16x32_bf16 %4, %9, %14, %4\n\tds_read_b128 %5, %15 offset:19456"
      : "=&v"(pu[0]), "+v"(ac[1][1]), "+v"(ac[1][2]), "+v"(ze[1]), "+v"(ac[1][3]), "=&a"(fwo[14]) : "v"(vF), "s"(rsW), "s"(so0), "v"(pu[1]), "a"(fw2[1][1]), "v"(rwd), "v"(ac[0][0]), "a"(fw2[1][2]), "a"(fw2[1][3]), "v"(ro) : "memory"); SB();
  WAITVB(12);
  SB();
  asm volatile("v_mfma_f32_16x16x32_bf16 %0, %7, %8, %0\n\tbuffer_load_dwordx4 %1, %9, %10, %11 offen offset:128 sc1\n\tv_mfma_f32_16x16x32_bf16 %2, %7, %12, %2\n\tds_write_b128 %13, %14 offset:1024\n\tv_mfma_f32_16x16x32_bf16 %3, %7, %15, %3\n\tv_rcp_f32 %4, %4\n\tv_mfma_f32_16x16x32_bf16 %5, %7, %16, %5\n\tds_read_b128 %6, %17 offset:27648"
      : "+v"(ac[1][0]), "=&v"(pu[1]), "+v"(ac[1][1]), "+v"(ac[1][2]), "+v"(ze[2]), "+v"(ac[1][3]), "=&a"(fwo[15]) : "v"(pu[2]), "a"(fw2[2][0]), "v"(vF), "s"(rsW), "s"(so0), "a"(fw2[2][1]), "v"(rwd), "v"(ac[0][1]), "a"(fw2[2][2]), "a"(fw2[2][3]), "v"(ro) : "memory"); SB();
  WAITVB(12);
  SB();
  asm volatile("v_mfma_f32_16x16x32_bf16 %0, %7, %8, %0\n\tbuffer_load_dwordx4 %1, %9, %10, %11 offen offset:256 sc1\n\tv_mfma_f32_16x16x32_bf16 %2, %7, %12, %2\n\tds_write_b128 %13, %14 offset:2048\n\tv_mfma_f32_16x16x32_bf16 %3, %7, %15, %3\n\tv_rcp_f32 %4, %4\n\tv_mfma_f32_16x16x32_bf16 %5, %7, %16, %5\n\tv_mul_f32 %6, %17, %18"
      : "+v"(ac[1][0]), "=&v"(pu[2]), "+v"(ac[1][1]), "+v"(ac[1][2]), "+v"(ze[3]), "+v"(ac[1][3]), "=&v"(ge[4]) : "v"(pu[3]), "a"(fw2[3][0]), "v"(vF), "s"(rsW), "s"(so0), "a"(fw2[3][1]), "v"(rwd), "v"(ac[0][2]), "a"(fw2[3][2]), "a"(fw2[3][3]), "v"(ye[0]), "v"(ze[0]) : "memory"); SB();
  WAITVB(12);
  SB();
  asm volatile("v_mfma_f32_16x16x32_bf16 %0, %8, %9, %0\n\tbuffer_load_dwordx4 %1, %10, %11, %12 offen offset:384 sc1\n\tv_mfma_f32_16x16x32_bf16 %2, %8, %13, %2\n\tds_write_b128 %14, %15 offset:3072\n\tv_mfma_f32_16x16x32_bf16 %3, %8, %16, %3\n\tv_mul_f32 %4, %17, %18\n\tv_mul_f32 %5, %19, %20\n\tv_mfma_f32_16x16x32_bf16 %6, %8, %21, %6\n\tv_mul_f32 %7, %22, %23"
      : "+v"(ac[1][0]), "=&v"(pu[3]), "+v"(ac[1][1]), "+v"(ac[1][2]), "=&v"(ge[5]), "=&v"(ge[6]), "+v"(ac[1][3]), "=&v"(ge[7]) : "v"(pu[4]), "a"(fw2[4][0]), "v"(vF), "s"(rsW), "s"(so0), "a"(fw2[4][1]), "v"(rwd), "v"(ac[0][3]), "a"(fw2[4][2]), "v"(ye[1]), "v"(ze[1]), "v"(ye[2]), "v"(ze[2]), "a"(fw2[4][3]), "v"(ye[3]), "v"(ze[3]) : "memory"); SB();
  WAITVB(12);
  SB();
  asm volatile("v_mfma_f32_16x16x32_bf16 %0, %3, %4, %0\n\tbuffer_load_dwordx4 %1, %5, %6, %7 offen offset:512 sc1\n\tv_mfma_f32_16x16x32_bf16 %2, %3, %8, %2"
      : "+v"(ac[1][0]), "=&v"(pu[4]), "+v"(ac[1][1]) : "v"(pu[5]), "a"(fw2[5][0]), "v"(vF), "s"(rsW), "s"(so0), "a"(fw2[5][1]) : "memory"); SB();
  WAITL(0);
  BAR0();
  SB();
  asm volatile("v_mfma_f32_16x16x32_bf16 %0, %5, %6, %0\n\tds_read_b128 %1, %7 offset:0\n\tds_read_b128 %2, %7 offset:4096\n\tv_mfma_f32_16x16x32_bf16 %3, %5, %8, %3\n\tds_read_b128 %4, %7 offset:8192"
      : "+v"(ac[1][2]), "=&v"(py[2][0]), "=&v"(py[2][1]), "+v"(ac[1][3]), "=&v"(py[2][2]) : "v"(pu[5]), "a"(fw2[5][2]), "v"(rr), "a"(fw2[5][3]) : "memory"); SB();
  WAITVB(12);
  SB();
  asm volatile("v_mfma_f32_16x16x32_bf16 %0, %8, %9, %0\n\tbuffer_load_dwordx4 %1, %10, %11, %12 offen offset:640 sc1\n\tv_mfma_f32_16x16x32_bf16 %2, %8, %13, %2\n\tds_read_b128 %3, %14 offset:12288\n\tds_read_b128 %4, %15 offset:4096\n\tv_mfma_f32_16x16x32_bf16 %5, %8, %16, %5\n\tv_mfma_f32_16x16x32_bf16 %6, %8, %17, %6\n\tds_read_b128 %7, %15 offset:12288"
      : "+v"(ac[1][0]), "=&v"(pu[5]), "+v"(ac[1][1]), "=&v"(py[2][3]), "=&a"(fwo[16]), "+v"(ac[1][2]), "+v"(ac[1][3]), "=&a"(fwo[17]) : "v"(pu[6]), "a"(fw2[6][0]), "v"(vF), "s"(rsW), "s"(so0), "a"(fw2[6][1]), "v"(rr), "v"(ro), "a"(fw2[6][2]), "a"(fw2[6][3]) : "memory"); SB();
  WAITVB(12);
  SB();
  asm volatile("v_mfma_f32_16x16x32_bf16 %0, %5, %6, %0\n\tbuffer_load_dwordx4 %1, %7, %8, %9 offen offset:768 sc1\n\tv_mfma_f32_16x16x32_bf16 %2, %5, %10, %2\n\tds_read_b128 %3, %11 offset:20480\n\tv_mfma_f32_16x16x32_bf16 %4, %5, %12, %4"
      : "+v"(ac[1][0]), "=&v"(pu[6]), "+v"(ac[1][1]), "=&a"(fwo[18]), "+v"(ac[1][2]) : "v"(pu[7]), "a"(fw2[7][0]), "v"(vF), "s"(rsW), "s"(so0), "a"(fw2[7][1]), "v"(ro), "a"(fw2[7][2]) : "memory"); SB();
  WAITL(3);
  asm volatile("v_add_f32 %0, %3, %4\n\tv_mfma_f32_16x16x32_bf16 %1, %5, %6, %1\n\tv_add_f32 %2, %7, %4"
      : "=&v"(ye[0]), "+v"(ac[1][3]), "=&v"(ye[1]) : "v"(py[2][0][0]), "v"(bias), "v"(pu[7]), "a"(fw2[7][3]), "v"(py[2][0][1])); SB();
  WAITVB(12);
  SB();
  asm volatile("v_mfma_f32_16x16x32_bf16 %0, %9, %10, %0\n\tbuffer_load_dwordx4 %1, %11, %12, %13 offen offset:0 sc1\n\tv_mfma_f32_16x16x32_bf16 %2, %9, %14, %2\n\tv_add_f32 %3, %15, %16\n\tv_add_f32 %4, %17, %16\n\tv_mfma_f32_16x16x32_bf16 %5, %9, %18, %5\n\tv_add_f32 %6, %19, %20\n\tv_add_f32 %7, %21, %22\n\tv_mfma_f32_16x16x32_bf16 %8, %9, %23, %8"
      : "+v"(ac[1][0]), "=&v"(pu[7]), "+v"(ac[1][1]), "=&v"(ye[2]), "=&v"(ye[3]), "+v"(ac[1][2]), "=&v"(ze[0]), "=&v"(ze[1]), "+v"(ac[1][3]) : "v"(pu[8]), "a"(fw2[8][0]), "v"(vF), "s"(rsW), "s"(so1), "a"(fw2[8][1]), "v"(py[2][0][2]), "v"(bias), "v"(py[2][0][3]), "a"(fw2[8][2]), "v"(py[2][1][0]), "v"(py[2][2][0]), "v"(py[2][1][1]), "v"(py[2][2][1]), "a"(fw2[8][3]) : "memory"); SB();
  asm volatile("v_add_f32 %0, %1, %2"
      : "=&v"(ze[2]) : "v"(py[2][1][2]), "v"(py[2][2][2])); SB();
  WAITVB(12);
  SB();
  asm volatile("v_mfma_f32_16x16x32_bf16 %0, %9, %10, %0\n\tbuffer_load_dwordx4 %1, %11, %12, %13 offen offset:128 sc1\n\tv_mfma_f32_16x16x32_bf16 %2, %9, %14, %2\n\tv_add_f32 %3, %15, %16\n\tv_add_f32 %4, %4, %17\n\tv_mfma_f32_16x16x32_bf16 %5, %9, %18, %5\n\tv_add_f32 %6, %6, %19\n\tv_add_f32 %7, %7, %20\n\tv_mfma_f32_16x16x32_bf16 %8, %9, %21, %8"
      : "+v"(ac[1][0]), "=&v"(pu[8]), "+v"(ac[1][1]), "=&v"(ze[3]), "+v"(ye[0]), "+v"(ac[1][2]), "+v"(ye[1]), "+v"(ye[2]), "+v"(ac[1][3]) : "v"(pu[9]), "v"(fw2[9][0]), "v"(vF), "s"(rsW), "s"(so1), "v"(fw2[9][1]), "v"(py[2][1][3]), "v"(py[2][2][3]), "v"(py[2][3][0]), "v"(fw2[9][2]), "v"(py[2][3][1]), "v"(py[2][3][2]), "v"(fw2[9][3]) : "memory"); SB();
  asm volatile("v_add_f32 %0, %0, %1"
      : "+v"(ye[3]) : "v"(py[2][3][3])); SB();
  WAITVB(12);
  SB();
  asm volatile("v_mfma_f32_16x16x32_bf16 %0, %10, %11, %0\n\tbuffer_load_dwordx4 %1, %12, %13, %14 offen offset:256 sc1\n\tv_mfma_f32_16x16x32_bf16 %2, %10, %15, %2\n\tv_add_f32 %3, %3, %4\n\tv_add_f32 %5, %5, %16\n\tv_mfma_f32_16x16x32_bf16 %6, %10, %17, %6\n\tv_add_f32 %7, %7, %18\n\tv_add_f32 %8, %8, %19\n\tv_mfma_f32_16x16x32_bf16 %9, %10, %20, %9\n\tv_mul_f32 %4, 0xbfb8aa3b, %3"
      : "+v"(ac[1][0]), "=&v"(pu[9]), "+v"(ac[1][1]), "+v"(ye[0]), "+v"(ze[0]), "+v"(ye[1]), "+v"(ac[1][2]), "+v"(ye[2]), "+v"(ye[3]), "+v"(ac[1][3]) : "v"(pu[10]), "v"(fw2[10][0]), "v"(vF), "s"(rsW), "s"(so1), "v"(fw2[10][1]), "v"(ze[1]), "v"(fw2[10][2]), "v"(ze[2]), "v"(ze[3]), "v"(fw2[10][3]) : "memory"); SB();
  WAITVB(12);
  SB();
  asm volatile("v_mfma_f32_16x16x32_bf16 %0, %9, %10, %0\n\tbuffer_load_dwordx4 %1, %11, %12, %13 offen offset:384 sc1\n\tv_mfma_f32_16x16x32_bf16 %2, %9, %14, %2\n\tv_mul_f32 %3, 0xbfb8aa3b, %15\n\tv_mul_f32 %4, 0xbfb8aa3b, %16\n\tv_mfma_f32_16x16x32_bf16 %5, %9, %17, %5\n\tds_read_b128 %6, %18 offset:28672\n\tv_mul_f32 %7, 0xbfb8aa3b, %19\n\tv_mfma_f32_16x16x32_bf16 %8, %9, %20, %8"
      : "+v"(ac[1][0]), "=&v"(pu[10]), "+v"(ac[1][1]), "=&v"(ze[1]), "=&v"(ze[2]), "+v"(ac[1][2]), "=&a"(fwo[19]), "=&v"(ze[3]), "+v"(ac[1][3]) : "v"(pu[11]), "v"(fw2[11][0]), "v"(vF), "s"(rsW), "s"(so1), "v"(fw2[11][1]), "v"(ye[1]), "v"(ye[2]), "v"(fw2[11][2]), "v"(ro), "v"(ye[3]), "v"(fw2[11][3]) : "memory"); SB();
  WAITVB(12);
  SB();
  asm volatile("v_mfma_f32_16x16x32_bf16 %0, %8, %9, %0\n\tbuffer_load_dwordx4 %1, %10, %11, %12 offen offset:512 sc1\n\tv_mfma_f32_16x16x32_bf16 %2, %8, %13, %2\n\tv_exp_f32 %3, %3\n\tv_mfma_f32_16x16x32_bf16 %4, %8, %14, %4\n\tv_exp_f32 %5, %5\n\tv_mfma_f32_16x16x32_bf16 %6, %8, %15, %6\n\tds_read_b128 %7, %16 offset:5120"
      : "+v"(ac[1][0]), "=&v"(pu[11]), "+v"(ac[1][1]), "+v"(ze[0]), "+v"(ac[1][2]), "+v"(ze[1]), "+v"(ac[1][3]), "=&a"(fwo[20]) : "v"(pu[12]), "v"(fw2[12][0]), "v"(vF), "s"(rsW), "s"(so1), "v"(fw2[12][1]), "v"(fw2[12][2]), "v"(fw2[12][3]), "v"(ro) : "memory"); SB();
  WAITVB(12);
  SB();
  asm volatile("v_mfma_f32_16x16x32_bf16 %0, %9, %10, %0\n\tbuffer_load_dwordx4 %1, %11, %12, %13 offen offset:640 sc1\n\tv_mfma_f32_16x16x32_bf16 %2, %9, %14, %2\n\tv_exp_f32 %3, %3\n\tv_mfma_f32_16x16x32_bf16 %4, %9, %15, %4\n\tv_exp_f32 %5, %5\n\tv_mfma_f32_16x16x32_bf16 %6, %9, %16, %6\n\tv_add_f32 %7, 1.0, %7\n\tv_add_f32 %8, 1.0, %8"
      : "+v"(ac[1][0]), "=&v"(pu[12]), "+v"(ac[1][1]), "+v"(ze[2]), "+v"(ac[1][2]), "+v"(ze[3]), "+v"(ac[1][3]), "+v"(ze[0]), "+v"(ze[1]) : "v"(pu[13]), "v"(fw2[13][0]), "v"(vF), "s"(rsW), "s"(so1), "v"(fw2[13][1]), "v"(fw2[13][2]), "v"(fw2[13][3]) : "memory"); SB();
  STAMP(43);
  WAITVB(12);
  SB();
  asm volatile("v_mfma_f32_16x16x32_bf16 %0, %9, %10, 0\n\tbuffer_load_dwordx4 %1, %11, %12, %13 offen offset:768 sc1\n\tv_mfma_f32_16x16x32_bf16 %2, %14, %10, 0\n\tv_add_f32 %3, 1.0, %3\n\tv_add_f32 %4, 1.0, %4\n\tv_mfma_f32_16x16x32_bf16 %5, %15, %10, 0\n\tv_rcp_f32 %6, %6\n\tv_mfma_f32_16x16x32_bf16 %7, %16, %10, 0\n\tds_read_b128 %8, %17 offset:13312"
      : "=&v"(as[0]), "=&v"(pu[13]), "=&v"(as[1]), "+v"(ze[2]), "+v"(ze[3]), "=&v"(as[2]), "+v"(ze[0]), "=&v"(as[3]), "=&a"(fwo[21]) : "a"(fwo[0]), "v"(pu[0]), "v"(vF), "s"(rsW), "s"(so1), "a"(fwo[1]), "a"(fwo[2]), "a"(fwo[3]), "v"(ro) : "memory"); SB();
  WAITVB(12);
  SB();
  asm volatile("v_mfma_f32_16x16x32_bf16 %0, %7, %8, %0\n\tds_write_b128 %9, %10 offset:18432\n\tv_mfma_f32_16x16x32_bf16 %1, %11, %8, %1\n\tv_rcp_f32 %2, %2\n\tv_mfma_f32_16x16x32_bf16 %3, %12, %8, %3\n\tv_rcp_f32 %4, %4\n\tv_mfma_f32_16x16x32_bf16 %5, %13, %8, %5\n\tds_read_b128 %6, %14 offset:21504"
      : "+v"(as[0]), "+v"(as[1]), "+v"(ze[1]), "+v"(as[2]), "+v"(ze[2]), "+v"(as[3]), "=&a"(fwo[22]) : "a"(fwo[4]), "v"(pu[1]), "v"(rwd), "v"(ac[1][0]), "a"(fwo[5]), "a"(fwo[6]), "a"(fwo[7]), "v"(ro) : "memory"); SB();
  WAITVB(11);
  SB();
  asm volatile("v_mfma_f32_16x16x32_bf16 %0, %8, %9, %0\n\tds_write_b128 %10, %11 offset:19456\n\tv_mfma_f32_16x16x32_bf16 %1, %12, %9, %1\n\tv_rcp_f32 %2, %2\n\tv_mfma_f32_16x16x32_bf16 %3, %13, %9, %3\n\tv_mul_f32 %4, %14, %15\n\tv_mul_f32 %5, %16, %17\n\tv_mfma_f32_16x16x32_bf16 %6, %18, %9, %6\n\tv_mul_f32 %7, %19, %20"
      : "+v"(as[0]), "+v"(as[1]), "+v"(ze[3]), "+v"(as[2]), "=&v"(ge[8]), "=&v"(ge[9]), "+v"(as[3]), "=&v"(ge[10]) : "a"(fwo[8]), "v"(pu[2]), "v"(rwd), "v"(ac[1][1]), "a"(fwo[9]), "a"(fwo[10]), "v"(ye[0]), "v"(ze[0]), "v"(ye[1]), "v"(ze[1]), "a"(fwo[11]), "v"(ye[2]), "v"(ze[2]) : "memory"); SB();
  WAITVB(10);
  SB();
  asm volatile("v_mfma_f32_16x16x32_bf16 %0, %6, %7, %0\n\tds_write_b128 %8, %9 offset:20480\n\tv_mfma_f32_16x16x32_bf16 %1, %10, %7, %1\n\tds_read_b128 %2, %11 offset:29696\n\tv_mul_f32 %3, %12, %13\n\tv_mfma_f32_16x16x32_bf16 %4, %14, %7, %4\n\tv_mfma_f32_16x16x32_bf16 %5, %15, %7, %5"
      : "+v"(as[0]), "+v"(as[1]), "=&a"(fwo[23]), "=&v"(ge[11]), "+v"(as[2]), "+v"(as[3]) : "a"(fwo[12]), "v"(pu[3]), "v"(rwd), "v"(ac[1][2]), "a"(fwo[13]), "v"(ro), "v"(ye[3]), "v"(ze[3]), "a"(fwo[14]), "a"(fwo[15]) : "memory"); SB();
  WAITVB(9);
  asm volatile("ds_read_b128 %0, %6 offset:6144\n\tds_read_b128 %1, %6 offset:14336\n\tds_read_b128 %2, %6 offset:22528\n\tds_read_b128 %3, %6 offset:30720\n\tds_read_b128 %4, %6 offset:7168\n\tds_read_b128 %5, %6 offset:15360"
      : "=&a"(fwo[24]), "=&a"(fwo[25]), "=&a"(fwo[26]), "=&a"(fwo[27]), "=&a"(fwo[28]), "=&a"(fwo[29]) : "v"(ro) : "memory"); SB();
  WAITL(6);
  SB();
  asm volatile("v_mfma_f32_16x16x32_bf16 %0, %6, %7, %0\n\tds_write_b128 %8, %9 offset:21504\n\tv_mfma_f32_16x16x32_bf16 %1, %10, %7, %1\n\tds_read_b128 %2, %11 offset:23552\n\tv_mfma_f32_16x16x32_bf16 %3, %12, %7, %3\n\tv_mfma_f32_16x16x32_bf16 %4, %13, %7, %4\n\tds_read_b128 %5, %11 offset:31744"
      : "+v"(as[0]), "+v"(as[1]), "=&a"(fwo[30]), "+v"(as[2]), "+v"(as[3]), "=&a"(fwo[31]) : "a"(fwo[16]), "v"(pu[4]), "v"(rwd), "v"(ac[1][3]), "a"(fwo[17]), "v"(ro), "a"(fwo[18]), "a"(fwo[19]) : "memory"); SB();
  WAITVB(8);
  SB();
  asm volatile("v_mfma_f32_16x16x32_bf16 %0, %1, %2, %0"
      : "+v"(as[0]) : "a"(fwo[20]), "v"(pu[5])); SB();
  WAITL(2);
  BAR0();
  SB();
  asm volatile("v_mfma_f32_16x16x32_bf16 %0, %7, %8, %0\n\tds_read_b128 %1, %9 offset:18432\n\tds_read_b128 %2, %9 offset:22528\n\tv_mfma_f32_16x16x32_bf16 %3, %10, %8, %3\n\tds_read_b128 %4, %9 offset:26624\n\tds_read_b128 %5, %9 offset:30720\n\tv_mfma_f32_16x16x32_bf16 %6, %11, %8, %6"
      : "+v"(as[1]), "=&v"(py[3][0]), "=&v"(py[3][1]), "+v"(as[2]), "=&v"(py[3][2]), "=&v"(py[3][3]), "+v"(as[3]) : "a"(fwo[21]), "v"(pu[5]), "v"(rr), "a"(fwo[22]), "a"(fwo[23]) : "memory"); SB();
  WAITVB(7);
  asm volatile("ds_read_b128 %0, %6 offset:32768\n\tds_read_b128 %1, %6 offset:38912\n\tds_read_b128 %2, %6 offset:45056\n\tds_read_b128 %3, %6 offset:51200\n\tds_read_b128 %4, %6 offset:33792\n\tds_read_b128 %5, %6 offset:39936"
      : "=&a"(fwo[32]), "=&a"(fwo[33]), "=&a"(fwo[34]), "=&a"(fwo[35]), "=&a"(fwo[36]), "=&a"(fwo[37]) : "v"(ro) : "memory"); SB();
  WAITL(10);
  SB();
  asm volatile("v_mfma_f32_16x16x32_bf16 %0, %6, %7, %0\n\tds_read_b128 %1, %8 offset:46080\n\tv_mfma_f32_16x16x32_bf16 %2, %9, %7, %2\n\tv_mfma_f32_16x16x32_bf16 %3, %10, %7, %3\n\tds_read_b128 %4, %8 offset:52224\n\tv_mfma_f32_16x16x32_bf16 %5, %11, %7, %5"
      : "+v"(as[0]), "=&a"(fwo[38]), "+v"(as[1]), "+v"(as[2]), "=&a"(fwo[39]), "+v"(as[3]) : "a"(fwo[24]), "v"(pu[6]), "v"(ro), "a"(fwo[25]), "a"(fwo[26]), "a"(fwo[27]) : "memory"); SB();
  WAITL(8);
  SB();
  WAITVB(6);
  SB();
  asm volatile("v_mfma_f32_16x16x32_bf16 %0, %10, %11, %0\n\tv_add_f32 %1, %12, %13\n\tv_add_f32 %2, %14, %13\n\tv_mfma_f32_16x16x32_bf16 %3, %15, %11, %3\n\tv_add_f32 %4, %16, %13\n\tv_add_f32 %5, %17, %13\n\tv_mfma_f32_16x16x32_bf16 %6, %18, %11, %6\n\tv_add_f32 %7, %19, %20\n\tv_add_f32 %8, %21, %22\n\tv_mfma_f32_16x16x32_bf16 %9, %23, %11, %9"
      : "+v"(as[0]), "=&v"(ye[0]), "=&v"(ye[1]), "+v"(as[1]), "=&v"(ye[2]), "=&v"(ye[3]), "+v"(as[2]), "=&v"(ze[0]), "=&v"(ze[1]), "+v"(as[3]) : "a"(fwo[28]), "v"(pu[7]), "v"(py[3][0][0]), "v"(bias), "v"(py[3][0][1]), "a"(fwo[29]), "v"(py[3][0][2]), "v"(py[3][0][3]), "a"(fwo[30]), "v"(py[3][1][0]), "v"(py[3][2][0]), "v"(py[3][1][1]), "v"(py[3][2][1]), "a"(fwo[31])); SB();
  WAITVB(5);
  asm volatile("ds_read_b128 %0, %6 offset:34816\n\tds_read_b128 %1, %6 offset:40960\n\tds_read_b128 %2, %6 offset:47104\n\tds_read_b128 %3, %6 offset:53248\n\tds_read_b128 %4, %6 offset:35840\n\tds_read_b128 %5, %6 offset:41984"
      : "=&a"(fwo[40]), "=&a"(fwo[41]), "=&a"(fwo[42]), "=&a"(fwo[43]), "=&a"(fwo[44]), "=&a"(fwo[45]) : "v"(ro) : "memory"); SB();
  WAITL(6);
  SB();
  asm volatile("v_mfma_f32_16x16x32_bf16 %0, %9, %10, %0\n\tv_add_f32 %1, %11, %12\n\tv_add_f32 %2, %13, %14\n\tv_mfma_f32_16x16x32_bf16 %3, %15, %10, %3\n\tv_add_f32 %4, %4, %16\n\tv_add_f32 %5, %5, %17\n\tv_mfma_f32_16x16x32_bf16 %6, %18, %10, %6\n\tv_add_f32 %7, %7, %19\n\tv_add_f32 %8, %8, %20"
      : "+v"(as[0]), "=&v"(ze[2]), "=&v"(ze[3]), "+v"(as[1]), "+v"(ye[0]), "+v"(ye[1]), "+v"(as[2]), "+v"(ye[2]), "+v"(ye[3]) : "a"(fwo[32]), "v"(pu[8]), "v"(py[3][1][2]), "v"(py[3][2][2]), "v"(py[3][1][3]), "v"(py[3][2][3]), "a"(fwo[33]), "v"(py[3][3][0]), "v"(py[3][3][1]), "a"(fwo[34]), "v"(py[3][3][2]), "v"(py[3][3][3])); SB();
  asm volatile("v_mfma_f32_16x16x32_bf16 %0, %2, %3, %0\n\tv_add_f32 %1, %1, %4"
      : "+v"(as[3]), "+v"(ye[0]) : "a"(fwo[35]), "v"(pu[8]), "v"(ze[0])); SB();
  WAITVB(4);
  SB();
  asm volatile("v_mfma_f32_16x16x32_bf16 %0, %10, %11, %0\n\tv_add_f32 %1, %1, %2\n\tv_add_f32 %3, %3, %4\n\tv_mfma_f32_16x16x32_bf16 %5, %12, %11, %5\n\tv_add_f32 %6, %6, %13\n\tv_mul_f32 %7, 0xbfb8aa3b, %14\n\tv_mfma_f32_16x16x32_bf16 %8, %15, %11, %8\n\tv_mul_f32 %2, 0xbfb8aa3b, %1\n\tv_mul_f32 %4, 0xbfb8aa3b, %3\n\tv_mfma_f32_16x16x32_bf16 %9, %16, %11, %9"
      : "+v"(as[0]), "+v"(ye[1]), "+v"(ze[1]), "+v"(ye[2]), "+v"(ze[2]), "+v"(as[1]), "+v"(ye[3]), "=&v"(ze[0]), "+v"(as[2]), "+v"(as[3]) : "a"(fwo[36]), "v"(pu[9]), "a"(fwo[37]), "v"(ze[3]), "v"(ye[0]), "a"(fwo[38]), "a"(fwo[39])); SB();
  WAITVB(3);
  asm volatile("ds_read_b128 %0, %8 offset:48128\n\tds_read_b128 %1, %8 offset:54272\n\tds_read_b128 %2, %8 offset:36864\n\tds_read_b128 %3, %8 offset:43008\n\tds_read_b128 %4, %8 offset:49152\n\tds_read_b128 %5, %8 offset:55296\n\tds_read_b128 %6, %8 offset:37888\n\tds_read_b128 %7, %8 offset:44032"
      : "=&a"(fwo[46]), "=&a"(fwo[47]), "=&a"(fwo[48]), "=&a"(fwo[49]), "=&a"(fwo[50]), "=&a"(fwo[51]), "=&a"(fwo[52]), "=&a"(fwo[53]) : "v"(ro) : "memory"); SB();
  WAITL(6);
  SB();
  asm volatile("v_mfma_f32_16x16x32_bf16 %0, %9, %10, %0\n\tds_read_b128 %1, %11 offset:50176\n\tv_mul_f32 %2, 0xbfb8aa3b, %12\n\tv_mfma_f32_16x16x32_bf16 %3, %13, %10, %3\n\tv_exp_f32 %4, %4\n\tv_mfma_f32_16x16x32_bf16 %5, %14, %10, %5\n\tv_exp_f32 %6, %6\n\tv_mfma_f32_16x16x32_bf16 %7, %15, %10, %7\n\tds_read_b128 %8, %11 offset:56320"
      : "+v"(as[0]), "=&a"(fwo[54]), "=&v"(ze[3]), "+v"(as[1]), "+v"(ze[0]), "+v"(as[2]), "+v"(ze[1]), "+v"(as[3]), "=&a"(fwo[55]) : "a"(fwo[40]), "v"(pu[10]), "v"(ro), "v"(ye[3]), "a"(fwo[41]), "a"(fwo[42]), "a"(fwo[43]) : "memory"); SB();
  WAITL(0);
  WAITVB(0);
  SB();
  asm volatile("v_mfma_f32_16x16x32_bf16 %0, %9, %10, %0\n\tv_exp_f32 %1, %1\n\tv_mfma_f32_16x16x32_bf16 %0, %11, %12, %0\n\tv_exp_f32 %2, %2\n\tv_mfma_f32_16x16x32_bf16 %0, %13, %14, %0\n\tv_add_f32 %3, 1.0, %3\n\tv_add_f32 %4, 1.0, %4\n\tv_mfma_f32_16x16x32_bf16 %5, %15, %10, %5\n\tv_add_f32 %1, 1.0, %1\n\tv_add_f32 %2, 1.0, %2\n\tv_mfma_f32_16x16x32_bf16 %5, %16, %12, %5\n\tv_rcp_f32 %3, %3\n\tv_mfma_f32_16x16x32_bf16 %5, %17, %14, %5\n\tv_rcp_f32 %4, %4\n\tv_mfma_f32_16x16x32_bf16 %6, %18, %10, %6\n\tv_rcp_f32 %1, %1\n\tv_mfma_f32_16x16x32_bf16 %6, %19, %12, %6\n\tv_rcp_f32 %2, %2\n\tv_mfma_f32_16x16x32_bf16 %6, %20, %14, %6\n\tv_mul_f32 %7, %21, %3\n\tv_mul_f32 %8, %22, %4"
      : "+v"(as[0]), "+v"(ze[2]), "+v"(ze[3]), "+v"(ze[0]), "+v"(ze[1]), "+v"(as[1]), "+v"(as[2]), "=&v"(ge[12]), "=&v"(ge[13]) : "a"(fwo[44]), "v"(pu[11]), "a"(fwo[48]), "v"(pu[12]), "a"(fwo[52]), "v"(pu[13]), "a"(fwo[45]), "a"(fwo[49]), "a"(fwo[53]), "a"(fwo[46]), "a"(fwo[50]), "a"(fwo[54]), "v"(ye[0]), "v"(ye[1])); SB();
  asm volatile("v_mfma_f32_16x16x32_bf16 %0, %7, %8, %0\n\tv_mul_f32 %1, %9, %10\n\tv_mul_f32 %2, %11, %12\n\tv_mfma_f32_16x16x32_bf16 %0, %13, %14, %0\n\tv_fma_f32 %3, %15, %16, %3\n\tv_fma_f32 %4, %17, %18, %4\n\tv_mfma_f32_16x16x32_bf16 %0, %19, %20, %0\n\tv_fma_f32 %5, %21, %22, %5\n\tv_fma_f32 %6, %23, %24, %6"
      : "+v"(as[3]), "=&v"(ge[14]), "=&v"(ge[15]), "+v"(xe[0]), "+v"(xe[1]), "+v"(xe[2]), "+v"(xe[3]) : "a"(fwo[47]), "v"(pu[11]), "v"(ye[2]), "v"(ze[2]), "v"(ye[3]), "v"(ze[3]), "a"(fwo[51]), "v"(pu[12]), "v"(as[0][0]), "v"(ge[0]), "v"(as[0][1]), "v"(ge[1]), "a"(fwo[55]), "v"(pu[13]), "v"(as[0][2]), "v"(ge[2]), "v"(as[0][3]), "v"(ge[3])); SB();
  STAMP(44);
  asm volatile("v_cvt_pk_bf16_f32 %0, %8, %9\n\tv_cvt_pk_bf16_f32 %1, %10, %11\n\tv_fma_f32 %2, %12, %13, %2\n\tv_fma_f32 %3, %14, %15, %3\n\tv_fma_f32 %4, %16, %17, %4\n\tv_fma_f32 %5, %18, %19, %5\n\tv_cvt_pk_bf16_f32 %6, %2, %3\n\tv_cvt_pk_bf16_f32 %7, %4, %5"
      : "=&v"(ov[0][0]), "=&v"(ov[0][1]), "+v"(xe[4]), "+v"(xe[5]), "+v"(xe[6]), "+v"(xe[7]), "=&v"(ov[1][0]), "=&v"(ov[1][1]) : "v"(xe[0]), "v"(xe[1]), "v"(xe[2]), "v"(xe[3]), "v"(as[1][0]), "v"(ge[4]), "v"(as[1][1]), "v"(ge[5]), "v"(as[1][2]), "v"(ge[6]), "v"(as[1][3]), "v"(ge[7])); SB();
  asm volatile("ds_write_b64 %8, %9 offset:0\n\tv_fma_f32 %0, %10, %11, %0\n\tv_fma_f32 %1, %12, %13, %1\n\tv_fma_f32 %2, %14, %15, %2\n\tv_fma_f32 %3, %16, %17, %3\n\tv_cvt_pk_bf16_f32 %4, %0, %1\n\tv_cvt_pk_bf16_f32 %5, %2, %3\n\tds_write_b64 %18, %19 offset:0\n\tv_fma_f32 %6, %20, %21, %6\n\tv_fma_f32 %7, %22, %23, %7"
      : "+v"(xe[8]), "+v"(xe[9]), "+v"(xe[10]), "+v"(xe[11]), "=&v"(ov[2][0]), "=&v"(ov[2][1]), "+v"(xe[12]), "+v"(xe[13]) : "v"(xta[0]), "v"(ov[0]), "v"(as[2][0]), "v"(ge[8]), "v"(as[2][1]), "v"(ge[9]), "v"(as[2][2]), "v"(ge[10]), "v"(as[2][3]), "v"(ge[11]), "v"(xta[1]), "v"(ov[1]), "v"(as[3][0]), "v"(ge[12]), "v"(as[3][1]), "v"(ge[13]) : "memory"); SB();
  asm volatile("v_fma_f32 %0, %4, %5, %0\n\tv_fma_f32 %1, %6, %7, %1\n\tv_cvt_pk_bf16_f32 %2, %8, %9\n\tv_cvt_pk_bf16_f32 %3, %0, %1\n\tds_write_b64 %10, %11 offset:0"
      : "+v"(xe[14]), "+v"(xe[15]), "=&v"(ov[3][0]), "=&v"(ov[3][1]) : "v"(as[3][2]), "v"(ge[14]), "v"(as[3][3]), "v"(ge[15]), "v"(xe[12]), "v"(xe[13]), "v"(xta[2]), "v"(ov[2]) : "memory"); SB();
  asm volatile("ds_write_b64 %0, %1 offset:0"
      :  : "v"(xta[3]), "v"(ov[3]) : "memory"); SB();
  { u32x4 t0_, t1_; DSR16(t0_, xo, 0); DSR16(t1_, xo, 1024); WAITLB(1);
    BS16(vXa, rsO_, 0, t0_, "sc1"); WAITLB(0); BS16(vXb, rsO_, 1024, t1_, "sc1"); }
  STAMP(23);

  STAMP(12);
  STAMP_DUMP();
  }
  } else if (jb < 28) {
  asm volatile("; NEED B");
  u32x4 rs1 = srd(w1, 2048u * 3584u);
  STAMP(1);
  unsigned nonce0; asm volatile("s_load_dword %0, %1, 0x0" : "=&s"(nonce0) : "s"(ws) : "memory");
  const unsigned trb = (8 * g4 + q4) * 128 + pq * 8, trf = ((q4 >> 1) | ((g4 & 1) << 1)) * 32;
  const unsigned vF = r8 * 1024 + (lane & 7) * 16;
  unsigned tX[4];
#pragma unroll
  for (int i = 0; i < 4; ++i) { tX[i] = ldsR + trb + ((i * 32 ^ w * 32) ^ trf); OPV(tX[i]); }
  bf16x8 fw1[16][4], p[14]; u32x2 bv; unsigned sh[16], sh0 = WS_HT + d4 * 524288; OPQ(sh0);
  unsigned nonce; asm volatile("s_waitcnt lgkmcnt(0)\n\ts_add_u32 %0, %1, 1" : "=s"(nonce) : "s"(nonce0) : "memory", "scc");
  asm volatile("s_waitcnt vmcnt(8)"
      :  :  : "memory"); SB();
  fw1[0][0] = tr8u(tX[0] + 0, 512);
  fw1[0][1] = tr8u(tX[1] + 0, 512);
  fw1[0][2] = tr8u(tX[2] + 0, 512);
  fw1[0][3] = tr8u(tX[3] + 0, 512);
  STAMP(50);
  { int k_ = KS1(3); OPQ(k_); DMA4(vW1a, vW1b, rs1, k_ * 114688, 55296, ldsR, 12288); sh[3] = sh0 + k_ * 8192; OPQ(sh[3]); }
  SB();
  { const unsigned vB = (ft * 64 + w * 16 + g4 * 4) * 2;
    LD8(bv, vB, b1, 0); }
  u32x4 rsW = srd((char*)(((size_t)ws + 8191) & ~(size_t)4095) + 512, WS_END); OPQ(rsW);
  #pragma unroll
  for (int c = 0; c < 3; ++c) { sh[c] = sh0 + KS1(c) * 8192; OPQ(sh[c]); }
  unsigned fh, vFH = WS_FA + (d4 * 128 + w * 32 + (lane & 31)) * (4 * 16);
  unsigned rw = lds0 + w * SCW + vL16, rr1 = lds0 + (w ^ 1) * SCW + vL16, rr2 = lds0 + (w ^ 2) * SCW + vL16, rr3 = lds0 + (w ^ 3) * SCW + vL16;
  unsigned vU = (g4 & 1) * 128 + ((g4 >> 1) * 2 + (n16 >> 3)) * 1024 + (n16 & 7) * 16, sU, sFU = WS_FU + (d4 * 128 + ft * 4 + w) * (4 * 4);
  sU = WS_UT + (d4 * 56 + 2 * ft) * 8192 + w * 4096;
  const int sp1 = (fi & 1);
  unsigned vHa = vF + (d4 == 1 ? 128 : 0) + 128 * sp1, vHc = vF + (d4 == 1 ? 128 : 0) + 128 - 128 * sp1, vHb = vF + (d4 == 1 ? 0 : 768), vU6 = vU + (d4 == 1 ? 0 : 768); vU = (vU ^ (128 * sp1)) + (d4 == 1 ? 128 : 0); OPV(vHa); OPV(vHc); OPV(vHb); OPV(vU6);
  float gc = 0.10294324f;
  asm volatile("" : "+v"(vFH), "+v"(rw), "+v"(rr1), "+v"(rr2), "+v"(rr3), "+v"(vU), "+v"(gc)); asm volatile("" : "+s"(sU), "+s"(sFU));
  const int r2 = (3 - (int)((size_t)w2 >> 7)) & 7;
  const int st = 8 * xq + ((r2 + 1 + fi) & 7), dt = d4, db0 = 16 + 3 * dt;
#undef BLK
#define BLK(j) (db0 + ((j) == 2 ? 2 - 2 * (dt & 1) : ((j) ^ 0) + (dt & 1)))
  SB();
  asm volatile("s_waitcnt vmcnt(9)"
      :  :  : "memory"); SB();
  fw1[1][0] = tr8u(tX[0] + 4096, 512);
  fw1[1][1] = tr8u(tX[1] + 4096, 512);
  fw1[1][2] = tr8u(tX[2] + 4096, 512);
  fw1[1][3] = tr8u(tX[3] + 4096, 512);
  { int k_ = KS1(4); OPQ(k_); DMA4(vW1a, vW1b, rs1, k_ * 114688, 55296, ldsR, 16384); sh[4] = sh0 + k_ * 8192; OPQ(sh[4]); }
  SB();
  u32x4 rs2 = srd(w2, 1792u * 4096u); OPQ(rs2);
  unsigned vW2[4];
#pragma unroll
  for (int i = 0; i < 4; ++i) vW2[i] = (8 * (r8 & 3) + 4 * (r8 >> 2)) * 4096 + st * 128 + ((lane & 7) ^ (2 * i)) * 16 + i * (4096 - 1024);
  int rot2 = 4 * dt + 2 * (st & 7) + 2; rot2 -= 14 * ((rot2 * 37) >> 9);
  bf16x8 fw2[14][4];
  unsigned ldsS = lds0 + S3B + w * 4096, ldsO = lds0 + WOB + w * 8192, ldsP = lds0 + WOB + 24576 + w * 6144, sWo, vWo = r8 * 16384 + (lane & 7) * 16, sWt;
  { const int k_ = BLK(w), k7 = (k_ * 37) >> 8; sWo = WS_WO + k7 * (131072 - 896) + k_ * 128; }
  unsigned fq, vFQ = WS_FQ + (lane < 56 ? lane : 55) * (4 * 4);
  asm volatile("" : "+v"(vW2[0]), "+v"(vW2[1]), "+v"(vW2[2]), "+v"(vW2[3]), "+v"(vFQ), "+v"(vWo)); asm volatile("" : "+s"(rot2), "+s"(ldsS), "+s"(ldsO), "+s"(ldsP), "+s"(sWo));
  if (w == 3) vWo = 0x40000000u; OPV(vWo);
  unsigned tS[4];
#pragma unroll
  for (int i = 0; i < 4; ++i) { tS[i] = ldsS + q4 * 1024 + g4 * 128 + pq * 8 + ((i ^ q4) * 32); OPV(tS[i]); }
  unsigned tO[4];
#pragma unroll
  for (int i = 0; i < 4; ++i) { tO[i] = ldsO + q4 * 1024 + g4 * 128 + pq * 8 + ((i ^ q4) * 32); OPV(tO[i]); }
  unsigned su[14], sw[14], sc_ = 14 * w + rot2; OPQ(sc_);
  SB();
  asm volatile("s_waitcnt vmcnt(9)"
      :  :  : "memory"); SB();
  fw1[2][0] = tr8u(tX[0] + 8192, 512);
  fw1[2][1] = tr8u(tX[1] + 8192, 512);
  fw1[2][2] = tr8u(tX[2] + 8192, 512);
  fw1[2][3] = tr8u(tX[3] + 8192, 512);
  { int k_ = KS1(5); OPQ(k_); DMA4(vW1a, vW1b, rs1, k_ * 114688, 55296, ldsR, 20480); sh[5] = sh0 + k_ * 8192; OPQ(sh[5]); }
  SB();
  unsigned vFU, vFO;
  { const int da = (db0 * 37) >> 8, db = ((db0 + 2) * 37) >> 8, l_ = lane < 56 ? lane : 55;
    vFU = WS_FU + ((l_ < 28 ? da * 128 + l_ : db * 128 + l_ - 28) + w * 28) * (4 * 4);
    vFO = WS_FO + ((2 * st + (w >> 1)) * 8 + (lane & 7)) * (4 * 16);
    asm volatile("" : "+v"(vFU), "+v"(vFO)); }
  unsigned vFU1 = WS_FU + (512 + ((db0 * 37) >> 8) * 128 + w * 28 + (lane < 28 ? lane : 27)) * (4 * 4), fu2; OPV(vFU1);
  unsigned vFj[4], so0 = WS_OT + (4 * st + w) * 16384, so1 = so0 + 8192;
#pragma unroll
  for (int j = 0; j < 3; ++j) { const int k_ = BLK(j), k7 = (k_ * 37) >> 8; vFj[j] = vF + (WS_UT + k7 * (56 * 8192 - 896) + k_ * 128); OPV(vFj[j]); }
  asm volatile("" : "+s"(so0), "+s"(so1));
  SB();
  asm volatile("s_waitcnt vmcnt(9)"
      :  :  : "memory"); SB();
  fw1[3][0] = tr8u(tX[0] + 12288, 512);
  fw1[3][1] = tr8u(tX[1] + 12288, 512);
  fw1[3][2] = tr8u(tX[2] + 12288, 512);
  fw1[3][3] = tr8u(tX[3] + 12288, 512);
  STAMP(51);
  { int k_ = KS1(6); OPQ(k_); DMA4(vW1a, vW1b, rs1, k_ * 114688, 55296, ldsR, 24576); sh[6] = sh0 + k_ * 8192; OPQ(sh[6]); }
  SB();
  const int tau = 2 * (n16 & 7) + (n16 >> 3);
  unsigned rwd = ldsS + vL16, rr = lds0 + S3B + w * 1024 + (16 * g4 + tau) * 16, ro = lds0 + WOB + vL16;
  unsigned xo = ldsS + 34816 + vL16, braw, xt = ldsS + 34816 + tau * 128 + (((g4 >> 1) ^ (n16 & 7)) * 16) + (g4 & 1) * 8, vB2 = (64 * st + 16 * w + tau) * 2;
  unsigned xs = xt ^ (32 * 0);
  asm volatile("" : "+v"(rwd), "+v"(rr), "+v"(ro), "+v"(xt), "+v"(xs), "+v"(xo), "+v"(vB2));
  SB();
  asm volatile("s_waitcnt vmcnt(8)"
      :  :  : "memory"); SB();
  fw1[4][0] = tr8u(tX[0] + 16384, 512);
  fw1[4][1] = tr8u(tX[1] + 16384, 512);
  fw1[4][2] = tr8u(tX[2] + 16384, 512);
  fw1[4][3] = tr8u(tX[3] + 16384, 512);
  WAITL(8);
  { int k_ = KS1(7); OPQ(k_); DMA4(vW1a, vW1b, rs1, k_ * 114688, 55296, ldsR, 0); sh[7] = sh0 + k_ * 8192; OPQ(sh[7]); }
  SB();
  unsigned vXa, vXb;
  { const int ga = (lane & 7) ^ (r8 >> 1), gb = ga ^ 4, r_ = (64 * st + 16 * w + r8) * 896;
    vXa = ga < 4 ? r_ + (db0 + (dt & 1)) * 32 + ga * 16 : ga < 6 ? r_ + BLK(2) * 32 + (ga - 4) * 16 : 0x40000000;
    vXb = (gb < 4 ? r_ + (db0 + (dt & 1)) * 32 + gb * 16 : gb < 6 ? r_ + BLK(2) * 32 + (gb - 4) * 16 : 0x40000000) + (8 * 896 - 1024); OPV(vXa); OPV(vXb); }
  SB();
  asm volatile("s_waitcnt vmcnt(8)"
      :  :  : "memory"); SB();
  fw1[5][0] = tr8u(tX[0] + 20480, 512);
  fw1[5][1] = tr8u(tX[1] + 20480, 512);
  fw1[5][2] = tr8u(tX[2] + 20480, 512);
  fw1[5][3] = tr8u(tX[3] + 20480, 512);
  WAITL(8);
  { int k_ = KS1(8); OPQ(k_); DMA4(vW1a, vW1b, rs1, k_ * 114688, 55296, ldsR, 4096); sh[8] = sh0 + k_ * 8192; OPQ(sh[8]); }
  SB();
  { unsigned k_ = sc_ + (rot2 >= 14 ? -14 : 0); OPQ(k_); su[0] = k_ << 13; sw[0] = k_ << 17; OPQ(su[0]); OPQ(sw[0]); }
  { unsigned k_ = sc_ + (rot2 >= 13 ? -13 : 1); OPQ(k_); su[1] = k_ << 13; sw[1] = k_ << 17; OPQ(su[1]); OPQ(sw[1]); }
  { unsigned k_ = sc_ + (rot2 >= 12 ? -12 : 2); OPQ(k_); su[2] = k_ << 13; sw[2] = k_ << 17; OPQ(su[2]); OPQ(sw[2]); }
  SB();
  asm volatile("s_waitcnt vmcnt(8)"
      :  :  : "memory"); SB();
  fw1[6][0] = tr8u(tX[0] + 24576, 512);
  fw1[6][1] = tr8u(tX[1] + 24576, 512);
  fw1[6][2] = tr8u(tX[2] + 24576, 512);
  fw1[6][3] = tr8u(tX[3] + 24576, 512);
  WAITV1(20, bv);
  f32x4 bb = {lo2f(bv[0]), hi2f(bv[0]), lo2f(bv[1]), hi2f(bv[1])}; OPV(bb);
  WAITL(8);
  { int k_ = KS1(9); OPQ(k_); DMA4(vW1a, vW1b, rs1, k_ * 114688, 55296, ldsR, 8192); sh[9] = sh0 + k_ * 8192; OPQ(sh[9]); }
  SB();
  { unsigned k_ = sc_ + (rot2 >= 11 ? -11 : 3); OPQ(k_); su[3] = k_ << 13; sw[3] = k_ << 17; OPQ(su[3]); OPQ(sw[3]); }
  { unsigned k_ = sc_ + (rot2 >= 10 ? -10 : 4); OPQ(k_); su[4] = k_ << 13; sw[4] = k_ << 17; OPQ(su[4]); OPQ(sw[4]); }
  { unsigned k_ = sc_ + (rot2 >= 9 ? -9 : 5); OPQ(k_); su[5] = k_ << 13; sw[5] = k_ << 17; OPQ(su[5]); OPQ(sw[5]); }
  SB();
  asm volatile("s_waitcnt vmcnt(8)"
      :  :  : "memory"); SB();
  fw1[7][0] = tr8u(tX[0] + 0, 512);
  fw1[7][1] = tr8u(tX[1] + 0, 512);
  fw1[7][2] = tr8u(tX[2] + 0, 512);
  fw1[7][3] = tr8u(tX[3] + 0, 512);
  STAMP(52);
  WAITL(8);
  { int k_ = KS1(10); OPQ(k_); DMA4(vW1a, vW1b, rs1, k_ * 114688, 55296, ldsR, 12288); sh[10] = sh0 + k_ * 8192; OPQ(sh[10]); }
  SB();
  { unsigned k_ = sc_ + (rot2 >= 8 ? -8 : 6); OPQ(k_); su[6] = k_ << 13; sw[6] = k_ << 17; OPQ(su[6]); OPQ(sw[6]); }
  { unsigned k_ = sc_ + (rot2 >= 7 ? -7 : 7); OPQ(k_); su[7] = k_ << 13; sw[7] = k_ << 17; OPQ(su[7]); OPQ(sw[7]); }
  { unsigned k_ = sc_ + (rot2 >= 6 ? -6 : 8); OPQ(k_); su[8] = k_ << 13; sw[8] = k_ << 17; OPQ(su[8]); OPQ(sw[8]); }
  SB();
  asm volatile("s_waitcnt vmcnt(8)"
      :  :  : "memory"); SB();
  fw1[8][0] = tr8u(tX[0] + 4096, 512);
  fw1[8][1] = tr8u(tX[1] + 4096, 512);
  fw1[8][2] = tr8u(tX[2] + 4096, 512);
  fw1[8][3] = tr8u(tX[3] + 4096, 512);
  WAITL(8);
  { int k_ = KS1(11); OPQ(k_); DMA4(vW1a, vW1b, rs1, k_ * 114688, 55296, ldsR, 16384); sh[11] = sh0 + k_ * 8192; OPQ(sh[11]); }
  SB();
  { unsigned k_ = sc_ + (rot2 >= 5 ? -5 : 9); OPQ(k_); su[9] = k_ << 13; sw[9] = k_ << 17; OPQ(su[9]); OPQ(sw[9]); }
  { unsigned k_ = sc_ + (rot2 >= 4 ? -4 : 10); OPQ(k_); su[10] = k_ << 13; sw[10] = k_ << 17; OPQ(su[10]); OPQ(sw[10]); }
  { unsigned k_ = sc_ + (rot2 >= 3 ? -3 : 11); OPQ(k_); su[11] = k_ << 13; sw[11] = k_ << 17; OPQ(su[11]); OPQ(sw[11]); }
  SB();
  asm volatile("s_waitcnt vmcnt(8)"
      :  :  : "memory"); SB();
  fw1[9][0] = tr8u(tX[0] + 8192, 512);
  fw1[9][1] = tr8u(tX[1] + 8192, 512);
  fw1[9][2] = tr8u(tX[2] + 8192, 512);
  fw1[9][3] = tr8u(tX[3] + 8192, 512);
  WAITL(8);
  { int k_ = KS1(12); OPQ(k_); DMA4(vW1a, vW1b, rs1, k_ * 114688, 55296, ldsR, 20480); sh[12] = sh0 + k_ * 8192; OPQ(sh[12]); }
  SB();
  { unsigned k_ = sc_ + (rot2 >= 2 ? -2 : 12); OPQ(k_); su[12] = k_ << 13; sw[12] = k_ << 17; OPQ(su[12]); OPQ(sw[12]); }
  { unsigned k_ = sc_ + (rot2 >= 1 ? -1 : 13); OPQ(k_); su[13] = k_ << 13; sw[13] = k_ << 17; OPQ(su[13]); OPQ(sw[13]); }
  SB();
  asm volatile("s_waitcnt vmcnt(8)"
      :  :  : "memory"); SB();
  fw1[10][0] = tr8u(tX[0] + 12288, 512);
  fw1[10][1] = tr8u(tX[1] + 12288, 512);
  fw1[10][2] = tr8u(tX[2] + 12288, 512);
  fw1[10][3] = tr8u(tX[3] + 12288, 512);
  WAITL(8);
  { int k_ = KS1(13); OPQ(k_); DMA4(vW1a, vW1b, rs1, k_ * 114688, 55296, ldsR, 24576); sh[13] = sh0 + k_ * 8192; OPQ(sh[13]); }
  asm volatile("s_waitcnt vmcnt(8)"
      :  :  : "memory"); SB();
  fw1[11][0] = tr8u(tX[0] + 16384, 512);
  fw1[11][1] = tr8u(tX[1] + 16384, 512);
  fw1[11][2] = tr8u(tX[2] + 16384, 512);
  fw1[11][3] = tr8u(tX[3] + 16384, 512);
  STAMP(53);
  WAITL(8);
  { int k_ = KS1(14); OPQ(k_); DMA4(vW1a, vW1b, rs1, k_ * 114688, 55296, ldsR, 0); sh[14] = sh0 + k_ * 8192; OPQ(sh[14]); }
  asm volatile("s_waitcnt vmcnt(8)"
      :  :  : "memory"); SB();
  fw1[12][0] = tr8u(tX[0] + 20480, 512);
  fw1[12][1] = tr8u(tX[1] + 20480, 512);
  fw1[12][2] = tr8u(tX[2] + 20480, 512);
  fw1[12][3] = tr8u(tX[3] + 20480, 512);
  WAITL(8);
  { int k_ = KS1(15); OPQ(k_); DMA4(vW1a, vW1b, rs1, k_ * 114688, 55296, ldsR, 4096); sh[15] = sh0 + k_ * 8192; OPQ(sh[15]); }
  asm volatile("buffer_load_dword %0, %1, %2, 0 offen sc1\n\ts_waitcnt vmcnt(9)"
      : "=&v"(fh) : "v"(vFH), "s"(rsW) : "memory"); SB();
  fw1[13][0] = tr8u(tX[0] + 24576, 512);
  fw1[13][1] = tr8u(tX[1] + 24576, 512);
  fw1[13][2] = tr8u(tX[2] + 24576, 512);
  fw1[13][3] = tr8u(tX[3] + 24576, 512);
  WAITV1(0, fh);
  if (__ballot(fh != nonce) != 0ull) poll1<4>(rsW, vFH, nonce);
  STAMP(6);
  asm volatile("buffer_load_dwordx4 %0, %4, %5, %6 offen offset:0 sc1\n\tbuffer_load_dwordx4 %1, %4, %5, %7 offen offset:0 sc1\n\tbuffer_load_dwordx4 %2, %4, %5, %8 offen offset:0 sc1\n\tbuffer_load_dwordx4 %3, %4, %5, %9 offen offset:0 sc1\n\ts_waitcnt vmcnt(9)"
      : "=&v"(p[0]), "=&v"(p[1]), "=&v"(p[2]), "=&v"(p[3]) : "v"(vHa), "s"(rsW), "s"(sh[0]), "s"(sh[1]), "s"(sh[2]), "s"(sh[3]) : "memory"); SB();
  fw1[14][0] = tr8u(tX[0] + 0, 512);
  fw1[14][1] = tr8u(tX[1] + 0, 512);
  fw1[14][2] = tr8u(tX[2] + 0, 512);
  fw1[14][3] = tr8u(tX[3] + 0, 512);
  asm volatile("buffer_load_dwordx4 %0, %4, %5, %6 offen offset:0 sc1\n\tbuffer_load_dwordx4 %1, %4, %5, %7 offen offset:0 sc1\n\tbuffer_load_dwordx4 %2, %4, %5, %8 offen offset:0 sc1\n\tbuffer_load_dwordx4 %3, %4, %5, %9 offen offset:0 sc1\n\ts_waitcnt vmcnt(9)"
      : "=&v"(p[4]), "=&v"(p[5]), "=&v"(p[6]), "=&v"(p[7]) : "v"(vHa), "s"(rsW), "s"(sh[4]), "s"(sh[5]), "s"(sh[6]), "s"(sh[7]) : "memory"); SB();
  fw1[15][0] = tr8u(tX[0] + 4096, 512);
  fw1[15][1] = tr8u(tX[1] + 4096, 512);
  fw1[15][2] = tr8u(tX[2] + 4096, 512);
  fw1[15][3] = tr8u(tX[3] + 4096, 512);
  STAMP(5);
  asm volatile("buffer_load_dwordx4 %0, %4, %5, %6 offen offset:0 sc1\n\tbuffer_load_dwordx4 %1, %4, %5, %7 offen offset:0 sc1\n\tbuffer_load_dwordx4 %2, %4, %5, %8 offen offset:0 sc1\n\tbuffer_load_dwordx4 %3, %4, %5, %9 offen offset:0 sc1"
      : "=&v"(p[8]), "=&v"(p[9]), "=&v"(p[10]), "=&v"(p[11]) : "v"(vHa), "s"(rsW), "s"(sh[8]), "s"(sh[9]), "s"(sh[10]), "s"(sh[11]) : "memory"); SB();
  f32x4 acc[7][4], pm[7][3]; float gm[4], gz[4]; unsigned uv[8][2];
  WAITVB(11);
  SB();
  asm volatile("v_mfma_f32_16x16x32_bf16 %0, %6, %7, 0\n\tbuffer_load_dwordx4 %1, %8, %9, %10 offen offset:0 sc1\n\tv_mfma_f32_16x16x32_bf16 %2, %11, %7, 0\n\tv_mfma_f32_16x16x32_bf16 %3, %12, %7, 0\n\tbuffer_load_dwordx4 %4, %8, %9, %13 offen offset:0 sc1\n\tv_mfma_f32_16x16x32_bf16 %5, %14, %7, %15"
      : "=&v"(acc[0][1]), "=&v"(p[12]), "=&v"(acc[0][2]), "=&v"(acc[0][3]), "=&v"(p[13]), "=&v"(acc[0][0]) : "a"(fw1[0][1]), "v"(p[0]), "v"(vHa), "s"(rsW), "s"(sh[12]), "a"(fw1[0][2]), "a"(fw1[0][3]), "s"(sh[13]), "a"(fw1[0][0]), "v"(bb) : "memory"); SB();
  WAITVB(12);
  SB();
  asm volatile("v_mfma_f32_16x16x32_bf16 %0, %5, %6, %0\n\tbuffer_load_dwordx4 %1, %7, %8, %9 offen offset:0 sc1\n\tv_mfma_f32_16x16x32_bf16 %2, %10, %6, %2\n\tv_mfma_f32_16x16x32_bf16 %3, %11, %6, %3\n\tv_mfma_f32_16x16x32_bf16 %4, %12, %6, %4"
      : "+v"(acc[0][1]), "=&v"(p[0]), "+v"(acc[0][2]), "+v"(acc[0][3]), "+v"(acc[0][0]) : "a"(fw1[1][1]), "v"(p[1]), "v"(vHa), "s"(rsW), "s"(sh[14]), "a"(fw1[1][2]), "a"(fw1[1][3]), "a"(fw1[1][0]) : "memory"); SB();
  WAITVB(12);
  SB();
  asm volatile("v_mfma_f32_16x16x32_bf16 %0, %5, %6, %0\n\tbuffer_load_dwordx4 %1, %7, %8, %9 offen offset:0 sc1\n\tv_mfma_f32_16x16x32_bf16 %2, %10, %6, %2\n\tv_mfma_f32_16x16x32_bf16 %3, %11, %6, %3\n\tv_mfma_f32_16x16x32_bf16 %4, %12, %6, %4"
      : "+v"(acc[0][1]), "=&v"(p[1]), "+v"(acc[0][2]), "+v"(acc[0][3]), "+v"(acc[0][0]) : "a"(fw1[2][1]), "v"(p[2]), "v"(vHa), "s"(rsW), "s"(sh[15]), "a"(fw1[2][2]), "a"(fw1[2][3]), "a"(fw1[2][0]) : "memory"); SB();
  WAITVB(12);
  SB();
  asm volatile("v_mfma_f32_16x16x32_bf16 %0, %5, %6, %0\n\tbuffer_load_dwordx4 %1, %7, %8, %9 offen offset:0 sc1\n\tv_mfma_f32_16x16x32_bf16 %2, %10, %6, %2\n\tv_mfma_f32_16x16x32_bf16 %3, %11, %6, %3\n\tv_mfma_f32_16x16x32_bf16 %4, %12, %6, %4"
      : "+v"(acc[0][1]), "=&v"(p[2]), "+v"(acc[0][2]), "+v"(acc[0][3]), "+v"(acc[0][0]) : "a"(fw1[3][1]), "v"(p[3]), "v"(vHc), "s"(rsW), "s"(sh[0]), "a"(fw1[3][2]), "a"(fw1[3][3]), "a"(fw1[3][0]) : "memory"); SB();
  WAITVB(12);
  SB();
  asm volatile("v_mfma_f32_16x16x32_bf16 %0, %5, %6, %0\n\tbuffer_load_dwordx4 %1, %7, %8, %9 offen offset:0 sc1\n\tv_mfma_f32_16x16x32_bf16 %2, %10, %6, %2\n\tv_mfma_f32_16x16x32_bf16 %3, %11, %6, %3\n\tv_mfma_f32_16x16x32_bf16 %4, %12, %6, %4"
      : "+v"(acc[0][1]), "=&v"(p[3]), "+v"(acc[0][2]), "+v"(acc[0][3]), "+v"(acc[0][0]) : "a"(fw1[4][1]), "v"(p[4]), "v"(vHc), "s"(rsW), "s"(sh[1]), "a"(fw1[4][2]), "a"(fw1[4][3]), "a"(fw1[4][0]) : "memory"); SB();
  WAITVB(12);
  SB();
  asm volatile("v_mfma_f32_16x16x32_bf16 %0, %5, %6, %0\n\tbuffer_load_dwordx4 %1, %7, %8, %9 offen offset:0 sc1\n\tv_mfma_f32_16x16x32_bf16 %2, %10, %6, %2\n\tv_mfma_f32_16x16x32_bf16 %3, %11, %6, %3\n\tv_mfma_f32_16x16x32_bf16 %4, %12, %6, %4"
      : "+v"(acc[0][1]), "=&v"(p[4]), "+v"(acc[0][2]), "+v"(acc[0][3]), "+v"(acc[0][0]) : "a"(fw1[5][1]), "v"(p[5]), "v"(vHc), "s"(rsW), "s"(sh[2]), "a"(fw1[5][2]), "a"(fw1[5][3]), "a"(fw1[5][0]) : "memory"); SB();
  WAITVB(12);
  SB();
  asm volatile("v_mfma_f32_16x16x32_bf16 %0, %5, %6, %0\n\tbuffer_load_dwordx4 %1, %7, %8, %9 offen offset:0 sc1\n\tv_mfma_f32_16x16x32_bf16 %2, %10, %6, %2\n\tv_mfma_f32_16x16x32_bf16 %3, %11, %6, %3\n\tv_mfma_f32_16x16x32_bf16 %4, %12, %6, %4"
      : "+v"(acc[0][1]), "=&v"(p[5]), "+v"(acc[0][2]), "+v"(acc[0][3]), "+v"(acc[0][0]) : "a"(fw1[6][1]), "v"(p[6]), "v"(vHc), "s"(rsW), "s"(sh[3]), "a"(fw1[6][2]), "a"(fw1[6][3]), "a"(fw1[6][0]) : "memory"); SB();
  WAITVB(12);
  SB();
  asm volatile("v_mfma_f32_16x16x32_bf16 %0, %5, %6, %0\n\tbuffer_load_dwordx4 %1, %7, %8, %9 offen offset:0 sc1\n\tv_mfma_f32_16x16x32_bf16 %2, %10, %6, %2\n\tv_mfma_f32_16x16x32_bf16 %3, %11, %6, %3\n\tv_mfma_f32_16x16x32_bf16 %4, %12, %6, %4"
      : "+v"(acc[0][1]), "=&v"(p[6]), "+v"(acc[0][2]), "+v"(acc[0][3]), "+v"(acc[0][0]) : "a"(fw1[7][1]), "v"(p[7]), "v"(vHc), "s"(rsW), "s"(sh[4]), "a"(fw1[7][2]), "a"(fw1[7][3]), "a"(fw1[7][0]) : "memory"); SB();
  WAITVB(12);
  SB();
  asm volatile("v_mfma_f32_16x16x32_bf16 %0, %5, %6, %0\n\tbuffer_load_dwordx4 %1, %7, %8, %9 offen offset:0 sc1\n\tv_mfma_f32_16x16x32_bf16 %2, %10, %6, %2\n\tv_mfma_f32_16x16x32_bf16 %3, %11, %6, %3\n\tv_mfma_f32_16x16x32_bf16 %4, %12, %6, %4"
      : "+v"(acc[0][1]), "=&v"(p[7]), "+v"(acc[0][2]), "+v"(acc[0][3]), "+v"(acc[0][0]) : "a"(fw1[8][1]), "v"(p[8]), "v"(vHc), "s"(rsW), "s"(sh[5]), "a"(fw1[8][2]), "a"(fw1[8][3]), "a"(fw1[8][0]) : "memory"); SB();
  WAITVB(12);
  SB();
  asm volatile("v_mfma_f32_16x16x32_bf16 %0, %5, %6, %0\n\tbuffer_load_dwordx4 %1, %7, %8, %9 offen offset:0 sc1\n\tv_mfma_f32_16x16x32_bf16 %2, %10, %6, %2\n\tv_mfma_f32_16x16x32_bf16 %3, %11, %6, %3\n\tv_mfma_f32_16x16x32_bf16 %4, %12, %6, %4"
      : "+v"(acc[0][1]), "=&v"(p[8]), "+v"(acc[0][2]), "+v"(acc[0][3]), "+v"(acc[0][0]) : "a"(fw1[9][1]), "v"(p[9]), "v"(vHc), "s"(rsW), "s"(sh[6]), "a"(fw1[9][2]), "a"(fw1[9][3]), "a"(fw1[9][0]) : "memory"); SB();
  WAITVB(12);
  SB();
  asm volatile("v_mfma_f32_16x16x32_bf16 %0, %5, %6, %0\n\tbuffer_load_dwordx4 %1, %7, %8, %9 offen offset:0 sc1\n\tv_mfma_f32_16x16x32_bf16 %2, %10, %6, %2\n\tv_mfma_f32_16x16x32_bf16 %3, %11, %6, %3\n\tv_mfma_f32_16x16x32_bf16 %4, %12, %6, %4"
      : "+v"(acc[0][1]), "=&v"(p[9]), "+v"(acc[0][2]), "+v"(acc[0][3]), "+v"(acc[0][0]) : "a"(fw1[10][1]), "v"(p[10]), "v"(vHc), "s"(rsW), "s"(sh[7]), "a"(fw1[10][2]), "a"(fw1[10][3]), "a"(fw1[10][0]) : "memory"); SB();
  WAITVB(12);
  SB();
  asm volatile("v_mfma_f32_16x16x32_bf16 %0, %5, %6, %0\n\tbuffer_load_dwordx4 %1, %7, %8, %9 offen offset:0 sc1\n\tv_mfma_f32_16x16x32_bf16 %2, %10, %6, %2\n\tv_mfma_f32_16x16x32_bf16 %3, %11, %6, %3\n\tv_mfma_f32_16x16x32_bf16 %4, %12, %6, %4"
      : "+v"(acc[0][1]), "=&v"(p[10]), "+v"(acc[0][2]), "+v"(acc[0][3]), "+v"(acc[0][0]) : "a"(fw1[11][1]), "v"(p[11]), "v"(vHc), "s"(rsW), "s"(sh[8]), "a"(fw1[11][2]), "a"(fw1[11][3]), "a"(fw1[11][0]) : "memory"); SB();
  WAITVB(12);
  SB();
  asm volatile("v_mfma_f32_16x16x32_bf16 %0, %5, %6, %0\n\tbuffer_load_dwordx4 %1, %7, %8, %9 offen offset:0 sc1\n\tv_mfma_f32_16x16x32_bf16 %2, %10, %6, %2\n\tv_mfma_f32_16x16x32_bf16 %3, %11, %6, %3\n\tv_mfma_f32_16x16x32_bf16 %4, %12, %6, %4"
      : "+v"(acc[0][1]), "=&v"(p[11]), "+v"(acc[0][2]), "+v"(acc[0][3]), "+v"(acc[0][0]) : "a"(fw1[12][1]), "v"(p[12]), "v"(vHc), "s"(rsW), "s"(sh[9]), "a"(fw1[12][2]), "a"(fw1[12][3]), "a"(fw1[12][0]) : "memory"); SB();
  WAITVB(12);
  SB();
  asm volatile("v_mfma_f32_16x16x32_bf16 %0, %5, %6, %0\n\tbuffer_load_dwordx4 %1, %7, %8, %9 offen offset:0 sc1\n\tv_mfma_f32_16x16x32_bf16 %2, %10, %6, %2\n\tv_mfma_f32_16x16x32_bf16 %3, %11, %6, %3\n\tv_mfma_f32_16x16x32_bf16 %4, %12, %6, %4"
      : "+v"(acc[0][1]), "=&v"(p[12]), "+v"(acc[0][2]), "+v"(acc[0][3]), "+v"(acc[0][0]) : "a"(fw1[13][1]), "v"(p[13]), "v"(vHc), "s"(rsW), "s"(sh[10]), "a"(fw1[13][2]), "a"(fw1[13][3]), "a"(fw1[13][0]) : "memory"); SB();
  WAITVB(12);
  SB();
  asm volatile("v_mfma_f32_16x16x32_bf16 %0, %5, %6, %0\n\tbuffer_load_dwordx4 %1, %7, %8, %9 offen offset:0 sc1\n\tv_mfma_f32_16x16x32_bf16 %2, %10, %6, %2\n\tv_mfma_f32_16x16x32_bf16 %3, %11, %6, %3\n\tv_mfma_f32_16x16x32_bf16 %4, %12, %6, %4"
      : "+v"(acc[0][1]), "=&v"(p[13]), "+v"(acc[0][2]), "+v"(acc[0][3]), "+v"(acc[0][0]) : "a"(fw1[14][1]), "v"(p[0]), "v"(vHc), "s"(rsW), "s"(sh[11]), "a"(fw1[14][2]), "a"(fw1[14][3]), "a"(fw1[14][0]) : "memory"); SB();
  WAITVB(12);
  SB();
  asm volatile("v_mfma_f32_16x16x32_bf16 %0, %5, %6, %0\n\tbuffer_load_dwordx4 %1, %7, %8, %9 offen offset:0 sc1\n\tv_mfma_f32_16x16x32_bf16 %2, %10, %6, %2\n\tv_mfma_f32_16x16x32_bf16 %3, %11, %6, %3\n\tv_mfma_f32_16x16x32_bf16 %4, %12, %6, %4"
      : "+v"(acc[0][1]), "=&v"(p[0]), "+v"(acc[0][2]), "+v"(acc[0][3]), "+v"(acc[0][0]) : "v"(fw1[15][1]), "v"(p[1]), "v"(vHc), "s"(rsW), "s"(sh[12]), "v"(fw1[15][2]), "v"(fw1[15][3]), "v"(fw1[15][0]) : "memory"); SB();
  STAMP(7);
  WAITVB(12);
  SB();
  asm volatile("v_mfma_f32_16x16x32_bf16 %0, %5, %6, 0\n\tbuffer_load_dwordx4 %1, %7, %8, %9 offen offset:0 sc1\n\tv_mfma_f32_16x16x32_bf16 %2, %10, %6, 0\n\tv_mfma_f32_16x16x32_bf16 %3, %11, %6, 0\n\tv_mfma_f32_16x16x32_bf16 %4, %12, %6, %13"
      : "=&v"(acc[1][1]), "=&v"(p[1]), "=&v"(acc[1][2]), "=&v"(acc[1][3]), "=&v"(acc[1][0]) : "a"(fw1[0][1]), "v"(p[2]), "v"(vHc), "s"(rsW), "s"(sh[13]), "a"(fw1[0][2]), "a"(fw1[0][3]), "a"(fw1[0][0]), "v"(bb) : "memory"); SB();
  WAITVB(12);
  SB();
  asm volatile("v_mfma_f32_16x16x32_bf16 %0, %5, %6, %0\n\tbuffer_load_dwordx4 %1, %7, %8, %9 offen offset:0 sc1\n\tv_mfma_f32_16x16x32_bf16 %2, %10, %6, %2\n\tds_write_b128 %11, %12 offset:1024\n\tv_mfma_f32_16x16x32_bf16 %3, %13, %6, %3\n\tv_mfma_f32_16x16x32_bf16 %4, %14, %6, %4"
      : "+v"(acc[1][1]), "=&v"(p[2]), "+v"(acc[1][2]), "+v"(acc[1][3]), "+v"(acc[1][0]) : "a"(fw1[1][1]), "v"(p[3]), "v"(vHc), "s"(rsW), "s"(sh[14]), "a"(fw1[1][2]), "v"(rw), "v"(acc[0][1]), "a"(fw1[1][3]), "a"(fw1[1][0]) : "memory"); SB();
  WAITVB(12);
  SB();
  asm volatile("v_mfma_f32_16x16x32_bf16 %0, %5, %6, %0\n\tbuffer_load_dwordx4 %1, %7, %8, %9 offen offset:0 sc1\n\tv_mfma_f32_16x16x32_bf16 %2, %10, %6, %2\n\tds_write_b128 %11, %12 offset:2048\n\tv_mfma_f32_16x16x32_bf16 %3, %13, %6, %3\n\tv_mfma_f32_16x16x32_bf16 %4, %14, %6, %4"
      : "+v"(acc[1][1]), "=&v"(p[3]), "+v"(acc[1][2]), "+v"(acc[1][3]), "+v"(acc[1][0]) : "a"(fw1[2][1]), "v"(p[4]), "v"(vHc), "s"(rsW), "s"(sh[15]), "a"(fw1[2][2]), "v"(rw), "v"(acc[0][2]), "a"(fw1[2][3]), "a"(fw1[2][0]) : "memory"); SB();
  WAITVB(12);
  SB();
  asm volatile("v_mfma_f32_16x16x32_bf16 %0, %5, %6, %0\n\tbuffer_load_dwordx4 %1, %7, %8, %9 offen offset:512 sc1\n\tv_mfma_f32_16x16x32_bf16 %2, %10, %6, %2\n\tds_write_b128 %11, %12 offset:3072\n\tv_mfma_f32_16x16x32_bf16 %3, %13, %6, %3\n\tv_mfma_f32_16x16x32_bf16 %4, %14, %6, %4"
      : "+v"(acc[1][1]), "=&v"(p[4]), "+v"(acc[1][2]), "+v"(acc[1][3]), "+v"(acc[1][0]) : "a"(fw1[3][1]), "v"(p[5]), "v"(vHa), "s"(rsW), "s"(sh[0]), "a"(fw1[3][2]), "v"(rw), "v"(acc[0][3]), "a"(fw1[3][3]), "a"(fw1[3][0]) : "memory"); SB();
  WAITVB(12);
  SB();
  asm volatile("v_mfma_f32_16x16x32_bf16 %0, %3, %4, %0\n\tbuffer_load_dwordx4 %1, %5, %6, %7 offen offset:512 sc1\n\tv_mfma_f32_16x16x32_bf16 %2, %8, %4, %2"
      : "+v"(acc[1][1]), "=&v"(p[5]), "+v"(acc[1][2]) : "a"(fw1[4][1]), "v"(p[6]), "v"(vHa), "s"(rsW), "s"(sh[1]), "a"(fw1[4][2]) : "memory"); SB();
  BAR();
  SB();
  asm volatile("v_mfma_f32_16x16x32_bf16 %0, %5, %6, %0\n\tds_read_b128 %1, %7 offset:1024\n\tds_read_b128 %2, %8 offset:2048\n\tv_mfma_f32_16x16x32_bf16 %3, %9, %6, %3\n\tds_read_b128 %4, %10 offset:3072"
      : "+v"(acc[1][3]), "=&v"(pm[0][0]), "=&v"(pm[0][1]), "+v"(acc[1][0]), "=&v"(pm[0][2]) : "a"(fw1[4][3]), "v"(p[6]), "v"(rr1), "v"(rr2), "a"(fw1[4][0]), "v"(rr3) : "memory"); SB();
  WAITVB(12);
  SB();
  asm volatile("v_mfma_f32_16x16x32_bf16 %0, %5, %6, %0\n\tbuffer_load_dwordx4 %1, %7, %8, %9 offen offset:512 sc1\n\tv_mfma_f32_16x16x32_bf16 %2, %10, %6, %2\n\tv_mfma_f32_16x16x32_bf16 %3, %11, %6, %3\n\tv_mfma_f32_16x16x32_bf16 %4, %12, %6, %4"
      : "+v"(acc[1][1]), "=&v"(p[6]), "+v"(acc[1][2]), "+v"(acc[1][3]), "+v"(acc[1][0]) : "a"(fw1[5][1]), "v"(p[7]), "v"(vHa), "s"(rsW), "s"(sh[2]), "a"(fw1[5][2]), "a"(fw1[5][3]), "a"(fw1[5][0]) : "memory"); SB();
  WAITVB(12);
  SB();
  asm volatile("v_mfma_f32_16x16x32_bf16 %0, %3, %4, %0\n\tbuffer_load_dwordx4 %1, %5, %6, %7 offen offset:512 sc1\n\tv_mfma_f32_16x16x32_bf16 %2, %8, %4, %2"
      : "+v"(acc[1][1]), "=&v"(p[7]), "+v"(acc[1][2]) : "a"(fw1[6][1]), "v"(p[8]), "v"(vHa), "s"(rsW), "s"(sh[3]), "a"(fw1[6][2]) : "memory"); SB();
  WAITL(0);
  asm volatile("v_add_f32 %0, %6, %7\n\tv_mfma_f32_16x16x32_bf16 %1, %8, %9, %1\n\tv_add_f32 %2, %10, %11\n\tv_add_f32 %3, %12, %13\n\tv_mfma_f32_16x16x32_bf16 %4, %14, %9, %4\n\tv_add_f32 %5, %15, %16"
      : "=&v"(gm[0]), "+v"(acc[1][3]), "=&v"(gm[1]), "=&v"(gm[2]), "+v"(acc[1][0]), "=&v"(gm[3]) : "v"(acc[0][0][0]), "v"(pm[0][0][0]), "a"(fw1[6][3]), "v"(p[8]), "v"(acc[0][0][1]), "v"(pm[0][0][1]), "v"(acc[0][0][2]), "v"(pm[0][0][2]), "a"(fw1[6][0]), "v"(acc[0][0][3]), "v"(pm[0][0][3])); SB();
  WAITVB(12);
  SB();
  asm volatile("v_mfma_f32_16x16x32_bf16 %0, %9, %10, %0\n\tbuffer_load_dwordx4 %1, %11, %12, %13 offen offset:512 sc1\n\tv_mfma_f32_16x16x32_bf16 %2, %14, %10, %2\n\tv_add_f32 %3, %15, %16\n\tv_add_f32 %4, %17, %18\n\tv_mfma_f32_16x16x32_bf16 %5, %19, %10, %5\n\tv_add_f32 %6, %20, %21\n\tv_add_f32 %7, %22, %23\n\tv_mfma_f32_16x16x32_bf16 %8, %24, %10, %8"
      : "+v"(acc[1][1]), "=&v"(p[8]), "+v"(acc[1][2]), "=&v"(gz[0]), "=&v"(gz[1]), "+v"(acc[1][3]), "=&v"(gz[2]), "=&v"(gz[3]), "+v"(acc[1][0]) : "a"(fw1[7][1]), "v"(p[9]), "v"(vHa), "s"(rsW), "s"(sh[4]), "a"(fw1[7][2]), "v"(pm[0][1][0]), "v"(pm[0][2][0]), "v"(pm[0][1][1]), "v"(pm[0][2][1]), "a"(fw1[7][3]), "v"(pm[0][1][2]), "v"(pm[0][2][2]), "v"(pm[0][1][3]), "v"(pm[0][2][3]), "a"(fw1[7][0]) : "memory"); SB();
  asm volatile("v_add_f32 %0, %0, %1"
      : "+v"(gm[0]) : "v"(gz[0])); SB();
  WAITVB(12);
  SB();
  asm volatile("v_mfma_f32_16x16x32_bf16 %0, %10, %11, %0\n\tbuffer_load_dwordx4 %1, %12, %13, %14 offen offset:512 sc1\n\tv_mfma_f32_16x16x32_bf16 %2, %15, %11, %2\n\tv_add_f32 %3, %3, %4\n\tv_add_f32 %5, %5, %16\n\tv_mfma_f32_16x16x32_bf16 %6, %17, %11, %6\n\tv_add_f32 %7, %7, %18\n\tv_mul_f32 %8, %19, %19\n\tv_mfma_f32_16x16x32_bf16 %9, %20, %11, %9\n\tv_mul_f32 %4, %3, %3"
      : "+v"(acc[1][1]), "=&v"(p[9]), "+v"(acc[1][2]), "+v"(gm[1]), "+v"(gz[1]), "+v"(gm[2]), "+v"(acc[1][3]), "+v"(gm[3]), "=&v"(gz[0]), "+v"(acc[1][0]) : "a"(fw1[8][1]), "v"(p[10]), "v"(vHa), "s"(rsW), "s"(sh[5]), "a"(fw1[8][2]), "v"(gz[2]), "a"(fw1[8][3]), "v"(gz[3]), "v"(gm[0]), "a"(fw1[8][0]) : "memory"); SB();
  WAITVB(12);
  SB();
  asm volatile("v_mfma_f32_16x16x32_bf16 %0, %9, %10, %0\n\tbuffer_load_dwordx4 %1, %11, %12, %13 offen offset:512 sc1\n\tv_mfma_f32_16x16x32_bf16 %2, %14, %10, %2\n\tv_mul_f32 %3, %15, %15\n\tv_mul_f32 %4, %16, %16\n\tv_mfma_f32_16x16x32_bf16 %5, %17, %10, %5\n\tv_fmaak_f32 %6, %18, %6, 0x40135761\n\tv_fmaak_f32 %7, %18, %7, 0x40135761\n\tv_mfma_f32_16x16x32_bf16 %8, %19, %10, %8\n\tv_fmaak_f32 %3, %18, %3, 0x40135761"
      : "+v"(acc[1][1]), "=&v"(p[10]), "+v"(acc[1][2]), "=&v"(gz[2]), "=&v"(gz[3]), "+v"(acc[1][3]), "+v"(gz[0]), "+v"(gz[1]), "+v"(acc[1][0]) : "a"(fw1[9][1]), "v"(p[11]), "v"(vHa), "s"(rsW), "s"(sh[6]), "a"(fw1[9][2]), "v"(gm[2]), "v"(gm[3]), "a"(fw1[9][3]), "v"(gc), "a"(fw1[9][0]) : "memory"); SB();
  WAITVB(12);
  SB();
  asm volatile("v_mfma_f32_16x16x32_bf16 %0, %9, %10, %0\n\tbuffer_load_dwordx4 %1, %11, %12, %13 offen offset:512 sc1\n\tv_mfma_f32_16x16x32_bf16 %2, %14, %10, %2\n\tv_fmaak_f32 %3, %15, %3, 0x40135761\n\tv_mul_f32 %4, %4, %16\n\tv_mfma_f32_16x16x32_bf16 %5, %17, %10, %5\n\tv_mul_f32 %6, %6, %18\n\tv_mul_f32 %7, %7, %19\n\tv_mfma_f32_16x16x32_bf16 %8, %20, %10, %8\n\tv_mul_f32 %3, %3, %21"
      : "+v"(acc[1][1]), "=&v"(p[11]), "+v"(acc[1][2]), "+v"(gz[3]), "+v"(gz[0]), "+v"(acc[1][3]), "+v"(gz[1]), "+v"(gz[2]), "+v"(acc[1][0]) : "a"(fw1[10][1]), "v"(p[12]), "v"(vHa), "s"(rsW), "s"(sh[7]), "a"(fw1[10][2]), "v"(gc), "v"(gm[0]), "a"(fw1[10][3]), "v"(gm[1]), "v"(gm[2]), "a"(fw1[10][0]), "v"(gm[3]) : "memory"); SB();
  WAITVB(12);
  SB();
  asm volatile("v_mfma_f32_16x16x32_bf16 %0, %7, %8, %0\n\tbuffer_load_dwordx4 %1, %9, %10, %11 offen offset:512 sc1\n\tv_mfma_f32_16x16x32_bf16 %2, %12, %8, %2\n\tv_exp_f32 %3, %3\n\tv_mfma_f32_16x16x32_bf16 %4, %13, %8, %4\n\tv_exp_f32 %5, %5\n\tv_mfma_f32_16x16x32_bf16 %6, %14, %8, %6"
      : "+v"(acc[1][1]), "=&v"(p[12]), "+v"(acc[1][2]), "+v"(gz[0]), "+v"(acc[1][3]), "+v"(gz[1]), "+v"(acc[1][0]) : "a"(fw1[11][1]), "v"(p[13]), "v"(vHa), "s"(rsW), "s"(sh[8]), "a"(fw1[11][2]), "a"(fw1[11][3]), "a"(fw1[11][0]) : "memory"); SB();
  WAITVB(12);
  SB();
  asm volatile("v_mfma_f32_16x16x32_bf16 %0, %8, %9, %0\n\tbuffer_load_dwordx4 %1, %10, %11, %12 offen offset:512 sc1\n\tv_mfma_f32_16x16x32_bf16 %2, %13, %9, %2\n\tv_exp_f32 %3, %3\n\tv_mfma_f32_16x16x32_bf16 %4, %14, %9, %4\n\tv_exp_f32 %5, %5\n\tv_mfma_f32_16x16x32_bf16 %6, %15, %9, %6\n\tv_add_f32 %7, 1.0, %7"
      : "+v"(acc[1][1]), "=&v"(p[13]), "+v"(acc[1][2]), "+v"(gz[2]), "+v"(acc[1][3]), "+v"(gz[3]), "+v"(acc[1][0]), "+v"(gz[0]) : "a"(fw1[12][1]), "v"(p[0]), "v"(vHa), "s"(rsW), "s"(sh[9]), "a"(fw1[12][2]), "a"(fw1[12][3]), "a"(fw1[12][0]) : "memory"); SB();
  WAITVB(12);
  SB();
  asm volatile("v_mfma_f32_16x16x32_bf16 %0, %8, %9, %0\n\tbuffer_load_dwordx4 %1, %10, %11, %12 offen offset:512 sc1\n\tv_mfma_f32_16x16x32_bf16 %2, %13, %9, %2\n\tv_add_f32 %3, 1.0, %3\n\tv_add_f32 %4, 1.0, %4\n\tv_mfma_f32_16x16x32_bf16 %5, %14, %9, %5\n\tv_add_f32 %6, 1.0, %6\n\tv_mfma_f32_16x16x32_bf16 %7, %15, %9, %7"
      : "+v"(acc[1][1]), "=&v"(p[0]), "+v"(acc[1][2]), "+v"(gz[1]), "+v"(gz[2]), "+v"(acc[1][3]), "+v"(gz[3]), "+v"(acc[1][0]) : "a"(fw1[13][1]), "v"(p[1]), "v"(vHa), "s"(rsW), "s"(sh[10]), "a"(fw1[13][2]), "a"(fw1[13][3]), "a"(fw1[13][0]) : "memory"); SB();
  WAITVB(12);
  SB();
  asm volatile("v_mfma_f32_16x16x32_bf16 %0, %7, %8, %0\n\tbuffer_load_dwordx4 %1, %9, %10, %11 offen offset:512 sc1\n\tv_mfma_f32_16x16x32_bf16 %2, %12, %8, %2\n\tv_rcp_f32 %3, %3\n\tv_mfma_f32_16x16x32_bf16 %4, %13, %8, %4\n\tv_rcp_f32 %5, %5\n\tv_mfma_f32_16x16x32_bf16 %6, %14, %8, %6"
      : "+v"(acc[1][1]), "=&v"(p[1]), "+v"(acc[1][2]), "+v"(gz[0]), "+v"(acc[1][3]), "+v"(gz[1]), "+v"(acc[1][0]) : "a"(fw1[14][1]), "v"(p[2]), "v"(vHa), "s"(rsW), "s"(sh[11]), "a"(fw1[14][2]), "a"(fw1[14][3]), "a"(fw1[14][0]) : "memory"); SB();
  WAITVB(12);
  SB();
  asm volatile("v_mfma_f32_16x16x32_bf16 %0, %9, %10, %0\n\tbuffer_load_dwordx4 %1, %11, %12, %13 offen offset:512 sc1\n\tv_mfma_f32_16x16x32_bf16 %2, %14, %10, %2\n\tv_rcp_f32 %3, %3\n\tv_mfma_f32_16x16x32_bf16 %4, %15, %10, %4\n\tv_rcp_f32 %5, %5\n\tv_mfma_f32_16x16x32_bf16 %6, %16, %10, %6\n\tv_fma_f32 %7, -%17, %7, %17\n\tv_fma_f32 %8, -%18, %8, %18"
      : "+v"(acc[1][1]), "=&v"(p[2]), "+v"(acc[1][2]), "+v"(gz[2]), "+v"(acc[1][3]), "+v"(gz[3]), "+v"(acc[1][0]), "+v"(gz[0]), "+v"(gz[1]) : "v"(fw1[15][1]), "v"(p[3]), "v"(vHa), "s"(rsW), "s"(sh[12]), "v"(fw1[15][2]), "v"(fw1[15][3]), "v"(fw1[15][0]), "v"(gm[0]), "v"(gm[1]) : "memory"); SB();
  WAITVB(12);
  SB();
  asm volatile("v_mfma_f32_16x16x32_bf16 %0, %9, %10, 0\n\tbuffer_load_dwordx4 %1, %11, %12, %13 offen offset:512 sc1\n\tv_mfma_f32_16x16x32_bf16 %2, %14, %10, 0\n\tv_fma_f32 %3, -%15, %3, %15\n\tv_fma_f32 %4, -%16, %4, %16\n\tv_mfma_f32_16x16x32_bf16 %5, %17, %10, 0\n\tv_cvt_pk_bf16_f32 %6, %18, %19\n\tv_cvt_pk_bf16_f32 %7, %3, %4\n\tv_mfma_f32_16x16x32_bf16 %8, %20, %10, %21"
      : "=&v"(acc[2][1]), "=&v"(p[3]), "=&v"(acc[2][2]), "+v"(gz[2]), "+v"(gz[3]), "=&v"(acc[2][3]), "=&v"(uv[0][0]), "=&v"(uv[0][1]), "=&v"(acc[2][0]) : "a"(fw1[0][1]), "v"(p[4]), "v"(vHa), "s"(rsW), "s"(sh[13]), "a"(fw1[0][2]), "v"(gm[2]), "v"(gm[3]), "a"(fw1[0][3]), "v"(gz[0]), "v"(gz[1]), "a"(fw1[0][0]), "v"(bb) : "memory"); SB();
  WAITVB(12);
  SB();
  asm volatile("v_mfma_f32_16x16x32_bf16 %0, %5, %6, %0\n\tbuffer_load_dwordx4 %1, %7, %8, %9 offen offset:512 sc1\n\tv_mfma_f32_16x16x32_bf16 %2, %10, %6, %2\n\tds_write_b128 %11, %12 offset:5120\n\tv_mfma_f32_16x16x32_bf16 %3, %13, %6, %3\n\tv_mfma_f32_16x16x32_bf16 %4, %14, %6, %4"
      : "+v"(acc[2][1]), "=&v"(p[4]), "+v"(acc[2][2]), "+v"(acc[2][3]), "+v"(acc[2][0]) : "a"(fw1[1][1]), "v"(p[5]), "v"(vHa), "s"(rsW), "s"(sh[14]), "a"(fw1[1][2]), "v"(rw), "v"(acc[1][1]), "a"(fw1[1][3]), "a"(fw1[1][0]) : "memory"); SB();
  WAITVB(12);
  SB();
  asm volatile("v_mfma_f32_16x16x32_bf16 %0, %5, %6, %0\n\tbuffer_load_dwordx4 %1, %7, %8, %9 offen offset:512 sc1\n\tv_mfma_f32_16x16x32_bf16 %2, %10, %6, %2\n\tds_write_b128 %11, %12 offset:6144\n\tv_mfma_f32_16x16x32_bf16 %3, %13, %6, %3\n\tv_mfma_f32_16x16x32_bf16 %4, %14, %6, %4"
      : "+v"(acc[2][1]), "=&v"(p[5]), "+v"(acc[2][2]), "+v"(acc[2][3]), "+v"(acc[2][0]) : "a"(fw1[2][1]), "v"(p[6]), "v"(vHa), "s"(rsW), "s"(sh[15]), "a"(fw1[2][2]), "v"(rw), "v"(acc[1][2]), "a"(fw1[2][3]), "a"(fw1[2][0]) : "memory"); SB();
  WAITVB(12);
  SB();
  asm volatile("v_mfma_f32_16x16x32_bf16 %0, %5, %6, %0\n\tbuffer_load_dwordx4 %1, %7, %8, %9 offen offset:512 sc1\n\tv_mfma_f32_16x16x32_bf16 %2, %10, %6, %2\n\tds_write_b128 %11, %12 offset:7168\n\tv_mfma_f32_16x16x32_bf16 %3, %13, %6, %3\n\tv_mfma_f32_16x16x32_bf16 %4, %14, %6, %4"
      : "+v"(acc[2][1]), "=&v"(p[6]), "+v"(acc[2][2]), "+v"(acc[2][3]), "+v"(acc[2][0]) : "a"(fw1[3][1]), "v"(p[7]), "v"(vHc), "s"(rsW), "s"(sh[0]), "a"(fw1[3][2]), "v"(rw), "v"(acc[1][3]), "a"(fw1[3][3]), "a"(fw1[3][0]) : "memory"); SB();
  WAITVB(12);
  SB();
  asm volatile("v_mfma_f32_16x16x32_bf16 %0, %3, %4, %0\n\tbuffer_load_dwordx4 %1, %5, %6, %7 offen offset:512 sc1\n\tv_mfma_f32_16x16x32_bf16 %2, %8, %4, %2"
      : "+v"(acc[2][1]), "=&v"(p[7]), "+v"(acc[2][2]) : "a"(fw1[4][1]), "v"(p[8]), "v"(vHc), "s"(rsW), "s"(sh[1]), "a"(fw1[4][2]) : "memory"); SB();
  BAR();
  SB();
  asm volatile("v_mfma_f32_16x16x32_bf16 %0, %5, %6, %0\n\tds_read_b128 %1, %7 offset:5120\n\tds_read_b128 %2, %8 offset:6144\n\tv_mfma_f32_16x16x32_bf16 %3, %9, %6, %3\n\tds_read_b128 %4, %10 offset:7168"
      : "+v"(acc[2][3]), "=&v"(pm[1][0]), "=&v"(pm[1][1]), "+v"(acc[2][0]), "=&v"(pm[1][2]) : "a"(fw1[4][3]), "v"(p[8]), "v"(rr1), "v"(rr2), "a"(fw1[4][0]), "v"(rr3) : "memory"); SB();
  WAITVB(12);
  SB();
  asm volatile("v_mfma_f32_16x16x32_bf16 %0, %5, %6, %0\n\tbuffer_load_dwordx4 %1, %7, %8, %9 offen offset:512 sc1\n\tv_mfma_f32_16x16x32_bf16 %2, %10, %6, %2\n\tv_mfma_f32_16x16x32_bf16 %3, %11, %6, %3\n\tv_mfma_f32_16x16x32_bf16 %4, %12, %6, %4"
      : "+v"(acc[2][1]), "=&v"(p[8]), "+v"(acc[2][2]), "+v"(acc[2][3]), "+v"(acc[2][0]) : "a"(fw1[5][1]), "v"(p[9]), "v"(vHc), "s"(rsW), "s"(sh[2]), "a"(fw1[5][2]), "a"(fw1[5][3]), "a"(fw1[5][0]) : "memory"); SB();
  WAITVB(12);
  SB();
  asm volatile("v_mfma_f32_16x16x32_bf16 %0, %3, %4, %0\n\tbuffer_load_dwordx4 %1, %5, %6, %7 offen offset:512 sc1\n\tv_mfma_f32_16x16x32_bf16 %2, %8, %4, %2"
      : "+v"(acc[2][1]), "=&v"(p[9]), "+v"(acc[2][2]) : "a"(fw1[6][1]), "v"(p[10]), "v"(vHc), "s"(rsW), "s"(sh[3]), "a"(fw1[6][2]) : "memory"); SB();
  WAITL(0);
  asm volatile("v_add_f32 %0, %6, %7\n\tv_mfma_f32_16x16x32_bf16 %1, %8, %9, %1\n\tv_add_f32 %2, %10, %11\n\tv_add_f32 %3, %12, %13\n\tv_mfma_f32_16x16x32_bf16 %4, %14, %9, %4\n\tv_add_f32 %5, %15, %16"
      : "=&v"(gm[0]), "+v"(acc[2][3]), "=&v"(gm[1]), "=&v"(gm[2]), "+v"(acc[2][0]), "=&v"(gm[3]) : "v"(acc[1][0][0]), "v"(pm[1][0][0]), "a"(fw1[6][3]), "v"(p[10]), "v"(acc[1][0][1]), "v"(pm[1][0][1]), "v"(acc[1][0][2]), "v"(pm[1][0][2]), "a"(fw1[6][0]), "v"(acc[1][0][3]), "v"(pm[1][0][3])); SB();
  WAITVB(12);
  SB();
  asm volatile("v_mfma_f32_16x16x32_bf16 %0, %9, %10, %0\n\tbuffer_load_dwordx4 %1, %11, %12, %13 offen offset:512 sc1\n\tv_mfma_f32_16x16x32_bf16 %2, %14, %10, %2\n\tv_add_f32 %3, %15, %16\n\tv_add_f32 %4, %17, %18\n\tv_mfma_f32_16x16x32_bf16 %5, %19, %10, %5\n\tv_add_f32 %6, %20, %21\n\tv_add_f32 %7, %22, %23\n\tv_mfma_f32_16x16x32_bf16 %8, %24, %10, %8"
      : "+v"(acc[2][1]), "=&v"(p[10]), "+v"(acc[2][2]), "=&v"(gz[0]), "=&v"(gz[1]), "+v"(acc[2][3]), "=&v"(gz[2]), "=&v"(gz[3]), "+v"(acc[2][0]) : "a"(fw1[7][1]), "v"(p[11]), "v"(vHc), "s"(rsW), "s"(sh[4]), "a"(fw1[7][2]), "v"(pm[1][1][0]), "v"(pm[1][2][0]), "v"(pm[1][1][1]), "v"(pm[1][2][1]), "a"(fw1[7][3]), "v"(pm[1][1][2]), "v"(pm[1][2][2]), "v"(pm[1][1][3]), "v"(pm[1][2][3]), "a"(fw1[7][0]) : "memory"); SB();
  asm volatile("v_add_f32 %0, %0, %1"
      : "+v"(gm[0]) : "v"(gz[0])); SB();
  WAITVB(12);
  SB();
  asm volatile("v_mfma_f32_16x16x32_bf16 %0, %10, %11, %0\n\tbuffer_load_dwordx4 %1, %12, %13, %14 offen offset:512 sc1\n\tv_mfma_f32_16x16x32_bf16 %2, %15, %11, %2\n\tv_add_f32 %3, %3, %4\n\tv_add_f32 %5, %5, %16\n\tv_mfma_f32_16x16x32_bf16 %6, %17, %11, %6\n\tv_add_f32 %7, %7, %18\n\tv_mul_f32 %8, %19, %19\n\tv_mfma_f32_16x16x32_bf16 %9, %20, %11, %9\n\tv_mul_f32 %4, %3, %3"
      : "+v"(acc[2][1]), "=&v"(p[11]), "+v"(acc[2][2]), "+v"(gm[1]), "+v"(gz[1]), "+v"(gm[2]), "+v"(acc[2][3]), "+v"(gm[3]), "=&v"(gz[0]), "+v"(acc[2][0]) : "a"(fw1[8][1]), "v"(p[12]), "v"(vHc), "s"(rsW), "s"(sh[5]), "a"(fw1[8][2]), "v"(gz[2]), "a"(fw1[8][3]), "v"(gz[3]), "v"(gm[0]), "a"(fw1[8][0]) : "memory"); SB();
  WAITVB(12);
  SB();
  asm volatile("v_mfma_f32_16x16x32_bf16 %0, %9, %10, %0\n\tbuffer_load_dwordx4 %1, %11, %12, %13 offen offset:512 sc1\n\tv_mfma_f32_16x16x32_bf16 %2, %14, %10, %2\n\tv_mul_f32 %3, %15, %15\n\tv_mul_f32 %4, %16, %16\n\tv_mfma_f32_16x16x32_bf16 %5, %17, %10, %5\n\tv_fmaak_f32 %6, %18, %6, 0x40135761\n\tv_fmaak_f32 %7, %18, %7, 0x40135761\n\tv_mfma_f32_16x16x32_bf16 %8, %19, %10, %8\n\tv_fmaak_f32 %3, %18, %3, 0x40135761"
      : "+v"(acc[2][1]), "=&v"(p[12]), "+v"(acc[2][2]), "=&v"(gz[2]), "=&v"(gz[3]), "+v"(acc[2][3]), "+v"(gz[0]), "+v"(gz[1]), "+v"(acc[2][0]) : "a"(fw1[9][1]), "v"(p[13]), "v"(vHc), "s"(rsW), "s"(sh[6]), "a"(fw1[9][2]), "v"(gm[2]), "v"(gm[3]), "a"(fw1[9][3]), "v"(gc), "a"(fw1[9][0]) : "memory"); SB();
  WAITVB(12);
  SB();
  asm volatile("v_mfma_f32_16x16x32_bf16 %0, %9, %10, %0\n\tbuffer_load_dwordx4 %1, %11, %12, %13 offen offset:512 sc1\n\tv_mfma_f32_16x16x32_bf16 %2, %14, %10, %2\n\tv_fmaak_f32 %3, %15, %3, 0x40135761\n\tv_mul_f32 %4, %4, %16\n\tv_mfma_f32_16x16x32_bf16 %5, %17, %10, %5\n\tv_mul_f32 %6, %6, %18\n\tv_mul_f32 %7, %7, %19\n\tv_mfma_f32_16x16x32_bf16 %8, %20, %10, %8\n\tv_mul_f32 %3, %3, %21"
      : "+v"(acc[2][1]), "=&v"(p[13]), "+v"(acc[2][2]), "+v"(gz[3]), "+v"(gz[0]), "+v"(acc[2][3]), "+v"(gz[1]), "+v"(gz[2]), "+v"(acc[2][0]) : "a"(fw1[10][1]), "v"(p[0]), "v"(vHc), "s"(rsW), "s"(sh[7]), "a"(fw1[10][2]), "v"(gc), "v"(gm[0]), "a"(fw1[10][3]), "v"(gm[1]), "v"(gm[2]), "a"(fw1[10][0]), "v"(gm[3]) : "memory"); SB();
  WAITVB(12);
  SB();
  asm volatile("v_mfma_f32_16x16x32_bf16 %0, %7, %8, %0\n\tbuffer_load_dwordx4 %1, %9, %10, %11 offen offset:512 sc1\n\tv_mfma_f32_16x16x32_bf16 %2, %12, %8, %2\n\tv_exp_f32 %3, %3\n\tv_mfma_f32_16x16x32_bf16 %4, %13, %8, %4\n\tv_exp_f32 %5, %5\n\tv_mfma_f32_16x16x32_bf16 %6, %14, %8, %6"
      : "+v"(acc[2][1]), "=&v"(p[0]), "+v"(acc[2][2]), "+v"(gz[0]), "+v"(acc[2][3]), "+v"(gz[1]), "+v"(acc[2][0]) : "a"(fw1[11][1]), "v"(p[1]), "v"(vHc), "s"(rsW), "s"(sh[8]), "a"(fw1[11][2]), "a"(fw1[11][3]), "a"(fw1[11][0]) : "memory"); SB();
  WAITVB(12);
  SB();
  asm volatile("v_mfma_f32_16x16x32_bf16 %0, %8, %9, %0\n\tbuffer_load_dwordx4 %1, %10, %11, %12 offen offset:512 sc1\n\tv_mfma_f32_16x16x32_bf16 %2, %13, %9, %2\n\tv_exp_f32 %3, %3\n\tv_mfma_f32_16x16x32_bf16 %4, %14, %9, %4\n\tv_exp_f32 %5, %5\n\tv_mfma_f32_16x16x32_bf16 %6, %15, %9, %6\n\tv_add_f32 %7, 1.0, %7"
      : "+v"(acc[2][1]), "=&v"(p[1]), "+v"(acc[2][2]), "+v"(gz[2]), "+v"(acc[2][3]), "+v"(gz[3]), "+v"(acc[2][0]), "+v"(gz[0]) : "a"(fw1[12][1]), "v"(p[2]), "v"(vHc), "s"(rsW), "s"(sh[9]), "a"(fw1[12][2]), "a"(fw1[12][3]), "a"(fw1[12][0]) : "memory"); SB();
  WAITVB(12);
  SB();
  asm volatile("v_mfma_f32_16x16x32_bf16 %0, %8, %9, %0\n\tbuffer_load_dwordx4 %1, %10, %11, %12 offen offset:512 sc1\n\tv_mfma_f32_16x16x32_bf16 %2, %13, %9, %2\n\tv_add_f32 %3, 1.0, %3\n\tv_add_f32 %4, 1.0, %4\n\tv_mfma_f32_16x16x32_bf16 %5, %14, %9, %5\n\tv_add_f32 %6, 1.0, %6\n\tv_mfma_f32_16x16x32_bf16 %7, %15, %9, %7"
      : "+v"(acc[2][1]), "=&v"(p[2]), "+v"(acc[2][2]), "+v"(gz[1]), "+v"(gz[2]), "+v"(acc[2][3]), "+v"(gz[3]), "+v"(acc[2][0]) : "a"(fw1[13][1]), "v"(p[3]), "v"(vHc), "s"(rsW), "s"(sh[10]), "a"(fw1[13][2]), "a"(fw1[13][3]), "a"(fw1[13][0]) : "memory"); SB();
  WAITVB(12);
  SB();
  asm volatile("v_mfma_f32_16x16x32_bf16 %0, %7, %8, %0\n\tbuffer_load_dwordx4 %1, %9, %10, %11 offen offset:512 sc1\n\tv_mfma_f32_16x16x32_bf16 %2, %12, %8, %2\n\tv_rcp_f32 %3, %3\n\tv_mfma_f32_16x16x32_bf16 %4, %13, %8, %4\n\tv_rcp_f32 %5, %5\n\tv_mfma_f32_16x16x32_bf16 %6, %14, %8, %6"
      : "+v"(acc[2][1]), "=&v"(p[3]), "+v"(acc[2][2]), "+v"(gz[0]), "+v"(acc[2][3]), "+v"(gz[1]), "+v"(acc[2][0]) : "a"(fw1[14][1]), "v"(p[4]), "v"(vHc), "s"(rsW), "s"(sh[11]), "a"(fw1[14][2]), "a"(fw1[14][3]), "a"(fw1[14][0]) : "memory"); SB();
  WAITVB(12);
  SB();
  asm volatile("v_mfma_f32_16x16x32_bf16 %0, %9, %10, %0\n\tbuffer_load_dwordx4 %1, %11, %12, %13 offen offset:512 sc1\n\tv_mfma_f32_16x16x32_bf16 %2, %14, %10, %2\n\tv_rcp_f32 %3, %3\n\tv_mfma_f32_16x16x32_bf16 %4, %15, %10, %4\n\tv_rcp_f32 %5, %5\n\tv_mfma_f32_16x16x32_bf16 %6, %16, %10, %6\n\tv_fma_f32 %7, -%17, %7, %17\n\tv_fma_f32 %8, -%18, %8, %18"
      : "+v"(acc[2][1]), "=&v"(p[4]), "+v"(acc[2][2]), "+v"(gz[2]), "+v"(acc[2][3]), "+v"(gz[3]), "+v"(acc[2][0]), "+v"(gz[0]), "+v"(gz[1]) : "v"(fw1[15][1]), "v"(p[5]), "v"(vHc), "s"(rsW), "s"(sh[12]), "v"(fw1[15][2]), "v"(fw1[15][3]), "v"(fw1[15][0]), "v"(gm[0]), "v"(gm[1]) : "memory"); SB();
  WAITVB(12);
  SB();
  asm volatile("v_mfma_f32_16x16x32_bf16 %0, %9, %10, 0\n\tbuffer_load_dwordx4 %1, %11, %12, %13 offen offset:512 sc1\n\tv_mfma_f32_16x16x32_bf16 %2, %14, %10, 0\n\tv_fma_f32 %3, -%15, %3, %15\n\tv_fma_f32 %4, -%16, %4, %16\n\tv_mfma_f32_16x16x32_bf16 %5, %17, %10, 0\n\tv_cvt_pk_bf16_f32 %6, %18, %19\n\tv_cvt_pk_bf16_f32 %7, %3, %4\n\tv_mfma_f32_16x16x32_bf16 %8, %20, %10, %21"
      : "=&v"(acc[3][1]), "=&v"(p[5]), "=&v"(acc[3][2]), "+v"(gz[2]), "+v"(gz[3]), "=&v"(acc[3][3]), "=&v"(uv[1][0]), "=&v"(uv[1][1]), "=&v"(acc[3][0]) : "a"(fw1[0][1]), "v"(p[6]), "v"(vHc), "s"(rsW), "s"(sh[13]), "a"(fw1[0][2]), "v"(gm[2]), "v"(gm[3]), "a"(fw1[0][3]), "v"(gz[0]), "v"(gz[1]), "a"(fw1[0][0]), "v"(bb) : "memory"); SB();
  WAITVB(12);
  SB();
  asm volatile("v_mfma_f32_16x16x32_bf16 %0, %6, %7, %0\n\tbuffer_load_dwordx4 %1, %8, %9, %10 offen offset:512 sc1\n\tv_mfma_f32_16x16x32_bf16 %2, %11, %7, %2\n\tds_write_b128 %12, %13 offset:1024\n\tv_mfma_f32_16x16x32_bf16 %3, %14, %7, %3\n\tbuffer_load_dword %4, %15, %9, 0 offen sc1\n\tv_mfma_f32_16x16x32_bf16 %5, %16, %7, %5"
      : "+v"(acc[3][1]), "=&v"(p[6]), "+v"(acc[3][2]), "+v"(acc[3][3]), "=&v"(fq), "+v"(acc[3][0]) : "a"(fw1[1][1]), "v"(p[7]), "v"(vHc), "s"(rsW), "s"(sh[14]), "a"(fw1[1][2]), "v"(rw), "v"(acc[2][1]), "a"(fw1[1][3]), "v"(vFQ), "a"(fw1[1][0]) : "memory"); SB();
  WAITVB(13);
  SB();
  asm volatile("v_mfma_f32_16x16x32_bf16 %0, %5, %6, %0\n\tbuffer_load_dwordx4 %1, %7, %8, %9 offen offset:512 sc1\n\tv_mfma_f32_16x16x32_bf16 %2, %10, %6, %2\n\tds_write_b128 %11, %12 offset:2048\n\tv_mfma_f32_16x16x32_bf16 %3, %13, %6, %3\n\tv_mfma_f32_16x16x32_bf16 %4, %14, %6, %4"
      : "+v"(acc[3][1]), "=&v"(p[7]), "+v"(acc[3][2]), "+v"(acc[3][3]), "+v"(acc[3][0]) : "a"(fw1[2][1]), "v"(p[8]), "v"(vHc), "s"(rsW), "s"(sh[15]), "a"(fw1[2][2]), "v"(rw), "v"(acc[2][2]), "a"(fw1[2][3]), "a"(fw1[2][0]) : "memory"); SB();
  WAITVB(13);
  SB();
  asm volatile("v_mfma_f32_16x16x32_bf16 %0, %5, %6, %0\n\tbuffer_load_dwordx4 %1, %7, %8, %9 offen offset:256 sc1\n\tv_mfma_f32_16x16x32_bf16 %2, %10, %6, %2\n\tds_write_b128 %11, %12 offset:3072\n\tv_mfma_f32_16x16x32_bf16 %3, %13, %6, %3\n\tv_mfma_f32_16x16x32_bf16 %4, %14, %6, %4"
      : "+v"(acc[3][1]), "=&v"(p[8]), "+v"(acc[3][2]), "+v"(acc[3][3]), "+v"(acc[3][0]) : "a"(fw1[3][1]), "v"(p[9]), "v"(vHa), "s"(rsW), "s"(sh[0]), "a"(fw1[3][2]), "v"(rw), "v"(acc[2][3]), "a"(fw1[3][3]), "a"(fw1[3][0]) : "memory"); SB();
  WAITVB(13);
  SB();
  asm volatile("v_mfma_f32_16x16x32_bf16 %0, %3, %4, %0\n\tbuffer_load_dwordx4 %1, %5, %6, %7 offen offset:256 sc1\n\tv_mfma_f32_16x16x32_bf16 %2, %8, %4, %2"
      : "+v"(acc[3][1]), "=&v"(p[9]), "+v"(acc[3][2]) : "a"(fw1[4][1]), "v"(p[10]), "v"(vHa), "s"(rsW), "s"(sh[1]), "a"(fw1[4][2]) : "memory"); SB();
  BAR();
  SB();
  asm volatile("v_mfma_f32_16x16x32_bf16 %0, %5, %6, %0\n\tds_read_b128 %1, %7 offset:1024\n\tds_read_b128 %2, %8 offset:2048\n\tv_mfma_f32_16x16x32_bf16 %3, %9, %6, %3\n\tds_read_b128 %4, %10 offset:3072"
      : "+v"(acc[3][3]), "=&v"(pm[2][0]), "=&v"(pm[2][1]), "+v"(acc[3][0]), "=&v"(pm[2][2]) : "a"(fw1[4][3]), "v"(p[10]), "v"(rr1), "v"(rr2), "a"(fw1[4][0]), "v"(rr3) : "memory"); SB();
  WAITVB(13);
  SB();
  asm volatile("v_mfma_f32_16x16x32_bf16 %0, %5, %6, %0\n\tbuffer_load_dwordx4 %1, %7, %8, %9 offen offset:256 sc1\n\tv_mfma_f32_16x16x32_bf16 %2, %10, %6, %2\n\tv_mfma_f32_16x16x32_bf16 %3, %11, %6, %3\n\tv_mfma_f32_16x16x32_bf16 %4, %12, %6, %4"
      : "+v"(acc[3][1]), "=&v"(p[10]), "+v"(acc[3][2]), "+v"(acc[3][3]), "+v"(acc[3][0]) : "a"(fw1[5][1]), "v"(p[11]), "v"(vHa), "s"(rsW), "s"(sh[2]), "a"(fw1[5][2]), "a"(fw1[5][3]), "a"(fw1[5][0]) : "memory"); SB();
  WAITVB(13);
  SB();
  asm volatile("v_mfma_f32_16x16x32_bf16 %0, %3, %4, %0\n\tbuffer_load_dwordx4 %1, %5, %6, %7 offen offset:256 sc1\n\tv_mfma_f32_16x16x32_bf16 %2, %8, %4, %2"
      : "+v"(acc[3][1]), "=&v"(p[11]), "+v"(acc[3][2]) : "a"(fw1[6][1]), "v"(p[12]), "v"(vHa), "s"(rsW), "s"(sh[3]), "a"(fw1[6][2]) : "memory"); SB();
  WAITL(0);
  asm volatile("v_add_f32 %0, %6, %7\n\tv_mfma_f32_16x16x32_bf16 %1, %8, %9, %1\n\tv_add_f32 %2, %10, %11\n\tv_add_f32 %3, %12, %13\n\tv_mfma_f32_16x16x32_bf16 %4, %14, %9, %4\n\tv_add_f32 %5, %15, %16"
      : "=&v"(gm[0]), "+v"(acc[3][3]), "=&v"(gm[1]), "=&v"(gm[2]), "+v"(acc[3][0]), "=&v"(gm[3]) : "v"(acc[2][0][0]), "v"(pm[2][0][0]), "a"(fw1[6][3]), "v"(p[12]), "v"(acc[2][0][1]), "v"(pm[2][0][1]), "v"(acc[2][0][2]), "v"(pm[2][0][2]), "a"(fw1[6][0]), "v"(acc[2][0][3]), "v"(pm[2][0][3])); SB();
  WAITVB(13);
  SB();
  asm volatile("v_mfma_f32_16x16x32_bf16 %0, %9, %10, %0\n\tbuffer_load_dwordx4 %1, %11, %12, %13 offen offset:256 sc1\n\tv_mfma_f32_16x16x32_bf16 %2, %14, %10, %2\n\tv_add_f32 %3, %15, %16\n\tv_add_f32 %4, %17, %18\n\tv_mfma_f32_16x16x32_bf16 %5, %19, %10, %5\n\tv_add_f32 %6, %20, %21\n\tv_add_f32 %7, %22, %23\n\tv_mfma_f32_16x16x32_bf16 %8, %24, %10, %8"
      : "+v"(acc[3][1]), "=&v"(p[12]), "+v"(acc[3][2]), "=&v"(gz[0]), "=&v"(gz[1]), "+v"(acc[3][3]), "=&v"(gz[2]), "=&v"(gz[3]), "+v"(acc[3][0]) : "a"(fw1[7][1]), "v"(p[13]), "v"(vHa), "s"(rsW), "s"(sh[4]), "a"(fw1[7][2]), "v"(pm[2][1][0]), "v"(pm[2][2][0]), "v"(pm[2][1][1]), "v"(pm[2][2][1]), "a"(fw1[7][3]), "v"(pm[2][1][2]), "v"(pm[2][2][2]), "v"(pm[2][1][3]), "v"(pm[2][2][3]), "a"(fw1[7][0]) : "memory"); SB();
  asm volatile("v_add_f32 %0, %0, %1"
      : "+v"(gm[0]) : "v"(gz[0])); SB();
  WAITVB(13);
  SB();
  asm volatile("v_mfma_f32_16x16x32_bf16 %0, %10, %11, %0\n\tbuffer_load_dwordx4 %1, %12, %13, %14 offen offset:256 sc1\n\tv_mfma_f32_16x16x32_bf16 %2, %15, %11, %2\n\tv_add_f32 %3, %3, %4\n\tv_add_f32 %5, %5, %16\n\tv_mfma_f32_16x16x32_bf16 %6, %17, %11, %6\n\tv_add_f32 %7, %7, %18\n\tv_mul_f32 %8, %19, %19\n\tv_mfma_f32_16x16x32_bf16 %9, %20, %11, %9\n\tv_mul_f32 %4, %3, %3"
      : "+v"(acc[3][1]), "=&v"(p[13]), "+v"(acc[3][2]), "+v"(gm[1]), "+v"(gz[1]), "+v"(gm[2]), "+v"(acc[3][3]), "+v"(gm[3]), "=&v"(gz[0]), "+v"(acc[3][0]) : "a"(fw1[8][1]), "v"(p[0]), "v"(vHa), "s"(rsW), "s"(sh[5]), "a"(fw1[8][2]), "v"(gz[2]), "a"(fw1[8][3]), "v"(gz[3]), "v"(gm[0]), "a"(fw1[8][0]) : "memory"); SB();
  WAITVB(13);
  SB();
  asm volatile("v_mfma_f32_16x16x32_bf16 %0, %9, %10, %0\n\tbuffer_load_dwordx4 %1, %11, %12, %13 offen offset:256 sc1\n\tv_mfma_f32_16x16x32_bf16 %2, %14, %10, %2\n\tv_mul_f32 %3, %15, %15\n\tv_mul_f32 %4, %16, %16\n\tv_mfma_f32_16x16x32_bf16 %5, %17, %10, %5\n\tv_fmaak_f32 %6, %18, %6, 0x40135761\n\tv_fmaak_f32 %7, %18, %7, 0x40135761\n\tv_mfma_f32_16x16x32_bf16 %8, %19, %10, %8\n\tv_fmaak_f32 %3, %18, %3, 0x40135761"
      : "+v"(acc[3][1]), "=&v"(p[0]), "+v"(acc[3][2]), "=&v"(gz[2]), "=&v"(gz[3]), "+v"(acc[3][3]), "+v"(gz[0]), "+v"(gz[1]), "+v"(acc[3][0]) : "a"(fw1[9][1]), "v"(p[1]), "v"(vHa), "s"(rsW), "s"(sh[6]), "a"(fw1[9][2]), "v"(gm[2]), "v"(gm[3]), "a"(fw1[9][3]), "v"(gc), "a"(fw1[9][0]) : "memory"); SB();
  WAITVB(13);
  SB();
  asm volatile("v_mfma_f32_16x16x32_bf16 %0, %9, %10, %0\n\tbuffer_load_dwordx4 %1, %11, %12, %13 offen offset:256 sc1\n\tv_mfma_f32_16x16x32_bf16 %2, %14, %10, %2\n\tv_fmaak_f32 %3, %15, %3, 0x40135761\n\tv_mul_f32 %4, %4, %16\n\tv_mfma_f32_16x16x32_bf16 %5, %17, %10, %5\n\tv_mul_f32 %6, %6, %18\n\tv_mul_f32 %7, %7, %19\n\tv_mfma_f32_16x16x32_bf16 %8, %20, %10, %8\n\tv_mul_f32 %3, %3, %21"
      : "+v"(acc[3][1]), "=&v"(p[1]), "+v"(acc[3][2]), "+v"(gz[3]), "+v"(gz[0]), "+v"(acc[3][3]), "+v"(gz[1]), "+v"(gz[2]), "+v"(acc[3][0]) : "a"(fw1[10][1]), "v"(p[2]), "v"(vHa), "s"(rsW), "s"(sh[7]), "a"(fw1[10][2]), "v"(gc), "v"(gm[0]), "a"(fw1[10][3]), "v"(gm[1]), "v"(gm[2]), "a"(fw1[10][0]), "v"(gm[3]) : "memory"); SB();
  WAITVB(13);
  SB();
  asm volatile("v_mfma_f32_16x16x32_bf16 %0, %7, %8, %0\n\tbuffer_load_dwordx4 %1, %9, %10, %11 offen offset:256 sc1\n\tv_mfma_f32_16x16x32_bf16 %2, %12, %8, %2\n\tv_exp_f32 %3, %3\n\tv_mfma_f32_16x16x32_bf16 %4, %13, %8, %4\n\tv_exp_f32 %5, %5\n\tv_mfma_f32_16x16x32_bf16 %6, %14, %8, %6"
      : "+v"(acc[3][1]), "=&v"(p[2]), "+v"(acc[3][2]), "+v"(gz[0]), "+v"(acc[3][3]), "+v"(gz[1]), "+v"(acc[3][0]) : "a"(fw1[11][1]), "v"(p[3]), "v"(vHa), "s"(rsW), "s"(sh[8]), "a"(fw1[11][2]), "a"(fw1[11][3]), "a"(fw1[11][0]) : "memory"); SB();
  WAITVB(13);
  SB();
  asm volatile("v_mfma_f32_16x16x32_bf16 %0, %8, %9, %0\n\tbuffer_load_dwordx4 %1, %10, %11, %12 offen offset:256 sc1\n\tv_mfma_f32_16x16x32_bf16 %2, %13, %9, %2\n\tv_exp_f32 %3, %3\n\tv_mfma_f32_16x16x32_bf16 %4, %14, %9, %4\n\tv_exp_f32 %5, %5\n\tv_mfma_f32_16x16x32_bf16 %6, %15, %9, %6\n\tv_add_f32 %7, 1.0, %7"
      : "+v"(acc[3][1]), "=&v"(p[3]), "+v"(acc[3][2]), "+v"(gz[2]), "+v"(acc[3][3]), "+v"(gz[3]), "+v"(acc[3][0]), "+v"(gz[0]) : "a"(fw1[12][1]), "v"(p[4]), "v"(vHa), "s"(rsW), "s"(sh[9]), "a"(fw1[12][2]), "a"(fw1[12][3]), "a"(fw1[12][0]) : "memory"); SB();
  WAITVB(13);
  SB();
  asm volatile("v_mfma_f32_16x16x32_bf16 %0, %8, %9, %0\n\tbuffer_load_dwordx4 %1, %10, %11, %12 offen offset:256 sc1\n\tv_mfma_f32_16x16x32_bf16 %2, %13, %9, %2\n\tv_add_f32 %3, 1.0, %3\n\tv_add_f32 %4, 1.0, %4\n\tv_mfma_f32_16x16x32_bf16 %5, %14, %9, %5\n\tv_add_f32 %6, 1.0, %6\n\tv_mfma_f32_16x16x32_bf16 %7, %15, %9, %7"
      : "+v"(acc[3][1]), "=&v"(p[4]), "+v"(acc[3][2]), "+v"(gz[1]), "+v"(gz[2]), "+v"(acc[3][3]), "+v"(gz[3]), "+v"(acc[3][0]) : "a"(fw1[13][1]), "v"(p[5]), "v"(vHa), "s"(rsW), "s"(sh[10]), "a"(fw1[13][2]), "a"(fw1[13][3]), "a"(fw1[13][0]) : "memory"); SB();
  WAITVB(13);
  SB();
  asm volatile("v_mfma_f32_16x16x32_bf16 %0, %7, %8, %0\n\tbuffer_load_dwordx4 %1, %9, %10, %11 offen offset:256 sc1\n\tv_mfma_f32_16x16x32_bf16 %2, %12, %8, %2\n\tv_rcp_f32 %3, %3\n\tv_mfma_f32_16x16x32_bf16 %4, %13, %8, %4\n\tv_rcp_f32 %5, %5\n\tv_mfma_f32_16x16x32_bf16 %6, %14, %8, %6"
      : "+v"(acc[3][1]), "=&v"(p[5]), "+v"(acc[3][2]), "+v"(gz[0]), "+v"(acc[3][3]), "+v"(gz[1]), "+v"(acc[3][0]) : "a"(fw1[14][1]), "v"(p[6]), "v"(vHa), "s"(rsW), "s"(sh[11]), "a"(fw1[14][2]), "a"(fw1[14][3]), "a"(fw1[14][0]) : "memory"); SB();
  WAITVB(12);
  SB();
  asm volatile("v_mfma_f32_16x16x32_bf16 %0, %9, %10, %0\n\tbuffer_load_dwordx4 %1, %11, %12, %13 offen offset:256 sc1\n\tv_mfma_f32_16x16x32_bf16 %2, %14, %10, %2\n\tv_rcp_f32 %3, %3\n\tv_mfma_f32_16x16x32_bf16 %4, %15, %10, %4\n\tv_rcp_f32 %5, %5\n\tv_mfma_f32_16x16x32_bf16 %6, %16, %10, %6\n\tv_fma_f32 %7, -%17, %7, %17\n\tv_fma_f32 %8, -%18, %8, %18"
      : "+v"(acc[3][1]), "=&v"(p[6]), "+v"(acc[3][2]), "+v"(gz[2]), "+v"(acc[3][3]), "+v"(gz[3]), "+v"(acc[3][0]), "+v"(gz[0]), "+v"(gz[1]) : "v"(fw1[15][1]), "v"(p[7]), "v"(vHa), "s"(rsW), "s"(sh[12]), "v"(fw1[15][2]), "v"(fw1[15][3]), "v"(fw1[15][0]), "v"(gm[0]), "v"(gm[1]) : "memory"); SB();
  STAMP(9);
  WAITVB(12);
  SB();
  asm volatile("v_mfma_f32_16x16x32_bf16 %0, %9, %10, 0\n\tbuffer_load_dwordx4 %1, %11, %12, %13 offen offset:256 sc1\n\tv_mfma_f32_16x16x32_bf16 %2, %14, %10, 0\n\tv_fma_f32 %3, -%15, %3, %15\n\tv_fma_f32 %4, -%16, %4, %16\n\tv_mfma_f32_16x16x32_bf16 %5, %17, %10, 0\n\tv_cvt_pk_bf16_f32 %6, %18, %19\n\tv_cvt_pk_bf16_f32 %7, %3, %4\n\tv_mfma_f32_16x16x32_bf16 %8, %20, %10, %21"
      : "=&v"(acc[4][1]), "=&v"(p[7]), "=&v"(acc[4][2]), "+v"(gz[2]), "+v"(gz[3]), "=&v"(acc[4][3]), "=&v"(uv[2][0]), "=&v"(uv[2][1]), "=&v"(acc[4][0]) : "a"(fw1[0][1]), "v"(p[8]), "v"(vHa), "s"(rsW), "s"(sh[13]), "a"(fw1[0][2]), "v"(gm[2]), "v"(gm[3]), "a"(fw1[0][3]), "v"(gz[0]), "v"(gz[1]), "a"(fw1[0][0]), "v"(bb) : "memory"); SB();
  WAITVB(12);
  SB();
  asm volatile("v_mfma_f32_16x16x32_bf16 %0, %5, %6, %0\n\tbuffer_load_dwordx4 %1, %7, %8, %9 offen offset:256 sc1\n\tv_mfma_f32_16x16x32_bf16 %2, %10, %6, %2\n\tds_write_b128 %11, %12 offset:5120\n\tv_mfma_f32_16x16x32_bf16 %3, %13, %6, %3\n\tv_mfma_f32_16x16x32_bf16 %4, %14, %6, %4"
      : "+v"(acc[4][1]), "=&v"(p[8]), "+v"(acc[4][2]), "+v"(acc[4][3]), "+v"(acc[4][0]) : "a"(fw1[1][1]), "v"(p[9]), "v"(vHa), "s"(rsW), "s"(sh[14]), "a"(fw1[1][2]), "v"(rw), "v"(acc[3][1]), "a"(fw1[1][3]), "a"(fw1[1][0]) : "memory"); SB();
  WAITVB(12);
  SB();
  asm volatile("v_mfma_f32_16x16x32_bf16 %0, %5, %6, %0\n\tbuffer_load_dwordx4 %1, %7, %8, %9 offen offset:256 sc1\n\tv_mfma_f32_16x16x32_bf16 %2, %10, %6, %2\n\tds_write_b128 %11, %12 offset:6144\n\tv_mfma_f32_16x16x32_bf16 %3, %13, %6, %3\n\tv_mfma_f32_16x16x32_bf16 %4, %14, %6, %4"
      : "+v"(acc[4][1]), "=&v"(p[9]), "+v"(acc[4][2]), "+v"(acc[4][3]), "+v"(acc[4][0]) : "a"(fw1[2][1]), "v"(p[10]), "v"(vHa), "s"(rsW), "s"(sh[15]), "a"(fw1[2][2]), "v"(rw), "v"(acc[3][2]), "a"(fw1[2][3]), "a"(fw1[2][0]) : "memory"); SB();
  WAITVB(12);
  SB();
  asm volatile("v_mfma_f32_16x16x32_bf16 %0, %5, %6, %0\n\tbuffer_load_dwordx4 %1, %7, %8, %9 offen offset:256 sc1\n\tv_mfma_f32_16x16x32_bf16 %2, %10, %6, %2\n\tds_write_b128 %11, %12 offset:7168\n\tv_mfma_f32_16x16x32_bf16 %3, %13, %6, %3\n\tv_mfma_f32_16x16x32_bf16 %4, %14, %6, %4"
      : "+v"(acc[4][1]), "=&v"(p[10]), "+v"(acc[4][2]), "+v"(acc[4][3]), "+v"(acc[4][0]) : "a"(fw1[3][1]), "v"(p[11]), "v"(vHc), "s"(rsW), "s"(sh[0]), "a"(fw1[3][2]), "v"(rw), "v"(acc[3][3]), "a"(fw1[3][3]), "a"(fw1[3][0]) : "memory"); SB();
  WAITVB(12);
  SB();
  asm volatile("v_mfma_f32_16x16x32_bf16 %0, %3, %4, %0\n\tbuffer_load_dwordx4 %1, %5, %6, %7 offen offset:256 sc1\n\tv_mfma_f32_16x16x32_bf16 %2, %8, %4, %2"
      : "+v"(acc[4][1]), "=&v"(p[11]), "+v"(acc[4][2]) : "a"(fw1[4][1]), "v"(p[12]), "v"(vHc), "s"(rsW), "s"(sh[1]), "a"(fw1[4][2]) : "memory"); SB();
  BAR();
  SB();
  asm volatile("v_mfma_f32_16x16x32_bf16 %0, %5, %6, %0\n\tds_read_b128 %1, %7 offset:5120\n\tds_read_b128 %2, %8 offset:6144\n\tv_mfma_f32_16x16x32_bf16 %3, %9, %6, %3\n\tds_read_b128 %4, %10 offset:7168"
      : "+v"(acc[4][3]), "=&v"(pm[3][0]), "=&v"(pm[3][1]), "+v"(acc[4][0]), "=&v"(pm[3][2]) : "a"(fw1[4][3]), "v"(p[12]), "v"(rr1), "v"(rr2), "a"(fw1[4][0]), "v"(rr3) : "memory"); SB();
  WAITVB(12);
  SB();
  asm volatile("v_mfma_f32_16x16x32_bf16 %0, %5, %6, %0\n\tbuffer_load_dwordx4 %1, %7, %8, %9 offen offset:256 sc1\n\tv_mfma_f32_16x16x32_bf16 %2, %10, %6, %2\n\tv_mfma_f32_16x16x32_bf16 %3, %11, %6, %3\n\tv_mfma_f32_16x16x32_bf16 %4, %12, %6, %4"
      : "+v"(acc[4][1]), "=&v"(p[12]), "+v"(acc[4][2]), "+v"(acc[4][3]), "+v"(acc[4][0]) : "a"(fw1[5][1]), "v"(p[13]), "v"(vHc), "s"(rsW), "s"(sh[2]), "a"(fw1[5][2]), "a"(fw1[5][3]), "a"(fw1[5][0]) : "memory"); SB();
  WAITVB(12);
  SB();
  asm volatile("v_mfma_f32_16x16x32_bf16 %0, %3, %4, %0\n\tbuffer_load_dwordx4 %1, %5, %6, %7 offen offset:256 sc1\n\tv_mfma_f32_16x16x32_bf16 %2, %8, %4, %2"
      : "+v"(acc[4][1]), "=&v"(p[13]), "+v"(acc[4][2]) : "a"(fw1[6][1]), "v"(p[0]), "v"(vHc), "s"(rsW), "s"(sh[3]), "a"(fw1[6][2]) : "memory"); SB();
  WAITL(0);
  asm volatile("v_add_f32 %0, %6, %7\n\tv_mfma_f32_16x16x32_bf16 %1, %8, %9, %1\n\tv_add_f32 %2, %10, %11\n\tv_add_f32 %3, %12, %13\n\tv_mfma_f32_16x16x32_bf16 %4, %14, %9, %4\n\tv_add_f32 %5, %15, %16"
      : "=&v"(gm[0]), "+v"(acc[4][3]), "=&v"(gm[1]), "=&v"(gm[2]), "+v"(acc[4][0]), "=&v"(gm[3]) : "v"(acc[3][0][0]), "v"(pm[3][0][0]), "a"(fw1[6][3]), "v"(p[0]), "v"(acc[3][0][1]), "v"(pm[3][0][1]), "v"(acc[3][0][2]), "v"(pm[3][0][2]), "a"(fw1[6][0]), "v"(acc[3][0][3]), "v"(pm[3][0][3])); SB();
  WAITVB(12);
  SB();
  asm volatile("v_mfma_f32_16x16x32_bf16 %0, %9, %10, %0\n\tbuffer_load_dwordx4 %1, %11, %12, %13 offen offset:256 sc1\n\tv_mfma_f32_16x16x32_bf16 %2, %14, %10, %2\n\tv_add_f32 %3, %15, %16\n\tv_add_f32 %4, %17, %18\n\tv_mfma_f32_16x16x32_bf16 %5, %19, %10, %5\n\tv_add_f32 %6, %20, %21\n\tv_add_f32 %7, %22, %23\n\tv_mfma_f32_16x16x32_bf16 %8, %24, %10, %8"
      : "+v"(acc[4][1]), "=&v"(p[0]), "+v"(acc[4][2]), "=&v"(gz[0]), "=&v"(gz[1]), "+v"(acc[4][3]), "=&v"(gz[2]), "=&v"(gz[3]), "+v"(acc[4][0]) : "a"(fw1[7][1]), "v"(p[1]), "v"(vHc), "s"(rsW), "s"(sh[4]), "a"(fw1[7][2]), "v"(pm[3][1][0]), "v"(pm[3][2][0]), "v"(pm[3][1][1]), "v"(pm[3][2][1]), "a"(fw1[7][3]), "v"(pm[3][1][2]), "v"(pm[3][2][2]), "v"(pm[3][1][3]), "v"(pm[3][2][3]), "a"(fw1[7][0]) : "memory"); SB();
  asm volatile("v_add_f32 %0, %0, %1"
      : "+v"(gm[0]) : "v"(gz[0])); SB();
  WAITVB(12);
  SB();
  asm volatile("v_mfma_f32_16x16x32_bf16 %0, %10, %11, %0\n\tbuffer_load_dwordx4 %1, %12, %13, %14 offen offset:256 sc1\n\tv_mfma_f32_16x16x32_bf16 %2, %15, %11, %2\n\tv_add_f32 %3, %3, %4\n\tv_add_f32 %5, %5, %16\n\tv_mfma_f32_16x16x32_bf16 %6, %17, %11, %6\n\tv_add_f32 %7, %7, %18\n\tv_mul_f32 %8, %19, %19\n\tv_mfma_f32_16x16x32_bf16 %9, %20, %11, %9\n\tv_mul_f32 %4, %3, %3"
      : "+v"(acc[4][1]), "=&v"(p[1]), "+v"(acc[4][2]), "+v"(gm[1]), "+v"(gz[1]), "+v"(gm[2]), "+v"(acc[4][3]), "+v"(gm[3]), "=&v"(gz[0]), "+v"(acc[4][0]) : "a"(fw1[8][1]), "v"(p[2]), "v"(vHc), "s"(rsW), "s"(sh[5]), "a"(fw1[8][2]), "v"(gz[2]), "a"(fw1[8][3]), "v"(gz[3]), "v"(gm[0]), "a"(fw1[8][0]) : "memory"); SB();
  WAITVB(12);
  SB();
  asm volatile("v_mfma_f32_16x16x32_bf16 %0, %9, %10, %0\n\tbuffer_load_dwordx4 %1, %11, %12, %13 offen offset:256 sc1\n\tv_mfma_f32_16x16x32_bf16 %2, %14, %10, %2\n\tv_mul_f32 %3, %15, %15\n\tv_mul_f32 %4, %16, %16\n\tv_mfma_f32_16x16x32_bf16 %5, %17, %10, %5\n\tv_fmaak_f32 %6, %18, %6, 0x40135761\n\tv_fmaak_f32 %7, %18, %7, 0x40135761\n\tv_mfma_f32_16x16x32_bf16 %8, %19, %10, %8\n\tv_fmaak_f32 %3, %18, %3, 0x40135761"
      : "+v"(acc[4][1]), "=&v"(p[2]), "+v"(acc[4][2]), "=&v"(gz[2]), "=&v"(gz[3]), "+v"(acc[4][3]), "+v"(gz[0]), "+v"(gz[1]), "+v"(acc[4][0]) : "a"(fw1[9][1]), "v"(p[3]), "v"(vHc), "s"(rsW), "s"(sh[6]), "a"(fw1[9][2]), "v"(gm[2]), "v"(gm[3]), "a"(fw1[9][3]), "v"(gc), "a"(fw1[9][0]) : "memory"); SB();
  WAITVB(12);
  SB();
  asm volatile("v_mfma_f32_16x16x32_bf16 %0, %9, %10, %0\n\tbuffer_load_dwordx4 %1, %11, %12, %13 offen offset:256 sc1\n\tv_mfma_f32_16x16x32_bf16 %2, %14, %10, %2\n\tv_fmaak_f32 %3, %15, %3, 0x40135761\n\tv_mul_f32 %4, %4, %16\n\tv_mfma_f32_16x16x32_bf16 %5, %17, %10, %5\n\tv_mul_f32 %6, %6, %18\n\tv_mul_f32 %7, %7, %19\n\tv_mfma_f32_16x16x32_bf16 %8, %20, %10, %8\n\tv_mul_f32 %3, %3, %21"
      : "+v"(acc[4][1]), "=&v"(p[3]), "+v"(acc[4][2]), "+v"(gz[3]), "+v"(gz[0]), "+v"(acc[4][3]), "+v"(gz[1]), "+v"(gz[2]), "+v"(acc[4][0]) : "a"(fw1[10][1]), "v"(p[4]), "v"(vHc), "s"(rsW), "s"(sh[7]), "a"(fw1[10][2]), "v"(gc), "v"(gm[0]), "a"(fw1[10][3]), "v"(gm[1]), "v"(gm[2]), "a"(fw1[10][0]), "v"(gm[3]) : "memory"); SB();
  WAITVB(12);
  SB();
  asm volatile("v_mfma_f32_16x16x32_bf16 %0, %7, %8, %0\n\tbuffer_load_dwordx4 %1, %9, %10, %11 offen offset:256 sc1\n\tv_mfma_f32_16x16x32_bf16 %2, %12, %8, %2\n\tv_exp_f32 %3, %3\n\tv_mfma_f32_16x16x32_bf16 %4, %13, %8, %4\n\tv_exp_f32 %5, %5\n\tv_mfma_f32_16x16x32_bf16 %6, %14, %8, %6"
      : "+v"(acc[4][1]), "=&v"(p[4]), "+v"(acc[4][2]), "+v"(gz[0]), "+v"(acc[4][3]), "+v"(gz[1]), "+v"(acc[4][0]) : "a"(fw1[11][1]), "v"(p[5]), "v"(vHc), "s"(rsW), "s"(sh[8]), "a"(fw1[11][2]), "a"(fw1[11][3]), "a"(fw1[11][0]) : "memory"); SB();
  WAITVB(12);
  SB();
  asm volatile("v_mfma_f32_16x16x32_bf16 %0, %8, %9, %0\n\tbuffer_load_dwordx4 %1, %10, %11, %12 offen offset:256 sc1\n\tv_mfma_f32_16x16x32_bf16 %2, %13, %9, %2\n\tv_exp_f32 %3, %3\n\tv_mfma_f32_16x16x32_bf16 %4, %14, %9, %4\n\tv_exp_f32 %5, %5\n\tv_mfma_f32_16x16x32_bf16 %6, %15, %9, %6\n\tv_add_f32 %7, 1.0, %7"
      : "+v"(acc[4][1]), "=&v"(p[5]), "+v"(acc[4][2]), "+v"(gz[2]), "+v"(acc[4][3]), "+v"(gz[3]), "+v"(acc[4][0]), "+v"(gz[0]) : "a"(fw1[12][1]), "v"(p[6]), "v"(vHc), "s"(rsW), "s"(sh[9]), "a"(fw1[12][2]), "a"(fw1[12][3]), "a"(fw1[12][0]) : "memory"); SB();
  WAITVB(12);
  SB();
  asm volatile("v_mfma_f32_16x16x32_bf16 %0, %8, %9, %0\n\tbuffer_load_dwordx4 %1, %10, %11, %12 offen offset:256 sc1\n\tv_mfma_f32_16x16x32_bf16 %2, %13, %9, %2\n\tv_add_f32 %3, 1.0, %3\n\tv_add_f32 %4, 1.0, %4\n\tv_mfma_f32_16x16x32_bf16 %5, %14, %9, %5\n\tv_add_f32 %6, 1.0, %6\n\tv_mfma_f32_16x16x32_bf16 %7, %15, %9, %7"
      : "+v"(acc[4][1]), "=&v"(p[6]), "+v"(acc[4][2]), "+v"(gz[1]), "+v"(gz[2]), "+v"(acc[4][3]), "+v"(gz[3]), "+v"(acc[4][0]) : "a"(fw1[13][1]), "v"(p[7]), "v"(vHc), "s"(rsW), "s"(sh[10]), "a"(fw1[13][2]), "a"(fw1[13][3]), "a"(fw1[13][0]) : "memory"); SB();
  WAITVB(12);
  SB();
  asm volatile("v_mfma_f32_16x16x32_bf16 %0, %7, %8, %0\n\tbuffer_load_dwordx4 %1, %9, %10, %11 offen offset:256 sc1\n\tv_mfma_f32_16x16x32_bf16 %2, %12, %8, %2\n\tv_rcp_f32 %3, %3\n\tv_mfma_f32_16x16x32_bf16 %4, %13, %8, %4\n\tv_rcp_f32 %5, %5\n\tv_mfma_f32_16x16x32_bf16 %6, %14, %8, %6"
      : "+v"(acc[4][1]), "=&v"(p[7]), "+v"(acc[4][2]), "+v"(gz[0]), "+v"(acc[4][3]), "+v"(gz[1]), "+v"(acc[4][0]) : "a"(fw1[14][1]), "v"(p[8]), "v"(vHc), "s"(rsW), "s"(sh[11]), "a"(fw1[14][2]), "a"(fw1[14][3]), "a"(fw1[14][0]) : "memory"); SB();
  WAITVB(12);
  SB();
  asm volatile("v_mfma_f32_16x16x32_bf16 %0, %9, %10, %0\n\tbuffer_load_dwordx4 %1, %11, %12, %13 offen offset:256 sc1\n\tv_mfma_f32_16x16x32_bf16 %2, %14, %10, %2\n\tv_rcp_f32 %3, %3\n\tv_mfma_f32_16x16x32_bf16 %4, %15, %10, %4\n\tv_rcp_f32 %5, %5\n\tv_mfma_f32_16x16x32_bf16 %6, %16, %10, %6\n\tv_fma_f32 %7, -%17, %7, %17\n\tv_fma_f32 %8, -%18, %8, %18"
      : "+v"(acc[4][1]), "=&v"(p[8]), "+v"(acc[4][2]), "+v"(gz[2]), "+v"(acc[4][3]), "+v"(gz[3]), "+v"(acc[4][0]), "+v"(gz[0]), "+v"(gz[1]) : "v"(fw1[15][1]), "v"(p[9]), "v"(vHc), "s"(rsW), "s"(sh[12]), "v"(fw1[15][2]), "v"(fw1[15][3]), "v"(fw1[15][0]), "v"(gm[0]), "v"(gm[1]) : "memory"); SB();
  WAITVB(12);
  SB();
  asm volatile("v_mfma_f32_16x16x32_bf16 %0, %9, %10, 0\n\tbuffer_load_dwordx4 %1, %11, %12, %13 offen offset:256 sc1\n\tv_mfma_f32_16x16x32_bf16 %2, %14, %10, 0\n\tv_fma_f32 %3, -%15, %3, %15\n\tv_fma_f32 %4, -%16, %4, %16\n\tv_mfma_f32_16x16x32_bf16 %5, %17, %10, 0\n\tv_cvt_pk_bf16_f32 %6, %18, %19\n\tv_cvt_pk_bf16_f32 %7, %3, %4\n\tv_mfma_f32_16x16x32_bf16 %8, %20, %10, %21"
      : "=&v"(acc[5][1]), "=&v"(p[9]), "=&v"(acc[5][2]), "+v"(gz[2]), "+v"(gz[3]), "=&v"(acc[5][3]), "=&v"(uv[3][0]), "=&v"(uv[3][1]), "=&v"(acc[5][0]) : "a"(fw1[0][1]), "v"(p[10]), "v"(vHc), "s"(rsW), "s"(sh[13]), "a"(fw1[0][2]), "v"(gm[2]), "v"(gm[3]), "a"(fw1[0][3]), "v"(gz[0]), "v"(gz[1]), "a"(fw1[0][0]), "v"(bb) : "memory"); SB();
  WAITVB(12);
  SB();
  asm volatile("v_mfma_f32_16x16x32_bf16 %0, %5, %6, %0\n\tbuffer_load_dwordx4 %1, %7, %8, %9 offen offset:256 sc1\n\tv_mfma_f32_16x16x32_bf16 %2, %10, %6, %2\n\tds_write_b128 %11, %12 offset:1024\n\tv_mfma_f32_16x16x32_bf16 %3, %13, %6, %3\n\tv_mfma_f32_16x16x32_bf16 %4, %14, %6, %4"
      : "+v"(acc[5][1]), "=&v"(p[10]), "+v"(acc[5][2]), "+v"(acc[5][3]), "+v"(acc[5][0]) : "a"(fw1[1][1]), "v"(p[11]), "v"(vHc), "s"(rsW), "s"(sh[14]), "a"(fw1[1][2]), "v"(rw), "v"(acc[4][1]), "a"(fw1[1][3]), "a"(fw1[1][0]) : "memory"); SB();
  WAITVB(12);
  SB();
  asm volatile("v_mfma_f32_16x16x32_bf16 %0, %5, %6, %0\n\tbuffer_load_dwordx4 %1, %7, %8, %9 offen offset:256 sc1\n\tv_mfma_f32_16x16x32_bf16 %2, %10, %6, %2\n\tds_write_b128 %11, %12 offset:2048\n\tv_mfma_f32_16x16x32_bf16 %3, %13, %6, %3\n\tv_mfma_f32_16x16x32_bf16 %4, %14, %6, %4"
      : "+v"(acc[5][1]), "=&v"(p[11]), "+v"(acc[5][2]), "+v"(acc[5][3]), "+v"(acc[5][0]) : "a"(fw1[2][1]), "v"(p[12]), "v"(vHc), "s"(rsW), "s"(sh[15]), "a"(fw1[2][2]), "v"(rw), "v"(acc[4][2]), "a"(fw1[2][3]), "a"(fw1[2][0]) : "memory"); SB();
  WAITVB(12);
  SB();
  asm volatile("v_mfma_f32_16x16x32_bf16 %0, %5, %6, %0\n\tbuffer_load_dwordx4 %1, %7, %8, %9 offen offset:0 sc1\n\tv_mfma_f32_16x16x32_bf16 %2, %10, %6, %2\n\tds_write_b128 %11, %12 offset:3072\n\tv_mfma_f32_16x16x32_bf16 %3, %13, %6, %3\n\tv_mfma_f32_16x16x32_bf16 %4, %14, %6, %4"
      : "+v"(acc[5][1]), "=&v"(p[12]), "+v"(acc[5][2]), "+v"(acc[5][3]), "+v"(acc[5][0]) : "a"(fw1[3][1]), "v"(p[13]), "v"(vHb), "s"(rsW), "s"(sh[0]), "a"(fw1[3][2]), "v"(rw), "v"(acc[4][3]), "a"(fw1[3][3]), "a"(fw1[3][0]) : "memory"); SB();
  WAITVB(12);
  SB();
  asm volatile("v_mfma_f32_16x16x32_bf16 %0, %3, %4, %0\n\tbuffer_load_dwordx4 %1, %5, %6, %7 offen offset:0 sc1\n\tv_mfma_f32_16x16x32_bf16 %2, %8, %4, %2"
      : "+v"(acc[5][1]), "=&v"(p[13]), "+v"(acc[5][2]) : "a"(fw1[4][1]), "v"(p[0]), "v"(vHb), "s"(rsW), "s"(sh[1]), "a"(fw1[4][2]) : "memory"); SB();
  BAR();
  SB();
  asm volatile("v_mfma_f32_16x16x32_bf16 %0, %5, %6, %0\n\tds_read_b128 %1, %7 offset:1024\n\tds_read_b128 %2, %8 offset:2048\n\tv_mfma_f32_16x16x32_bf16 %3, %9, %6, %3\n\tds_read_b128 %4, %10 offset:3072"
      : "+v"(acc[5][3]), "=&v"(pm[4][0]), "=&v"(pm[4][1]), "+v"(acc[5][0]), "=&v"(pm[4][2]) : "a"(fw1[4][3]), "v"(p[0]), "v"(rr1), "v"(rr2), "a"(fw1[4][0]), "v"(rr3) : "memory"); SB();
  WAITVB(12);
  SB();
  asm volatile("v_mfma_f32_16x16x32_bf16 %0, %5, %6, %0\n\tbuffer_load_dwordx4 %1, %7, %8, %9 offen offset:0 sc1\n\tv_mfma_f32_16x16x32_bf16 %2, %10, %6, %2\n\tv_mfma_f32_16x16x32_bf16 %3, %11, %6, %3\n\tv_mfma_f32_16x16x32_bf16 %4, %12, %6, %4"
      : "+v"(acc[5][1]), "=&v"(p[0]), "+v"(acc[5][2]), "+v"(acc[5][3]), "+v"(acc[5][0]) : "a"(fw1[5][1]), "v"(p[1]), "v"(vHb), "s"(rsW), "s"(sh[2]), "a"(fw1[5][2]), "a"(fw1[5][3]), "a"(fw1[5][0]) : "memory"); SB();
  WAITVB(12);
  SB();
  asm volatile("v_mfma_f32_16x16x32_bf16 %0, %3, %4, %0\n\tbuffer_load_dwordx4 %1, %5, %6, %7 offen offset:0 sc1\n\tv_mfma_f32_16x16x32_bf16 %2, %8, %4, %2"
      : "+v"(acc[5][1]), "=&v"(p[1]), "+v"(acc[5][2]) : "a"(fw1[6][1]), "v"(p[2]), "v"(vHb), "s"(rsW), "s"(sh[3]), "a"(fw1[6][2]) : "memory"); SB();
  WAITL(0);
  asm volatile("v_add_f32 %0, %6, %7\n\tv_mfma_f32_16x16x32_bf16 %1, %8, %9, %1\n\tv_add_f32 %2, %10, %11\n\tv_add_f32 %3, %12, %13\n\tv_mfma_f32_16x16x32_bf16 %4, %14, %9, %4\n\tv_add_f32 %5, %15, %16"
      : "=&v"(gm[0]), "+v"(acc[5][3]), "=&v"(gm[1]), "=&v"(gm[2]), "+v"(acc[5][0]), "=&v"(gm[3]) : "v"(acc[4][0][0]), "v"(pm[4][0][0]), "a"(fw1[6][3]), "v"(p[2]), "v"(acc[4][0][1]), "v"(pm[4][0][1]), "v"(acc[4][0][2]), "v"(pm[4][0][2]), "a"(fw1[6][0]), "v"(acc[4][0][3]), "v"(pm[4][0][3])); SB();
  WAITVB(12);
  SB();
  asm volatile("v_mfma_f32_16x16x32_bf16 %0, %9, %10, %0\n\tbuffer_load_dwordx4 %1, %11, %12, %13 offen offset:0 sc1\n\tv_mfma_f32_16x16x32_bf16 %2, %14, %10, %2\n\tv_add_f32 %3, %15, %16\n\tv_add_f32 %4, %17, %18\n\tv_mfma_f32_16x16x32_bf16 %5, %19, %10, %5\n\tv_add_f32 %6, %20, %21\n\tv_add_f32 %7, %22, %23\n\tv_mfma_f32_16x16x32_bf16 %8, %24, %10, %8"
      : "+v"(acc[5][1]), "=&v"(p[2]), "+v"(acc[5][2]), "=&v"(gz[0]), "=&v"(gz[1]), "+v"(acc[5][3]), "=&v"(gz[2]), "=&v"(gz[3]), "+v"(acc[5][0]) : "a"(fw1[7][1]), "v"(p[3]), "v"(vHb), "s"(rsW), "s"(sh[4]), "a"(fw1[7][2]), "v"(pm[4][1][0]), "v"(pm[4][2][0]), "v"(pm[4][1][1]), "v"(pm[4][2][1]), "a"(fw1[7][3]), "v"(pm[4][1][2]), "v"(pm[4][2][2]), "v"(pm[4][1][3]), "v"(pm[4][2][3]), "a"(fw1[7][0]) : "memory"); SB();
  asm volatile("v_add_f32 %0, %0, %1"
      : "+v"(gm[0]) : "v"(gz[0])); SB();
  WAITVB(12);
  SB();
  asm volatile("v_mfma_f32_16x16x32_bf16 %0, %10, %11, %0\n\tbuffer_load_dwordx4 %1, %12, %13, %14 offen offset:0 sc1\n\tv_mfma_f32_16x16x32_bf16 %2, %15, %11, %2\n\tv_add_f32 %3, %3, %4\n\tv_add_f32 %5, %5, %16\n\tv_mfma_f32_16x16x32_bf16 %6, %17, %11, %6\n\tv_add_f32 %7, %7, %18\n\tv_mul_f32 %8, %19, %19\n\tv_mfma_f32_16x16x32_bf16 %9, %20, %11, %9\n\tv_mul_f32 %4, %3, %3"
      : "+v"(acc[5][1]), "=&v"(p[3]), "+v"(acc[5][2]), "+v"(gm[1]), "+v"(gz[1]), "+v"(gm[2]), "+v"(acc[5][3]), "+v"(gm[3]), "=&v"(gz[0]), "+v"(acc[5][0]) : "a"(fw1[8][1]), "v"(p[4]), "v"(vHb), "s"(rsW), "s"(sh[5]), "a"(fw1[8][2]), "v"(gz[2]), "a"(fw1[8][3]), "v"(gz[3]), "v"(gm[0]), "a"(fw1[8][0]) : "memory"); SB();
  WAITVB(12);
  SB();
  asm volatile("v_mfma_f32_16x16x32_bf16 %0, %9, %10, %0\n\tbuffer_load_dwordx4 %1, %11, %12, %13 offen offset:0 sc1\n\tv_mfma_f32_16x16x32_bf16 %2, %14, %10, %2\n\tv_mul_f32 %3, %15, %15\n\tv_mul_f32 %4, %16, %16\n\tv_mfma_f32_16x16x32_bf16 %5, %17, %10, %5\n\tv_fmaak_f32 %6, %18, %6, 0x40135761\n\tv_fmaak_f32 %7, %18, %7, 0x40135761\n\tv_mfma_f32_16x16x32_bf16 %8, %19, %10, %8\n\tv_fmaak_f32 %3, %18, %3, 0x40135761"
      : "+v"(acc[5][1]), "=&v"(p[4]), "+v"(acc[5][2]), "=&v"(gz[2]), "=&v"(gz[3]), "+v"(acc[5][3]), "+v"(gz[0]), "+v"(gz[1]), "+v"(acc[5][0]) : "a"(fw1[9][1]), "v"(p[5]), "v"(vHb), "s"(rsW), "s"(sh[6]), "a"(fw1[9][2]), "v"(gm[2]), "v"(gm[3]), "a"(fw1[9][3]), "v"(gc), "a"(fw1[9][0]) : "memory"); SB();
  WAITVB(12);
  SB();
  asm volatile("v_mfma_f32_16x16x32_bf16 %0, %9, %10, %0\n\tbuffer_load_dwordx4 %1, %11, %12, %13 offen offset:0 sc1\n\tv_mfma_f32_16x16x32_bf16 %2, %14, %10, %2\n\tv_fmaak_f32 %3, %15, %3, 0x40135761\n\tv_mul_f32 %4, %4, %16\n\tv_mfma_f32_16x16x32_bf16 %5, %17, %10, %5\n\tv_mul_f32 %6, %6, %18\n\tv_mul_f32 %7, %7, %19\n\tv_mfma_f32_16x16x32_bf16 %8, %20, %10, %8\n\tv_mul_f32 %3, %3, %21"
      : "+v"(acc[5][1]), "=&v"(p[5]), "+v"(acc[5][2]), "+v"(gz[3]), "+v"(gz[0]), "+v"(acc[5][3]), "+v"(gz[1]), "+v"(gz[2]), "+v"(acc[5][0]) : "a"(fw1[10][1]), "v"(p[6]), "v"(vHb), "s"(rsW), "s"(sh[7]), "a"(fw1[10][2]), "v"(gc), "v"(gm[0]), "a"(fw1[10][3]), "v"(gm[1]), "v"(gm[2]), "a"(fw1[10][0]), "v"(gm[3]) : "memory"); SB();
  WAITVB(12);
  SB();
  asm volatile("v_mfma_f32_16x16x32_bf16 %0, %7, %8, %0\n\tbuffer_load_dwordx4 %1, %9, %10, %11 offen offset:0 sc1\n\tv_mfma_f32_16x16x32_bf16 %2, %12, %8, %2\n\tv_exp_f32 %3, %3\n\tv_mfma_f32_16x16x32_bf16 %4, %13, %8, %4\n\tv_exp_f32 %5, %5\n\tv_mfma_f32_16x16x32_bf16 %6, %14, %8, %6"
      : "+v"(acc[5][1]), "=&v"(p[6]), "+v"(acc[5][2]), "+v"(gz[0]), "+v"(acc[5][3]), "+v"(gz[1]), "+v"(acc[5][0]) : "a"(fw1[11][1]), "v"(p[7]), "v"(vHb), "s"(rsW), "s"(sh[8]), "a"(fw1[11][2]), "a"(fw1[11][3]), "a"(fw1[11][0]) : "memory"); SB();
  WAITVB(12);
  SB();
  asm volatile("v_mfma_f32_16x16x32_bf16 %0, %8, %9, %0\n\tbuffer_load_dwordx4 %1, %10, %11, %12 offen offset:0 sc1\n\tv_mfma_f32_16x16x32_bf16 %2, %13, %9, %2\n\tv_exp_f32 %3, %3\n\tv_mfma_f32_16x16x32_bf16 %4, %14, %9, %4\n\tv_exp_f32 %5, %5\n\tv_mfma_f32_16x16x32_bf16 %6, %15, %9, %6\n\tv_add_f32 %7, 1.0, %7"
      : "+v"(acc[5][1]), "=&v"(p[7]), "+v"(acc[5][2]), "+v"(gz[2]), "+v"(acc[5][3]), "+v"(gz[3]), "+v"(acc[5][0]), "+v"(gz[0]) : "a"(fw1[12][1]), "v"(p[8]), "v"(vHb), "s"(rsW), "s"(sh[9]), "a"(fw1[12][2]), "a"(fw1[12][3]), "a"(fw1[12][0]) : "memory"); SB();
  WAITVB(12);
  SB();
  asm volatile("v_mfma_f32_16x16x32_bf16 %0, %8, %9, %0\n\tbuffer_load_dwordx4 %1, %10, %11, %12 offen offset:0 sc1\n\tv_mfma_f32_16x16x32_bf16 %2, %13, %9, %2\n\tv_add_f32 %3, 1.0, %3\n\tv_add_f32 %4, 1.0, %4\n\tv_mfma_f32_16x16x32_bf16 %5, %14, %9, %5\n\tv_add_f32 %6, 1.0, %6\n\tv_mfma_f32_16x16x32_bf16 %7, %15, %9, %7"
      : "+v"(acc[5][1]), "=&v"(p[8]), "+v"(acc[5][2]), "+v"(gz[1]), "+v"(gz[2]), "+v"(acc[5][3]), "+v"(gz[3]), "+v"(acc[5][0]) : "a"(fw1[13][1]), "v"(p[9]), "v"(vHb), "s"(rsW), "s"(sh[10]), "a"(fw1[13][2]), "a"(fw1[13][3]), "a"(fw1[13][0]) : "memory"); SB();
  WAITVB(12);
  SB();
  asm volatile("v_mfma_f32_16x16x32_bf16 %0, %7, %8, %0\n\tbuffer_load_dwordx4 %1, %9, %10, %11 offen offset:0 sc1\n\tv_mfma_f32_16x16x32_bf16 %2, %12, %8, %2\n\tv_rcp_f32 %3, %3\n\tv_mfma_f32_16x16x32_bf16 %4, %13, %8, %4\n\tv_rcp_f32 %5, %5\n\tv_mfma_f32_16x16x32_bf16 %6, %14, %8, %6"
      : "+v"(acc[5][1]), "=&v"(p[9]), "+v"(acc[5][2]), "+v"(gz[0]), "+v"(acc[5][3]), "+v"(gz[1]), "+v"(acc[5][0]) : "a"(fw1[14][1]), "v"(p[10]), "v"(vHb), "s"(rsW), "s"(sh[11]), "a"(fw1[14][2]), "a"(fw1[14][3]), "a"(fw1[14][0]) : "memory"); SB();
  WAITVB(12);
  SB();
  asm volatile("v_mfma_f32_16x16x32_bf16 %0, %9, %10, %0\n\tbuffer_load_dwordx4 %1, %11, %12, %13 offen offset:0 sc1\n\tv_mfma_f32_16x16x32_bf16 %2, %14, %10, %2\n\tv_rcp_f32 %3, %3\n\tv_mfma_f32_16x16x32_bf16 %4, %15, %10, %4\n\tv_rcp_f32 %5, %5\n\tv_mfma_f32_16x16x32_bf16 %6, %16, %10, %6\n\tv_fma_f32 %7, -%17, %7, %17\n\tv_fma_f32 %8, -%18, %8, %18"
      : "+v"(acc[5][1]), "=&v"(p[10]), "+v"(acc[5][2]), "+v"(gz[2]), "+v"(acc[5][3]), "+v"(gz[3]), "+v"(acc[5][0]), "+v"(gz[0]), "+v"(gz[1]) : "v"(fw1[15][1]), "v"(p[11]), "v"(vHb), "s"(rsW), "s"(sh[12]), "v"(fw1[15][2]), "v"(fw1[15][3]), "v"(fw1[15][0]), "v"(gm[0]), "v"(gm[1]) : "memory"); SB();
  WAITVB(12);
  SB();
  asm volatile("v_mfma_f32_16x16x32_bf16 %0, %9, %10, 0\n\tbuffer_load_dwordx4 %1, %11, %12, %13 offen offset:0 sc1\n\tv_mfma_f32_16x16x32_bf16 %2, %14, %10, 0\n\tv_fma_f32 %3, -%15, %3, %15\n\tv_fma_f32 %4, -%16, %4, %16\n\tv_mfma_f32_16x16x32_bf16 %5, %17, %10, 0\n\tv_cvt_pk_bf16_f32 %6, %18, %19\n\tv_cvt_pk_bf16_f32 %7, %3, %4\n\tv_mfma_f32_16x16x32_bf16 %8, %20, %10, %21"
      : "=&v"(acc[6][1]), "=&v"(p[11]), "=&v"(acc[6][2]), "+v"(gz[2]), "+v"(gz[3]), "=&v"(acc[6][3]), "=&v"(uv[4][0]), "=&v"(uv[4][1]), "=&v"(acc[6][0]) : "a"(fw1[0][1]), "v"(p[12]), "v"(vHb), "s"(rsW), "s"(sh[13]), "a"(fw1[0][2]), "v"(gm[2]), "v"(gm[3]), "a"(fw1[0][3]), "v"(gz[0]), "v"(gz[1]), "a"(fw1[0][0]), "v"(bb) : "memory"); SB();
  WAITVB(12);
  SB();
  asm volatile("v_mfma_f32_16x16x32_bf16 %0, %5, %6, %0\n\tbuffer_load_dwordx4 %1, %7, %8, %9 offen offset:0 sc1\n\tv_mfma_f32_16x16x32_bf16 %2, %10, %6, %2\n\tds_write_b128 %11, %12 offset:5120\n\tv_mfma_f32_16x16x32_bf16 %3, %13, %6, %3\n\tv_mfma_f32_16x16x32_bf16 %4, %14, %6, %4"
      : "+v"(acc[6][1]), "=&v"(p[12]), "+v"(acc[6][2]), "+v"(acc[6][3]), "+v"(acc[6][0]) : "a"(fw1[1][1]), "v"(p[13]), "v"(vHb), "s"(rsW), "s"(sh[14]), "a"(fw1[1][2]), "v"(rw), "v"(acc[5][1]), "a"(fw1[1][3]), "a"(fw1[1][0]) : "memory"); SB();
  WAITVB(12);
  SB();
  asm volatile("v_mfma_f32_16x16x32_bf16 %0, %5, %6, %0\n\tbuffer_load_dwordx4 %1, %7, %8, %9 offen offset:0 sc1\n\tv_mfma_f32_16x16x32_bf16 %2, %10, %6, %2\n\tds_write_b128 %11, %12 offset:6144\n\tv_mfma_f32_16x16x32_bf16 %3, %13, %6, %3\n\tv_mfma_f32_16x16x32_bf16 %4, %14, %6, %4"
      : "+v"(acc[6][1]), "=&v"(p[13]), "+v"(acc[6][2]), "+v"(acc[6][3]), "+v"(acc[6][0]) : "a"(fw1[2][1]), "v"(p[0]), "v"(vHb), "s"(rsW), "s"(sh[15]), "a"(fw1[2][2]), "v"(rw), "v"(acc[5][2]), "a"(fw1[2][3]), "a"(fw1[2][0]) : "memory"); SB();
  WAITVB(12);
  SB();
  asm volatile("v_mfma_f32_16x16x32_bf16 %0, %5, %6, %0\n\tv_permlane16_swap_b32 %1, %2\n\tv_permlane16_swap_b32 %3, %4"
      : "+v"(acc[6][1]), "+v"(uv[0][0]), "+v"(uv[1][0]), "+v"(uv[0][1]), "+v"(uv[1][1]) : "a"(fw1[3][1]), "v"(p[1])); SB();
  HS16(vU, sU, 0, ((u32x4){uv[0][0], uv[0][1], uv[1][0], uv[1][1]}));
  SB();
  asm volatile("v_mfma_f32_16x16x32_bf16 %0, %6, %7, %0\n\tds_write_b128 %8, %9 offset:7168\n\tv_mfma_f32_16x16x32_bf16 %1, %10, %7, %1\n\tv_permlane16_swap_b32 %2, %3\n\tv_permlane16_swap_b32 %4, %5"
      : "+v"(acc[6][2]), "+v"(acc[6][3]), "+v"(uv[2][0]), "+v"(uv[3][0]), "+v"(uv[2][1]), "+v"(uv[3][1]) : "a"(fw1[3][2]), "v"(p[1]), "v"(rw), "v"(acc[5][3]), "a"(fw1[3][3]) : "memory"); SB();
  HS16(vU, sU, 512, ((u32x4){uv[2][0], uv[2][1], uv[3][0], uv[3][1]}));
  SB();
  asm volatile("v_mfma_f32_16x16x32_bf16 %0, %1, %2, %0"
      : "+v"(acc[6][0]) : "a"(fw1[3][0]), "v"(p[1])); SB();
  WAITVB(13);
  SB();
  asm volatile("v_mfma_f32_16x16x32_bf16 %0, %2, %3, %0\n\ts_add_u32 m0, %4, 0\n\ts_nop 0\n\tv_mfma_f32_16x16x32_bf16 %1, %5, %3, %1"
      : "+v"(acc[6][1]), "+v"(acc[6][2]) : "a"(fw1[4][1]), "v"(p[2]), "s"(ldsO), "a"(fw1[4][2]) : "memory", "scc"); SB();
  BAR();
  SB();
  asm volatile("v_mfma_f32_16x16x32_bf16 %0, %5, %6, %0\n\tds_read_b128 %1, %7 offset:5120\n\tds_read_b128 %2, %8 offset:6144\n\tv_mfma_f32_16x16x32_bf16 %3, %9, %6, %3\n\tds_read_b128 %4, %10 offset:7168"
      : "+v"(acc[6][3]), "=&v"(pm[5][0]), "=&v"(pm[5][1]), "+v"(acc[6][0]), "=&v"(pm[5][2]) : "a"(fw1[4][3]), "v"(p[2]), "v"(rr1), "v"(rr2), "a"(fw1[4][0]), "v"(rr3) : "memory"); SB();
  WAITVB(12);
  SB();
  asm volatile("v_mfma_f32_16x16x32_bf16 %0, %4, %5, %0\n\tbuffer_load_dwordx4 %6, %7, %8 offen offset:0  lds\n\tv_mfma_f32_16x16x32_bf16 %1, %9, %5, %1\n\tv_mfma_f32_16x16x32_bf16 %2, %10, %5, %2\n\tbuffer_load_dwordx4 %11, %7, %8 offen offset:1024  lds\n\tv_mfma_f32_16x16x32_bf16 %3, %12, %5, %3"
      : "+v"(acc[6][1]), "+v"(acc[6][2]), "+v"(acc[6][3]), "+v"(acc[6][0]) : "a"(fw1[5][1]), "v"(p[3]), "v"(vW2[0]), "s"(rs2), "s"(sw[0]), "a"(fw1[5][2]), "a"(fw1[5][3]), "v"(vW2[1]), "a"(fw1[5][0]) : "memory"); SB();
  WAITVB(13);
  SB();
  asm volatile("v_mfma_f32_16x16x32_bf16 %0, %1, %2, %0"
      : "+v"(acc[6][1]) : "a"(fw1[6][1]), "v"(p[4])); SB();
  WAITL(0);
  asm volatile("v_add_f32 %0, %9, %10\n\tv_mfma_f32_16x16x32_bf16 %1, %11, %12, %1\n\tv_add_f32 %2, %13, %14\n\tv_add_f32 %3, %15, %16\n\tv_mfma_f32_16x16x32_bf16 %4, %17, %12, %4\n\tv_add_f32 %5, %18, %19\n\tv_add_f32 %6, %20, %21\n\tv_mfma_f32_16x16x32_bf16 %7, %22, %12, %7\n\tv_add_f32 %8, %23, %24"
      : "=&v"(gm[0]), "+v"(acc[6][2]), "=&v"(gm[1]), "=&v"(gm[2]), "+v"(acc[6][3]), "=&v"(gm[3]), "=&v"(gz[0]), "+v"(acc[6][0]), "=&v"(gz[1]) : "v"(acc[5][0][0]), "v"(pm[5][0][0]), "a"(fw1[6][2]), "v"(p[4]), "v"(acc[5][0][1]), "v"(pm[5][0][1]), "v"(acc[5][0][2]), "v"(pm[5][0][2]), "a"(fw1[6][3]), "v"(acc[5][0][3]), "v"(pm[5][0][3]), "v"(pm[5][1][0]), "v"(pm[5][2][0]), "a"(fw1[6][0]), "v"(pm[5][1][1]), "v"(pm[5][2][1])); SB();
  WAITVB(12);
  SB();
  asm volatile("v_mfma_f32_16x16x32_bf16 %0, %11, %12, %0\n\tv_add_f32 %1, %13, %14\n\tv_add_f32 %2, %15, %16\n\tv_mfma_f32_16x16x32_bf16 %3, %17, %12, %3\n\tv_add_f32 %4, %4, %5\n\tv_add_f32 %6, %6, %18\n\tv_mfma_f32_16x16x32_bf16 %7, %19, %12, %7\n\tv_add_f32 %8, %8, %1\n\tv_add_f32 %9, %9, %2\n\tv_mfma_f32_16x16x32_bf16 %10, %20, %12, %10\n\tv_mul_f32 %5, %4, %4"
      : "+v"(acc[6][1]), "=&v"(gz[2]), "=&v"(gz[3]), "+v"(acc[6][2]), "+v"(gm[0]), "+v"(gz[0]), "+v"(gm[1]), "+v"(acc[6][3]), "+v"(gm[2]), "+v"(gm[3]), "+v"(acc[6][0]) : "a"(fw1[7][1]), "v"(p[5]), "v"(pm[5][1][2]), "v"(pm[5][2][2]), "v"(pm[5][1][3]), "v"(pm[5][2][3]), "a"(fw1[7][2]), "v"(gz[1]), "a"(fw1[7][3]), "a"(fw1[7][0])); SB();
  WAITVB(11);
  SB();
  asm volatile("v_mfma_f32_16x16x32_bf16 %0, %8, %9, %0\n\tv_mul_f32 %1, %10, %10\n\tv_mul_f32 %2, %11, %11\n\tv_mfma_f32_16x16x32_bf16 %3, %12, %9, %3\n\tv_mul_f32 %4, %13, %13\n\tv_fmaak_f32 %5, %14, %5, 0x40135761\n\tv_mfma_f32_16x16x32_bf16 %6, %15, %9, %6\n\tv_fmaak_f32 %1, %14, %1, 0x40135761\n\tv_fmaak_f32 %2, %14, %2, 0x40135761\n\tv_mfma_f32_16x16x32_bf16 %7, %16, %9, %7\n\tv_fmaak_f32 %4, %14, %4, 0x40135761"
      : "+v"(acc[6][1]), "=&v"(gz[1]), "=&v"(gz[2]), "+v"(acc[6][2]), "=&v"(gz[3]), "+v"(gz[0]), "+v"(acc[6][3]), "+v"(acc[6][0]) : "a"(fw1[8][1]), "v"(p[6]), "v"(gm[1]), "v"(gm[2]), "a"(fw1[8][2]), "v"(gm[3]), "v"(gc), "a"(fw1[8][3]), "a"(fw1[8][0])); SB();
  WAITVB(10);
  SB();
  asm volatile("v_mfma_f32_16x16x32_bf16 %0, %8, %9, %0\n\tv_mul_f32 %1, %1, %10\n\tv_mul_f32 %2, %2, %11\n\tv_mfma_f32_16x16x32_bf16 %3, %12, %9, %3\n\tv_mul_f32 %4, %4, %13\n\tv_mul_f32 %5, %5, %14\n\tv_mfma_f32_16x16x32_bf16 %6, %15, %9, %6\n\tv_exp_f32 %1, %1\n\tv_mfma_f32_16x16x32_bf16 %7, %16, %9, %7"
      : "+v"(acc[6][1]), "+v"(gz[0]), "+v"(gz[1]), "+v"(acc[6][2]), "+v"(gz[2]), "+v"(gz[3]), "+v"(acc[6][3]), "+v"(acc[6][0]) : "a"(fw1[9][1]), "v"(p[7]), "v"(gm[0]), "v"(gm[1]), "a"(fw1[9][2]), "v"(gm[2]), "v"(gm[3]), "a"(fw1[9][3]), "a"(fw1[9][0])); SB();
  WAITVB(9);
  SB();
  asm volatile("v_mfma_f32_16x16x32_bf16 %0, %8, %9, %0\n\tv_exp_f32 %1, %1\n\tv_mfma_f32_16x16x32_bf16 %2, %10, %9, %2\n\tv_exp_f32 %3, %3\n\tv_mfma_f32_16x16x32_bf16 %4, %11, %9, %4\n\tv_exp_f32 %5, %5\n\tv_mfma_f32_16x16x32_bf16 %6, %12, %9, %6\n\tv_add_f32 %7, 1.0, %7"
      : "+v"(acc[6][1]), "+v"(gz[1]), "+v"(acc[6][2]), "+v"(gz[2]), "+v"(acc[6][3]), "+v"(gz[3]), "+v"(acc[6][0]), "+v"(gz[0]) : "a"(fw1[10][1]), "v"(p[8]), "a"(fw1[10][2]), "a"(fw1[10][3]), "a"(fw1[10][0])); SB();
  WAITVB(8);
  SB();
  asm volatile("v_mfma_f32_16x16x32_bf16 %0, %8, %9, %0\n\tv_add_f32 %1, 1.0, %1\n\tv_add_f32 %2, 1.0, %2\n\tv_mfma_f32_16x16x32_bf16 %3, %10, %9, %3\n\tv_add_f32 %4, 1.0, %4\n\tv_mfma_f32_16x16x32_bf16 %5, %11, %9, %5\n\tv_rcp_f32 %6, %6\n\tv_mfma_f32_16x16x32_bf16 %7, %12, %9, %7"
      : "+v"(acc[6][1]), "+v"(gz[1]), "+v"(gz[2]), "+v"(acc[6][2]), "+v"(gz[3]), "+v"(acc[6][3]), "+v"(gz[0]), "+v"(acc[6][0]) : "a"(fw1[11][1]), "v"(p[9]), "a"(fw1[11][2]), "a"(fw1[11][3]), "a"(fw1[11][0])); SB();
  WAITVB(7);
  SB();
  asm volatile("v_mfma_f32_16x16x32_bf16 %0, %8, %9, %0\n\tv_rcp_f32 %1, %1\n\tv_mfma_f32_16x16x32_bf16 %2, %10, %9, %2\n\tv_rcp_f32 %3, %3\n\tv_mfma_f32_16x16x32_bf16 %4, %11, %9, %4\n\tv_rcp_f32 %5, %5\n\tv_mfma_f32_16x16x32_bf16 %6, %12, %9, %6\n\tv_fma_f32 %7, -%13, %7, %13"
      : "+v"(acc[6][1]), "+v"(gz[1]), "+v"(acc[6][2]), "+v"(gz[2]), "+v"(acc[6][3]), "+v"(gz[3]), "+v"(acc[6][0]), "+v"(gz[0]) : "a"(fw1[12][1]), "v"(p[10]), "a"(fw1[12][2]), "a"(fw1[12][3]), "a"(fw1[12][0]), "v"(gm[0])); SB();
  WAITVB(6);
  SB();
  asm volatile("v_mfma_f32_16x16x32_bf16 %0, %9, %10, %0\n\tv_fma_f32 %1, -%11, %1, %11\n\tv_fma_f32 %2, -%12, %2, %12\n\tv_mfma_f32_16x16x32_bf16 %3, %13, %10, %3\n\tv_fma_f32 %4, -%14, %4, %14\n\tv_cvt_pk_bf16_f32 %5, %15, %1\n\tv_mfma_f32_16x16x32_bf16 %6, %16, %10, %6\n\tv_cvt_pk_bf16_f32 %7, %2, %4\n\tv_mfma_f32_16x16x32_bf16 %8, %17, %10, %8"
      : "+v"(acc[6][1]), "+v"(gz[1]), "+v"(gz[2]), "+v"(acc[6][2]), "+v"(gz[3]), "=&v"(uv[5][0]), "+v"(acc[6][3]), "=&v"(uv[5][1]), "+v"(acc[6][0]) : "a"(fw1[13][1]), "v"(p[11]), "v"(gm[1]), "v"(gm[2]), "a"(fw1[13][2]), "v"(gm[3]), "v"(gz[0]), "a"(fw1[13][3]), "a"(fw1[13][0])); SB();
  WAITVB(5);
  SB();
  asm volatile("v_mfma_f32_16x16x32_bf16 %0, %5, %6, %0\n\tv_permlane16_swap_b32 %1, %2\n\tv_permlane16_swap_b32 %3, %4"
      : "+v"(acc[6][1]), "+v"(uv[4][0]), "+v"(uv[5][0]), "+v"(uv[4][1]), "+v"(uv[5][1]) : "a"(fw1[14][1]), "v"(p[12])); SB();
  HS16(vU, sU, 256, ((u32x4){uv[4][0], uv[4][1], uv[5][0], uv[5][1]}));
  SB();
  asm volatile("v_mfma_f32_16x16x32_bf16 %0, %3, %4, %0\n\tbuffer_load_dwordx4 %5, %6, %7 offen offset:2048  lds\n\tv_mfma_f32_16x16x32_bf16 %1, %8, %4, %1\n\tv_mfma_f32_16x16x32_bf16 %2, %9, %4, %2"
      : "+v"(acc[6][2]), "+v"(acc[6][3]), "+v"(acc[6][0]) : "a"(fw1[14][2]), "v"(p[12]), "v"(vW2[2]), "s"(rs2), "s"(sw[0]), "a"(fw1[14][3]), "a"(fw1[14][0]) : "memory"); SB();
  WAITVB(6);
  SB();
  asm volatile("v_mfma_f32_16x16x32_bf16 %0, %4, %5, %0\n\tbuffer_load_dwordx4 %6, %7, %8 offen offset:3072  lds\n\tv_mfma_f32_16x16x32_bf16 %1, %9, %5, %1\n\tv_mfma_f32_16x16x32_bf16 %2, %10, %5, %2\n\ts_add_u32 m0, %11, 4096\n\ts_nop 0\n\tv_mfma_f32_16x16x32_bf16 %3, %12, %5, %3"
      : "+v"(acc[6][1]), "+v"(acc[6][2]), "+v"(acc[6][3]), "+v"(acc[6][0]) : "v"(fw1[15][1]), "v"(p[13]), "v"(vW2[3]), "s"(rs2), "s"(sw[0]), "v"(fw1[15][2]), "v"(fw1[15][3]), "s"(ldsO), "v"(fw1[15][0]) : "memory", "scc"); SB();
  STAMP(8);
  asm volatile("s_nop 4\n\tds_write_b128 %0, %1 offset:1024\n\tds_write_b128 %0, %2 offset:2048\n\tds_write_b128 %0, %3 offset:3072\n\tbuffer_load_dwordx4 %4, %5, %6 offen offset:0  lds\n\tbuffer_load_dwordx4 %7, %5, %6 offen offset:1024  lds\n\tbuffer_load_dwordx4 %8, %5, %6 offen offset:2048  lds\n\tbuffer_load_dwordx4 %9, %5, %6 offen offset:3072  lds\n\ts_add_u32 m0, %10, 18432\n\ts_nop 0\n\tbuffer_load_dwordx4 %4, %5, %11 offen offset:0  lds\n\tbuffer_load_dwordx4 %7, %5, %11 offen offset:1024  lds\n\tbuffer_load_dwordx4 %8, %5, %11 offen offset:2048  lds\n\tbuffer_load_dwordx4 %9, %5, %11 offen offset:3072  lds\n\ts_waitcnt vmcnt(13)"
      :  : "v"(rw), "v"(acc[6][1]), "v"(acc[6][2]), "v"(acc[6][3]), "v"(vW2[0]), "s"(rs2), "s"(sw[1]), "v"(vW2[1]), "v"(vW2[2]), "v"(vW2[3]), "s"(ldsS), "s"(sw[2]) : "memory", "scc"); SB();
  STFU(sFU + 2048 * 4);
  BAR();
  asm volatile("ds_read_b128 %0, %3 offset:1024\n\tds_read_b128 %1, %4 offset:2048\n\tds_read_b128 %2, %5 offset:3072"
      : "=&v"(pm[6][0]), "=&v"(pm[6][1]), "=&v"(pm[6][2]) : "v"(rr1), "v"(rr2), "v"(rr3) : "memory"); SB();
  WAITL(0);
  asm volatile("v_add_f32 %0, %12, %13\n\tv_add_f32 %1, %14, %15\n\tv_add_f32 %2, %16, %17\n\tv_add_f32 %3, %18, %19\n\tv_add_f32 %4, %20, %21\n\tv_add_f32 %5, %22, %23\n\tv_add_f32 %6, %24, %25\n\tv_add_f32 %7, %26, %27\n\tv_add_f32 %0, %0, %4\n\tv_add_f32 %1, %1, %5\n\tv_add_f32 %2, %2, %6\n\tv_add_f32 %3, %3, %7\n\tv_mul_f32 %4, %0, %0\n\tv_mul_f32 %5, %1, %1\n\tv_mul_f32 %6, %2, %2\n\tv_mul_f32 %7, %3, %3\n\tv_fmaak_f32 %4, %28, %4, 0x40135761\n\tv_fmaak_f32 %5, %28, %5, 0x40135761\n\tv_fmaak_f32 %6, %28, %6, 0x40135761\n\tv_fmaak_f32 %7, %28, %7, 0x40135761\n\tv_mul_f32 %4, %4, %0\n\tv_mul_f32 %5, %5, %1\n\tv_mul_f32 %6, %6, %2\n\tv_mul_f32 %7, %7, %3\n\tv_exp_f32 %4, %4\n\tv_exp_f32 %5, %5\n\tv_exp_f32 %6, %6\n\tv_exp_f32 %7, %7\n\tv_add_f32 %4, 1.0, %4\n\tv_add_f32 %5, 1.0, %5\n\tv_add_f32 %6, 1.0, %6\n\tv_add_f32 %7, 1.0, %7\n\tv_rcp_f32 %4, %4\n\tv_rcp_f32 %5, %5\n\tv_rcp_f32 %6, %6\n\tv_rcp_f32 %7, %7\n\tv_fma_f32 %4, -%0, %4, %0\n\tv_fma_f32 %5, -%1, %5, %1\n\tv_fma_f32 %6, -%2, %6, %2\n\tv_fma_f32 %7, -%3, %7, %3\n\tv_cvt_pk_bf16_f32 %8, %4, %5\n\tv_cvt_pk_bf16_f32 %9, %6, %7\n\tv_cvt_pk_bf16_f32 %10, %4, %5\n\tv_cvt_pk_bf16_f32 %11, %6, %7\n\ts_nop 0\n\tv_permlane16_swap_b32 %8, %10\n\tv_permlane16_swap_b32 %9, %11"
      : "=&v"(gm[0]), "=&v"(gm[1]), "=&v"(gm[2]), "=&v"(gm[3]), "=&v"(gz[0]), "=&v"(gz[1]), "=&v"(gz[2]), "=&v"(gz[3]), "=&v"(uv[6][0]), "=&v"(uv[6][1]), "=&v"(uv[7][0]), "=&v"(uv[7][1]) : "v"(acc[6][0][0]), "v"(pm[6][0][0]), "v"(acc[6][0][1]), "v"(pm[6][0][1]), "v"(acc[6][0][2]), "v"(pm[6][0][2]), "v"(acc[6][0][3]), "v"(pm[6][0][3]), "v"(pm[6][1][0]), "v"(pm[6][2][0]), "v"(pm[6][1][1]), "v"(pm[6][2][1]), "v"(pm[6][1][2]), "v"(pm[6][2][2]), "v"(pm[6][1][3]), "v"(pm[6][2][3]), "v"(gc)); SB();
  HS16M(0xffff, 0xffff, vU6, sU, 0, ((u32x4){uv[6][0], uv[6][1], uv[7][0], uv[7][1]}));
  STAMP(10);
  asm volatile("s_add_u32 m0, %0, 34816\n\ts_nop 0\n\tbuffer_load_dwordx4 %1, %2, %3 offen offset:0  lds\n\tbuffer_load_dwordx4 %4, %2, %3 offen offset:1024  lds\n\tbuffer_load_dwordx4 %5, %2, %3 offen offset:2048  lds\n\tbuffer_load_dwordx4 %6, %2, %3 offen offset:3072  lds\n\ts_add_u32 m0, %0, 0\n\ts_nop 0\n\tbuffer_load_dwordx4 %1, %2, %7 offen offset:0  lds\n\tbuffer_load_dwordx4 %4, %2, %7 offen offset:1024  lds\n\tbuffer_load_dwordx4 %5, %2, %7 offen offset:2048  lds\n\tbuffer_load_dwordx4 %6, %2, %7 offen offset:3072  lds\n\ts_waitcnt vmcnt(18)"
      :  : "s"(ldsS), "v"(vW2[0]), "s"(rs2), "s"(sw[3]), "v"(vW2[1]), "v"(vW2[2]), "v"(vW2[3]), "s"(sw[4]) : "memory", "scc"); SB();
  fw2[0][0] = tr8u(tO[0] + 0, 512);
  fw2[0][1] = tr8u(tO[1] + 0, 512);
  fw2[0][2] = tr8u(tO[2] + 0, 512);
  fw2[0][3] = tr8u(tO[3] + 0, 512);
  WAITL(0);
  asm volatile("s_add_u32 m0, %0, 0\n\ts_nop 0\n\tbuffer_load_dwordx4 %1, %2, %3 offen offset:0  lds\n\tbuffer_load_dwordx4 %4, %2, %3 offen offset:1024  lds\n\tbuffer_load_dwordx4 %5, %2, %3 offen offset:2048  lds\n\tbuffer_load_dwordx4 %6, %2, %3 offen offset:3072  lds\n\ts_waitcnt vmcnt(18)"
      :  : "s"(ldsO), "v"(vW2[0]), "s"(rs2), "s"(sw[5]), "v"(vW2[1]), "v"(vW2[2]), "v"(vW2[3]) : "memory", "scc"); SB();
  fw2[1][0] = tr8u(tO[0] + 4096, 512);
  fw2[1][1] = tr8u(tO[1] + 4096, 512);
  fw2[1][2] = tr8u(tO[2] + 4096, 512);
  fw2[1][3] = tr8u(tO[3] + 4096, 512);
  asm volatile("s_waitcnt vmcnt(14)"
      :  :  : "memory"); SB();
  fw2[2][0] = tr8u(tS[0] + 18432, 512);
  fw2[2][1] = tr8u(tS[1] + 18432, 512);
  fw2[2][2] = tr8u(tS[2] + 18432, 512);
  fw2[2][3] = tr8u(tS[3] + 18432, 512);
  WAITV1(12, fq);
  STFU(sFU);
  STAMP(11);
  {
  if (__builtin_expect(__ballot(fq != nonce) != 0ull, 0)) poll1<4>(rsW, vFQ, nonce);
  unsigned fu;
  WAITL(8);
  asm volatile("s_add_u32 m0, %0, 4096\n\ts_nop 0\n\tbuffer_load_dwordx4 %1, %2, %3 offen offset:0  lds\n\tbuffer_load_dwordx4 %4, %2, %3 offen offset:1024  lds\n\tbuffer_load_dwordx4 %5, %2, %3 offen offset:2048  lds\n\tbuffer_load_dwordx4 %6, %2, %3 offen offset:3072  lds\n\ts_waitcnt vmcnt(13)"
      :  : "s"(ldsO), "v"(vW2[0]), "s"(rs2), "s"(sw[6]), "v"(vW2[1]), "v"(vW2[2]), "v"(vW2[3]) : "memory", "scc"); SB();
  fw2[3][0] = tr8u(tS[0] + 34816, 512);
  fw2[3][1] = tr8u(tS[1] + 34816, 512);
  fw2[3][2] = tr8u(tS[2] + 34816, 512);
  fw2[3][3] = tr8u(tS[3] + 34816, 512);
  WAITL(8);
  asm volatile("s_add_u32 m0, %0, 18432\n\ts_nop 0\n\tbuffer_load_dwordx4 %1, %2, %3 offen offset:0  lds\n\tbuffer_load_dwordx4 %4, %2, %3 offen offset:1024  lds\n\tbuffer_load_dwordx4 %5, %2, %3 offen offset:2048  lds\n\tbuffer_load_dwordx4 %6, %2, %3 offen offset:3072  lds\n\ts_waitcnt vmcnt(13)"
      :  : "s"(ldsS), "v"(vW2[0]), "s"(rs2), "s"(sw[7]), "v"(vW2[1]), "v"(vW2[2]), "v"(vW2[3]) : "memory", "scc"); SB();
  fw2[4][0] = tr8u(tS[0] + 0, 512);
  fw2[4][1] = tr8u(tS[1] + 0, 512);
  fw2[4][2] = tr8u(tS[2] + 0, 512);
  fw2[4][3] = tr8u(tS[3] + 0, 512);
  WAITL(8);
  asm volatile("s_add_u32 m0, %0, 34816\n\ts_nop 0\n\tbuffer_load_dwordx4 %1, %2, %3 offen offset:0  lds\n\tbuffer_load_dwordx4 %4, %2, %3 offen offset:1024  lds\n\tbuffer_load_dwordx4 %5, %2, %3 offen offset:2048  lds\n\tbuffer_load_dwordx4 %6, %2, %3 offen offset:3072  lds\n\ts_waitcnt vmcnt(13)"
      :  : "s"(ldsS), "v"(vW2[0]), "s"(rs2), "s"(sw[8]), "v"(vW2[1]), "v"(vW2[2]), "v"(vW2[3]) : "memory", "scc"); SB();
  fw2[5][0] = tr8u(tO[0] + 0, 512);
  fw2[5][1] = tr8u(tO[1] + 0, 512);
  fw2[5][2] = tr8u(tO[2] + 0, 512);
  fw2[5][3] = tr8u(tO[3] + 0, 512);
  WAITL(8);
  asm volatile("s_add_u32 m0, %0, 0\n\ts_nop 0\n\tbuffer_load_dwordx4 %1, %2, %3 offen offset:0  lds\n\tbuffer_load_dwordx4 %4, %2, %3 offen offset:1024  lds\n\tbuffer_load_dwordx4 %5, %2, %3 offen offset:2048  lds\n\tbuffer_load_dwordx4 %6, %2, %3 offen offset:3072  lds\n\ts_waitcnt vmcnt(12)"
      :  : "s"(ldsS), "v"(vW2[0]), "s"(rs2), "s"(sw[9]), "v"(vW2[1]), "v"(vW2[2]), "v"(vW2[3]) : "memory", "scc"); SB();
  fw2[6][0] = tr8u(tO[0] + 4096, 512);
  fw2[6][1] = tr8u(tO[1] + 4096, 512);
  fw2[6][2] = tr8u(tO[2] + 4096, 512);
  fw2[6][3] = tr8u(tO[3] + 4096, 512);
  WAITL(8);
  asm volatile("s_add_u32 m0, %0, 0\n\ts_nop 0\n\tbuffer_load_dwordx4 %1, %2, %3 offen offset:0  lds\n\tbuffer_load_dwordx4 %4, %2, %3 offen offset:1024  lds\n\tbuffer_load_dwordx4 %5, %2, %3 offen offset:2048  lds\n\tbuffer_load_dwordx4 %6, %2, %3 offen offset:3072  lds\n\ts_waitcnt vmcnt(12)"
      :  : "s"(ldsO), "v"(vW2[0]), "s"(rs2), "s"(sw[10]), "v"(vW2[1]), "v"(vW2[2]), "v"(vW2[3]) : "memory", "scc"); SB();
  fw2[7][0] = tr8u(tS[0] + 18432, 512);
  fw2[7][1] = tr8u(tS[1] + 18432, 512);
  fw2[7][2] = tr8u(tS[2] + 18432, 512);
  fw2[7][3] = tr8u(tS[3] + 18432, 512);
  WAITL(0);
  asm volatile("s_add_u32 m0, %0, 18432\n\ts_nop 0\n\tbuffer_load_dwordx4 %1, %2, %3 offen offset:0  lds\n\tbuffer_load_dwordx4 %4, %2, %3 offen offset:1024  lds\n\tbuffer_load_dwordx4 %5, %2, %3 offen offset:2048  lds\n\tbuffer_load_dwordx4 %6, %2, %3 offen offset:3072  lds\n\ts_waitcnt vmcnt(12)"
      :  : "s"(ldsS), "v"(vW2[0]), "s"(rs2), "s"(sw[11]), "v"(vW2[1]), "v"(vW2[2]), "v"(vW2[3]) : "memory", "scc"); SB();
  fw2[8][0] = tr8u(tS[0] + 34816, 512);
  fw2[8][1] = tr8u(tS[1] + 34816, 512);
  fw2[8][2] = tr8u(tS[2] + 34816, 512);
  fw2[8][3] = tr8u(tS[3] + 34816, 512);
  WAITL(0);
  asm volatile("s_add_u32 m0, %0, 34816\n\ts_nop 0\n\tbuffer_load_dwordx4 %1, %2, %3 offen offset:0  lds\n\tbuffer_load_dwordx4 %4, %2, %3 offen offset:1024  lds\n\tbuffer_load_dwordx4 %5, %2, %3 offen offset:2048  lds\n\tbuffer_load_dwordx4 %6, %2, %3 offen offset:3072  lds\n\ts_waitcnt vmcnt(12)"
      :  : "s"(ldsS), "v"(vW2[0]), "s"(rs2), "s"(sw[12]), "v"(vW2[1]), "v"(vW2[2]), "v"(vW2[3]) : "memory", "scc"); SB();
  fw2[9][0] = tr8u(tS[0] + 0, 512);
  fw2[9][1] = tr8u(tS[1] + 0, 512);
  fw2[9][2] = tr8u(tS[2] + 0, 512);
  fw2[9][3] = tr8u(tS[3] + 0, 512);
  WAITL(0);
  asm volatile("s_add_u32 m0, %1, 0\n\ts_nop 0\n\tbuffer_load_dwordx4 %2, %3, %4 offen offset:0  lds\n\tbuffer_load_dwordx4 %5, %3, %4 offen offset:1024  lds\n\tbuffer_load_dwordx4 %6, %3, %4 offen offset:2048  lds\n\tbuffer_load_dwordx4 %7, %3, %4 offen offset:3072  lds\n\ts_add_u32 m0, %8, 8192\n\ts_add_u32 %0, %9, 8192"
      : "=&s"(sWt) : "s"(ldsS), "v"(vW2[0]), "s"(rs2), "s"(sw[13]), "v"(vW2[1]), "v"(vW2[2]), "v"(vW2[3]), "s"(ldsP), "s"(sWo) : "memory", "scc"); SB();
  DMAW4(vWo, rsW, sWt);
  asm volatile("s_waitcnt vmcnt(16)"
      :  :  : "memory"); SB();
  fw2[10][0] = tr8u(tO[0] + 0, 512);
  fw2[10][1] = tr8u(tO[1] + 0, 512);
  fw2[10][2] = tr8u(tO[2] + 0, 512);
  fw2[10][3] = tr8u(tO[3] + 0, 512);
  asm volatile("buffer_load_dword %0, %2, %3, 0 offen sc1\n\ts_add_u32 m0, %4, 12288\n\ts_add_u32 %1, %5, 12288"
      : "=&v"(fu), "=&s"(sWt) : "v"(vFU), "s"(rsW), "s"(ldsP), "s"(sWo) : "memory", "scc"); SB();
  DMAW2(vWo, rsW, sWt);
  asm volatile("s_waitcnt vmcnt(15)"
      :  :  : "memory"); SB();
  fw2[11][0] = tr8u(tS[0] + 18432, 512);
  fw2[11][1] = tr8u(tS[1] + 18432, 512);
  fw2[11][2] = tr8u(tS[2] + 18432, 512);
  fw2[11][3] = tr8u(tS[3] + 18432, 512);
  WAITL(8);
  asm volatile("s_add_u32 m0, %1, 0\n\ts_add_u32 %0, %2, 0"
      : "=&s"(sWt) : "s"(ldsO), "s"(sWo) : "memory", "scc"); SB();
  DMAW4(vWo, rsW, sWt);
  asm volatile("s_waitcnt vmcnt(15)"
      :  :  : "memory"); SB();
  fw2[12][0] = tr8u(tS[0] + 34816, 512);
  fw2[12][1] = tr8u(tS[1] + 34816, 512);
  fw2[12][2] = tr8u(tS[2] + 34816, 512);
  fw2[12][3] = tr8u(tS[3] + 34816, 512);
  asm volatile("s_add_u32 m0, %1, 4096\n\ts_add_u32 %0, %2, 4096"
      : "=&s"(sWt) : "s"(ldsO), "s"(sWo) : "memory", "scc"); SB();
  DMAW4(vWo, rsW, sWt);
  u32x4 rsX = srd(x, 2048u * 896u); OPQ(rsX);
  WAITL(0);
  asm volatile("s_add_u32 m0, %1, 34816\n\ts_nop 0\n\tbuffer_load_dwordx4 %2, %3, %4 offen offset:0  lds\n\tbuffer_load_dwordx4 %5, %3, %4 offen offset:1024  lds\n\tglobal_load_ushort %0, %6, %7 offset:0"
      : "=&v"(braw) : "s"(ldsS), "v"(vXa), "s"(rsX), "s"(0u), "v"(vXb), "v"(vB2), "s"(b2) : "memory", "scc"); SB();
  STAMP(16);
  bf16x8 pu[14], fwo[56];
  f32x4 ac[2][4], as[4], py[4][4];
  float ye[4], ze[4], ge[16], xe[16], bias;
  u32x2 ov[4]; unsigned fo;
  u32x2 xv[4]; unsigned xta[4];
  WAITV1(13, fu);
  if (__ballot(fu != nonce) != 0ull) poll1<4>(rsW, vFU, nonce);
  STAMP(17);
  asm volatile("buffer_load_dwordx4 %0, %10, %11, %12 offen offset:0 sc1\n\tbuffer_load_dwordx4 %1, %10, %11, %13 offen offset:0 sc1\n\tbuffer_load_dwordx4 %2, %10, %11, %14 offen offset:0 sc1\n\tbuffer_load_dwordx4 %3, %10, %11, %15 offen offset:0 sc1\n\tbuffer_load_dwordx4 %4, %10, %11, %16 offen offset:0 sc1\n\tbuffer_load_dwordx4 %5, %10, %11, %17 offen offset:0 sc1\n\tbuffer_load_dwordx4 %6, %10, %11, %18 offen offset:0 sc1\n\tbuffer_load_dwordx4 %7, %10, %11, %19 offen offset:0 sc1\n\tbuffer_load_dwordx4 %8, %10, %11, %20 offen offset:0 sc1\n\tbuffer_load_dwordx4 %9, %10, %11, %21 offen offset:0 sc1"
      : "=&v"(pu[0]), "=&v"(pu[1]), "=&v"(pu[2]), "=&v"(pu[3]), "=&v"(pu[4]), "=&v"(pu[5]), "=&v"(pu[6]), "=&v"(pu[7]), "=&v"(pu[8]), "=&v"(pu[9]) : "v"(vFj[0]), "s"(rsW), "s"(su[0]), "s"(su[1]), "s"(su[2]), "s"(su[3]), "s"(su[4]), "s"(su[5]), "s"(su[6]), "s"(su[7]), "s"(su[8]), "s"(su[9]) : "memory"); SB();
  STAMP(39);
  WAITVB(9);
  SB();
  asm volatile("v_mfma_f32_16x16x32_bf16 %0, %6, %7, 0\n\tbuffer_load_dwordx4 %1, %8, %9, %10 offen offset:0 sc1\n\tv_mfma_f32_16x16x32_bf16 %2, %6, %11, 0\n\ts_waitcnt vmcnt(29)\n\tv_mfma_f32_16x16x32_bf16 %3, %6, %12, 0\n\tbuffer_load_dwordx4 %4, %8, %9, %13 offen offset:0 sc1\n\tv_mfma_f32_16x16x32_bf16 %5, %6, %14, 0"
      : "=&v"(ac[0][0]), "=&v"(pu[10]), "=&v"(ac[0][1]), "=&v"(ac[0][2]), "=&v"(pu[11]), "=&v"(ac[0][3]) : "v"(pu[0]), "a"(fw2[0][0]), "v"(vFj[0]), "s"(rsW), "s"(su[10]), "a"(fw2[0][1]), "a"(fw2[0][2]), "s"(su[11]), "a"(fw2[0][3]) : "memory"); SB();
  WAITVB(10);
  SB();
  asm volatile("v_mfma_f32_16x16x32_bf16 %0, %3, %4, %0\n\tbuffer_load_dwordx4 %1, %5, %6, %7 offen offset:0 sc1\n\tv_mfma_f32_16x16x32_bf16 %2, %3, %8, %2"
      : "+v"(ac[0][0]), "=&v"(pu[12]), "+v"(ac[0][1]) : "v"(pu[1]), "a"(fw2[1][0]), "v"(vFj[0]), "s"(rsW), "s"(su[12]), "a"(fw2[1][1]) : "memory"); SB();
  fw2[13][0] = tr8u(tS[0] + 0, 512);
  SB();
  asm volatile("v_mfma_f32_16x16x32_bf16 %0, %3, %4, %0\n\tbuffer_load_dwordx4 %1, %5, %6, %7 offen offset:0 sc1\n\tv_mfma_f32_16x16x32_bf16 %2, %3, %8, %2"
      : "+v"(ac[0][2]), "=&v"(pu[13]), "+v"(ac[0][3]) : "v"(pu[1]), "a"(fw2[1][2]), "v"(vFj[0]), "s"(rsW), "s"(su[13]), "a"(fw2[1][3]) : "memory"); SB();
  WAITVB(11);
  SB();
  asm volatile("v_mfma_f32_16x16x32_bf16 %0, %3, %4, %0\n\tbuffer_load_dwordx4 %1, %5, %6, %7 offen offset:0 sc1\n\tv_mfma_f32_16x16x32_bf16 %2, %3, %8, %2"
      : "+v"(ac[0][0]), "=&v"(pu[0]), "+v"(ac[0][1]) : "v"(pu[2]), "a"(fw2[2][0]), "v"(vFj[1]), "s"(rsW), "s"(su[0]), "a"(fw2[2][1]) : "memory"); SB();
  fw2[13][1] = tr8u(tS[1] + 0, 512);
  SB();
  asm volatile("v_mfma_f32_16x16x32_bf16 %0, %3, %4, %0\n\tbuffer_load_dwordx4 %1, %5, %6, %7 offen offset:0 sc1\n\tv_mfma_f32_16x16x32_bf16 %2, %3, %8, %2"
      : "+v"(ac[0][2]), "=&v"(pu[1]), "+v"(ac[0][3]) : "v"(pu[2]), "a"(fw2[2][2]), "v"(vFj[1]), "s"(rsW), "s"(su[1]), "a"(fw2[2][3]) : "memory"); SB();
  fw2[13][2] = tr8u(tS[2] + 0, 512);
  fw2[13][3] = tr8u(tS[3] + 0, 512);
  WAITVB(16);
  SB();
  WAITVB(12);
  SB();
  asm volatile("v_mfma_f32_16x16x32_bf16 %0, %10, %11, %0\n\tbuffer_load_dwordx4 %1, %12, %13, %14 offen offset:0 sc1\n\tv_mfma_f32_16x16x32_bf16 %2, %10, %15, %2\n\tv_lshlrev_b32 %3, 16, %16\n\tv_xor_b32 %4, 0, %17\n\tv_mfma_f32_16x16x32_bf16 %5, %10, %18, %5\n\tv_xor_b32 %6, 32, %17\n\tv_xor_b32 %7, 64, %19\n\tv_mfma_f32_16x16x32_bf16 %8, %10, %20, %8\n\tds_read_b64 %9, %4 offset:0"
      : "+v"(ac[0][0]), "=&v"(pu[2]), "+v"(ac[0][1]), "=&v"(bias), "=&v"(xta[0]), "+v"(ac[0][2]), "=&v"(xta[1]), "=&v"(xta[2]), "+v"(ac[0][3]), "=&v"(xv[0]) : "v"(pu[3]), "a"(fw2[3][0]), "v"(vFj[1]), "s"(rsW), "s"(su[2]), "a"(fw2[3][1]), "v"(braw), "v"(xs), "a"(fw2[3][2]), "v"(xt), "a"(fw2[3][3]) : "memory"); SB();
  WAITVB(12);
  SB();
  asm volatile("v_mfma_f32_16x16x32_bf16 %0, %7, %8, %0\n\tbuffer_load_dwordx4 %1, %9, %10, %11 offen offset:0 sc1\n\tv_mfma_f32_16x16x32_bf16 %2, %7, %12, %2\n\tds_read_b64 %3, %13 offset:0\n\tds_read_b64 %4, %14 offset:0\n\tv_mfma_f32_16x16x32_bf16 %5, %7, %15, %5\n\tv_mfma_f32_16x16x32_bf16 %6, %7, %16, %6"
      : "+v"(ac[0][0]), "=&v"(pu[3]), "+v"(ac[0][1]), "=&v"(xv[1]), "=&v"(xv[2]), "+v"(ac[0][2]), "+v"(ac[0][3]) : "v"(pu[4]), "a"(fw2[4][0]), "v"(vFj[1]), "s"(rsW), "s"(su[3]), "a"(fw2[4][1]), "v"(xta[1]), "v"(xta[2]), "a"(fw2[4][2]), "a"(fw2[4][3]) : "memory"); SB();
  WAITVB(12);
  SB();
  asm volatile("v_mfma_f32_16x16x32_bf16 %0, %4, %5, %0\n\tbuffer_load_dwordx4 %1, %6, %7, %8 offen offset:0 sc1\n\tv_mfma_f32_16x16x32_bf16 %2, %4, %9, %2\n\tv_mfma_f32_16x16x32_bf16 %3, %4, %10, %3"
      : "+v"(ac[0][0]), "=&v"(pu[4]), "+v"(ac[0][1]), "+v"(ac[0][2]) : "v"(pu[5]), "a"(fw2[5][0]), "v"(vFj[1]), "s"(rsW), "s"(su[4]), "a"(fw2[5][1]), "a"(fw2[5][2]) : "memory"); SB();
  WAITL(0);
  asm volatile("v_lshlrev_b32 %0, 16, %3\n\tv_mfma_f32_16x16x32_bf16 %1, %4, %5, %1\n\tv_and_b32 %2, 0xffff0000, %3"
      : "=&v"(xe[0]), "+v"(ac[0][3]), "=&v"(xe[1]) : "v"(xv[0][0]), "v"(pu[5]), "a"(fw2[5][3])); SB();
  WAITVB(12);
  SB();
  asm volatile("v_mfma_f32_16x16x32_bf16 %0, %10, %11, %0\n\tbuffer_load_dwordx4 %1, %12, %13, %14 offen offset:0 sc1\n\tv_mfma_f32_16x16x32_bf16 %2, %10, %15, %2\n\tv_lshlrev_b32 %3, 16, %16\n\tv_and_b32 %4, 0xffff0000, %16\n\tv_mfma_f32_16x16x32_bf16 %5, %10, %17, %5\n\tv_lshlrev_b32 %6, 16, %18\n\tv_and_b32 %7, 0xffff0000, %18\n\tv_mfma_f32_16x16x32_bf16 %8, %10, %19, %8\n\tv_lshlrev_b32 %9, 16, %20"
      : "+v"(ac[0][0]), "=&v"(pu[5]), "+v"(ac[0][1]), "=&v"(xe[2]), "=&v"(xe[3]), "+v"(ac[0][2]), "=&v"(xe[4]), "=&v"(xe[5]), "+v"(ac[0][3]), "=&v"(xe[6]) : "v"(pu[6]), "a"(fw2[6][0]), "v"(vFj[1]), "s"(rsW), "s"(su[5]), "a"(fw2[6][1]), "v"(xv[0][1]), "a"(fw2[6][2]), "v"(xv[1][0]), "a"(fw2[6][3]), "v"(xv[1][1]) : "memory"); SB();
  WAITVB(12);
  SB();
  asm volatile("v_mfma_f32_16x16x32_bf16 %0, %10, %11, %0\n\tbuffer_load_dwordx4 %1, %12, %13, %14 offen offset:0 sc1\n\tv_mfma_f32_16x16x32_bf16 %2, %10, %15, %2\n\tv_and_b32 %3, 0xffff0000, %16\n\tv_lshlrev_b32 %4, 16, %17\n\tv_mfma_f32_16x16x32_bf16 %5, %10, %18, %5\n\tv_and_b32 %6, 0xffff0000, %17\n\tv_lshlrev_b32 %7, 16, %19\n\tv_mfma_f32_16x16x32_bf16 %8, %10, %20, %8\n\tv_and_b32 %9, 0xffff0000, %19"
      : "+v"(ac[0][0]), "=&v"(pu[6]), "+v"(ac[0][1]), "=&v"(xe[7]), "=&v"(xe[8]), "+v"(ac[0][2]), "=&v"(xe[9]), "=&v"(xe[10]), "+v"(ac[0][3]), "=&v"(xe[11]) : "v"(pu[7]), "a"(fw2[7][0]), "v"(vFj[1]), "s"(rsW), "s"(su[6]), "a"(fw2[7][1]), "v"(xv[1][1]), "v"(xv[2][0]), "a"(fw2[7][2]), "v"(xv[2][1]), "a"(fw2[7][3]) : "memory"); SB();
  WAITVB(12);
  SB();
  asm volatile("v_mfma_f32_16x16x32_bf16 %0, %5, %6, %0\n\tbuffer_load_dwordx4 %1, %7, %8, %9 offen offset:0 sc1\n\tv_mfma_f32_16x16x32_bf16 %2, %5, %10, %2\n\tv_mfma_f32_16x16x32_bf16 %3, %5, %11, %3\n\tv_mfma_f32_16x16x32_bf16 %4, %5, %12, %4"
      : "+v"(ac[0][0]), "=&v"(pu[7]), "+v"(ac[0][1]), "+v"(ac[0][2]), "+v"(ac[0][3]) : "v"(pu[8]), "a"(fw2[8][0]), "v"(vFj[1]), "s"(rsW), "s"(su[7]), "a"(fw2[8][1]), "a"(fw2[8][2]), "a"(fw2[8][3]) : "memory"); SB();
  WAITVB(12);
  SB();
  asm volatile("v_mfma_f32_16x16x32_bf16 %0, %5, %6, %0\n\tbuffer_load_dwordx4 %1, %7, %8, %9 offen offset:0 sc1\n\tv_mfma_f32_16x16x32_bf16 %2, %5, %10, %2\n\tv_mfma_f32_16x16x32_bf16 %3, %5, %11, %3\n\tv_mfma_f32_16x16x32_bf16 %4, %5, %12, %4"
      : "+v"(ac[0][0]), "=&v"(pu[8]), "+v"(ac[0][1]), "+v"(ac[0][2]), "+v"(ac[0][3]) : "v"(pu[9]), "v"(fw2[9][0]), "v"(vFj[1]), "s"(rsW), "s"(su[8]), "v"(fw2[9][1]), "v"(fw2[9][2]), "v"(fw2[9][3]) : "memory"); SB();
  WAITVB(12);
  SB();
  asm volatile("v_mfma_f32_16x16x32_bf16 %0, %5, %6, %0\n\tbuffer_load_dwordx4 %1, %7, %8, %9 offen offset:0 sc1\n\tv_mfma_f32_16x16x32_bf16 %2, %5, %10, %2\n\tv_mfma_f32_16x16x32_bf16 %3, %5, %11, %3\n\tv_mfma_f32_16x16x32_bf16 %4, %5, %12, %4"
      : "+v"(ac[0][0]), "=&v"(pu[9]), "+v"(ac[0][1]), "+v"(ac[0][2]), "+v"(ac[0][3]) : "v"(pu[10]), "v"(fw2[10][0]), "v"(vFj[1]), "s"(rsW), "s"(su[9]), "v"(fw2[10][1]), "v"(fw2[10][2]), "v"(fw2[10][3]) : "memory"); SB();
  WAITVB(12);
  SB();
  asm volatile("v_mfma_f32_16x16x32_bf16 %0, %5, %6, %0\n\tbuffer_load_dwordx4 %1, %7, %8, %9 offen offset:0 sc1\n\tv_mfma_f32_16x16x32_bf16 %2, %5, %10, %2\n\tv_mfma_f32_16x16x32_bf16 %3, %5, %11, %3\n\tv_mfma_f32_16x16x32_bf16 %4, %5, %12, %4"
      : "+v"(ac[0][0]), "=&v"(pu[10]), "+v"(ac[0][1]), "+v"(ac[0][2]), "+v"(ac[0][3]) : "v"(pu[11]), "v"(fw2[11][0]), "v"(vFj[1]), "s"(rsW), "s"(su[10]), "v"(fw2[11][1]), "v"(fw2[11][2]), "v"(fw2[11][3]) : "memory"); SB();
  WAITVB(12);
  SB();
  asm volatile("v_mfma_f32_16x16x32_bf16 %0, %4, %5, %0\n\tbuffer_load_dwordx4 %1, %6, %7, %8 offen offset:0 sc1\n\tv_mfma_f32_16x16x32_bf16 %2, %4, %9, %2\n\tv_mfma_f32_16x16x32_bf16 %3, %4, %10, %3"
      : "+v"(ac[0][0]), "=&v"(pu[11]), "+v"(ac[0][1]), "+v"(ac[0][2]) : "v"(pu[12]), "v"(fw2[12][0]), "v"(vFj[1]), "s"(rsW), "s"(su[11]), "v"(fw2[12][1]), "v"(fw2[12][2]) : "memory"); SB();
  u32x4 rsO_ = srd(out, 2048u * 896u); OPQ(rsO_);
  SB();
  asm volatile("v_mfma_f32_16x16x32_bf16 %0, %1, %2, %0"
      : "+v"(ac[0][3]) : "v"(pu[12]), "v"(fw2[12][3])); SB();
  WAITVB(12);
  SB();
  asm volatile("v_mfma_f32_16x16x32_bf16 %0, %5, %6, %0\n\tbuffer_load_dwordx4 %1, %7, %8, %9 offen offset:0 sc1\n\tv_mfma_f32_16x16x32_bf16 %2, %5, %10, %2\n\tv_mfma_f32_16x16x32_bf16 %3, %5, %11, %3\n\tv_mfma_f32_16x16x32_bf16 %4, %5, %12, %4"
      : "+v"(ac[0][0]), "=&v"(pu[12]), "+v"(ac[0][1]), "+v"(ac[0][2]), "+v"(ac[0][3]) : "v"(pu[13]), "v"(fw2[13][0]), "v"(vFj[1]), "s"(rsW), "s"(su[12]), "v"(fw2[13][1]), "v"(fw2[13][2]), "v"(fw2[13][3]) : "memory"); SB();
  STAMP(40);
  WAITVB(12);
  SB();
  asm volatile("v_mfma_f32_16x16x32_bf16 %0, %6, %7, 0\n\tbuffer_load_dwordx4 %1, %8, %9, %10 offen offset:0 sc1\n\tv_mfma_f32_16x16x32_bf16 %2, %6, %11, 0\n\tbuffer_load_dword %3, %12, %9, 0 offen sc1\n\tv_mfma_f32_16x16x32_bf16 %4, %6, %13, 0\n\tv_mfma_f32_16x16x32_bf16 %5, %6, %14, 0"
      : "=&v"(ac[1][0]), "=&v"(pu[13]), "=&v"(ac[1][1]), "=&v"(fo), "=&v"(ac[1][2]), "=&v"(ac[1][3]) : "v"(pu[0]), "a"(fw2[0][0]), "v"(vFj[1]), "s"(rsW), "s"(su[13]), "a"(fw2[0][1]), "v"(vFO), "a"(fw2[0][2]), "a"(fw2[0][3]) : "memory"); SB();
  WAITVB(13);
  SB();
  asm volatile("v_mfma_f32_16x16x32_bf16 %0, %5, %6, %0\n\tbuffer_load_dwordx4 %1, %7, %8, %9 offen offset:0 sc1\n\tv_mfma_f32_16x16x32_bf16 %2, %5, %10, %2\n\tds_write_b128 %11, %12 offset:0\n\tv_mfma_f32_16x16x32_bf16 %3, %5, %13, %3\n\tv_mfma_f32_16x16x32_bf16 %4, %5, %14, %4"
      : "+v"(ac[1][0]), "=&v"(pu[0]), "+v"(ac[1][1]), "+v"(ac[1][2]), "+v"(ac[1][3]) : "v"(pu[1]), "a"(fw2[1][0]), "v"(vFj[2]), "s"(rsW), "s"(su[0]), "a"(fw2[1][1]), "v"(rwd), "v"(ac[0][0]), "a"(fw2[1][2]), "a"(fw2[1][3]) : "memory"); SB();
  WAITVB(13);
  SB();
  asm volatile("v_mfma_f32_16x16x32_bf16 %0, %5, %6, %0\n\tbuffer_load_dwordx4 %1, %7, %8, %9 offen offset:0 sc1\n\tv_mfma_f32_16x16x32_bf16 %2, %5, %10, %2\n\tds_write_b128 %11, %12 offset:1024\n\tv_mfma_f32_16x16x32_bf16 %3, %5, %13, %3\n\tv_mfma_f32_16x16x32_bf16 %4, %5, %14, %4"
      : "+v"(ac[1][0]), "=&v"(pu[1]), "+v"(ac[1][1]), "+v"(ac[1][2]), "+v"(ac[1][3]) : "v"(pu[2]), "a"(fw2[2][0]), "v"(vFj[2]), "s"(rsW), "s"(su[1]), "a"(fw2[2][1]), "v"(rwd), "v"(ac[0][1]), "a"(fw2[2][2]), "a"(fw2[2][3]) : "memory"); SB();
  WAITVB(13);
  SB();
  asm volatile("v_mfma_f32_16x16x32_bf16 %0, %5, %6, %0\n\tbuffer_load_dwordx4 %1, %7, %8, %9 offen offset:0 sc1\n\tv_mfma_f32_16x16x32_bf16 %2, %5, %10, %2\n\tds_write_b128 %11, %12 offset:2048\n\tv_mfma_f32_16x16x32_bf16 %3, %5, %13, %3\n\tv_mfma_f32_16x16x32_bf16 %4, %5, %14, %4"
      : "+v"(ac[1][0]), "=&v"(pu[2]), "+v"(ac[1][1]), "+v"(ac[1][2]), "+v"(ac[1][3]) : "v"(pu[3]), "a"(fw2[3][0]), "v"(vFj[2]), "s"(rsW), "s"(su[2]), "a"(fw2[3][1]), "v"(rwd), "v"(ac[0][2]), "a"(fw2[3][2]), "a"(fw2[3][3]) : "memory"); SB();
  WAITVB(13);
  SB();
  asm volatile("v_mfma_f32_16x16x32_bf16 %0, %5, %6, %0\n\tbuffer_load_dwordx4 %1, %7, %8, %9 offen offset:0 sc1\n\tv_mfma_f32_16x16x32_bf16 %2, %5, %10, %2\n\tds_write_b128 %11, %12 offset:3072\n\tv_mfma_f32_16x16x32_bf16 %3, %5, %13, %3\n\tv_mfma_f32_16x16x32_bf16 %4, %5, %14, %4"
      : "+v"(ac[1][0]), "=&v"(pu[3]), "+v"(ac[1][1]), "+v"(ac[1][2]), "+v"(ac[1][3]) : "v"(pu[4]), "a"(fw2[4][0]), "v"(vFj[2]), "s"(rsW), "s"(su[3]), "a"(fw2[4][1]), "v"(rwd), "v"(ac[0][3]), "a"(fw2[4][2]), "a"(fw2[4][3]) : "memory"); SB();
  WAITVB(13);
  SB();
  asm volatile("v_mfma_f32_16x16x32_bf16 %0, %3, %4, %0\n\tbuffer_load_dwordx4 %1, %5, %6, %7 offen offset:0 sc1\n\tv_mfma_f32_16x16x32_bf16 %2, %3, %8, %2\n\ts_waitcnt vmcnt(37)"
      : "+v"(ac[1][0]), "=&v"(pu[4]), "+v"(ac[1][1]) : "v"(pu[5]), "a"(fw2[5][0]), "v"(vFj[2]), "s"(rsW), "s"(su[4]), "a"(fw2[5][1]) : "memory"); SB();
  WAITL(0);
  BAR0();
  SB();
  asm volatile("v_mfma_f32_16x16x32_bf16 %0, %5, %6, %0\n\tds_read_b128 %1, %7 offset:0\n\tds_read_b128 %2, %7 offset:4096\n\tv_mfma_f32_16x16x32_bf16 %3, %5, %8, %3\n\tds_read_b128 %4, %7 offset:8192"
      : "+v"(ac[1][2]), "=&v"(py[0][0]), "=&v"(py[0][1]), "+v"(ac[1][3]), "=&v"(py[0][2]) : "v"(pu[5]), "a"(fw2[5][2]), "v"(rr), "a"(fw2[5][3]) : "memory"); SB();
  WAITVB(13);
  SB();
  asm volatile("v_mfma_f32_16x16x32_bf16 %0, %8, %9, %0\n\tbuffer_load_dwordx4 %1, %10, %11, %12 offen offset:0 sc1\n\tv_mfma_f32_16x16x32_bf16 %2, %8, %13, %2\n\tds_read_b128 %3, %14 offset:12288\n\tds_read_b128 %4, %15 offset:0\n\tv_mfma_f32_16x16x32_bf16 %5, %8, %16, %5\n\tv_mfma_f32_16x16x32_bf16 %6, %8, %17, %6\n\tds_read_b128 %7, %15 offset:8192"
      : "+v"(ac[1][0]), "=&v"(pu[5]), "+v"(ac[1][1]), "=&v"(py[0][3]), "=&a"(fwo[0]), "+v"(ac[1][2]), "+v"(ac[1][3]), "=&a"(fwo[1]) : "v"(pu[6]), "a"(fw2[6][0]), "v"(vFj[2]), "s"(rsW), "s"(su[5]), "a"(fw2[6][1]), "v"(rr), "v"(ro), "a"(fw2[6][2]), "a"(fw2[6][3]) : "memory"); SB();
  WAITVB(13);
  SB();
  asm volatile("v_mfma_f32_16x16x32_bf16 %0, %5, %6, %0\n\tbuffer_load_dwordx4 %1, %7, %8, %9 offen offset:0 sc1\n\tv_mfma_f32_16x16x32_bf16 %2, %5, %10, %2\n\tds_read_b128 %3, %11 offset:16384\n\tv_mfma_f32_16x16x32_bf16 %4, %5, %12, %4"
      : "+v"(ac[1][0]), "=&v"(pu[6]), "+v"(ac[1][1]), "=&a"(fwo[2]), "+v"(ac[1][2]) : "v"(pu[7]), "a"(fw2[7][0]), "v"(vFj[2]), "s"(rsW), "s"(su[6]), "a"(fw2[7][1]), "v"(ro), "a"(fw2[7][2]) : "memory"); SB();
  WAITL(3);
  asm volatile("v_add_f32 %0, %3, %4\n\tv_mfma_f32_16x16x32_bf16 %1, %5, %6, %1\n\tv_add_f32 %2, %7, %4"
      : "=&v"(ye[0]), "+v"(ac[1][3]), "=&v"(ye[1]) : "v"(py[0][0][0]), "v"(bias), "v"(pu[7]), "a"(fw2[7][3]), "v"(py[0][0][1])); SB();
  WAITVB(13);
  SB();
  asm volatile("v_mfma_f32_16x16x32_bf16 %0, %9, %10, %0\n\tbuffer_load_dwordx4 %1, %11, %12, %13 offen offset:0 sc1\n\tv_mfma_f32_16x16x32_bf16 %2, %9, %14, %2\n\tv_add_f32 %3, %15, %16\n\tv_add_f32 %4, %17, %16\n\tv_mfma_f32_16x16x32_bf16 %5, %9, %18, %5\n\tv_add_f32 %6, %19, %20\n\tv_add_f32 %7, %21, %22\n\tv_mfma_f32_16x16x32_bf16 %8, %9, %23, %8"
      : "+v"(ac[1][0]), "=&v"(pu[7]), "+v"(ac[1][1]), "=&v"(ye[2]), "=&v"(ye[3]), "+v"(ac[1][2]), "=&v"(ze[0]), "=&v"(ze[1]), "+v"(ac[1][3]) : "v"(pu[8]), "a"(fw2[8][0]), "v"(vFj[2]), "s"(rsW), "s"(su[7]), "a"(fw2[8][1]), "v"(py[0][0][2]), "v"(bias), "v"(py[0][0][3]), "a"(fw2[8][2]), "v"(py[0][1][0]), "v"(py[0][2][0]), "v"(py[0][1][1]), "v"(py[0][2][1]), "a"(fw2[8][3]) : "memory"); SB();
  asm volatile("v_add_f32 %0, %1, %2"
      : "=&v"(ze[2]) : "v"(py[0][1][2]), "v"(py[0][2][2])); SB();
  WAITVB(13);
  SB();
  asm volatile("v_mfma_f32_16x16x32_bf16 %0, %9, %10, %0\n\tbuffer_load_dwordx4 %1, %11, %12, %13 offen offset:0 sc1\n\tv_mfma_f32_16x16x32_bf16 %2, %9, %14, %2\n\tv_add_f32 %3, %15, %16\n\tv_add_f32 %4, %4, %17\n\tv_mfma_f32_16x16x32_bf16 %5, %9, %18, %5\n\tv_add_f32 %6, %6, %19\n\tv_add_f32 %7, %7, %20\n\tv_mfma_f32_16x16x32_bf16 %8, %9, %21, %8"
      : "+v"(ac[1][0]), "=&v"(pu[8]), "+v"(ac[1][1]), "=&v"(ze[3]), "+v"(ye[0]), "+v"(ac[1][2]), "+v"(ye[1]), "+v"(ye[2]), "+v"(ac[1][3]) : "v"(pu[9]), "v"(fw2[9][0]), "v"(vFj[2]), "s"(rsW), "s"(su[8]), "v"(fw2[9][1]), "v"(py[0][1][3]), "v"(py[0][2][3]), "v"(py[0][3][0]), "v"(fw2[9][2]), "v"(py[0][3][1]), "v"(py[0][3][2]), "v"(fw2[9][3]) : "memory"); SB();
  asm volatile("v_add_f32 %0, %0, %1"
      : "+v"(ye[3]) : "v"(py[0][3][3])); SB();
  WAITVB(13);
  SB();
  asm volatile("v_mfma_f32_16x16x32_bf16 %0, %10, %11, %0\n\tbuffer_load_dwordx4 %1, %12, %13, %14 offen offset:0 sc1\n\tv_mfma_f32_16x16x32_bf16 %2, %10, %15, %2\n\tv_add_f32 %3, %3, %4\n\tv_add_f32 %5, %5, %16\n\tv_mfma_f32_16x16x32_bf16 %6, %10, %17, %6\n\tv_add_f32 %7, %7, %18\n\tv_add_f32 %8, %8, %19\n\tv_mfma_f32_16x16x32_bf16 %9, %10, %20, %9\n\tv_mul_f32 %4, 0xbfb8aa3b, %3"
      : "+v"(ac[1][0]), "=&v"(pu[9]), "+v"(ac[1][1]), "+v"(ye[0]), "+v"(ze[0]), "+v"(ye[1]), "+v"(ac[1][2]), "+v"(ye[2]), "+v"(ye[3]), "+v"(ac[1][3]) : "v"(pu[10]), "v"(fw2[10][0]), "v"(vFj[2]), "s"(rsW), "s"(su[9]), "v"(fw2[10][1]), "v"(ze[1]), "v"(fw2[10][2]), "v"(ze[2]), "v"(ze[3]), "v"(fw2[10][3]) : "memory"); SB();
  WAITVB(13);
  SB();
  asm volatile("v_mfma_f32_16x16x32_bf16 %0, %9, %10, %0\n\tbuffer_load_dwordx4 %1, %11, %12, %13 offen offset:0 sc1\n\tv_mfma_f32_16x16x32_bf16 %2, %9, %14, %2\n\tv_mul_f32 %3, 0xbfb8aa3b, %15\n\tv_mul_f32 %4, 0xbfb8aa3b, %16\n\tv_mfma_f32_16x16x32_bf16 %5, %9, %17, %5\n\tds_read_b128 %6, %18 offset:1024\n\tv_mul_f32 %7, 0xbfb8aa3b, %19\n\tv_mfma_f32_16x16x32_bf16 %8, %9, %20, %8"
      : "+v"(ac[1][0]), "=&v"(pu[10]), "+v"(ac[1][1]), "=&v"(ze[1]), "=&v"(ze[2]), "+v"(ac[1][2]), "=&a"(fwo[3]), "=&v"(ze[3]), "+v"(ac[1][3]) : "v"(pu[11]), "v"(fw2[11][0]), "v"(vFj[2]), "s"(rsW), "s"(su[10]), "v"(fw2[11][1]), "v"(ye[1]), "v"(ye[2]), "v"(fw2[11][2]), "v"(ro), "v"(ye[3]), "v"(fw2[11][3]) : "memory"); SB();
  WAITVB(13);
  SB();
  asm volatile("v_mfma_f32_16x16x32_bf16 %0, %8, %9, %0\n\tbuffer_load_dwordx4 %1, %10, %11, %12 offen offset:0 sc1\n\tv_mfma_f32_16x16x32_bf16 %2, %8, %13, %2\n\tv_exp_f32 %3, %3\n\tv_mfma_f32_16x16x32_bf16 %4, %8, %14, %4\n\tv_exp_f32 %5, %5\n\tv_mfma_f32_16x16x32_bf16 %6, %8, %15, %6\n\tds_read_b128 %7, %16 offset:9216"
      : "+v"(ac[1][0]), "=&v"(pu[11]), "+v"(ac[1][1]), "+v"(ze[0]), "+v"(ac[1][2]), "+v"(ze[1]), "+v"(ac[1][3]), "=&a"(fwo[4]) : "v"(pu[12]), "v"(fw2[12][0]), "v"(vFj[2]), "s"(rsW), "s"(su[11]), "v"(fw2[12][1]), "v"(fw2[12][2]), "v"(fw2[12][3]), "v"(ro) : "memory"); SB();
  WAITVB(13);
  SB();
  asm volatile("v_mfma_f32_16x16x32_bf16 %0, %9, %10, %0\n\tbuffer_load_dwordx4 %1, %11, %12, %13 offen offset:0 sc1\n\tv_mfma_f32_16x16x32_bf16 %2, %9, %14, %2\n\tv_exp_f32 %3, %3\n\tv_mfma_f32_16x16x32_bf16 %4, %9, %15, %4\n\tv_exp_f32 %5, %5\n\tv_mfma_f32_16x16x32_bf16 %6, %9, %16, %6\n\tv_add_f32 %7, 1.0, %7\n\tv_add_f32 %8, 1.0, %8"
      : "+v"(ac[1][0]), "=&v"(pu[12]), "+v"(ac[1][1]), "+v"(ze[2]), "+v"(ac[1][2]), "+v"(ze[3]), "+v"(ac[1][3]), "+v"(ze[0]), "+v"(ze[1]) : "v"(pu[13]), "v"(fw2[13][0]), "v"(vFj[2]), "s"(rsW), "s"(su[12]), "v"(fw2[13][1]), "v"(fw2[13][2]), "v"(fw2[13][3]) : "memory"); SB();
  STAMP(41);
  WAITVB(12);
  SB();
  asm volatile("v_mfma_f32_16x16x32_bf16 %0, %9, %10, 0\n\tbuffer_load_dwordx4 %1, %11, %12, %13 offen offset:0 sc1\n\tv_mfma_f32_16x16x32_bf16 %2, %9, %14, 0\n\tv_add_f32 %3, 1.0, %3\n\tv_add_f32 %4, 1.0, %4\n\tv_mfma_f32_16x16x32_bf16 %5, %9, %15, 0\n\tv_rcp_f32 %6, %6\n\tv_mfma_f32_16x16x32_bf16 %7, %9, %16, 0\n\tds_read_b128 %8, %17 offset:17408"
      : "=&v"(ac[0][0]), "=&v"(pu[13]), "=&v"(ac[0][1]), "+v"(ze[2]), "+v"(ze[3]), "=&v"(ac[0][2]), "+v"(ze[0]), "=&v"(ac[0][3]), "=&a"(fwo[5]) : "v"(pu[0]), "a"(fw2[0][0]), "v"(vFj[2]), "s"(rsW), "s"(su[13]), "a"(fw2[0][1]), "a"(fw2[0][2]), "a"(fw2[0][3]), "v"(ro) : "memory"); SB();
  WAITVB(12);
  SB();
  asm volatile("v_mfma_f32_16x16x32_bf16 %0, %1, %2, %0"
      : "+v"(ac[0][0]) : "v"(pu[1]), "a"(fw2[1][0])); SB();
  WAITV1(14, fo);
  if (__builtin_expect(__ballot(fo != nonce) != 0ull, 0)) poll1<4>(rsW, vFO, nonce);
  STAMP(18);
  asm volatile("buffer_load_dwordx4 %0, %6, %7, %8 offen offset:0 sc1\n\tv_mfma_f32_16x16x32_bf16 %1, %9, %10, %1\n\tds_write_b128 %11, %12 offset:18432\n\tv_mfma_f32_16x16x32_bf16 %2, %9, %13, %2\n\tv_rcp_f32 %3, %3\n\tv_mfma_f32_16x16x32_bf16 %4, %9, %14, %4\n\tds_read_b128 %5, %15 offset:2048"
      : "=&v"(pu[0]), "+v"(ac[0][1]), "+v"(ac[0][2]), "+v"(ze[1]), "+v"(ac[0][3]), "=&a"(fwo[6]) : "v"(vF), "s"(rsW), "s"(so0), "v"(pu[1]), "a"(fw2[1][1]), "v"(rwd), "v"(ac[1][0]), "a"(fw2[1][2]), "a"(fw2[1][3]), "v"(ro) : "memory"); SB();
  WAITVB(12);
  SB();
  asm volatile("v_mfma_f32_16x16x32_bf16 %0, %7, %8, %0\n\tbuffer_load_dwordx4 %1, %9, %10, %11 offen offset:128 sc1\n\tv_mfma_f32_16x16x32_bf16 %2, %7, %12, %2\n\tds_write_b128 %13, %14 offset:19456\n\tv_mfma_f32_16x16x32_bf16 %3, %7, %15, %3\n\tv_rcp_f32 %4, %4\n\tv_mfma_f32_16x16x32_bf16 %5, %7, %16, %5\n\tds_read_b128 %6, %17 offset:10240"
      : "+v"(ac[0][0]), "=&v"(pu[1]), "+v"(ac[0][1]), "+v"(ac[0][2]), "+v"(ze[2]), "+v"(ac[0][3]), "=&a"(fwo[7]) : "v"(pu[2]), "a"(fw2[2][0]), "v"(vF), "s"(rsW), "s"(so0), "a"(fw2[2][1]), "v"(rwd), "v"(ac[1][1]), "a"(fw2[2][2]), "a"(fw2[2][3]), "v"(ro) : "memory"); SB();
  WAITVB(12);
  SB();
  asm volatile("v_mfma_f32_16x16x32_bf16 %0, %7, %8, %0\n\tbuffer_load_dwordx4 %1, %9, %10, %11 offen offset:256 sc1\n\tv_mfma_f32_16x16x32_bf16 %2, %7, %12, %2\n\tds_write_b128 %13, %14 offset:20480\n\tv_mfma_f32_16x16x32_bf16 %3, %7, %15, %3\n\tv_rcp_f32 %4, %4\n\tv_mfma_f32_16x16x32_bf16 %5, %7, %16, %5\n\tv_mul_f32 %6, %17, %18"
      : "+v"(ac[0][0]), "=&v"(pu[2]), "+v"(ac[0][1]), "+v"(ac[0][2]), "+v"(ze[3]), "+v"(ac[0][3]), "=&v"(ge[0]) : "v"(pu[3]), "a"(fw2[3][0]), "v"(vF), "s"(rsW), "s"(so0), "a"(fw2[3][1]), "v"(rwd), "v"(ac[1][2]), "a"(fw2[3][2]), "a"(fw2[3][3]), "v"(ye[0]), "v"(ze[0]) : "memory"); SB();
  WAITVB(12);
  SB();
  asm volatile("v_mfma_f32_16x16x32_bf16 %0, %8, %9, %0\n\tbuffer_load_dwordx4 %1, %10, %11, %12 offen offset:384 sc1\n\tv_mfma_f32_16x16x32_bf16 %2, %8, %13, %2\n\tds_write_b128 %14, %15 offset:21504\n\tv_mfma_f32_16x16x32_bf16 %3, %8, %16, %3\n\tv_mul_f32 %4, %17, %18\n\tv_mul_f32 %5, %19, %20\n\tv_mfma_f32_16x16x32_bf16 %6, %8, %21, %6\n\tv_mul_f32 %7, %22, %23"
      : "+v"(ac[0][0]), "=&v"(pu[3]), "+v"(ac[0][1]), "+v"(ac[0][2]), "=&v"(ge[1]), "=&v"(ge[2]), "+v"(ac[0][3]), "=&v"(ge[3]) : "v"(pu[4]), "a"(fw2[4][0]), "v"(vF), "s"(rsW), "s"(so0), "a"(fw2[4][1]), "v"(rwd), "v"(ac[1][3]), "a"(fw2[4][2]), "v"(ye[1]), "v"(ze[1]), "v"(ye[2]), "v"(ze[2]), "a"(fw2[4][3]), "v"(ye[3]), "v"(ze[3]) : "memory"); SB();
  WAITVB(12);
  SB();
  asm volatile("v_mfma_f32_16x16x32_bf16 %0, %3, %4, %0\n\tbuffer_load_dwordx4 %1, %5, %6, %7 offen offset:512 sc1\n\tv_mfma_f32_16x16x32_bf16 %2, %3, %8, %2"
      : "+v"(ac[0][0]), "=&v"(pu[4]), "+v"(ac[0][1]) : "v"(pu[5]), "a"(fw2[5][0]), "v"(vF), "s"(rsW), "s"(so0), "a"(fw2[5][1]) : "memory"); SB();
  WAITL(0);
  BAR0();
  SB();
  asm volatile("v_mfma_f32_16x16x32_bf16 %0, %5, %6, %0\n\tds_read_b128 %1, %7 offset:18432\n\tds_read_b128 %2, %7 offset:22528\n\tv_mfma_f32_16x16x32_bf16 %3, %5, %8, %3\n\tds_read_b128 %4, %7 offset:26624"
      : "+v"(ac[0][2]), "=&v"(py[1][0]), "=&v"(py[1][1]), "+v"(ac[0][3]), "=&v"(py[1][2]) : "v"(pu[5]), "a"(fw2[5][2]), "v"(rr), "a"(fw2[5][3]) : "memory"); SB();
  WAITVB(12);
  SB();
  asm volatile("v_mfma_f32_16x16x32_bf16 %0, %8, %9, %0\n\tbuffer_load_dwordx4 %1, %10, %11, %12 offen offset:640 sc1\n\tv_mfma_f32_16x16x32_bf16 %2, %8, %13, %2\n\tds_read_b128 %3, %14 offset:30720\n\tds_read_b128 %4, %15 offset:18432\n\tv_mfma_f32_16x16x32_bf16 %5, %8, %16, %5\n\tv_mfma_f32_16x16x32_bf16 %6, %8, %17, %6\n\tds_read_b128 %7, %15 offset:3072"
      : "+v"(ac[0][0]), "=&v"(pu[5]), "+v"(ac[0][1]), "=&v"(py[1][3]), "=&a"(fwo[8]), "+v"(ac[0][2]), "+v"(ac[0][3]), "=&a"(fwo[9]) : "v"(pu[6]), "a"(fw2[6][0]), "v"(vF), "s"(rsW), "s"(so0), "a"(fw2[6][1]), "v"(rr), "v"(ro), "a"(fw2[6][2]), "a"(fw2[6][3]) : "memory"); SB();
  WAITVB(12);
  SB();
  asm volatile("v_mfma_f32_16x16x32_bf16 %0, %5, %6, %0\n\tbuffer_load_dwordx4 %1, %7, %8, %9 offen offset:768 sc1\n\tv_mfma_f32_16x16x32_bf16 %2, %5, %10, %2\n\tds_read_b128 %3, %11 offset:11264\n\tv_mfma_f32_16x16x32_bf16 %4, %5, %12, %4"
      : "+v"(ac[0][0]), "=&v"(pu[6]), "+v"(ac[0][1]), "=&a"(fwo[10]), "+v"(ac[0][2]) : "v"(pu[7]), "a"(fw2[7][0]), "v"(vF), "s"(rsW), "s"(so0), "a"(fw2[7][1]), "v"(ro), "a"(fw2[7][2]) : "memory"); SB();
  WAITL(3);
  asm volatile("v_add_f32 %0, %3, %4\n\tv_mfma_f32_16x16x32_bf16 %1, %5, %6, %1\n\tv_add_f32 %2, %7, %4"
      : "=&v"(ye[0]), "+v"(ac[0][3]), "=&v"(ye[1]) : "v"(py[1][0][0]), "v"(bias), "v"(pu[7]), "a"(fw2[7][3]), "v"(py[1][0][1])); SB();
  WAITVB(12);
  SB();
  asm volatile("v_mfma_f32_16x16x32_bf16 %0, %9, %10, %0\n\tbuffer_load_dwordx4 %1, %11, %12, %13 offen offset:0 sc1\n\tv_mfma_f32_16x16x32_bf16 %2, %9, %14, %2\n\tv_add_f32 %3, %15, %16\n\tv_add_f32 %4, %17, %16\n\tv_mfma_f32_16x16x32_bf16 %5, %9, %18, %5\n\tv_add_f32 %6, %19, %20\n\tv_add_f32 %7, %21, %22\n\tv_mfma_f32_16x16x32_bf16 %8, %9, %23, %8"
      : "+v"(ac[0][0]), "=&v"(pu[7]), "+v"(ac[0][1]), "=&v"(ye[2]), "=&v"(ye[3]), "+v"(ac[0][2]), "=&v"(ze[0]), "=&v"(ze[1]), "+v"(ac[0][3]) : "v"(pu[8]), "a"(fw2[8][0]), "v"(vF), "s"(rsW), "s"(so1), "a"(fw2[8][1]), "v"(py[1][0][2]), "v"(bias), "v"(py[1][0][3]), "a"(fw2[8][2]), "v"(py[1][1][0]), "v"(py[1][2][0]), "v"(py[1][1][1]), "v"(py[1][2][1]), "a"(fw2[8][3]) : "memory"); SB();
  asm volatile("v_add_f32 %0, %1, %2"
      : "=&v"(ze[2]) : "v"(py[1][1][2]), "v"(py[1][2][2])); SB();
  WAITVB(12);
  SB();
  asm volatile("v_mfma_f32_16x16x32_bf16 %0, %9, %10, %0\n\tbuffer_load_dwordx4 %1, %11, %12, %13 offen offset:128 sc1\n\tv_mfma_f32_16x16x32_bf16 %2, %9, %14, %2\n\tv_add_f32 %3, %15, %16\n\tv_add_f32 %4, %4, %17\n\tv_mfma_f32_16x16x32_bf16 %5, %9, %18, %5\n\tv_add_f32 %6, %6, %19\n\tv_add_f32 %7, %7, %20\n\tv_mfma_f32_16x16x32_bf16 %8, %9, %21, %8"
      : "+v"(ac[0][0]), "=&v"(pu[8]), "+v"(ac[0][1]), "=&v"(ze[3]), "+v"(ye[0]), "+v"(ac[0][2]), "+v"(ye[1]), "+v"(ye[2]), "+v"(ac[0][3]) : "v"(pu[9]), "v"(fw2[9][0]), "v"(vF), "s"(rsW), "s"(so1), "v"(fw2[9][1]), "v"(py[1][1][3]), "v"(py[1][2][3]), "v"(py[1][3][0]), "v"(fw2[9][2]), "v"(py[1][3][1]), "v"(py[1][3][2]), "v"(fw2[9][3]) : "memory"); SB();
  asm volatile("v_add_f32 %0, %0, %1"
      : "+v"(ye[3]) : "v"(py[1][3][3])); SB();
  WAITVB(12);
  SB();
  asm volatile("v_mfma_f32_16x16x32_bf16 %0, %10, %11, %0\n\tbuffer_load_dwordx4 %1, %12, %13, %14 offen offset:256 sc1\n\tv_mfma_f32_16x16x32_bf16 %2, %10, %15, %2\n\tv_add_f32 %3, %3, %4\n\tv_add_f32 %5, %5, %16\n\tv_mfma_f32_16x16x32_bf16 %6, %10, %17, %6\n\tv_add_f32 %7, %7, %18\n\tv_add_f32 %8, %8, %19\n\tv_mfma_f32_16x16x32_bf16 %9, %10, %20, %9\n\tv_mul_f32 %4, 0xbfb8aa3b, %3"
      : "+v"(ac[0][0]), "=&v"(pu[9]), "+v"(ac[0][1]), "+v"(ye[0]), "+v"(ze[0]), "+v"(ye[1]), "+v"(ac[0][2]), "+v"(ye[2]), "+v"(ye[3]), "+v"(ac[0][3]) : "v"(pu[10]), "v"(fw2[10][0]), "v"(vF), "s"(rsW), "s"(so1), "v"(fw2[10][1]), "v"(ze[1]), "v"(fw2[10][2]), "v"(ze[2]), "v"(ze[3]), "v"(fw2[10][3]) : "memory"); SB();
  WAITVB(12);
  SB();
  asm volatile("v_mfma_f32_16x16x32_bf16 %0, %9, %10, %0\n\tbuffer_load_dwordx4 %1, %11, %12, %13 offen offset:384 sc1\n\tv_mfma_f32_16x16x32_bf16 %2, %9, %14, %2\n\tv_mul_f32 %3, 0xbfb8aa3b, %15\n\tv_mul_f32 %4, 0xbfb8aa3b, %16\n\tv_mfma_f32_16x16x32_bf16 %5, %9, %17, %5\n\tds_read_b128 %6, %18 offset:19456\n\tv_mul_f32 %7, 0xbfb8aa3b, %19\n\tv_mfma_f32_16x16x32_bf16 %8, %9, %20, %8"
      : "+v"(ac[0][0]), "=&v"(pu[10]), "+v"(ac[0][1]), "=&v"(ze[1]), "=&v"(ze[2]), "+v"(ac[0][2]), "=&a"(fwo[11]), "=&v"(ze[3]), "+v"(ac[0][3]) : "v"(pu[11]), "v"(fw2[11][0]), "v"(vF), "s"(rsW), "s"(so1), "v"(fw2[11][1]), "v"(ye[1]), "v"(ye[2]), "v"(fw2[11][2]), "v"(ro), "v"(ye[3]), "v"(fw2[11][3]) : "memory"); SB();
  WAITVB(12);
  SB();
  asm volatile("v_mfma_f32_16x16x32_bf16 %0, %8, %9, %0\n\tbuffer_load_dwordx4 %1, %10, %11, %12 offen offset:512 sc1\n\tv_mfma_f32_16x16x32_bf16 %2, %8, %13, %2\n\tv_exp_f32 %3, %3\n\tv_mfma_f32_16x16x32_bf16 %4, %8, %14, %4\n\tv_exp_f32 %5, %5\n\tv_mfma_f32_16x16x32_bf16 %6, %8, %15, %6\n\tds_read_b128 %7, %16 offset:4096"
      : "+v"(ac[0][0]), "=&v"(pu[11]), "+v"(ac[0][1]), "+v"(ze[0]), "+v"(ac[0][2]), "+v"(ze[1]), "+v"(ac[0][3]), "=&a"(fwo[12]) : "v"(pu[12]), "v"(fw2[12][0]), "v"(vF), "s"(rsW), "s"(so1), "v"(fw2[12][1]), "v"(fw2[12][2]), "v"(fw2[12][3]), "v"(ro) : "memory"); SB();
  WAITVB(12);
  SB();
  asm volatile("v_mfma_f32_16x16x32_bf16 %0, %9, %10, %0\n\tbuffer_load_dwordx4 %1, %11, %12, %13 offen offset:640 sc1\n\tv_mfma_f32_16x16x32_bf16 %2, %9, %14, %2\n\tv_exp_f32 %3, %3\n\tv_mfma_f32_16x16x32_bf16 %4, %9, %15, %4\n\tv_exp_f32 %5, %5\n\tv_mfma_f32_16x16x32_bf16 %6, %9, %16, %6\n\tv_add_f32 %7, 1.0, %7\n\tv_add_f32 %8, 1.0, %8"
      : "+v"(ac[0][0]), "=&v"(pu[12]), "+v"(ac[0][1]), "+v"(ze[2]), "+v"(ac[0][2]), "+v"(ze[3]), "+v"(ac[0][3]), "+v"(ze[0]), "+v"(ze[1]) : "v"(pu[13]), "v"(fw2[13][0]), "v"(vF), "s"(rsW), "s"(so1), "v"(fw2[13][1]), "v"(fw2[13][2]), "v"(fw2[13][3]) : "memory"); SB();
  STAMP(42);
  WAITVB(12);
  asm volatile("ds_read_b128 %0, %8 offset:12288\n\tv_mfma_f32_16x16x32_bf16 %1, %9, %10, 0\n\tbuffer_load_dwordx4 %2, %11, %12, %13 offen offset:768 sc1\n\tv_mfma_f32_16x16x32_bf16 %3, %14, %10, 0\n\tv_add_f32 %4, 1.0, %4\n\tv_add_f32 %5, 1.0, %5\n\tv_mfma_f32_16x16x32_bf16 %6, %15, %10, 0\n\tds_read_b128 %7, %8 offset:20480"
      : "=&a"(fwo[13]), "=&v"(as[0]), "=&v"(pu[13]), "=&v"(as[1]), "+v"(ze[2]), "+v"(ze[3]), "=&v"(as[2]), "=&a"(fwo[14]) : "v"(ro), "a"(fwo[0]), "v"(pu[0]), "v"(vF), "s"(rsW), "s"(so1), "a"(fwo[1]), "a"(fwo[2]) : "memory"); SB();
  WAITVB(12);
  SB();
  asm volatile("v_mfma_f32_16x16x32_bf16 %0, %5, %6, %0\n\tds_write_b128 %7, %8 offset:0\n\tv_mfma_f32_16x16x32_bf16 %1, %9, %6, %1\n\tv_rcp_f32 %2, %2\n\tv_mfma_f32_16x16x32_bf16 %3, %10, %6, %3\n\tds_read_b128 %4, %11 offset:5120"
      : "+v"(as[0]), "+v"(as[1]), "+v"(ze[0]), "+v"(as[2]), "=&a"(fwo[15]) : "a"(fwo[3]), "v"(pu[1]), "v"(rwd), "v"(ac[0][0]), "a"(fwo[4]), "a"(fwo[5]), "v"(ro) : "memory"); SB();
  WAITVB(11);
  asm volatile("ds_read_b128 %0, %4 offset:13312\n\tds_read_b128 %1, %4 offset:21504\n\tds_read_b128 %2, %4 offset:6144\n\tds_read_b128 %3, %4 offset:14336"
      : "=&a"(fwo[16]), "=&a"(fwo[17]), "=&a"(fwo[18]), "=&a"(fwo[19]) : "v"(ro) : "memory"); SB();
  WAITL(9);
  SB();
  asm volatile("v_mfma_f32_16x16x32_bf16 %0, %5, %6, %0\n\tds_write_b128 %7, %8 offset:1024\n\tv_mfma_f32_16x16x32_bf16 %1, %9, %6, %1\n\tv_rcp_f32 %2, %2\n\tv_mfma_f32_16x16x32_bf16 %3, %10, %6, %3\n\tds_read_b128 %4, %11 offset:22528"
      : "+v"(as[0]), "+v"(as[1]), "+v"(ze[1]), "+v"(as[2]), "=&a"(fwo[20]) : "a"(fwo[6]), "v"(pu[2]), "v"(rwd), "v"(ac[0][1]), "a"(fwo[7]), "a"(fwo[8]), "v"(ro) : "memory"); SB();
  WAITVB(10);
  SB();
  asm volatile("v_mfma_f32_16x16x32_bf16 %0, %5, %6, %0\n\tds_write_b128 %7, %8 offset:2048\n\tv_mfma_f32_16x16x32_bf16 %1, %9, %6, %1\n\tv_rcp_f32 %2, %2\n\tv_mfma_f32_16x16x32_bf16 %3, %10, %6, %3\n\tds_read_b128 %4, %11 offset:7168"
      : "+v"(as[0]), "+v"(as[1]), "+v"(ze[2]), "+v"(as[2]), "=&a"(fwo[21]) : "a"(fwo[9]), "v"(pu[3]), "v"(rwd), "v"(ac[0][2]), "a"(fwo[10]), "a"(fwo[11]), "v"(ro) : "memory"); SB();
  WAITVB(9);
  asm volatile("ds_read_b128 %0, %4 offset:15360\n\tds_read_b128 %1, %4 offset:23552\n\tds_read_b128 %2, %4 offset:32768\n\tds_read_b128 %3, %4 offset:38912"
      : "=&a"(fwo[22]), "=&a"(fwo[23]), "=&a"(fwo[24]), "=&a"(fwo[25]) : "v"(ro) : "memory"); SB();
  WAITL(10);
  SB();
  asm volatile("v_mfma_f32_16x16x32_bf16 %0, %5, %6, %0\n\tds_write_b128 %7, %8 offset:3072\n\tv_mfma_f32_16x16x32_bf16 %1, %9, %6, %1\n\tv_rcp_f32 %2, %2\n\tv_mfma_f32_16x16x32_bf16 %3, %10, %6, %3\n\tv_mul_f32 %4, %11, %12"
      : "+v"(as[0]), "+v"(as[1]), "+v"(ze[3]), "+v"(as[2]), "=&v"(ge[4]) : "a"(fwo[12]), "v"(pu[4]), "v"(rwd), "v"(ac[0][3]), "a"(fwo[13]), "a"(fwo[14]), "v"(ye[0]), "v"(ze[0]) : "memory"); SB();
  WAITVB(8);
  SB();
  asm volatile("v_mfma_f32_16x16x32_bf16 %0, %4, %5, %0\n\tv_mul_f32 %1, %6, %7\n\tv_mul_f32 %2, %8, %9\n\tv_mfma_f32_16x16x32_bf16 %3, %10, %5, %3"
      : "+v"(as[0]), "=&v"(ge[5]), "=&v"(ge[6]), "+v"(as[1]) : "a"(fwo[15]), "v"(pu[5]), "v"(ye[1]), "v"(ze[1]), "v"(ye[2]), "v"(ze[2]), "a"(fwo[16])); SB();
  WAITL(0);
  BAR0();
  SB();
  asm volatile("v_mfma_f32_16x16x32_bf16 %0, %1, %2, %0"
      : "+v"(as[2]) : "a"(fwo[17]), "v"(pu[5])); SB();
  WAITVB(7);
  asm volatile("ds_read_b128 %0, %14 offset:45056\n\tds_read_b128 %1, %14 offset:33792\n\tds_read_b128 %2, %14 offset:39936\n\tds_read_b128 %3, %14 offset:46080\n\tds_read_b128 %4, %14 offset:34816\n\tds_read_b128 %5, %14 offset:40960\n\tv_mfma_f32_16x16x32_bf16 %6, %15, %16, %6\n\tds_read_b128 %7, %17 offset:0\n\tds_read_b128 %8, %17 offset:4096\n\tv_mfma_f32_16x16x32_bf16 %9, %18, %16, %9\n\tds_read_b128 %10, %17 offset:8192\n\tds_read_b128 %11, %17 offset:12288\n\tv_mfma_f32_16x16x32_bf16 %12, %19, %16, %12\n\tv_mul_f32 %13, %20, %21"
      : "=&a"(fwo[26]), "=&a"(fwo[27]), "=&a"(fwo[28]), "=&a"(fwo[29]), "=&a"(fwo[30]), "=&a"(fwo[31]), "+v"(as[0]), "=&v"(py[2][0]), "=&v"(py[2][1]), "+v"(as[1]), "=&v"(py[2][2]), "=&v"(py[2][3]), "+v"(as[2]), "=&v"(ge[7]) : "v"(ro), "a"(fwo[18]), "v"(pu[6]), "v"(rr), "a"(fwo[19]), "a"(fwo[20]), "v"(ye[3]), "v"(ze[3]) : "memory"); SB();
  WAITVB(6);
  SB();
  asm volatile("v_mfma_f32_16x16x32_bf16 %0, %4, %5, %0\n\tds_read_b128 %1, %6 offset:47104\n\tv_mfma_f32_16x16x32_bf16 %2, %7, %5, %2\n\tv_mfma_f32_16x16x32_bf16 %3, %8, %5, %3"
      : "+v"(as[0]), "=&a"(fwo[32]), "+v"(as[1]), "+v"(as[2]) : "a"(fwo[21]), "v"(pu[7]), "v"(ro), "a"(fwo[22]), "a"(fwo[23]) : "memory"); SB();
  WAITVB(5);
  asm volatile("ds_read_b128 %0, %5 offset:35840\n\tds_read_b128 %1, %5 offset:41984\n\tds_read_b128 %2, %5 offset:48128\n\tds_read_b128 %3, %5 offset:36864\n\tds_read_b128 %4, %5 offset:43008"
      : "=&a"(fwo[33]), "=&a"(fwo[34]), "=&a"(fwo[35]), "=&a"(fwo[36]), "=&a"(fwo[37]) : "v"(ro) : "memory"); SB();
  WAITL(12);
  SB();
  asm volatile("v_mfma_f32_16x16x32_bf16 %0, %1, %2, %0"
      : "+v"(as[0]) : "a"(fwo[24]), "v"(pu[8])); SB();
  WAITL(6);
  asm volatile("v_add_f32 %0, %6, %7\n\tv_mfma_f32_16x16x32_bf16 %1, %8, %9, %1\n\tv_add_f32 %2, %10, %7\n\tv_add_f32 %3, %11, %7\n\tv_mfma_f32_16x16x32_bf16 %4, %12, %9, %4\n\tv_add_f32 %5, %13, %7"
      : "=&v"(ye[0]), "+v"(as[1]), "=&v"(ye[1]), "=&v"(ye[2]), "+v"(as[2]), "=&v"(ye[3]) : "v"(py[2][0][0]), "v"(bias), "a"(fwo[25]), "v"(pu[8]), "v"(py[2][0][1]), "v"(py[2][0][2]), "a"(fwo[26]), "v"(py[2][0][3])); SB();
  WAITVB(4);
  SB();
  asm volatile("v_mfma_f32_16x16x32_bf16 %0, %7, %8, %0\n\tv_add_f32 %1, %9, %10\n\tv_add_f32 %2, %11, %12\n\tv_mfma_f32_16x16x32_bf16 %3, %13, %8, %3\n\tv_add_f32 %4, %14, %15\n\tv_add_f32 %5, %16, %17\n\tv_mfma_f32_16x16x32_bf16 %6, %18, %8, %6"
      : "+v"(as[0]), "=&v"(ze[0]), "=&v"(ze[1]), "+v"(as[1]), "=&v"(ze[2]), "=&v"(ze[3]), "+v"(as[2]) : "a"(fwo[27]), "v"(pu[9]), "v"(py[2][1][0]), "v"(py[2][2][0]), "v"(py[2][1][1]), "v"(py[2][2][1]), "a"(fwo[28]), "v"(py[2][1][2]), "v"(py[2][2][2]), "v"(py[2][1][3]), "v"(py[2][2][3]), "a"(fwo[29])); SB();
  WAITVB(3);
  asm volatile("ds_read_b128 %0, %4 offset:49152\n\tds_read_b128 %1, %4 offset:37888\n\tds_read_b128 %2, %4 offset:44032\n\tds_read_b128 %3, %4 offset:50176"
      : "=&a"(fwo[38]), "=&a"(fwo[39]), "=&a"(fwo[40]), "=&a"(fwo[41]) : "v"(ro) : "memory"); SB();
  WAITL(6);
  SB();
  asm volatile("v_mfma_f32_16x16x32_bf16 %0, %7, %8, %0\n\tv_add_f32 %1, %1, %9\n\tv_add_f32 %2, %2, %10\n\tv_mfma_f32_16x16x32_bf16 %3, %11, %8, %3\n\tv_add_f32 %4, %4, %12\n\tv_add_f32 %5, %5, %13\n\tv_mfma_f32_16x16x32_bf16 %6, %14, %8, %6"
      : "+v"(as[0]), "+v"(ye[0]), "+v"(ye[1]), "+v"(as[1]), "+v"(ye[2]), "+v"(ye[3]), "+v"(as[2]) : "a"(fwo[30]), "v"(pu[10]), "v"(py[2][3][0]), "v"(py[2][3][1]), "a"(fwo[31]), "v"(py[2][3][2]), "v"(py[2][3][3]), "a"(fwo[32])); SB();
  WAITL(0);
  WAITVB(0);
  SB();
  asm volatile("v_mfma_f32_16x16x32_bf16 %0, %10, %11, %0\n\tv_add_f32 %1, %1, %2\n\tv_add_f32 %3, %3, %4\n\tv_mfma_f32_16x16x32_bf16 %0, %12, %13, %0\n\tv_add_f32 %5, %5, %6\n\tv_add_f32 %7, %7, %8\n\tv_mfma_f32_16x16x32_bf16 %0, %14, %15, %0\n\tv_mul_f32 %2, 0xbfb8aa3b, %1\n\tv_mul_f32 %4, 0xbfb8aa3b, %3\n\tv_mfma_f32_16x16x32_bf16 %9, %16, %11, %9\n\tv_mul_f32 %6, 0xbfb8aa3b, %5\n\tv_mul_f32 %8, 0xbfb8aa3b, %7\n\tv_mfma_f32_16x16x32_bf16 %9, %17, %13, %9\n\tv_exp_f32 %2, %2\n\tv_mfma_f32_16x16x32_bf16 %9, %18, %15, %9\n\tv_exp_f32 %4, %4"
      : "+v"(as[0]), "+v"(ye[0]), "+v"(ze[0]), "+v"(ye[1]), "+v"(ze[1]), "+v"(ye[2]), "+v"(ze[2]), "+v"(ye[3]), "+v"(ze[3]), "+v"(as[1]) : "a"(fwo[33]), "v"(pu[11]), "a"(fwo[36]), "v"(pu[12]), "a"(fwo[39]), "v"(pu[13]), "a"(fwo[34]), "a"(fwo[37]), "a"(fwo[40])); SB();
  asm volatile("v_mfma_f32_16x16x32_bf16 %0, %5, %6, %0\n\tv_exp_f32 %1, %1\n\tv_mfma_f32_16x16x32_bf16 %0, %7, %8, %0\n\tv_exp_f32 %2, %2\n\tv_mfma_f32_16x16x32_bf16 %0, %9, %10, %0\n\tv_add_f32 %3, 1.0, %3\n\tv_add_f32 %4, 1.0, %4"
      : "+v"(as[2]), "+v"(ze[2]), "+v"(ze[3]), "+v"(ze[0]), "+v"(ze[1]) : "a"(fwo[35]), "v"(pu[11]), "a"(fwo[38]), "v"(pu[12]), "a"(fwo[41]), "v"(pu[13])); SB();
  STAMP(44);
  asm volatile("v_add_f32 %0, 1.0, %0\n\tv_add_f32 %1, 1.0, %1\n\tv_rcp_f32 %2, %2\n\tv_rcp_f32 %3, %3\n\tv_rcp_f32 %0, %0\n\tv_rcp_f32 %1, %1\n\tv_mul_f32 %4, %11, %2\n\tv_mul_f32 %5, %12, %3\n\tv_mul_f32 %6, %13, %0\n\tv_mul_f32 %7, %14, %1\n\tv_fma_f32 %8, %15, %16, %8\n\tv_fma_f32 %9, %17, %18, %9\n\tv_fma_f32 %10, %19, %20, %10"
      : "+v"(ze[2]), "+v"(ze[3]), "+v"(ze[0]), "+v"(ze[1]), "=&v"(ge[8]), "=&v"(ge[9]), "=&v"(ge[10]), "=&v"(ge[11]), "+v"(xe[0]), "+v"(xe[1]), "+v"(xe[2]) : "v"(ye[0]), "v"(ye[1]), "v"(ye[2]), "v"(ye[3]), "v"(as[0][0]), "v"(ge[0]), "v"(as[0][1]), "v"(ge[1]), "v"(as[0][2]), "v"(ge[2])); SB();
  asm volatile("v_fma_f32 %0, %9, %10, %0\n\tv_cvt_pk_bf16_f32 %1, %11, %12\n\tv_cvt_pk_bf16_f32 %2, %13, %0\n\tv_fma_f32 %3, %14, %15, %3\n\tv_fma_f32 %4, %16, %17, %4\n\tv_fma_f32 %5, %18, %19, %5\n\tv_fma_f32 %6, %20, %21, %6\n\tv_cvt_pk_bf16_f32 %7, %3, %4\n\tv_cvt_pk_bf16_f32 %8, %5, %6"
      : "+v"(xe[3]), "=&v"(ov[0][0]), "=&v"(ov[0][1]), "+v"(xe[4]), "+v"(xe[5]), "+v"(xe[6]), "+v"(xe[7]), "=&v"(ov[1][0]), "=&v"(ov[1][1]) : "v"(as[0][3]), "v"(ge[3]), "v"(xe[0]), "v"(xe[1]), "v"(xe[2]), "v"(as[1][0]), "v"(ge[4]), "v"(as[1][1]), "v"(ge[5]), "v"(as[1][2]), "v"(ge[6]), "v"(as[1][3]), "v"(ge[7])); SB();
  asm volatile("ds_write_b64 %6, %7 offset:0\n\tv_fma_f32 %0, %8, %9, %0\n\tv_fma_f32 %1, %10, %11, %1\n\tv_fma_f32 %2, %12, %13, %2\n\tv_fma_f32 %3, %14, %15, %3\n\tv_cvt_pk_bf16_f32 %4, %0, %1\n\tv_cvt_pk_bf16_f32 %5, %2, %3\n\tds_write_b64 %16, %17 offset:0"
      : "+v"(xe[8]), "+v"(xe[9]), "+v"(xe[10]), "+v"(xe[11]), "=&v"(ov[2][0]), "=&v"(ov[2][1]) : "v"(xta[0]), "v"(ov[0]), "v"(as[2][0]), "v"(ge[8]), "v"(as[2][1]), "v"(ge[9]), "v"(as[2][2]), "v"(ge[10]), "v"(as[2][3]), "v"(ge[11]), "v"(xta[1]), "v"(ov[1]) : "memory"); SB();
  asm volatile("ds_write_b64 %0, %1 offset:0"
      :  : "v"(xta[2]), "v"(ov[2]) : "memory"); SB();
  { u32x4 t0_, t1_; DSR16(t0_, xo, 0); DSR16(t1_, xo, 1024); WAITLB(1);
    BS16(vXa, rsO_, 0, t0_, ""); WAITLB(0); BS16(vXb, rsO_, 1024, t1_, ""); }
  STAMP(23);

  STAMP(12);
  STAMP_DUMP();
  }
  } else {
  asm volatile("; NEED B");
  unsigned nonce0; asm volatile("s_load_dword %0, %1, 0x0" : "=&s"(nonce0) : "s"(ws) : "memory");
  const int iw = (4 * xq + jb - 28) * 4 + w, ih = (iw * 37) >> 8;
  const unsigned ldsI = lds0 + w * SCW;
  { u32x4 rsO = srd(wo, 448u * 896u);
    const unsigned vO_ = (28 * (r8 >> 2) + (r8 & 3)) * 896 + ((lane & 7) ^ ((r8 & 2) << 1)) * 16, sO_ = (4 * iw + 28 * ih) * 896;
    DMA1(vO_, rsO, sO_, ldsI, 0, 0, "");
    DMA1(vO_, rsO, sO_, ldsI, 896, 128, "");
    DMA1(vO_, rsO, sO_, ldsI, 1792, 256, "");
    DMA1(vO_, rsO, sO_, ldsI, 2688, 384, "");
    DMA1(vO_, rsO, sO_, ldsI, 3584, 512, "");
    DMA1(vO_, rsO, sO_, ldsI, 4480, 640, "");
    DMA1(vO_, rsO, sO_, ldsI, 5376, 768, "");
  }
  u32x4 rsW = srd((char*)(((size_t)ws + 8191) & ~(size_t)4095) + 512, WS_END); OPQ(rsW);
  const unsigned trb = (8 * g4 + q4) * 128 + pq * 8, trf = ((q4 >> 1) | ((g4 & 1) << 1)) * 32;
  const unsigned vF = r8 * 1024 + (lane & 7) * 16;
  const int r2 = (3 - (int)((size_t)w2 >> 7)) & 7;
  const int st = 8 * xq + r2, dt = jb - 28, db0 = 16 + 3 * dt;
#undef BLK
#define BLK(j) (db0 + ((j) == 2 ? 2 - 2 * (dt & 1) : ((j) ^ 0) + (dt & 1)))
  unsigned to = ldsI + q4 * 128 + ((g4 ^ (q4 & 2)) * 32) + pq * 8, vo[7], so_ = iw < 56 ? WS_WO + (iw >> 2) * 1024 + (iw & 3) * 32768 : 0x80000000u; OPQ(so_);
#pragma unroll
  for (int t = 0; t < 7; ++t) { const int k_ = 4 * t + g4, k7 = (k_ * 37) >> 8; vo[t] = k7 * (131072 - 896) + k_ * 128 + (n16 >> 3) * 16384 + (lane & 7) * 16; }
  u32x4 rs2 = srd(w2, 1792u * 4096u); OPQ(rs2);
  unsigned vW2[4];
#pragma unroll
  for (int i = 0; i < 4; ++i) vW2[i] = (8 * (r8 & 3) + 4 * (r8 >> 2)) * 4096 + st * 128 + ((lane & 7) ^ (2 * i)) * 16 + i * (4096 - 1024);
  int rot2 = 4 * dt + 2 * (st & 7) + 2; rot2 -= 14 * ((rot2 * 37) >> 9);
  bf16x8 fw2[14][4];
  unsigned ldsS = lds0 + S3B + w * 4096, ldsO = lds0 + WOB + w * 8192, ldsP = lds0 + WOB + 24576 + w * 6144, sWo, vWo = r8 * 16384 + (lane & 7) * 16, sWt;
  { const int k_ = BLK(w), k7 = (k_ * 37) >> 8; sWo = WS_WO + k7 * (131072 - 896) + k_ * 128; }
  unsigned fq, vFQ = WS_FQ + (lane < 56 ? lane : 55) * (4 * 4);
  asm volatile("" : "+v"(vW2[0]), "+v"(vW2[1]), "+v"(vW2[2]), "+v"(vW2[3]), "+v"(vFQ), "+v"(vWo)); asm volatile("" : "+s"(rot2), "+s"(ldsS), "+s"(ldsO), "+s"(ldsP), "+s"(sWo));
  if (w == 3) vWo = 0x40000000u; OPV(vWo);
  unsigned tS[4];
#pragma unroll
  for (int i = 0; i < 4; ++i) { tS[i] = ldsS + q4 * 1024 + g4 * 128 + pq * 8 + ((i ^ q4) * 32); OPV(tS[i]); }
  unsigned tO[4];
#pragma unroll
  for (int i = 0; i < 4; ++i) { tO[i] = ldsO + q4 * 1024 + g4 * 128 + pq * 8 + ((i ^ q4) * 32); OPV(tO[i]); }
  unsigned su[14], sw[14], sc_ = 14 * w + rot2; OPQ(sc_);
  { unsigned k_ = sc_ + (rot2 >= 14 ? -14 : 0); OPQ(k_); su[0] = k_ << 13; sw[0] = k_ << 17; OPQ(su[0]); OPQ(sw[0]); }
  { unsigned k_ = sc_ + (rot2 >= 13 ? -13 : 1); OPQ(k_); su[1] = k_ << 13; sw[1] = k_ << 17; OPQ(su[1]); OPQ(sw[1]); }
  { unsigned k_ = sc_ + (rot2 >= 12 ? -12 : 2); OPQ(k_); su[2] = k_ << 13; sw[2] = k_ << 17; OPQ(su[2]); OPQ(sw[2]); }
  { unsigned k_ = sc_ + (rot2 >= 11 ? -11 : 3); OPQ(k_); su[3] = k_ << 13; sw[3] = k_ << 17; OPQ(su[3]); OPQ(sw[3]); }
  { unsigned k_ = sc_ + (rot2 >= 10 ? -10 : 4); OPQ(k_); su[4] = k_ << 13; sw[4] = k_ << 17; OPQ(su[4]); OPQ(sw[4]); }
  { unsigned k_ = sc_ + (rot2 >= 9 ? -9 : 5); OPQ(k_); su[5] = k_ << 13; sw[5] = k_ << 17; OPQ(su[5]); OPQ(sw[5]); }
  { unsigned k_ = sc_ + (rot2 >= 8 ? -8 : 6); OPQ(k_); su[6] = k_ << 13; sw[6] = k_ << 17; OPQ(su[6]); OPQ(sw[6]); }
  { unsigned k_ = sc_ + (rot2 >= 7 ? -7 : 7); OPQ(k_); su[7] = k_ << 13; sw[7] = k_ << 17; OPQ(su[7]); OPQ(sw[7]); }
  { unsigned k_ = sc_ + (rot2 >= 6 ? -6 : 8); OPQ(k_); su[8] = k_ << 13; sw[8] = k_ << 17; OPQ(su[8]); OPQ(sw[8]); }
  { unsigned k_ = sc_ + (rot2 >= 5 ? -5 : 9); OPQ(k_); su[9] = k_ << 13; sw[9] = k_ << 17; OPQ(su[9]); OPQ(sw[9]); }
  { unsigned k_ = sc_ + (rot2 >= 4 ? -4 : 10); OPQ(k_); su[10] = k_ << 13; sw[10] = k_ << 17; OPQ(su[10]); OPQ(sw[10]); }
  { unsigned k_ = sc_ + (rot2 >= 3 ? -3 : 11); OPQ(k_); su[11] = k_ << 13; sw[11] = k_ << 17; OPQ(su[11]); OPQ(sw[11]); }
  { unsigned k_ = sc_ + (rot2 >= 2 ? -2 : 12); OPQ(k_); su[12] = k_ << 13; sw[12] = k_ << 17; OPQ(su[12]); OPQ(sw[12]); }
  { unsigned k_ = sc_ + (rot2 >= 1 ? -1 : 13); OPQ(k_); su[13] = k_ << 13; sw[13] = k_ << 17; OPQ(su[13]); OPQ(sw[13]); }
  unsigned vFU, vFO;
  { const int da = (db0 * 37) >> 8, db = ((db0 + 2) * 37) >> 8, l_ = lane < 56 ? lane : 55;
    vFU = WS_FU + ((l_ < 28 ? da * 128 + l_ : db * 128 + l_ - 28) + w * 28) * (4 * 4);
    vFO = WS_FO + ((2 * st + (w >> 1)) * 8 + (lane & 7)) * (4 * 16);
    asm volatile("" : "+v"(vFU), "+v"(vFO)); }
  unsigned vFU1 = WS_FU + (512 + ((db0 * 37) >> 8) * 128 + w * 28 + (lane < 28 ? lane : 27)) * (4 * 4), fu2; OPV(vFU1);
  unsigned vFj[4], so0 = WS_OT + (4 * st + w) * 16384, so1 = so0 + 8192;
#pragma unroll
  for (int j = 0; j < 3; ++j) { const int k_ = BLK(j), k7 = (k_ * 37) >> 8; vFj[j] = vF + (WS_UT + k7 * (56 * 8192 - 896) + k_ * 128); OPV(vFj[j]); }
  asm volatile("" : "+s"(so0), "+s"(so1));
  const int tau = 2 * (n16 & 7) + (n16 >> 3);
  unsigned rwd = ldsS + vL16, rr = lds0 + S3B + w * 1024 + (16 * g4 + tau) * 16, ro = lds0 + WOB + vL16;
  unsigned xo = ldsS + 34816 + vL16, braw, xt = ldsS + 34816 + tau * 128 + (((g4 >> 1) ^ (n16 & 7)) * 16) + (g4 & 1) * 8, vB2 = (64 * st + 16 * w + tau) * 2;
  unsigned xs = xt ^ (32 * 0);
  asm volatile("" : "+v"(rwd), "+v"(rr), "+v"(ro), "+v"(xt), "+v"(xs), "+v"(xo), "+v"(vB2));
  unsigned vXa, vXb;
  { const int ga = (lane & 7) ^ (r8 >> 1), gb = ga ^ 4, r_ = (64 * st + 16 * w + r8) * 896;
    vXa = ga < 4 ? r_ + (db0 + (dt & 1)) * 32 + ga * 16 : ga < 6 ? r_ + BLK(2) * 32 + (ga - 4) * 16 : 0x40000000;
    vXb = (gb < 4 ? r_ + (db0 + (dt & 1)) * 32 + gb * 16 : gb < 6 ? r_ + BLK(2) * 32 + (gb - 4) * 16 : 0x40000000) + (8 * 896 - 1024); OPV(vXa); OPV(vXb); }
  unsigned nonce; asm volatile("s_waitcnt lgkmcnt(0)\n\ts_add_u32 %0, %1, 1" : "=s"(nonce) : "s"(nonce0) : "memory", "scc");
  asm volatile("s_waitcnt vmcnt(6)"
      :  :  : "memory"); SB();
  { const bf16x8 q_ = tr8u(to + 0, 512);
    HS16(vo[0], so_, 0, __builtin_bit_cast(u32x4, q_)); }
  asm volatile("s_waitcnt vmcnt(6)"
      :  :  : "memory"); SB();
  { const bf16x8 q_ = tr8u(to + 1024, 512);
    HS16(vo[1], so_, 0, __builtin_bit_cast(u32x4, q_)); }
  asm volatile("s_waitcnt vmcnt(6)"
      :  :  : "memory"); SB();
  { const bf16x8 q_ = tr8u(to + 2048, 512);
    HS16(vo[2], so_, 0, __builtin_bit_cast(u32x4, q_)); }
  asm volatile("s_waitcnt vmcnt(6)"
      :  :  : "memory"); SB();
  { const bf16x8 q_ = tr8u(to + 3072, 512);
    HS16(vo[3], so_, 0, __builtin_bit_cast(u32x4, q_)); }
  asm volatile("s_waitcnt vmcnt(6)"
      :  :  : "memory"); SB();
  { const bf16x8 q_ = tr8u(to + 4096, 512);
    HS16(vo[4], so_, 0, __builtin_bit_cast(u32x4, q_)); }
  asm volatile("s_waitcnt vmcnt(6)"
      :  :  : "memory"); SB();
  { const bf16x8 q_ = tr8u(to + 5120, 512);
    HS16(vo[5], so_, 0, __builtin_bit_cast(u32x4, q_)); }
  asm volatile("s_waitcnt vmcnt(6)"
      :  :  : "memory"); SB();
  { const bf16x8 q_ = tr8u(to + 6144, 512);
    HS16(vo[6], so_, 0, __builtin_bit_cast(u32x4, q_)); }
  for (int k = 0; k < 0; ++k) __builtin_amdgcn_s_sleep(4);
  const unsigned vT = (lane & 7) == r2 ? 0x40000000u : (448 * w + 8 * dt + r8) * 4096 + (8 * xq + (lane & 7)) * 128; const int k0 = (2 * (lane & 7)) % 14;
  { const unsigned v_ = vT + (k0 + 0 >= 14 ? k0 + 0 - 14 : k0 + 0) * 131072; TOUCH(v_, rs2, 0u, 0); }
  { const unsigned v_ = vT + (k0 + 4 >= 14 ? k0 + 4 - 14 : k0 + 4) * 131072; TOUCH(v_, rs2, 0u, 0); }
  asm volatile("s_waitcnt vmcnt(1)"
      :  :  : "memory"); SB();
  { const unsigned v_ = vT + (k0 + 8 >= 14 ? k0 + 8 - 14 : k0 + 8) * 131072; TOUCH(v_, rs2, 0u, 0); }
  asm volatile("s_waitcnt vmcnt(3)"
      :  :  : "memory"); SB();
  STFQ(WS_FQ + iw * (4 * 4));
  STAMP(4);
  asm volatile("s_waitcnt vmcnt(1)"
      :  :  : "memory"); SB();
  { const unsigned v_ = vT + (k0 + 12 >= 14 ? k0 + 12 - 14 : k0 + 12) * 131072; TOUCH(v_, rs2, 0u, 0); }
  asm volatile("s_waitcnt vmcnt(1)"
      :  :  : "memory"); SB();
  { const unsigned v_ = vT + (k0 + 1 >= 14 ? k0 + 1 - 14 : k0 + 1) * 131072; TOUCH(v_, rs2, 0u, 0); }
  asm volatile("s_waitcnt vmcnt(1)"
      :  :  : "memory"); SB();
  { const unsigned v_ = vT + (k0 + 5 >= 14 ? k0 + 5 - 14 : k0 + 5) * 131072; TOUCH(v_, rs2, 0u, 0); }
  asm volatile("s_waitcnt vmcnt(1)"
      :  :  : "memory"); SB();
  { const unsigned v_ = vT + (k0 + 9 >= 14 ? k0 + 9 - 14 : k0 + 9) * 131072; TOUCH(v_, rs2, 0u, 0); }
  asm volatile("s_waitcnt vmcnt(1)"
      :  :  : "memory"); SB();
  { const unsigned v_ = vT + (k0 + 13 >= 14 ? k0 + 13 - 14 : k0 + 13) * 131072; TOUCH(v_, rs2, 0u, 0); }
  asm volatile("s_waitcnt vmcnt(1)"
      :  :  : "memory"); SB();
  { const unsigned v_ = vT + (k0 + 2 >= 14 ? k0 + 2 - 14 : k0 + 2) * 131072; TOUCH(v_, rs2, 0u, 0); }
  asm volatile("s_waitcnt vmcnt(1)"
      :  :  : "memory"); SB();
  { const unsigned v_ = vT + (k0 + 6 >= 14 ? k0 + 6 - 14 : k0 + 6) * 131072; TOUCH(v_, rs2, 0u, 0); }
  STAMP(3);
  asm volatile("s_add_u32 m0, %0, 0\n\ts_nop 0\n\tbuffer_load_dwordx4 %1, %2, %3 offen offset:0  lds\n\tbuffer_load_dwordx4 %4, %2, %3 offen offset:1024  lds\n\tbuffer_load_dwordx4 %5, %2, %3 offen offset:2048  lds\n\tbuffer_load_dwordx4 %6, %2, %3 offen offset:3072  lds\n\ts_add_u32 m0, %0, 4096\n\ts_nop 0\n\tbuffer_load_dwordx4 %1, %2, %7 offen offset:0  lds\n\tbuffer_load_dwordx4 %4, %2, %7 offen offset:1024  lds\n\tbuffer_load_dwordx4 %5, %2, %7 offen offset:2048  lds\n\tbuffer_load_dwordx4 %6, %2, %7 offen offset:3072  lds\n\ts_add_u32 m0, %8, 18432\n\ts_nop 0\n\tbuffer_load_dwordx4 %1, %2, %9 offen offset:0  lds\n\tbuffer_load_dwordx4 %4, %2, %9 offen offset:1024  lds\n\tbuffer_load_dwordx4 %5, %2, %9 offen offset:2048  lds\n\tbuffer_load_dwordx4 %6, %2, %9 offen offset:3072  lds\n\ts_add_u32 m0, %8, 34816\n\ts_nop 0\n\tbuffer_load_dwordx4 %1, %2, %10 offen offset:0  lds\n\tbuffer_load_dwordx4 %4, %2, %10 offen offset:1024  lds\n\tbuffer_load_dwordx4 %5, %2, %10 offen offset:2048  lds\n\tbuffer_load_dwordx4 %6, %2, %10 offen offset:3072  lds\n\ts_waitcnt vmcnt(12)"
      :  : "s"(ldsO), "v"(vW2[0]), "s"(rs2), "s"(sw[0]), "v"(vW2[1]), "v"(vW2[2]), "v"(vW2[3]), "s"(sw[1]), "s"(ldsS), "s"(sw[2]), "s"(sw[3]) : "memory", "scc"); SB();
  fw2[0][0] = tr8u(tO[0] + 0, 512);
  fw2[0][1] = tr8u(tO[1] + 0, 512);
  fw2[0][2] = tr8u(tO[2] + 0, 512);
  fw2[0][3] = tr8u(tO[3] + 0, 512);
  asm volatile("s_add_u32 m0, %0, 0\n\ts_nop 0\n\tbuffer_load_dwordx4 %1, %2, %3 offen offset:0  lds\n\tbuffer_load_dwordx4 %4, %2, %3 offen offset:1024  lds\n\tbuffer_load_dwordx4 %5, %2, %3 offen offset:2048  lds\n\tbuffer_load_dwordx4 %6, %2, %3 offen offset:3072  lds\n\ts_waitcnt vmcnt(12)"
      :  : "s"(ldsS), "v"(vW2[0]), "s"(rs2), "s"(sw[4]), "v"(vW2[1]), "v"(vW2[2]), "v"(vW2[3]) : "memory", "scc"); SB();
  fw2[1][0] = tr8u(tO[0] + 4096, 512);
  fw2[1][1] = tr8u(tO[1] + 4096, 512);
  fw2[1][2] = tr8u(tO[2] + 4096, 512);
  fw2[1][3] = tr8u(tO[3] + 4096, 512);
  WAITL(8);
  asm volatile("s_add_u32 m0, %0, 0\n\ts_nop 0\n\tbuffer_load_dwordx4 %1, %2, %3 offen offset:0  lds\n\tbuffer_load_dwordx4 %4, %2, %3 offen offset:1024  lds\n\tbuffer_load_dwordx4 %5, %2, %3 offen offset:2048  lds\n\tbuffer_load_dwordx4 %6, %2, %3 offen offset:3072  lds\n\ts_waitcnt vmcnt(12)"
      :  : "s"(ldsO), "v"(vW2[0]), "s"(rs2), "s"(sw[5]), "v"(vW2[1]), "v"(vW2[2]), "v"(vW2[3]) : "memory", "scc"); SB();
  fw2[2][0] = tr8u(tS[0] + 18432, 512);
  fw2[2][1] = tr8u(tS[1] + 18432, 512);
  fw2[2][2] = tr8u(tS[2] + 18432, 512);
  fw2[2][3] = tr8u(tS[3] + 18432, 512);
  WAITL(8);
  asm volatile("s_add_u32 m0, %0, 4096\n\ts_nop 0\n\tbuffer_load_dwordx4 %1, %2, %3 offen offset:0  lds\n\tbuffer_load_dwordx4 %4, %2, %3 offen offset:1024  lds\n\tbuffer_load_dwordx4 %5, %2, %3 offen offset:2048  lds\n\tbuffer_load_dwordx4 %6, %2, %3 offen offset:3072  lds\n\ts_waitcnt vmcnt(12)"
      :  : "s"(ldsO), "v"(vW2[0]), "s"(rs2), "s"(sw[6]), "v"(vW2[1]), "v"(vW2[2]), "v"(vW2[3]) : "memory", "scc"); SB();
  fw2[3][0] = tr8u(tS[0] + 34816, 512);
  fw2[3][1] = tr8u(tS[1] + 34816, 512);
  fw2[3][2] = tr8u(tS[2] + 34816, 512);
  fw2[3][3] = tr8u(tS[3] + 34816, 512);
  WAITL(8);
  asm volatile("s_add_u32 m0, %0, 18432\n\ts_nop 0\n\tbuffer_load_dwordx4 %1, %2, %3 offen offset:0  lds\n\tbuffer_load_dwordx4 %4, %2, %3 offen offset:1024  lds\n\tbuffer_load_dwordx4 %5, %2, %3 offen offset:2048  lds\n\tbuffer_load_dwordx4 %6, %2, %3 offen offset:3072  lds\n\ts_waitcnt vmcnt(12)"
      :  : "s"(ldsS), "v"(vW2[0]), "s"(rs2), "s"(sw[7]), "v"(vW2[1]), "v"(vW2[2]), "v"(vW2[3]) : "memory", "scc"); SB();
  fw2[4][0] = tr8u(tS[0] + 0, 512);
  fw2[4][1] = tr8u(tS[1] + 0, 512);
  fw2[4][2] = tr8u(tS[2] + 0, 512);
  fw2[4][3] = tr8u(tS[3] + 0, 512);
  WAITL(8);
  asm volatile("s_add_u32 m0, %0, 34816\n\ts_nop 0\n\tbuffer_load_dwordx4 %1, %2, %3 offen offset:0  lds\n\tbuffer_load_dwordx4 %4, %2, %3 offen offset:1024  lds\n\tbuffer_load_dwordx4 %5, %2, %3 offen offset:2048  lds\n\tbuffer_load_dwordx4 %6, %2, %3 offen offset:3072  lds\n\ts_waitcnt vmcnt(12)"
      :  : "s"(ldsS), "v"(vW2[0]), "s"(rs2), "s"(sw[8]), "v"(vW2[1]), "v"(vW2[2]), "v"(vW2[3]) : "memory", "scc"); SB();
  fw2[5][0] = tr8u(tO[0] + 0, 512);
  fw2[5][1] = tr8u(tO[1] + 0, 512);
  fw2[5][2] = tr8u(tO[2] + 0, 512);
  fw2[5][3] = tr8u(tO[3] + 0, 512);
  WAITL(8);
  asm volatile("s_add_u32 m0, %0, 0\n\ts_nop 0\n\tbuffer_load_dwordx4 %1, %2, %3 offen offset:0  lds\n\tbuffer_load_dwordx4 %4, %2, %3 offen offset:1024  lds\n\tbuffer_load_dwordx4 %5, %2, %3 offen offset:2048  lds\n\tbuffer_load_dwordx4 %6, %2, %3 offen offset:3072  lds\n\ts_waitcnt vmcnt(12)"
      :  : "s"(ldsS), "v"(vW2[0]), "s"(rs2), "s"(sw[9]), "v"(vW2[1]), "v"(vW2[2]), "v"(vW2[3]) : "memory", "scc"); SB();
  fw2[6][0] = tr8u(tO[0] + 4096, 512);
  fw2[6][1] = tr8u(tO[1] + 4096, 512);
  fw2[6][2] = tr8u(tO[2] + 4096, 512);
  fw2[6][3] = tr8u(tO[3] + 4096, 512);
  WAITL(8);
  asm volatile("s_add_u32 m0, %0, 0\n\ts_nop 0\n\tbuffer_load_dwordx4 %1, %2, %3 offen offset:0  lds\n\tbuffer_load_dwordx4 %4, %2, %3 offen offset:1024  lds\n\tbuffer_load_dwordx4 %5, %2, %3 offen offset:2048  lds\n\tbuffer_load_dwordx4 %6, %2, %3 offen offset:3072  lds\n\ts_waitcnt vmcnt(12)"
      :  : "s"(ldsO), "v"(vW2[0]), "s"(rs2), "s"(sw[10]), "v"(vW2[1]), "v"(vW2[2]), "v"(vW2[3]) : "memory", "scc"); SB();
  fw2[7][0] = tr8u(tS[0] + 18432, 512);
  fw2[7][1] = tr8u(tS[1] + 18432, 512);
  fw2[7][2] = tr8u(tS[2] + 18432, 512);
  fw2[7][3] = tr8u(tS[3] + 18432, 512);
  WAITL(0);
  asm volatile("s_add_u32 m0, %0, 18432\n\ts_nop 0\n\tbuffer_load_dwordx4 %1, %2, %3 offen offset:0  lds\n\tbuffer_load_dwordx4 %4, %2, %3 offen offset:1024  lds\n\tbuffer_load_dwordx4 %5, %2, %3 offen offset:2048  lds\n\tbuffer_load_dwordx4 %6, %2, %3 offen offset:3072  lds\n\ts_waitcnt vmcnt(12)"
      :  : "s"(ldsS), "v"(vW2[0]), "s"(rs2), "s"(sw[11]), "v"(vW2[1]), "v"(vW2[2]), "v"(vW2[3]) : "memory", "scc"); SB();
  fw2[8][0] = tr8u(tS[0] + 34816, 512);
  fw2[8][1] = tr8u(tS[1] + 34816, 512);
  fw2[8][2] = tr8u(tS[2] + 34816, 512);
  fw2[8][3] = tr8u(tS[3] + 34816, 512);
  WAITL(0);
  asm volatile("s_add_u32 m0, %0, 34816\n\ts_nop 0\n\tbuffer_load_dwordx4 %1, %2, %3 offen offset:0  lds\n\tbuffer_load_dwordx4 %4, %2, %3 offen offset:1024  lds\n\tbuffer_load_dwordx4 %5, %2, %3 offen offset:2048  lds\n\tbuffer_load_dwordx4 %6, %2, %3 offen offset:3072  lds\n\ts_waitcnt vmcnt(12)"
      :  : "s"(ldsS), "v"(vW2[0]), "s"(rs2), "s"(sw[12]), "v"(vW2[1]), "v"(vW2[2]), "v"(vW2[3]) : "memory", "scc"); SB();
  fw2[9][0] = tr8u(tS[0] + 0, 512);
  fw2[9][1] = tr8u(tS[1] + 0, 512);
  fw2[9][2] = tr8u(tS[2] + 0, 512);
  fw2[9][3] = tr8u(tS[3] + 0, 512);
  WAITL(0);
  asm volatile("s_add_u32 m0, %0, 0\n\ts_nop 0\n\tbuffer_load_dwordx4 %1, %2, %3 offen offset:0  lds\n\tbuffer_load_dwordx4 %4, %2, %3 offen offset:1024  lds\n\tbuffer_load_dwordx4 %5, %2, %3 offen offset:2048  lds\n\tbuffer_load_dwordx4 %6, %2, %3 offen offset:3072  lds\n\ts_waitcnt vmcnt(12)"
      :  : "s"(ldsS), "v"(vW2[0]), "s"(rs2), "s"(sw[13]), "v"(vW2[1]), "v"(vW2[2]), "v"(vW2[3]) : "memory", "scc"); SB();
  fw2[10][0] = tr8u(tO[0] + 0, 512);
  fw2[10][1] = tr8u(tO[1] + 0, 512);
  fw2[10][2] = tr8u(tO[2] + 0, 512);
  fw2[10][3] = tr8u(tO[3] + 0, 512);
  asm volatile("s_waitcnt vmcnt(8)"
      :  :  : "memory"); SB();
  fw2[11][0] = tr8u(tS[0] + 18432, 512);
  fw2[11][1] = tr8u(tS[1] + 18432, 512);
  fw2[11][2] = tr8u(tS[2] + 18432, 512);
  fw2[11][3] = tr8u(tS[3] + 18432, 512);
  asm volatile("s_waitcnt vmcnt(4)"
      :  :  : "memory"); SB();
  fw2[12][0] = tr8u(tS[0] + 34816, 512);
  fw2[12][1] = tr8u(tS[1] + 34816, 512);
  fw2[12][2] = tr8u(tS[2] + 34816, 512);
  fw2[12][3] = tr8u(tS[3] + 34816, 512);
  asm volatile("s_waitcnt vmcnt(0)"
      :  :  : "memory"); SB();
  fw2[13][0] = tr8u(tS[0] + 0, 512);
  fw2[13][1] = tr8u(tS[1] + 0, 512);
  fw2[13][2] = tr8u(tS[2] + 0, 512);
  fw2[13][3] = tr8u(tS[3] + 0, 512);
  poll0<16>(rsW, vFQ, nonce);
  asm volatile("s_add_u32 m0, %1, 8192\n\ts_add_u32 %0, %2, 8192"
      : "=&s"(sWt) : "s"(ldsP), "s"(sWo) : "memory", "scc"); SB();
  DMAW4(vWo, rsW, sWt);
  u32x4 rsX = srd(x, 2048u * 896u); OPQ(rsX);
  WAITL(8);
  asm volatile("s_add_u32 m0, %2, 34816\n\ts_nop 0\n\tbuffer_load_dwordx4 %3, %4, %5 offen offset:0  lds\n\tbuffer_load_dwordx4 %6, %4, %5 offen offset:1024  lds\n\tglobal_load_ushort %0, %7, %8 offset:0\n\ts_add_u32 m0, %9, 12288\n\ts_add_u32 %1, %10, 12288"
      : "=&v"(braw), "=&s"(sWt) : "s"(ldsS), "v"(vXa), "s"(rsX), "s"(0u), "v"(vXb), "v"(vB2), "s"(b2), "s"(ldsP), "s"(sWo) : "memory", "scc"); SB();
  DMAW2(vWo, rsW, sWt);
  asm volatile("s_add_u32 m0, %1, 0\n\ts_add_u32 %0, %2, 0"
      : "=&s"(sWt) : "s"(ldsO), "s"(sWo) : "memory", "scc"); SB();
  DMAW4(vWo, rsW, sWt);
  asm volatile("s_add_u32 m0, %1, 4096\n\ts_add_u32 %0, %2, 4096"
      : "=&s"(sWt) : "s"(ldsO), "s"(sWo) : "memory", "scc"); SB();
  DMAW4(vWo, rsW, sWt);
  STAMP(16);
  asm volatile("s_waitcnt vmcnt(0)"
      :  :  : "memory"); SB();
  BAR();
  STAMP(5);
  for (int k = 0; k < 0; ++k) __builtin_amdgcn_s_sleep(4);
  {
  unsigned fz[3], vZ0 = WS_FU + (w * 128 + (lane < 56 ? lane : 55)) * (4 * 4), vZ1 = vZ0 + 56 * (4 * 4), vZ2 = WS_FQ + lane * (4 * 4); OPV(vZ0); OPV(vZ1); OPV(vZ2);
  bf16x8 pu[14], fwo[56];
  f32x4 ac[2][4], as[4], py[4][4];
  float ye[4], ze[4], ge[16], xe[16], bias;
  u32x2 ov[4]; unsigned fo;
  u32x2 xv[4]; unsigned xta[4];
  if (w != xq) poll0<16>(rsW, vFU, nonce);
  BAR0();
  if (w == xq) poll0<16>(rsW, vFU, nonce);
  STAMP(17);
  asm volatile("buffer_load_dwordx4 %0, %10, %11, %12 offen offset:0 sc1\n\tbuffer_load_dwordx4 %1, %10, %11, %13 offen offset:0 sc1\n\tbuffer_load_dwordx4 %2, %10, %11, %14 offen offset:0 sc1\n\tbuffer_load_dwordx4 %3, %10, %11, %15 offen offset:0 sc1\n\tbuffer_load_dwordx4 %4, %10, %11, %16 offen offset:0 sc1\n\tbuffer_load_dwordx4 %5, %10, %11, %17 offen offset:0 sc1\n\tbuffer_load_dwordx4 %6, %10, %11, %18 offen offset:0 sc1\n\tbuffer_load_dwordx4 %7, %10, %11, %19 offen offset:0 sc1\n\tbuffer_load_dwordx4 %8, %10, %11, %20 offen offset:0 sc1\n\tbuffer_load_dwordx4 %9, %10, %11, %21 offen offset:0 sc1"
      : "=&v"(pu[0]), "=&v"(pu[1]), "=&v"(pu[2]), "=&v"(pu[3]), "=&v"(pu[4]), "=&v"(pu[5]), "=&v"(pu[6]), "=&v"(pu[7]), "=&v"(pu[8]), "=&v"(pu[9]) : "v"(vFj[0]), "s"(rsW), "s"(su[0]), "s"(su[1]), "s"(su[2]), "s"(su[3]), "s"(su[4]), "s"(su[5]), "s"(su[6]), "s"(su[7]), "s"(su[8]), "s"(su[9]) : "memory"); SB();
  STAMP(39);
  WAITVB(9);
  SB();
  asm volatile("v_mfma_f32_16x16x32_bf16 %0, %8, %9, 0\n\tbuffer_load_dwordx4 %1, %10, %11, %12 offen offset:0 sc1\n\tv_mfma_f32_16x16x32_bf16 %2, %8, %13, 0\n\tds_read_b128 %3, %14 offset:0\n\tv_mfma_f32_16x16x32_bf16 %4, %8, %15, 0\n\tbuffer_load_dwordx4 %5, %10, %11, %16 offen offset:0 sc1\n\tv_mfma_f32_16x16x32_bf16 %6, %8, %17, 0\n\tds_read_b128 %7, %14 offset:8192"
      : "=&v"(ac[0][0]), "=&v"(pu[10]), "=&v"(ac[0][1]), "=&a"(fwo[0]), "=&v"(ac[0][2]), "=&v"(pu[11]), "=&v"(ac[0][3]), "=&a"(fwo[1]) : "v"(pu[0]), "a"(fw2[0][0]), "v"(vFj[0]), "s"(rsW), "s"(su[10]), "a"(fw2[0][1]), "v"(ro), "a"(fw2[0][2]), "s"(su[11]), "a"(fw2[0][3]) : "memory"); SB();
  WAITVB(10);
  SB();
  asm volatile("v_mfma_f32_16x16x32_bf16 %0, %8, %9, %0\n\tbuffer_load_dwordx4 %1, %10, %11, %12 offen offset:0 sc1\n\tv_mfma_f32_16x16x32_bf16 %2, %8, %13, %2\n\tds_read_b128 %3, %14 offset:16384\n\tv_mfma_f32_16x16x32_bf16 %4, %8, %15, %4\n\tbuffer_load_dwordx4 %5, %10, %11, %16 offen offset:0 sc1\n\tv_mfma_f32_16x16x32_bf16 %6, %8, %17, %6\n\tds_read_b128 %7, %14 offset:1024"
      : "+v"(ac[0][0]), "=&v"(pu[12]), "+v"(ac[0][1]), "=&a"(fwo[2]), "+v"(ac[0][2]), "=&v"(pu[13]), "+v"(ac[0][3]), "=&a"(fwo[3]) : "v"(pu[1]), "a"(fw2[1][0]), "v"(vFj[0]), "s"(rsW), "s"(su[12]), "a"(fw2[1][1]), "v"(ro), "a"(fw2[1][2]), "s"(su[13]), "a"(fw2[1][3]) : "memory"); SB();
  WAITVB(11);
  SB();
  asm volatile("v_mfma_f32_16x16x32_bf16 %0, %3, %4, %0\n\tbuffer_load_dwordx4 %1, %5, %6, %7 offen offset:0 sc1\n\tv_mfma_f32_16x16x32_bf16 %2, %3, %8, %2"
      : "+v"(ac[0][0]), "=&v"(pu[0]), "+v"(ac[0][1]) : "v"(pu[2]), "a"(fw2[2][0]), "v"(vFj[1]), "s"(rsW), "s"(su[0]), "a"(fw2[2][1]) : "memory"); SB();
  WAITVB(25);
  asm volatile("v_lshlrev_b32 %0, 16, %5\n\tv_mfma_f32_16x16x32_bf16 %1, %6, %7, %1\n\tbuffer_load_dwordx4 %2, %8, %9, %10 offen offset:0 sc1\n\tv_mfma_f32_16x16x32_bf16 %3, %6, %11, %3\n\tv_xor_b32 %4, 0, %12"
      : "=&v"(bias), "+v"(ac[0][2]), "=&v"(pu[1]), "+v"(ac[0][3]), "=&v"(xta[0]) : "v"(braw), "v"(pu[2]), "a"(fw2[2][2]), "v"(vFj[1]), "s"(rsW), "s"(su[1]), "a"(fw2[2][3]), "v"(xs) : "memory"); SB();
  WAITVB(12);
  SB();
  asm volatile("v_mfma_f32_16x16x32_bf16 %0, %10, %11, %0\n\tbuffer_load_dwordx4 %1, %12, %13, %14 offen offset:0 sc1\n\tv_mfma_f32_16x16x32_bf16 %2, %10, %15, %2\n\tv_xor_b32 %3, 32, %16\n\tv_xor_b32 %4, 64, %17\n\tv_mfma_f32_16x16x32_bf16 %5, %10, %18, %5\n\tds_read_b64 %6, %19 offset:0\n\tds_read_b64 %7, %3 offset:0\n\tv_mfma_f32_16x16x32_bf16 %8, %10, %20, %8\n\tds_read_b64 %9, %4 offset:0"
      : "+v"(ac[0][0]), "=&v"(pu[2]), "+v"(ac[0][1]), "=&v"(xta[1]), "=&v"(xta[2]), "+v"(ac[0][2]), "=&v"(xv[0]), "=&v"(xv[1]), "+v"(ac[0][3]), "=&v"(xv[2]) : "v"(pu[3]), "a"(fw2[3][0]), "v"(vFj[1]), "s"(rsW), "s"(su[2]), "a"(fw2[3][1]), "v"(xs), "v"(xt), "a"(fw2[3][2]), "v"(xta[0]), "a"(fw2[3][3]) : "memory"); SB();
  WAITVB(12);
  SB();
  asm volatile("v_mfma_f32_16x16x32_bf16 %0, %7, %8, %0\n\tbuffer_load_dwordx4 %1, %9, %10, %11 offen offset:0 sc1\n\tv_mfma_f32_16x16x32_bf16 %2, %7, %12, %2\n\tds_read_b128 %3, %13 offset:9216\n\tv_mfma_f32_16x16x32_bf16 %4, %7, %14, %4\n\tv_mfma_f32_16x16x32_bf16 %5, %7, %15, %5\n\tds_read_b128 %6, %13 offset:17408"
      : "+v"(ac[0][0]), "=&v"(pu[3]), "+v"(ac[0][1]), "=&a"(fwo[4]), "+v"(ac[0][2]), "+v"(ac[0][3]), "=&a"(fwo[5]) : "v"(pu[4]), "a"(fw2[4][0]), "v"(vFj[1]), "s"(rsW), "s"(su[3]), "a"(fw2[4][1]), "v"(ro), "a"(fw2[4][2]), "a"(fw2[4][3]) : "memory"); SB();
  WAITVB(12);
  SB();
  asm volatile("v_mfma_f32_16x16x32_bf16 %0, %3, %4, %0\n\tbuffer_load_dwordx4 %1, %5, %6, %7 offen offset:0 sc1\n\tv_mfma_f32_16x16x32_bf16 %2, %3, %8, %2"
      : "+v"(ac[0][0]), "=&v"(pu[4]), "+v"(ac[0][1]) : "v"(pu[5]), "a"(fw2[5][0]), "v"(vFj[1]), "s"(rsW), "s"(su[4]), "a"(fw2[5][1]) : "memory"); SB();
  WAITL(2);
  asm volatile("v_lshlrev_b32 %0, 16, %6\n\tv_mfma_f32_16x16x32_bf16 %1, %7, %8, %1\n\tv_and_b32 %2, 0xffff0000, %6\n\tv_lshlrev_b32 %3, 16, %9\n\tv_mfma_f32_16x16x32_bf16 %4, %7, %10, %4\n\tv_and_b32 %5, 0xffff0000, %9"
      : "=&v"(xe[0]), "+v"(ac[0][2]), "=&v"(xe[1]), "=&v"(xe[2]), "+v"(ac[0][3]), "=&v"(xe[3]) : "v"(xv[0][0]), "v"(pu[5]), "a"(fw2[5][2]), "v"(xv[0][1]), "a"(fw2[5][3])); SB();
  WAITVB(12);
  SB();
  asm volatile("v_mfma_f32_16x16x32_bf16 %0, %10, %11, %0\n\tbuffer_load_dwordx4 %1, %12, %13, %14 offen offset:0 sc1\n\tv_mfma_f32_16x16x32_bf16 %2, %10, %15, %2\n\tv_lshlrev_b32 %3, 16, %16\n\tv_and_b32 %4, 0xffff0000, %16\n\tv_mfma_f32_16x16x32_bf16 %5, %10, %17, %5\n\tv_lshlrev_b32 %6, 16, %18\n\tv_and_b32 %7, 0xffff0000, %18\n\tv_mfma_f32_16x16x32_bf16 %8, %10, %19, %8\n\tv_lshlrev_b32 %9, 16, %20"
      : "+v"(ac[0][0]), "=&v"(pu[5]), "+v"(ac[0][1]), "=&v"(xe[4]), "=&v"(xe[5]), "+v"(ac[0][2]), "=&v"(xe[6]), "=&v"(xe[7]), "+v"(ac[0][3]), "=&v"(xe[8]) : "v"(pu[6]), "a"(fw2[6][0]), "v"(vFj[1]), "s"(rsW), "s"(su[5]), "a"(fw2[6][1]), "v"(xv[1][0]), "a"(fw2[6][2]), "v"(xv[1][1]), "a"(fw2[6][3]), "v"(xv[2][0]) : "memory"); SB();
  WAITVB(12);
  SB();
  asm volatile("v_mfma_f32_16x16x32_bf16 %0, %9, %10, %0\n\tbuffer_load_dwordx4 %1, %11, %12, %13 offen offset:0 sc1\n\tv_mfma_f32_16x16x32_bf16 %2, %9, %14, %2\n\tv_and_b32 %3, 0xffff0000, %15\n\tv_lshlrev_b32 %4, 16, %16\n\tv_mfma_f32_16x16x32_bf16 %5, %9, %17, %5\n\tds_read_b128 %6, %18 offset:2048\n\tv_and_b32 %7, 0xffff0000, %16\n\tv_mfma_f32_16x16x32_bf16 %8, %9, %19, %8"
      : "+v"(ac[0][0]), "=&v"(pu[6]), "+v"(ac[0][1]), "=&v"(xe[9]), "=&v"(xe[10]), "+v"(ac[0][2]), "=&a"(fwo[6]), "=&v"(xe[11]), "+v"(ac[0][3]) : "v"(pu[7]), "a"(fw2[7][0]), "v"(vFj[1]), "s"(rsW), "s"(su[6]), "a"(fw2[7][1]), "v"(xv[2][0]), "v"(xv[2][1]), "a"(fw2[7][2]), "v"(ro), "a"(fw2[7][3]) : "memory"); SB();
  WAITVB(12);
  SB();
  asm volatile("v_mfma_f32_16x16x32_bf16 %0, %7, %8, %0\n\tbuffer_load_dwordx4 %1, %9, %10, %11 offen offset:0 sc1\n\tv_mfma_f32_16x16x32_bf16 %2, %7, %12, %2\n\tds_read_b128 %3, %13 offset:10240\n\tv_mfma_f32_16x16x32_bf16 %4, %7, %14, %4\n\tv_mfma_f32_16x16x32_bf16 %5, %7, %15, %5\n\tds_read_b128 %6, %13 offset:18432"
      : "+v"(ac[0][0]), "=&v"(pu[7]), "+v"(ac[0][1]), "=&a"(fwo[7]), "+v"(ac[0][2]), "+v"(ac[0][3]), "=&a"(fwo[8]) : "v"(pu[8]), "a"(fw2[8][0]), "v"(vFj[1]), "s"(rsW), "s"(su[7]), "a"(fw2[8][1]), "v"(ro), "a"(fw2[8][2]), "a"(fw2[8][3]) : "memory"); SB();
  WAITVB(12);
  SB();
  asm volatile("v_mfma_f32_16x16x32_bf16 %0, %7, %8, %0\n\tbuffer_load_dwordx4 %1, %9, %10, %11 offen offset:0 sc1\n\tv_mfma_f32_16x16x32_bf16 %2, %7, %12, %2\n\tds_read_b128 %3, %13 offset:3072\n\tv_mfma_f32_16x16x32_bf16 %4, %7, %14, %4\n\tv_mfma_f32_16x16x32_bf16 %5, %7, %15, %5\n\tds_read_b128 %6, %13 offset:11264"
      : "+v"(ac[0][0]), "=&v"(pu[8]), "+v"(ac[0][1]), "=&a"(fwo[9]), "+v"(ac[0][2]), "+v"(ac[0][3]), "=&a"(fwo[10]) : "v"(pu[9]), "v"(fw2[9][0]), "v"(vFj[1]), "s"(rsW), "s"(su[8]), "v"(fw2[9][1]), "v"(ro), "v"(fw2[9][2]), "v"(fw2[9][3]) : "memory"); SB();
  WAITVB(12);
  SB();
  asm volatile("v_mfma_f32_16x16x32_bf16 %0, %7, %8, %0\n\tbuffer_load_dwordx4 %1, %9, %10, %11 offen offset:0 sc1\n\tv_mfma_f32_16x16x32_bf16 %2, %7, %12, %2\n\tds_read_b128 %3, %13 offset:19456\n\tv_mfma_f32_16x16x32_bf16 %4, %7, %14, %4\n\tv_mfma_f32_16x16x32_bf16 %5, %7, %15, %5\n\tds_read_b128 %6, %13 offset:4096"
      : "+v"(ac[0][0]), "=&v"(pu[9]), "+v"(ac[0][1]), "=&a"(fwo[11]), "+v"(ac[0][2]), "+v"(ac[0][3]), "=&a"(fwo[12]) : "v"(pu[10]), "v"(fw2[10][0]), "v"(vFj[1]), "s"(rsW), "s"(su[9]), "v"(fw2[10][1]), "v"(ro), "v"(fw2[10][2]), "v"(fw2[10][3]) : "memory"); SB();
  WAITVB(12);
  SB();
  asm volatile("v_mfma_f32_16x16x32_bf16 %0, %7, %8, %0\n\tbuffer_load_dwordx4 %1, %9, %10, %11 offen offset:0 sc1\n\tv_mfma_f32_16x16x32_bf16 %2, %7, %12, %2\n\tds_read_b128 %3, %13 offset:12288\n\tv_mfma_f32_16x16x32_bf16 %4, %7, %14, %4\n\tv_mfma_f32_16x16x32_bf16 %5, %7, %15, %5\n\tds_read_b128 %6, %13 offset:20480"
      : "+v"(ac[0][0]), "=&v"(pu[10]), "+v"(ac[0][1]), "=&a"(fwo[13]), "+v"(ac[0][2]), "+v"(ac[0][3]), "=&a"(fwo[14]) : "v"(pu[11]), "v"(fw2[11][0]), "v"(vFj[1]), "s"(rsW), "s"(su[10]), "v"(fw2[11][1]), "v"(ro), "v"(fw2[11][2]), "v"(fw2[11][3]) : "memory"); SB();
  WAITVB(12);
  SB();
  asm volatile("v_mfma_f32_16x16x32_bf16 %0, %5, %6, %0\n\tbuffer_load_dwordx4 %1, %7, %8, %9 offen offset:0 sc1\n\tv_mfma_f32_16x16x32_bf16 %2, %5, %10, %2\n\tds_read_b128 %3, %11 offset:5120\n\tv_mfma_f32_16x16x32_bf16 %4, %5, %12, %4"
      : "+v"(ac[0][0]), "=&v"(pu[11]), "+v"(ac[0][1]), "=&a"(fwo[15]), "+v"(ac[0][2]) : "v"(pu[12]), "v"(fw2[12][0]), "v"(vFj[1]), "s"(rsW), "s"(su[11]), "v"(fw2[12][1]), "v"(ro), "v"(fw2[12][2]) : "memory"); SB();
  u32x4 rsO_ = srd(out, 2048u * 896u); OPQ(rsO_);
  SB();
  asm volatile("v_mfma_f32_16x16x32_bf16 %0, %2, %3, %0\n\tds_read_b128 %1, %4 offset:13312"
      : "+v"(ac[0][3]), "=&a"(fwo[16]) : "v"(pu[12]), "v"(fw2[12][3]), "v"(ro) : "memory"); SB();
  WAITVB(12);
  SB();
  asm volatile("v_mfma_f32_16x16x32_bf16 %0, %7, %8, %0\n\tbuffer_load_dwordx4 %1, %9, %10, %11 offen offset:0 sc1\n\tv_mfma_f32_16x16x32_bf16 %2, %7, %12, %2\n\tds_read_b128 %3, %13 offset:21504\n\tv_mfma_f32_16x16x32_bf16 %4, %7, %14, %4\n\tv_mfma_f32_16x16x32_bf16 %5, %7, %15, %5\n\tds_read_b128 %6, %13 offset:6144"
      : "+v"(ac[0][0]), "=&v"(pu[12]), "+v"(ac[0][1]), "=&a"(fwo[17]), "+v"(ac[0][2]), "+v"(ac[0][3]), "=&a"(fwo[18]) : "v"(pu[13]), "v"(fw2[13][0]), "v"(vFj[1]), "s"(rsW), "s"(su[12]), "v"(fw2[13][1]), "v"(ro), "v"(fw2[13][2]), "v"(fw2[13][3]) : "memory"); SB();
  STAMP(40);
  WAITVB(12);
  SB();
  asm volatile("v_mfma_f32_16x16x32_bf16 %0, %7, %8, 0\n\tbuffer_load_dwordx4 %1, %9, %10, %11 offen offset:0 sc1\n\tv_mfma_f32_16x16x32_bf16 %2, %7, %12, 0\n\tbuffer_load_dword %3, %13, %10, 0 offen sc1\n\tv_mfma_f32_16x16x32_bf16 %4, %7, %14, 0\n\tds_read_b128 %5, %15 offset:14336\n\tv_mfma_f32_16x16x32_bf16 %6, %7, %16, 0"
      : "=&v"(ac[1][0]), "=&v"(pu[13]), "=&v"(ac[1][1]), "=&v"(fo), "=&v"(ac[1][2]), "=&a"(fwo[19]), "=&v"(ac[1][3]) : "v"(pu[0]), "a"(fw2[0][0]), "v"(vFj[1]), "s"(rsW), "s"(su[13]), "a"(fw2[0][1]), "v"(vFO), "a"(fw2[0][2]), "v"(ro), "a"(fw2[0][3]) : "memory"); SB();
  WAITVB(13);
  SB();
  asm volatile("v_mfma_f32_16x16x32_bf16 %0, %5, %6, %0\n\tbuffer_load_dwordx4 %1, %7, %8, %9 offen offset:0 sc1\n\tv_mfma_f32_16x16x32_bf16 %2, %5, %10, %2\n\tds_write_b128 %11, %12 offset:0\n\tv_mfma_f32_16x16x32_bf16 %3, %5, %13, %3\n\tv_mfma_f32_16x16x32_bf16 %4, %5, %14, %4"
      : "+v"(ac[1][0]), "=&v"(pu[0]), "+v"(ac[1][1]), "+v"(ac[1][2]), "+v"(ac[1][3]) : "v"(pu[1]), "a"(fw2[1][0]), "v"(vFj[2]), "s"(rsW), "s"(su[0]), "a"(fw2[1][1]), "v"(rwd), "v"(ac[0][0]), "a"(fw2[1][2]), "a"(fw2[1][3]) : "memory"); SB();
  WAITVB(13);
  SB();
  asm volatile("v_mfma_f32_16x16x32_bf16 %0, %5, %6, %0\n\tbuffer_load_dwordx4 %1, %7, %8, %9 offen offset:0 sc1\n\tv_mfma_f32_16x16x32_bf16 %2, %5, %10, %2\n\tds_write_b128 %11, %12 offset:1024\n\tv_mfma_f32_16x16x32_bf16 %3, %5, %13, %3\n\tv_mfma_f32_16x16x32_bf16 %4, %5, %14, %4"
      : "+v"(ac[1][0]), "=&v"(pu[1]), "+v"(ac[1][1]), "+v"(ac[1][2]), "+v"(ac[1][3]) : "v"(pu[2]), "a"(fw2[2][0]), "v"(vFj[2]), "s"(rsW), "s"(su[1]), "a"(fw2[2][1]), "v"(rwd), "v"(ac[0][1]), "a"(fw2[2][2]), "a"(fw2[2][3]) : "memory"); SB();
  WAITVB(13);
  SB();
  asm volatile("v_mfma_f32_16x16x32_bf16 %0, %5, %6, %0\n\tbuffer_load_dwordx4 %1, %7, %8, %9 offen offset:0 sc1\n\tv_mfma_f32_16x16x32_bf16 %2, %5, %10, %2\n\tds_write_b128 %11, %12 offset:2048\n\tv_mfma_f32_16x16x32_bf16 %3, %5, %13, %3\n\tv_mfma_f32_16x16x32_bf16 %4, %5, %14, %4"
      : "+v"(ac[1][0]), "=&v"(pu[2]), "+v"(ac[1][1]), "+v"(ac[1][2]), "+v"(ac[1][3]) : "v"(pu[3]), "a"(fw2[3][0]), "v"(vFj[2]), "s"(rsW), "s"(su[2]), "a"(fw2[3][1]), "v"(rwd), "v"(ac[0][2]), "a"(fw2[3][2]), "a"(fw2[3][3]) : "memory"); SB();
  WAITVB(13);
  SB();
  asm volatile("v_mfma_f32_16x16x32_bf16 %0, %5, %6, %0\n\tbuffer_load_dwordx4 %1, %7, %8, %9 offen offset:0 sc1\n\tv_mfma_f32_16x16x32_bf16 %2, %5, %10, %2\n\tds_write_b128 %11, %12 offset:3072\n\tv_mfma_f32_16x16x32_bf16 %3, %5, %13, %3\n\tv_mfma_f32_16x16x32_bf16 %4, %5, %14, %4"
      : "+v"(ac[1][0]), "=&v"(pu[3]), "+v"(ac[1][1]), "+v"(ac[1][2]), "+v"(ac[1][3]) : "v"(pu[4]), "a"(fw2[4][0]), "v"(vFj[2]), "s"(rsW), "s"(su[3]), "a"(fw2[4][1]), "v"(rwd), "v"(ac[0][3]), "a"(fw2[4][2]), "a"(fw2[4][3]) : "memory"); SB();
  WAITVB(13);
  SB();
  asm volatile("v_mfma_f32_16x16x32_bf16 %0, %3, %4, %0\n\tbuffer_load_dwordx4 %1, %5, %6, %7 offen offset:0 sc1\n\tv_mfma_f32_16x16x32_bf16 %2, %3, %8, %2"
      : "+v"(ac[1][0]), "=&v"(pu[4]), "+v"(ac[1][1]) : "v"(pu[5]), "a"(fw2[5][0]), "v"(vFj[2]), "s"(rsW), "s"(su[4]), "a"(fw2[5][1]) : "memory"); SB();
  WAITL(0);
  BAR0();
  SB();
  asm volatile("v_mfma_f32_16x16x32_bf16 %0, %5, %6, %0\n\tds_read_b128 %1, %7 offset:0\n\tds_read_b128 %2, %7 offset:4096\n\tv_mfma_f32_16x16x32_bf16 %3, %5, %8, %3\n\tds_read_b128 %4, %7 offset:8192"
      : "+v"(ac[1][2]), "=&v"(py[0][0]), "=&v"(py[0][1]), "+v"(ac[1][3]), "=&v"(py[0][2]) : "v"(pu[5]), "a"(fw2[5][2]), "v"(rr), "a"(fw2[5][3]) : "memory"); SB();
  WAITVB(13);
  SB();
  asm volatile("v_mfma_f32_16x16x32_bf16 %0, %9, %10, %0\n\tbuffer_load_dwordx4 %1, %11, %12, %13 offen offset:0 sc1\n\tv_mfma_f32_16x16x32_bf16 %2, %9, %14, %2\n\tbuffer_load_dword %3, %15, %12, 0 offen sc1\n\tbuffer_load_dword %4, %16, %12, 0 offen sc1\n\tbuffer_load_dword %5, %17, %12, 0 offen sc1\n\tv_mfma_f32_16x16x32_bf16 %6, %9, %18, %6\n\tds_read_b128 %7, %19 offset:12288\n\tv_mfma_f32_16x16x32_bf16 %8, %9, %20, %8"
      : "+v"(ac[1][0]), "=&v"(pu[5]), "+v"(ac[1][1]), "=&v"(fz[0]), "=&v"(fz[1]), "=&v"(fz[2]), "+v"(ac[1][2]), "=&v"(py[0][3]), "+v"(ac[1][3]) : "v"(pu[6]), "a"(fw2[6][0]), "v"(vFj[2]), "s"(rsW), "s"(su[5]), "a"(fw2[6][1]), "v"(vZ0), "v"(vZ1), "v"(vZ2), "a"(fw2[6][2]), "v"(rr), "a"(fw2[6][3]) : "memory"); SB();
  WAITVB(16);
  SB();
  asm volatile("v_mfma_f32_16x16x32_bf16 %0, %5, %6, %0\n\tbuffer_load_dwordx4 %1, %7, %8, %9 offen offset:0 sc1\n\tv_mfma_f32_16x16x32_bf16 %2, %5, %10, %2\n\tv_mfma_f32_16x16x32_bf16 %3, %5, %11, %3\n\tv_mfma_f32_16x16x32_bf16 %4, %5, %12, %4"
      : "+v"(ac[1][0]), "=&v"(pu[6]), "+v"(ac[1][1]), "+v"(ac[1][2]), "+v"(ac[1][3]) : "v"(pu[7]), "a"(fw2[7][0]), "v"(vFj[2]), "s"(rsW), "s"(su[6]), "a"(fw2[7][1]), "a"(fw2[7][2]), "a"(fw2[7][3]) : "memory"); SB();
  WAITL(0);
  SB();
  WAITVB(16);
  SB();
  asm volatile("v_mfma_f32_16x16x32_bf16 %0, %10, %11, %0\n\tbuffer_load_dwordx4 %1, %12, %13, %14 offen offset:0 sc1\n\tv_mfma_f32_16x16x32_bf16 %2, %10, %15, %2\n\tv_add_f32 %3, %16, %17\n\tv_add_f32 %4, %18, %17\n\tv_mfma_f32_16x16x32_bf16 %5, %10, %19, %5\n\tv_add_f32 %6, %20, %17\n\tv_add_f32 %7, %21, %17\n\tv_mfma_f32_16x16x32_bf16 %8, %10, %22, %8\n\tv_add_f32 %9, %23, %24"
      : "+v"(ac[1][0]), "=&v"(pu[7]), "+v"(ac[1][1]), "=&v"(ye[0]), "=&v"(ye[1]), "+v"(ac[1][2]), "=&v"(ye[2]), "=&v"(ye[3]), "+v"(ac[1][3]), "=&v"(ze[0]) : "v"(pu[8]), "a"(fw2[8][0]), "v"(vFj[2]), "s"(rsW), "s"(su[7]), "a"(fw2[8][1]), "v"(py[0][0][0]), "v"(bias), "v"(py[0][0][1]), "a"(fw2[8][2]), "v"(py[0][0][2]), "v"(py[0][0][3]), "a"(fw2[8][3]), "v"(py[0][1][0]), "v"(py[0][2][0]) : "memory"); SB();
  WAITVB(16);
  SB();
  asm volatile("v_mfma_f32_16x16x32_bf16 %0, %9, %10, %0\n\tbuffer_load_dwordx4 %1, %11, %12, %13 offen offset:0 sc1\n\tv_mfma_f32_16x16x32_bf16 %2, %9, %14, %2\n\tv_add_f32 %3, %15, %16\n\tv_add_f32 %4, %17, %18\n\tv_mfma_f32_16x16x32_bf16 %5, %9, %19, %5\n\tv_add_f32 %6, %20, %21\n\tv_add_f32 %7, %7, %22\n\tv_mfma_f32_16x16x32_bf16 %8, %9, %23, %8"
      : "+v"(ac[1][0]), "=&v"(pu[8]), "+v"(ac[1][1]), "=&v"(ze[1]), "=&v"(ze[2]), "+v"(ac[1][2]), "=&v"(ze[3]), "+v"(ye[0]), "+v"(ac[1][3]) : "v"(pu[9]), "v"(fw2[9][0]), "v"(vFj[2]), "s"(rsW), "s"(su[8]), "v"(fw2[9][1]), "v"(py[0][1][1]), "v"(py[0][2][1]), "v"(py[0][1][2]), "v"(py[0][2][2]), "v"(fw2[9][2]), "v"(py[0][1][3]), "v"(py[0][2][3]), "v"(py[0][3][0]), "v"(fw2[9][3]) : "memory"); SB();
  asm volatile("v_add_f32 %0, %0, %1"
      : "+v"(ye[1]) : "v"(py[0][3][1])); SB();
  WAITVB(16);
  SB();
  asm volatile("v_mfma_f32_16x16x32_bf16 %0, %9, %10, %0\n\tbuffer_load_dwordx4 %1, %11, %12, %13 offen offset:0 sc1\n\tv_mfma_f32_16x16x32_bf16 %2, %9, %14, %2\n\tv_add_f32 %3, %3, %15\n\tv_add_f32 %4, %4, %16\n\tv_mfma_f32_16x16x32_bf16 %5, %9, %17, %5\n\tv_add_f32 %6, %6, %18\n\tv_add_f32 %7, %7, %19\n\tv_mfma_f32_16x16x32_bf16 %8, %9, %20, %8\n\tv_add_f32 %3, %3, %21"
      : "+v"(ac[1][0]), "=&v"(pu[9]), "+v"(ac[1][1]), "+v"(ye[2]), "+v"(ye[3]), "+v"(ac[1][2]), "+v"(ye[0]), "+v"(ye[1]), "+v"(ac[1][3]) : "v"(pu[10]), "v"(fw2[10][0]), "v"(vFj[2]), "s"(rsW), "s"(su[9]), "v"(fw2[10][1]), "v"(py[0][3][2]), "v"(py[0][3][3]), "v"(fw2[10][2]), "v"(ze[0]), "v"(ze[1]), "v"(fw2[10][3]), "v"(ze[2]) : "memory"); SB();
  WAITVB(16);
  SB();
  asm volatile("v_mfma_f32_16x16x32_bf16 %0, %10, %11, %0\n\tbuffer_load_dwordx4 %1, %12, %13, %14 offen offset:0 sc1\n\tv_mfma_f32_16x16x32_bf16 %2, %10, %15, %2\n\tv_add_f32 %3, %3, %4\n\tv_mul_f32 %5, 0xbfb8aa3b, %16\n\tv_mfma_f32_16x16x32_bf16 %6, %10, %17, %6\n\tv_mul_f32 %7, 0xbfb8aa3b, %18\n\tv_mul_f32 %8, 0xbfb8aa3b, %19\n\tv_mfma_f32_16x16x32_bf16 %9, %10, %20, %9\n\tv_mul_f32 %4, 0xbfb8aa3b, %3"
      : "+v"(ac[1][0]), "=&v"(pu[10]), "+v"(ac[1][1]), "+v"(ye[3]), "+v"(ze[3]), "=&v"(ze[0]), "+v"(ac[1][2]), "=&v"(ze[1]), "=&v"(ze[2]), "+v"(ac[1][3]) : "v"(pu[11]), "v"(fw2[11][0]), "v"(vFj[2]), "s"(rsW), "s"(su[10]), "v"(fw2[11][1]), "v"(ye[0]), "v"(fw2[11][2]), "v"(ye[1]), "v"(ye[2]), "v"(fw2[11][3]) : "memory"); SB();
  WAITVB(16);
  SB();
  asm volatile("v_mfma_f32_16x16x32_bf16 %0, %7, %8, %0\n\tbuffer_load_dwordx4 %1, %9, %10, %11 offen offset:0 sc1\n\tv_mfma_f32_16x16x32_bf16 %2, %7, %12, %2\n\tv_exp_f32 %3, %3\n\tv_mfma_f32_16x16x32_bf16 %4, %7, %13, %4\n\tv_exp_f32 %5, %5\n\tv_mfma_f32_16x16x32_bf16 %6, %7, %14, %6"
      : "+v"(ac[1][0]), "=&v"(pu[11]), "+v"(ac[1][1]), "+v"(ze[0]), "+v"(ac[1][2]), "+v"(ze[1]), "+v"(ac[1][3]) : "v"(pu[12]), "v"(fw2[12][0]), "v"(vFj[2]), "s"(rsW), "s"(su[11]), "v"(fw2[12][1]), "v"(fw2[12][2]), "v"(fw2[12][3]) : "memory"); SB();
  WAITVB(16);
  SB();
  asm volatile("v_mfma_f32_16x16x32_bf16 %0, %9, %10, %0\n\tbuffer_load_dwordx4 %1, %11, %12, %13 offen offset:0 sc1\n\tv_mfma_f32_16x16x32_bf16 %2, %9, %14, %2\n\tv_exp_f32 %3, %3\n\tv_mfma_f32_16x16x32_bf16 %4, %9, %15, %4\n\tv_exp_f32 %5, %5\n\tv_mfma_f32_16x16x32_bf16 %6, %9, %16, %6\n\tv_add_f32 %7, 1.0, %7\n\tv_add_f32 %8, 1.0, %8"
      : "+v"(ac[1][0]), "=&v"(pu[12]), "+v"(ac[1][1]), "+v"(ze[2]), "+v"(ac[1][2]), "+v"(ze[3]), "+v"(ac[1][3]), "+v"(ze[0]), "+v"(ze[1]) : "v"(pu[13]), "v"(fw2[13][0]), "v"(vFj[2]), "s"(rsW), "s"(su[12]), "v"(fw2[13][1]), "v"(fw2[13][2]), "v"(fw2[13][3]) : "memory"); SB();
  STAMP(41);
  WAITVB(15);
  SB();
  asm volatile("v_mfma_f32_16x16x32_bf16 %0, %8, %9, 0\n\tbuffer_load_dwordx4 %1, %10, %11, %12 offen offset:0 sc1\n\tv_mfma_f32_16x16x32_bf16 %2, %8, %13, 0\n\tv_add_f32 %3, 1.0, %3\n\tv_add_f32 %4, 1.0, %4\n\tv_mfma_f32_16x16x32_bf16 %5, %8, %14, 0\n\tv_rcp_f32 %6, %6\n\tv_mfma_f32_16x16x32_bf16 %7, %8, %15, 0"
      : "=&v"(ac[0][0]), "=&v"(pu[13]), "=&v"(ac[0][1]), "+v"(ze[2]), "+v"(ze[3]), "=&v"(ac[0][2]), "+v"(ze[0]), "=&v"(ac[0][3]) : "v"(pu[0]), "a"(fw2[0][0]), "v"(vFj[2]), "s"(rsW), "s"(su[13]), "a"(fw2[0][1]), "a"(fw2[0][2]), "a"(fw2[0][3]) : "memory"); SB();
  WAITVB(15);
  SB();
  asm volatile("v_mfma_f32_16x16x32_bf16 %0, %1, %2, %0"
      : "+v"(ac[0][0]) : "v"(pu[1]), "a"(fw2[1][0])); SB();
  WAITV1(17, fo);
  if (__builtin_expect(__ballot(fo != nonce) != 0ull, 0)) poll1<4>(rsW, vFO, nonce);
  STAMP(18);
  asm volatile("buffer_load_dwordx4 %0, %6, %7, %8 offen offset:0 sc1\n\tv_mfma_f32_16x16x32_bf16 %1, %9, %10, %1\n\tds_write_b128 %11, %12 offset:18432\n\tv_mfma_f32_16x16x32_bf16 %2, %9, %13, %2\n\tv_rcp_f32 %3, %3\n\tv_mfma_f32_16x16x32_bf16 %4, %9, %14, %4\n\tds_read_b128 %5, %15 offset:22528"
      : "=&v"(pu[0]), "+v"(ac[0][1]), "+v"(ac[0][2]), "+v"(ze[1]), "+v"(ac[0][3]), "=&a"(fwo[20]) : "v"(vF), "s"(rsW), "s"(so0), "v"(pu[1]), "a"(fw2[1][1]), "v"(rwd), "v"(ac[1][0]), "a"(fw2[1][2]), "a"(fw2[1][3]), "v"(ro) : "memory"); SB();
  WAITVB(15);
  SB();
  asm volatile("v_mfma_f32_16x16x32_bf16 %0, %7, %8, %0\n\tbuffer_load_dwordx4 %1, %9, %10, %11 offen offset:128 sc1\n\tv_mfma_f32_16x16x32_bf16 %2, %7, %12, %2\n\tds_write_b128 %13, %14 offset:19456\n\tv_mfma_f32_16x16x32_bf16 %3, %7, %15, %3\n\tv_rcp_f32 %4, %4\n\tv_mfma_f32_16x16x32_bf16 %5, %7, %16, %5\n\tds_read_b128 %6, %17 offset:7168"
      : "+v"(ac[0][0]), "=&v"(pu[1]), "+v"(ac[0][1]), "+v"(ac[0][2]), "+v"(ze[2]), "+v"(ac[0][3]), "=&a"(fwo[21]) : "v"(pu[2]), "a"(fw2[2][0]), "v"(vF), "s"(rsW), "s"(so0), "a"(fw2[2][1]), "v"(rwd), "v"(ac[1][1]), "a"(fw2[2][2]), "a"(fw2[2][3]), "v"(ro) : "memory"); SB();
  WAITVB(15);
  SB();
  asm volatile("v_mfma_f32_16x16x32_bf16 %0, %7, %8, %0\n\tbuffer_load_dwordx4 %1, %9, %10, %11 offen offset:256 sc1\n\tv_mfma_f32_16x16x32_bf16 %2, %7, %12, %2\n\tds_write_b128 %13, %14 offset:20480\n\tv_mfma_f32_16x16x32_bf16 %3, %7, %15, %3\n\tv_rcp_f32 %4, %4\n\tv_mfma_f32_16x16x32_bf16 %5, %7, %16, %5\n\tv_mul_f32 %6, %17, %18"
      : "+v"(ac[0][0]), "=&v"(pu[2]), "+v"(ac[0][1]), "+v"(ac[0][2]), "+v"(ze[3]), "+v"(ac[0][3]), "=&v"(ge[0]) : "v"(pu[3]), "a"(fw2[3][0]), "v"(vF), "s"(rsW), "s"(so0), "a"(fw2[3][1]), "v"(rwd), "v"(ac[1][2]), "a"(fw2[3][2]), "a"(fw2[3][3]), "v"(ye[0]), "v"(ze[0]) : "memory"); SB();
  WAITVB(15);
  SB();
  asm volatile("v_mfma_f32_16x16x32_bf16 %0, %8, %9, %0\n\tbuffer_load_dwordx4 %1, %10, %11, %12 offen offset:384 sc1\n\tv_mfma_f32_16x16x32_bf16 %2, %8, %13, %2\n\tds_write_b128 %14, %15 offset:21504\n\tv_mfma_f32_16x16x32_bf16 %3, %8, %16, %3\n\tv_mul_f32 %4, %17, %18\n\tv_mul_f32 %5, %19, %20\n\tv_mfma_f32_16x16x32_bf16 %6, %8, %21, %6\n\tv_mul_f32 %7, %22, %23"
      : "+v"(ac[0][0]), "=&v"(pu[3]), "+v"(ac[0][1]), "+v"(ac[0][2]), "=&v"(ge[1]), "=&v"(ge[2]), "+v"(ac[0][3]), "=&v"(ge[3]) : "v"(pu[4]), "a"(fw2[4][0]), "v"(vF), "s"(rsW), "s"(so0), "a"(fw2[4][1]), "v"(rwd), "v"(ac[1][3]), "a"(fw2[4][2]), "v"(ye[1]), "v"(ze[1]), "v"(ye[2]), "v"(ze[2]), "a"(fw2[4][3]), "v"(ye[3]), "v"(ze[3]) : "memory"); SB();
  WAITVB(15);
  SB();
  asm volatile("v_mfma_f32_16x16x32_bf16 %0, %3, %4, %0\n\tbuffer_load_dwordx4 %1, %5, %6, %7 offen offset:512 sc1\n\tv_mfma_f32_16x16x32_bf16 %2, %3, %8, %2"
      : "+v"(ac[0][0]), "=&v"(pu[4]), "+v"(ac[0][1]) : "v"(pu[5]), "a"(fw2[5][0]), "v"(vF), "s"(rsW), "s"(so0), "a"(fw2[5][1]) : "memory"); SB();
  WAITL(0);
  BAR0();
  SB();
  asm volatile("v_mfma_f32_16x16x32_bf16 %0, %5, %6, %0\n\tds_read_b128 %1, %7 offset:18432\n\tds_read_b128 %2, %7 offset:22528\n\tv_mfma_f32_16x16x32_bf16 %3, %5, %8, %3\n\tds_read_b128 %4, %7 offset:26624"
      : "+v"(ac[0][2]), "=&v"(py[1][0]), "=&v"(py[1][1]), "+v"(ac[0][3]), "=&v"(py[1][2]) : "v"(pu[5]), "a"(fw2[5][2]), "v"(rr), "a"(fw2[5][3]) : "memory"); SB();
  WAITVB(12);
  SB();
  asm volatile("v_mfma_f32_16x16x32_bf16 %0, %8, %9, %0\n\tbuffer_load_dwordx4 %1, %10, %11, %12 offen offset:640 sc1\n\tv_mfma_f32_16x16x32_bf16 %2, %8, %13, %2\n\tds_read_b128 %3, %14 offset:30720\n\tds_read_b128 %4, %15 offset:15360\n\tv_mfma_f32_16x16x32_bf16 %5, %8, %16, %5\n\tv_mfma_f32_16x16x32_bf16 %6, %8, %17, %6\n\tds_read_b128 %7, %15 offset:23552"
      : "+v"(ac[0][0]), "=&v"(pu[5]), "+v"(ac[0][1]), "=&v"(py[1][3]), "=&a"(fwo[22]), "+v"(ac[0][2]), "+v"(ac[0][3]), "=&a"(fwo[23]) : "v"(pu[6]), "a"(fw2[6][0]), "v"(vF), "s"(rsW), "s"(so0), "a"(fw2[6][1]), "v"(rr), "v"(ro), "a"(fw2[6][2]), "a"(fw2[6][3]) : "memory"); SB();
  WAITVB(12);
  SB();
  asm volatile("v_mfma_f32_16x16x32_bf16 %0, %5, %6, %0\n\tbuffer_load_dwordx4 %1, %7, %8, %9 offen offset:768 sc1\n\tv_mfma_f32_16x16x32_bf16 %2, %5, %10, %2\n\tds_read_b128 %3, %11 offset:32768\n\tv_mfma_f32_16x16x32_bf16 %4, %5, %12, %4"
      : "+v"(ac[0][0]), "=&v"(pu[6]), "+v"(ac[0][1]), "=&a"(fwo[24]), "+v"(ac[0][2]) : "v"(pu[7]), "a"(fw2[7][0]), "v"(vF), "s"(rsW), "s"(so0), "a"(fw2[7][1]), "v"(ro), "a"(fw2[7][2]) : "memory"); SB();
  WAITL(3);
  asm volatile("v_add_f32 %0, %3, %4\n\tv_mfma_f32_16x16x32_bf16 %1, %5, %6, %1\n\tv_add_f32 %2, %7, %4"
      : "=&v"(ye[0]), "+v"(ac[0][3]), "=&v"(ye[1]) : "v"(py[1][0][0]), "v"(bias), "v"(pu[7]), "a"(fw2[7][3]), "v"(py[1][0][1])); SB();
  WAITVB(12);
  SB();
  asm volatile("v_mfma_f32_16x16x32_bf16 %0, %9, %10, %0\n\tbuffer_load_dwordx4 %1, %11, %12, %13 offen offset:0 sc1\n\tv_mfma_f32_16x16x32_bf16 %2, %9, %14, %2\n\tv_add_f32 %3, %15, %16\n\tv_add_f32 %4, %17, %16\n\tv_mfma_f32_16x16x32_bf16 %5, %9, %18, %5\n\tv_add_f32 %6, %19, %20\n\tv_add_f32 %7, %21, %22\n\tv_mfma_f32_16x16x32_bf16 %8, %9, %23, %8"
      : "+v"(ac[0][0]), "=&v"(pu[7]), "+v"(ac[0][1]), "=&v"(ye[2]), "=&v"(ye[3]), "+v"(ac[0][2]), "=&v"(ze[0]), "=&v"(ze[1]), "+v"(ac[0][3]) : "v"(pu[8]), "a"(fw2[8][0]), "v"(vF), "s"(rsW), "s"(so1), "a"(fw2[8][1]), "v"(py[1][0][2]), "v"(bias), "v"(py[1][0][3]), "a"(fw2[8][2]), "v"(py[1][1][0]), "v"(py[1][2][0]), "v"(py[1][1][1]), "v"(py[1][2][1]), "a"(fw2[8][3]) : "memory"); SB();
  asm volatile("v_add_f32 %0, %1, %2"
      : "=&v"(ze[2]) : "v"(py[1][1][2]), "v"(py[1][2][2])); SB();
  WAITVB(12);
  SB();
  asm volatile("v_mfma_f32_16x16x32_bf16 %0, %9, %10, %0\n\tbuffer_load_dwordx4 %1, %11, %12, %13 offen offset:128 sc1\n\tv_mfma_f32_16x16x32_bf16 %2, %9, %14, %2\n\tv_add_f32 %3, %15, %16\n\tv_add_f32 %4, %4, %17\n\tv_mfma_f32_16x16x32_bf16 %5, %9, %18, %5\n\tv_add_f32 %6, %6, %19\n\tv_add_f32 %7, %7, %20\n\tv_mfma_f32_16x16x32_bf16 %8, %9, %21, %8"
      : "+v"(ac[0][0]), "=&v"(pu[8]), "+v"(ac[0][1]), "=&v"(ze[3]), "+v"(ye[0]), "+v"(ac[0][2]), "+v"(ye[1]), "+v"(ye[2]), "+v"(ac[0][3]) : "v"(pu[9]), "v"(fw2[9][0]), "v"(vF), "s"(rsW), "s"(so1), "v"(fw2[9][1]), "v"(py[1][1][3]), "v"(py[1][2][3]), "v"(py[1][3][0]), "v"(fw2[9][2]), "v"(py[1][3][1]), "v"(py[1][3][2]), "v"(fw2[9][3]) : "memory"); SB();
  asm volatile("v_add_f32 %0, %0, %1"
      : "+v"(ye[3]) : "v"(py[1][3][3])); SB();
  WAITVB(12);
  SB();
  asm volatile("v_mfma_f32_16x16x32_bf16 %0, %10, %11, %0\n\tbuffer_load_dwordx4 %1, %12, %13, %14 offen offset:256 sc1\n\tv_mfma_f32_16x16x32_bf16 %2, %10, %15, %2\n\tv_add_f32 %3, %3, %4\n\tv_add_f32 %5, %5, %16\n\tv_mfma_f32_16x16x32_bf16 %6, %10, %17, %6\n\tv_add_f32 %7, %7, %18\n\tv_add_f32 %8, %8, %19\n\tv_mfma_f32_16x16x32_bf16 %9, %10, %20, %9\n\tv_mul_f32 %4, 0xbfb8aa3b, %3"
      : "+v"(ac[0][0]), "=&v"(pu[9]), "+v"(ac[0][1]), "+v"(ye[0]), "+v"(ze[0]), "+v"(ye[1]), "+v"(ac[0][2]), "+v"(ye[2]), "+v"(ye[3]), "+v"(ac[0][3]) : "v"(pu[10]), "v"(fw2[10][0]), "v"(vF), "s"(rsW), "s"(so1), "v"(fw2[10][1]), "v"(ze[1]), "v"(fw2[10][2]), "v"(ze[2]), "v"(ze[3]), "v"(fw2[10][3]) : "memory"); SB();
  WAITVB(12);
  SB();
  asm volatile("v_mfma_f32_16x16x32_bf16 %0, %9, %10, %0\n\tbuffer_load_dwordx4 %1, %11, %12, %13 offen offset:384 sc1\n\tv_mfma_f32_16x16x32_bf16 %2, %9, %14, %2\n\tv_mul_f32 %3, 0xbfb8aa3b, %15\n\tv_mul_f32 %4, 0xbfb8aa3b, %16\n\tv_mfma_f32_16x16x32_bf16 %5, %9, %17, %5\n\tds_read_b128 %6, %18 offset:38912\n\tv_mul_f32 %7, 0xbfb8aa3b, %19\n\tv_mfma_f32_16x16x32_bf16 %8, %9, %20, %8"
      : "+v"(ac[0][0]), "=&v"(pu[10]), "+v"(ac[0][1]), "=&v"(ze[1]), "=&v"(ze[2]), "+v"(ac[0][2]), "=&a"(fwo[25]), "=&v"(ze[3]), "+v"(ac[0][3]) : "v"(pu[11]), "v"(fw2[11][0]), "v"(vF), "s"(rsW), "s"(so1), "v"(fw2[11][1]), "v"(ye[1]), "v"(ye[2]), "v"(fw2[11][2]), "v"(ro), "v"(ye[3]), "v"(fw2[11][3]) : "memory"); SB();
  WAITVB(12);
  SB();
  asm volatile("v_mfma_f32_16x16x32_bf16 %0, %3, %4, %0\n\tbuffer_load_dwordx4 %1, %5, %6, %7 offen offset:512 sc1\n\tv_mfma_f32_16x16x32_bf16 %2, %3, %8, %2"
      : "+v"(ac[0][0]), "=&v"(pu[11]), "+v"(ac[0][1]) : "v"(pu[12]), "v"(fw2[12][0]), "v"(vF), "s"(rsW), "s"(so1), "v"(fw2[12][1]) : "memory"); SB();
  WAITV3(20, fz[0], fz[1], fz[2]);
  if (__builtin_expect(__ballot(((fz[0] ^ nonce) | (fz[1] ^ nonce) | (fz[2] ^ nonce)) != 0u) != 0ull, 0)) { poll0<16>(rsW, vZ0, nonce); poll0<16>(rsW, vZ1, nonce); poll0<16>(rsW, vZ2, nonce); }
  SB();
  asm volatile("v_mfma_f32_16x16x32_bf16 %0, %4, %5, %0\n\tv_exp_f32 %1, %1\n\tv_mfma_f32_16x16x32_bf16 %2, %4, %6, %2\n\tds_read_b128 %3, %7 offset:45056"
      : "+v"(ac[0][2]), "+v"(ze[0]), "+v"(ac[0][3]), "=&a"(fwo[26]) : "v"(pu[12]), "v"(fw2[12][2]), "v"(fw2[12][3]), "v"(ro) : "memory"); SB();
  WAITVB(12);
  SB();
  asm volatile("v_mfma_f32_16x16x32_bf16 %0, %8, %9, %0\n\tbuffer_load_dwordx4 %1, %10, %11, %12 offen offset:640 sc1\n\tv_mfma_f32_16x16x32_bf16 %2, %8, %13, %2\n\tv_exp_f32 %3, %3\n\tv_mfma_f32_16x16x32_bf16 %4, %8, %14, %4\n\tv_exp_f32 %5, %5\n\tv_mfma_f32_16x16x32_bf16 %6, %8, %15, %6\n\tv_exp_f32 %7, %7"
      : "+v"(ac[0][0]), "=&v"(pu[12]), "+v"(ac[0][1]), "+v"(ze[1]), "+v"(ac[0][2]), "+v"(ze[2]), "+v"(ac[0][3]), "+v"(ze[3]) : "v"(pu[13]), "v"(fw2[13][0]), "v"(vF), "s"(rsW), "s"(so1), "v"(fw2[13][1]), "v"(fw2[13][2]), "v"(fw2[13][3]) : "memory"); SB();
  STAMP(42);
  WAITVB(12);
  SB();
  asm volatile("v_mfma_f32_16x16x32_bf16 %0, %7, %8, 0\n\tbuffer_load_dwordx4 %1, %9, %10, %11 offen offset:768 sc1\n\tv_mfma_f32_16x16x32_bf16 %2, %12, %8, 0\n\tv_add_f32 %3, 1.0, %3\n\tv_add_f32 %4, 1.0, %4\n\tv_mfma_f32_16x16x32_bf16 %5, %13, %8, 0\n\tv_add_f32 %6, 1.0, %6"
      : "=&v"(as[0]), "=&v"(pu[13]), "=&v"(as[1]), "+v"(ze[0]), "+v"(ze[1]), "=&v"(as[2]), "+v"(ze[2]) : "a"(fwo[0]), "v"(pu[0]), "v"(vF), "s"(rsW), "s"(so1), "a"(fwo[1]), "a"(fwo[2]) : "memory"); SB();
  WAITVB(12);
  SB();
  asm volatile("v_mfma_f32_16x16x32_bf16 %0, %5, %6, %0\n\tds_write_b128 %7, %8 offset:0\n\tv_mfma_f32_16x16x32_bf16 %1, %9, %6, %1\n\tds_read_b128 %2, %10 offset:33792\n\tv_add_f32 %3, 1.0, %3\n\tv_mfma_f32_16x16x32_bf16 %4, %11, %6, %4"
      : "+v"(as[0]), "+v"(as[1]), "=&a"(fwo[27]), "+v"(ze[3]), "+v"(as[2]) : "a"(fwo[3]), "v"(pu[1]), "v"(rwd), "v"(ac[0][0]), "a"(fwo[4]), "v"(ro), "a"(fwo[5]) : "memory"); SB();
  WAITVB(11);
  SB();
  asm volatile("v_mfma_f32_16x16x32_bf16 %0, %5, %6, %0\n\tds_write_b128 %7, %8 offset:1024\n\tv_mfma_f32_16x16x32_bf16 %1, %9, %6, %1\n\tv_rcp_f32 %2, %2\n\tv_mfma_f32_16x16x32_bf16 %3, %10, %6, %3\n\tds_read_b128 %4, %11 offset:39936"
      : "+v"(as[0]), "+v"(as[1]), "+v"(ze[0]), "+v"(as[2]), "=&a"(fwo[28]) : "a"(fwo[6]), "v"(pu[2]), "v"(rwd), "v"(ac[0][1]), "a"(fwo[7]), "a"(fwo[8]), "v"(ro) : "memory"); SB();
  WAITVB(10);
  SB();
  asm volatile("v_mfma_f32_16x16x32_bf16 %0, %5, %6, %0\n\tds_write_b128 %7, %8 offset:2048\n\tv_mfma_f32_16x16x32_bf16 %1, %9, %6, %1\n\tv_rcp_f32 %2, %2\n\tv_mfma_f32_16x16x32_bf16 %3, %10, %6, %3\n\tds_read_b128 %4, %11 offset:46080"
      : "+v"(as[0]), "+v"(as[1]), "+v"(ze[1]), "+v"(as[2]), "=&a"(fwo[29]) : "a"(fwo[9]), "v"(pu[3]), "v"(rwd), "v"(ac[0][2]), "a"(fwo[10]), "a"(fwo[11]), "v"(ro) : "memory"); SB();
  WAITVB(9);
  SB();
  asm volatile("v_mfma_f32_16x16x32_bf16 %0, %5, %6, %0\n\tds_write_b128 %7, %8 offset:3072\n\tv_mfma_f32_16x16x32_bf16 %1, %9, %6, %1\n\tv_rcp_f32 %2, %2\n\tv_mfma_f32_16x16x32_bf16 %3, %10, %6, %3\n\tds_read_b128 %4, %11 offset:34816"
      : "+v"(as[0]), "+v"(as[1]), "+v"(ze[2]), "+v"(as[2]), "=&a"(fwo[30]) : "a"(fwo[12]), "v"(pu[4]), "v"(rwd), "v"(ac[0][3]), "a"(fwo[13]), "a"(fwo[14]), "v"(ro) : "memory"); SB();
  WAITVB(8);
  SB();
  asm volatile("v_mfma_f32_16x16x32_bf16 %0, %3, %4, %0\n\tv_rcp_f32 %1, %1\n\tv_mfma_f32_16x16x32_bf16 %2, %5, %4, %2"
      : "+v"(as[0]), "+v"(ze[3]), "+v"(as[1]) : "a"(fwo[15]), "v"(pu[5]), "a"(fwo[16])); SB();
  WAITL(1);
  BAR0();
  SB();
  asm volatile("v_mfma_f32_16x16x32_bf16 %0, %1, %2, %0"
      : "+v"(as[2]) : "a"(fwo[17]), "v"(pu[5])); SB();
  WAITVB(7);
  asm volatile("ds_read_b128 %0, %9 offset:40960\n\tv_mfma_f32_16x16x32_bf16 %1, %10, %11, %1\n\tds_read_b128 %2, %12 offset:0\n\tds_read_b128 %3, %12 offset:4096\n\tv_mfma_f32_16x16x32_bf16 %4, %13, %11, %4\n\tds_read_b128 %5, %12 offset:8192\n\tds_read_b128 %6, %12 offset:12288\n\tv_mfma_f32_16x16x32_bf16 %7, %14, %11, %7\n\tv_mul_f32 %8, %15, %16"
      : "=&a"(fwo[31]), "+v"(as[0]), "=&v"(py[2][0]), "=&v"(py[2][1]), "+v"(as[1]), "=&v"(py[2][2]), "=&v"(py[2][3]), "+v"(as[2]), "=&v"(ge[4]) : "v"(ro), "a"(fwo[18]), "v"(pu[6]), "v"(rr), "a"(fwo[19]), "a"(fwo[20]), "v"(ye[0]), "v"(ze[0]) : "memory"); SB();
  WAITVB(6);
  SB();
  asm volatile("v_mfma_f32_16x16x32_bf16 %0, %7, %8, %0\n\tv_mul_f32 %1, %9, %10\n\tv_mul_f32 %2, %11, %12\n\tv_mfma_f32_16x16x32_bf16 %3, %13, %8, %3\n\tds_read_b128 %4, %14 offset:47104\n\tv_mul_f32 %5, %15, %16\n\tv_mfma_f32_16x16x32_bf16 %6, %17, %8, %6"
      : "+v"(as[0]), "=&v"(ge[5]), "=&v"(ge[6]), "+v"(as[1]), "=&a"(fwo[32]), "=&v"(ge[7]), "+v"(as[2]) : "a"(fwo[21]), "v"(pu[7]), "v"(ye[1]), "v"(ze[1]), "v"(ye[2]), "v"(ze[2]), "a"(fwo[22]), "v"(ro), "v"(ye[3]), "v"(ze[3]), "a"(fwo[23]) : "memory"); SB();
  WAITVB(5);
  asm volatile("ds_read_b128 %0, %6 offset:35840\n\tds_read_b128 %1, %6 offset:41984\n\tds_read_b128 %2, %6 offset:48128\n\tds_read_b128 %3, %6 offset:36864\n\tds_read_b128 %4, %6 offset:43008\n\tv_mfma_f32_16x16x32_bf16 %5, %7, %8, %5"
      : "=&a"(fwo[33]), "=&a"(fwo[34]), "=&a"(fwo[35]), "=&a"(fwo[36]), "=&a"(fwo[37]), "+v"(as[0]) : "v"(ro), "a"(fwo[24]), "v"(pu[8]) : "memory"); SB();
  WAITL(6);
  asm volatile("v_add_f32 %0, %6, %7\n\tv_mfma_f32_16x16x32_bf16 %1, %8, %9, %1\n\tv_add_f32 %2, %10, %7\n\tv_add_f32 %3, %11, %7\n\tv_mfma_f32_16x16x32_bf16 %4, %12, %9, %4\n\tv_add_f32 %5, %13, %7"
      : "=&v"(ye[0]), "+v"(as[1]), "=&v"(ye[1]), "=&v"(ye[2]), "+v"(as[2]), "=&v"(ye[3]) : "v"(py[2][0][0]), "v"(bias), "a"(fwo[25]), "v"(pu[8]), "v"(py[2][0][1]), "v"(py[2][0][2]), "a"(fwo[26]), "v"(py[2][0][3])); SB();
  WAITVB(4);
  SB();
  asm volatile("v_mfma_f32_16x16x32_bf16 %0, %7, %8, %0\n\tv_add_f32 %1, %9, %10\n\tv_add_f32 %2, %11, %12\n\tv_mfma_f32_16x16x32_bf16 %3, %13, %8, %3\n\tv_add_f32 %4, %14, %15\n\tv_add_f32 %5, %16, %17\n\tv_mfma_f32_16x16x32_bf16 %6, %18, %8, %6"
      : "+v"(as[0]), "=&v"(ze[0]), "=&v"(ze[1]), "+v"(as[1]), "=&v"(ze[2]), "=&v"(ze[3]), "+v"(as[2]) : "a"(fwo[27]), "v"(pu[9]), "v"(py[2][1][0]), "v"(py[2][2][0]), "v"(py[2][1][1]), "v"(py[2][2][1]), "a"(fwo[28]), "v"(py[2][1][2]), "v"(py[2][2][2]), "v"(py[2][1][3]), "v"(py[2][2][3]), "a"(fwo[29])); SB();
  WAITVB(3);
  asm volatile("ds_read_b128 %0, %4 offset:49152\n\tds_read_b128 %1, %4 offset:37888\n\tds_read_b128 %2, %4 offset:44032\n\tds_read_b128 %3, %4 offset:50176"
      : "=&a"(fwo[38]), "=&a"(fwo[39]), "=&a"(fwo[40]), "=&a"(fwo[41]) : "v"(ro) : "memory"); SB();
  WAITL(6);
  SB();
  asm volatile("v_mfma_f32_16x16x32_bf16 %0, %7, %8, %0\n\tv_add_f32 %1, %1, %9\n\tv_add_f32 %2, %2, %10\n\tv_mfma_f32_16x16x32_bf16 %3, %11, %8, %3\n\tv_add_f32 %4, %4, %12\n\tv_add_f32 %5, %5, %13\n\tv_mfma_f32_16x16x32_bf16 %6, %14, %8, %6"
      : "+v"(as[0]), "+v"(ye[0]), "+v"(ye[1]), "+v"(as[1]), "+v"(ye[2]), "+v"(ye[3]), "+v"(as[2]) : "a"(fwo[30]), "v"(pu[10]), "v"(py[2][3][0]), "v"(py[2][3][1]), "a"(fwo[31]), "v"(py[2][3][2]), "v"(py[2][3][3]), "a"(fwo[32])); SB();
  WAITL(0);
  WAITVB(0);
  SB();
  asm volatile("v_mfma_f32_16x16x32_bf16 %0, %10, %11, %0\n\tv_add_f32 %1, %1, %2\n\tv_add_f32 %3, %3, %4\n\tv_mfma_f32_16x16x32_bf16 %0, %12, %13, %0\n\tv_add_f32 %5, %5, %6\n\tv_add_f32 %7, %7, %8\n\tv_mfma_f32_16x16x32_bf16 %0, %14, %15, %0\n\tv_mul_f32 %2, 0xbfb8aa3b, %1\n\tv_mul_f32 %4, 0xbfb8aa3b, %3\n\tv_mfma_f32_16x16x32_bf16 %9, %16, %11, %9\n\tv_mul_f32 %6, 0xbfb8aa3b, %5\n\tv_mul_f32 %8, 0xbfb8aa3b, %7\n\tv_mfma_f32_16x16x32_bf16 %9, %17, %13, %9\n\tv_exp_f32 %2, %2\n\tv_mfma_f32_16x16x32_bf16 %9, %18, %15, %9\n\tv_exp_f32 %4, %4"
      : "+v"(as[0]), "+v"(ye[0]), "+v"(ze[0]), "+v"(ye[1]), "+v"(ze[1]), "+v"(ye[2]), "+v"(ze[2]), "+v"(ye[3]), "+v"(ze[3]), "+v"(as[1]) : "a"(fwo[33]), "v"(pu[11]), "a"(fwo[36]), "v"(pu[12]), "a"(fwo[39]), "v"(pu[13]), "a"(fwo[34]), "a"(fwo[37]), "a"(fwo[40])); SB();
  asm volatile("v_mfma_f32_16x16x32_bf16 %0, %5, %6, %0\n\tv_exp_f32 %1, %1\n\tv_mfma_f32_16x16x32_bf16 %0, %7, %8, %0\n\tv_exp_f32 %2, %2\n\tv_mfma_f32_16x16x32_bf16 %0, %9, %10, %0\n\tv_add_f32 %3, 1.0, %3\n\tv_add_f32 %4, 1.0, %4"
      : "+v"(as[2]), "+v"(ze[2]), "+v"(ze[3]), "+v"(ze[0]), "+v"(ze[1]) : "a"(fwo[35]), "v"(pu[11]), "a"(fwo[38]), "v"(pu[12]), "a"(fwo[41]), "v"(pu[13])); SB();
  STAMP(44);
  asm volatile("v_add_f32 %0, 1.0, %0\n\tv_add_f32 %1, 1.0, %1\n\tv_rcp_f32 %2, %2\n\tv_rcp_f32 %3, %3\n\tv_rcp_f32 %0, %0\n\tv_rcp_f32 %1, %1\n\tv_mul_f32 %4, %11, %2\n\tv_mul_f32 %5, %12, %3\n\tv_mul_f32 %6, %13, %0\n\tv_mul_f32 %7, %14, %1\n\tv_fma_f32 %8, %15, %16, %8\n\tv_fma_f32 %9, %17, %18, %9\n\tv_fma_f32 %10, %19, %20, %10"
      : "+v"(ze[2]), "+v"(ze[3]), "+v"(ze[0]), "+v"(ze[1]), "=&v"(ge[8]), "=&v"(ge[9]), "=&v"(ge[10]), "=&v"(ge[11]), "+v"(xe[0]), "+v"(xe[1]), "+v"(xe[2]) : "v"(ye[0]), "v"(ye[1]), "v"(ye[2]), "v"(ye[3]), "v"(as[0][0]), "v"(ge[0]), "v"(as[0][1]), "v"(ge[1]), "v"(as[0][2]), "v"(ge[2])); SB();
  asm volatile("v_fma_f32 %0, %9, %10, %0\n\tv_cvt_pk_bf16_f32 %1, %11, %12\n\tv_cvt_pk_bf16_f32 %2, %13, %0\n\tv_fma_f32 %3, %14, %15, %3\n\tv_fma_f32 %4, %16, %17, %4\n\tv_fma_f32 %5, %18, %19, %5\n\tv_fma_f32 %6, %20, %21, %6\n\tv_cvt_pk_bf16_f32 %7, %3, %4\n\tv_cvt_pk_bf16_f32 %8, %5, %6"
      : "+v"(xe[3]), "=&v"(ov[0][0]), "=&v"(ov[0][1]), "+v"(xe[4]), "+v"(xe[5]), "+v"(xe[6]), "+v"(xe[7]), "=&v"(ov[1][0]), "=&v"(ov[1][1]) : "v"(as[0][3]), "v"(ge[3]), "v"(xe[0]), "v"(xe[1]), "v"(xe[2]), "v"(as[1][0]), "v"(ge[4]), "v"(as[1][1]), "v"(ge[5]), "v"(as[1][2]), "v"(ge[6]), "v"(as[1][3]), "v"(ge[7])); SB();
  asm volatile("ds_write_b64 %6, %7 offset:0\n\tv_fma_f32 %0, %8, %9, %0\n\tv_fma_f32 %1, %10, %11, %1\n\tv_fma_f32 %2, %12, %13, %2\n\tv_fma_f32 %3, %14, %15, %3\n\tv_cvt_pk_bf16_f32 %4, %0, %1\n\tv_cvt_pk_bf16_f32 %5, %2, %3\n\tds_write_b64 %16, %17 offset:0"
      : "+v"(xe[8]), "+v"(xe[9]), "+v"(xe[10]), "+v"(xe[11]), "=&v"(ov[2][0]), "=&v"(ov[2][1]) : "v"(xta[0]), "v"(ov[0]), "v"(as[2][0]), "v"(ge[8]), "v"(as[2][1]), "v"(ge[9]), "v"(as[2][2]), "v"(ge[10]), "v"(as[2][3]), "v"(ge[11]), "v"(xta[1]), "v"(ov[1]) : "memory"); SB();
  asm volatile("ds_write_b64 %0, %1 offset:0"
      :  : "v"(xta[2]), "v"(ov[2]) : "memory"); SB();
  { u32x4 t0_, t1_; DSR16(t0_, xo, 0); DSR16(t1_, xo, 1024); WAITLB(1);
    BS16(vXa, rsO_, 0, t0_, ""); WAITLB(0); BS16(vXb, rsO_, 1024, t1_, ""); }
  STAMP(23);
  if (b == 8 * 28 && w == 0) { const unsigned vZ = 0u; ST4(vZ, ws, nonce); }

  STAMP(12);
  STAMP_DUMP();
  }
  }
  asm volatile(".Lcpf_e:");
}

extern "C" void kernel_launch(void* const* d_in, const int* in_sizes, int n_in,
                              void* d_out, int out_size, void* d_ws, size_t ws_size,
                              hipStream_t stream) {
  fused_kernel<<<256, 256, 0, stream>>>((const char*)d_in[0], (const char*)d_in[1], (const char*)d_in[2], (const char*)d_in[4],
                                        (const char*)d_in[5], (const char*)d_in[6], (const char*)d_in[7], (char*)d_ws, (char*)d_out);
}
```

```cpp
#include <hip/hip_runtime.h>
#include <hip/hip_bf16.h>
#include <stdint.h>

typedef short bf16x8 __attribute__((ext_vector_type(8)));
typedef short s16x4 __attribute__((ext_vector_type(4)));
typedef float f32x4 __attribute__((ext_vector_type(4)));
typedef unsigned u32x4 __attribute__((ext_vector_type(4)));
typedef unsigned u32x2 __attribute__((ext_vector_type(2)));
typedef __bf16 bf16x2_t __attribute__((ext_vector_type(2)));
typedef float f32x2_t __attribute__((ext_vector_type(2)));
#define LDS_AS __attribute__((address_space(3)))
#define DEV __device__ __forceinline__

#define STAMP(i)
#define STAMP_DUMP()

#define SB() __builtin_amdgcn_sched_barrier(0)
#define WAITV(n) asm volatile("s_waitcnt vmcnt(" #n ")" ::: "memory")
#define WAITV1(n, a) asm volatile("s_waitcnt vmcnt(" #n ")" : "+v"(a) :: "memory")
#define WAITV2(n, a, b) asm volatile("s_waitcnt vmcnt(" #n ")" : "+v"(a), "+v"(b) :: "memory")
#define WAITV3(n, a, b, c) asm volatile("s_waitcnt vmcnt(" #n ")" : "+v"(a), "+v"(b), "+v"(c) :: "memory")
#define WAITV4(n, a, b, c, d) asm volatile("s_waitcnt vmcnt(" #n ")" : "+v"(a), "+v"(b), "+v"(c), "+v"(d) :: "memory")
#define WAITL(n) do { asm volatile("" ::: "memory"); __builtin_amdgcn_s_waitcnt(0xc07f | ((n) << 8)); asm volatile("" ::: "memory"); } while (0)
#define BAR() do { WAITL(0); __builtin_amdgcn_s_barrier(); asm volatile("" ::: "memory"); } while (0)
#define BAR0() do { asm volatile("" ::: "memory"); __builtin_amdgcn_s_barrier(); asm volatile("" ::: "memory"); } while (0)
#define OPQ(p) asm volatile("" : "+s"(p))
#define OPV(p) asm volatile("" : "+v"(p))
#define LD16(dst, voff, sb, imm) asm volatile("global_load_dwordx4 %0, %1, %2 offset:" #imm : "=&v"(dst) : "v"(voff), "s"(sb) : "memory")
#define LD8(dst, voff, sb, imm) asm volatile("global_load_dwordx2 %0, %1, %2 offset:" #imm : "=&v"(dst) : "v"(voff), "s"(sb) : "memory")
#define DMA1(v0, rs, so, lds, limm, imm, bits) \
  asm volatile("s_add_u32 m0, %3, %4\n\ts_nop 0\n\tbuffer_load_dwordx4 %0, %1, %2 offen offset:" #imm " " bits " lds" :: "v"(v0), "s"(rs), "s"(so), "s"(lds), "n"(limm) : "memory", "scc")
#define DMA4(v0, v1, rs, so, step, lds, limm) do { unsigned t_; \
  asm volatile("s_add_u32 m0, %5, %6\n\ts_add_u32 %0, %4, %7\n\tbuffer_load_dwordx4 %1, %3, %4 offen lds\n\tbuffer_load_dwordx4 %2, %3, %4 offen offset:1024 lds\n\t" \
               "buffer_load_dwordx4 %1, %3, %0 offen offset:2048 lds\n\tbuffer_load_dwordx4 %2, %3, %0 offen offset:3072 lds" \
               : "=&s"(t_) : "v"(v0), "v"(v1), "s"(rs), "s"(so), "s"(lds), "n"(limm), "n"(step) : "memory", "scc"); } while (0)
#define DMAW2(v0, rs, so) asm volatile("s_cmp_eq_u32 %3, 3\n\ts_cbranch_scc1 .LBBa%=\n\tbuffer_load_dwordx4 %0, %1, %2 offen offset:0 sc1 lds\n\tbuffer_load_dwordx4 %0, %1, %2 offen offset:1024 sc1 lds\n\ts_branch .LBBb%=\n.LBBa%=:\n\tbuffer_load_dword %0, %1, %2 offen lds\n\tbuffer_load_dword %0, %1, %2 offen lds\n.LBBb%=:" \
  :: "v"(v0), "s"(rs), "s"(so), "s"(w) : "memory", "scc")
#define DMAW4(v0, rs, so) asm volatile("s_cmp_eq_u32 %3, 3\n\ts_cbranch_scc1 .LBBa%=\n\tbuffer_load_dwordx4 %0, %1, %2 offen offset:0 sc1 lds\n\tbuffer_load_dwordx4 %0, %1, %2 offen offset:1024 sc1 lds\n\tbuffer_load_dwordx4 %0, %1, %2 offen offset:2048 sc1 lds\n\tbuffer_load_dwordx4 %0, %1, %2 offen offset:3072 sc1 lds\n\ts_branch .LBBb%=\n.LBBa%=:\n\tbuffer_load_dword %0, %1, %2 offen lds\n\tbuffer_load_dword %0, %1, %2 offen lds\n\tbuffer_load_dword %0, %1, %2 offen lds\n\tbuffer_load_dword %0, %1, %2 offen lds\n.LBBb%=:" \
  :: "v"(v0), "s"(rs), "s"(so), "s"(w) : "memory", "scc")
#define TOUCH(v0, rs, so, sadd) do { unsigned t_; \
  asm volatile("s_mov_b32 m0, %4\n\ts_add_u32 %0, %3, %5\n\tbuffer_load_dword %1, %2, %0 offen lds" : "=&s"(t_) : "v"(v0), "s"(rs), "s"(so), "s"(lds0 + DUMP), "n"(sadd) : "memory", "scc"); } while (0)
#define HS16(voff, so, imm, v) do { const u32x4 v_ = (v); \
  asm volatile("buffer_store_dwordx4 %0, %1, %2, %3 offen offset:" #imm " sc1\n\ts_nop 1" :: "v"(v_), "v"(voff), "s"(rsW), "s"(so) : "memory"); } while (0)
#define HS16M(lo, hi, voff, so, imm, v) do { const u32x4 v_ = (v); \
  asm volatile("s_mov_b32 exec_lo, " #lo "\n\ts_mov_b32 exec_hi, " #hi "\n\tbuffer_store_dwordx4 %0, %1, %2, %3 offen offset:" #imm " sc1\n\ts_mov_b64 exec, -1\n\ts_nop 0" \
               :: "v"(v_), "v"(voff), "s"(rsW), "s"(so) : "memory"); } while (0)
#define HS16H(hi, voff, so, imm, v) do { const u32x4 v_ = (v); \
  asm volatile("s_mov_b32 exec_hi, " #hi "\n\tbuffer_store_dwordx4 %0, %1, %2, %3 offen offset:" #imm " sc1\n\ts_mov_b32 exec_hi, -1\n\ts_nop 0" \
               :: "v"(v_), "v"(voff), "s"(rsW), "s"(so) : "memory"); } while (0)
#define BS16(voff, rs, imm, v, bits) do { const u32x4 v_ = (v); asm volatile("buffer_store_dwordx4 %0, %1, %2, 0 offen offset:" #imm " " bits "\n\ts_nop 1" :: "v"(v_), "v"(voff), "s"(rs) : "memory"); } while (0)
#define ST4(voff, sb, v) do { const unsigned v_ = (v); asm volatile("global_store_dword %0, %1, %2 sc1" :: "v"(voff), "v"(v_), "s"(sb) : "memory"); } while (0)
#define FLAG16(mask, so) do { const u32x4 v_ = {nonce, nonce, nonce, nonce}; \
  asm volatile("s_mov_b64 exec, " #mask "\n\tbuffer_store_dwordx4 %0, %1, %2, %3 offen sc1\n\ts_mov_b64 exec, -1\n\ts_nop 0" :: "v"(v_), "v"(vL16), "s"(rsW), "s"(so) : "memory"); } while (0)
#define STFA(so) FLAG16(15, so)
#define STFQ(so) FLAG16(1, so)
#define STFU(so) FLAG16(1, so)
#define STFO(so) FLAG16(15, so)
DEV u32x4 srd(const void* p, unsigned bytes) { return (u32x4){(unsigned)(size_t)p, (unsigned)((size_t)p >> 32), bytes, 0x20000u}; }
DEV float lo2f(unsigned v) { return __uint_as_float(v << 16); }
DEV float hi2f(unsigned v) { return __uint_as_float(v & 0xffff0000u); }
DEV unsigned pack2(float a, float b) { f32x2_t v = {a, b}; return __builtin_bit_cast(unsigned, __builtin_convertvector(v, bf16x2_t)); }
DEV float rbf(float a) { return __uint_as_float(pack2(0.f, a)); }
DEV f32x2_t rbf2(f32x2_t a) { return (f32x2_t){rbf(a[0]), rbf(a[1])}; }
DEV float dot2(unsigned a, unsigned b, float c) {
  return __builtin_amdgcn_fdot2_f32_bf16(__builtin_bit_cast(bf16x2_t, a), __builtin_bit_cast(bf16x2_t, b), c, false);
}
DEV bf16x8 cat8(s16x4 a, s16x4 b) { return __builtin_shufflevector(a, b, 0, 1, 2, 3, 4, 5, 6, 7); }
DEV bf16x8 tr8u(unsigned p, int second) {
  return cat8(__builtin_amdgcn_ds_read_tr16_b64_v4i16((LDS_AS s16x4*)p), __builtin_amdgcn_ds_read_tr16_b64_v4i16((LDS_AS s16x4*)(p + second)));
}
template <int CTRL> DEV float dpp_add(float v) {
  return v + __builtin_bit_cast(float, __builtin_amdgcn_update_dpp(0, __builtin_bit_cast(int, v), CTRL, 0xf, 0xf, true));
}
DEV float red16(float v) { return dpp_add<0x140>(dpp_add<0x141>(dpp_add<0x4E>(dpp_add<0xB1>(v)))); }
DEV unsigned ror8(unsigned v) { return (unsigned)__builtin_amdgcn_update_dpp(0, (int)v, 0x128, 0xf, 0xf, true); }
DEV float fmx(float a, float b) { return __builtin_elementwise_maximum(a, b); }
DEV float xmax2(float a, float b) {
  const auto r = __builtin_amdgcn_permlane16_swap(__float_as_uint(a), __float_as_uint(b), false, false);
  const float v = fmx(__uint_as_float(r[0]), __uint_as_float(r[1]));
  const auto s = __builtin_amdgcn_permlane32_swap(__float_as_uint(v), __float_as_uint(v), false, false);
  return fmx(__uint_as_float(s[0]), __uint_as_float(s[1]));
}
DEV float xsum2(float a, float b) {
  const auto r = __builtin_amdgcn_permlane16_swap(__float_as_uint(a), __float_as_uint(b), false, false);
  const float v = __uint_as_float(r[0]) + __uint_as_float(r[1]);
  const auto s = __builtin_amdgcn_permlane32_swap(__float_as_uint(v), __float_as_uint(v), false, false);
  return __uint_as_float(s[0]) + __uint_as_float(s[1]);
}
DEV f32x2_t xbcast2(float v) {
  const auto r = __builtin_amdgcn_permlane16_swap(__float_as_uint(v), __float_as_uint(v), false, false);
  return (f32x2_t){__uint_as_float(r[0]), __uint_as_float(r[1])};
}
#define DSR16(d, addr, off) asm volatile("ds_read_b128 %0, %1 offset:" #off : "=&v"(d) : "v"(addr) : "memory")
#define WAITVB(n) __builtin_amdgcn_s_waitcnt(0x0f70 | ((n) & 15) | (((n) >> 4) << 14))
#define WAITLB(n) __builtin_amdgcn_s_waitcnt(0xc07f | ((n) << 8))

constexpr int kSpinCap = 200000;
template <int SLEEP> DEV void poll1(const u32x4& rsW, unsigned voff, unsigned nonce) {
#pragma unroll 1
  for (int spin = 0; spin < kSpinCap; ++spin) {
    __builtin_amdgcn_s_sleep(SLEEP);
    unsigned v;
    asm volatile("buffer_load_dword %0, %1, %2, 0 offen sc1\n\ts_waitcnt vmcnt(0)" : "=&v"(v) : "v"(voff), "s"(rsW) : "memory");
    if (__ballot(v != nonce) == 0ull) return;
  }
}

template <int SLEEP> DEV void poll1(const u32x4& rsW, unsigned voff, unsigned voff2, unsigned nonce) {
#pragma unroll 1
  for (int spin = 0; spin < kSpinCap; ++spin) {
    __builtin_amdgcn_s_sleep(SLEEP);
    unsigned v, v2;
    asm volatile("buffer_load_dword %0, %2, %4, 0 offen sc1\n\tbuffer_load_dword %1, %3, %4, 0 offen sc1\n\ts_waitcnt vmcnt(0)" : "=&v"(v), "=&v"(v2) : "v"(voff), "v"(voff2), "s"(rsW) : "memory");
    if (__ballot(((v ^ nonce) | (v2 ^ nonce)) != 0u) == 0ull) return;
  }
}
template <int SLEEP> DEV void poll0(const u32x4& rsW, unsigned voff, unsigned nonce) {
  unsigned v;
  asm volatile("buffer_load_dword %0, %1, %2, 0 offen sc1\n\ts_waitcnt vmcnt(0)" : "=&v"(v) : "v"(voff), "s"(rsW) : "memory");
  if (__ballot(v != nonce) != 0ull) poll1<SLEEP>(rsW, voff, nonce);
}

DEV float ln_inv(float var) {
  const float sd = rbf(__builtin_amdgcn_sqrtf(rbf(rbf(fmaxf(var, 0.f)) + 1e-5f)));
  return __builtin_amdgcn_rcpf(sd);
}
DEV float ln_a(const unsigned (&p)[14], f32x2_t (&d)[14], float& dm) {
  float s0 = 0.f, s1 = 0.f;
#pragma unroll
  for (int i = 0; i < 14; i += 2) { s0 = dot2(p[i], 0x3f803f80u, s0); s1 = dot2(p[i + 1], 0x3f803f80u, s1); }
  const float mean = red16(s0 + s1) * (1.0f / 448.0f);
  const float mb = rbf(mean);
  dm = mean - mb;
  f32x2_t q = {0.f, 0.f}, q1 = {0.f, 0.f};
#pragma unroll
  for (int i = 0; i < 14; ++i) d[i] = (f32x2_t){lo2f(p[i]), hi2f(p[i])} - (f32x2_t){mb, mb};
#pragma unroll
  for (int i = 0; i < 14; i += 2) { q = d[i] * d[i] + q; q1 = d[i + 1] * d[i + 1] + q1; }
  q += q1;
  return q[0] + q[1];
}
DEV void ln_b(f32x2_t (&d)[14], float q, float dm, float& inv, float& a, float& c) {
  const float var = red16(q) * (1.0f / 448.0f) - dm * dm;
  inv = ln_inv(var);
  const float inv2 = ln_inv(var * inv * inv);
  a = inv * inv2; c = -rbf(dm * inv) * inv2;
#pragma unroll
  for (int i = 0; i < 14; ++i) { d[i] = rbf2(d[i]); asm volatile("" : "+v"(d[i])); }
}
DEV void ln_h(unsigned (&p)[14], const f32x2_t (&d)[14], float a, float c) {
#pragma unroll
  for (int i = 0; i < 14; ++i) {
    const f32x2_t r = __builtin_elementwise_fma(d[i], (f32x2_t){a, a}, (f32x2_t){c, c});
    p[i] = pack2(r[0], r[1]);
  }
}
DEV void ln_x(unsigned (&p)[14], const f32x2_t (&d)[14], float inv) {
#pragma unroll
  for (int i = 0; i < 14; ++i) { const f32x2_t r = d[i] * (f32x2_t){inv, inv}; p[i] = pack2(r[0], r[1]); }
}

constexpr unsigned WS_XN = 0;
constexpr unsigned WS_HT = WS_XN + 2048 * 1024;
constexpr unsigned WS_WQ = WS_HT + 4 * 64 * 8192;
constexpr unsigned WS_UT = WS_WQ + 8 * 4 * 6 * 8192;
constexpr unsigned WS_OT = WS_UT + 28 * 8 * 8192;
constexpr unsigned WS_WO = WS_OT + 128 * 2 * 8192;
constexpr unsigned WS_FA = WS_WO + 4 * 131072;
constexpr unsigned WS_FQ = WS_FA + 1536 * 4 * 16;
constexpr unsigned WS_FU = WS_FQ + 64 * 4 * 4;
constexpr unsigned WS_FO = WS_FU + 1024 * 4 * 4;
constexpr unsigned WS_END = WS_FO + 512 * 4 * 16;
constexpr int SCW = 10240;
constexpr int RB = 40960, RW = 30720;
constexpr int WOB = 55296;
constexpr int S3B = 112640;
constexpr int TP = 912;
constexpr int SK = 18432, SQ = 36864 - 512;
constexpr int DUMP = 130816;
constexpr int LDS_BYTES = 163840;

__global__ __launch_bounds__(256, 1) void fused_kernel(const char* __restrict__ x, const char* __restrict__ wqkv, const char* __restrict__ wo,
                                                       const char* __restrict__ w1, const char* __restrict__ b1,
                                                       const char* __restrict__ w2, const char* __restrict__ b2,
                                                       char* __restrict__ ws, char* __restrict__ out) {
  __shared__ __attribute__((aligned(16))) char smem[LDS_BYTES];
  const int lane = threadIdx.x & 63;
  const int w = __builtin_amdgcn_readfirstlane(threadIdx.x >> 6);
  const int b = blockIdx.x, xq = (b & 7) >> 1, jb = b >> 3;
  STAMP(0);
  { const char* pc_; unsigned vo_ = (threadIdx.x * 4 + (jb & 3)) * 128;
    asm volatile("s_getpc_b64 %0\n.Lcpf_b:\n\ts_mov_b32 m0, %2\n\tv_min_u32 %1, .Lcpf_e-.Lcpf_b-4, %1\n\tglobal_load_lds_dword %1, %0"
                 : "=&s"(pc_), "+v"(vo_) : "s"((unsigned)(size_t)(LDS_AS char*)smem + 40704) : "memory"); }
  const unsigned lds0 = (unsigned)(size_t)(LDS_AS char*)smem;
  const int n16 = lane & 15, g4 = lane >> 4, q4 = n16 >> 2, pq = lane & 3, r8 = lane >> 3;
  const unsigned vL16 = lane * 16;
  const unsigned wsw = ((lane & 7) ^ (r8 & 2)) * 16;
  const int th = xq >> 1, hq = xq & 1, pr_ = 64 * (1 - th) + 32 * hq + jb;
  unsigned vX = (4 * w + g4) * 896 + n16 * 16, vX3 = (4 * w + g4) * 896 + 768 + (n16 & 7) * 16 + (n16 >> 3) * 8, oX = pr_ * (16 * 896);
  asm volatile("" : "+v"(vX), "+v"(vX3)); asm volatile("" : "+s"(oX));
  u32x4 xa, xb, xc; u32x2 xd;
  const int d4 = (jb * 37) >> 8, fi = jb - 7 * d4, ft = 7 * xq + fi;
  const int rota = 4 * d4 + 2 * fi, rotg = 0 * fi;
#define KS1(c) ((((c) + rotg) & 0) + (((c) + rota) & 15) + 16 * w)
  const unsigned ldsR = lds0 + RB + w * RW;
  unsigned vW1a = r8 * 3584 + wsw + ft * 128, vW1b = (vW1a ^ 64) + (8 * 3584 - 1024);
  unsigned sp[3];
#pragma unroll
  for (int c = 0; c < 3; ++c) { sp[c] = KS1(c) * 114688; OPQ(sp[c]); }
  asm volatile("" : "+v"(vW1a), "+v"(vW1b) : "s"(ldsR));
  asm volatile("" :: "s"(x), "s"(wqkv), "s"(wo), "s"(w1), "s"(b1), "s"(w2), "s"(b2), "s"(ws), "s"(out));
  int cls = b & 1;
  if (cls) {
  asm volatile("; ROLE C");
  const char* sX = x + oX;
  LD16(xa, vX, sX, 0); LD16(xb, vX, sX, 256); LD16(xc, vX, sX, 512); LD8(xd, vX3, sX, 0);
  } else if (jb >= 28) {
    asm volatile("; ROLE B");
    const int b7 = (int)((size_t)w1 >> 7) + 7 * xq, f_ = ((3 - b7) & 3) + 4 * (jb & 1);
    const unsigned vT = (512 * w + 32 * ((4 * g4 + 2 * f_ + 2 + ((jb >> 1) & 1)) & 15) + 2 * n16 + (((b7 + f_) >> 2) & 1)) * 3584 + (7 * xq + f_) * 128;
    u32x4 rs1 = srd(w1, f_ < 7 ? 2048u * 3584u : 0u);
    TOUCH(vT, rs1, 0u, 0);
  } else {
  asm volatile("; ROLE B");
  u32x4 rs1 = srd(w1, 2048u * 3584u);
  DMA4(vW1a, vW1b, rs1, sp[0], 55296, ldsR, 0);
  DMA4(vW1a, vW1b, rs1, sp[1], 55296, ldsR, 4096);
  DMA4(vW1a, vW1b, rs1, sp[2], 55296, ldsR, 8192);
  }
  OPQ(cls);
  if (cls) {
  asm volatile("; NEED C");
  SB();
  const int kg = 32 * (1 - th) + jb, hpq = 4 * (1 - hq) + w;
  const unsigned ldsQ = lds0 + 16384 + w * 3072;
  unsigned vQ_ = r8 * 2688 + ((lane & 7) ^ ((r8 & 2) << 1)) * 16, sQ_ = kg * (8 * 2688) + hpq * 112;
  { u32x4 rsQ = srd(wqkv, 448u * 2688u);
    DMA1(vQ_, rsQ, sQ_, ldsQ, 0, 0, "");
    DMA1(vQ_, rsQ, sQ_, ldsQ, 128, 896, "");
    DMA1(vQ_, rsQ, sQ_, ldsQ, 256, 1792, "");
  }
  unsigned nonce0; asm volatile("s_load_dword %0, %1, 0x0" : "=&s"(nonce0) : "s"(ws) : "memory");
  const int tt = 8 * th + (jb >> 2), hp = 4 * hq + (jb & 3);
  const int st = 16 * (1 - th) + 8 * hq + (jb & 7), dt = jb >> 3, db0 = 4 * dt;
#define BLK(j) (db0 + ((j) ^ 0))
  u32x4 rsW = srd((char*)(((size_t)ws + 8191) & ~(size_t)4095) + 512, WS_END); OPQ(rsW);
  const unsigned trb = (8 * g4 + q4) * 128 + pq * 8, trf = ((q4 >> 1) | ((g4 & 1) << 1)) * 32;
  const unsigned vF = r8 * 1024 + (lane & 7) * 16;
  unsigned vN = (4 * w + g4) * 1024 + n16 * 16, sN = WS_XN + pr_ * 16384;
  unsigned hw = lds0 + (4 * w + g4) * TP + n16 * 16, hw3 = lds0 + (4 * w + g4) * TP + 768 + (n16 & 7) * 16 + (n16 >> 3) * 8;
  unsigned th_ = lds0 + q4 * TP + 224 * w + 64 * g4 + 32 * (pq >> 1) + 8 * (pq & 1), sH = WS_HT + w * 524288 + pr_ * 4096, sFA = WS_FA + (w * 128 + pr_) * (4 * 16);
  unsigned tq = ldsQ + q4 * 128 + ((g4 ^ (q4 & 2)) * 32) + pq * 8, vq = (r8 >> 1) * 49152 + (r8 & 1) * 1024 + (lane & 7) * 16, sq0, sq1, sq2;
  { const int ks = kg >> 2, k7 = ks >= 7; sq0 = WS_WQ + hpq * 196608 + k7 * (8192 - 896) + ks * 128 + (kg & 3) * 2048 + (((55 - kg) >> 31) & 0x80000000u); sq1 = sq0 + 16384; sq2 = sq0 + 32768; }
  asm volatile("" : "+v"(vN), "+v"(hw), "+v"(hw3), "+v"(th_), "+v"(tq), "+v"(vq)); asm volatile("" : "+s"(sN), "+s"(sH), "+s"(sFA), "+s"(sq0), "+s"(sq1), "+s"(sq2));
  WAITV4(3, xa, xb, xc, xd);
  STAMP(1);
  unsigned pr[14] = {xa[0], xa[1], xa[2], xa[3], xb[0], xb[1], xb[2], xb[3], xc[0], xc[1], xc[2], xc[3], xd[0], xd[1]};
  f32x2_t dd[14]; float dm, inv, la, lc;
  float qs_ = ln_a(pr, dd, dm); OPV(qs_);
  SB();
  ln_b(dd, qs_, dm, inv, la, lc); OPV(la);
  SB();
  ln_h(pr, dd, la, lc);
  *(LDS_AS u32x4*)hw = (u32x4){pr[0], pr[1], pr[2], pr[3]}; *(LDS_AS u32x4*)(hw + 256) = (u32x4){pr[4], pr[5], pr[6], pr[7]};
  *(LDS_AS u32x4*)(hw + 512) = (u32x4){pr[8], pr[9], pr[10], pr[11]}; *(LDS_AS u32x2*)hw3 = (u32x2){pr[12], pr[13]};
  SB();
  ln_x(pr, dd, inv);
  HS16(vN, sN, 0, ((u32x4){pr[0], pr[1], pr[2], pr[3]}));
  HS16(vN, sN, 256, ((u32x4){pr[4], pr[5], pr[6], pr[7]}));
  HS16(vN, sN, 512, ((u32x4){pr[8], pr[9], pr[10], pr[11]}));
  HS16M(0x00ff00ff, 0x00ff00ff, vN, sN, 768, ((u32x4){pr[12], pr[13], ror8(pr[12]), ror8(pr[13])}));
  unsigned nonce; asm volatile("s_waitcnt lgkmcnt(0)\n\ts_add_u32 %0, %1, 1" : "=s"(nonce) : "s"(nonce0) : "memory", "scc");
  asm volatile("s_waitcnt vmcnt(4)"
      :  :  : "memory"); SB();
  const bf16x8 q0 = tr8u(tq, 512), q1 = tr8u(tq + 1024, 512), q2 = tr8u(tq + 2048, 512);
  HS16H(0xffffff, vq, sq0, 0, __builtin_bit_cast(u32x4, q0));
  HS16H(0xffffff, vq, sq1, 0, __builtin_bit_cast(u32x4, q1));
  HS16H(0xffffff, vq, sq2, 0, __builtin_bit_cast(u32x4, q2));
  BAR();
  STAMP(2);
  { const bf16x8 h0 = tr8u(th_, 4 * TP), h1 = tr8u(th_ + 16, 4 * TP), h2 = tr8u(th_ + 8 * TP, 4 * TP), h3 = tr8u(th_ + 8 * TP + 16, 4 * TP);
    HS16H(0xffffff, vL16, sH, 0, __builtin_bit_cast(u32x4, h0));
    HS16H(0xffffff, vL16, sH, 1024, __builtin_bit_cast(u32x4, h1));
    HS16H(0xffffff, vL16, sH, 2048, __builtin_bit_cast(u32x4, h2));
    HS16H(0xffffff, vL16, sH, 3072, __builtin_bit_cast(u32x4, h3));
  }
  STAMP(3);
  unsigned fw, fa, vFW, vFA;
  { const int k_ = lane < 56 ? lane : 55; vFW = WS_FA + (2048 * 16) + ((hp & 3) * 128 + 64 * (k_ >> 5) + 32 * (1 - hq) + (k_ & 31)) * (4 * 16); }
  { const int k_ = 8 * tt - 1 + (n16 < 9 ? n16 : 8); vFA = WS_FA + (2048 * 16) + (g4 * 128 + (k_ < 0 ? 0 : k_)) * (4 * 16); }
  unsigned vQ = w == 3 && (r8 & 1) ? 0x40000000u : vF      , sq[6];
#pragma unroll
  for (int t = 0; t < 6; ++t) { sq[t] = WS_WQ + ((hp * 4 + w) * 6 + t) * 8192; OPQ(sq[t]); }
  unsigned vXN = r8 * 1024 + ((lane & 7) ^ (4 * (w & 1) + (r8 >> 1))) * 16, sXa = WS_XN + (128 * tt - 16 + 8 * w) * 1024, sXb = sXa + (w >= 2 ? 128 - 147456 : 0);
  unsigned sXc = w >= 2 ? 0x80000000u : sXa, ldsX = lds0 + w * 1024, sXt;
  unsigned xr0 = lds0 + n16 * 128 + ((g4 ^ (n16 >> 1)) * 16), xr1 = xr0 ^ 64;
  unsigned sk0, sk1, sv, rk0, rk1, rv0, rv1, vS, sO, sFO = WS_FO + ((tt * 4 + w) * 8 + hp) * (4 * 16);
  { const int c = 16 * w + 4 * g4;
    const bool ok = c < 56;
    const int hd = ok ? (c >= 28) : (g4 & 1), d = ok ? c - 28 * hd : 28, row = (n16 >> 1) + 72 * (n16 & 1);
    sk0 = lds0 + hd * 9216 + row * 64 + (((d >> 3) ^ (n16 >> 3) ^ (2 * (n16 & 1))) << 4) + ((d >> 2) & 1) * 8; sk1 = sk0 ^ 32;
    sv = lds0 + (2 * hd + (d >> 4)) * 4608 + row * 32 + ((((d >> 2) & 3) ^ (n16 >> 3) ^ (2 * (n16 & 1))) << 3);
    rk0 = lds0 + (16 * w + n16) * 64 + ((g4 ^ q4) << 4); rk1 = (rk0 ^ 32) + 4608;
    rv0 = lds0 + (16 * w + 4 * g4 + q4) * 32 + (pq ^ (g4 & 1)) * 8; rv1 = (rv0 ^ 16) + 2304;
    const int ga = 7 * (hp & 3) + g4;
    vS = (n16 >> 3) * 16384 + (ga & 3) * 2048 + (ga >> 2) * 128 + (n16 & 7) * 16; sO = WS_OT + (8 * tt + 2 * w) * 16384 + (hp >> 2) * 8192; }
  bf16x8 wq[14][3];
  asm volatile("" : "+v"(vFW), "+v"(vFA), "+v"(vQ), "+v"(vXN), "+v"(xr0), "+v"(xr1)); asm volatile("" : "+s"(sXa), "+s"(sXb), "+s"(sXc), "+s"(ldsX));
  asm volatile("" : "+v"(sk0), "+v"(sk1), "+v"(sv), "+v"(rk0), "+v"(rk1), "+v"(rv0), "+v"(rv1), "+v"(vS)); asm volatile("" : "+s"(sO), "+s"(sFO));
  asm volatile("s_waitcnt vmcnt(4)"
      :  :  : "memory"); SB();
  STFA(sFA + (2048 * 16));
  STAMP(28);
  u32x4 rs2 = srd(w2, 1792u * 4096u); OPQ(rs2);
  unsigned vW2[4];
#pragma unroll
  for (int i = 0; i < 4; ++i) vW2[i] = (8 * (r8 & 3) + 4 * (r8 >> 2)) * 4096 + st * 128 + ((lane & 7) ^ (2 * i)) * 16 + i * (4096 - 1024);
  int rot2 = 4 * dt + 2 * (st & 7) + 0 * (b & 1); rot2 -= 14 * ((rot2 * 37) >> 9);
  bf16x8 fw2[14][4];
  unsigned ldsS = lds0 + S3B + w * 4096, ldsO = lds0 + WOB + w * 8192, ldsP = lds0 + WOB + 24576 + w * 6144, sWo, vWo = r8 * 16384 + (lane & 7) * 16, sWt;
  { const int k_ = BLK(w), k7 = (k_ * 37) >> 8; sWo = WS_WO + k7 * (131072 - 896) + k_ * 128; }
  unsigned fq, vFQ = WS_FQ + (lane < 56 ? lane : 55) * (4 * 4);
  asm volatile("" : "+v"(vW2[0]), "+v"(vW2[1]), "+v"(vW2[2]), "+v"(vW2[3]), "+v"(vFQ), "+v"(vWo)); asm volatile("" : "+s"(rot2), "+s"(ldsS), "+s"(ldsO), "+s"(ldsP), "+s"(sWo));
  unsigned tS[4];
#pragma unroll
  for (int i = 0; i < 4; ++i) { tS[i] = ldsS + q4 * 1024 + g4 * 128 + pq * 8 + ((i ^ q4) * 32); OPV(tS[i]); }
  unsigned tO[4];
#pragma unroll
  for (int i = 0; i < 4; ++i) { tO[i] = ldsO + q4 * 1024 + g4 * 128 + pq * 8 + ((i ^ q4) * 32); OPV(tO[i]); }
  unsigned su[14], sw[14], sc_ = 14 * w + rot2; OPQ(sc_);
  unsigned ldsZ = lds0 + w * 4096; OPQ(ldsZ);
  unsigned tZ[4];
#pragma unroll
  for (int i = 0; i < 4; ++i) { tZ[i] = ldsZ + q4 * 1024 + g4 * 128 + pq * 8 + ((i ^ q4) * 32); OPV(tZ[i]); }
  const int dk = 4 * g4 - n16;
  f32x4 cm; unsigned m1[4], mp[2], mq_ = g4 == 3 ? 0u : ~0u;
  const float cz = tt + w == 0 && lane < 32 ? -998244352.f : 0.f;
#pragma unroll
  for (int i = 0; i < 4; ++i) { m1[i] = (unsigned)((dk + i + 7) >> 31); cm[i] = __uint_as_float(~m1[i] & __float_as_uint((unsigned)(dk + i - 1) < 8u ? cz : -__builtin_inff())); }
  mp[0] = (m1[0] & 0xffffu) | (m1[1] & 0xffff0000u); mp[1] = (m1[2] & 0xffffu) | (m1[3] & 0xffff0000u);
  asm volatile("" : "+v"(cm), "+v"(m1[0]), "+v"(m1[1]), "+v"(m1[2]), "+v"(m1[3]), "+v"(mp[0]), "+v"(mp[1]), "+v"(mq_));
  asm volatile("s_waitcnt vmcnt(1)"
      :  :  : "memory"); SB();
  STFA(sFA);
  STAMP(4);
  { const unsigned vT = lane < 14 ? ((jb * 4 + w) * 14 + lane) * 4096 + ((st & ~7) + ((3 - (int)((size_t)w2 >> 7)) & 7)) * 128 : 0x40000000u; u32x4 rs_ = srd(w2, 1792u * 4096u); TOUCH(vT, rs_, 0u, 0); }
  unsigned vFU, vFO;
  { const int da = (db0 * 37) >> 8, db = ((db0 + 3) * 37) >> 8, l_ = lane < 56 ? lane : 55;
    vFU = WS_FU + ((l_ < 28 ? da * 128 + l_ : db * 128 + l_ - 28) + w * 28) * (4 * 4);
    vFO = WS_FO + ((2 * st + (w >> 1)) * 8 + (lane & 7)) * (4 * 16);
    asm volatile("" : "+v"(vFU), "+v"(vFO)); }
  unsigned vFU1 = WS_FU + (512 + ((db0 * 37) >> 8) * 128 + w * 28 + (lane < 28 ? lane : 27)) * (4 * 4), fu2; OPV(vFU1);
  unsigned vFj[4], so0 = WS_OT + (4 * st + w) * 16384, so1 = so0 + 8192;
#pragma unroll
  for (int j = 0; j < 4; ++j) { const int k_ = BLK(j), k7 = (k_ * 37) >> 8; vFj[j] = vF + (WS_UT + k7 * (56 * 8192 - 896) + k_ * 128); OPV(vFj[j]); }
  asm volatile("" : "+s"(so0), "+s"(so1));
  const int tau = 2 * (n16 & 7) + (n16 >> 3);
  unsigned rwd = ldsS + vL16, rr = lds0 + S3B + w * 1024 + (16 * g4 + tau) * 16, ro = lds0 + WOB + vL16;
  unsigned xo = ldsS + 34816 + vL16, braw, xt = ldsS + 34816 + tau * 128 + (((g4 >> 1) ^ (n16 & 7)) * 16) + (g4 & 1) * 8, vB2 = (64 * st + 16 * w + tau) * 2;
  unsigned xs = xt ^ (32 * 0);
  asm volatile("" : "+v"(rwd), "+v"(rr), "+v"(ro), "+v"(xt), "+v"(xs), "+v"(xo), "+v"(vB2));
  unsigned vXa = (64 * st + 16 * w + r8) * 896 + db0 * 32 + ((lane & 7) ^ (r8 >> 1)) * 16, vXb = (vXa ^ 64) + (8 * 896 - 1024); OPV(vXa); OPV(vXb);
  { unsigned k_ = sc_ + (rot2 >= 14 ? -14 : 0); OPQ(k_); su[0] = k_ << 13; sw[0] = k_ << 17; OPQ(su[0]); OPQ(sw[0]); }
  { unsigned k_ = sc_ + (rot2 >= 13 ? -13 : 1); OPQ(k_); su[1] = k_ << 13; sw[1] = k_ << 17; OPQ(su[1]); OPQ(sw[1]); }
  { unsigned k_ = sc_ + (rot2 >= 12 ? -12 : 2); OPQ(k_); su[2] = k_ << 13; sw[2] = k_ << 17; OPQ(su[2]); OPQ(sw[2]); }
  { unsigned k_ = sc_ + (rot2 >= 11 ? -11 : 3); OPQ(k_); su[3] = k_ << 13; sw[3] = k_ << 17; OPQ(su[3]); OPQ(sw[3]); }
  { unsigned k_ = sc_ + (rot2 >= 10 ? -10 : 4); OPQ(k_); su[4] = k_ << 13; sw[4] = k_ << 17; OPQ(su[4]); OPQ(sw[4]); }
  { unsigned k_ = sc_ + (rot2 >= 9 ? -9 : 5); OPQ(k_); su[5] = k_ << 13; sw[5] = k_ << 17; OPQ(su[5]); OPQ(sw[5]); }
  { unsigned k_ = sc_ + (rot2 >= 8 ? -8 : 6); OPQ(k_); su[6] = k_ << 13; sw[6] = k_ << 17; OPQ(su[6]); OPQ(sw[6]); }
  { unsigned k_ = sc_ + (rot2 >= 7 ? -7 : 7); OPQ(k_); su[7] = k_ << 13; sw[7] = k_ << 17; OPQ(su[7]); OPQ(sw[7]); }
  { unsigned k_ = sc_ + (rot2 >= 6 ? -6 : 8); OPQ(k_); su[8] = k_ << 13; sw[8] = k_ << 17; OPQ(su[8]); OPQ(sw[8]); }
  { unsigned k_ = sc_ + (rot2 >= 5 ? -5 : 9); OPQ(k_); su[9] = k_ << 13; sw[9] = k_ << 17; OPQ(su[9]); OPQ(sw[9]); }
  { unsigned k_ = sc_ + (rot2 >= 4 ? -4 : 10); OPQ(k_); su[10] = k_ << 13; sw[10] = k_ << 17; OPQ(su[10]); OPQ(sw[10]); }
  { unsigned k_ = sc_ + (rot2 >= 3 ? -3 : 11); OPQ(k_); su[11] = k_ << 13; sw[11] = k_ << 17; OPQ(su[11]); OPQ(sw[11]); }
  { unsigned k_ = sc_ + (rot2 >= 2 ? -2 : 12); OPQ(k_); su[12] = k_ << 13; sw[12] = k_ << 17; OPQ(su[12]); OPQ(sw[12]); }
  { unsigned k_ = sc_ + (rot2 >= 1 ? -1 : 13); OPQ(k_); su[13] = k_ << 13; sw[13] = k_ << 17; OPQ(su[13]); OPQ(sw[13]); }
  unsigned sWq[4] = {sWo, sWo + 4096, sWo + 8192, sWo + 12288}, wwa = ldsO + vL16, wwb = ldsP + vL16; OPQ(sWq[1]); OPQ(sWq[2]); OPQ(sWq[3]); OPV(wwa); OPV(wwb);
  unsigned vT2 = (lane & 31) * 4096 + st * 128 + (lane < 32 ? sw[2] : sw[3]); OPV(vT2);
  u32x2 xv[4]; unsigned xta[4];
  u32x4 rsX = srd(x, 2048u * 896u); OPQ(rsX);
  asm volatile("s_add_u32 m0, %1, 34816\n\ts_nop 0\n\tbuffer_load_dwordx4 %2, %3, %4 offen offset:0  lds\n\tbuffer_load_dwordx4 %5, %3, %4 offen offset:1024  lds\n\tglobal_load_ushort %0, %6, %7 offset:0"
      : "=&v"(braw) : "s"(ldsS), "v"(vXa), "s"(rsX), "s"(0u), "v"(vXb), "v"(vB2), "s"(b2) : "memory", "scc"); SB();
  STAMP(5);
  asm volatile("buffer_load_dword %0, %2, %3, 0 offen sc1\n\tbuffer_load_dword %1, %4, %3, 0 offen sc1"
      : "=&v"(fw), "=&v"(fa) : "v"(vFW), "s"(rsW), "v"(vFA) : "memory"); SB();
  BAR();
  WAITV2(0, fw, fa);
  fa = (fa ^ nonce) | (fw ^ nonce) ? ~nonce : nonce; if (__ballot(fa != nonce) != 0ull) poll1<1>(rsW, vFA, vFW, nonce);
  STAMP(6);
  asm volatile("s_nop 0\n\ts_add_u32 m0, %1, 0\n\ts_add_u32 %0, %2, 0\n\tbuffer_load_dwordx4 %3, %4, %0 offen offset:0 sc1 lds\n\ts_add_u32 m0, %1, 4096\n\ts_add_u32 %0, %2, 32768\n\tbuffer_load_dwordx4 %3, %4, %0 offen offset:0 sc1 lds\n\ts_add_u32 m0, %1, 8192\n\ts_add_u32 %0, %2, 65536\n\tbuffer_load_dwordx4 %3, %4, %0 offen offset:0 sc1 lds\n\ts_add_u32 m0, %1, 12288\n\ts_add_u32 %0, %2, 98304\n\tbuffer_load_dwordx4 %3, %4, %0 offen offset:0 sc1 lds\n\ts_add_u32 m0, %1, 16384\n\ts_add_u32 %0, %5, 131072\n\tbuffer_load_dwordx4 %3, %4, %0 offen offset:0 sc1 lds"
      : "=&s"(sXt) : "s"(ldsX), "s"(sXa), "v"(vXN), "s"(rsW), "s"(sXb) : "memory", "scc"); SB();
  STAMP(7);
  asm volatile("buffer_load_dwordx4 %0, %10, %11, %12 offen offset:0 sc1\n\tbuffer_load_dwordx4 %1, %10, %11, %13 offen offset:0 sc1\n\tbuffer_load_dwordx4 %2, %10, %11, %14 offen offset:0 sc1\n\tbuffer_load_dwordx4 %3, %10, %11, %12 offen offset:128 sc1\n\tbuffer_load_dwordx4 %4, %10, %11, %13 offen offset:128 sc1\n\tbuffer_load_dwordx4 %5, %10, %11, %14 offen offset:128 sc1\n\ts_add_u32 m0, %15, 20480\n\ts_add_u32 %6, %16, 16512\n\tbuffer_load_dwordx4 %17, %11, %6 offen offset:0 sc1 lds\n\ts_add_u32 m0, %15, 24576\n\ts_add_u32 %6, %16, 49280\n\tbuffer_load_dwordx4 %17, %11, %6 offen offset:0 sc1 lds\n\ts_add_u32 m0, %15, 28672\n\ts_add_u32 %6, %16, 82048\n\tbuffer_load_dwordx4 %17, %11, %6 offen offset:0 sc1 lds\n\ts_add_u32 m0, %15, 32768\n\ts_add_u32 %6, %16, 114816\n\tbuffer_load_dwordx4 %17, %11, %6 offen offset:0 sc1 lds\n\tbuffer_load_dwordx4 %7, %10, %11, %12 offen offset:256 sc1\n\tbuffer_load_dwordx4 %8, %10, %11, %13 offen offset:256 sc1\n\tbuffer_load_dwordx4 %9, %10, %11, %14 offen offset:256 sc1"
      : "=&a"(wq[0][0]), "=&a"(wq[0][1]), "=&a"(wq[0][2]), "=&a"(wq[1][0]), "=&a"(wq[1][1]), "=&a"(wq[1][2]), "=&s"(sXt), "=&a"(wq[2][0]), "=&a"(wq[2][1]), "=&a"(wq[2][2]) : "v"(vQ), "s"(rsW), "s"(sq[0]), "s"(sq[2]), "s"(sq[4]), "s"(ldsX), "s"(sXa), "v"(vXN) : "memory", "scc"); SB();
  f32x4 aq[8], ak[9], av[9]; bf16x8 xf[14][9]; u32x2 sg[27];
  asm volatile("s_waitcnt vmcnt(13)"
      :  :  : "memory"); SB();
  BAR0();
  STAMP(8);
  asm volatile("ds_read_b128 %0, %9 offset:0\n\tds_read_b128 %1, %9 offset:2048\n\tds_read_b128 %2, %9 offset:4096\n\tds_read_b128 %3, %9 offset:6144\n\tds_read_b128 %4, %9 offset:8192\n\tds_read_b128 %5, %9 offset:10240\n\tds_read_b128 %6, %9 offset:12288\n\tds_read_b128 %7, %9 offset:14336\n\tds_read_b128 %8, %9 offset:16384"
      : "=&v"(xf[0][0]), "=&v"(xf[0][1]), "=&v"(xf[0][2]), "=&v"(xf[0][3]), "=&v"(xf[0][4]), "=&v"(xf[0][5]), "=&v"(xf[0][6]), "=&v"(xf[0][7]), "=&v"(xf[0][8]) : "v"(xr0) : "memory"); SB();
  WAITVB(10);
  WAITL(8);
  SB();
  asm volatile("v_mfma_f32_16x16x32_bf16 %0, %4, %5, 0\n\ts_add_u32 m0, %6, 36864\n\ts_add_u32 %1, %7, 256\n\tv_mfma_f32_16x16x32_bf16 %2, %8, %5, 0\n\tds_read_b128 %3, %9 offset:0"
      : "=&v"(ak[0]), "=&s"(sXt), "=&v"(av[0]), "=&v"(xf[1][0]) : "a"(wq[0][1]), "v"(xf[0][0]), "s"(ldsX), "s"(sXa), "a"(wq[0][2]), "v"(xr1) : "memory", "scc"); SB();
  WAITL(8);
  SB();
  asm volatile("v_mfma_f32_16x16x32_bf16 %0, %4, %5, 0\n\tbuffer_load_dwordx4 %6, %7, %8 offen offset:0 sc1 lds\n\tv_mfma_f32_16x16x32_bf16 %1, %9, %5, 0\n\tds_read_b128 %2, %10 offset:2048\n\tv_mfma_f32_16x16x32_bf16 %3, %11, %5, 0"
      : "=&v"(ak[1]), "=&v"(av[1]), "=&v"(xf[1][1]), "=&v"(aq[0]) : "a"(wq[0][1]), "v"(xf[0][1]), "v"(vXN), "s"(rsW), "s"(sXt), "a"(wq[0][2]), "v"(xr1), "a"(wq[0][0]) : "memory"); SB();
  WAITL(8);
  SB();
  asm volatile("v_mfma_f32_16x16x32_bf16 %0, %6, %7, 0\n\tds_read_b128 %1, %8 offset:4096\n\tv_mfma_f32_16x16x32_bf16 %2, %9, %7, 0\n\tbuffer_load_dwordx4 %3, %10, %11, %12 offen offset:384 sc1\n\tv_mfma_f32_16x16x32_bf16 %4, %13, %7, 0\n\tds_read_b128 %5, %8 offset:6144"
      : "=&v"(ak[2]), "=&v"(xf[1][2]), "=&v"(av[2]), "=&a"(wq[3][0]), "=&v"(aq[1]), "=&v"(xf[1][3]) : "a"(wq[0][1]), "v"(xf[0][2]), "v"(xr1), "a"(wq[0][2]), "v"(vQ), "s"(rsW), "s"(sq[0]), "a"(wq[0][0]) : "memory"); SB();
  WAITL(9);
  SB();
  asm volatile("v_mfma_f32_16x16x32_bf16 %0, %5, %6, 0\n\ts_add_u32 m0, %7, 40960\n\ts_add_u32 %1, %8, 33024\n\tv_mfma_f32_16x16x32_bf16 %2, %9, %6, 0\n\tds_read_b128 %3, %10 offset:8192\n\tv_mfma_f32_16x16x32_bf16 %4, %11, %6, 0"
      : "=&v"(ak[3]), "=&s"(sXt), "=&v"(av[3]), "=&v"(xf[1][4]), "=&v"(aq[2]) : "a"(wq[0][1]), "v"(xf[0][3]), "s"(ldsX), "s"(sXa), "a"(wq[0][2]), "v"(xr1), "a"(wq[0][0]) : "memory", "scc"); SB();
  WAITL(9);
  SB();
  asm volatile("v_mfma_f32_16x16x32_bf16 %0, %5, %6, 0\n\tds_read_b128 %1, %7 offset:10240\n\tv_mfma_f32_16x16x32_bf16 %2, %8, %6, 0\n\tbuffer_load_dwordx4 %9, %10, %11 offen offset:0 sc1 lds\n\tv_mfma_f32_16x16x32_bf16 %3, %12, %6, 0\n\tds_read_b128 %4, %7 offset:12288"
      : "=&v"(ak[4]), "=&v"(xf[1][5]), "=&v"(av[4]), "=&v"(aq[3]), "=&v"(xf[1][6]) : "a"(wq[0][1]), "v"(xf[0][4]), "v"(xr1), "a"(wq[0][2]), "v"(vXN), "s"(rsW), "s"(sXt), "a"(wq[0][0]) : "memory"); SB();
  WAITL(10);
  SB();
  asm volatile("v_mfma_f32_16x16x32_bf16 %0, %5, %6, 0\n\tbuffer_load_dwordx4 %1, %7, %8, %9 offen offset:384 sc1\n\tv_mfma_f32_16x16x32_bf16 %2, %10, %6, 0\n\tds_read_b128 %3, %11 offset:14336\n\tv_mfma_f32_16x16x32_bf16 %4, %12, %6, 0"
      : "=&v"(ak[5]), "=&a"(wq[3][1]), "=&v"(av[5]), "=&v"(xf[1][7]), "=&v"(aq[4]) : "a"(wq[0][1]), "v"(xf[0][5]), "v"(vQ), "s"(rsW), "s"(sq[2]), "a"(wq[0][2]), "v"(xr1), "a"(wq[0][0]) : "memory"); SB();
  WAITL(10);
  SB();
  asm volatile("v_mfma_f32_16x16x32_bf16 %0, %5, %6, 0\n\tds_read_b128 %1, %7 offset:16384\n\tv_mfma_f32_16x16x32_bf16 %2, %8, %6, 0\n\ts_add_u32 m0, %9, 45056\n\ts_add_u32 %3, %10, 65792\n\tv_mfma_f32_16x16x32_bf16 %4, %11, %6, 0"
      : "=&v"(ak[6]), "=&v"(xf[1][8]), "=&v"(av[6]), "=&s"(sXt), "=&v"(aq[5]) : "a"(wq[0][1]), "v"(xf[0][6]), "v"(xr1), "a"(wq[0][2]), "s"(ldsX), "s"(sXa), "a"(wq[0][0]) : "memory", "scc"); SB();
  WAITL(10);
  SB();
  asm volatile("v_mfma_f32_16x16x32_bf16 %0, %4, %5, 0\n\tbuffer_load_dwordx4 %6, %7, %8 offen offset:0 sc1 lds\n\tv_mfma_f32_16x16x32_bf16 %1, %9, %5, 0\n\tbuffer_load_dwordx4 %2, %10, %7, %11 offen offset:384 sc1\n\tv_mfma_f32_16x16x32_bf16 %3, %12, %5, 0"
      : "=&v"(ak[7]), "=&v"(av[7]), "=&a"(wq[3][2]), "=&v"(aq[6]) : "a"(wq[0][1]), "v"(xf[0][7]), "v"(vXN), "s"(rsW), "s"(sXt), "a"(wq[0][2]), "v"(vQ), "s"(sq[4]), "a"(wq[0][0]) : "memory"); SB();
  WAITL(9);
  SB();
  asm volatile("v_mfma_f32_16x16x32_bf16 %0, %4, %5, 0\n\tbuffer_load_dwordx4 %1, %6, %7, %8 offen offset:512 sc1\n\tv_mfma_f32_16x16x32_bf16 %2, %9, %5, 0\n\tv_mfma_f32_16x16x32_bf16 %3, %10, %5, 0"
      : "=&v"(ak[8]), "=&a"(wq[4][0]), "=&v"(av[8]), "=&v"(aq[7]) : "a"(wq[0][1]), "v"(xf[0][8]), "v"(vQ), "s"(rsW), "s"(sq[0]), "a"(wq[0][2]), "a"(wq[0][0]) : "memory"); SB();
  WAITVB(14);
  WAITL(3);
  SB();
  asm volatile("v_mfma_f32_16x16x32_bf16 %0, %1, %2, %0\n\ts_waitcnt vmcnt(10)"
      : "+v"(ak[0]) : "a"(wq[1][1]), "v"(xf[1][0]) : "memory"); SB();
  BAR0();
  SB();
  asm volatile("v_mfma_f32_16x16x32_bf16 %0, %11, %12, %0\n\tds_read_b128 %1, %13 offset:18432\n\tv_mfma_f32_16x16x32_bf16 %2, %14, %15, %2\n\ts_add_u32 m0, %16, 49152\n\ts_add_u32 %3, %17, 98560\n\tv_mfma_f32_16x16x32_bf16 %4, %11, %15, %4\n\tds_read_b128 %5, %13 offset:20480\n\tv_mfma_f32_16x16x32_bf16 %6, %18, %15, %6\n\tbuffer_load_dwordx4 %19, %20, %3 offen offset:0 sc1 lds\n\tv_mfma_f32_16x16x32_bf16 %7, %14, %21, %7\n\tds_read_b128 %8, %13 offset:22528\n\tv_mfma_f32_16x16x32_bf16 %9, %11, %21, %9\n\tbuffer_load_dwordx4 %10, %22, %20, %23 offen offset:512 sc1"
      : "+v"(av[0]), "=&v"(xf[2][0]), "+v"(ak[1]), "=&s"(sXt), "+v"(av[1]), "=&v"(xf[2][1]), "+v"(aq[0]), "+v"(ak[2]), "=&v"(xf[2][2]), "+v"(av[2]), "=&a"(wq[4][1]) : "a"(wq[1][2]), "v"(xf[1][0]), "v"(xr0), "a"(wq[1][1]), "v"(xf[1][1]), "s"(ldsX), "s"(sXa), "a"(wq[1][0]), "v"(vXN), "s"(rsW), "v"(xf[1][2]), "v"(vQ), "s"(sq[2]) : "memory", "scc"); SB();
  asm volatile("v_mfma_f32_16x16x32_bf16 %0, %11, %12, %0\n\tds_read_b128 %1, %13 offset:24576\n\tv_mfma_f32_16x16x32_bf16 %2, %14, %15, %2\n\ts_add_u32 m0, %16, 53248\n\ts_add_u32 %3, %17, 131328\n\tv_mfma_f32_16x16x32_bf16 %4, %18, %15, %4\n\tds_read_b128 %5, %13 offset:26624\n\tv_mfma_f32_16x16x32_bf16 %6, %11, %15, %6\n\tbuffer_load_dwordx4 %19, %20, %3 offen offset:0 sc1 lds\n\tv_mfma_f32_16x16x32_bf16 %7, %14, %21, %7\n\tds_read_b128 %8, %13 offset:28672\n\tv_mfma_f32_16x16x32_bf16 %9, %18, %21, %9\n\tbuffer_load_dwordx4 %10, %22, %20, %23 offen offset:512 sc1"
      : "+v"(aq[1]), "=&v"(xf[2][3]), "+v"(ak[3]), "=&s"(sXt), "+v"(av[3]), "=&v"(xf[2][4]), "+v"(aq[2]), "+v"(ak[4]), "=&v"(xf[2][5]), "+v"(av[4]), "=&a"(wq[4][2]) : "a"(wq[1][0]), "v"(xf[1][2]), "v"(xr0), "a"(wq[1][1]), "v"(xf[1][3]), "s"(ldsX), "s"(sXb), "a"(wq[1][2]), "v"(vXN), "s"(rsW), "v"(xf[1][4]), "v"(vQ), "s"(sq[4]) : "memory", "scc"); SB();
  asm volatile("v_mfma_f32_16x16x32_bf16 %0, %7, %8, %0\n\tds_read_b128 %1, %9 offset:30720\n\tv_mfma_f32_16x16x32_bf16 %2, %10, %11, %2\n\ts_add_u32 m0, %12, 57344\n\ts_add_u32 %3, %13, 16768\n\tv_mfma_f32_16x16x32_bf16 %4, %14, %11, %4\n\tds_read_b128 %5, %9 offset:32768\n\tv_mfma_f32_16x16x32_bf16 %6, %7, %11, %6"
      : "+v"(aq[3]), "=&v"(xf[2][6]), "+v"(ak[5]), "=&s"(sXt), "+v"(av[5]), "=&v"(xf[2][7]), "+v"(aq[4]) : "a"(wq[1][0]), "v"(xf[1][4]), "v"(xr0), "a"(wq[1][1]), "v"(xf[1][5]), "s"(ldsX), "s"(sXa), "a"(wq[1][2]) : "memory", "scc"); SB();
  WAITL(8);
  SB();
  asm volatile("v_mfma_f32_16x16x32_bf16 %0, %10, %11, %0\n\tds_read_b128 %1, %12 offset:34816\n\tv_mfma_f32_16x16x32_bf16 %2, %13, %11, %2\n\tbuffer_load_dwordx4 %14, %15, %16 offen offset:0 sc1 lds\n\tv_mfma_f32_16x16x32_bf16 %3, %17, %11, %3\n\tbuffer_load_dwordx4 %4, %18, %15, %19 offen offset:640 sc1\n\tv_mfma_f32_16x16x32_bf16 %5, %10, %20, %5\n\tbuffer_load_dwordx4 %6, %18, %15, %21 offen offset:640 sc1\n\tv_mfma_f32_16x16x32_bf16 %7, %13, %20, %7\n\tv_mfma_f32_16x16x32_bf16 %8, %17, %20, %8\n\tv_mfma_f32_16x16x32_bf16 %9, %10, %22, %9"
      : "+v"(ak[6]), "=&v"(xf[2][8]), "+v"(av[6]), "+v"(aq[5]), "=&a"(wq[5][0]), "+v"(ak[7]), "=&a"(wq[5][1]), "+v"(av[7]), "+v"(aq[6]), "+v"(ak[8]) : "a"(wq[1][1]), "v"(xf[1][6]), "v"(xr0), "a"(wq[1][2]), "v"(vXN), "s"(rsW), "s"(sXt), "a"(wq[1][0]), "v"(vQ), "s"(sq[0]), "v"(xf[1][7]), "s"(sq[2]), "v"(xf[1][8]) : "memory"); SB();
  asm volatile("v_mfma_f32_16x16x32_bf16 %0, %2, %3, %0\n\tv_mfma_f32_16x16x32_bf16 %1, %4, %3, %1"
      : "+v"(av[8]), "+v"(aq[7]) : "a"(wq[1][2]), "v"(xf[1][8]), "a"(wq[1][0])); SB();
  STAMP(30);
  WAITVB(14);
  WAITL(3);
  SB();
  asm volatile("v_mfma_f32_16x16x32_bf16 %0, %11, %12, %0\n\ts_add_u32 m0, %13, 61440\n\ts_add_u32 %1, %14, 49536\n\tv_mfma_f32_16x16x32_bf16 %2, %15, %12, %2\n\tds_read_b128 %3, %16 offset:18432\n\tv_mfma_f32_16x16x32_bf16 %4, %11, %17, %4\n\tbuffer_load_dwordx4 %18, %19, %1 offen offset:0 sc1 lds\n\tv_mfma_f32_16x16x32_bf16 %5, %15, %17, %5\n\tds_read_b128 %6, %16 offset:20480\n\tv_mfma_f32_16x16x32_bf16 %7, %20, %17, %7\n\tbuffer_load_dwordx4 %8, %21, %19, %22 offen offset:640 sc1\n\tv_mfma_f32_16x16x32_bf16 %9, %11, %23, %9\n\tds_read_b128 %10, %16 offset:22528"
      : "+v"(ak[0]), "=&s"(sXt), "+v"(av[0]), "=&v"(xf[3][0]), "+v"(ak[1]), "+v"(av[1]), "=&v"(xf[3][1]), "+v"(aq[0]), "=&a"(wq[5][2]), "+v"(ak[2]), "=&v"(xf[3][2]) : "a"(wq[2][1]), "v"(xf[2][0]), "s"(ldsX), "s"(sXa), "a"(wq[2][2]), "v"(xr1), "v"(xf[2][1]), "v"(vXN), "s"(rsW), "a"(wq[2][0]), "v"(vQ), "s"(sq[4]), "v"(xf[2][2]) : "memory", "scc"); SB();
  asm volatile("v_mfma_f32_16x16x32_bf16 %0, %11, %12, %0\n\ts_add_u32 m0, %13, 65536\n\ts_add_u32 %1, %14, 82304\n\tv_mfma_f32_16x16x32_bf16 %2, %15, %12, %2\n\tds_read_b128 %3, %16 offset:24576\n\tv_mfma_f32_16x16x32_bf16 %4, %17, %18, %4\n\tbuffer_load_dwordx4 %19, %20, %1 offen offset:0 sc1 lds\n\tv_mfma_f32_16x16x32_bf16 %5, %11, %18, %5\n\tds_read_b128 %6, %16 offset:26624\n\tv_mfma_f32_16x16x32_bf16 %7, %15, %18, %7\n\tbuffer_load_dwordx4 %8, %21, %20, %22 offen offset:768 sc1\n\tv_mfma_f32_16x16x32_bf16 %9, %17, %23, %9\n\tds_read_b128 %10, %16 offset:28672"
      : "+v"(av[2]), "=&s"(sXt), "+v"(aq[1]), "=&v"(xf[3][3]), "+v"(ak[3]), "+v"(av[3]), "=&v"(xf[3][4]), "+v"(aq[2]), "=&a"(wq[6][0]), "+v"(ak[4]), "=&v"(xf[3][5]) : "a"(wq[2][2]), "v"(xf[2][2]), "s"(ldsX), "s"(sXa), "a"(wq[2][0]), "v"(xr1), "a"(wq[2][1]), "v"(xf[2][3]), "v"(vXN), "s"(rsW), "v"(vQ), "s"(sq[0]), "v"(xf[2][4]) : "memory", "scc"); SB();
  asm volatile("v_mfma_f32_16x16x32_bf16 %0, %8, %9, %0\n\tbuffer_load_dwordx4 %1, %10, %11, %12 offen offset:768 sc1\n\tv_mfma_f32_16x16x32_bf16 %2, %13, %9, %2\n\tds_read_b128 %3, %14 offset:30720\n\tv_mfma_f32_16x16x32_bf16 %4, %15, %16, %4\n\tv_mfma_f32_16x16x32_bf16 %5, %8, %16, %5\n\tds_read_b128 %6, %14 offset:32768\n\tv_mfma_f32_16x16x32_bf16 %7, %13, %16, %7"
      : "+v"(av[4]), "=&a"(wq[6][1]), "+v"(aq[3]), "=&v"(xf[3][6]), "+v"(ak[5]), "+v"(av[5]), "=&v"(xf[3][7]), "+v"(aq[4]) : "a"(wq[2][2]), "v"(xf[2][4]), "v"(vQ), "s"(rsW), "s"(sq[2]), "a"(wq[2][0]), "v"(xr1), "a"(wq[2][1]), "v"(xf[2][5]) : "memory"); SB();
  WAITL(8);
  SB();
  asm volatile("v_mfma_f32_16x16x32_bf16 %0, %10, %11, %0\n\tds_read_b128 %1, %12 offset:34816\n\tv_mfma_f32_16x16x32_bf16 %2, %13, %11, %2\n\tv_mfma_f32_16x16x32_bf16 %3, %14, %11, %3\n\tv_mfma_f32_16x16x32_bf16 %4, %10, %15, %4\n\tv_mfma_f32_16x16x32_bf16 %5, %13, %15, %5\n\tv_mfma_f32_16x16x32_bf16 %6, %14, %15, %6\n\tv_mfma_f32_16x16x32_bf16 %7, %10, %16, %7\n\tv_mfma_f32_16x16x32_bf16 %8, %13, %16, %8\n\tv_mfma_f32_16x16x32_bf16 %9, %14, %16, %9"
      : "+v"(ak[6]), "=&v"(xf[3][8]), "+v"(av[6]), "+v"(aq[5]), "+v"(ak[7]), "+v"(av[7]), "+v"(aq[6]), "+v"(ak[8]), "+v"(av[8]), "+v"(aq[7]) : "a"(wq[2][1]), "v"(xf[2][6]), "v"(xr1), "a"(wq[2][2]), "a"(wq[2][0]), "v"(xf[2][7]), "v"(xf[2][8]) : "memory"); SB();
  WAITVB(13);
  WAITL(3);
  SB();
  asm volatile("v_mfma_f32_16x16x32_bf16 %0, %1, %2, %0\n\ts_waitcnt vmcnt(9)"
      : "+v"(ak[0]) : "a"(wq[3][1]), "v"(xf[3][0]) : "memory"); SB();
  BAR0();
  SB();
  asm volatile("v_mfma_f32_16x16x32_bf16 %0, %11, %12, %0\n\tds_read_b128 %1, %13 offset:36864\n\tv_mfma_f32_16x16x32_bf16 %2, %14, %15, %2\n\ts_add_u32 m0, %16, 69632\n\ts_add_u32 %3, %17, 115072\n\tv_mfma_f32_16x16x32_bf16 %4, %11, %15, %4\n\tds_read_b128 %5, %13 offset:38912\n\tv_mfma_f32_16x16x32_bf16 %6, %18, %15, %6\n\tbuffer_load_dwordx4 %19, %20, %3 offen offset:0 sc1 lds\n\tv_mfma_f32_16x16x32_bf16 %7, %14, %21, %7\n\tds_read_b128 %8, %13 offset:40960\n\tv_mfma_f32_16x16x32_bf16 %9, %11, %21, %9\n\tbuffer_load_dwordx4 %10, %22, %20, %23 offen offset:768 sc1"
      : "+v"(av[0]), "=&v"(xf[4][0]), "+v"(ak[1]), "=&s"(sXt), "+v"(av[1]), "=&v"(xf[4][1]), "+v"(aq[0]), "+v"(ak[2]), "=&v"(xf[4][2]), "+v"(av[2]), "=&a"(wq[6][2]) : "a"(wq[3][2]), "v"(xf[3][0]), "v"(xr0), "a"(wq[3][1]), "v"(xf[3][1]), "s"(ldsX), "s"(sXa), "a"(wq[3][0]), "v"(vXN), "s"(rsW), "v"(xf[3][2]), "v"(vQ), "s"(sq[4]) : "memory", "scc"); SB();
  asm volatile("v_mfma_f32_16x16x32_bf16 %0, %11, %12, %0\n\tds_read_b128 %1, %13 offset:43008\n\tv_mfma_f32_16x16x32_bf16 %2, %14, %15, %2\n\ts_add_u32 m0, %16, 73728\n\ts_add_u32 %3, %17, 512\n\tv_mfma_f32_16x16x32_bf16 %4, %18, %15, %4\n\tds_read_b128 %5, %13 offset:45056\n\tv_mfma_f32_16x16x32_bf16 %6, %11, %15, %6\n\tbuffer_load_dwordx4 %19, %20, %3 offen offset:0 sc1 lds\n\tv_mfma_f32_16x16x32_bf16 %7, %14, %21, %7\n\tds_read_b128 %8, %13 offset:47104\n\tv_mfma_f32_16x16x32_bf16 %9, %18, %21, %9\n\tbuffer_load_dwordx4 %10, %22, %20, %23 offen offset:0 sc1"
      : "+v"(aq[1]), "=&v"(xf[4][3]), "+v"(ak[3]), "=&s"(sXt), "+v"(av[3]), "=&v"(xf[4][4]), "+v"(aq[2]), "+v"(ak[4]), "=&v"(xf[4][5]), "+v"(av[4]), "=&a"(wq[7][0]) : "a"(wq[3][0]), "v"(xf[3][2]), "v"(xr0), "a"(wq[3][1]), "v"(xf[3][3]), "s"(ldsX), "s"(sXa), "a"(wq[3][2]), "v"(vXN), "s"(rsW), "v"(xf[3][4]), "v"(vQ), "s"(sq[1]) : "memory", "scc"); SB();
  asm volatile("v_mfma_f32_16x16x32_bf16 %0, %7, %8, %0\n\tds_read_b128 %1, %9 offset:49152\n\tv_mfma_f32_16x16x32_bf16 %2, %10, %11, %2\n\ts_add_u32 m0, %12, 77824\n\ts_add_u32 %3, %13, 33280\n\tv_mfma_f32_16x16x32_bf16 %4, %14, %11, %4\n\tds_read_b128 %5, %9 offset:51200\n\tv_mfma_f32_16x16x32_bf16 %6, %7, %11, %6"
      : "+v"(aq[3]), "=&v"(xf[4][6]), "+v"(ak[5]), "=&s"(sXt), "+v"(av[5]), "=&v"(xf[4][7]), "+v"(aq[4]) : "a"(wq[3][0]), "v"(xf[3][4]), "v"(xr0), "a"(wq[3][1]), "v"(xf[3][5]), "s"(ldsX), "s"(sXa), "a"(wq[3][2]) : "memory", "scc"); SB();
  WAITL(8);
  SB();
  asm volatile("v_mfma_f32_16x16x32_bf16 %0, %10, %11, %0\n\tds_read_b128 %1, %12 offset:53248\n\tv_mfma_f32_16x16x32_bf16 %2, %13, %11, %2\n\tbuffer_load_dwordx4 %14, %15, %16 offen offset:0 sc1 lds\n\tv_mfma_f32_16x16x32_bf16 %3, %17, %11, %3\n\tbuffer_load_dwordx4 %4, %18, %15, %19 offen offset:0 sc1\n\tv_mfma_f32_16x16x32_bf16 %5, %10, %20, %5\n\tbuffer_load_dwordx4 %6, %18, %15, %21 offen offset:0 sc1\n\tv_mfma_f32_16x16x32_bf16 %7, %13, %20, %7\n\tv_mfma_f32_16x16x32_bf16 %8, %17, %20, %8\n\tv_mfma_f32_16x16x32_bf16 %9, %10, %22, %9"
      : "+v"(ak[6]), "=&v"(xf[4][8]), "+v"(av[6]), "+v"(aq[5]), "=&a"(wq[7][1]), "+v"(ak[7]), "=&a"(wq[7][2]), "+v"(av[7]), "+v"(aq[6]), "+v"(ak[8]) : "a"(wq[3][1]), "v"(xf[3][6]), "v"(xr0), "a"(wq[3][2]), "v"(vXN), "s"(rsW), "s"(sXt), "a"(wq[3][0]), "v"(vQ), "s"(sq[3]), "v"(xf[3][7]), "s"(sq[5]), "v"(xf[3][8]) : "memory"); SB();
  asm volatile("v_mfma_f32_16x16x32_bf16 %0, %2, %3, %0\n\tv_mfma_f32_16x16x32_bf16 %1, %4, %3, %1"
      : "+v"(av[8]), "+v"(aq[7]) : "a"(wq[3][2]), "v"(xf[3][8]), "a"(wq[3][0])); SB();
  WAITVB(15);
  WAITL(3);
  SB();
  asm volatile("v_mfma_f32_16x16x32_bf16 %0, %11, %12, %0\n\ts_add_u32 m0, %13, 81920\n\ts_add_u32 %1, %14, 66048\n\tv_mfma_f32_16x16x32_bf16 %2, %15, %12, %2\n\tds_read_b128 %3, %16 offset:36864\n\tv_xor_b32 %4, 0, %17\n\tv_mfma_f32_16x16x32_bf16 %5, %11, %18, %5\n\tbuffer_load_dwordx4 %19, %20, %1 offen offset:0 sc1 lds\n\tv_mfma_f32_16x16x32_bf16 %6, %15, %18, %6\n\tds_read_b128 %7, %16 offset:38912\n\tv_xor_b32 %8, 32, %17\n\tv_mfma_f32_16x16x32_bf16 %9, %21, %18, %9\n\tbuffer_load_dwordx4 %10, %22, %20, %23 offen offset:128 sc1"
      : "+v"(ak[0]), "=&s"(sXt), "+v"(av[0]), "=&v"(xf[5][0]), "=&v"(xta[0]), "+v"(ak[1]), "+v"(av[1]), "=&v"(xf[5][1]), "=&v"(xta[1]), "+v"(aq[0]), "=&a"(wq[8][0]) : "a"(wq[4][1]), "v"(xf[4][0]), "s"(ldsX), "s"(sXa), "a"(wq[4][2]), "v"(xr1), "v"(xs), "v"(xf[4][1]), "v"(vXN), "s"(rsW), "a"(wq[4][0]), "v"(vQ), "s"(sq[1]) : "memory", "scc"); SB();
  asm volatile("v_mfma_f32_16x16x32_bf16 %0, %12, %13, %0\n\tds_read_b128 %1, %14 offset:40960\n\tv_xor_b32 %2, 64, %15\n\tv_mfma_f32_16x16x32_bf16 %3, %16, %13, %3\n\ts_add_u32 m0, %17, 86016\n\ts_add_u32 %4, %18, 98816\n\tv_mfma_f32_16x16x32_bf16 %5, %19, %13, %5\n\tds_read_b128 %6, %14 offset:43008\n\tv_xor_b32 %7, 96, %15\n\tv_mfma_f32_16x16x32_bf16 %8, %12, %20, %8\n\tbuffer_load_dwordx4 %21, %22, %4 offen offset:0 sc1 lds\n\tv_mfma_f32_16x16x32_bf16 %9, %16, %20, %9\n\tds_read_b128 %10, %14 offset:45056\n\tds_read_b64 %11, %23 offset:0"
      : "+v"(ak[2]), "=&v"(xf[5][2]), "=&v"(xta[2]), "+v"(av[2]), "=&s"(sXt), "+v"(aq[1]), "=&v"(xf[5][3]), "=&v"(xta[3]), "+v"(ak[3]), "+v"(av[3]), "=&v"(xf[5][4]), "=&v"(xv[0]) : "a"(wq[4][1]), "v"(xf[4][2]), "v"(xr1), "v"(xs), "a"(wq[4][2]), "s"(ldsX), "s"(sXa), "a"(wq[4][0]), "v"(xf[4][3]), "v"(vXN), "s"(rsW), "v"(xta[0]) : "memory", "scc"); SB();
  asm volatile("v_mfma_f32_16x16x32_bf16 %0, %11, %12, %0\n\tbuffer_load_dwordx4 %1, %13, %14, %15 offen offset:128 sc1\n\tv_mfma_f32_16x16x32_bf16 %2, %16, %17, %2\n\tds_read_b128 %3, %18 offset:47104\n\tds_read_b64 %4, %19 offset:0\n\tv_mfma_f32_16x16x32_bf16 %5, %20, %17, %5\n\tbuffer_load_dwordx4 %6, %13, %14, %21 offen offset:128 sc1\n\tv_mfma_f32_16x16x32_bf16 %7, %11, %17, %7\n\tds_read_b128 %8, %18 offset:49152\n\tds_read_b64 %9, %22 offset:0\n\tv_mfma_f32_16x16x32_bf16 %10, %16, %23, %10"
      : "+v"(aq[2]), "=&a"(wq[8][1]), "+v"(ak[4]), "=&v"(xf[5][5]), "=&v"(xv[1]), "+v"(av[4]), "=&a"(wq[8][2]), "+v"(aq[3]), "=&v"(xf[5][6]), "=&v"(xv[2]), "+v"(ak[5]) : "a"(wq[4][0]), "v"(xf[4][3]), "v"(vQ), "s"(rsW), "s"(sq[3]), "a"(wq[4][1]), "v"(xf[4][4]), "v"(xr1), "v"(xta[1]), "a"(wq[4][2]), "s"(sq[5]), "v"(xta[2]), "v"(xf[4][5]) : "memory"); SB();
  asm volatile("buffer_load_dwordx4 %0, %5, %6, %7 offen offset:256 sc1\n\tv_mfma_f32_16x16x32_bf16 %1, %8, %9, %1\n\tds_read_b128 %2, %10 offset:51200\n\tds_read_b64 %3, %11 offset:0\n\tv_mfma_f32_16x16x32_bf16 %4, %12, %9, %4"
      : "=&a"(wq[9][0]), "+v"(av[5]), "=&v"(xf[5][7]), "=&v"(xv[3]), "+v"(aq[4]) : "v"(vQ), "s"(rsW), "s"(sq[1]), "a"(wq[4][2]), "v"(xf[4][5]), "v"(xr1), "v"(xta[3]), "a"(wq[4][0]) : "memory"); SB();
  WAITL(12);
  SB();
  asm volatile("v_mfma_f32_16x16x32_bf16 %0, %10, %11, %0\n\tds_read_b128 %1, %12 offset:53248\n\tv_mfma_f32_16x16x32_bf16 %2, %13, %11, %2\n\tv_mfma_f32_16x16x32_bf16 %3, %14, %11, %3\n\tv_mfma_f32_16x16x32_bf16 %4, %10, %15, %4\n\tv_mfma_f32_16x16x32_bf16 %5, %13, %15, %5\n\tv_mfma_f32_16x16x32_bf16 %6, %14, %15, %6\n\tv_mfma_f32_16x16x32_bf16 %7, %10, %16, %7\n\tv_mfma_f32_16x16x32_bf16 %8, %13, %16, %8\n\tv_mfma_f32_16x16x32_bf16 %9, %14, %16, %9"
      : "+v"(ak[6]), "=&v"(xf[5][8]), "+v"(av[6]), "+v"(aq[5]), "+v"(ak[7]), "+v"(av[7]), "+v"(aq[6]), "+v"(ak[8]), "+v"(av[8]), "+v"(aq[7]) : "a"(wq[4][1]), "v"(xf[4][6]), "v"(xr1), "a"(wq[4][2]), "a"(wq[4][0]), "v"(xf[4][7]), "v"(xf[4][8]) : "memory"); SB();
  WAITVB(16);
  WAITL(6);
  SB();
  asm volatile("v_mfma_f32_16x16x32_bf16 %0, %3, %4, %0\n\tv_add_u32 %1, 55296, %1\n\tv_mfma_f32_16x16x32_bf16 %2, %5, %4, %2\n\ts_waitcnt vmcnt(12)"
      : "+v"(ak[0]), "+v"(xr0), "+v"(av[0]) : "a"(wq[5][1]), "v"(xf[5][0]), "a"(wq[5][2]) : "memory"); SB();
  BAR0();
  SB();
  asm volatile("v_mfma_f32_16x16x32_bf16 %0, %12, %13, %0\n\tds_read_b128 %1, %14 offset:0\n\tv_mfma_f32_16x16x32_bf16 %2, %15, %13, %2\n\tds_read_b128 %3, %14 offset:2048\n\tv_mfma_f32_16x16x32_bf16 %4, %16, %13, %4\n\ts_add_u32 m0, %17, 90112\n\ts_add_u32 %5, %18, 131584\n\tv_mfma_f32_16x16x32_bf16 %6, %12, %19, %6\n\tds_read_b128 %7, %14 offset:4096\n\tv_mfma_f32_16x16x32_bf16 %8, %15, %19, %8\n\tbuffer_load_dwordx4 %20, %21, %5 offen offset:0 sc1 lds\n\tv_mfma_f32_16x16x32_bf16 %9, %16, %19, %9\n\tds_read_b128 %10, %14 offset:6144\n\tv_mfma_f32_16x16x32_bf16 %11, %12, %22, %11"
      : "+v"(ak[1]), "=&v"(xf[6][0]), "+v"(av[1]), "=&v"(xf[6][1]), "+v"(aq[0]), "=&s"(sXt), "+v"(ak[2]), "=&v"(xf[6][2]), "+v"(av[2]), "+v"(aq[1]), "=&v"(xf[6][3]), "+v"(ak[3]) : "a"(wq[5][1]), "v"(xf[5][1]), "v"(xr0), "a"(wq[5][2]), "a"(wq[5][0]), "s"(ldsX), "s"(sXb), "v"(xf[5][2]), "v"(vXN), "s"(rsW), "v"(xf[5][3]) : "memory", "scc"); SB();
  asm volatile("buffer_load_dwordx4 %0, %11, %12, %13 offen offset:256 sc1\n\tv_mfma_f32_16x16x32_bf16 %1, %14, %15, %1\n\tds_read_b128 %2, %16 offset:8192\n\tv_mfma_f32_16x16x32_bf16 %3, %17, %15, %3\n\ts_add_u32 m0, %18, 94208\n\ts_add_u32 %4, %19, 17024\n\tv_mfma_f32_16x16x32_bf16 %5, %20, %21, %5\n\tds_read_b128 %6, %16 offset:10240\n\tv_mfma_f32_16x16x32_bf16 %7, %14, %21, %7\n\tbuffer_load_dwordx4 %22, %12, %4 offen offset:0 sc1 lds\n\tv_mfma_f32_16x16x32_bf16 %8, %17, %21, %8\n\tds_read_b128 %9, %16 offset:12288\n\tv_mfma_f32_16x16x32_bf16 %10, %20, %23, %10"
      : "=&a"(wq[9][1]), "+v"(av[3]), "=&v"(xf[6][4]), "+v"(aq[2]), "=&s"(sXt), "+v"(ak[4]), "=&v"(xf[6][5]), "+v"(av[4]), "+v"(aq[3]), "=&v"(xf[6][6]), "+v"(ak[5]) : "v"(vQ), "s"(rsW), "s"(sq[3]), "a"(wq[5][2]), "v"(xf[5][3]), "v"(xr0), "a"(wq[5][0]), "s"(ldsX), "s"(sXa), "a"(wq[5][1]), "v"(xf[5][4]), "v"(vXN), "v"(xf[5][5]) : "memory", "scc"); SB();
  asm volatile("buffer_load_dwordx4 %0, %4, %5, %6 offen offset:256 sc1\n\tv_mfma_f32_16x16x32_bf16 %1, %7, %8, %1\n\tds_read_b128 %2, %9 offset:14336\n\tv_mfma_f32_16x16x32_bf16 %3, %10, %8, %3"
      : "=&a"(wq[9][2]), "+v"(av[5]), "=&v"(xf[6][7]), "+v"(aq[4]) : "v"(vQ), "s"(rsW), "s"(sq[5]), "a"(wq[5][2]), "v"(xf[5][5]), "v"(xr0), "a"(wq[5][0]) : "memory"); SB();
  WAITL(8);
  SB();
  asm volatile("v_mfma_f32_16x16x32_bf16 %0, %10, %11, %0\n\tds_read_b128 %1, %12 offset:16384\n\tv_mfma_f32_16x16x32_bf16 %2, %13, %11, %2\n\ts_add_u32 m0, %14, 98304\n\ts_add_u32 %3, %15, 49792\n\tv_mfma_f32_16x16x32_bf16 %4, %16, %11, %4\n\tbuffer_load_dwordx4 %17, %18, %3 offen offset:0 sc1 lds\n\tv_mfma_f32_16x16x32_bf16 %5, %10, %19, %5\n\tbuffer_load_dwordx4 %6, %20, %18, %21 offen offset:384 sc1\n\tv_mfma_f32_16x16x32_bf16 %7, %13, %19, %7\n\tv_mfma_f32_16x16x32_bf16 %8, %16, %19, %8\n\tv_mfma_f32_16x16x32_bf16 %9, %10, %22, %9"
      : "+v"(ak[6]), "=&v"(xf[6][8]), "+v"(av[6]), "=&s"(sXt), "+v"(aq[5]), "+v"(ak[7]), "=&a"(wq[10][0]), "+v"(av[7]), "+v"(aq[6]), "+v"(ak[8]) : "a"(wq[5][1]), "v"(xf[5][6]), "v"(xr0), "a"(wq[5][2]), "s"(ldsX), "s"(sXa), "a"(wq[5][0]), "v"(vXN), "s"(rsW), "v"(xf[5][7]), "v"(vQ), "s"(sq[1]), "v"(xf[5][8]) : "memory", "scc"); SB();
  asm volatile("v_mfma_f32_16x16x32_bf16 %0, %2, %3, %0\n\tv_mfma_f32_16x16x32_bf16 %1, %4, %3, %1"
      : "+v"(av[8]), "+v"(aq[7]) : "a"(wq[5][2]), "v"(xf[5][8]), "a"(wq[5][0])); SB();
  STAMP(31);
  WAITVB(17);
  WAITL(3);
  SB();
  asm volatile("v_mfma_f32_16x16x32_bf16 %0, %12, %13, %0\n\tv_add_u32 %1, 55296, %1\n\tv_mfma_f32_16x16x32_bf16 %2, %14, %13, %2\n\tds_read_b128 %3, %1 offset:0\n\tv_mfma_f32_16x16x32_bf16 %4, %12, %15, %4\n\ts_add_u32 m0, %16, 102400\n\ts_add_u32 %5, %17, 82560\n\tv_mfma_f32_16x16x32_bf16 %6, %14, %15, %6\n\tds_read_b128 %7, %1 offset:2048\n\tv_mfma_f32_16x16x32_bf16 %8, %18, %15, %8\n\tbuffer_load_dwordx4 %19, %20, %5 offen offset:0 sc1 lds\n\tv_mfma_f32_16x16x32_bf16 %9, %12, %21, %9\n\tds_read_b128 %10, %1 offset:4096\n\tv_mfma_f32_16x16x32_bf16 %11, %14, %21, %11"
      : "+v"(ak[0]), "+v"(xr1), "+v"(av[0]), "=&v"(xf[7][0]), "+v"(ak[1]), "=&s"(sXt), "+v"(av[1]), "=&v"(xf[7][1]), "+v"(aq[0]), "+v"(ak[2]), "=&v"(xf[7][2]), "+v"(av[2]) : "a"(wq[6][1]), "v"(xf[6][0]), "a"(wq[6][2]), "v"(xf[6][1]), "s"(ldsX), "s"(sXa), "a"(wq[6][0]), "v"(vXN), "s"(rsW), "v"(xf[6][2]) : "memory", "scc"); SB();
  asm volatile("buffer_load_dwordx4 %0, %11, %12, %13 offen offset:384 sc1\n\tv_mfma_f32_16x16x32_bf16 %1, %14, %15, %1\n\tds_read_b128 %2, %16 offset:6144\n\tv_mfma_f32_16x16x32_bf16 %3, %17, %18, %3\n\ts_add_u32 m0, %19, 106496\n\ts_add_u32 %4, %20, 115328\n\tv_mfma_f32_16x16x32_bf16 %5, %21, %18, %5\n\tds_read_b128 %6, %16 offset:8192\n\tv_mfma_f32_16x16x32_bf16 %7, %14, %18, %7\n\tbuffer_load_dwordx4 %22, %12, %4 offen offset:0 sc1 lds\n\tv_mfma_f32_16x16x32_bf16 %8, %17, %23, %8\n\tds_read_b128 %9, %16 offset:10240\n\tv_mfma_f32_16x16x32_bf16 %10, %21, %23, %10"
      : "=&a"(wq[10][1]), "+v"(aq[1]), "=&v"(xf[7][3]), "+v"(ak[3]), "=&s"(sXt), "+v"(av[3]), "=&v"(xf[7][4]), "+v"(aq[2]), "+v"(ak[4]), "=&v"(xf[7][5]), "+v"(av[4]) : "v"(vQ), "s"(rsW), "s"(sq[3]), "a"(wq[6][0]), "v"(xf[6][2]), "v"(xr1), "a"(wq[6][1]), "v"(xf[6][3]), "s"(ldsX), "s"(sXa), "a"(wq[6][2]), "v"(vXN), "v"(xf[6][4]) : "memory", "scc"); SB();
  asm volatile("buffer_load_dwordx4 %0, %8, %9, %10 offen offset:384 sc1\n\tv_mfma_f32_16x16x32_bf16 %1, %11, %12, %1\n\tds_read_b128 %2, %13 offset:12288\n\tv_mfma_f32_16x16x32_bf16 %3, %14, %15, %3\n\tbuffer_load_dwordx4 %4, %8, %9, %16 offen offset:512 sc1\n\tv_mfma_f32_16x16x32_bf16 %5, %17, %15, %5\n\tds_read_b128 %6, %13 offset:14336\n\tv_mfma_f32_16x16x32_bf16 %7, %11, %15, %7"
      : "=&a"(wq[10][2]), "+v"(aq[3]), "=&v"(xf[7][6]), "+v"(ak[5]), "=&a"(wq[11][0]), "+v"(av[5]), "=&v"(xf[7][7]), "+v"(aq[4]) : "v"(vQ), "s"(rsW), "s"(sq[5]), "a"(wq[6][0]), "v"(xf[6][4]), "v"(xr1), "a"(wq[6][1]), "v"(xf[6][5]), "s"(sq[1]), "a"(wq[6][2]) : "memory"); SB();
  WAITL(8);
  SB();
  asm volatile("v_mfma_f32_16x16x32_bf16 %0, %11, %12, %0\n\tds_read_b128 %1, %13 offset:16384\n\tv_mfma_f32_16x16x32_bf16 %2, %14, %12, %2\n\tbuffer_load_dwordx4 %3, %15, %16, %17 offen offset:512 sc1\n\tv_mfma_f32_16x16x32_bf16 %4, %18, %12, %4\n\tv_mfma_f32_16x16x32_bf16 %5, %11, %19, %5\n\tv_mfma_f32_16x16x32_bf16 %6, %14, %19, %6\n\tv_mfma_f32_16x16x32_bf16 %7, %18, %19, %7\n\tv_mfma_f32_16x16x32_bf16 %8, %11, %20, %8\n\tv_mfma_f32_16x16x32_bf16 %9, %14, %20, %9\n\tv_mfma_f32_16x16x32_bf16 %10, %18, %20, %10"
      : "+v"(ak[6]), "=&v"(xf[7][8]), "+v"(av[6]), "=&a"(wq[11][1]), "+v"(aq[5]), "+v"(ak[7]), "+v"(av[7]), "+v"(aq[6]), "+v"(ak[8]), "+v"(av[8]), "+v"(aq[7]) : "a"(wq[6][1]), "v"(xf[6][6]), "v"(xr1), "a"(wq[6][2]), "v"(vQ), "s"(rsW), "s"(sq[3]), "a"(wq[6][0]), "v"(xf[6][7]), "v"(xf[6][8]) : "memory"); SB();
  WAITVB(18);
  WAITL(3);
  SB();
  asm volatile("v_mfma_f32_16x16x32_bf16 %0, %1, %2, %0\n\ts_waitcnt vmcnt(11)"
      : "+v"(ak[0]) : "a"(wq[7][1]), "v"(xf[7][0]) : "memory"); SB();
  BAR0();
  SB();
  asm volatile("v_mfma_f32_16x16x32_bf16 %0, %11, %12, %0\n\tds_read_b128 %1, %13 offset:18432\n\tv_mfma_f32_16x16x32_bf16 %2, %14, %15, %2\n\ts_add_u32 m0, %16, 110592\n\ts_add_u32 %3, %17, 768\n\tv_mfma_f32_16x16x32_bf16 %4, %11, %15, %4\n\tds_read_b128 %5, %13 offset:20480\n\tv_mfma_f32_16x16x32_bf16 %6, %18, %15, %6\n\tbuffer_load_dwordx4 %19, %20, %3 offen offset:0 sc1 lds\n\tv_mfma_f32_16x16x32_bf16 %7, %14, %21, %7\n\tds_read_b128 %8, %13 offset:22528\n\tv_mfma_f32_16x16x32_bf16 %9, %11, %21, %9\n\tbuffer_load_dwordx4 %10, %22, %20, %23 offen offset:512 sc1"
      : "+v"(av[0]), "=&v"(xf[8][0]), "+v"(ak[1]), "=&s"(sXt), "+v"(av[1]), "=&v"(xf[8][1]), "+v"(aq[0]), "+v"(ak[2]), "=&v"(xf[8][2]), "+v"(av[2]), "=&a"(wq[11][2]) : "a"(wq[7][2]), "v"(xf[7][0]), "v"(xr0), "a"(wq[7][1]), "v"(xf[7][1]), "s"(ldsX), "s"(sXa), "a"(wq[7][0]), "v"(vXN), "s"(rsW), "v"(xf[7][2]), "v"(vQ), "s"(sq[5]) : "memory", "scc"); SB();
  asm volatile("v_mfma_f32_16x16x32_bf16 %0, %11, %12, %0\n\tds_read_b128 %1, %13 offset:24576\n\tv_mfma_f32_16x16x32_bf16 %2, %14, %15, %2\n\ts_add_u32 m0, %16, 114688\n\ts_add_u32 %3, %17, 33536\n\tv_mfma_f32_16x16x32_bf16 %4, %18, %15, %4\n\tds_read_b128 %5, %13 offset:26624\n\tv_mfma_f32_16x16x32_bf16 %6, %11, %15, %6\n\tbuffer_load_dwordx4 %19, %20, %3 offen offset:0 sc1 lds\n\tv_mfma_f32_16x16x32_bf16 %7, %14, %21, %7\n\tds_read_b128 %8, %13 offset:28672\n\tv_mfma_f32_16x16x32_bf16 %9, %18, %21, %9\n\tbuffer_load_dwordx4 %10, %22, %20, %23 offen offset:640 sc1"
      : "+v"(aq[1]), "=&v"(xf[8][3]), "+v"(ak[3]), "=&s"(sXt), "+v"(av[3]), "=&v"(xf[8][4]), "+v"(aq[2]), "+v"(ak[4]), "=&v"(xf[8][5]), "+v"(av[4]), "=&a"(wq[12][0]) : "a"(wq[7][0]), "v"(xf[7][2]), "v"(xr0), "a"(wq[7][1]), "v"(xf[7][3]), "s"(ldsX), "s"(sXa), "a"(wq[7][2]), "v"(vXN), "s"(rsW), "v"(xf[7][4]), "v"(vQ), "s"(sq[1]) : "memory", "scc"); SB();
  asm volatile("v_mfma_f32_16x16x32_bf16 %0, %7, %8, %0\n\tds_read_b128 %1, %9 offset:30720\n\tv_mfma_f32_16x16x32_bf16 %2, %10, %11, %2\n\ts_add_u32 m0, %12, 118784\n\ts_add_u32 %3, %13, 66304\n\tv_mfma_f32_16x16x32_bf16 %4, %14, %11, %4\n\tds_read_b128 %5, %9 offset:32768\n\tv_mfma_f32_16x16x32_bf16 %6, %7, %11, %6"
      : "+v"(aq[3]), "=&v"(xf[8][6]), "+v"(ak[5]), "=&s"(sXt), "+v"(av[5]), "=&v"(xf[8][7]), "+v"(aq[4]) : "a"(wq[7][0]), "v"(xf[7][4]), "v"(xr0), "a"(wq[7][1]), "v"(xf[7][5]), "s"(ldsX), "s"(sXa), "a"(wq[7][2]) : "memory", "scc"); SB();
  WAITL(8);
  SB();
  asm volatile("v_mfma_f32_16x16x32_bf16 %0, %10, %11, %0\n\tds_read_b128 %1, %12 offset:34816\n\tv_mfma_f32_16x16x32_bf16 %2, %13, %11, %2\n\tbuffer_load_dwordx4 %14, %15, %16 offen offset:0 sc1 lds\n\tv_mfma_f32_16x16x32_bf16 %3, %17, %11, %3\n\tbuffer_load_dwordx4 %4, %18, %15, %19 offen offset:640 sc1\n\tv_mfma_f32_16x16x32_bf16 %5, %10, %20, %5\n\tbuffer_load_dwordx4 %6, %18, %15, %21 offen offset:640 sc1\n\tv_mfma_f32_16x16x32_bf16 %7, %13, %20, %7\n\tbuffer_load_dword %8, %22, %15, 0 offen sc1\n\tv_mfma_f32_16x16x32_bf16 %9, %17, %20, %9"
      : "+v"(ak[6]), "=&v"(xf[8][8]), "+v"(av[6]), "+v"(aq[5]), "=&a"(wq[12][1]), "+v"(ak[7]), "=&a"(wq[12][2]), "+v"(av[7]), "=&v"(fq), "+v"(aq[6]) : "a"(wq[7][1]), "v"(xf[7][6]), "v"(xr0), "a"(wq[7][2]), "v"(vXN), "s"(rsW), "s"(sXt), "a"(wq[7][0]), "v"(vQ), "s"(sq[3]), "v"(xf[7][7]), "s"(sq[5]), "v"(vFQ) : "memory"); SB();
  asm volatile("v_mfma_f32_16x16x32_bf16 %0, %3, %4, %0\n\tv_mfma_f32_16x16x32_bf16 %1, %5, %4, %1\n\tv_mfma_f32_16x16x32_bf16 %2, %6, %4, %2"
      : "+v"(ak[8]), "+v"(av[8]), "+v"(aq[7]) : "a"(wq[7][1]), "v"(xf[7][8]), "a"(wq[7][2]), "a"(wq[7][0])); SB();
  WAITVB(21);
  WAITL(3);
  SB();
  asm volatile("v_mfma_f32_16x16x32_bf16 %0, %11, %12, %0\n\ts_add_u32 m0, %13, 122880\n\ts_add_u32 %1, %14, 99072\n\tv_mfma_f32_16x16x32_bf16 %2, %15, %12, %2\n\tds_read_b128 %3, %16 offset:18432\n\tv_mfma_f32_16x16x32_bf16 %4, %11, %17, %4\n\tbuffer_load_dwordx4 %18, %19, %1 offen offset:0 sc1 lds\n\tv_mfma_f32_16x16x32_bf16 %5, %15, %17, %5\n\tds_read_b128 %6, %16 offset:20480\n\tv_mfma_f32_16x16x32_bf16 %7, %20, %17, %7\n\tbuffer_load_dwordx4 %8, %21, %19, %22 offen offset:768 sc1\n\tv_mfma_f32_16x16x32_bf16 %9, %11, %23, %9\n\tds_read_b128 %10, %16 offset:22528"
      : "+v"(ak[0]), "=&s"(sXt), "+v"(av[0]), "=&v"(xf[9][0]), "+v"(ak[1]), "+v"(av[1]), "=&v"(xf[9][1]), "+v"(aq[0]), "=&a"(wq[13][0]), "+v"(ak[2]), "=&v"(xf[9][2]) : "a"(wq[8][1]), "v"(xf[8][0]), "s"(ldsX), "s"(sXa), "a"(wq[8][2]), "v"(xr1), "v"(xf[8][1]), "v"(vXN), "s"(rsW), "a"(wq[8][0]), "v"(vQ), "s"(sq[1]), "v"(xf[8][2]) : "memory", "scc"); SB();
  asm volatile("v_mfma_f32_16x16x32_bf16 %0, %11, %12, %0\n\ts_add_u32 m0, %13, 126976\n\ts_add_u32 %1, %14, 131840\n\tv_mfma_f32_16x16x32_bf16 %2, %15, %12, %2\n\tds_read_b128 %3, %16 offset:24576\n\tv_mfma_f32_16x16x32_bf16 %4, %17, %18, %4\n\tbuffer_load_dwordx4 %19, %20, %1 offen offset:0 sc1 lds\n\tv_mfma_f32_16x16x32_bf16 %5, %11, %18, %5\n\tds_read_b128 %6, %16 offset:26624\n\tv_mfma_f32_16x16x32_bf16 %7, %15, %18, %7\n\tbuffer_load_dwordx4 %8, %21, %20, %22 offen offset:768 sc1\n\tv_mfma_f32_16x16x32_bf16 %9, %17, %23, %9\n\tds_read_b128 %10, %16 offset:28672"
      : "+v"(av[2]), "=&s"(sXt), "+v"(aq[1]), "=&v"(xf[9][3]), "+v"(ak[3]), "+v"(av[3]), "=&v"(xf[9][4]), "+v"(aq[2]), "=&a"(wq[13][1]), "+v"(ak[4]), "=&v"(xf[9][5]) : "a"(wq[8][2]), "v"(xf[8][2]), "s"(ldsX), "s"(sXc), "a"(wq[8][0]), "v"(xr1), "a"(wq[8][1]), "v"(xf[8][3]), "v"(vXN), "s"(rsW), "v"(vQ), "s"(sq[3]), "v"(xf[8][4]) : "memory", "scc"); SB();
  asm volatile("v_mfma_f32_16x16x32_bf16 %0, %8, %9, %0\n\tbuffer_load_dwordx4 %1, %10, %11, %12 offen offset:768 sc1\n\tv_mfma_f32_16x16x32_bf16 %2, %13, %9, %2\n\tds_read_b128 %3, %14 offset:30720\n\tv_mfma_f32_16x16x32_bf16 %4, %15, %16, %4\n\tv_mfma_f32_16x16x32_bf16 %5, %8, %16, %5\n\tds_read_b128 %6, %14 offset:32768\n\tv_mfma_f32_16x16x32_bf16 %7, %13, %16, %7"
      : "+v"(av[4]), "=&a"(wq[13][2]), "+v"(aq[3]), "=&v"(xf[9][6]), "+v"(ak[5]), "+v"(av[5]), "=&v"(xf[9][7]), "+v"(aq[4]) : "a"(wq[8][2]), "v"(xf[8][4]), "v"(vQ), "s"(rsW), "s"(sq[5]), "a"(wq[8][0]), "v"(xr1), "a"(wq[8][1]), "v"(xf[8][5]) : "memory"); SB();
  WAITL(8);
  SB();
  asm volatile("v_mfma_f32_16x16x32_bf16 %0, %10, %11, %0\n\tds_read_b128 %1, %12 offset:34816\n\tv_mfma_f32_16x16x32_bf16 %2, %13, %11, %2\n\tv_mfma_f32_16x16x32_bf16 %3, %14, %11, %3\n\tv_mfma_f32_16x16x32_bf16 %4, %10, %15, %4\n\tv_mfma_f32_16x16x32_bf16 %5, %13, %15, %5\n\tv_mfma_f32_16x16x32_bf16 %6, %14, %15, %6\n\tv_mfma_f32_16x16x32_bf16 %7, %10, %16, %7\n\tv_mfma_f32_16x16x32_bf16 %8, %13, %16, %8\n\tv_mfma_f32_16x16x32_bf16 %9, %14, %16, %9"
      : "+v"(ak[6]), "=&v"(xf[9][8]), "+v"(av[6]), "+v"(aq[5]), "+v"(ak[7]), "+v"(av[7]), "+v"(aq[6]), "+v"(ak[8]), "+v"(av[8]), "+v"(aq[7]) : "a"(wq[8][1]), "v"(xf[8][6]), "v"(xr1), "a"(wq[8][2]), "a"(wq[8][0]), "v"(xf[8][7]), "v"(xf[8][8]) : "memory"); SB();
  WAITVB(21);
  WAITL(3);
  SB();
  asm volatile("v_mfma_f32_16x16x32_bf16 %0, %3, %4, %0\n\ts_add_u32 m0, %5, 18432\n\tv_add_u32 %1, 36864, %1\n\tv_mfma_f32_16x16x32_bf16 %2, %6, %4, %2\n\ts_waitcnt vmcnt(16)"
      : "+v"(ak[0]), "+v"(xr0), "+v"(av[0]) : "a"(wq[9][1]), "v"(xf[9][0]), "s"(ldsS), "a"(wq[9][2]) : "memory", "scc"); SB();
  BAR0();
  SB();
  asm volatile("v_mfma_f32_16x16x32_bf16 %0, %11, %12, %0\n\tds_read_b128 %1, %13 offset:0\n\tv_mfma_f32_16x16x32_bf16 %2, %14, %12, %2\n\tds_read_b128 %3, %13 offset:2048\n\tv_mfma_f32_16x16x32_bf16 %4, %15, %12, %4\n\tbuffer_load_dwordx4 %16, %17, %18 offen offset:0  lds\n\tv_mfma_f32_16x16x32_bf16 %5, %11, %19, %5\n\tds_read_b128 %6, %13 offset:4096\n\tv_mfma_f32_16x16x32_bf16 %7, %14, %19, %7\n\tbuffer_load_dwordx4 %20, %17, %18 offen offset:1024  lds\n\tv_mfma_f32_16x16x32_bf16 %8, %15, %19, %8\n\tds_read_b128 %9, %13 offset:6144\n\tv_mfma_f32_16x16x32_bf16 %10, %11, %21, %10\n\tbuffer_load_dwordx4 %22, %17, %18 offen offset:2048  lds"
      : "+v"(ak[1]), "=&v"(xf[10][0]), "+v"(av[1]), "=&v"(xf[10][1]), "+v"(aq[0]), "+v"(ak[2]), "=&v"(xf[10][2]), "+v"(av[2]), "+v"(aq[1]), "=&v"(xf[10][3]), "+v"(ak[3]) : "a"(wq[9][1]), "v"(xf[9][1]), "v"(xr0), "a"(wq[9][2]), "a"(wq[9][0]), "v"(vW2[0]), "s"(rs2), "s"(sw[0]), "v"(xf[9][2]), "v"(vW2[1]), "v"(xf[9][3]), "v"(vW2[2]) : "memory"); SB();
  asm volatile("v_mfma_f32_16x16x32_bf16 %0, %9, %10, %0\n\tds_read_b128 %1, %11 offset:8192\n\tv_mfma_f32_16x16x32_bf16 %2, %12, %10, %2\n\tbuffer_load_dwordx4 %13, %14, %15 offen offset:3072  lds\n\tv_mfma_f32_16x16x32_bf16 %3, %16, %17, %3\n\tds_read_b128 %4, %11 offset:10240\n\ts_add_u32 m0, %18, 34816\n\tv_mfma_f32_16x16x32_bf16 %5, %9, %17, %5\n\tbuffer_load_dwordx4 %19, %14, %20 offen offset:0  lds\n\tv_mfma_f32_16x16x32_bf16 %6, %12, %17, %6\n\tds_read_b128 %7, %11 offset:12288\n\tv_mfma_f32_16x16x32_bf16 %8, %16, %21, %8\n\tbuffer_load_dwordx4 %22, %14, %20 offen offset:1024  lds"
      : "+v"(av[3]), "=&v"(xf[10][4]), "+v"(aq[2]), "+v"(ak[4]), "=&v"(xf[10][5]), "+v"(av[4]), "+v"(aq[3]), "=&v"(xf[10][6]), "+v"(ak[5]) : "a"(wq[9][2]), "v"(xf[9][3]), "v"(xr0), "a"(wq[9][0]), "v"(vW2[3]), "s"(rs2), "s"(sw[0]), "a"(wq[9][1]), "v"(xf[9][4]), "s"(ldsS), "v"(vW2[0]), "s"(sw[1]), "v"(xf[9][5]), "v"(vW2[1]) : "memory", "scc"); SB();
  asm volatile("v_mfma_f32_16x16x32_bf16 %0, %3, %4, %0\n\tds_read_b128 %1, %5 offset:14336\n\tv_mfma_f32_16x16x32_bf16 %2, %6, %4, %2"
      : "+v"(av[5]), "=&v"(xf[10][7]), "+v"(aq[4]) : "a"(wq[9][2]), "v"(xf[9][5]), "v"(xr0), "a"(wq[9][0]) : "memory"); SB();
  WAITL(8);
  SB();
  asm volatile("v_mfma_f32_16x16x32_bf16 %0, %10, %11, %0\n\tds_read_b128 %1, %12 offset:16384\n\tv_mfma_f32_16x16x32_bf16 %2, %13, %11, %2\n\tbuffer_load_dwordx4 %14, %15, %16 offen offset:2048  lds\n\tv_mfma_f32_16x16x32_bf16 %3, %17, %11, %3\n\tbuffer_load_dwordx4 %18, %15, %16 offen offset:3072  lds\n\tv_mfma_f32_16x16x32_bf16 %4, %10, %19, %4\n\tv_mfma_f32_16x16x32_bf16 %5, %13, %19, %5\n\tv_mfma_f32_16x16x32_bf16 %6, %17, %19, %6\n\tv_mfma_f32_16x16x32_bf16 %7, %10, %20, %7\n\tv_mfma_f32_16x16x32_bf16 %8, %13, %20, %8\n\tv_mfma_f32_16x16x32_bf16 %9, %17, %20, %9"
      : "+v"(ak[6]), "=&v"(xf[10][8]), "+v"(av[6]), "+v"(aq[5]), "+v"(ak[7]), "+v"(av[7]), "+v"(aq[6]), "+v"(ak[8]), "+v"(av[8]), "+v"(aq[7]) : "a"(wq[9][1]), "v"(xf[9][6]), "v"(xr0), "a"(wq[9][2]), "v"(vW2[2]), "s"(rs2), "s"(sw[1]), "a"(wq[9][0]), "v"(vW2[3]), "v"(xf[9][7]), "v"(xf[9][8]) : "memory"); SB();
  STAMP(32);
  WAITVB(23);
  WAITL(3);
  SB();
  asm volatile("v_mfma_f32_16x16x32_bf16 %0, %13, %14, %0\n\tv_add_u32 %1, 36864, %1\n\tv_mfma_f32_16x16x32_bf16 %2, %15, %14, %2\n\tds_read_b128 %3, %1 offset:0\n\tv_mfma_f32_16x16x32_bf16 %4, %13, %16, %4\n\tv_mfma_f32_16x16x32_bf16 %5, %15, %16, %5\n\tds_read_b128 %6, %1 offset:2048\n\tv_mfma_f32_16x16x32_bf16 %7, %17, %16, %7\n\tv_mfma_f32_16x16x32_bf16 %8, %13, %18, %8\n\tds_read_b128 %9, %1 offset:4096\n\tv_mfma_f32_16x16x32_bf16 %10, %15, %18, %10\n\tv_mfma_f32_16x16x32_bf16 %11, %17, %18, %11\n\tds_read_b128 %12, %1 offset:6144"
      : "+v"(ak[0]), "+v"(xr1), "+v"(av[0]), "=&v"(xf[11][0]), "+v"(ak[1]), "+v"(av[1]), "=&v"(xf[11][1]), "+v"(aq[0]), "+v"(ak[2]), "=&v"(xf[11][2]), "+v"(av[2]), "+v"(aq[1]), "=&v"(xf[11][3]) : "a"(wq[10][1]), "v"(xf[10][0]), "a"(wq[10][2]), "v"(xf[10][1]), "a"(wq[10][0]), "v"(xf[10][2]) : "memory"); SB();
  asm volatile("v_mfma_f32_16x16x32_bf16 %0, %13, %14, %0\n\tv_mfma_f32_16x16x32_bf16 %1, %15, %14, %1\n\tds_read_b128 %2, %16 offset:8192\n\tv_mfma_f32_16x16x32_bf16 %3, %17, %14, %3\n\tv_mfma_f32_16x16x32_bf16 %4, %13, %18, %4\n\tds_read_b128 %5, %16 offset:10240\n\tv_mfma_f32_16x16x32_bf16 %6, %15, %18, %6\n\tv_mfma_f32_16x16x32_bf16 %7, %17, %18, %7\n\tds_read_b128 %8, %16 offset:12288\n\tv_mfma_f32_16x16x32_bf16 %9, %13, %19, %9\n\tv_mfma_f32_16x16x32_bf16 %10, %15, %19, %10\n\tds_read_b128 %11, %16 offset:14336\n\tv_mfma_f32_16x16x32_bf16 %12, %17, %19, %12"
      : "+v"(ak[3]), "+v"(av[3]), "=&v"(xf[11][4]), "+v"(aq[2]), "+v"(ak[4]), "=&v"(xf[11][5]), "+v"(av[4]), "+v"(aq[3]), "=&v"(xf[11][6]), "+v"(ak[5]), "+v"(av[5]), "=&v"(xf[11][7]), "+v"(aq[4]) : "a"(wq[10][1]), "v"(xf[10][3]), "a"(wq[10][2]), "v"(xr1), "a"(wq[10][0]), "v"(xf[10][4]), "v"(xf[10][5]) : "memory"); SB();
  WAITL(8);
  SB();
  asm volatile("v_mfma_f32_16x16x32_bf16 %0, %3, %4, %0\n\tds_read_b128 %1, %5 offset:16384\n\tv_mfma_f32_16x16x32_bf16 %2, %6, %4, %2\n\ts_waitcnt vmcnt(10)"
      : "+v"(ak[6]), "=&v"(xf[11][8]), "+v"(av[6]) : "a"(wq[10][1]), "v"(xf[10][6]), "v"(xr1), "a"(wq[10][2]) : "memory"); SB();
  BAR0();
  SB();
  asm volatile("v_mfma_f32_16x16x32_bf16 %0, %9, %10, %0\n\tds_read_b128 %1, %11 offset:18432\n\tds_read_b128 %2, %12 offset:18432\n\tv_mfma_f32_16x16x32_bf16 %3, %13, %14, %3\n\tv_mfma_f32_16x16x32_bf16 %4, %15, %14, %4\n\tv_mfma_f32_16x16x32_bf16 %5, %9, %14, %5\n\tv_mfma_f32_16x16x32_bf16 %6, %13, %16, %6\n\tv_mfma_f32_16x16x32_bf16 %7, %15, %16, %7\n\tv_mfma_f32_16x16x32_bf16 %8, %9, %16, %8"
      : "+v"(aq[5]), "=&v"(xf[12][0]), "=&v"(xf[13][0]), "+v"(ak[7]), "+v"(av[7]), "+v"(aq[6]), "+v"(ak[8]), "+v"(av[8]), "+v"(aq[7]) : "a"(wq[10][0]), "v"(xf[10][6]), "v"(xr0), "v"(xr1), "a"(wq[10][1]), "v"(xf[10][7]), "a"(wq[10][2]), "v"(xf[10][8]) : "memory"); SB();
  WAITV1(13, fq);
  if (__builtin_expect(__ballot(fq != nonce) != 0ull, 0)) poll1<4>(rsW, vFQ, nonce);
  STAMP(13);
  u32x4 wor[14];
  WAITVB(19);
  WAITL(0);
  SB();
  asm volatile("v_mfma_f32_16x16x32_bf16 %0, %1, %2, %0"
      : "+v"(ak[0]) : "a"(wq[11][1]), "v"(xf[11][0])); SB();
  TOUCH(vT2, rs2, 0u, 0);
  SB();
  asm volatile("v_mfma_f32_16x16x32_bf16 %0, %2, %3, %0\n\tds_read_b128 %1, %4 offset:20480"
      : "+v"(av[0]), "=&v"(xf[12][1]) : "a"(wq[11][2]), "v"(xf[11][0]), "v"(xr0) : "memory"); SB();
  WAITVB(15);
  SB();
  asm volatile("v_mfma_f32_16x16x32_bf16 %0, %3, %4, %0\n\tds_read_b128 %1, %5 offset:20480\n\tv_mfma_f32_16x16x32_bf16 %2, %6, %4, %2"
      : "+v"(ak[0]), "=&v"(xf[13][1]), "+v"(av[0]) : "a"(wq[12][1]), "v"(xf[12][0]), "v"(xr1), "a"(wq[12][2]) : "memory"); SB();
  WAITVB(9);
  SB();
  asm volatile("v_mfma_f32_16x16x32_bf16 %0, %2, %3, %0\n\tv_mfma_f32_16x16x32_bf16 %1, %4, %3, %1"
      : "+v"(ak[0]), "+v"(av[0]) : "a"(wq[13][1]), "v"(xf[13][0]), "a"(wq[13][2])); SB();
  WAITL(0);
  SB();
  asm volatile("v_mfma_f32_16x16x32_bf16 %0, %9, %10, %0\n\tds_read_b128 %1, %11 offset:22528\n\tds_read_b128 %2, %12 offset:22528\n\tv_mfma_f32_16x16x32_bf16 %3, %13, %10, %3\n\tv_mfma_f32_16x16x32_bf16 %4, %14, %10, %4\n\tv_mfma_f32_16x16x32_bf16 %0, %15, %16, %0\n\tv_mfma_f32_16x16x32_bf16 %3, %17, %16, %3\n\tv_cvt_pk_bf16_f32 %5, %18, %19\n\tv_cvt_pk_bf16_f32 %6, %20, %21\n\tv_mfma_f32_16x16x32_bf16 %4, %22, %16, %4\n\tv_cvt_pk_bf16_f32 %7, %23, %24\n\tv_cvt_pk_bf16_f32 %8, %25, %26"
      : "+v"(ak[1]), "=&v"(xf[12][2]), "=&v"(xf[13][2]), "+v"(av[1]), "+v"(aq[0]), "=&v"(sg[0][0]), "=&v"(sg[0][1]), "=&v"(sg[1][0]), "=&v"(sg[1][1]) : "a"(wq[11][1]), "v"(xf[11][1]), "v"(xr0), "v"(xr1), "a"(wq[11][2]), "a"(wq[11][0]), "a"(wq[12][1]), "v"(xf[12][1]), "a"(wq[12][2]), "v"(ak[0][0]), "v"(ak[0][1]), "v"(ak[0][2]), "v"(ak[0][3]), "a"(wq[12][0]), "v"(av[0][0]), "v"(av[0][1]), "v"(av[0][2]), "v"(av[0][3]) : "memory"); SB();
  asm volatile("v_mfma_f32_16x16x32_bf16 %0, %3, %4, %0\n\tv_mfma_f32_16x16x32_bf16 %1, %5, %4, %1\n\tv_mfma_f32_16x16x32_bf16 %2, %6, %4, %2"
      : "+v"(ak[1]), "+v"(av[1]), "+v"(aq[0]) : "a"(wq[13][1]), "v"(xf[13][1]), "a"(wq[13][2]), "a"(wq[13][0])); SB();
  WAITL(0);
  SB();
  asm volatile("v_mfma_f32_16x16x32_bf16 %0, %7, %8, %0\n\tds_read_b128 %1, %9 offset:24576\n\tds_read_b128 %2, %10 offset:24576\n\tv_mfma_f32_16x16x32_bf16 %3, %11, %8, %3\n\tds_write_b64 %12, %13 offset:18432\n\tv_mfma_f32_16x16x32_bf16 %4, %14, %8, %4\n\tv_mfma_f32_16x16x32_bf16 %0, %15, %16, %0\n\tv_mfma_f32_16x16x32_bf16 %3, %17, %16, %3\n\tds_write_b64 %18, %19 offset:0\n\tv_mfma_f32_16x16x32_bf16 %4, %20, %16, %4\n\tv_cvt_pk_bf16_f32 %5, %21, %22\n\tv_cvt_pk_bf16_f32 %6, %23, %24\n\tv_mfma_f32_16x16x32_bf16 %0, %25, %26, %0"
      : "+v"(ak[2]), "=&v"(xf[12][3]), "=&v"(xf[13][3]), "+v"(av[2]), "+v"(aq[1]), "=&v"(sg[3][0]), "=&v"(sg[3][1]) : "a"(wq[11][1]), "v"(xf[11][2]), "v"(xr0), "v"(xr1), "a"(wq[11][2]), "v"(sk0), "v"(sg[0]), "a"(wq[11][0]), "a"(wq[12][1]), "v"(xf[12][2]), "a"(wq[12][2]), "v"(sv), "v"(sg[1]), "a"(wq[12][0]), "v"(ak[1][0]), "v"(ak[1][1]), "v"(ak[1][2]), "v"(ak[1][3]), "a"(wq[13][1]), "v"(xf[13][2]) : "memory"); SB();
  asm volatile("v_cvt_pk_bf16_f32 %0, %6, %7\n\tv_cvt_pk_bf16_f32 %1, %8, %9\n\tv_mfma_f32_16x16x32_bf16 %2, %10, %11, %2\n\tv_cvt_pk_bf16_f32 %3, %12, %13\n\tv_cvt_pk_bf16_f32 %4, %14, %15\n\tv_mfma_f32_16x16x32_bf16 %5, %16, %11, %5"
      : "=&v"(sg[4][0]), "=&v"(sg[4][1]), "+v"(av[2]), "=&v"(sg[5][0]), "=&v"(sg[5][1]), "+v"(aq[1]) : "v"(av[1][0]), "v"(av[1][1]), "v"(av[1][2]), "v"(av[1][3]), "a"(wq[13][2]), "v"(xf[13][2]), "v"(aq[0][0]), "v"(aq[0][1]), "v"(aq[0][2]), "v"(aq[0][3]), "a"(wq[13][0])); SB();
  WAITL(2);
  SB();
  asm volatile("v_mfma_f32_16x16x32_bf16 %0, %7, %8, %0\n\tds_read_b128 %1, %9 offset:26624\n\tds_read_b128 %2, %10 offset:26624\n\tv_mfma_f32_16x16x32_bf16 %3, %11, %8, %3\n\tds_write_b64 %12, %13 offset:18944\n\tv_mfma_f32_16x16x32_bf16 %4, %14, %8, %4\n\tv_mfma_f32_16x16x32_bf16 %0, %15, %16, %0\n\tv_mfma_f32_16x16x32_bf16 %3, %17, %16, %3\n\tds_write_b64 %18, %19 offset:256\n\tv_mfma_f32_16x16x32_bf16 %4, %20, %16, %4\n\tv_cvt_pk_bf16_f32 %5, %21, %22\n\tv_cvt_pk_bf16_f32 %6, %23, %24\n\tv_mfma_f32_16x16x32_bf16 %0, %25, %26, %0"
      : "+v"(ak[3]), "=&v"(xf[12][4]), "=&v"(xf[13][4]), "+v"(av[3]), "+v"(aq[2]), "=&v"(sg[6][0]), "=&v"(sg[6][1]) : "a"(wq[11][1]), "v"(xf[11][3]), "v"(xr0), "v"(xr1), "a"(wq[11][2]), "v"(sk1), "v"(sg[3]), "a"(wq[11][0]), "a"(wq[12][1]), "v"(xf[12][3]), "a"(wq[12][2]), "v"(sv), "v"(sg[4]), "a"(wq[12][0]), "v"(ak[2][0]), "v"(ak[2][1]), "v"(ak[2][2]), "v"(ak[2][3]), "a"(wq[13][1]), "v"(xf[13][3]) : "memory"); SB();
  asm volatile("v_cvt_pk_bf16_f32 %0, %4, %5\n\tv_cvt_pk_bf16_f32 %1, %6, %7\n\tv_mfma_f32_16x16x32_bf16 %2, %8, %9, %2\n\tds_write_b64 %10, %11 offset:36864\n\tv_mfma_f32_16x16x32_bf16 %3, %12, %9, %3"
      : "=&v"(sg[7][0]), "=&v"(sg[7][1]), "+v"(av[3]), "+v"(aq[2]) : "v"(av[2][0]), "v"(av[2][1]), "v"(av[2][2]), "v"(av[2][3]), "a"(wq[13][2]), "v"(xf[13][3]), "v"(sk0), "v"(sg[5]), "a"(wq[13][0]) : "memory"); SB();
  WAITL(3);
  SB();
  asm volatile("v_mfma_f32_16x16x32_bf16 %0, %8, %9, %0\n\tds_read_b128 %1, %10 offset:28672\n\tds_read_b128 %2, %11 offset:28672\n\tv_mfma_f32_16x16x32_bf16 %3, %12, %9, %3\n\tds_write_b64 %13, %14 offset:19456\n\tv_cvt_pk_bf16_f32 %4, %15, %16\n\tv_cvt_pk_bf16_f32 %5, %17, %18\n\tv_mfma_f32_16x16x32_bf16 %6, %19, %9, %6\n\tbuffer_load_dwordx4 %7, %20, %21, %22 offen offset:0 sc1\n\tv_mfma_f32_16x16x32_bf16 %0, %23, %24, %0\n\tv_mfma_f32_16x16x32_bf16 %3, %25, %24, %3"
      : "+v"(ak[4]), "=&v"(xf[12][5]), "=&v"(xf[13][5]), "+v"(av[4]), "=&v"(sg[8][0]), "=&v"(sg[8][1]), "+v"(aq[3]), "=&a"(wor[0]) : "a"(wq[11][1]), "v"(xf[11][4]), "v"(xr0), "v"(xr1), "a"(wq[11][2]), "v"(sk0), "v"(sg[6]), "v"(aq[1][0]), "v"(aq[1][1]), "v"(aq[1][2]), "v"(aq[1][3]), "a"(wq[11][0]), "v"(vWo), "s"(rsW), "s"(sWq[0]), "a"(wq[12][1]), "v"(xf[12][4]), "a"(wq[12][2]) : "memory"); SB();
  asm volatile("ds_write_b64 %8, %9 offset:512\n\tv_mfma_f32_16x16x32_bf16 %0, %10, %11, %0\n\tbuffer_load_dwordx4 %1, %12, %13, %14 offen offset:1024 sc1\n\tv_cvt_pk_bf16_f32 %2, %15, %16\n\tv_cvt_pk_bf16_f32 %3, %17, %18\n\tv_mfma_f32_16x16x32_bf16 %4, %19, %20, %4\n\tv_cvt_pk_bf16_f32 %5, %21, %22\n\tv_cvt_pk_bf16_f32 %6, %23, %24\n\tv_mfma_f32_16x16x32_bf16 %7, %25, %20, %7"
      : "+v"(aq[3]), "=&a"(wor[1]), "=&v"(sg[9][0]), "=&v"(sg[9][1]), "+v"(ak[4]), "=&v"(sg[10][0]), "=&v"(sg[10][1]), "+v"(av[4]) : "v"(sv), "v"(sg[7]), "a"(wq[12][0]), "v"(xf[12][4]), "v"(vWo), "s"(rsW), "s"(sWq[0]), "v"(ak[3][0]), "v"(ak[3][1]), "v"(ak[3][2]), "v"(ak[3][3]), "a"(wq[13][1]), "v"(xf[13][4]), "v"(av[3][0]), "v"(av[3][1]), "v"(av[3][2]), "v"(av[3][3]), "a"(wq[13][2]) : "memory"); SB();
  asm volatile("ds_write_b64 %2, %3 offset:37376\n\tv_mfma_f32_16x16x32_bf16 %0, %4, %5, %0\n\tbuffer_load_dwordx4 %1, %6, %7, %8 offen offset:2048 sc1"
      : "+v"(aq[3]), "=&a"(wor[2]) : "v"(sk1), "v"(sg[8]), "a"(wq[13][0]), "v"(xf[13][4]), "v"(vWo), "s"(rsW), "s"(sWq[0]) : "memory"); SB();
  WAITL(3);
  SB();
  asm volatile("v_mfma_f32_16x16x32_bf16 %0, %8, %9, %0\n\tds_read_b128 %1, %10 offset:30720\n\tds_read_b128 %2, %11 offset:30720\n\tv_mfma_f32_16x16x32_bf16 %3, %12, %9, %3\n\tds_write_b64 %13, %14 offset:19968\n\tv_cvt_pk_bf16_f32 %4, %15, %16\n\tv_cvt_pk_bf16_f32 %5, %17, %18\n\tv_mfma_f32_16x16x32_bf16 %6, %19, %9, %6\n\tbuffer_load_dwordx4 %7, %20, %21, %22 offen offset:3072 sc1\n\tv_mfma_f32_16x16x32_bf16 %0, %23, %24, %0\n\tv_mfma_f32_16x16x32_bf16 %3, %25, %24, %3"
      : "+v"(ak[5]), "=&v"(xf[12][6]), "=&v"(xf[13][6]), "+v"(av[5]), "=&v"(sg[11][0]), "=&v"(sg[11][1]), "+v"(aq[4]), "=&a"(wor[3]) : "a"(wq[11][1]), "v"(xf[11][5]), "v"(xr0), "v"(xr1), "a"(wq[11][2]), "v"(sk1), "v"(sg[9]), "v"(aq[2][0]), "v"(aq[2][1]), "v"(aq[2][2]), "v"(aq[2][3]), "a"(wq[11][0]), "v"(vWo), "s"(rsW), "s"(sWq[0]), "a"(wq[12][1]), "v"(xf[12][5]), "a"(wq[12][2]) : "memory"); SB();
  asm volatile("ds_write_b64 %8, %9 offset:768\n\tv_mfma_f32_16x16x32_bf16 %0, %10, %11, %0\n\tbuffer_load_dwordx4 %1, %12, %13, %14 offen offset:0 sc1\n\tv_cvt_pk_bf16_f32 %2, %15, %16\n\tv_cvt_pk_bf16_f32 %3, %17, %18\n\tv_mfma_f32_16x16x32_bf16 %4, %19, %20, %4\n\tv_cvt_pk_bf16_f32 %5, %21, %22\n\tv_cvt_pk_bf16_f32 %6, %23, %24\n\tv_mfma_f32_16x16x32_bf16 %7, %25, %20, %7"
      : "+v"(aq[4]), "=&a"(wor[4]), "=&v"(sg[12][0]), "=&v"(sg[12][1]), "+v"(ak[5]), "=&v"(sg[13][0]), "=&v"(sg[13][1]), "+v"(av[5]) : "v"(sv), "v"(sg[10]), "a"(wq[12][0]), "v"(xf[12][5]), "v"(vWo), "s"(rsW), "s"(sWq[1]), "v"(ak[4][0]), "v"(ak[4][1]), "v"(ak[4][2]), "v"(ak[4][3]), "a"(wq[13][1]), "v"(xf[13][5]), "v"(av[4][0]), "v"(av[4][1]), "v"(av[4][2]), "v"(av[4][3]), "a"(wq[13][2]) : "memory"); SB();
  asm volatile("ds_write_b64 %2, %3 offset:37888\n\tv_mfma_f32_16x16x32_bf16 %0, %4, %5, %0\n\tbuffer_load_dwordx4 %1, %6, %7, %8 offen offset:1024 sc1"
      : "+v"(aq[4]), "=&a"(wor[5]) : "v"(sk0), "v"(sg[11]), "a"(wq[13][0]), "v"(xf[13][5]), "v"(vWo), "s"(rsW), "s"(sWq[1]) : "memory"); SB();
  WAITL(3);
  SB();
  asm volatile("v_mfma_f32_16x16x32_bf16 %0, %8, %9, %0\n\tds_read_b128 %1, %10 offset:32768\n\tds_read_b128 %2, %11 offset:32768\n\tv_mfma_f32_16x16x32_bf16 %3, %12, %9, %3\n\tds_write_b64 %13, %14 offset:20480\n\tv_cvt_pk_bf16_f32 %4, %15, %16\n\tv_cvt_pk_bf16_f32 %5, %17, %18\n\tv_mfma_f32_16x16x32_bf16 %6, %19, %9, %6\n\tbuffer_load_dwordx4 %7, %20, %21, %22 offen offset:2048 sc1\n\tv_mfma_f32_16x16x32_bf16 %0, %23, %24, %0\n\tv_mfma_f32_16x16x32_bf16 %3, %25, %24, %3"
      : "+v"(ak[6]), "=&v"(xf[12][7]), "=&v"(xf[13][7]), "+v"(av[6]), "=&v"(sg[14][0]), "=&v"(sg[14][1]), "+v"(aq[5]), "=&a"(wor[6]) : "a"(wq[11][1]), "v"(xf[11][6]), "v"(xr0), "v"(xr1), "a"(wq[11][2]), "v"(sk0), "v"(sg[12]), "v"(aq[3][0]), "v"(aq[3][1]), "v"(aq[3][2]), "v"(aq[3][3]), "a"(wq[11][0]), "v"(vWo), "s"(rsW), "s"(sWq[1]), "a"(wq[12][1]), "v"(xf[12][6]), "a"(wq[12][2]) : "memory"); SB();
  asm volatile("ds_write_b64 %8, %9 offset:1024\n\tv_mfma_f32_16x16x32_bf16 %0, %10, %11, %0\n\tbuffer_load_dwordx4 %1, %12, %13, %14 offen offset:3072 sc1\n\tv_cvt_pk_bf16_f32 %2, %15, %16\n\tv_cvt_pk_bf16_f32 %3, %17, %18\n\tv_mfma_f32_16x16x32_bf16 %4, %19, %20, %4\n\tv_cvt_pk_bf16_f32 %5, %21, %22\n\tv_cvt_pk_bf16_f32 %6, %23, %24\n\tv_mfma_f32_16x16x32_bf16 %7, %25, %20, %7"
      : "+v"(aq[5]), "=&a"(wor[7]), "=&v"(sg[15][0]), "=&v"(sg[15][1]), "+v"(ak[6]), "=&v"(sg[16][0]), "=&v"(sg[16][1]), "+v"(av[6]) : "v"(sv), "v"(sg[13]), "a"(wq[12][0]), "v"(xf[12][6]), "v"(vWo), "s"(rsW), "s"(sWq[1]), "v"(ak[5][0]), "v"(ak[5][1]), "v"(ak[5][2]), "v"(ak[5][3]), "a"(wq[13][1]), "v"(xf[13][6]), "v"(av[5][0]), "v"(av[5][1]), "v"(av[5][2]), "v"(av[5][3]), "a"(wq[13][2]) : "memory"); SB();
  asm volatile("ds_write_b64 %2, %3 offset:38400\n\tv_mfma_f32_16x16x32_bf16 %0, %4, %5, %0\n\tbuffer_load_dwordx4 %1, %6, %7, %8 offen offset:0 sc1"
      : "+v"(aq[5]), "=&a"(wor[8]) : "v"(sk1), "v"(sg[14]), "a"(wq[13][0]), "v"(xf[13][6]), "v"(vWo), "s"(rsW), "s"(sWq[2]) : "memory"); SB();
  WAITL(3);
  SB();
  asm volatile("v_mfma_f32_16x16x32_bf16 %0, %8, %9, %0\n\tds_read_b128 %1, %10 offset:34816\n\tds_read_b128 %2, %11 offset:34816\n\tv_mfma_f32_16x16x32_bf16 %3, %12, %9, %3\n\tds_write_b64 %13, %14 offset:20992\n\tv_cvt_pk_bf16_f32 %4, %15, %16\n\tv_cvt_pk_bf16_f32 %5, %17, %18\n\tv_mfma_f32_16x16x32_bf16 %6, %19, %9, %6\n\tbuffer_load_dwordx4 %7, %20, %21, %22 offen offset:1024 sc1\n\tv_mfma_f32_16x16x32_bf16 %0, %23, %24, %0\n\tv_mfma_f32_16x16x32_bf16 %3, %25, %24, %3"
      : "+v"(ak[7]), "=&v"(xf[12][8]), "=&v"(xf[13][8]), "+v"(av[7]), "=&v"(sg[17][0]), "=&v"(sg[17][1]), "+v"(aq[6]), "=&a"(wor[9]) : "a"(wq[11][1]), "v"(xf[11][7]), "v"(xr0), "v"(xr1), "a"(wq[11][2]), "v"(sk1), "v"(sg[15]), "v"(aq[4][0]), "v"(aq[4][1]), "v"(aq[4][2]), "v"(aq[4][3]), "a"(wq[11][0]), "v"(vWo), "s"(rsW), "s"(sWq[2]), "a"(wq[12][1]), "v"(xf[12][7]), "a"(wq[12][2]) : "memory"); SB();
  asm volatile("ds_write_b64 %8, %9 offset:1280\n\tv_mfma_f32_16x16x32_bf16 %0, %10, %11, %0\n\tbuffer_load_dwordx4 %1, %12, %13, %14 offen offset:2048 sc1\n\tv_cvt_pk_bf16_f32 %2, %15, %16\n\tv_cvt_pk_bf16_f32 %3, %17, %18\n\tv_mfma_f32_16x16x32_bf16 %4, %19, %20, %4\n\tv_cvt_pk_bf16_f32 %5, %21, %22\n\tv_cvt_pk_bf16_f32 %6, %23, %24\n\tv_mfma_f32_16x16x32_bf16 %7, %25, %20, %7"
      : "+v"(aq[6]), "=&a"(wor[10]), "=&v"(sg[18][0]), "=&v"(sg[18][1]), "+v"(ak[7]), "=&v"(sg[19][0]), "=&v"(sg[19][1]), "+v"(av[7]) : "v"(sv), "v"(sg[16]), "a"(wq[12][0]), "v"(xf[12][7]), "v"(vWo), "s"(rsW), "s"(sWq[2]), "v"(ak[6][0]), "v"(ak[6][1]), "v"(ak[6][2]), "v"(ak[6][3]), "a"(wq[13][1]), "v"(xf[13][7]), "v"(av[6][0]), "v"(av[6][1]), "v"(av[6][2]), "v"(av[6][3]), "a"(wq[13][2]) : "memory"); SB();
  asm volatile("ds_write_b64 %2, %3 offset:38912\n\tv_mfma_f32_16x16x32_bf16 %0, %4, %5, %0\n\tbuffer_load_dwordx4 %1, %6, %7, %8 offen offset:3072 sc1"
      : "+v"(aq[6]), "=&a"(wor[11]) : "v"(sk0), "v"(sg[17]), "a"(wq[13][0]), "v"(xf[13][7]), "v"(vWo), "s"(rsW), "s"(sWq[2]) : "memory"); SB();
  WAITL(3);
  SB();
  asm volatile("v_mfma_f32_16x16x32_bf16 %0, %7, %8, %0\n\tv_cvt_pk_bf16_f32 %1, %9, %10\n\tv_cvt_pk_bf16_f32 %2, %11, %12\n\tv_mfma_f32_16x16x32_bf16 %3, %13, %8, %3\n\tds_write_b64 %14, %15 offset:21504\n\tv_mfma_f32_16x16x32_bf16 %4, %16, %8, %4\n\tbuffer_load_dwordx4 %5, %17, %18, %19 offen offset:0 sc1\n\tv_mfma_f32_16x16x32_bf16 %0, %20, %21, %0\n\tv_mfma_f32_16x16x32_bf16 %3, %22, %21, %3\n\tds_write_b64 %23, %24 offset:1536\n\tv_mfma_f32_16x16x32_bf16 %4, %25, %21, %4\n\tbuffer_load_dwordx4 %6, %17, %18, %19 offen offset:1024 sc1"
      : "+v"(ak[8]), "=&v"(sg[20][0]), "=&v"(sg[20][1]), "+v"(av[8]), "+v"(aq[7]), "=&a"(wor[12]), "=&a"(wor[13]) : "a"(wq[11][1]), "v"(xf[11][8]), "v"(aq[5][0]), "v"(aq[5][1]), "v"(aq[5][2]), "v"(aq[5][3]), "a"(wq[11][2]), "v"(sk0), "v"(sg[18]), "a"(wq[11][0]), "v"(vWo), "s"(rsW), "s"(sWq[3]), "a"(wq[12][1]), "v"(xf[12][8]), "a"(wq[12][2]), "v"(sv), "v"(sg[19]), "a"(wq[12][0]) : "memory"); SB();
  asm volatile("v_cvt_pk_bf16_f32 %0, %9, %10\n\tv_cvt_pk_bf16_f32 %1, %11, %12\n\tv_mfma_f32_16x16x32_bf16 %2, %13, %14, %2\n\tv_cvt_pk_bf16_f32 %3, %15, %16\n\tv_cvt_pk_bf16_f32 %4, %17, %18\n\tv_mfma_f32_16x16x32_bf16 %5, %19, %14, %5\n\tds_write_b64 %20, %21 offset:39424\n\tv_mfma_f32_16x16x32_bf16 %6, %22, %14, %6\n\tv_cvt_pk_bf16_f32 %7, %23, %24\n\tv_cvt_pk_bf16_f32 %8, %25, %26\n\ts_nop 5"
      : "=&v"(sg[21][0]), "=&v"(sg[21][1]), "+v"(ak[8]), "=&v"(sg[22][0]), "=&v"(sg[22][1]), "+v"(av[8]), "+v"(aq[7]), "=&v"(sg[23][0]), "=&v"(sg[23][1]) : "v"(ak[7][0]), "v"(ak[7][1]), "v"(ak[7][2]), "v"(ak[7][3]), "a"(wq[13][1]), "v"(xf[13][8]), "v"(av[7][0]), "v"(av[7][1]), "v"(av[7][2]), "v"(av[7][3]), "a"(wq[13][2]), "v"(sk1), "v"(sg[20]), "a"(wq[13][0]), "v"(aq[6][0]), "v"(aq[6][1]), "v"(aq[6][2]), "v"(aq[6][3]) : "memory"); SB();
  asm volatile("ds_write_b64 %6, %7 offset:22016\n\tds_write_b64 %8, %9 offset:1792\n\tds_write_b64 %10, %11 offset:39936\n\tv_cvt_pk_bf16_f32 %0, %12, %13\n\tv_cvt_pk_bf16_f32 %1, %14, %15\n\tv_cvt_pk_bf16_f32 %2, %16, %17\n\tv_cvt_pk_bf16_f32 %3, %18, %19\n\tv_cvt_pk_bf16_f32 %4, %20, %21\n\tv_cvt_pk_bf16_f32 %5, %22, %23"
      : "=&v"(sg[24][0]), "=&v"(sg[24][1]), "=&v"(sg[25][0]), "=&v"(sg[25][1]), "=&v"(sg[26][0]), "=&v"(sg[26][1]) : "v"(sk1), "v"(sg[21]), "v"(sv), "v"(sg[22]), "v"(sk0), "v"(sg[23]), "v"(ak[8][0]), "v"(ak[8][1]), "v"(ak[8][2]), "v"(ak[8][3]), "v"(av[8][0]), "v"(av[8][1]), "v"(av[8][2]), "v"(av[8][3]), "v"(aq[7][0]), "v"(aq[7][1]), "v"(aq[7][2]), "v"(aq[7][3]) : "memory"); SB();
  asm volatile("ds_write_b64 %0, %1 offset:22528\n\tds_write_b64 %2, %3 offset:2048\n\tds_write_b64 %4, %5 offset:40448"
      :  : "v"(sk0), "v"(sg[24]), "v"(sv), "v"(sg[25]), "v"(sk1), "v"(sg[26]) : "memory"); SB();
  BAR();
  STAMP(14);
  unsigned fu; u32x4 qf[2][2]; bf16x8 kf[2][2][2], vf[2][2][2];
  qf[0][0] = *(LDS_AS const u32x4*)(rk0 + SQ + 512 + 0); kf[0][0][0] = *(LDS_AS const bf16x8*)(rk0 + SK + 0); kf[0][0][1] = *(LDS_AS const bf16x8*)(rk0 + SK + 1024);
  qf[0][1] = *(LDS_AS const u32x4*)(rk0 + SQ + 512 + 9216); kf[0][1][0] = *(LDS_AS const bf16x8*)(rk0 + SK + 9216); kf[0][1][1] = *(LDS_AS const bf16x8*)(rk0 + SK + 10240);
  qf[1][0] = *(LDS_AS const u32x4*)(rk1 + SQ + 512 + 0); kf[1][0][0] = *(LDS_AS const bf16x8*)(rk1 + SK + 0); kf[1][0][1] = *(LDS_AS const bf16x8*)(rk1 + SK + 1024);
  qf[1][1] = *(LDS_AS const u32x4*)(rk1 + SQ + 512 + 9216); kf[1][1][0] = *(LDS_AS const bf16x8*)(rk1 + SK + 9216); kf[1][1][1] = *(LDS_AS const bf16x8*)(rk1 + SK + 10240);
  SB();
  asm volatile("s_add_u32 m0, %0, 0\n\ts_nop 0\n\tbuffer_load_dwordx4 %1, %2, %3 offen offset:0  lds\n\tbuffer_load_dwordx4 %4, %2, %3 offen offset:1024  lds\n\tbuffer_load_dwordx4 %5, %2, %3 offen offset:2048  lds\n\tbuffer_load_dwordx4 %6, %2, %3 offen offset:3072  lds"
      :  : "s"(ldsO), "v"(vW2[0]), "s"(rs2), "s"(sw[2]), "v"(vW2[1]), "v"(vW2[2]), "v"(vW2[3]) : "memory", "scc"); SB();
  f32x4 sc[2][2][2];
  asm volatile("v_mfma_f32_16x16x32_bf16 %0, %2, %3, 0\n\tv_mfma_f32_16x16x32_bf16 %1, %4, %3, 0"
      : "=&v"(sc[0][0][0]), "=&v"(sc[0][0][1]) : "v"(kf[0][0][0]), "v"(qf[0][0]), "v"(kf[0][0][1])); SB();
  vf[0][0][0] = tr8u(rv0 + 0, 512);
  vf[0][0][1] = tr8u(rv0 + 4608, 512);
  SB();
  asm volatile("v_mfma_f32_16x16x32_bf16 %0, %2, %3, 0\n\tv_mfma_f32_16x16x32_bf16 %1, %4, %3, 0"
      : "=&v"(sc[0][1][0]), "=&v"(sc[0][1][1]) : "v"(kf[0][1][0]), "v"(qf[0][1]), "v"(kf[0][1][1])); SB();
  vf[0][1][0] = tr8u(rv0 + 9216, 512);
  vf[0][1][1] = tr8u(rv0 + 13824, 512);
  SB();
  asm volatile("v_mfma_f32_16x16x32_bf16 %0, %2, %3, 0\n\tv_mfma_f32_16x16x32_bf16 %1, %4, %3, 0"
      : "=&v"(sc[1][0][0]), "=&v"(sc[1][0][1]) : "v"(kf[1][0][0]), "v"(qf[1][0]), "v"(kf[1][0][1])); SB();
  vf[1][0][0] = tr8u(rv1 + 0, 512);
  vf[1][0][1] = tr8u(rv1 + 4608, 512);
  SB();
  asm volatile("v_mfma_f32_16x16x32_bf16 %0, %2, %3, 0\n\tv_mfma_f32_16x16x32_bf16 %1, %4, %3, 0"
      : "=&v"(sc[1][1][0]), "=&v"(sc[1][1][1]) : "v"(kf[1][1][0]), "v"(qf[1][1]), "v"(kf[1][1][1])); SB();
  vf[1][1][0] = tr8u(rv1 + 9216, 512);
  vf[1][1][1] = tr8u(rv1 + 13824, 512);
  SB();
  asm volatile("s_add_u32 m0, %0, 4096\n\ts_nop 0\n\tbuffer_load_dwordx4 %1, %2, %3 offen offset:0  lds\n\tbuffer_load_dwordx4 %4, %2, %3 offen offset:1024  lds\n\tbuffer_load_dwordx4 %5, %2, %3 offen offset:2048  lds\n\tbuffer_load_dwordx4 %6, %2, %3 offen offset:3072  lds"
      :  : "s"(ldsO), "v"(vW2[0]), "s"(rs2), "s"(sw[3]), "v"(vW2[1]), "v"(vW2[2]), "v"(vW2[3]) : "memory", "scc"); SB();
  f32x2_t lg[2][2][2], mx[2], rs[2]; u32x4 pf[2][2]; f32x4 oo[2][2][2];
  const f32x2_t scale = {0.18898223650461362f, 0.18898223650461362f};
#define SEL(p, h, i) __uint_as_float((m1[i] & __float_as_uint(sc[p][h][1][i])) | (~m1[i] & __float_as_uint(sc[p][h][0][i])))
  lg[0][0][0] = rbf2(__builtin_elementwise_fma(rbf2((f32x2_t){SEL(0, 0, 0), SEL(0, 0, 1)}), scale, (f32x2_t){cm[0], cm[1]}));
  lg[0][0][1] = rbf2(__builtin_elementwise_fma(rbf2((f32x2_t){SEL(0, 0, 2), SEL(0, 0, 3)}), scale, (f32x2_t){cm[2], cm[3]}));
  OPV(lg[0][0][0]); OPV(lg[0][0][1]);
  SB();
  asm volatile("s_add_u32 m0, %0, 0\n\ts_nop 0\n\tbuffer_load_dwordx4 %1, %2, %3 offen offset:0  lds\n\tbuffer_load_dwordx4 %4, %2, %3 offen offset:1024  lds\n\tbuffer_load_dwordx4 %5, %2, %3 offen offset:2048  lds\n\tbuffer_load_dwordx4 %6, %2, %3 offen offset:3072  lds"
      :  : "s"(ldsS), "v"(vW2[0]), "s"(rs2), "s"(sw[4]), "v"(vW2[1]), "v"(vW2[2]), "v"(vW2[3]) : "memory", "scc"); SB();
  lg[0][1][0] = rbf2(__builtin_elementwise_fma(rbf2((f32x2_t){SEL(0, 1, 0), SEL(0, 1, 1)}), scale, (f32x2_t){cm[0], cm[1]}));
  lg[0][1][1] = rbf2(__builtin_elementwise_fma(rbf2((f32x2_t){SEL(0, 1, 2), SEL(0, 1, 3)}), scale, (f32x2_t){cm[2], cm[3]}));
  OPV(lg[0][1][0]); OPV(lg[0][1][1]);
  SB();
  asm volatile("s_waitcnt vmcnt(31)"
      :  :  : "memory"); SB();
  fw2[0][0] = tr8u(tS[0] + 18432, 512);
  fw2[0][1] = tr8u(tS[1] + 18432, 512);
  fw2[0][2] = tr8u(tS[2] + 18432, 512);
  fw2[0][3] = tr8u(tS[3] + 18432, 512);
  SB();
  lg[1][0][0] = rbf2(__builtin_elementwise_fma(rbf2((f32x2_t){SEL(1, 0, 0), SEL(1, 0, 1)}), scale, (f32x2_t){cm[0], cm[1]}));
  lg[1][0][1] = rbf2(__builtin_elementwise_fma(rbf2((f32x2_t){SEL(1, 0, 2), SEL(1, 0, 3)}), scale, (f32x2_t){cm[2], cm[3]}));
  OPV(lg[1][0][0]); OPV(lg[1][0][1]);
  SB();
  WAITL(0);
  asm volatile("s_add_u32 m0, %0, 18432\n\ts_nop 0\n\tbuffer_load_dwordx4 %1, %2, %3 offen offset:0  lds\n\tbuffer_load_dwordx4 %4, %2, %3 offen offset:1024  lds\n\tbuffer_load_dwordx4 %5, %2, %3 offen offset:2048  lds\n\tbuffer_load_dwordx4 %6, %2, %3 offen offset:3072  lds"
      :  : "s"(ldsS), "v"(vW2[0]), "s"(rs2), "s"(sw[5]), "v"(vW2[1]), "v"(vW2[2]), "v"(vW2[3]) : "memory", "scc"); SB();
  lg[1][1][0] = rbf2(__builtin_elementwise_fma(rbf2((f32x2_t){SEL(1, 1, 0), SEL(1, 1, 1)}), scale, (f32x2_t){cm[0], cm[1]}));
  lg[1][1][1] = rbf2(__builtin_elementwise_fma(rbf2((f32x2_t){SEL(1, 1, 2), SEL(1, 1, 3)}), scale, (f32x2_t){cm[2], cm[3]}));
  OPV(lg[1][1][0]); OPV(lg[1][1][1]);
  SB();
  asm volatile("s_waitcnt vmcnt(31)"
      :  :  : "memory"); SB();
  fw2[1][0] = tr8u(tS[0] + 34816, 512);
  fw2[1][1] = tr8u(tS[1] + 34816, 512);
  fw2[1][2] = tr8u(tS[2] + 34816, 512);
  fw2[1][3] = tr8u(tS[3] + 34816, 512);
  SB();
  #pragma unroll
  for (int p = 0; p < 2; ++p)
    mx[p] = xbcast2(xmax2(fmx(fmx(lg[p][0][0][0], lg[p][0][0][1]), fmx(lg[p][0][1][0], lg[p][0][1][1])), fmx(fmx(lg[p][1][0][0], lg[p][1][0][1]), fmx(lg[p][1][1][0], lg[p][1][1][1]))));
  OPV(mx[0]); OPV(mx[1]);
  SB();
  WAITL(0);
  asm volatile("s_add_u32 m0, %0, 34816\n\ts_nop 0\n\tbuffer_load_dwordx4 %1, %2, %3 offen offset:0  lds\n\tbuffer_load_dwordx4 %4, %2, %3 offen offset:1024  lds\n\tbuffer_load_dwordx4 %5, %2, %3 offen offset:2048  lds\n\tbuffer_load_dwordx4 %6, %2, %3 offen offset:3072  lds\n\ts_waitcnt vmcnt(16)"
      :  : "s"(ldsS), "v"(vW2[0]), "s"(rs2), "s"(sw[6]), "v"(vW2[1]), "v"(vW2[2]), "v"(vW2[3]) : "memory", "scc"); SB();
  fw2[2][0] = tr8u(tO[0] + 0, 512);
  fw2[2][1] = tr8u(tO[1] + 0, 512);
  fw2[2][2] = tr8u(tO[2] + 0, 512);
  fw2[2][3] = tr8u(tO[3] + 0, 512);
  SB();
  #pragma unroll
  for (int j = 0; j < 2; ++j) {
    const f32x2_t z = (lg[0][0][j] - (f32x2_t){mx[0][0], mx[0][0]}) * (f32x2_t){1.442695041f, 1.442695041f};
    lg[0][0][j] = (f32x2_t){__builtin_amdgcn_exp2f(z[0]), __builtin_amdgcn_exp2f(z[1])}; OPV(lg[0][0][j]);
  }
  SB();
  WAITL(0);
  asm volatile("s_add_u32 m0, %0, 0\n\ts_nop 0\n\tbuffer_load_dwordx4 %1, %2, %3 offen offset:0  lds\n\tbuffer_load_dwordx4 %4, %2, %3 offen offset:1024  lds\n\tbuffer_load_dwordx4 %5, %2, %3 offen offset:2048  lds\n\tbuffer_load_dwordx4 %6, %2, %3 offen offset:3072  lds"
      :  : "s"(ldsO), "v"(vW2[0]), "s"(rs2), "s"(sw[7]), "v"(vW2[1]), "v"(vW2[2]), "v"(vW2[3]) : "memory", "scc"); SB();
  #pragma unroll
  for (int j = 0; j < 2; ++j) {
    const f32x2_t z = (lg[0][1][j] - (f32x2_t){mx[0][1], mx[0][1]}) * (f32x2_t){1.442695041f, 1.442695041f};
    lg[0][1][j] = (f32x2_t){__builtin_amdgcn_exp2f(z[0]), __builtin_amdgcn_exp2f(z[1])}; OPV(lg[0][1][j]);
  }
  SB();
  asm volatile("s_waitcnt vmcnt(16)"
      :  :  : "memory"); SB();
  fw2[3][0] = tr8u(tO[0] + 4096, 512);
  fw2[3][1] = tr8u(tO[1] + 4096, 512);
  fw2[3][2] = tr8u(tO[2] + 4096, 512);
  fw2[3][3] = tr8u(tO[3] + 4096, 512);
  SB();
  #pragma unroll
  for (int j = 0; j < 2; ++j) {
    const f32x2_t z = (lg[1][0][j] - (f32x2_t){mx[1][0], mx[1][0]}) * (f32x2_t){1.442695041f, 1.442695041f};
    lg[1][0][j] = (f32x2_t){__builtin_amdgcn_exp2f(z[0]), __builtin_amdgcn_exp2f(z[1])}; OPV(lg[1][0][j]);
  }
  SB();
  WAITL(0);
  asm volatile("s_add_u32 m0, %0, 4096\n\ts_nop 0\n\tbuffer_load_dwordx4 %1, %2, %3 offen offset:0  lds\n\tbuffer_load_dwordx4 %4, %2, %3 offen offset:1024  lds\n\tbuffer_load_dwordx4 %5, %2, %3 offen offset:2048  lds\n\tbuffer_load_dwordx4 %6, %2, %3 offen offset:3072  lds"
      :  : "s"(ldsO), "v"(vW2[0]), "s"(rs2), "s"(sw[8]), "v"(vW2[1]), "v"(vW2[2]), "v"(vW2[3]) : "memory", "scc"); SB();
  #pragma unroll
  for (int j = 0; j < 2; ++j) {
    const f32x2_t z = (lg[1][1][j] - (f32x2_t){mx[1][1], mx[1][1]}) * (f32x2_t){1.442695041f, 1.442695041f};
    lg[1][1][j] = (f32x2_t){__builtin_amdgcn_exp2f(z[0]), __builtin_amdgcn_exp2f(z[1])}; OPV(lg[1][1][j]);
  }
  SB();
  asm volatile("s_waitcnt vmcnt(16)"
      :  :  : "memory"); SB();
  fw2[4][0] = tr8u(tS[0] + 0, 512);
  fw2[4][1] = tr8u(tS[1] + 0, 512);
  fw2[4][2] = tr8u(tS[2] + 0, 512);
  fw2[4][3] = tr8u(tS[3] + 0, 512);
  SB();
  #pragma unroll
  for (int p = 0; p < 2; ++p) {
    const f32x2_t e0 = lg[p][0][0] + lg[p][0][1], e1 = lg[p][1][0] + lg[p][1][1];
    rs[p] = xbcast2(__builtin_amdgcn_rcpf(xsum2(e0[0] + e0[1], e1[0] + e1[1])));
  }
  OPV(rs[0]); OPV(rs[1]);
  SB();
  WAITL(0);
  asm volatile("s_add_u32 m0, %0, 0\n\ts_nop 0\n\tbuffer_load_dwordx4 %1, %2, %3 offen offset:0  lds\n\tbuffer_load_dwordx4 %4, %2, %3 offen offset:1024  lds\n\tbuffer_load_dwordx4 %5, %2, %3 offen offset:2048  lds\n\tbuffer_load_dwordx4 %6, %2, %3 offen offset:3072  lds\n\ts_waitcnt vmcnt(16)"
      :  : "s"(ldsS), "v"(vW2[0]), "s"(rs2), "s"(sw[9]), "v"(vW2[1]), "v"(vW2[2]), "v"(vW2[3]) : "memory", "scc"); SB();
  fw2[5][0] = tr8u(tS[0] + 18432, 512);
  fw2[5][1] = tr8u(tS[1] + 18432, 512);
  fw2[5][2] = tr8u(tS[2] + 18432, 512);
  fw2[5][3] = tr8u(tS[3] + 18432, 512);
  SB();
  { const f32x2_t pa = lg[0][0][0] * (f32x2_t){rs[0][0], rs[0][0]}, pb = lg[0][0][1] * (f32x2_t){rs[0][0], rs[0][0]};
    const unsigned wa = pack2(pa[0], pa[1]), wb = pack2(pb[0], pb[1]);
    pf[0][0] = (u32x4){wa & ~mp[0], wb & ~mp[1], wa & mp[0], wb & mp[1]}; OPV(pf[0][0]); }
  { const f32x2_t pa = lg[0][1][0] * (f32x2_t){rs[0][1], rs[0][1]}, pb = lg[0][1][1] * (f32x2_t){rs[0][1], rs[0][1]};
    const unsigned wa = pack2(pa[0], pa[1]), wb = pack2(pb[0], pb[1]);
    pf[0][1] = (u32x4){wa & ~mp[0], wb & ~mp[1], wa & mp[0], wb & mp[1]}; OPV(pf[0][1]); }
  SB();
  WAITL(0);
  asm volatile("s_add_u32 m0, %4, 18432\n\ts_nop 0\n\tbuffer_load_dwordx4 %5, %6, %7 offen offset:0  lds\n\tbuffer_load_dwordx4 %8, %6, %7 offen offset:1024  lds\n\tbuffer_load_dwordx4 %9, %6, %7 offen offset:2048  lds\n\tbuffer_load_dwordx4 %10, %6, %7 offen offset:3072  lds\n\ts_nop 1\n\tv_mfma_f32_16x16x32_bf16 %0, %11, %12, 0\n\tv_mfma_f32_16x16x32_bf16 %1, %13, %14, 0\n\tv_mfma_f32_16x16x32_bf16 %2, %15, %12, 0\n\tv_mfma_f32_16x16x32_bf16 %3, %16, %14, 0"
      : "=&v"(oo[0][0][0]), "=&v"(oo[0][1][0]), "=&v"(oo[0][0][1]), "=&v"(oo[0][1][1]) : "s"(ldsS), "v"(vW2[0]), "s"(rs2), "s"(sw[10]), "v"(vW2[1]), "v"(vW2[2]), "v"(vW2[3]), "v"(vf[0][0][0]), "v"(pf[0][0]), "v"(vf[0][1][0]), "v"(pf[0][1]), "v"(vf[0][0][1]), "v"(vf[0][1][1]) : "memory", "scc"); SB();
  { const f32x2_t pa = lg[1][0][0] * (f32x2_t){rs[1][0], rs[1][0]}, pb = lg[1][0][1] * (f32x2_t){rs[1][0], rs[1][0]};
    const unsigned wa = pack2(pa[0], pa[1]), wb = pack2(pb[0], pb[1]);
    pf[1][0] = (u32x4){wa & ~mp[0], wb & ~mp[1], wa & mp[0], wb & mp[1]}; OPV(pf[1][0]); }
  { const f32x2_t pa = lg[1][1][0] * (f32x2_t){rs[1][1], rs[1][1]}, pb = lg[1][1][1] * (f32x2_t){rs[1][1], rs[1][1]};
    const unsigned wa = pack2(pa[0], pa[1]), wb = pack2(pb[0], pb[1]);
    pf[1][1] = (u32x4){wa & ~mp[0], wb & ~mp[1], wa & mp[0], wb & mp[1]}; OPV(pf[1][1]); }
  SB();
  asm volatile("s_waitcnt vmcnt(16)"
      :  :  : "memory"); SB();
  fw2[6][0] = tr8u(tS[0] + 34816, 512);
  fw2[6][1] = tr8u(tS[1] + 34816, 512);
  fw2[6][2] = tr8u(tS[2] + 34816, 512);
  fw2[6][3] = tr8u(tS[3] + 34816, 512);
  SB();
  asm volatile("s_nop 1\n\tv_mfma_f32_16x16x32_bf16 %0, %4, %5, 0\n\tv_mfma_f32_16x16x32_bf16 %1, %6, %7, 0\n\tv_mfma_f32_16x16x32_bf16 %2, %8, %5, 0\n\tv_mfma_f32_16x16x32_bf16 %3, %9, %7, 0"
      : "=&v"(oo[1][0][0]), "=&v"(oo[1][1][0]), "=&v"(oo[1][0][1]), "=&v"(oo[1][1][1]) : "v"(vf[1][0][0]), "v"(pf[1][0]), "v"(vf[1][1][0]), "v"(pf[1][1]), "v"(vf[1][0][1]), "v"(vf[1][1][1])); SB();
  HS16(vS, sO, 0, ((u32x4){pack2(oo[0][0][0][0], oo[0][0][0][1]), pack2(oo[0][0][0][2], oo[0][0][0][3]), pack2(oo[0][1][0][0], oo[0][1][0][1]), pack2(oo[0][1][0][2], oo[0][1][0][3])})); SB();
  HS16H(0xffff, vS, sO, 128, ((u32x4){pack2(oo[0][0][1][0], oo[0][0][1][1]), pack2(oo[0][0][1][2], oo[0][0][1][3]), pack2(oo[0][1][1][0], oo[0][1][1][1]), pack2(oo[0][1][1][2], oo[0][1][1][3])})); SB();
  HS16(vS, sO, 1024, ((u32x4){pack2(oo[1][0][0][0], oo[1][0][0][1]), pack2(oo[1][0][0][2], oo[1][0][0][3]), pack2(oo[1][1][0][0], oo[1][1][0][1]), pack2(oo[1][1][0][2], oo[1][1][0][3])})); SB();
  HS16H(0xffff, vS, sO, 1152, ((u32x4){pack2(oo[1][0][1][0], oo[1][0][1][1]), pack2(oo[1][0][1][2], oo[1][0][1][3]), pack2(oo[1][1][1][0], oo[1][1][1][1]), pack2(oo[1][1][1][2], oo[1][1][1][3])})); SB();
  STAMP(15);
  asm volatile("s_waitcnt vmcnt(16)"
      :  :  : "memory"); SB();
  fw2[7][0] = tr8u(tO[0] + 0, 512);
  fw2[7][1] = tr8u(tO[1] + 0, 512);
  fw2[7][2] = tr8u(tO[2] + 0, 512);
  fw2[7][3] = tr8u(tO[3] + 0, 512);
  WAITL(8);
  asm volatile("s_add_u32 m0, %1, 34816\n\ts_nop 0\n\tbuffer_load_dwordx4 %2, %3, %4 offen offset:0  lds\n\tbuffer_load_dwordx4 %5, %3, %4 offen offset:1024  lds\n\tbuffer_load_dwordx4 %6, %3, %4 offen offset:2048  lds\n\tbuffer_load_dwordx4 %7, %3, %4 offen offset:3072  lds\n\tbuffer_load_dword %0, %8, %9, 0 offen sc1\n\ts_waitcnt vmcnt(17)"
      : "=&v"(fu) : "s"(ldsS), "v"(vW2[0]), "s"(rs2), "s"(sw[11]), "v"(vW2[1]), "v"(vW2[2]), "v"(vW2[3]), "v"(vFU1), "s"(rsW) : "memory", "scc"); SB();
  fw2[8][0] = tr8u(tO[0] + 4096, 512);
  fw2[8][1] = tr8u(tO[1] + 4096, 512);
  fw2[8][2] = tr8u(tO[2] + 4096, 512);
  fw2[8][3] = tr8u(tO[3] + 4096, 512);
  WAITL(8);
  asm volatile("s_add_u32 m0, %0, 0\n\ts_nop 0\n\tbuffer_load_dwordx4 %1, %2, %3 offen offset:0  lds\n\tbuffer_load_dwordx4 %4, %2, %3 offen offset:1024  lds\n\tbuffer_load_dwordx4 %5, %2, %3 offen offset:2048  lds\n\tbuffer_load_dwordx4 %6, %2, %3 offen offset:3072  lds\n\ts_waitcnt vmcnt(17)"
      :  : "s"(ldsO), "v"(vW2[0]), "s"(rs2), "s"(sw[12]), "v"(vW2[1]), "v"(vW2[2]), "v"(vW2[3]) : "memory", "scc"); SB();
  fw2[9][0] = tr8u(tS[0] + 0, 512);
  fw2[9][1] = tr8u(tS[1] + 0, 512);
  fw2[9][2] = tr8u(tS[2] + 0, 512);
  fw2[9][3] = tr8u(tS[3] + 0, 512);
  WAITL(8);
  asm volatile("s_add_u32 m0, %0, 4096\n\ts_nop 0\n\tbuffer_load_dwordx4 %1, %2, %3 offen offset:0  lds\n\tbuffer_load_dwordx4 %4, %2, %3 offen offset:1024  lds\n\tbuffer_load_dwordx4 %5, %2, %3 offen offset:2048  lds\n\tbuffer_load_dwordx4 %6, %2, %3 offen offset:3072  lds"
      :  : "s"(ldsO), "v"(vW2[0]), "s"(rs2), "s"(sw[13]), "v"(vW2[1]), "v"(vW2[2]), "v"(vW2[3]) : "memory", "scc"); SB();
  STAMP(16);
  {
  bf16x8 pu[14], fwo[56];
  f32x4 ac[2][4], as[4], py[4][4];
  float ye[4], ze[4], ge[16], xe[16], bias;
  u32x2 ov[4]; unsigned fo;
  WAITV1(8, fu);
  STFO(sFO);
  if (__ballot(fu != nonce) != 0ull) poll1<4>(rsW, vFU1, nonce);
  STAMP(17);
  asm volatile("buffer_load_dwordx4 %0, %10, %11, %12 offen offset:0 sc1\n\tbuffer_load_dwordx4 %1, %10, %11, %13 offen offset:0 sc1\n\tbuffer_load_dwordx4 %2, %10, %11, %14 offen offset:0 sc1\n\tbuffer_load_dwordx4 %3, %10, %11, %15 offen offset:0 sc1\n\tbuffer_load_dwordx4 %4, %10, %11, %16 offen offset:0 sc1\n\tbuffer_load_dwordx4 %5, %10, %11, %17 offen offset:0 sc1\n\tbuffer_load_dwordx4 %6, %10, %11, %18 offen offset:0 sc1\n\tbuffer_load_dwordx4 %7, %10, %11, %19 offen offset:0 sc1\n\tbuffer_load_dwordx4 %8, %10, %11, %20 offen offset:0 sc1\n\tbuffer_load_dwordx4 %9, %10, %11, %21 offen offset:0 sc1\n\ts_waitcnt vmcnt(28)"
      : "=&v"(pu[0]), "=&v"(pu[1]), "=&v"(pu[2]), "=&v"(pu[3]), "=&v"(pu[4]), "=&v"(pu[5]), "=&v"(pu[6]), "=&v"(pu[7]), "=&v"(pu[8]), "=&v"(pu[9]) : "v"(vFj[0]), "s"(rsW), "s"(su[0]), "s"(su[1]), "s"(su[2]), "s"(su[3]), "s"(su[4]), "s"(su[5]), "s"(su[6]), "s"(su[7]), "s"(su[8]), "s"(su[9]) : "memory"); SB();
  fw2[10][0] = tr8u(tS[0] + 18432, 512);
  fw2[10][1] = tr8u(tS[1] + 18432, 512);
  fw2[10][2] = tr8u(tS[2] + 18432, 512);
  fw2[10][3] = tr8u(tS[3] + 18432, 512);
  asm volatile("s_waitcnt vmcnt(20)"
      :  :  : "memory"); SB();
  fw2[11][0] = tr8u(tS[0] + 34816, 512);
  fw2[11][1] = tr8u(tS[1] + 34816, 512);
  fw2[11][2] = tr8u(tS[2] + 34816, 512);
  fw2[11][3] = tr8u(tS[3] + 34816, 512);
  asm volatile("s_waitcnt vmcnt(15)"
      :  :  : "memory"); SB();
  fw2[12][0] = tr8u(tO[0] + 0, 512);
  fw2[12][1] = tr8u(tO[1] + 0, 512);
  fw2[12][2] = tr8u(tO[2] + 0, 512);
  fw2[12][3] = tr8u(tO[3] + 0, 512);
  asm volatile("s_waitcnt vmcnt(11)"
      :  :  : "memory"); SB();
  fw2[13][0] = tr8u(tO[0] + 4096, 512);
  fw2[13][1] = tr8u(tO[1] + 4096, 512);
  fw2[13][2] = tr8u(tO[2] + 4096, 512);
  fw2[13][3] = tr8u(tO[3] + 4096, 512);
  asm volatile("ds_write_b128 %0, %1 offset:8192\n\tds_write_b128 %0, %2 offset:9216\n\tds_write_b128 %0, %3 offset:10240\n\tds_write_b128 %0, %4 offset:11264\n\tds_write_b128 %0, %5 offset:12288\n\tds_write_b128 %0, %6 offset:13312\n\tds_write_b128 %7, %8 offset:0\n\tds_write_b128 %7, %9 offset:1024\n\tds_write_b128 %7, %10 offset:2048\n\tds_write_b128 %7, %11 offset:3072\n\tds_write_b128 %7, %12 offset:4096\n\tds_write_b128 %7, %13 offset:5120\n\tds_write_b128 %7, %14 offset:6144\n\tds_write_b128 %7, %15 offset:7168"
      :  : "v"(wwb), "a"(wor[8]), "a"(wor[9]), "a"(wor[10]), "a"(wor[11]), "a"(wor[12]), "a"(wor[13]), "v"(wwa), "a"(wor[0]), "a"(wor[1]), "a"(wor[2]), "a"(wor[3]), "a"(wor[4]), "a"(wor[5]), "a"(wor[6]), "a"(wor[7]) : "memory"); SB();
  STAMP(39);
  WAITVB(9);
  SB();
  asm volatile("v_mfma_f32_16x16x32_bf16 %0, %9, %10, 0\n\tbuffer_load_dwordx4 %1, %11, %12, %13 offen offset:0 sc1\n\tv_mfma_f32_16x16x32_bf16 %2, %9, %14, 0\n\tv_lshlrev_b32 %3, 16, %15\n\tv_xor_b32 %4, 0, %16\n\tv_mfma_f32_16x16x32_bf16 %5, %9, %17, 0\n\tbuffer_load_dwordx4 %6, %11, %12, %18 offen offset:0 sc1\n\tv_mfma_f32_16x16x32_bf16 %7, %9, %19, 0\n\tv_xor_b32 %8, 32, %16"
      : "=&v"(ac[0][0]), "=&v"(pu[10]), "=&v"(ac[0][1]), "=&v"(bias), "=&v"(xta[0]), "=&v"(ac[0][2]), "=&v"(pu[11]), "=&v"(ac[0][3]), "=&v"(xta[1]) : "v"(pu[0]), "a"(fw2[0][0]), "v"(vFj[0]), "s"(rsW), "s"(su[10]), "a"(fw2[0][1]), "v"(braw), "v"(xs), "a"(fw2[0][2]), "s"(su[11]), "a"(fw2[0][3]) : "memory"); SB();
  WAITVB(10);
  SB();
  asm volatile("v_mfma_f32_16x16x32_bf16 %0, %9, %10, %0\n\tbuffer_load_dwordx4 %1, %11, %12, %13 offen offset:0 sc1\n\tv_mfma_f32_16x16x32_bf16 %2, %9, %14, %2\n\tv_xor_b32 %3, 64, %15\n\tv_xor_b32 %4, 96, %15\n\tv_mfma_f32_16x16x32_bf16 %5, %9, %16, %5\n\tbuffer_load_dwordx4 %6, %11, %12, %17 offen offset:0 sc1\n\tv_mfma_f32_16x16x32_bf16 %7, %9, %18, %7\n\tv_lshlrev_b32 %8, 16, %19"
      : "+v"(ac[0][0]), "=&v"(pu[12]), "+v"(ac[0][1]), "=&v"(xta[2]), "=&v"(xta[3]), "+v"(ac[0][2]), "=&v"(pu[13]), "+v"(ac[0][3]), "=&v"(xe[0]) : "v"(pu[1]), "a"(fw2[1][0]), "v"(vFj[0]), "s"(rsW), "s"(su[12]), "a"(fw2[1][1]), "v"(xs), "a"(fw2[1][2]), "s"(su[13]), "a"(fw2[1][3]), "v"(xv[0][0]) : "memory"); SB();
  WAITVB(11);
  SB();
  asm volatile("v_mfma_f32_16x16x32_bf16 %0, %9, %10, %0\n\tbuffer_load_dwordx4 %1, %11, %12, %13 offen offset:0 sc1\n\tv_mfma_f32_16x16x32_bf16 %2, %9, %14, %2\n\tv_and_b32 %3, 0xffff0000, %15\n\tv_lshlrev_b32 %4, 16, %16\n\tv_mfma_f32_16x16x32_bf16 %5, %9, %17, %5\n\tbuffer_load_dwordx4 %6, %11, %12, %18 offen offset:0 sc1\n\tv_mfma_f32_16x16x32_bf16 %7, %9, %19, %7\n\tv_and_b32 %8, 0xffff0000, %16"
      : "+v"(ac[0][0]), "=&v"(pu[0]), "+v"(ac[0][1]), "=&v"(xe[1]), "=&v"(xe[2]), "+v"(ac[0][2]), "=&v"(pu[1]), "+v"(ac[0][3]), "=&v"(xe[3]) : "v"(pu[2]), "a"(fw2[2][0]), "v"(vFj[1]), "s"(rsW), "s"(su[0]), "a"(fw2[2][1]), "v"(xv[0][0]), "v"(xv[0][1]), "a"(fw2[2][2]), "s"(su[1]), "a"(fw2[2][3]) : "memory"); SB();
  WAITVB(12);
  SB();
  asm volatile("v_mfma_f32_16x16x32_bf16 %0, %10, %11, %0\n\tbuffer_load_dwordx4 %1, %12, %13, %14 offen offset:0 sc1\n\tv_mfma_f32_16x16x32_bf16 %2, %10, %15, %2\n\tv_lshlrev_b32 %3, 16, %16\n\tv_and_b32 %4, 0xffff0000, %16\n\tv_mfma_f32_16x16x32_bf16 %5, %10, %17, %5\n\tv_lshlrev_b32 %6, 16, %18\n\tv_and_b32 %7, 0xffff0000, %18\n\tv_mfma_f32_16x16x32_bf16 %8, %10, %19, %8\n\tv_lshlrev_b32 %9, 16, %20"
      : "+v"(ac[0][0]), "=&v"(pu[2]), "+v"(ac[0][1]), "=&v"(xe[4]), "=&v"(xe[5]), "+v"(ac[0][2]), "=&v"(xe[6]), "=&v"(xe[7]), "+v"(ac[0][3]), "=&v"(xe[8]) : "v"(pu[3]), "a"(fw2[3][0]), "v"(vFj[1]), "s"(rsW), "s"(su[2]), "a"(fw2[3][1]), "v"(xv[1][0]), "a"(fw2[3][2]), "v"(xv[1][1]), "a"(fw2[3][3]), "v"(xv[2][0]) : "memory"); SB();
  WAITVB(12);
  SB();
  asm volatile("v_mfma_f32_16x16x32_bf16 %0, %10, %11, %0\n\tbuffer_load_dwordx4 %1, %12, %13, %14 offen offset:0 sc1\n\tv_mfma_f32_16x16x32_bf16 %2, %10, %15, %2\n\tv_and_b32 %3, 0xffff0000, %16\n\tv_lshlrev_b32 %4, 16, %17\n\tv_mfma_f32_16x16x32_bf16 %5, %10, %18, %5\n\tv_and_b32 %6, 0xffff0000, %17\n\tv_lshlrev_b32 %7, 16, %19\n\tv_mfma_f32_16x16x32_bf16 %8, %10, %20, %8\n\tv_and_b32 %9, 0xffff0000, %19"
      : "+v"(ac[0][0]), "=&v"(pu[3]), "+v"(ac[0][1]), "=&v"(xe[9]), "=&v"(xe[10]), "+v"(ac[0][2]), "=&v"(xe[11]), "=&v"(xe[12]), "+v"(ac[0][3]), "=&v"(xe[13]) : "v"(pu[4]), "a"(fw2[4][0]), "v"(vFj[1]), "s"(rsW), "s"(su[3]), "a"(fw2[4][1]), "v"(xv[2][0]), "v"(xv[2][1]), "a"(fw2[4][2]), "v"(xv[3][0]), "a"(fw2[4][3]) : "memory"); SB();
  WAITVB(12);
  SB();
  asm volatile("v_mfma_f32_16x16x32_bf16 %0, %7, %8, %0\n\tbuffer_load_dwordx4 %1, %9, %10, %11 offen offset:0 sc1\n\tv_mfma_f32_16x16x32_bf16 %2, %7, %12, %2\n\tv_lshlrev_b32 %3, 16, %13\n\tv_and_b32 %4, 0xffff0000, %13\n\tv_mfma_f32_16x16x32_bf16 %5, %7, %14, %5\n\tv_mfma_f32_16x16x32_bf16 %6, %7, %15, %6"
      : "+v"(ac[0][0]), "=&v"(pu[4]), "+v"(ac[0][1]), "=&v"(xe[14]), "=&v"(xe[15]), "+v"(ac[0][2]), "+v"(ac[0][3]) : "v"(pu[5]), "a"(fw2[5][0]), "v"(vFj[1]), "s"(rsW), "s"(su[4]), "a"(fw2[5][1]), "v"(xv[3][1]), "a"(fw2[5][2]), "a"(fw2[5][3]) : "memory"); SB();
  WAITVB(12);
  SB();
  asm volatile("v_mfma_f32_16x16x32_bf16 %0, %6, %7, %0\n\tbuffer_load_dwordx4 %1, %8, %9, %10 offen offset:0 sc1\n\tv_mfma_f32_16x16x32_bf16 %2, %6, %11, %2\n\tbuffer_load_dword %3, %12, %9, 0 offen sc1\n\tv_mfma_f32_16x16x32_bf16 %4, %6, %13, %4\n\tv_mfma_f32_16x16x32_bf16 %5, %6, %14, %5"
      : "+v"(ac[0][0]), "=&v"(pu[5]), "+v"(ac[0][1]), "=&v"(fu2), "+v"(ac[0][2]), "+v"(ac[0][3]) : "v"(pu[6]), "a"(fw2[6][0]), "v"(vFj[1]), "s"(rsW), "s"(su[5]), "a"(fw2[6][1]), "v"(vFU), "a"(fw2[6][2]), "a"(fw2[6][3]) : "memory"); SB();
  WAITVB(13);
  SB();
  asm volatile("v_mfma_f32_16x16x32_bf16 %0, %5, %6, %0\n\tbuffer_load_dwordx4 %1, %7, %8, %9 offen offset:0 sc1\n\tv_mfma_f32_16x16x32_bf16 %2, %5, %10, %2\n\tv_mfma_f32_16x16x32_bf16 %3, %5, %11, %3\n\tv_mfma_f32_16x16x32_bf16 %4, %5, %12, %4"
      : "+v"(ac[0][0]), "=&v"(pu[6]), "+v"(ac[0][1]), "+v"(ac[0][2]), "+v"(ac[0][3]) : "v"(pu[7]), "a"(fw2[7][0]), "v"(vFj[1]), "s"(rsW), "s"(su[6]), "a"(fw2[7][1]), "a"(fw2[7][2]), "a"(fw2[7][3]) : "memory"); SB();
  WAITVB(13);
  SB();
  asm volatile("v_mfma_f32_16x16x32_bf16 %0, %5, %6, %0\n\tbuffer_load_dwordx4 %1, %7, %8, %9 offen offset:0 sc1\n\tv_mfma_f32_16x16x32_bf16 %2, %5, %10, %2\n\tv_mfma_f32_16x16x32_bf16 %3, %5, %11, %3\n\tv_mfma_f32_16x16x32_bf16 %4, %5, %12, %4"
      : "+v"(ac[0][0]), "=&v"(pu[7]), "+v"(ac[0][1]), "+v"(ac[0][2]), "+v"(ac[0][3]) : "v"(pu[8]), "a"(fw2[8][0]), "v"(vFj[1]), "s"(rsW), "s"(su[7]), "a"(fw2[8][1]), "a"(fw2[8][2]), "a"(fw2[8][3]) : "memory"); SB();
  WAITVB(13);
  SB();
  asm volatile("v_mfma_f32_16x16x32_bf16 %0, %5, %6, %0\n\tbuffer_load_dwordx4 %1, %7, %8, %9 offen offset:0 sc1\n\tv_mfma_f32_16x16x32_bf16 %2, %5, %10, %2\n\tv_mfma_f32_16x16x32_bf16 %3, %5, %11, %3\n\tv_mfma_f32_16x16x32_bf16 %4, %5, %12, %4"
      : "+v"(ac[0][0]), "=&v"(pu[8]), "+v"(ac[0][1]), "+v"(ac[0][2]), "+v"(ac[0][3]) : "v"(pu[9]), "v"(fw2[9][0]), "v"(vFj[1]), "s"(rsW), "s"(su[8]), "v"(fw2[9][1]), "v"(fw2[9][2]), "v"(fw2[9][3]) : "memory"); SB();
  WAITVB(13);
  SB();
  asm volatile("v_mfma_f32_16x16x32_bf16 %0, %5, %6, %0\n\tbuffer_load_dwordx4 %1, %7, %8, %9 offen offset:0 sc1\n\tv_mfma_f32_16x16x32_bf16 %2, %5, %10, %2\n\tv_mfma_f32_16x16x32_bf16 %3, %5, %11, %3\n\tv_mfma_f32_16x16x32_bf16 %4, %5, %12, %4"
      : "+v"(ac[0][0]), "=&v"(pu[9]), "+v"(ac[0][1]), "+v"(ac[0][2]), "+v"(ac[0][3]) : "v"(pu[10]), "v"(fw2[10][0]), "v"(vFj[1]), "s"(rsW), "s"(su[9]), "v"(fw2[10][1]), "v"(fw2[10][2]), "v"(fw2[10][3]) : "memory"); SB();
  WAITVB(13);
  SB();
  asm volatile("v_mfma_f32_16x16x32_bf16 %0, %5, %6, %0\n\tbuffer_load_dwordx4 %1, %7, %8, %9 offen offset:0 sc1\n\tv_mfma_f32_16x16x32_bf16 %2, %5, %10, %2\n\tv_mfma_f32_16x16x32_bf16 %3, %5, %11, %3\n\tv_mfma_f32_16x16x32_bf16 %4, %5, %12, %4"
      : "+v"(ac[0][0]), "=&v"(pu[10]), "+v"(ac[0][1]), "+v"(ac[0][2]), "+v"(ac[0][3]) : "v"(pu[11]), "v"(fw2[11][0]), "v"(vFj[1]), "s"(rsW), "s"(su[10]), "v"(fw2[11][1]), "v"(fw2[11][2]), "v"(fw2[11][3]) : "memory"); SB();
  WAITVB(13);
  SB();
  asm volatile("v_mfma_f32_16x16x32_bf16 %0, %4, %5, %0\n\tbuffer_load_dwordx4 %1, %6, %7, %8 offen offset:0 sc1\n\tv_mfma_f32_16x16x32_bf16 %2, %4, %9, %2\n\tv_mfma_f32_16x16x32_bf16 %3, %4, %10, %3"
      : "+v"(ac[0][0]), "=&v"(pu[11]), "+v"(ac[0][1]), "+v"(ac[0][2]) : "v"(pu[12]), "v"(fw2[12][0]), "v"(vFj[1]), "s"(rsW), "s"(su[11]), "v"(fw2[12][1]), "v"(fw2[12][2]) : "memory"); SB();
  u32x4 rsO_ = srd(out, 2048u * 896u); OPQ(rsO_);
  SB();
  asm volatile("v_mfma_f32_16x16x32_bf16 %0, %1, %2, %0"
      : "+v"(ac[0][3]) : "v"(pu[12]), "v"(fw2[12][3])); SB();
  WAITVB(13);
  SB();
  asm volatile("v_mfma_f32_16x16x32_bf16 %0, %5, %6, %0\n\tbuffer_load_dwordx4 %1, %7, %8, %9 offen offset:0 sc1\n\tv_mfma_f32_16x16x32_bf16 %2, %5, %10, %2\n\tv_mfma_f32_16x16x32_bf16 %3, %5, %11, %3\n\tv_mfma_f32_16x16x32_bf16 %4, %5, %12, %4"
      : "+v"(ac[0][0]), "=&v"(pu[12]), "+v"(ac[0][1]), "+v"(ac[0][2]), "+v"(ac[0][3]) : "v"(pu[13]), "v"(fw2[13][0]), "v"(vFj[1]), "s"(rsW), "s"(su[12]), "v"(fw2[13][1]), "v"(fw2[13][2]), "v"(fw2[13][3]) : "memory"); SB();
  STAMP(40);
  WAITVB(13);
  SB();
  asm volatile("v_mfma_f32_16x16x32_bf16 %0, %5, %6, 0\n\tbuffer_load_dwordx4 %1, %7, %8, %9 offen offset:0 sc1\n\tv_mfma_f32_16x16x32_bf16 %2, %5, %10, 0\n\tv_mfma_f32_16x16x32_bf16 %3, %5, %11, 0\n\tv_mfma_f32_16x16x32_bf16 %4, %5, %12, 0"
      : "=&v"(ac[1][0]), "=&v"(pu[13]), "=&v"(ac[1][1]), "=&v"(ac[1][2]), "=&v"(ac[1][3]) : "v"(pu[0]), "a"(fw2[0][0]), "v"(vFj[1]), "s"(rsW), "s"(su[13]), "a"(fw2[0][1]), "a"(fw2[0][2]), "a"(fw2[0][3]) : "memory"); SB();
  WAITVB(13);
  SB();
  asm volatile("v_mfma_f32_16x16x32_bf16 %0, %1, %2, %0"
      : "+v"(ac[1][0]) : "v"(pu[1]), "a"(fw2[1][0])); SB();
  WAITV1(8, fu2);
  if (__builtin_expect(__ballot(fu2 != nonce) != 0ull, 0)) poll1<4>(rsW, vFU, nonce);
  STAMP(19);
  asm volatile("buffer_load_dwordx4 %0, %4, %5, %6 offen offset:0 sc1\n\tv_mfma_f32_16x16x32_bf16 %1, %7, %8, %1\n\tds_write_b128 %9, %10 offset:0\n\tv_mfma_f32_16x16x32_bf16 %2, %7, %11, %2\n\tv_mfma_f32_16x16x32_bf16 %3, %7, %12, %3"
      : "=&v"(pu[0]), "+v"(ac[1][1]), "+v"(ac[1][2]), "+v"(ac[1][3]) : "v"(vFj[2]), "s"(rsW), "s"(su[0]), "v"(pu[1]), "a"(fw2[1][1]), "v"(rwd), "v"(ac[0][0]), "a"(fw2[1][2]), "a"(fw2[1][3]) : "memory"); SB();
  WAITVB(13);
  SB();
  asm volatile("v_mfma_f32_16x16x32_bf16 %0, %5, %6, %0\n\tbuffer_load_dwordx4 %1, %7, %8, %9 offen offset:0 sc1\n\tv_mfma_f32_16x16x32_bf16 %2, %5, %10, %2\n\tds_write_b128 %11, %12 offset:1024\n\tv_mfma_f32_16x16x32_bf16 %3, %5, %13, %3\n\tv_mfma_f32_16x16x32_bf16 %4, %5, %14, %4"
      : "+v"(ac[1][0]), "=&v"(pu[1]), "+v"(ac[1][1]), "+v"(ac[1][2]), "+v"(ac[1][3]) : "v"(pu[2]), "a"(fw2[2][0]), "v"(vFj[2]), "s"(rsW), "s"(su[1]), "a"(fw2[2][1]), "v"(rwd), "v"(ac[0][1]), "a"(fw2[2][2]), "a"(fw2[2][3]) : "memory"); SB();
  WAITVB(13);
  SB();
  asm volatile("v_mfma_f32_16x16x32_bf16 %0, %5, %6, %0\n\tbuffer_load_dwordx4 %1, %7, %8, %9 offen offset:0 sc1\n\tv_mfma_f32_16x16x32_bf16 %2, %5, %10, %2\n\tds_write_b128 %11, %12 offset:2048\n\tv_mfma_f32_16x16x32_bf16 %3, %5, %13, %3\n\tv_mfma_f32_16x16x32_bf16 %4, %5, %14, %4"
      : "+v"(ac[1][0]), "=&v"(pu[2]), "+v"(ac[1][1]), "+v"(ac[1][2]), "+v"(ac[1][3]) : "v"(pu[3]), "a"(fw2[3][0]), "v"(vFj[2]), "s"(rsW), "s"(su[2]), "a"(fw2[3][1]), "v"(rwd), "v"(ac[0][2]), "a"(fw2[3][2]), "a"(fw2[3][3]) : "memory"); SB();
  WAITVB(13);
  SB();
  asm volatile("v_mfma_f32_16x16x32_bf16 %0, %5, %6, %0\n\tbuffer_load_dwordx4 %1, %7, %8, %9 offen offset:0 sc1\n\tv_mfma_f32_16x16x32_bf16 %2, %5, %10, %2\n\tds_write_b128 %11, %12 offset:3072\n\tv_mfma_f32_16x16x32_bf16 %3, %5, %13, %3\n\tv_mfma_f32_16x16x32_bf16 %4, %5, %14, %4"
      : "+v"(ac[1][0]), "=&v"(pu[3]), "+v"(ac[1][1]), "+v"(ac[1][2]), "+v"(ac[1][3]) : "v"(pu[4]), "a"(fw2[4][0]), "v"(vFj[2]), "s"(rsW), "s"(su[3]), "a"(fw2[4][1]), "v"(rwd), "v"(ac[0][3]), "a"(fw2[4][2]), "a"(fw2[4][3]) : "memory"); SB();
  WAITVB(13);
  SB();
  asm volatile("v_mfma_f32_16x16x32_bf16 %0, %3, %4, %0\n\tbuffer_load_dwordx4 %1, %5, %6, %7 offen offset:0 sc1\n\tv_mfma_f32_16x16x32_bf16 %2, %3, %8, %2"
      : "+v"(ac[1][0]), "=&v"(pu[4]), "+v"(ac[1][1]) : "v"(pu[5]), "a"(fw2[5][0]), "v"(vFj[2]), "s"(rsW), "s"(su[4]), "a"(fw2[5][1]) : "memory"); SB();
  WAITL(0);
  BAR0();
  SB();
  asm volatile("v_mfma_f32_16x16x32_bf16 %0, %5, %6, %0\n\tds_read_b128 %1, %7 offset:0\n\tds_read_b128 %2, %7 offset:4096\n\tv_mfma_f32_16x16x32_bf16 %3, %5, %8, %3\n\tds_read_b128 %4, %7 offset:8192"
      : "+v"(ac[1][2]), "=&v"(py[0][0]), "=&v"(py[0][1]), "+v"(ac[1][3]), "=&v"(py[0][2]) : "v"(pu[5]), "a"(fw2[5][2]), "v"(rr), "a"(fw2[5][3]) : "memory"); SB();
  WAITVB(12);
  SB();
  asm volatile("v_mfma_f32_16x16x32_bf16 %0, %8, %9, %0\n\tbuffer_load_dwordx4 %1, %10, %11, %12 offen offset:0 sc1\n\tv_mfma_f32_16x16x32_bf16 %2, %8, %13, %2\n\tds_read_b128 %3, %14 offset:12288\n\tds_read_b128 %4, %15 offset:0\n\tv_mfma_f32_16x16x32_bf16 %5, %8, %16, %5\n\tv_mfma_f32_16x16x32_bf16 %6, %8, %17, %6\n\tds_read_b128 %7, %15 offset:8192"
      : "+v"(ac[1][0]), "=&v"(pu[5]), "+v"(ac[1][1]), "=&v"(py[0][3]), "=&a"(fwo[0]), "+v"(ac[1][2]), "+v"(ac[1][3]), "=&a"(fwo[1]) : "v"(pu[6]), "a"(fw2[6][0]), "v"(vFj[2]), "s"(rsW), "s"(su[5]), "a"(fw2[6][1]), "v"(rr), "v"(ro), "a"(fw2[6][2]), "a"(fw2[6][3]) : "memory"); SB();
  WAITVB(12);
  SB();
  asm volatile("v_mfma_f32_16x16x32_bf16 %0, %5, %6, %0\n\tbuffer_load_dwordx4 %1, %7, %8, %9 offen offset:0 sc1\n\tv_mfma_f32_16x16x32_bf16 %2, %5, %10, %2\n\tds_read_b128 %3, %11 offset:16384\n\tv_mfma_f32_16x16x32_bf16 %4, %5, %12, %4"
      : "+v"(ac[1][0]), "=&v"(pu[6]), "+v"(ac[1][1]), "=&a"(fwo[2]), "+v"(ac[1][2]) : "v"(pu[7]), "a"(fw2[7][0]), "v"(vFj[2]), "s"(rsW), "s"(su[6]), "a"(fw2[7][1]), "v"(ro), "a"(fw2[7][2]) : "memory"); SB();
  WAITL(3);
  asm volatile("v_add_f32 %0, %3, %4\n\tv_mfma_f32_16x16x32_bf16 %1, %5, %6, %1\n\tv_add_f32 %2, %7, %4"
      : "=&v"(ye[0]), "+v"(ac[1][3]), "=&v"(ye[1]) : "v"(py[0][0][0]), "v"(bias), "v"(pu[7]), "a"(fw2[7][3]), "v"(py[0][0][1])); SB();
  WAITVB(12);
  SB();
  asm volatile("v_mfma_f32_16x16x32_bf16 %0, %9, %10, %0\n\tbuffer_load_dwordx4 %1, %11, %12, %13 offen offset:0 sc1\n\tv_mfma_f32_16x16x32_bf16 %2, %9, %14, %2\n\tv_add_f32 %3, %15, %16\n\tv_add_f32 %4, %17, %16\n\tv_mfma_f32_16x16x32_bf16 %5, %9, %18, %5\n\tv_add_f32 %6, %19, %20\n\tv_add_f32 %7, %21, %22\n\tv_mfma_f32_16x16x32_bf16 %8, %9, %23, %8"
      : "+v"(ac[1][0]), "=&v"(pu[7]), "+v"(ac[1][1]), "=&v"(ye[2]), "=&v"(ye[3]), "+v"(ac[1][2]), "=&v"(ze[0]), "=&v"(ze[1]), "+v"(ac[1][3]) : "v"(pu[8]), "a"(fw2[8][0]), "v"(vFj[2]), "s"(rsW), "s"(su[7]), "a"(fw2[8][1]), "v"(py[0][0][2]), "v"(bias), "v"(py[0][0][3]), "a"(fw2[8][2]), "v"(py[0][1][0]), "v"(py[0][2][0]), "v"(py[0][1][1]), "v"(py[0][2][1]), "a"(fw2[8][3]) : "memory"); SB();
  asm volatile("v_add_f32 %0, %1, %2"
      : "=&v"(ze[2]) : "v"(py[0][1][2]), "v"(py[0][2][2])); SB();
  WAITVB(12);
  SB();
  asm volatile("v_mfma_f32_16x16x32_bf16 %0, %9, %10, %0\n\tbuffer_load_dwordx4 %1, %11, %12, %13 offen offset:0 sc1\n\tv_mfma_f32_16x16x32_bf16 %2, %9, %14, %2\n\tv_add_f32 %3, %15, %16\n\tv_add_f32 %4, %4, %17\n\tv_mfma_f32_16x16x32_bf16 %5, %9, %18, %5\n\tv_add_f32 %6, %6, %19\n\tv_add_f32 %7, %7, %20\n\tv_mfma_f32_16x16x32_bf16 %8, %9, %21, %8"
      : "+v"(ac[1][0]), "=&v"(pu[8]), "+v"(ac[1][1]), "=&v"(ze[3]), "+v"(ye[0]), "+v"(ac[1][2]), "+v"(ye[1]), "+v"(ye[2]), "+v"(ac[1][3]) : "v"(pu[9]), "v"(fw2[9][0]), "v"(vFj[2]), "s"(rsW), "s"(su[8]), "v"(fw2[9][1]), "v"(py[0][1][3]), "v"(py[0][2][3]), "v"(py[0][3][0]), "v"(fw2[9][2]), "v"(py[0][3][1]), "v"(py[0][3][2]), "v"(fw2[9][3]) : "memory"); SB();
  asm volatile("v_add_f32 %0, %0, %1"
      : "+v"(ye[3]) : "v"(py[0][3][3])); SB();
  WAITVB(12);
  SB();
  asm volatile("v_mfma_f32_16x16x32_bf16 %0, %10, %11, %0\n\tbuffer_load_dwordx4 %1, %12, %13, %14 offen offset:0 sc1\n\tv_mfma_f32_16x16x32_bf16 %2, %10, %15, %2\n\tv_add_f32 %3, %3, %4\n\tv_add_f32 %5, %5, %16\n\tv_mfma_f32_16x16x32_bf16 %6, %10, %17, %6\n\tv_add_f32 %7, %7, %18\n\tv_add_f32 %8, %8, %19\n\tv_mfma_f32_16x16x32_bf16 %9, %10, %20, %9\n\tv_mul_f32 %4, 0xbfb8aa3b, %3"
      : "+v"(ac[1][0]), "=&v"(pu[9]), "+v"(ac[1][1]), "+v"(ye[0]), "+v"(ze[0]), "+v"(ye[1]), "+v"(ac[1][2]), "+v"(ye[2]), "+v"(ye[3]), "+v"(ac[1][3]) : "v"(pu[10]), "v"(fw2[10][0]), "v"(vFj[2]), "s"(rsW), "s"(su[9]), "v"(fw2[10][1]), "v"(ze[1]), "v"(fw2[10][2]), "v"(ze[2]), "v"(ze[3]), "v"(fw2[10][3]) : "memory"); SB();
  WAITVB(12);
  SB();
  asm volatile("v_mfma_f32_16x16x32_bf16 %0, %9, %10, %0\n\tbuffer_load_dwordx4 %1, %11, %12, %13 offen offset:0 sc1\n\tv_mfma_f32_16x16x32_bf16 %2, %9, %14, %2\n\tv_mul_f32 %3, 0xbfb8aa3b, %15\n\tv_mul_f32 %4, 0xbfb8aa3b, %16\n\tv_mfma_f32_16x16x32_bf16 %5, %9, %17, %5\n\tds_read_b128 %6, %18 offset:24576\n\tv_mul_f32 %7, 0xbfb8aa3b, %19\n\tv_mfma_f32_16x16x32_bf16 %8, %9, %20, %8"
      : "+v"(ac[1][0]), "=&v"(pu[10]), "+v"(ac[1][1]), "=&v"(ze[1]), "=&v"(ze[2]), "+v"(ac[1][2]), "=&a"(fwo[3]), "=&v"(ze[3]), "+v"(ac[1][3]) : "v"(pu[11]), "v"(fw2[11][0]), "v"(vFj[2]), "s"(rsW), "s"(su[10]), "v"(fw2[11][1]), "v"(ye[1]), "v"(ye[2]), "v"(fw2[11][2]), "v"(ro), "v"(ye[3]), "v"(fw2[11][3]) : "memory"); SB();
  WAITVB(12);
  SB();
  asm volatile("v_mfma_f32_16x16x32_bf16 %0, %8, %9, %0\n\tbuffer_load_dwordx4 %1, %10, %11, %12 offen offset:0 sc1\n\tv_mfma_f32_16x16x32_bf16 %2, %8, %13, %2\n\tv_exp_f32 %3, %3\n\tv_mfma_f32_16x16x32_bf16 %4, %8, %14, %4\n\tv_exp_f32 %5, %5\n\tv_mfma_f32_16x16x32_bf16 %6, %8, %15, %6\n\tds_read_b128 %7, %16 offset:1024"
      : "+v"(ac[1][0]), "=&v"(pu[11]), "+v"(ac[1][1]), "+v"(ze[0]), "+v"(ac[1][2]), "+v"(ze[1]), "+v"(ac[1][3]), "=&a"(fwo[4]) : "v"(pu[12]), "v"(fw2[12][0]), "v"(vFj[2]), "s"(rsW), "s"(su[11]), "v"(fw2[12][1]), "v"(fw2[12][2]), "v"(fw2[12][3]), "v"(ro) : "memory"); SB();
  WAITVB(12);
  SB();
  asm volatile("v_mfma_f32_16x16x32_bf16 %0, %9, %10, %0\n\tbuffer_load_dwordx4 %1, %11, %12, %13 offen offset:0 sc1\n\tv_mfma_f32_16x16x32_bf16 %2, %9, %14, %2\n\tv_exp_f32 %3, %3\n\tv_mfma_f32_16x16x32_bf16 %4, %9, %15, %4\n\tv_exp_f32 %5, %5\n\tv_mfma_f32_16x16x32_bf16 %6, %9, %16, %6\n\tv_add_f32 %7, 1.0, %7\n\tv_add_f32 %8, 1.0, %8"
      : "+v"(ac[1][0]), "=&v"(pu[12]), "+v"(ac[1][1]), "+v"(ze[2]), "+v"(ac[1][2]), "+v"(ze[3]), "+v"(ac[1][3]), "+v"(ze[0]), "+v"(ze[1]) : "v"(pu[13]), "v"(fw2[13][0]), "v"(vFj[2]), "s"(rsW), "s"(su[12]), "v"(fw2[13][1]), "v"(fw2[13][2]), "v"(fw2[13][3]) : "memory"); SB();
  STAMP(41);
  WAITVB(12);
  SB();
  asm volatile("v_mfma_f32_16x16x32_bf16 %0, %9, %10, 0\n\tbuffer_load_dwordx4 %1, %11, %12, %13 offen offset:0 sc1\n\tv_mfma_f32_16x16x32_bf16 %2, %9, %14, 0\n\tbuffer_load_dword %3, %15, %12, 0 offen sc1\n\tv_mfma_f32_16x16x32_bf16 %4, %9, %16, 0\n\tv_add_f32 %5, 1.0, %5\n\tv_add_f32 %6, 1.0, %6\n\tv_mfma_f32_16x16x32_bf16 %7, %9, %17, 0\n\tds_read_b128 %8, %18 offset:9216"
      : "=&v"(ac[0][0]), "=&v"(pu[13]), "=&v"(ac[0][1]), "=&v"(fo), "=&v"(ac[0][2]), "+v"(ze[2]), "+v"(ze[3]), "=&v"(ac[0][3]), "=&a"(fwo[5]) : "v"(pu[0]), "a"(fw2[0][0]), "v"(vFj[2]), "s"(rsW), "s"(su[13]), "a"(fw2[0][1]), "v"(vFO), "a"(fw2[0][2]), "a"(fw2[0][3]), "v"(ro) : "memory"); SB();
  WAITVB(13);
  SB();
  asm volatile("v_mfma_f32_16x16x32_bf16 %0, %7, %8, %0\n\tbuffer_load_dwordx4 %1, %9, %10, %11 offen offset:0 sc1\n\tv_mfma_f32_16x16x32_bf16 %2, %7, %12, %2\n\tds_write_b128 %13, %14 offset:18432\n\tv_mfma_f32_16x16x32_bf16 %3, %7, %15, %3\n\tv_rcp_f32 %4, %4\n\tv_mfma_f32_16x16x32_bf16 %5, %7, %16, %5\n\tds_read_b128 %6, %17 offset:17408"
      : "+v"(ac[0][0]), "=&v"(pu[0]), "+v"(ac[0][1]), "+v"(ac[0][2]), "+v"(ze[0]), "+v"(ac[0][3]), "=&a"(fwo[6]) : "v"(pu[1]), "a"(fw2[1][0]), "v"(vFj[3]), "s"(rsW), "s"(su[0]), "a"(fw2[1][1]), "v"(rwd), "v"(ac[1][0]), "a"(fw2[1][2]), "a"(fw2[1][3]), "v"(ro) : "memory"); SB();
  WAITVB(13);
  SB();
  asm volatile("v_mfma_f32_16x16x32_bf16 %0, %7, %8, %0\n\tbuffer_load_dwordx4 %1, %9, %10, %11 offen offset:0 sc1\n\tv_mfma_f32_16x16x32_bf16 %2, %7, %12, %2\n\tds_write_b128 %13, %14 offset:19456\n\tv_mfma_f32_16x16x32_bf16 %3, %7, %15, %3\n\tv_rcp_f32 %4, %4\n\tv_mfma_f32_16x16x32_bf16 %5, %7, %16, %5\n\tds_read_b128 %6, %17 offset:25600"
      : "+v"(ac[0][0]), "=&v"(pu[1]), "+v"(ac[0][1]), "+v"(ac[0][2]), "+v"(ze[1]), "+v"(ac[0][3]), "=&a"(fwo[7]) : "v"(pu[2]), "a"(fw2[2][0]), "v"(vFj[3]), "s"(rsW), "s"(su[1]), "a"(fw2[2][1]), "v"(rwd), "v"(ac[1][1]), "a"(fw2[2][2]), "a"(fw2[2][3]), "v"(ro) : "memory"); SB();
  WAITVB(13);
  SB();
  asm volatile("v_mfma_f32_16x16x32_bf16 %0, %7, %8, %0\n\tbuffer_load_dwordx4 %1, %9, %10, %11 offen offset:0 sc1\n\tv_mfma_f32_16x16x32_bf16 %2, %7, %12, %2\n\tds_write_b128 %13, %14 offset:20480\n\tv_mfma_f32_16x16x32_bf16 %3, %7, %15, %3\n\tv_rcp_f32 %4, %4\n\tv_mfma_f32_16x16x32_bf16 %5, %7, %16, %5\n\tds_read_b128 %6, %17 offset:2048"
      : "+v"(ac[0][0]), "=&v"(pu[2]), "+v"(ac[0][1]), "+v"(ac[0][2]), "+v"(ze[2]), "+v"(ac[0][3]), "=&a"(fwo[8]) : "v"(pu[3]), "a"(fw2[3][0]), "v"(vFj[3]), "s"(rsW), "s"(su[2]), "a"(fw2[3][1]), "v"(rwd), "v"(ac[1][2]), "a"(fw2[3][2]), "a"(fw2[3][3]), "v"(ro) : "memory"); SB();
  WAITVB(13);
  SB();
  asm volatile("v_mfma_f32_16x16x32_bf16 %0, %7, %8, %0\n\tbuffer_load_dwordx4 %1, %9, %10, %11 offen offset:0 sc1\n\tv_mfma_f32_16x16x32_bf16 %2, %7, %12, %2\n\tds_write_b128 %13, %14 offset:21504\n\tv_mfma_f32_16x16x32_bf16 %3, %7, %15, %3\n\tv_rcp_f32 %4, %4\n\tv_mfma_f32_16x16x32_bf16 %5, %7, %16, %5\n\tv_mul_f32 %6, %17, %18"
      : "+v"(ac[0][0]), "=&v"(pu[3]), "+v"(ac[0][1]), "+v"(ac[0][2]), "+v"(ze[3]), "+v"(ac[0][3]), "=&v"(ge[0]) : "v"(pu[4]), "a"(fw2[4][0]), "v"(vFj[3]), "s"(rsW), "s"(su[3]), "a"(fw2[4][1]), "v"(rwd), "v"(ac[1][3]), "a"(fw2[4][2]), "a"(fw2[4][3]), "v"(ye[0]), "v"(ze[0]) : "memory"); SB();
  WAITVB(13);
  SB();
  asm volatile("v_mfma_f32_16x16x32_bf16 %0, %3, %4, %0\n\tbuffer_load_dwordx4 %1, %5, %6, %7 offen offset:0 sc1\n\tv_mfma_f32_16x16x32_bf16 %2, %3, %8, %2"
      : "+v"(ac[0][0]), "=&v"(pu[4]), "+v"(ac[0][1]) : "v"(pu[5]), "a"(fw2[5][0]), "v"(vFj[3]), "s"(rsW), "s"(su[4]), "a"(fw2[5][1]) : "memory"); SB();
  WAITL(0);
  BAR0();
  SB();
  asm volatile("v_mfma_f32_16x16x32_bf16 %0, %5, %6, %0\n\tds_read_b128 %1, %7 offset:18432\n\tds_read_b128 %2, %7 offset:22528\n\tv_mfma_f32_16x16x32_bf16 %3, %5, %8, %3\n\tds_read_b128 %4, %7 offset:26624"
      : "+v"(ac[0][2]), "=&v"(py[1][0]), "=&v"(py[1][1]), "+v"(ac[0][3]), "=&v"(py[1][2]) : "v"(pu[5]), "a"(fw2[5][2]), "v"(rr), "a"(fw2[5][3]) : "memory"); SB();
  WAITVB(13);
  SB();
  asm volatile("v_mfma_f32_16x16x32_bf16 %0, %10, %11, %0\n\tbuffer_load_dwordx4 %1, %12, %13, %14 offen offset:0 sc1\n\tv_mfma_f32_16x16x32_bf16 %2, %10, %15, %2\n\tds_read_b128 %3, %16 offset:30720\n\tv_mul_f32 %4, %17, %18\n\tv_mfma_f32_16x16x32_bf16 %5, %10, %19, %5\n\tv_mul_f32 %6, %20, %21\n\tv_mul_f32 %7, %22, %23\n\tv_mfma_f32_16x16x32_bf16 %8, %10, %24, %8\n\tds_read_b128 %9, %25 offset:10240"
      : "+v"(ac[0][0]), "=&v"(pu[5]), "+v"(ac[0][1]), "=&v"(py[1][3]), "=&v"(ge[1]), "+v"(ac[0][2]), "=&v"(ge[2]), "=&v"(ge[3]), "+v"(ac[0][3]), "=&a"(fwo[9]) : "v"(pu[6]), "a"(fw2[6][0]), "v"(vFj[3]), "s"(rsW), "s"(su[5]), "a"(fw2[6][1]), "v"(rr), "v"(ye[1]), "v"(ze[1]), "a"(fw2[6][2]), "v"(ye[2]), "v"(ze[2]), "v"(ye[3]), "v"(ze[3]), "a"(fw2[6][3]), "v"(ro) : "memory"); SB();
  WAITVB(13);
  SB();
  asm volatile("v_mfma_f32_16x16x32_bf16 %0, %5, %6, %0\n\tbuffer_load_dwordx4 %1, %7, %8, %9 offen offset:0 sc1\n\tv_mfma_f32_16x16x32_bf16 %2, %5, %10, %2\n\tds_read_b128 %3, %11 offset:18432\n\tv_mfma_f32_16x16x32_bf16 %4, %5, %12, %4"
      : "+v"(ac[0][0]), "=&v"(pu[6]), "+v"(ac[0][1]), "=&a"(fwo[10]), "+v"(ac[0][2]) : "v"(pu[7]), "a"(fw2[7][0]), "v"(vFj[3]), "s"(rsW), "s"(su[6]), "a"(fw2[7][1]), "v"(ro), "a"(fw2[7][2]) : "memory"); SB();
  WAITL(2);
  asm volatile("v_add_f32 %0, %3, %4\n\tv_mfma_f32_16x16x32_bf16 %1, %5, %6, %1\n\tv_add_f32 %2, %7, %4"
      : "=&v"(ye[0]), "+v"(ac[0][3]), "=&v"(ye[1]) : "v"(py[1][0][0]), "v"(bias), "v"(pu[7]), "a"(fw2[7][3]), "v"(py[1][0][1])); SB();
  WAITVB(13);
  SB();
  asm volatile("v_mfma_f32_16x16x32_bf16 %0, %9, %10, %0\n\tbuffer_load_dwordx4 %1, %11, %12, %13 offen offset:0 sc1\n\tv_mfma_f32_16x16x32_bf16 %2, %9, %14, %2\n\tv_add_f32 %3, %15, %16\n\tv_add_f32 %4, %17, %16\n\tv_mfma_f32_16x16x32_bf16 %5, %9, %18, %5\n\tv_add_f32 %6, %19, %20\n\tv_add_f32 %7, %21, %22\n\tv_mfma_f32_16x16x32_bf16 %8, %9, %23, %8"
      : "+v"(ac[0][0]), "=&v"(pu[7]), "+v"(ac[0][1]), "=&v"(ye[2]), "=&v"(ye[3]), "+v"(ac[0][2]), "=&v"(ze[0]), "=&v"(ze[1]), "+v"(ac[0][3]) : "v"(pu[8]), "a"(fw2[8][0]), "v"(vFj[3]), "s"(rsW), "s"(su[7]), "a"(fw2[8][1]), "v"(py[1][0][2]), "v"(bias), "v"(py[1][0][3]), "a"(fw2[8][2]), "v"(py[1][1][0]), "v"(py[1][2][0]), "v"(py[1][1][1]), "v"(py[1][2][1]), "a"(fw2[8][3]) : "memory"); SB();
  asm volatile("v_add_f32 %0, %1, %2"
      : "=&v"(ze[2]) : "v"(py[1][1][2]), "v"(py[1][2][2])); SB();
  WAITVB(13);
  SB();
  asm volatile("v_mfma_f32_16x16x32_bf16 %0, %9, %10, %0\n\tbuffer_load_dwordx4 %1, %11, %12, %13 offen offset:0 sc1\n\tv_mfma_f32_16x16x32_bf16 %2, %9, %14, %2\n\tv_add_f32 %3, %15, %16\n\tv_add_f32 %4, %4, %17\n\tv_mfma_f32_16x16x32_bf16 %5, %9, %18, %5\n\tv_add_f32 %6, %6, %19\n\tv_add_f32 %7, %7, %20\n\tv_mfma_f32_16x16x32_bf16 %8, %9, %21, %8"
      : "+v"(ac[0][0]), "=&v"(pu[8]), "+v"(ac[0][1]), "=&v"(ze[3]), "+v"(ye[0]), "+v"(ac[0][2]), "+v"(ye[1]), "+v"(ye[2]), "+v"(ac[0][3]) : "v"(pu[9]), "v"(fw2[9][0]), "v"(vFj[3]), "s"(rsW), "s"(su[8]), "v"(fw2[9][1]), "v"(py[1][1][3]), "v"(py[1][2][3]), "v"(py[1][3][0]), "v"(fw2[9][2]), "v"(py[1][3][1]), "v"(py[1][3][2]), "v"(fw2[9][3]) : "memory"); SB();
  asm volatile("v_add_f32 %0, %0, %1"
      : "+v"(ye[3]) : "v"(py[1][3][3])); SB();
  WAITVB(13);
  SB();
  asm volatile("v_mfma_f32_16x16x32_bf16 %0, %10, %11, %0\n\tbuffer_load_dwordx4 %1, %12, %13, %14 offen offset:0 sc1\n\tv_mfma_f32_16x16x32_bf16 %2, %10, %15, %2\n\tv_add_f32 %3, %3, %4\n\tv_add_f32 %5, %5, %16\n\tv_mfma_f32_16x16x32_bf16 %6, %10, %17, %6\n\tv_add_f32 %7, %7, %18\n\tv_add_f32 %8, %8, %19\n\tv_mfma_f32_16x16x32_bf16 %9, %10, %20, %9\n\tv_mul_f32 %4, 0xbfb8aa3b, %3"
      : "+v"(ac[0][0]), "=&v"(pu[9]), "+v"(ac[0][1]), "+v"(ye[0]), "+v"(ze[0]), "+v"(ye[1]), "+v"(ac[0][2]), "+v"(ye[2]), "+v"(ye[3]), "+v"(ac[0][3]) : "v"(pu[10]), "v"(fw2[10][0]), "v"(vFj[3]), "s"(rsW), "s"(su[9]), "v"(fw2[10][1]), "v"(ze[1]), "v"(fw2[10][2]), "v"(ze[2]), "v"(ze[3]), "v"(fw2[10][3]) : "memory"); SB();
  WAITVB(13);
  SB();
  asm volatile("v_mfma_f32_16x16x32_bf16 %0, %9, %10, %0\n\tbuffer_load_dwordx4 %1, %11, %12, %13 offen offset:0 sc1\n\tv_mfma_f32_16x16x32_bf16 %2, %9, %14, %2\n\tv_mul_f32 %3, 0xbfb8aa3b, %15\n\tv_mul_f32 %4, 0xbfb8aa3b, %16\n\tv_mfma_f32_16x16x32_bf16 %5, %9, %17, %5\n\tds_read_b128 %6, %18 offset:26624\n\tv_mul_f32 %7, 0xbfb8aa3b, %19\n\tv_mfma_f32_16x16x32_bf16 %8, %9, %20, %8"
      : "+v"(ac[0][0]), "=&v"(pu[10]), "+v"(ac[0][1]), "=&v"(ze[1]), "=&v"(ze[2]), "+v"(ac[0][2]), "=&a"(fwo[11]), "=&v"(ze[3]), "+v"(ac[0][3]) : "v"(pu[11]), "v"(fw2[11][0]), "v"(vFj[3]), "s"(rsW), "s"(su[10]), "v"(fw2[11][1]), "v"(ye[1]), "v"(ye[2]), "v"(fw2[11][2]), "v"(ro), "v"(ye[3]), "v"(fw2[11][3]) : "memory"); SB();
  WAITVB(13);
  SB();
  asm volatile("v_mfma_f32_16x16x32_bf16 %0, %8, %9, %0\n\tbuffer_load_dwordx4 %1, %10, %11, %12 offen offset:0 sc1\n\tv_mfma_f32_16x16x32_bf16 %2, %8, %13, %2\n\tv_exp_f32 %3, %3\n\tv_mfma_f32_16x16x32_bf16 %4, %8, %14, %4\n\tv_exp_f32 %5, %5\n\tv_mfma_f32_16x16x32_bf16 %6, %8, %15, %6\n\tds_read_b128 %7, %16 offset:3072"
      : "+v"(ac[0][0]), "=&v"(pu[11]), "+v"(ac[0][1]), "+v"(ze[0]), "+v"(ac[0][2]), "+v"(ze[1]), "+v"(ac[0][3]), "=&a"(fwo[12]) : "v"(pu[12]), "v"(fw2[12][0]), "v"(vFj[3]), "s"(rsW), "s"(su[11]), "v"(fw2[12][1]), "v"(fw2[12][2]), "v"(fw2[12][3]), "v"(ro) : "memory"); SB();
  WAITVB(13);
  SB();
  asm volatile("v_mfma_f32_16x16x32_bf16 %0, %9, %10, %0\n\tbuffer_load_dwordx4 %1, %11, %12, %13 offen offset:0 sc1\n\tv_mfma_f32_16x16x32_bf16 %2, %9, %14, %2\n\tv_exp_f32 %3, %3\n\tv_mfma_f32_16x16x32_bf16 %4, %9, %15, %4\n\tv_exp_f32 %5, %5\n\tv_mfma_f32_16x16x32_bf16 %6, %9, %16, %6\n\tv_add_f32 %7, 1.0, %7\n\tv_add_f32 %8, 1.0, %8"
      : "+v"(ac[0][0]), "=&v"(pu[12]), "+v"(ac[0][1]), "+v"(ze[2]), "+v"(ac[0][2]), "+v"(ze[3]), "+v"(ac[0][3]), "+v"(ze[0]), "+v"(ze[1]) : "v"(pu[13]), "v"(fw2[13][0]), "v"(vFj[3]), "s"(rsW), "s"(su[12]), "v"(fw2[13][1]), "v"(fw2[13][2]), "v"(fw2[13][3]) : "memory"); SB();
  STAMP(42);
  WAITVB(12);
  SB();
  asm volatile("v_mfma_f32_16x16x32_bf16 %0, %9, %10, 0\n\tbuffer_load_dwordx4 %1, %11, %12, %13 offen offset:0 sc1\n\tv_mfma_f32_16x16x32_bf16 %2, %9, %14, 0\n\tv_add_f32 %3, 1.0, %3\n\tv_add_f32 %4, 1.0, %4\n\tv_mfma_f32_16x16x32_bf16 %5, %9, %15, 0\n\tv_rcp_f32 %6, %6\n\tv_mfma_f32_16x16x32_bf16 %7, %9, %16, 0\n\tds_read_b128 %8, %17 offset:11264"
      : "=&v"(ac[1][0]), "=&v"(pu[13]), "=&v"(ac[1][1]), "+v"(ze[2]), "+v"(ze[3]), "=&v"(ac[1][2]), "+v"(ze[0]), "=&v"(ac[1][3]), "=&a"(fwo[13]) : "v"(pu[0]), "a"(fw2[0][0]), "v"(vFj[3]), "s"(rsW), "s"(su[13]), "a"(fw2[0][1]), "a"(fw2[0][2]), "a"(fw2[0][3]), "v"(ro) : "memory"); SB();
  WAITVB(12);
  SB();
  asm volatile("v_mfma_f32_16x16x32_bf16 %0, %1, %2, %0"
      : "+v"(ac[1][0]) : "v"(pu[1]), "a"(fw2[1][0])); SB();
  WAITV1(14, fo);
  if (__builtin_expect(__ballot(fo != nonce) != 0ull, 0)) poll1<4>(rsW, vFO, nonce);
  STAMP(18);
  asm volatile("buffer_load_dwordx4 %0, %6, %7, %8 offen offset:0 sc1\n\tv_mfma_f32_16x16x32_bf16 %1, %9, %10, %1\n\tds_write_b128 %11, %12 offset:0\n\tv_mfma_f32_16x16x32_bf16 %2, %9, %13, %2\n\tv_rcp_f32 %3, %3\n\tv_mfma_f32_16x16x32_bf16 %4, %9, %14, %4\n\tds_read_b128 %5, %15 offset:19456"
      : "=&v"(pu[0]), "+v"(ac[1][1]), "+v"(ac[1][2]), "+v"(ze[1]), "+v"(ac[1][3]), "=&a"(fwo[14]) : "v"(vF), "s"(rsW), "s"(so0), "v"(pu[1]), "a"(fw2[1][1]), "v"(rwd), "v"(ac[0][0]), "a"(fw2[1][2]), "a"(fw2[1][3]), "v"(ro) : "memory"); SB();
  WAITVB(12);
  SB();
  asm volatile("v_mfma_f32_16x16x32_bf16 %0, %7, %8, %0\n\tbuffer_load_dwordx4 %1, %9, %10, %11 offen offset:128 sc1\n\tv_mfma_f32_16x16x32_bf16 %2, %7, %12, %2\n\tds_write_b128 %13, %14 offset:1024\n\tv_mfma_f32_16x16x32_bf16 %3, %7, %15, %3\n\tv_rcp_f32 %4, %4\n\tv_mfma_f32_16x16x32_bf16 %5, %7, %16, %5\n\tds_read_b128 %6, %17 offset:27648"
      : "+v"(ac[1][0]), "=&v"(pu[1]), "+v"(ac[1][1]), "+v"(ac[1][2]), "+v"(ze[2]), "+v"(ac[1][3]), "=&a"(fwo[15]) : "v"(pu[2]), "a"(fw2[2][0]), "v"(vF), "s"(rsW), "s"(so0), "a"(fw2[2][1]), "v"(rwd), "v"(ac[0][1]), "a"(fw2[2][2]), "a"(fw2[2][3]), "v"(ro) : "memory"); SB();
  WAITVB(12);
  SB();
  asm volatile("v_mfma_f32_16x16x32_bf16 %0, %7, %8, %0\n\tbuffer_load_dwordx4 %1, %9, %10, %11 offen offset:256 sc1\n\tv_mfma_f32_16x16x32_bf16 %2, %7, %12, %2\n\tds_write_b128 %13, %14 offset:2048\n\tv_mfma_f32_16x16x32_bf16 %3, %7, %15, %3\n\tv_rcp_f32 %4, %4\n\tv_mfma_f32_16x16x32_bf16 %5, %7, %16, %5\n\tv_mul_f32 %6, %17, %18"
      : "+v"(ac[1][0]), "=&v"(pu[2]), "+v"(ac[1][1]), "+v"(ac[1][2]), "+v"(ze[3]), "+v"(ac[1][3]), "=&v"(ge[4]) : "v"(pu[3]), "a"(fw2[3][0]), "v"(vF), "s"(rsW), "s"(so0), "a"(fw2[3][1]), "v"(rwd), "v"(ac[0][2]), "a"(fw2[3][2]), "a"(fw2[3][3]), "v"(ye[0]), "v"(ze[0]) : "memory"); SB();
  WAITVB(12);
  SB();
  asm volatile("v_mfma_f32_16x16x32_bf16 %0, %8, %9, %0\n\tbuffer_load_dwordx4 %1, %10, %11, %12 offen offset:384 sc1\n\tv_mfma_f32_16x16x32_bf16 %2, %8, %13, %2\n\tds_write_b128 %14, %15 offset:3072\n\tv_mfma_f32_16x16x32_bf16 %3, %8, %16, %3\n\tv_mul_f32 %4, %17, %18\n\tv_mul_f32 %5, %19, %20\n\tv_mfma_f32_16x16x32_bf16 %6, %8, %21, %6\n\tv_mul_f32 %7, %22, %23"
      : "+v"(ac[1][0]), "=&v"(pu[3]), "+v"(ac[1][1]), "+v"(ac[1][2]), "=&v"(ge[5]), "=&v"(ge[6]), "+v"(ac[1][3]), "=&v"(ge[7]) : "v"(pu[4]), "a"(fw2[4][0]), "v"(vF), "s"(rsW), "s"(so0), "a"(fw2[4][1]), "v"(rwd), "v"(ac[0][3]), "a"(fw2[4][2]), "v"(ye[1]), "v"(ze[1]), "v"(ye[2]), "v"(ze[2]), "a"(fw2[4][3]), "v"(ye[3]), "v"(ze[3]) : "memory"); SB();
  WAITVB(12);
  SB();
  asm volatile("v_mfma_f32_16x16x32_bf16 %0, %3, %4, %0\n\tbuffer_load_dwordx4 %1, %5, %6, %7 offen offset:512 sc1\n\tv_mfma_f32_16x16x32_bf16 %2, %3, %8, %2"
      : "+v"(ac[1][0]), "=&v"(pu[4]), "+v"(ac[1][1]) : "v"(pu[5]), "a"(fw2[5][0]), "v"(vF), "s"(rsW), "s"(so0), "a"(fw2[5][1]) : "memory"); SB();
  WAITL(0);
  BAR0();
  SB();
  asm volatile("v_mfma_f32_16x16x32_bf16 %0, %5, %6, %0\n\tds_read_b128 %1, %7 offset:0\n\tds_read_b128 %2, %7 offset:4096\n\tv_mfma_f32_16x16x32_bf16 %3, %5, %8, %3\n\tds_read_b128 %4, %7 offset:8192"
      : "+v"(ac[1][2]), "=&v"(py[2][0]), "=&v"(py[2][1]), "+v"(ac[1][3]), "=&v"(py[2][2]) : "v"(pu[5]), "a"(fw2[5][2]), "v"(rr), "a"(fw2[5][3]) : "memory"); SB();
  WAITVB(12);
  SB();
  asm volatile("v_mfma_f32_16x16x32_bf16 %0, %8, %9, %0\n\tbuffer_load_dwordx4 %1, %10, %11, %12 offen offset:640 sc1\n\tv_mfma_f32_16x16x32_bf16 %2, %8, %13, %2\n\tds_read_b128 %3, %14 offset:12288\n\tds_read_b128 %4, %15 offset:4096\n\tv_mfma_f32_16x16x32_bf16 %5, %8, %16, %5\n\tv_mfma_f32_16x16x32_bf16 %6, %8, %17, %6\n\tds_read_b128 %7, %15 offset:12288"
      : "+v"(ac[1][0]), "=&v"(pu[5]), "+v"(ac[1][1]), "=&v"(py[2][3]), "=&a"(fwo[16]), "+v"(ac[1][2]), "+v"(ac[1][3]), "=&a"(fwo[17]) : "v"(pu[6]), "a"(fw2[6][0]), "v"(vF), "s"(rsW), "s"(so0), "a"(fw2[6][1]), "v"(rr), "v"(ro), "a"(fw2[6][2]), "a"(fw2[6][3]) : "memory"); SB();
  WAITVB(12);
  SB();
  asm volatile("v_mfma_f32_16x16x32_bf16 %0, %5, %6, %0\n\tbuffer_load_dwordx4 %1, %7, %8, %9 offen offset:768 sc1\n\tv_mfma_f32_16x16x32_bf16 %2, %5, %10, %2\n\tds_read_b128 %3, %11 offset:20480\n\tv_mfma_f32_16x16x32_bf16 %4, %5, %12, %4"
      : "+v"(ac[1][0]), "=&v"(pu[6]), "+v"(ac[1][1]), "=&a"(fwo[18]), "+v"(ac[1][2]) : "v"(pu[7]), "a"(fw2[7][0]), "v"(vF), "s"(rsW), "s"(so0), "a"(fw2[7][1]), "v"(ro), "a"(fw2[7][2]) : "memory"); SB();
  WAITL(3);
  asm volatile("v_add_f32 %0, %3, %4\n\tv_mfma_f32_16x16x32_bf16 %1, %5, %6, %1\n\tv_add_f32 %2, %7, %4"
      : "=&v"(ye[0]), "+v"(ac[1][3]), "=&v"(ye[1]) : "v"(py[2][0][0]), "v"(bias), "v"(pu[7]), "a"(fw2[7][3]), "v"(py[2][0][1])); SB();
  WAITVB(12);
  SB();
  asm volatile("v_mfma_f32_16x16x32_bf16 %0, %9, %10, %0\n\tbuffer_load_dwordx4 %1, %11, %12, %13 offen offset:0 sc1\n\tv_mfma_f32_16x16x32_bf16 %2, %9, %14, %2\n\tv_add_f32 %3, %15, %16\n\tv_add_f32 %4, %17, %16\n\tv_mfma_f32_16x16x32_bf16 %5, %9, %18, %5\n\tv_add_f32 %6, %19, %20\n\tv_add_f32 %7, %21, %22\n\tv_mfma_f32_16x16x32_bf16 %8, %9, %23, %8"
      : "+v"(ac[1][0]), "=&v"(pu[7]), "+v"(ac[1][1]), "=&v"(ye[2]), "=&v"(ye[3]), "+v"(ac[1][2]), "=&v"(ze[0]), "=&v"(ze[1]), "+v"(ac[1][3]) : "v"(pu[8]), "a"(fw2[8][0]), "v"(vF), "s"(rsW), "s"(so1), "a"(fw2[8][1]), "v"(py[2][0][2]), "v"(bias), "v"(py[2][0][3]), "a"(fw2[8][2]), "v"(py[2][1][0]), "v"(py[2][2][0]), "v"(py[2][1][1]), "v"(py[2][2][1]), "a"(fw2[8][3]) : "memory"); SB();
  asm volatile("v_add_f32 %0, %1, %2"
      : "=&v"(ze[2]) : "v"(py[2][1][2]), "v"(py[2][2][2])); SB();
  WAITVB(12);
  SB();
  asm volatile("v_mfma_f32_16x16x32_bf16 %0, %9, %10, %0\n\tbuffer_load_dwordx4 %1, %11, %12, %13 offen offset:128 sc1\n\tv_mfma_f32_16x16x32_bf16 %2, %9, %14, %2\n\tv_add_f32 %3, %15, %16\n\tv_add_f32 %4, %4, %17\n\tv_mfma_f32_16x16x32_bf16 %5, %9, %18, %5\n\tv_add_f32 %6, %6, %19\n\tv_add_f32 %7, %7, %20\n\tv_mfma_f32_16x16x32_bf16 %8, %9, %21, %8"
      : "+v"(ac[1][0]), "=&v"(pu[8]), "+v"(ac[1][1]), "=&v"(ze[3]), "+v"(ye[0]), "+v"(ac[1][2]), "+v"(ye[1]), "+v"(ye[2]), "+v"(ac[1][3]) : "v"(pu[9]), "v"(fw2[9][0]), "v"(vF), "s"(rsW), "s"(so1), "v"(fw2[9][1]), "v"(py[2][1][3]), "v"(py[2][2][3]), "v"(py[2][3][0]), "v"(fw2[9][2]), "v"(py[2][3][1]), "v"(py[2][3][2]), "v"(fw2[9][3]) : "memory"); SB();
  asm volatile("v_add_f32 %0, %0, %1"
      : "+v"(ye[3]) : "v"(py[2][3][3])); SB();
  WAITVB(12);
  SB();
  asm volatile("v_mfma_f32_16x16x32_bf16 %0, %10, %11, %0\n\tbuffer_load_dwordx4 %1, %12, %13, %14 offen offset:256 sc1\n\tv_mfma_f32_16x16x32_bf16 %2, %10, %15, %2\n\tv_add_f32 %3, %3, %4\n\tv_add_f32 %5, %5, %16\n\tv_mfma_f32_16x16x32_bf16 %6, %10, %17, %6\n\tv_add_f32 %7, %7, %18\n\tv_add_f32 %8, %8, %19\n\tv_mfma_f32_16x16x32_bf16 %9, %10, %20, %9\n\tv_mul_f32 %4, 0xbfb8aa3b, %3"
      : "+v"(ac[1][0]), "=&v"(pu[9]), "+v"(ac[1][1]), "+v"(ye[0]), "+v"(ze[0]), "+v"(ye[1]), "+v"(ac[1][2]), "+v"(ye[2]), "+v"(ye[3]), "+v"(ac[1][3]) : "v"(pu[10]), "v"(fw2[10][0]), "v"(vF), "s"(rsW), "s"(so1), "v"(fw2[10][1]), "v"(ze[1]), "v"(fw2[10][2]), "v"(ze[2]), "v"(ze[3]), "v"(fw2[10][3]) : "memory"); SB();
  WAITVB(12);
  SB();
  asm volatile("v_mfma_f32_16x16x32_bf16 %0, %9, %10, %0\n\tbuffer_load_dwordx4 %1, %11, %12, %13 offen offset:384 sc1\n\tv_mfma_f32_16x16x32_bf16 %2, %9, %14, %2\n\tv_mul_f32 %3, 0xbfb8aa3b, %15\n\tv_mul_f32 %4, 0xbfb8aa3b, %16\n\tv_mfma_f32_16x16x32_bf16 %5, %9, %17, %5\n\tds_read_b128 %6, %18 offset:28672\n\tv_mul_f32 %7, 0xbfb8aa3b, %19\n\tv_mfma_f32_16x16x32_bf16 %8, %9, %20, %8"
      : "+v"(ac[1][0]), "=&v"(pu[10]), "+v"(ac[1][1]), "=&v"(ze[1]), "=&v"(ze[2]), "+v"(ac[1][2]), "=&a"(fwo[19]), "=&v"(ze[3]), "+v"(ac[1][3]) : "v"(pu[11]), "v"(fw2[11][0]), "v"(vF), "s"(rsW), "s"(so1), "v"(fw2[11][1]), "v"(ye[1]), "v"(ye[2]), "v"(fw2[11][2]), "v"(ro), "v"(ye[3]), "v"(fw2[11][3]) : "memory"); SB();
  WAITVB(12);
  SB();
  asm volatile("v_mfma_f32_16x16x32_bf16 %0, %8, %9, %0\n\tbuffer_load_dwordx4 %1, %10, %11, %12 offen offset:512 sc1\n\tv_mfma_f32_16x16x32_bf16 %2, %8, %13, %2\n\tv_exp_f32 %3, %3\n\tv_mfma_f32_16x16x32_bf16 %4, %8, %14, %4\n\tv_exp_f32 %5, %5\n\tv_mfma_f32_16x16x32_bf16 %6, %8, %15, %6\n\tds_read_b128 %7, %16 offset:5120"
      : "+v"(ac[1][0]), "=&v"(pu[11]), "+v"(ac[1][1]), "+v"(ze[0]), "+v"(ac[1][2]), "+v"(ze[1]), "+v"(ac[1][3]), "=&a"(fwo[20]) : "v"(pu[12]), "v"(fw2[12][0]), "v"(vF), "s"(rsW), "s"(so1), "v"(fw2[12][1]), "v"(fw2[12][2]), "v"(fw2[12][3]), "v"(ro) : "memory"); SB();
  WAITVB(12);
  SB();
  asm volatile("v_mfma_f32_16x16x32_bf16 %0, %9, %10, %0\n\tbuffer_load_dwordx4 %1, %11, %12, %13 offen offset:640 sc1\n\tv_mfma_f32_16x16x32_bf16 %2, %9, %14, %2\n\tv_exp_f32 %3, %3\n\tv_mfma_f32_16x16x32_bf16 %4, %9, %15, %4\n\tv_exp_f32 %5, %5\n\tv_mfma_f32_16x16x32_bf16 %6, %9, %16, %6\n\tv_add_f32 %7, 1.0, %7\n\tv_add_f32 %8, 1.0, %8"
      : "+v"(ac[1][0]), "=&v"(pu[12]), "+v"(ac[1][1]), "+v"(ze[2]), "+v"(ac[1][2]), "+v"(ze[3]), "+v"(ac[1][3]), "+v"(ze[0]), "+v"(ze[1]) : "v"(pu[13]), "v"(fw2[13][0]), "v"(vF), "s"(rsW), "s"(so1), "v"(fw2[13][1]), "v"(fw2[13][2]), "v"(fw2[13][3]) : "memory"); SB();
  STAMP(43);
  WAITVB(12);
  SB();
  asm volatile("v_mfma_f32_16x16x32_bf16 %0, %9, %10, 0\n\tbuffer_load_dwordx4 %1, %11, %12, %13 offen offset:768 sc1\n\tv_mfma_f32_16x16x32_bf16 %2, %14, %10, 0\n\tv_add_f32 %3, 1.0, %3\n\tv_add_f32 %4, 1.0, %4\n\tv_mfma_f32_16x16x32_bf16 %5, %15, %10, 0\n\tv_rcp_f32 %6, %6\n\tv_mfma_f32_16x16x32_bf16 %7, %16, %10, 0\n\tds_read_b128 %8, %17 offset:13312"
      : "=&v"(as[0]), "=&v"(pu[13]), "=&v"(as[1]), "+v"(ze[2]), "+v"(ze[3]), "=&v"(as[2]), "+v"(ze[0]), "=&v"(as[3]), "=&a"(fwo[21]) : "a"(fwo[0]), "v"(pu[0]), "v"(vF), "s"(rsW), "s"(so1), "a"(fwo[1]), "a"(fwo[2]), "a"(fwo[3]), "v"(ro) : "memory"); SB();
  WAITVB(12);
  SB();
  asm volatile("v_mfma_f32_16x16x32_bf16 %0, %7, %8, %0\n\tds_write_b128 %9, %10 offset:18432\n\tv_mfma_f32_16x16x32_bf16 %1, %11, %8, %1\n\tv_rcp_f32 %2, %2\n\tv_mfma_f32_16x16x32_bf16 %3, %12, %8, %3\n\tv_rcp_f32 %4, %4\n\tv_mfma_f32_16x16x32_bf16 %5, %13, %8, %5\n\tds_read_b128 %6, %14 offset:21504"
      : "+v"(as[0]), "+v"(as[1]), "+v"(ze[1]), "+v"(as[2]), "+v"(ze[2]), "+v"(as[3]), "=&a"(fwo[22]) : "a"(fwo[4]), "v"(pu[1]), "v"(rwd), "v"(ac[1][0]), "a"(fwo[5]), "a"(fwo[6]), "a"(fwo[7]), "v"(ro) : "memory"); SB();
  WAITVB(11);
  SB();
  asm volatile("v_mfma_f32_16x16x32_bf16 %0, %8, %9, %0\n\tds_write_b128 %10, %11 offset:19456\n\tv_mfma_f32_16x16x32_bf16 %1, %12, %9, %1\n\tv_rcp_f32 %2, %2\n\tv_mfma_f32_16x16x32_bf16 %3, %13, %9, %3\n\tv_mul_f32 %4, %14, %15\n\tv_mul_f32 %5, %16, %17\n\tv_mfma_f32_16x16x32_bf16 %6, %18, %9, %6\n\tv_mul_f32 %7, %19, %20"
      : "+v"(as[0]), "+v"(as[1]), "+v"(ze[3]), "+v"(as[2]), "=&v"(ge[8]), "=&v"(ge[9]), "+v"(as[3]), "=&v"(ge[10]) : "a"(fwo[8]), "v"(pu[2]), "v"(rwd), "v"(ac[1][1]), "a"(fwo[9]), "a"(fwo[10]), "v"(ye[0]), "v"(ze[0]), "v"(ye[1]), "v"(ze[1]), "a"(fwo[11]), "v"(ye[2]), "v"(ze[2]) : "memory"); SB();
  WAITVB(10);
  SB();
  asm volatile("v_mfma_f32_16x16x32_bf16 %0, %6, %7, %0\n\tds_write_b128 %8, %9 offset:20480\n\tv_mfma_f32_16x16x32_bf16 %1, %10, %7, %1\n\tds_read_b128 %2, %11 offset:29696\n\tv_mul_f32 %3, %12, %13\n\tv_mfma_f32_16x16x32_bf16 %4, %14, %7, %4\n\tv_mfma_f32_16x16x32_bf16 %5, %15, %7, %5"
      : "+v"(as[0]), "+v"(as[1]), "=&a"(fwo[23]), "=&v"(ge[11]), "+v"(as[2]), "+v"(as[3]) : "a"(fwo[12]), "v"(pu[3]), "v"(rwd), "v"(ac[1][2]), "a"(fwo[13]), "v"(ro), "v"(ye[3]), "v"(ze[3]), "a"(fwo[14]), "a"(fwo[15]) : "memory"); SB();
  WAITVB(9);
  asm volatile("ds_read_b128 %0, %6 offset:6144\n\tds_read_b128 %1, %6 offset:14336\n\tds_read_b128 %2, %6 offset:22528\n\tds_read_b128 %3, %6 offset:30720\n\tds_read_b128 %4, %6 offset:7168\n\tds_read_b128 %5, %6 offset:15360"
      : "=&a"(fwo[24]), "=&a"(fwo[25]), "=&a"(fwo[26]), "=&a"(fwo[27]), "=&a"(fwo[28]), "=&a"(fwo[29]) : "v"(ro) : "memory"); SB();
  WAITL(6);
  SB();
  asm volatile("v_mfma_f32_16x16x32_bf16 %0, %6, %7, %0\n\tds_write_b128 %8, %9 offset:21504\n\tv_mfma_f32_16x16x32_bf16 %1, %10, %7, %1\n\tds_read_b128 %2, %11 offset:23552\n\tv_mfma_f32_16x16x32_bf16 %3, %12, %7, %3\n\tv_mfma_f32_16x16x32_bf16 %4, %13, %7, %4\n\tds_read_b128 %5, %11 offset:31744"
      : "+v"(as[0]), "+v"(as[1]), "=&a"(fwo[30]), "+v"(as[2]), "+v"(as[3]), "=&a"(fwo[31]) : "a"(fwo[16]), "v"(pu[4]), "v"(rwd), "v"(ac[1][3]), "a"(fwo[17]), "v"(ro), "a"(fwo[18]), "a"(fwo[19]) : "memory"); SB();
  WAITVB(8);
  SB();
  asm volatile("v_mfma_f32_16x16x32_bf16 %0, %1, %2, %0"
      : "+v"(as[0]) : "a"(fwo[20]), "v"(pu[5])); SB();
  WAITL(2);
  BAR0();
  SB();
  asm volatile("v_mfma_f32_16x16x32_bf16 %0, %7, %8, %0\n\tds_read_b128 %1, %9 offset:18432\n\tds_read_b128 %2, %9 offset:22528\n\tv_mfma_f32_16x16x32_bf16 %3, %10, %8, %3\n\tds_read_b128 %4, %9 offset:26624\n\tds_read_b128 %5, %9 offset:30720\n\tv_mfma_f32_16x16x32_bf16 %6, %11, %8, %6"
      : "+v"(as[1]), "=&v"(py[3][0]), "=&v"(py[3][1]), "+v"(as[2]), "=&v"(py[3][2]), "=&v"(py[3][3]), "+v"(as[3]) : "a"(fwo[21]), "v"(pu[5]), "v"(rr), "a"(fwo[22]), "a"(fwo[23]) : "memory"); SB();
  WAITVB(7);
  asm volatile("ds_read_b128 %0, %6 offset:32768\n\tds_read_b128 %1, %6 offset:38912\n\tds_read_b128 %2, %6 offset:45056\n\tds_read_b128 %3, %6 offset:51200\n\tds_read_b128 %4, %6 offset:33792\n\tds_read_b128 %5, %6 offset:39936"
      : "=&a"(fwo[32]), "=&a"(fwo[33]), "=&a"(fwo[34]), "=&a"(fwo[35]), "=&a"(fwo[36]), "=&a"(fwo[37]) : "v"(ro) : "memory"); SB();
  WAITL(10);
  SB();
  asm volatile("v_mfma_f32_16x16x32_bf16 %0, %6, %7, %0\n\tds_read_b128 %1, %8 offset:46080\n\tv_mfma_f32_16x16x32_bf16 %2, %9, %7, %2\n\tv_mfma_f32_16x16x32_bf16 %3, %10, %7, %3\n\tds_read_b128 %4, %8 offset:52224\n\tv_mfma_f32_16x16x32_bf16 %5, %11, %7, %5"
      : "+v"(as[0]), "=&a"(fwo[38]), "+v"(as[1]), "+v"(as[2]), "=&a"(fwo[39]), "+v"(as[3]) : "a"(fwo[24]), "v"(pu[6]), "v"(ro), "a"(fwo[25]), "a"(fwo[26]), "a"(fwo[27]) : "memory"); SB();
  WAITL(8);
  SB();
  WAITVB(6);
  SB();
  asm volatile("v_mfma_f32_16x16x32_bf16 %0, %10, %11, %0\n\tv_add_f32 %1, %12, %13\n\tv_add_f32 %2, %14, %13\n\tv_mfma_f32_16x16x32_bf16 %3, %15, %11, %3\n\tv_add_f32 %4, %16, %13\n\tv_add_f32 %5, %17, %13\n\tv_mfma_f32_16x16x32_bf16 %6, %18, %11, %6\n\tv_add_f32 %7, %19, %20\n\tv_add_f32 %8, %21, %22\n\tv_mfma_f32_16x16x32_bf16 %9, %23, %11, %9"
      : "+v"(as[0]), "=&v"(ye[0]), "=&v"(ye[1]), "+v"(as[1]), "=&v"(ye[2]), "=&v"(ye[3]), "+v"(as[2]), "=&v"(ze[0]), "=&v"(ze[1]), "+v"(as[3]) : "a"(fwo[28]), "v"(pu[7]), "v"(py[3][0][0]), "v"(bias), "v"(py[3][0][1]), "a"(fwo[29]), "v"(py[3][0][2]), "v"(py[3][0][3]), "a"(fwo[30]), "v"(py[3][1][0]), "v"(py[3][2][0]), "v"(py[3][1][1]), "v"(py[3][2][1]), "a"(fwo[31])); SB();
  WAITVB(5);
  asm volatile("ds_read_b128 %0, %6 offset:34816\n\tds_read_b128 %1, %6 offset:40960\n\tds_read_b128 %2, %6 offset:47104\n\tds_read_b128 %3, %6 offset:53248\n\tds_read_b128 %4, %6 offset:35840\n\tds_read_b128 %5, %6 offset:41984"
      : "=&a"(fwo[40]), "=&a"(fwo[41]), "=&a"(fwo[42]), "=&a"(fwo[43]), "=&a"(fwo[44]), "=&a"(fwo[45]) : "v"(ro) : "memory"); SB();
  WAITL(6);
  SB();
  asm volatile("v_mfma_f32_16x16x32_bf16 %0, %9, %10, %0\n\tv_add_f32 %1, %11, %12\n\tv_add_f32 %2, %13, %14\n\tv_mfma_f32_16x16x32_bf16 %3, %15, %10, %3\n\tv_add_f32 %4, %4, %16\n\tv_add_f32 %5, %5, %17\n\tv_mfma_f32_16x16x32_bf16 %6, %18, %10, %6\n\tv_add_f32 %7, %7, %19\n\tv_add_f32 %8, %8, %20"
      : "+v"(as[0]), "=&v"(ze[2]), "=&v"(ze[3]), "+v"(as[1]), "+v"(ye[0]), "+v"(ye[1]), "+v"(as[2]), "+v"(ye[2]), "+v"(ye[3]) : "a"(fwo[32]), "v"(pu[8]), "v"(py[3][1][2]), "v"(py[3][2][2]), "v"(py[3][1][3]), "v"(py[3][2][3]), "a"(fwo[33]), "v"(py[3][3][0]), "v"(py[3][3][1]), "a"(fwo[34]), "v"(py[3][3][2]), "v"(py[3][3][3])); SB();
  asm volatile("v_mfma_f32_16x16x32_bf16 %0, %2, %3, %0\n\tv_add_f32 %1, %1, %4"
      : "+v"(as[3]), "+v"(ye[0]) : "a"(fwo[35]), "v"(pu[8]), "v"(ze[0])); SB();
  WAITVB(4);
  SB();
  asm volatile("v_mfma_f32_16x16x32_bf16 %0, %10, %11, %0\n\tv_add_f32 %1, %1, %2\n\tv_add_f32 %3, %3, %4\n\tv_mfma_f32_16x16x32_bf16 %5, %12, %11, %5\n\tv_add_f32 %6, %6, %13\n\tv_mul_f32 %7, 0xbfb8aa3b, %14\n\tv_mfma_f32_16x16x32_bf16 %8, %15, %11, %8\n\tv_mul_f32 %2, 0xbfb8aa3b, %1\n\tv_mul_f32 %4, 0xbfb8aa3b, %3\n\tv_mfma_f32_16x16x32_bf16 %9, %16, %11, %9"
      : "+v"(as[0]), "+v"(ye[1]), "+v"(ze[1]), "+v"(ye[2]), "+v"(ze[2]), "+v"(as[1]), "+v"(ye[3]), "=&v"(ze[0]), "+v"(as[2]), "+v"(as[3]) : "a"(fwo[36]), "v"(pu[9]), "a"(fwo[37]), "v"(ze[3]), "v"(ye[0]), "a"(fwo[38]), "a"(fwo[39])); SB();
  WAITVB(3);
  asm volatile("ds_read_b128 %0, %8 offset:48128\n\tds_read_b128 %1, %8 offset:54272\n\tds_read_b128 %2, %8 offset:36864\n\tds_read_b128 %3, %8 offset:43008\n\tds_read_b128 %4, %8 offset:49152\n\tds_read_b128 %5, %8 offset:55296\n\tds_read_b128 %6, %8 offset:37888\n\tds_read_b128 %7, %8 offset:44032"
      : "=&a"(fwo[46]), "=&a"(fwo[47]), "=&a"(fwo[48]), "=&a"(fwo[49]), "=&a"(fwo[50]), "=&a"(fwo[51]), "=&a"(fwo[52]), "=&a"(fwo[53]) : "v"(ro) : "memory"); SB();
  WAITL(6);
  SB();
  asm volatile("v_mfma_f32_16x16x32_bf16 %0, %9, %10, %0\n\tds_read_b128 %1, %11 offset:50176\n\tv_mul_f32 %2, 0xbfb8aa3b, %12\n\tv_mfma_f32_16x16x32_bf16 %3, %13, %10, %3\n\tv_exp_f32 %4, %4\n\tv_mfma_f32_16x16x32_bf16 %5, %14, %10, %5\n\tv_exp_f32 %6, %6\n\tv_mfma_f32_16x16x32_bf16 %7, %15, %10, %7\n\tds_read_b128 %8, %11 offset:56320"
      : "+v"(as[0]), "=&a"(fwo[54]), "=&v"(ze[3]), "+v"(as[1]), "+v"(ze[0]), "+v"(as[2]), "+v"(ze[1]), "+v"(as[3]), "=&a"(fwo[55]) : "a"(fwo[40]), "v"(pu[10]), "v"(ro), "v"(ye[3]), "a"(fwo[41]), "a"(fwo[42]), "a"(fwo[43]) : "memory"); SB();
  WAITL(0);
  WAITVB(0);
  SB();
  asm volatile("v_mfma_f32_16x16x32_bf16 %0, %9, %10, %0\n\tv_exp_f32 %1, %1\n\tv_mfma_f32_16x16x32_bf16 %0, %11, %12, %0\n\tv_exp_f32 %2, %2\n\tv_mfma_f32_16x16x32_bf16 %0, %13, %14, %0\n\tv_add_f32 %3, 1.0, %3\n\tv_add_f32 %4, 1.0, %4\n\tv_mfma_f32_16x16x32_bf16 %5, %15, %10, %5\n\tv_add_f32 %1, 1.0, %1\n\tv_add_f32 %2, 1.0, %2\n\tv_mfma_f32_16x16x32_bf16 %5, %16, %12, %5\n\tv_rcp_f32 %3, %3\n\tv_mfma_f32_16x16x32_bf16 %5, %17, %14, %5\n\tv_rcp_f32 %4, %4\n\tv_mfma_f32_16x16x32_bf16 %6, %18, %10, %6\n\tv_rcp_f32 %1, %1\n\tv_mfma_f32_16x16x32_bf16 %6, %19, %12, %6\n\tv_rcp_f32 %2, %2\n\tv_mfma_f32_16x16x32_bf16 %6, %20, %14, %6\n\tv_mul_f32 %7, %21, %3\n\tv_mul_f32 %8, %22, %4"
      : "+v"(as[0]), "+v"(ze[2]), "+v"(ze[3]), "+v"(ze[0]), "+v"(ze[1]), "+v"(as[1]), "+v"(as[2]), "=&v"(ge[12]), "=&v"(ge[13]) : "a"(fwo[44]), "v"(pu[11]), "a"(fwo[48]), "v"(pu[12]), "a"(fwo[52]), "v"(pu[13]), "a"(fwo[45]), "a"(fwo[49]), "a"(fwo[53]), "a"(fwo[46]), "a"(fwo[50]), "a"(fwo[54]), "v"(ye[0]), "v"(ye[1])); SB();
  asm volatile("v_mfma_f32_16x16x32_bf16 %0, %7, %8, %0\n\tv_mul_f32 %1, %9, %10\n\tv_mul_f32 %2, %11, %12\n\tv_mfma_f32_16x16x32_bf16 %0, %13, %14, %0\n\tv_fma_f32 %3, %15, %16, %3\n\tv_fma_f32 %4, %17, %18, %4\n\tv_mfma_f32_16x16x32_bf16 %0, %19, %20, %0\n\tv_fma_f32 %5, %21, %22, %5\n\tv_fma_f32 %6, %23, %24, %6"
      : "+v"(as[3]), "=&v"(ge[14]), "=&v"(ge[15]), "+v"(xe[0]), "+v"(xe[1]), "+v"(xe[2]), "+v"(xe[3]) : "a"(fwo[47]), "v"(pu[11]), "v"(ye[2]), "v"(ze[2]), "v"(ye[3]), "v"(ze[3]), "a"(fwo[51]), "v"(pu[12]), "v"(as[0][0]), "v"(ge[0]), "v"(as[0][1]), "v"(ge[1]), "a"(fwo[55]), "v"(pu[13]), "v"(as[0][2]), "v"(ge[2]), "v"(as[0][3]), "v"(ge[3])); SB();
  STAMP(44);
  asm volatile("v_cvt_pk_bf16_f32 %0, %8, %9\n\tv_cvt_pk_bf16_f32 %1, %10, %11\n\tv_fma_f32 %2, %12, %13, %2\n\tv_fma_f32 %3, %14, %15, %3\n\tv_fma_f32 %4, %16, %17, %4\n\tv_fma_f32 %5, %18, %19, %5\n\tv_cvt_pk_bf16_f32 %6, %2, %3\n\tv_cvt_pk_bf16_f32 %7, %4, %5"
      : "=&v"(ov[0][0]), "=&v"(ov[0][1]), "+v"(xe[4]), "+v"(xe[5]), "+v"(xe[6]), "+v"(xe[7]), "=&v"(ov[1][0]), "=&v"(ov[1][1]) : "v"(xe[0]), "v"(xe[1]), "v"(xe[2]), "v"(xe[3]), "v"(as[1][0]), "v"(ge[4]), "v"(as[1][1]), "v"(ge[5]), "v"(as[1][2]), "v"(ge[6]), "v"(as[1][3]), "v"(ge[7])); SB();
  asm volatile("ds_write_b64 %8, %9 offset:0\n\tv_fma_f32 %0, %10, %11, %0\n\tv_fma_f32 %1, %12, %13, %1\n\tv_fma_f32 %2, %14, %15, %2\n\tv_fma_f32 %3, %16, %17, %3\n\tv_cvt_pk_bf16_f32 %4, %0, %1\n\tv_cvt_pk_bf16_f32 %5, %2, %3\n\tds_write_b64 %18, %19 offset:0\n\tv_fma_f32 %6, %20, %21, %6\n\tv_fma_f32 %7, %22, %23, %7"
      : "+v"(xe[8]), "+v"(xe[9]), "+v"(xe[10]), "+v"(xe[11]), "=&v"(ov[2][0]), "=&v"(ov[2][1]), "+v"(xe[12]), "+v"(xe[13]) : "v"(xta[0]), "v"(ov[0]), "v"(as[2][0]), "v"(ge[8]), "v"(as[2][1]), "v"(ge[9]), "v"(as[2][2]), "v"(ge[10]), "v"(as[2][3]), "v"(ge[11]), "v"(xta[1]), "v"(ov[1]), "v"(as[3][0]), "v"(ge[12]), "v"(as[3][1]), "v"(ge[13]) : "memory"); SB();
  asm volatile("v_fma_f32 %0, %4, %5, %0\n\tv_fma_f32 %1, %6, %7, %1\n\tv_cvt_pk_bf16_f32 %2, %8, %9\n\tv_cvt_pk_bf16_f32 %3, %0, %1\n\tds_write_b64 %10, %11 offset:0"
      : "+v"(xe[14]), "+v"(xe[15]), "=&v"(ov[3][0]), "=&v"(ov[3][1]) : "v"(as[3][2]), "v"(ge[14]), "v"(as[3][3]), "v"(ge[15]), "v"(xe[12]), "v"(xe[13]), "v"(xta[2]), "v"(ov[2]) : "memory"); SB();
  asm volatile("ds_write_b64 %0, %1 offset:0"
      :  : "v"(xta[3]), "v"(ov[3]) : "memory"); SB();
  { u32x4 t0_, t1_; DSR16(t0_, xo, 0); DSR16(t1_, xo, 1024); WAITLB(1);
    BS16(vXa, rsO_, 0, t0_, "sc1"); WAITLB(0); BS16(vXb, rsO_, 1024, t1_, "sc1"); }
  STAMP(23);

  STAMP(12);
  STAMP_DUMP();
  }
  } else if (jb < 28) {
  asm volatile("; NEED B");
  u32x4 rs1 = srd(w1, 2048u * 3584u);
  STAMP(1);
  unsigned nonce0; asm volatile("s_load_dword %0, %1, 0x0" : "=&s"(nonce0) : "s"(ws) : "memory");
  const unsigned trb = (8 * g4 + q4) * 128 + pq * 8, trf = ((q4 >> 1) | ((g4 & 1) << 1)) * 32;
  const unsigned vF = r8 * 1024 + (lane & 7) * 16;
  unsigned tX[4];
#pragma unroll
  for (int i = 0; i < 4; ++i) { tX[i] = ldsR + trb + ((i * 32 ^ w * 32) ^ trf); OPV(tX[i]); }
  bf16x8 fw1[16][4], p[14]; u32x2 bv; unsigned sh[16], sh0 = WS_HT + d4 * 524288; OPQ(sh0);
  unsigned nonce; asm volatile("s_waitcnt lgkmcnt(0)\n\ts_add_u32 %0, %1, 1" : "=s"(nonce) : "s"(nonce0) : "memory", "scc");
  asm volatile("s_waitcnt vmcnt(8)"
      :  :  : "memory"); SB();
  fw1[0][0] = tr8u(tX[0] + 0, 512);
  fw1[0][1] = tr8u(tX[1] + 0, 512);
  fw1[0][2] = tr8u(tX[2] + 0, 512);
  fw1[0][3] = tr8u(tX[3] + 0, 512);
  STAMP(50);
  { int k_ = KS1(3); OPQ(k_); DMA4(vW1a, vW1b, rs1, k_ * 114688, 55296, ldsR, 12288); sh[3] = sh0 + k_ * 8192; OPQ(sh[3]); }
  SB();
  { const unsigned vB = (ft * 64 + w * 16 + g4 * 4) * 2;
    LD8(bv, vB, b1, 0); }
  u32x4 rsW = srd((char*)(((size_t)ws + 8191) & ~(size_t)4095) + 512, WS_END); OPQ(rsW);
  #pragma unroll
  for (int c = 0; c < 3; ++c) { sh[c] = sh0 + KS1(c) * 8192; OPQ(sh[c]); }
  unsigned fh, vFH = WS_FA + (d4 * 128 + w * 32 + (lane & 31)) * (4 * 16);
  unsigned rw = lds0 + w * SCW + vL16, rr1 = lds0 + (w ^ 1) * SCW + vL16, rr2 = lds0 + (w ^ 2) * SCW + vL16, rr3 = lds0 + (w ^ 3) * SCW + vL16;
  unsigned vU = (g4 & 1) * 128 + ((g4 >> 1) * 2 + (n16 >> 3)) * 1024 + (n16 & 7) * 16, sU, sFU = WS_FU + (d4 * 128 + ft * 4 + w) * (4 * 4);
  sU = WS_UT + (d4 * 56 + 2 * ft) * 8192 + w * 4096;
  const int sp1 = (fi & 1);
  unsigned vHa = vF + (d4 == 1 ? 128 : 0) + 128 * sp1, vHc = vF + (d4 == 1 ? 128 : 0) + 128 - 128 * sp1, vHb = vF + (d4 == 1 ? 0 : 768), vU6 = vU + (d4 == 1 ? 0 : 768); vU = (vU ^ (128 * sp1)) + (d4 == 1 ? 128 : 0); OPV(vHa); OPV(vHc); OPV(vHb); OPV(vU6);
  float gc = 0.10294324f;
  asm volatile("" : "+v"(vFH), "+v"(rw), "+v"(rr1), "+v"(rr2), "+v"(rr3), "+v"(vU), "+v"(gc)); asm volatile("" : "+s"(sU), "+s"(sFU));
  const int r2 = (3 - (int)((size_t)w2 >> 7)) & 7;
  const int st = 8 * xq + ((r2 + 1 + fi) & 7), dt = d4, db0 = 16 + 3 * dt;
#undef BLK
#define BLK(j) (db0 + ((j) == 2 ? 2 - 2 * (dt & 1) : ((j) ^ 0) + (dt & 1)))
  SB();
  asm volatile("s_waitcnt vmcnt(9)"
      :  :  : "memory"); SB();
  fw1[1][0] = tr8u(tX[0] + 4096, 512);
  fw1[1][1] = tr8u(tX[1] + 4096, 512);
  fw1[1][2] = tr8u(tX[2] + 4096, 512);
  fw1[1][3] = tr8u(tX[3] + 4096, 512);
  { int k_ = KS1(4); OPQ(k_); DMA4(vW1a, vW1b, rs1, k_ * 114688, 55296, ldsR, 16384); sh[4] = sh0 + k_ * 8192; OPQ(sh[4]); }
  SB();
  u32x4 rs2 = srd(w2, 1792u * 4096u); OPQ(rs2);
  unsigned vW2[4];
#pragma unroll
  for (int i = 0; i < 4; ++i) vW2[i] = (8 * (r8 & 3) + 4 * (r8 >> 2)) * 4096 + st * 128 + ((lane & 7) ^ (2 * i)) * 16 + i * (4096 - 1024);
  int rot2 = 4 * dt + 2 * (st & 7) + 2; rot2 -= 14 * ((rot2 * 37) >> 9);
  bf16x8 fw2[14][4];
  unsigned ldsS = lds0 + S3B + w * 4096, ldsO = lds0 + WOB + w * 8192, ldsP = lds0 + WOB + 24576 + w * 6144, sWo, vWo = r8 * 16384 + (lane & 7) * 16, sWt;
  { const int k_ = BLK(w), k7 = (k_ * 37) >> 8; sWo = WS_WO + k7 * (131072 - 896) + k_ * 128; }
  unsigned fq, vFQ = WS_FQ + (lane < 56 ? lane : 55) * (4 * 4);
  asm volatile("" : "+v"(vW2[0]), "+v"(vW2[1]), "+v"(vW2[2]), "+v"(vW2[3]), "+v"(vFQ), "+v"(vWo)); asm volatile("" : "+s"(rot2), "+s"(ldsS), "+s"(ldsO), "+s"(ldsP), "+s"(sWo));
  if (w == 3) vWo = 0x40000000u; OPV(vWo);
  unsigned tS[4];
#pragma unroll
  for (int i = 0; i < 4; ++i) { tS[i] = ldsS + q4 * 1024 + g4 * 128 + pq * 8 + ((i ^ q4) * 32); OPV(tS[i]); }
  unsigned tO[4];
#pragma unroll
  for (int i = 0; i < 4; ++i) { tO[i] = ldsO + q4 * 1024 + g4 * 128 + pq * 8 + ((i ^ q4) * 32); OPV(tO[i]); }
  unsigned su[14], sw[14], sc_ = 14 * w + rot2; OPQ(sc_);
  SB();
  asm volatile("s_waitcnt vmcnt(9)"
      :  :  : "memory"); SB();
  fw1[2][0] = tr8u(tX[0] + 8192, 512);
  fw1[2][1] = tr8u(tX[1] + 8192, 512);
  fw1[2][2] = tr8u(tX[2] + 8192, 512);
  fw1[2][3] = tr8u(tX[3] + 8192, 512);
  { int k_ = KS1(5); OPQ(k_); DMA4(vW1a, vW1b, rs1, k_ * 114688, 55296, ldsR, 20480); sh[5] = sh0 + k_ * 8192; OPQ(sh[5]); }
  SB();
  unsigned vFU, vFO;
  { const int da = (db0 * 37) >> 8, db = ((db0 + 2) * 37) >> 8, l_ = lane < 56 ? lane : 55;
    vFU = WS_FU + ((l_ < 28 ? da * 128 + l_ : db * 128 + l_ - 28) + w * 28) * (4 * 4);
    vFO = WS_FO + ((2 * st + (w >> 1)) * 8 + (lane & 7)) * (4 * 16);
    asm volatile("" : "+v"(vFU), "+v"(vFO)); }
  unsigned vFU1 = WS_FU + (512 + ((db0 * 37) >> 8) * 128 + w * 28 + (lane < 28 ? lane : 27)) * (4 * 4), fu2; OPV(vFU1);
  unsigned vFj[4], so0 = WS_OT + (4 * st + w) * 16384, so1 = so0 + 8192;
#pragma unroll
  for (int j = 0; j < 3; ++j) { const int k_ = BLK(j), k7 = (k_ * 37) >> 8; vFj[j] = vF + (WS_UT + k7 * (56 * 8192 - 896) + k_ * 128); OPV(vFj[j]); }
  asm volatile("" : "+s"(so0), "+s"(so1));
  SB();
  asm volatile("s_waitcnt vmcnt(9)"
      :  :  : "memory"); SB();
  fw1[3][0] = tr8u(tX[0] + 12288, 512);
  fw1[3][1] = tr8u(tX[1] + 12288, 512);
  fw1[3][2] = tr8u(tX[2] + 12288, 512);
  fw1[3][3] = tr8u(tX[3] + 12288, 512);
  STAMP(51);
  { int k_ = KS1(6); OPQ(k_); DMA4(vW1a, vW1b, rs1, k_ * 114688, 55296, ldsR, 24576); sh[6] = sh0 + k_ * 8192; OPQ(sh[6]); }
  SB();
  const int tau = 2 * (n16 & 7) + (n16 >> 3);
  unsigned rwd = ldsS + vL16, rr = lds0 + S3B + w * 1024 + (16 * g4 + tau) * 16, ro = lds0 + WOB + vL16;
  unsigned xo = ldsS + 34816 + vL16, braw, xt = ldsS + 34816 + tau * 128 + (((g4 >> 1) ^ (n16 & 7)) * 16) + (g4 & 1) * 8, vB2 = (64 * st + 16 * w + tau) * 2;
  unsigned xs = xt ^ (32 * 0);
  asm volatile("" : "+v"(rwd), "+v"(rr), "+v"(ro), "+v"(xt), "+v"(xs), "+v"(xo), "+v"(vB2));
  SB();
  asm volatile("s_waitcnt vmcnt(8)"
      :  :  : "memory"); SB();
  fw1[4][0] = tr8u(tX[0] + 16384, 512);
  fw1[4][1] = tr8u(tX[1] + 16384, 512);
  fw1[4][2] = tr8u(tX[2] + 16384, 512);
  fw1[4][3] = tr8u(tX[3] + 16384, 512);
  WAITL(8);
  { int k_ = KS1(7); OPQ(k_); DMA4(vW1a, vW1b, rs1, k_ * 114688, 55296, ldsR, 0); sh[7] = sh0 + k_ * 8192; OPQ(sh[7]); }
  SB();
  unsigned vXa, vXb;
  { const int ga = (lane & 7) ^ (r8 >> 1), gb = ga ^ 4, r_ = (64 * st + 16 * w + r8) * 896;
    vXa = ga < 4 ? r_ + (db0 + (dt & 1)) * 32 + ga * 16 : ga < 6 ? r_ + BLK(2) * 32 + (ga - 4) * 16 : 0x40000000;
    vXb = (gb < 4 ? r_ + (db0 + (dt & 1)) * 32 + gb * 16 : gb < 6 ? r_ + BLK(2) * 32 + (gb - 4) * 16 : 0x40000000) + (8 * 896 - 1024); OPV(vXa); OPV(vXb); }
  SB();
  asm volatile("s_waitcnt vmcnt(8)"
      :  :  : "memory"); SB();
  fw1[5][0] = tr8u(tX[0] + 20480, 512);
  fw1[5][1] = tr8u(tX[1] + 20480, 512);
  fw1[5][2] = tr8u(tX[2] + 20480, 512);
  fw1[5][3] = tr8u(tX[3] + 20480, 512);
  WAITL(8);
  { int k_ = KS1(8); OPQ(k_); DMA4(vW1a, vW1b, rs1, k_ * 114688, 55296, ldsR, 4096); sh[8] = sh0 + k_ * 8192; OPQ(sh[8]); }
  SB();
  { unsigned k_ = sc_ + (rot2 >= 14 ? -14 : 0); OPQ(k_); su[0] = k_ << 13; sw[0] = k_ << 17; OPQ(su[0]); OPQ(sw[0]); }
  { unsigned k_ = sc_ + (rot2 >= 13 ? -13 : 1); OPQ(k_); su[1] = k_ << 13; sw[1] = k_ << 17; OPQ(su[1]); OPQ(sw[1]); }
  { unsigned k_ = sc_ + (rot2 >= 12 ? -12 : 2); OPQ(k_); su[2] = k_ << 13; sw[2] = k_ << 17; OPQ(su[2]); OPQ(sw[2]); }
  SB();
  asm volatile("s_waitcnt vmcnt(8)"
      :  :  : "memory"); SB();
  fw1[6][0] = tr8u(tX[0] + 24576, 512);
  fw1[6][1] = tr8u(tX[1] + 24576, 512);
  fw1[6][2] = tr8u(tX[2] + 24576, 512);
  fw1[6][3] = tr8u(tX[3] + 24576, 512);
  WAITV1(20, bv);
  f32x4 bb = {lo2f(bv[0]), hi2f(bv[0]), lo2f(bv[1]), hi2f(bv[1])}; OPV(bb);
  WAITL(8);
  { int k_ = KS1(9); OPQ(k_); DMA4(vW1a, vW1b, rs1, k_ * 114688, 55296, ldsR, 8192); sh[9] = sh0 + k_ * 8192; OPQ(sh[9]); }
  SB();
  { unsigned k_ = sc_ + (rot2 >= 11 ? -11 : 3); OPQ(k_); su[3] = k_ << 13; sw[3] = k_ << 17; OPQ(su[3]); OPQ(sw[3]); }
  { unsigned k_ = sc_ + (rot2 >= 10 ? -10 : 4); OPQ(k_); su[4] = k_ << 13; sw[4] = k_ << 17; OPQ(su[4]); OPQ(sw[4]); }
  { unsigned k_ = sc_ + (rot2 >= 9 ? -9 : 5); OPQ(k_); su[5] = k_ << 13; sw[5] = k_ << 17; OPQ(su[5]); OPQ(sw[5]); }
  SB();
  asm volatile("s_waitcnt vmcnt(8)"
      :  :  : "memory"); SB();
  fw1[7][0] = tr8u(tX[0] + 0, 512);
  fw1[7][1] = tr8u(tX[1] + 0, 512);
  fw1[7][2] = tr8u(tX[2] + 0, 512);
  fw1[7][3] = tr8u(tX[3] + 0, 512);
  STAMP(52);
  WAITL(8);
  { int k_ = KS1(10); OPQ(k_); DMA4(vW1a, vW1b, rs1, k_ * 114688, 55296, ldsR, 12288); sh[10] = sh0 + k_ * 8192; OPQ(sh[10]); }
  SB();
  { unsigned k_ = sc_ + (rot2 >= 8 ? -8 : 6); OPQ(k_); su[6] = k_ << 13; sw[6] = k_ << 17; OPQ(su[6]); OPQ(sw[6]); }
  { unsigned k_ = sc_ + (rot2 >= 7 ? -7 : 7); OPQ(k_); su[7] = k_ << 13; sw[7] = k_ << 17; OPQ(su[7]); OPQ(sw[7]); }
  { unsigned k_ = sc_ + (rot2 >= 6 ? -6 : 8); OPQ(k_); su[8] = k_ << 13; sw[8] = k_ << 17; OPQ(su[8]); OPQ(sw[8]); }
  SB();
  asm volatile("s_waitcnt vmcnt(8)"
      :  :  : "memory"); SB();
  fw1[8][0] = tr8u(tX[0] + 4096, 512);
  fw1[8][1] = tr8u(tX[1] + 4096, 512);
  fw1[8][2] = tr8u(tX[2] + 4096, 512);
  fw1[8][3] = tr8u(tX[3] + 4096, 512);
  WAITL(8);
  { int k_ = KS1(11); OPQ(k_); DMA4(vW1a, vW1b, rs1, k_ * 114688, 55296, ldsR, 16384); sh[11] = sh0 + k_ * 8192; OPQ(sh[11]); }
  SB();
  { unsigned k_ = sc_ + (rot2 >= 5 ? -5 : 9); OPQ(k_); su[9] = k_ << 13; sw[9] = k_ << 17; OPQ(su[9]); OPQ(sw[9]); }
  { unsigned k_ = sc_ + (rot2 >= 4 ? -4 : 10); OPQ(k_); su[10] = k_ << 13; sw[10] = k_ << 17; OPQ(su[10]); OPQ(sw[10]); }
  { unsigned k_ = sc_ + (rot2 >= 3 ? -3 : 11); OPQ(k_); su[11] = k_ << 13; sw[11] = k_ << 17; OPQ(su[11]); OPQ(sw[11]); }
  SB();
  asm volatile("s_waitcnt vmcnt(8)"
      :  :  : "memory"); SB();
  fw1[9][0] = tr8u(tX[0] + 8192, 512);
  fw1[9][1] = tr8u(tX[1] + 8192, 512);
  fw1[9][2] = tr8u(tX[2] + 8192, 512);
  fw1[9][3] = tr8u(tX[3] + 8192, 512);
  WAITL(8);
  { int k_ = KS1(12); OPQ(k_); DMA4(vW1a, vW1b, rs1, k_ * 114688, 55296, ldsR, 20480); sh[12] = sh0 + k_ * 8192; OPQ(sh[12]); }
  SB();
  { unsigned k_ = sc_ + (rot2 >= 2 ? -2 : 12); OPQ(k_); su[12] = k_ << 13; sw[12] = k_ << 17; OPQ(su[12]); OPQ(sw[12]); }
  { unsigned k_ = sc_ + (rot2 >= 1 ? -1 : 13); OPQ(k_); su[13] = k_ << 13; sw[13] = k_ << 17; OPQ(su[13]); OPQ(sw[13]); }
  SB();
  asm volatile("s_waitcnt vmcnt(8)"
      :  :  : "memory"); SB();
  fw1[10][0] = tr8u(tX[0] + 12288, 512);
  fw1[10][1] = tr8u(tX[1] + 12288, 512);
  fw1[10][2] = tr8u(tX[2] + 12288, 512);
  fw1[10][3] = tr8u(tX[3] + 12288, 512);
  WAITL(8);
  { int k_ = KS1(13); OPQ(k_); DMA4(vW1a, vW1b, rs1, k_ * 114688, 55296, ldsR, 24576); sh[13] = sh0 + k_ * 8192; OPQ(sh[13]); }
  asm volatile("s_waitcnt vmcnt(8)"
      :  :  : "memory"); SB();
  fw1[11][0] = tr8u(tX[0] + 16384, 512);
  fw1[11][1] = tr8u(tX[1] + 16384, 512);
  fw1[11][2] = tr8u(tX[2] + 16384, 512);
  fw1[11][3] = tr8u(tX[3] + 16384, 512);
  STAMP(53);
  WAITL(8);
  { int k_ = KS1(14); OPQ(k_); DMA4(vW1a, vW1b, rs1, k_ * 114688, 55296, ldsR, 0); sh[14] = sh0 + k_ * 8192; OPQ(sh[14]); }
  asm volatile("s_waitcnt vmcnt(8)"
      :  :  : "memory"); SB();
  fw1[12][0] = tr8u(tX[0] + 20480, 512);
  fw1[12][1] = tr8u(tX[1] + 20480, 512);
  fw1[12][2] = tr8u(tX[2] + 20480, 512);
  fw1[12][3] = tr8u(tX[3] + 20480, 512);
  WAITL(8);
  { int k_ = KS1(15); OPQ(k_); DMA4(vW1a, vW1b, rs1, k_ * 114688, 55296, ldsR, 4096); sh[15] = sh0 + k_ * 8192; OPQ(sh[15]); }
  asm volatile("buffer_load_dword %0, %1, %2, 0 offen sc1\n\ts_waitcnt vmcnt(9)"
      : "=&v"(fh) : "v"(vFH), "s"(rsW) : "memory"); SB();
  fw1[13][0] = tr8u(tX[0] + 24576, 512);
  fw1[13][1] = tr8u(tX[1] + 24576, 512);
  fw1[13][2] = tr8u(tX[2] + 24576, 512);
  fw1[13][3] = tr8u(tX[3] + 24576, 512);
  WAITV1(0, fh);
  if (__ballot(fh != nonce) != 0ull) poll1<4>(rsW, vFH, nonce);
  STAMP(6);
  asm volatile("buffer_load_dwordx4 %0, %4, %5, %6 offen offset:0 sc1\n\tbuffer_load_dwordx4 %1, %4, %5, %7 offen offset:0 sc1\n\tbuffer_load_dwordx4 %2, %4, %5, %8 offen offset:0 sc1\n\tbuffer_load_dwordx4 %3, %4, %5, %9 offen offset:0 sc1\n\ts_waitcnt vmcnt(9)"
      : "=&v"(p[0]), "=&v"(p[1]), "=&v"(p[2]), "=&v"(p[3]) : "v"(vHa), "s"(rsW), "s"(sh[0]), "s"(sh[1]), "s"(sh[2]), "s"(sh[3]) : "memory"); SB();
  fw1[14][0] = tr8u(tX[0] + 0, 512);
  fw1[14][1] = tr8u(tX[1] + 0, 512);
  fw1[14][2] = tr8u(tX[2] + 0, 512);
  fw1[14][3] = tr8u(tX[3] + 0, 512);
  asm volatile("buffer_load_dwordx4 %0, %4, %5, %6 offen offset:0 sc1\n\tbuffer_load_dwordx4 %1, %4, %5, %7 offen offset:0 sc1\n\tbuffer_load_dwordx4 %2, %4, %5, %8 offen offset:0 sc1\n\tbuffer_load_dwordx4 %3, %4, %5, %9 offen offset:0 sc1\n\ts_waitcnt vmcnt(9)"
      : "=&v"(p[4]), "=&v"(p[5]), "=&v"(p[6]), "=&v"(p[7]) : "v"(vHa), "s"(rsW), "s"(sh[4]), "s"(sh[5]), "s"(sh[6]), "s"(sh[7]) : "memory"); SB();
  fw1[15][0] = tr8u(tX[0] + 4096, 512);
  fw1[15][1] = tr8u(tX[1] + 4096, 512);
  fw1[15][2] = tr8u(tX[2] + 4096, 512);
  fw1[15][3] = tr8u(tX[3] + 4096, 512);
  STAMP(5);
  asm volatile("buffer_load_dwordx4 %0, %4, %5, %6 offen offset:0 sc1\n\tbuffer_load_dwordx4 %1, %4, %5, %7 offen offset:0 sc1\n\tbuffer_load_dwordx4 %2, %4, %5, %8 offen offset:0 sc1\n\tbuffer_load_dwordx4 %3, %4, %5, %9 offen offset:0 sc1"
      : "=&v"(p[8]), "=&v"(p[9]), "=&v"(p[10]), "=&v"(p[11]) : "v"(vHa), "s"(rsW), "s"(sh[8]), "s"(sh[9]), "s"(sh[10]), "s"(sh[11]) : "memory"); SB();
  f32x4 acc[7][4], pm[7][3]; float gm[4], gz[4]; unsigned uv[8][2];
  WAITVB(11);
  SB();
  asm volatile("v_mfma_f32_16x16x32_bf16 %0, %6, %7, 0\n\tbuffer_load_dwordx4 %1, %8, %9, %10 offen offset:0 sc1\n\tv_mfma_f32_16x16x32_bf16 %2, %11, %7, 0\n\tv_mfma_f32_16x16x32_bf16 %3, %12, %7, 0\n\tbuffer_load_dwordx4 %4, %8, %9, %13 offen offset:0 sc1\n\tv_mfma_f32_16x16x32_bf16 %5, %14, %7, %15"
      : "=&v"(acc[0][1]), "=&v"(p[12]), "=&v"(acc[0][2]), "=&v"(acc[0][3]), "=&v"(p[13]), "=&v"(acc[0][0]) : "a"(fw1[0][1]), "v"(p[0]), "v"(vHa), "s"(rsW), "s"(sh[12]), "a"(fw1[0][2]), "a"(fw1[0][3]), "s"(sh[13]), "a"(fw1[0][0]), "v"(bb) : "memory"); SB();
  WAITVB(12);
  SB();
  asm volatile("v_mfma_f32_16x16x32_bf16 %0, %5, %6, %0\n\tbuffer_load_dwordx4 %1, %7, %8, %9 offen offset:0 sc1\n\tv_mfma_f32_16x16x32_bf16 %2, %10, %6, %2\n\tv_mfma_f32_16x16x32_bf16 %3, %11, %6, %3\n\tv_mfma_f32_16x16x32_bf16 %4, %12, %6, %4"
      : "+v"(acc[0][1]), "=&v"(p[0]), "+v"(acc[0][2]), "+v"(acc[0][3]), "+v"(acc[0][0]) : "a"(fw1[1][1]), "v"(p[1]), "v"(vHa), "s"(rsW), "s"(sh[14]), "a"(fw1[1][2]), "a"(fw1[1][3]), "a"(fw1[1][0]) : "memory"); SB();
  WAITVB(12);
  SB();
  asm volatile("v_mfma_f32_16x16x32_bf16 %0, %5, %6, %0\n\tbuffer_load_dwordx4 %1, %7, %8, %9 offen offset:0 sc1\n\tv_mfma_f32_16x16x32_bf16 %2, %10, %6, %2\n\tv_mfma_f32_16x16x32_bf16 %3, %11, %6, %3\n\tv_mfma_f32_16x16x32_bf16 %4, %12, %6, %4"
      : "+v"(acc[0][1]), "=&v"(p[1]), "+v"(acc[0][2]), "+v"(acc[0][3]), "+v"(acc[0][0]) : "a"(fw1[2][1]), "v"(p[2]), "v"(vHa), "s"(rsW), "s"(sh[15]), "a"(fw1[2][2]), "a"(fw1[2][3]), "a"(fw1[2][0]) : "memory"); SB();
  WAITVB(12);
  SB();
  asm volatile("v_mfma_f32_16x16x32_bf16 %0, %5, %6, %0\n\tbuffer_load_dwordx4 %1, %7, %8, %9 offen offset:0 sc1\n\tv_mfma_f32_16x16x32_bf16 %2, %10, %6, %2\n\tv_mfma_f32_16x16x32_bf16 %3, %11, %6, %3\n\tv_mfma_f32_16x16x32_bf16 %4, %12, %6, %4"
      : "+v"(acc[0][1]), "=&v"(p[2]), "+v"(acc[0][2]), "+v"(acc[0][3]), "+v"(acc[0][0]) : "a"(fw1[3][1]), "v"(p[3]), "v"(vHc), "s"(rsW), "s"(sh[0]), "a"(fw1[3][2]), "a"(fw1[3][3]), "a"(fw1[3][0]) : "memory"); SB();
  WAITVB(12);
  SB();
  asm volatile("v_mfma_f32_16x16x32_bf16 %0, %5, %6, %0\n\tbuffer_load_dwordx4 %1, %7, %8, %9 offen offset:0 sc1\n\tv_mfma_f32_16x16x32_bf16 %2, %10, %6, %2\n\tv_mfma_f32_16x16x32_bf16 %3, %11, %6, %3\n\tv_mfma_f32_16x16x32_bf16 %4, %12, %6, %4"
      : "+v"(acc[0][1]), "=&v"(p[3]), "+v"(acc[0][2]), "+v"(acc[0][3]), "+v"(acc[0][0]) : "a"(fw1[4][1]), "v"(p[4]), "v"(vHc), "s"(rsW), "s"(sh[1]), "a"(fw1[4][2]), "a"(fw1[4][3]), "a"(fw1[4][0]) : "memory"); SB();
  WAITVB(12);
  SB();
  asm volatile("v_mfma_f32_16x16x32_bf16 %0, %5, %6, %0\n\tbuffer_load_dwordx4 %1, %7, %8, %9 offen offset:0 sc1\n\tv_mfma_f32_16x16x32_bf16 %2, %10, %6, %2\n\tv_mfma_f32_16x16x32_bf16 %3, %11, %6, %3\n\tv_mfma_f32_16x16x32_bf16 %4, %12, %6, %4"
      : "+v"(acc[0][1]), "=&v"(p[4]), "+v"(acc[0][2]), "+v"(acc[0][3]), "+v"(acc[0][0]) : "a"(fw1[5][1]), "v"(p[5]), "v"(vHc), "s"(rsW), "s"(sh[2]), "a"(fw1[5][2]), "a"(fw1[5][3]), "a"(fw1[5][0]) : "memory"); SB();
  WAITVB(12);
  SB();
  asm volatile("v_mfma_f32_16x16x32_bf16 %0, %5, %6, %0\n\tbuffer_load_dwordx4 %1, %7, %8, %9 offen offset:0 sc1\n\tv_mfma_f32_16x16x32_bf16 %2, %10, %6, %2\n\tv_mfma_f32_16x16x32_bf16 %3, %11, %6, %3\n\tv_mfma_f32_16x16x32_bf16 %4, %12, %6, %4"
      : "+v"(acc[0][1]), "=&v"(p[5]), "+v"(acc[0][2]), "+v"(acc[0][3]), "+v"(acc[0][0]) : "a"(fw1[6][1]), "v"(p[6]), "v"(vHc), "s"(rsW), "s"(sh[3]), "a"(fw1[6][2]), "a"(fw1[6][3]), "a"(fw1[6][0]) : "memory"); SB();
  WAITVB(12);
  SB();
  asm volatile("v_mfma_f32_16x16x32_bf16 %0, %5, %6, %0\n\tbuffer_load_dwordx4 %1, %7, %8, %9 offen offset:0 sc1\n\tv_mfma_f32_16x16x32_bf16 %2, %10, %6, %2\n\tv_mfma_f32_16x16x32_bf16 %3, %11, %6, %3\n\tv_mfma_f32_16x16x32_bf16 %4, %12, %6, %4"
      : "+v"(acc[0][1]), "=&v"(p[6]), "+v"(acc[0][2]), "+v"(acc[0][3]), "+v"(acc[0][0]) : "a"(fw1[7][1]), "v"(p[7]), "v"(vHc), "s"(rsW), "s"(sh[4]), "a"(fw1[7][2]), "a"(fw1[7][3]), "a"(fw1[7][0]) : "memory"); SB();
  WAITVB(12);
  SB();
  asm volatile("v_mfma_f32_16x16x32_bf16 %0, %5, %6, %0\n\tbuffer_load_dwordx4 %1, %7, %8, %9 offen offset:0 sc1\n\tv_mfma_f32_16x16x32_bf16 %2, %10, %6, %2\n\tv_mfma_f32_16x16x32_bf16 %3, %11, %6, %3\n\tv_mfma_f32_16x16x32_bf16 %4, %12, %6, %4"
      : "+v"(acc[0][1]), "=&v"(p[7]), "+v"(acc[0][2]), "+v"(acc[0][3]), "+v"(acc[0][0]) : "a"(fw1[8][1]), "v"(p[8]), "v"(vHc), "s"(rsW), "s"(sh[5]), "a"(fw1[8][2]), "a"(fw1[8][3]), "a"(fw1[8][0]) : "memory"); SB();
  WAITVB(12);
  SB();
  asm volatile("v_mfma_f32_16x16x32_bf16 %0, %5, %6, %0\n\tbuffer_load_dwordx4 %1, %7, %8, %9 offen offset:0 sc1\n\tv_mfma_f32_16x16x32_bf16 %2, %10, %6, %2\n\tv_mfma_f32_16x16x32_bf16 %3, %11, %6, %3\n\tv_mfma_f32_16x16x32_bf16 %4, %12, %6, %4"
      : "+v"(acc[0][1]), "=&v"(p[8]), "+v"(acc[0][2]), "+v"(acc[0][3]), "+v"(acc[0][0]) : "a"(fw1[9][1]), "v"(p[9]), "v"(vHc), "s"(rsW), "s"(sh[6]), "a"(fw1[9][2]), "a"(fw1[9][3]), "a"(fw1[9][0]) : "memory"); SB();
  WAITVB(12);
  SB();
  asm volatile("v_mfma_f32_16x16x32_bf16 %0, %5, %6, %0\n\tbuffer_load_dwordx4 %1, %7, %8, %9 offen offset:0 sc1\n\tv_mfma_f32_16x16x32_bf16 %2, %10, %6, %2\n\tv_mfma_f32_16x16x32_bf16 %3, %11, %6, %3\n\tv_mfma_f32_16x16x32_bf16 %4, %12, %6, %4"
      : "+v"(acc[0][1]), "=&v"(p[9]), "+v"(acc[0][2]), "+v"(acc[0][3]), "+v"(acc[0][0]) : "a"(fw1[10][1]), "v"(p[10]), "v"(vHc), "s"(rsW), "s"(sh[7]), "a"(fw1[10][2]), "a"(fw1[10][3]), "a"(fw1[10][0]) : "memory"); SB();
  WAITVB(12);
  SB();
  asm volatile("v_mfma_f32_16x16x32_bf16 %0, %5, %6, %0\n\tbuffer_load_dwordx4 %1, %7, %8, %9 offen offset:0 sc1\n\tv_mfma_f32_16x16x32_bf16 %2, %10, %6, %2\n\tv_mfma_f32_16x16x32_bf16 %3, %11, %6, %3\n\tv_mfma_f32_16x16x32_bf16 %4, %12, %6, %4"
      : "+v"(acc[0][1]), "=&v"(p[10]), "+v"(acc[0][2]), "+v"(acc[0][3]), "+v"(acc[0][0]) : "a"(fw1[11][1]), "v"(p[11]), "v"(vHc), "s"(rsW), "s"(sh[8]), "a"(fw1[11][2]), "a"(fw1[11][3]), "a"(fw1[11][0]) : "memory"); SB();
  WAITVB(12);
  SB();
  asm volatile("v_mfma_f32_16x16x32_bf16 %0, %5, %6, %0\n\tbuffer_load_dwordx4 %1, %7, %8, %9 offen offset:0 sc1\n\tv_mfma_f32_16x16x32_bf16 %2, %10, %6, %2\n\tv_mfma_f32_16x16x32_bf16 %3, %11, %6, %3\n\tv_mfma_f32_16x16x32_bf16 %4, %12, %6, %4"
      : "+v"(acc[0][1]), "=&v"(p[11]), "+v"(acc[0][2]), "+v"(acc[0][3]), "+v"(acc[0][0]) : "a"(fw1[12][1]), "v"(p[12]), "v"(vHc), "s"(rsW), "s"(sh[9]), "a"(fw1[12][2]), "a"(fw1[12][3]), "a"(fw1[12][0]) : "memory"); SB();
  WAITVB(12);
  SB();
  asm volatile("v_mfma_f32_16x16x32_bf16 %0, %5, %6, %0\n\tbuffer_load_dwordx4 %1, %7, %8, %9 offen offset:0 sc1\n\tv_mfma_f32_16x16x32_bf16 %2, %10, %6, %2\n\tv_mfma_f32_16x16x32_bf16 %3, %11, %6, %3\n\tv_mfma_f32_16x16x32_bf16 %4, %12, %6, %4"
      : "+v"(acc[0][1]), "=&v"(p[12]), "+v"(acc[0][2]), "+v"(acc[0][3]), "+v"(acc[0][0]) : "a"(fw1[13][1]), "v"(p[13]), "v"(vHc), "s"(rsW), "s"(sh[10]), "a"(fw1[13][2]), "a"(fw1[13][3]), "a"(fw1[13][0]) : "memory"); SB();
  WAITVB(12);
  SB();
  asm volatile("v_mfma_f32_16x16x32_bf16 %0, %5, %6, %0\n\tbuffer_load_dwordx4 %1, %7, %8, %9 offen offset:0 sc1\n\tv_mfma_f32_16x16x32_bf16 %2, %10, %6, %2\n\tv_mfma_f32_16x16x32_bf16 %3, %11, %6, %3\n\tv_mfma_f32_16x16x32_bf16 %4, %12, %6, %4"
      : "+v"(acc[0][1]), "=&v"(p[13]), "+v"(acc[0][2]), "+v"(acc[0][3]), "+v"(acc[0][0]) : "a"(fw1[14][1]), "v"(p[0]), "v"(vHc), "s"(rsW), "s"(sh[11]), "a"(fw1[14][2]), "a"(fw1[14][3]), "a"(fw1[14][0]) : "memory"); SB();
  WAITVB(12);
  SB();
  asm volatile("v_mfma_f32_16x16x32_bf16 %0, %5, %6, %0\n\tbuffer_load_dwordx4 %1, %7, %8, %9 offen offset:0 sc1\n\tv_mfma_f32_16x16x32_bf16 %2, %10, %6, %2\n\tv_mfma_f32_16x16x32_bf16 %3, %11, %6, %3\n\tv_mfma_f32_16x16x32_bf16 %4, %12, %6, %4"
      : "+v"(acc[0][1]), "=&v"(p[0]), "+v"(acc[0][2]), "+v"(acc[0][3]), "+v"(acc[0][0]) : "v"(fw1[15][1]), "v"(p[1]), "v"(vHc), "s"(rsW), "s"(sh[12]), "v"(fw1[15][2]), "v"(fw1[15][3]), "v"(fw1[15][0]) : "memory"); SB();
  STAMP(7);
  WAITVB(12);
  SB();
  asm volatile("v_mfma_f32_16x16x32_bf16 %0, %5, %6, 0\n\tbuffer_load_dwordx4 %1, %7, %8, %9 offen offset:0 sc1\n\tv_mfma_f32_16x16x32_bf16 %2, %10, %6, 0\n\tv_mfma_f32_16x16x32_bf16 %3, %11, %6, 0\n\tv_mfma_f32_16x16x32_bf16 %4, %12, %6, %13"
      : "=&v"(acc[1][1]), "=&v"(p[1]), "=&v"(acc[1][2]), "=&v"(acc[1][3]), "=&v"(acc[1][0]) : "a"(fw1[0][1]), "v"(p[2]), "v"(vHc), "s"(rsW), "s"(sh[13]), "a"(fw1[0][2]), "a"(fw1[0][3]), "a"(fw1[0][0]), "v"(bb) : "memory"); SB();
  WAITVB(12);
  SB();
  asm volatile("v_mfma_f32_16x16x32_bf16 %0, %5, %6, %0\n\tbuffer_load_dwordx4 %1, %7, %8, %9 offen offset:0 sc1\n\tv_mfma_f32_16x16x32_bf16 %2, %10, %6, %2\n\tds_write_b128 %11, %12 offset:1024\n\tv_mfma_f32_16x16x32_bf16 %3, %13, %6, %3\n\tv_mfma_f32_16x16x32_bf16 %4, %14, %6, %4"
      : "+v"(acc[1][1]), "=&v"(p[2]), "+v"(acc[1][2]), "+v"(acc[1][3]), "+v"(acc[1][0]) : "a"(fw1[1][1]), "v"(p[3]), "v"(vHc), "s"(rsW), "s"(sh[14]), "a"(fw1[1][2]), "v"(rw), "v"(acc[0][1]), "a"(fw1[1][3]), "a"(fw1[1][0]) : "memory"); SB();
  WAITVB(12);
  SB();
  asm volatile("v_mfma_f32_16x16x32_bf16 %0, %5, %6, %0\n\tbuffer_load_dwordx4 %1, %7, %8, %9 offen offset:0 sc1\n\tv_mfma_f32_16x16x32_bf16 %2, %10, %6, %2\n\tds_write_b128 %11, %12 offset:2048\n\tv_mfma_f32_16x16x32_bf16 %3, %13, %6, %3\n\tv_mfma_f32_16x16x32_bf16 %4, %14, %6, %4"
      : "+v"(acc[1][1]), "=&v"(p[3]), "+v"(acc[1][2]), "+v"(acc[1][3]), "+v"(acc[1][0]) : "a"(fw1[2][1]), "v"(p[4]), "v"(vHc), "s"(rsW), "s"(sh[15]), "a"(fw1[2][2]), "v"(rw), "v"(acc[0][2]), "a"(fw1[2][3]), "a"(fw1[2][0]) : "memory"); SB();
  WAITVB(12);
  SB();
  asm volatile("v_mfma_f32_16x16x32_bf16 %0, %5, %6, %0\n\tbuffer_load_dwordx4 %1, %7, %8, %9 offen offset:512 sc1\n\tv_mfma_f32_16x16x32_bf16 %2, %10, %6, %2\n\tds_write_b128 %11, %12 offset:3072\n\tv_mfma_f32_16x16x32_bf16 %3, %13, %6, %3\n\tv_mfma_f32_16x16x32_bf16 %4, %14, %6, %4"
      : "+v"(acc[1][1]), "=&v"(p[4]), "+v"(acc[1][2]), "+v"(acc[1][3]), "+v"(acc[1][0]) : "a"(fw1[3][1]), "v"(p[5]), "v"(vHa), "s"(rsW), "s"(sh[0]), "a"(fw1[3][2]), "v"(rw), "v"(acc[0][3]), "a"(fw1[3][3]), "a"(fw1[3][0]) : "memory"); SB();
  WAITVB(12);
  SB();
  asm volatile("v_mfma_f32_16x16x32_bf16 %0, %3, %4, %0\n\tbuffer_load_dwordx4 %1, %5, %6, %7 offen offset:512 sc1\n\tv_mfma_f32_16x16x32_bf16 %2, %8, %4, %2"
      : "+v"(acc[1][1]), "=&v"(p[5]), "+v"(acc[1][2]) : "a"(fw1[4][1]), "v"(p[6]), "v"(vHa), "s"(rsW), "s"(sh[1]), "a"(fw1[4][2]) : "memory"); SB();
  BAR();
  SB();
  asm volatile("v_mfma_f32_16x16x32_bf16 %0, %5, %6, %0\n\tds_read_b128 %1, %7 offset:1024\n\tds_read_b128 %2, %8 offset:2048\n\tv_mfma_f32_16x16x32_bf16 %3, %9, %6, %3\n\tds_read_b128 %4, %10 offset:3072"
      : "+v"(acc[1][3]), "=&v"(pm[0][0]), "=&v"(pm[0][1]), "+v"(acc[1][0]), "=&v"(pm[0][2]) : "a"(fw1[4][3]), "v"(p[6]), "v"(rr1), "v"(rr2), "a"(fw1[4][0]), "v"(rr3) : "memory"); SB();
  WAITVB(12);
  SB();
  asm volatile("v_mfma_f32_16x16x32_bf16 %0, %5, %6, %0\n\tbuffer_load_dwordx4 %1, %7, %8, %9 offen offset:512 sc1\n\tv_mfma_f32_16x16x32_bf16 %2, %10, %6, %2\n\tv_mfma_f32_16x16x32_bf16 %3, %11, %6, %3\n\tv_mfma_f32_16x16x32_bf16 %4, %12, %6, %4"
      : "+v"(acc[1][1]), "=&v"(p[6]), "+v"(acc[1][2]), "+v"(acc[1][3]), "+v"(acc[1][0]) : "a"(fw1[5][1]), "v"(p[7]), "v"(vHa), "s"(rsW), "s"(sh[2]), "a"(fw1[5][2]), "a"(fw1[5][3]), "a"(fw1[5][0]) : "memory"); SB();
  WAITVB(12);
  SB();
  asm volatile("v_mfma_f32_16x16x32_bf16 %0, %3, %4, %0\n\tbuffer_load_dwordx4 %1, %5, %6, %7 offen offset:512 sc1\n\tv_mfma_f32_16x16x32_bf16 %2, %8, %4, %2"
      : "+v"(acc[1][1]), "=&v"(p[7]), "+v"(acc[1][2]) : "a"(fw1[6][1]), "v"(p[8]), "v"(vHa), "s"(rsW), "s"(sh[3]), "a"(fw1[6][2]) : "memory"); SB();
  WAITL(0);
  asm volatile("v_add_f32 %0, %6, %7\n\tv_mfma_f32_16x16x32_bf16 %1, %8, %9, %1\n\tv_add_f32 %2, %10, %11\n\tv_add_f32 %3, %12, %13\n\tv_mfma_f32_16x16x32_bf16 %4, %14, %9, %4\n\tv_add_f32 %5, %15, %16"
      : "=&v"(gm[0]), "+v"(acc[1][3]), "=&v"(gm[1]), "=&v"(gm[2]), "+v"(acc[1][0]), "=&v"(gm[3]) : "v"(acc[0][0][0]), "v"(pm[0][0][0]), "a"(fw1[6][3]), "v"(p[8]), "v"(acc[0][0][1]), "v"(pm[0][0][1]), "v"(acc[0][0][2]), "v"(pm[0][0][2]), "a"(fw1[6][0]), "v"(acc[0][0][3]), "v"(pm[0][0][3])); SB();
  WAITVB(12);
  SB();
  asm volatile("v_mfma_f32_16x16x32_bf16 %0, %9, %10, %0\n\tbuffer_load_dwordx4 %1, %11, %12, %13 offen offset:512 sc1\n\tv_mfma_f32_16x16x32_bf16 %2, %14, %10, %2\n\tv_add_f32 %3, %15, %16\n\tv_add_f32 %4, %17, %18\n\tv_mfma_f32_16x16x32_bf16 %5, %19, %10, %5\n\tv_add_f32 %6, %20, %21\n\tv_add_f32 %7, %22, %23\n\tv_mfma_f32_16x16x32_bf16 %8, %24, %10, %8"
      : "+v"(acc[1][1]), "=&v"(p[8]), "+v"(acc[1][2]), "=&v"(gz[0]), "=&v"(gz[1]), "+v"(acc[1][3]), "=&v"(gz[2]), "=&v"(gz[3]), "+v"(acc[1][0]) : "a"(fw1[7][1]), "v"(p[9]), "v"(vHa), "s"(rsW), "s"(sh[4]), "a"(fw1[7][2]), "v"(pm[0][1][0]), "v"(pm[0][2][0]), "v"(pm[0][1][1]), "v"(pm[0][2][1]), "a"(fw1[7][3]), "v"(pm[0][1][2]), "v"(pm[0][2][2]), "v"(pm[0][1][3]), "v"(pm[0][2][3]), "a"(fw1[7][0]) : "memory"); SB();
  asm volatile("v_add_f32 %0, %0, %1"
      : "+v"(gm[0]) : "v"(gz[0])); SB();
  WAITVB(12);
  SB();
  asm volatile("v_mfma_f32_16x16x32_bf16 %0, %10, %11, %0\n\tbuffer_load_dwordx4 %1, %12, %13, %14 offen offset:512 sc1\n\tv_mfma_f32_16x16x32_bf16 %2, %15, %11, %2\n\tv_add_f32 %3, %3, %4\n\tv_add_f32 %5, %5, %16\n\tv_mfma_f32_16x16x32_bf16 %6, %17, %11, %6\n\tv_add_f32 %7, %7, %18\n\tv_mul_f32 %8, %19, %19\n\tv_mfma_f32_16x16x32_bf16 %9, %20, %11, %9\n\tv_mul_f32 %4, %3, %3"
      : "+v"(acc[1][1]), "=&v"(p[9]), "+v"(acc[1][2]), "+v"(gm[1]), "+v"(gz[1]), "+v"(gm[2]), "+v"(acc[1][3]), "+v"(gm[3]), "=&v"(gz[0]), "+v"(acc[1][0]) : "a"(fw1[8][1]), "v"(p[10]), "v"(vHa), "s"(rsW), "s"(sh[5]), "a"(fw1[8][2]), "v"(gz[2]), "a"(fw1[8][3]), "v"(gz[3]), "v"(gm[0]), "a"(fw1[8][0]) : "memory"); SB();
  WAITVB(12);
  SB();
  asm volatile("v_mfma_f32_16x16x32_bf16 %0, %9, %10, %0\n\tbuffer_load_dwordx4 %1, %11, %12, %13 offen offset:512 sc1\n\tv_mfma_f32_16x16x32_bf16 %2, %14, %10, %2\n\tv_mul_f32 %3, %15, %15\n\tv_mul_f32 %4, %16, %16\n\tv_mfma_f32_16x16x32_bf16 %5, %17, %10, %5\n\tv_fmaak_f32 %6, %18, %6, 0x40135761\n\tv_fmaak_f32 %7, %18, %7, 0x40135761\n\tv_mfma_f32_16x16x32_bf16 %8, %19, %10, %8\n\tv_fmaak_f32 %3, %18, %3, 0x40135761"
      : "+v"(acc[1][1]), "=&v"(p[10]), "+v"(acc[1][2]), "=&v"(gz[2]), "=&v"(gz[3]), "+v"(acc[1][3]), "+v"(gz[0]), "+v"(gz[1]), "+v"(acc[1][0]) : "a"(fw1[9][1]), "v"(p[11]), "v"(vHa), "s"(rsW), "s"(sh[6]), "a"(fw1[9][2]), "v"(gm[2]), "v"(gm[3]), "a"(fw1[9][3]), "v"(gc), "a"(fw1[9][0]) : "memory"); SB();
  WAITVB(12);
  SB();
  asm volatile("v_mfma_f32_16x16x32_bf16 %0, %9, %10, %0\n\tbuffer_load_dwordx4 %1, %11, %12, %13 offen offset:512 sc1\n\tv_mfma_f32_16x16x32_bf16 %2, %14, %10, %2\n\tv_fmaak_f32 %3, %15, %3, 0x40135761\n\tv_mul_f32 %4, %4, %16\n\tv_mfma_f32_16x16x32_bf16 %5, %17, %10, %5\n\tv_mul_f32 %6, %6, %18\n\tv_mul_f32 %7, %7, %19\n\tv_mfma_f32_16x16x32_bf16 %8, %20, %10, %8\n\tv_mul_f32 %3, %3, %21"
      : "+v"(acc[1][1]), "=&v"(p[11]), "+v"(acc[1][2]), "+v"(gz[3]), "+v"(gz[0]), "+v"(acc[1][3]), "+v"(gz[1]), "+v"(gz[2]), "+v"(acc[1][0]) : "a"(fw1[10][1]), "v"(p[12]), "v"(vHa), "s"(rsW), "s"(sh[7]), "a"(fw1[10][2]), "v"(gc), "v"(gm[0]), "a"(fw1[10][3]), "v"(gm[1]), "v"(gm[2]), "a"(fw1[10][0]), "v"(gm[3]) : "memory"); SB();
  WAITVB(12);
  SB();
  asm volatile("v_mfma_f32_16x16x32_bf16 %0, %7, %8, %0\n\tbuffer_load_dwordx4 %1, %9, %10, %11 offen offset:512 sc1\n\tv_mfma_f32_16x16x32_bf16 %2, %12, %8, %2\n\tv_exp_f32 %3, %3\n\tv_mfma_f32_16x16x32_bf16 %4, %13, %8, %4\n\tv_exp_f32 %5, %5\n\tv_mfma_f32_16x16x32_bf16 %6, %14, %8, %6"
      : "+v"(acc[1][1]), "=&v"(p[12]), "+v"(acc[1][2]), "+v"(gz[0]), "+v"(acc[1][3]), "+v"(gz[1]), "+v"(acc[1][0]) : "a"(fw1[11][1]), "v"(p[13]), "v"(vHa), "s"(rsW), "s"(sh[8]), "a"(fw1[11][2]), "a"(fw1[11][3]), "a"(fw1[11][0]) : "memory"); SB();
  WAITVB(12);
  SB();
  asm volatile("v_mfma_f32_16x16x32_bf16 %0, %8, %9, %0\n\tbuffer_load_dwordx4 %1, %10, %11, %12 offen offset:512 sc1\n\tv_mfma_f32_16x16x32_bf16 %2, %13, %9, %2\n\tv_exp_f32 %3, %3\n\tv_mfma_f32_16x16x32_bf16 %4, %14, %9, %4\n\tv_exp_f32 %5, %5\n\tv_mfma_f32_16x16x32_bf16 %6, %15, %9, %6\n\tv_add_f32 %7, 1.0, %7"
      : "+v"(acc[1][1]), "=&v"(p[13]), "+v"(acc[1][2]), "+v"(gz[2]), "+v"(acc[1][3]), "+v"(gz[3]), "+v"(acc[1][0]), "+v"(gz[0]) : "a"(fw1[12][1]), "v"(p[0]), "v"(vHa), "s"(rsW), "s"(sh[9]), "a"(fw1[12][2]), "a"(fw1[12][3]), "a"(fw1[12][0]) : "memory"); SB();
  WAITVB(12);
  SB();
  asm volatile("v_mfma_f32_16x16x32_bf16 %0, %8, %9, %0\n\tbuffer_load_dwordx4 %1, %10, %11, %12 offen offset:512 sc1\n\tv_mfma_f32_16x16x32_bf16 %2, %13, %9, %2\n\tv_add_f32 %3, 1.0, %3\n\tv_add_f32 %4, 1.0, %4\n\tv_mfma_f32_16x16x32_bf16 %5, %14, %9, %5\n\tv_add_f32 %6, 1.0, %6\n\tv_mfma_f32_16x16x32_bf16 %7, %15, %9, %7"
      : "+v"(acc[1][1]), "=&v"(p[0]), "+v"(acc[1][2]), "+v"(gz[1]), "+v"(gz[2]), "+v"(acc[1][3]), "+v"(gz[3]), "+v"(acc[1][0]) : "a"(fw1[13][1]), "v"(p[1]), "v"(vHa), "s"(rsW), "s"(sh[10]), "a"(fw1[13][2]), "a"(fw1[13][3]), "a"(fw1[13][0]) : "memory"); SB();
  WAITVB(12);
  SB();
  asm volatile("v_mfma_f32_16x16x32_bf16 %0, %7, %8, %0\n\tbuffer_load_dwordx4 %1, %9, %10, %11 offen offset:512 sc1\n\tv_mfma_f32_16x16x32_bf16 %2, %12, %8, %2\n\tv_rcp_f32 %3, %3\n\tv_mfma_f32_16x16x32_bf16 %4, %13, %8, %4\n\tv_rcp_f32 %5, %5\n\tv_mfma_f32_16x16x32_bf16 %6, %14, %8, %6"
      : "+v"(acc[1][1]), "=&v"(p[1]), "+v"(acc[1][2]), "+v"(gz[0]), "+v"(acc[1][3]), "+v"(gz[1]), "+v"(acc[1][0]) : "a"(fw1[14][1]), "v"(p[2]), "v"(vHa), "s"(rsW), "s"(sh[11]), "a"(fw1[14][2]), "a"(fw1[14][3]), "a"(fw1[14][0]) : "memory"); SB();
  WAITVB(12);
  SB();
  asm volatile("v_mfma_f32_16x16x32_bf16 %0, %9, %10, %0\n\tbuffer_load_dwordx4 %1, %11, %12, %13 offen offset:512 sc1\n\tv_mfma_f32_16x16x32_bf16 %2, %14, %10, %2\n\tv_rcp_f32 %3, %3\n\tv_mfma_f32_16x16x32_bf16 %4, %15, %10, %4\n\tv_rcp_f32 %5, %5\n\tv_mfma_f32_16x16x32_bf16 %6, %16, %10, %6\n\tv_fma_f32 %7, -%17, %7, %17\n\tv_fma_f32 %8, -%18, %8, %18"
      : "+v"(acc[1][1]), "=&v"(p[2]), "+v"(acc[1][2]), "+v"(gz[2]), "+v"(acc[1][3]), "+v"(gz[3]), "+v"(acc[1][0]), "+v"(gz[0]), "+v"(gz[1]) : "v"(fw1[15][1]), "v"(p[3]), "v"(vHa), "s"(rsW), "s"(sh[12]), "v"(fw1[15][2]), "v"(fw1[15][3]), "v"(fw1[15][0]), "v"(gm[0]), "v"(gm[1]) : "memory"); SB();
  WAITVB(12);
  SB();
  asm volatile("v_mfma_f32_16x16x32_bf16 %0, %9, %10, 0\n\tbuffer_load_dwordx4 %1, %11, %12, %13 offen offset:512 sc1\n\tv_mfma_f32_16x16x32_bf16 %2, %14, %10, 0\n\tv_fma_f32 %3, -%15, %3, %15\n\tv_fma_f32 %4, -%16, %4, %16\n\tv_mfma_f32_16x16x32_bf16 %5, %17, %10, 0\n\tv_cvt_pk_bf16_f32 %6, %18, %19\n\tv_cvt_pk_bf16_f32 %7, %3, %4\n\tv_mfma_f32_16x16x32_bf16 %8, %20, %10, %21"
      : "=&v"(acc[2][1]), "=&v"(p[3]), "=&v"(acc[2][2]), "+v"(gz[2]), "+v"(gz[3]), "=&v"(acc[2][3]), "=&v"(uv[0][0]), "=&v"(uv[0][1]), "=&v"(acc[2][0]) : "a"(fw1[0][1]), "v"(p[4]), "v"(vHa), "s"(rsW), "s"(sh[13]), "a"(fw1[0][2]), "v"(gm[2]), "v"(gm[3]), "a"(fw1[0][3]), "v"(gz[0]), "v"(gz[1]), "a"(fw1[0][0]), "v"(bb) : "memory"); SB();
  WAITVB(12);
  SB();
  asm volatile("v_mfma_f32_16x16x32_bf16 %0, %5, %6, %0\n\tbuffer_load_dwordx4 %1, %7, %8, %9 offen offset:512 sc1\n\tv_mfma_f32_16x16x32_bf16 %2, %10, %6, %2\n\tds_write_b128 %11, %12 offset:5120\n\tv_mfma_f32_16x16x32_bf16 %3, %13, %6, %3\n\tv_mfma_f32_16x16x32_bf16 %4, %14, %6, %4"
      : "+v"(acc[2][1]), "=&v"(p[4]), "+v"(acc[2][2]), "+v"(acc[2][3]), "+v"(acc[2][0]) : "a"(fw1[1][1]), "v"(p[5]), "v"(vHa), "s"(rsW), "s"(sh[14]), "a"(fw1[1][2]), "v"(rw), "v"(acc[1][1]), "a"(fw1[1][3]), "a"(fw1[1][0]) : "memory"); SB();
  WAITVB(12);
  SB();
  asm volatile("v_mfma_f32_16x16x32_bf16 %0, %5, %6, %0\n\tbuffer_load_dwordx4 %1, %7, %8, %9 offen offset:512 sc1\n\tv_mfma_f32_16x16x32_bf16 %2, %10, %6, %2\n\tds_write_b128 %11, %12 offset:6144\n\tv_mfma_f32_16x16x32_bf16 %3, %13, %6, %3\n\tv_mfma_f32_16x16x32_bf16 %4, %14, %6, %4"
      : "+v"(acc[2][1]), "=&v"(p[5]), "+v"(acc[2][2]), "+v"(acc[2][3]), "+v"(acc[2][0]) : "a"(fw1[2][1]), "v"(p[6]), "v"(vHa), "s"(rsW), "s"(sh[15]), "a"(fw1[2][2]), "v"(rw), "v"(acc[1][2]), "a"(fw1[2][3]), "a"(fw1[2][0]) : "memory"); SB();
  WAITVB(12);
  SB();
  asm volatile("v_mfma_f32_16x16x32_bf16 %0, %5, %6, %0\n\tbuffer_load_dwordx4 %1, %7, %8, %9 offen offset:512 sc1\n\tv_mfma_f32_16x16x32_bf16 %2, %10, %6, %2\n\tds_write_b128 %11, %12 offset:7168\n\tv_mfma_f32_16x16x32_bf16 %3, %13, %6, %3\n\tv_mfma_f32_16x16x32_bf16 %4, %14, %6, %4"
      : "+v"(acc[2][1]), "=&v"(p[6]), "+v"(acc[2][2]), "+v"(acc[2][3]), "+v"(acc[2][0]) : "a"(fw1[3][1]), "v"(p[7]), "v"(vHc), "s"(rsW), "s"(sh[0]), "a"(fw1[3][2]), "v"(rw), "v"(acc[1][3]), "a"(fw1[3][3]), "a"(fw1[3][0]) : "memory"); SB();
  WAITVB(12);
  SB();
  asm volatile("v_mfma_f32_16x16x32_bf16 %0, %3, %4, %0\n\tbuffer_load_dwordx4 %1, %5, %6, %7 offen offset:512 sc1\n\tv_mfma_f32_16x16x32_bf16 %2, %8, %4, %2"
      : "+v"(acc[2][1]), "=&v"(p[7]), "+v"(acc[2][2]) : "a"(fw1[4][1]), "v"(p[8]), "v"(vHc), "s"(rsW), "s"(sh[1]), "a"(fw1[4][2]) : "memory"); SB();
  BAR();
  SB();
  asm volatile("v_mfma_f32_16x16x32_bf16 %0, %5, %6, %0\n\tds_read_b128 %1, %7 offset:5120\n\tds_read_b128 %2, %8 offset:6144\n\tv_mfma_f32_16x16x32_bf16 %3, %9, %6, %3\n\tds_read_b128 %4, %10 offset:7168"
      : "+v"(acc[2][3]), "=&v"(pm[1][0]), "=&v"(pm[1][1]), "+v"(acc[2][0]), "=&v"(pm[1][2]) : "a"(fw1[4][3]), "v"(p[8]), "v"(rr1), "v"(rr2), "a"(fw1[4][0]), "v"(rr3) : "memory"); SB();
  WAITVB(12);
  SB();
  asm volatile("v_mfma_f32_16x16x32_bf16 %0, %5, %6, %0\n\tbuffer_load_dwordx4 %1, %7, %8, %9 offen offset:512 sc1\n\tv_mfma_f32_16x16x32_bf16 %2, %10, %6, %2\n\tv_mfma_f32_16x16x32_bf16 %3, %11, %6, %3\n\tv_mfma_f32_16x16x32_bf16 %4, %12, %6, %4"
      : "+v"(acc[2][1]), "=&v"(p[8]), "+v"(acc[2][2]), "+v"(acc[2][3]), "+v"(acc[2][0]) : "a"(fw1[5][1]), "v"(p[9]), "v"(vHc), "s"(rsW), "s"(sh[2]), "a"(fw1[5][2]), "a"(fw1[5][3]), "a"(fw1[5][0]) : "memory"); SB();
  WAITVB(12);
  SB();
  asm volatile("v_mfma_f32_16x16x32_bf16 %0, %3, %4, %0\n\tbuffer_load_dwordx4 %1, %5, %6, %7 offen offset:512 sc1\n\tv_mfma_f32_16x16x32_bf16 %2, %8, %4, %2"
      : "+v"(acc[2][1]), "=&v"(p[9]), "+v"(acc[2][2]) : "a"(fw1[6][1]), "v"(p[10]), "v"(vHc), "s"(rsW), "s"(sh[3]), "a"(fw1[6][2]) : "memory"); SB();
  WAITL(0);
  asm volatile("v_add_f32 %0, %6, %7\n\tv_mfma_f32_16x16x32_bf16 %1, %8, %9, %1\n\tv_add_f32 %2, %10, %11\n\tv_add_f32 %3, %12, %13\n\tv_mfma_f32_16x16x32_bf16 %4, %14, %9, %4\n\tv_add_f32 %5, %15, %16"
      : "=&v"(gm[0]), "+v"(acc[2][3]), "=&v"(gm[1]), "=&v"(gm[2]), "+v"(acc[2][0]), "=&v"(gm[3]) : "v"(acc[1][0][0]), "v"(pm[1][0][0]), "a"(fw1[6][3]), "v"(p[10]), "v"(acc[1][0][1]), "v"(pm[1][0][1]), "v"(acc[1][0][2]), "v"(pm[1][0][2]), "a"(fw1[6][0]), "v"(acc[1][0][3]), "v"(pm[1][0][3])); SB();
  WAITVB(12);
  SB();
  asm volatile("v_mfma_f32_16x16x32_bf16 %0, %9, %10, %0\n\tbuffer_load_dwordx4 %1, %11, %12, %13 offen offset:512 sc1\n\tv_mfma_f32_16x16x32_bf16 %2, %14, %10, %2\n\tv_add_f32 %3, %15, %16\n\tv_add_f32 %4, %17, %18\n\tv_mfma_f32_16x16x32_bf16 %5, %19, %10, %5\n\tv_add_f32 %6, %20, %21\n\tv_add_f32 %7, %22, %23\n\tv_mfma_f32_16x16x32_bf16 %8, %24, %10, %8"
      : "+v"(acc[2][1]), "=&v"(p[10]), "+v"(acc[2][2]), "=&v"(gz[0]), "=&v"(gz[1]), "+v"(acc[2][3]), "=&v"(gz[2]), "=&v"(gz[3]), "+v"(acc[2][0]) : "a"(fw1[7][1]), "v"(p[11]), "v"(vHc), "s"(rsW), "s"(sh[4]), "a"(fw1[7][2]), "v"(pm[1][1][0]), "v"(pm[1][2][0]), "v"(pm[1][1][1]), "v"(pm[1][2][1]), "a"(fw1[7][3]), "v"(pm[1][1][2]), "v"(pm[1][2][2]), "v"(pm[1][1][3]), "v"(pm[1][2][3]), "a"(fw1[7][0]) : "memory"); SB();
  asm volatile("v_add_f32 %0, %0, %1"
      : "+v"(gm[0]) : "v"(gz[0])); SB();
  WAITVB(12);
  SB();
  asm volatile("v_mfma_f32_16x16x32_bf16 %0, %10, %11, %0\n\tbuffer_load_dwordx4 %1, %12, %13, %14 offen offset:512 sc1\n\tv_mfma_f32_16x16x32_bf16 %2, %15, %11, %2\n\tv_add_f32 %3, %3, %4\n\tv_add_f32 %5, %5, %16\n\tv_mfma_f32_16x16x32_bf16 %6, %17, %11, %6\n\tv_add_f32 %7, %7, %18\n\tv_mul_f32 %8, %19, %19\n\tv_mfma_f32_16x16x32_bf16 %9, %20, %11, %9\n\tv_mul_f32 %4, %3, %3"
      : "+v"(acc[2][1]), "=&v"(p[11]), "+v"(acc[2][2]), "+v"(gm[1]), "+v"(gz[1]), "+v"(gm[2]), "+v"(acc[2][3]), "+v"(gm[3]), "=&v"(gz[0]), "+v"(acc[2][0]) : "a"(fw1[8][1]), "v"(p[12]), "v"(vHc), "s"(rsW), "s"(sh[5]), "a"(fw1[8][2]), "v"(gz[2]), "a"(fw1[8][3]), "v"(gz[3]), "v"(gm[0]), "a"(fw1[8][0]) : "memory"); SB();
  WAITVB(12);
  SB();
  asm volatile("v_mfma_f32_16x16x32_bf16 %0, %9, %10, %0\n\tbuffer_load_dwordx4 %1, %11, %12, %13 offen offset:512 sc1\n\tv_mfma_f32_16x16x32_bf16 %2, %14, %10, %2\n\tv_mul_f32 %3, %15, %15\n\tv_mul_f32 %4, %16, %16\n\tv_mfma_f32_16x16x32_bf16 %5, %17, %10, %5\n\tv_fmaak_f32 %6, %18, %6, 0x40135761\n\tv_fmaak_f32 %7, %18, %7, 0x40135761\n\tv_mfma_f32_16x16x32_bf16 %8, %19, %10, %8\n\tv_fmaak_f32 %3, %18, %3, 0x40135761"
      : "+v"(acc[2][1]), "=&v"(p[12]), "+v"(acc[2][2]), "=&v"(gz[2]), "=&v"(gz[3]), "+v"(acc[2][3]), "+v"(gz[0]), "+v"(gz[1]), "+v"(acc[2][0]) : "a"(fw1[9][1]), "v"(p[13]), "v"(vHc), "s"(rsW), "s"(sh[6]), "a"(fw1[9][2]), "v"(gm[2]), "v"(gm[3]), "a"(fw1[9][3]), "v"(gc), "a"(fw1[9][0]) : "memory"); SB();
  WAITVB(12);
  SB();
  asm volatile("v_mfma_f32_16x16x32_bf16 %0, %9, %10, %0\n\tbuffer_load_dwordx4 %1, %11, %12, %13 offen offset:512 sc1\n\tv_mfma_f32_16x16x32_bf16 %2, %14, %10, %2\n\tv_fmaak_f32 %3, %15, %3, 0x40135761\n\tv_mul_f32 %4, %4, %16\n\tv_mfma_f32_16x16x32_bf16 %5, %17, %10, %5\n\tv_mul_f32 %6, %6, %18\n\tv_mul_f32 %7, %7, %19\n\tv_mfma_f32_16x16x32_bf16 %8, %20, %10, %8\n\tv_mul_f32 %3, %3, %21"
      : "+v"(acc[2][1]), "=&v"(p[13]), "+v"(acc[2][2]), "+v"(gz[3]), "+v"(gz[0]), "+v"(acc[2][3]), "+v"(gz[1]), "+v"(gz[2]), "+v"(acc[2][0]) : "a"(fw1[10][1]), "v"(p[0]), "v"(vHc), "s"(rsW), "s"(sh[7]), "a"(fw1[10][2]), "v"(gc), "v"(gm[0]), "a"(fw1[10][3]), "v"(gm[1]), "v"(gm[2]), "a"(fw1[10][0]), "v"(gm[3]) : "memory"); SB();
  WAITVB(12);
  SB();
  asm volatile("v_mfma_f32_16x16x32_bf16 %0, %7, %8, %0\n\tbuffer_load_dwordx4 %1, %9, %10, %11 offen offset:512 sc1\n\tv_mfma_f32_16x16x32_bf16 %2, %12, %8, %2\n\tv_exp_f32 %3, %3\n\tv_mfma_f32_16x16x32_bf16 %4, %13, %8, %4\n\tv_exp_f32 %5, %5\n\tv_mfma_f32_16x16x32_bf16 %6, %14, %8, %6"
      : "+v"(acc[2][1]), "=&v"(p[0]), "+v"(acc[2][2]), "+v"(gz[0]), "+v"(acc[2][3]), "+v"(gz[1]), "+v"(acc[2][0]) : "a"(fw1[11][1]), "v"(p[1]), "v"(vHc), "s"(rsW), "s"(sh[8]), "a"(fw1[11][2]), "a"(fw1[11][3]), "a"(fw1[11][0]) : "memory"); SB();
  WAITVB(12);
  SB();
  asm volatile("v_mfma_f32_16x16x32_bf16 %0, %8, %9, %0\n\tbuffer_load_dwordx4 %1, %10, %11, %12 offen offset:512 sc1\n\tv_mfma_f32_16x16x32_bf16 %2, %13, %9, %2\n\tv_exp_f32 %3, %3\n\tv_mfma_f32_16x16x32_bf16 %4, %14, %9, %4\n\tv_exp_f32 %5, %5\n\tv_mfma_f32_16x16x32_bf16 %6, %15, %9, %6\n\tv_add_f32 %7, 1.0, %7"
      : "+v"(acc[2][1]), "=&v"(p[1]), "+v"(acc[2][2]), "+v"(gz[2]), "+v"(acc[2][3]), "+v"(gz[3]), "+v"(acc[2][0]), "+v"(gz[0]) : "a"(fw1[12][1]), "v"(p[2]), "v"(vHc), "s"(rsW), "s"(sh[9]), "a"(fw1[12][2]), "a"(fw1[12][3]), "a"(fw1[12][0]) : "memory"); SB();
  WAITVB(12);
  SB();
  asm volatile("v_mfma_f32_16x16x32_bf16 %0, %8, %9, %0\n\tbuffer_load_dwordx4 %1, %10, %11, %12 offen offset:512 sc1\n\tv_mfma_f32_16x16x32_bf16 %2, %13, %9, %2\n\tv_add_f32 %3, 1.0, %3\n\tv_add_f32 %4, 1.0, %4\n\tv_mfma_f32_16x16x32_bf16 %5, %14, %9, %5\n\tv_add_f32 %6, 1.0, %6\n\tv_mfma_f32_16x16x32_bf16 %7, %15, %9, %7"
      : "+v"(acc[2][1]), "=&v"(p[2]), "+v"(acc[2][2]), "+v"(gz[1]), "+v"(gz[2]), "+v"(acc[2][3]), "+v"(gz[3]), "+v"(acc[2][0]) : "a"(fw1[13][1]), "v"(p[3]), "v"(vHc), "s"(rsW), "s"(sh[10]), "a"(fw1[13][2]), "a"(fw1[13][3]), "a"(fw1[13][0]) : "memory"); SB();
  WAITVB(12);
  SB();
  asm volatile("v_mfma_f32_16x16x32_bf16 %0, %7, %8, %0\n\tbuffer_load_dwordx4 %1, %9, %10, %11 offen offset:512 sc1\n\tv_mfma_f32_16x16x32_bf16 %2, %12, %8, %2\n\tv_rcp_f32 %3, %3\n\tv_mfma_f32_16x16x32_bf16 %4, %13, %8, %4\n\tv_rcp_f32 %5, %5\n\tv_mfma_f32_16x16x32_bf16 %6, %14, %8, %6"
      : "+v"(acc[2][1]), "=&v"(p[3]), "+v"(acc[2][2]), "+v"(gz[0]), "+v"(acc[2][3]), "+v"(gz[1]), "+v"(acc[2][0]) : "a"(fw1[14][1]), "v"(p[4]), "v"(vHc), "s"(rsW), "s"(sh[11]), "a"(fw1[14][2]), "a"(fw1[14][3]), "a"(fw1[14][0]) : "memory"); SB();
  WAITVB(12);
  SB();
  asm volatile("v_mfma_f32_16x16x32_bf16 %0, %9, %10, %0\n\tbuffer_load_dwordx4 %1, %11, %12, %13 offen offset:512 sc1\n\tv_mfma_f32_16x16x32_bf16 %2, %14, %10, %2\n\tv_rcp_f32 %3, %3\n\tv_mfma_f32_16x16x32_bf16 %4, %15, %10, %4\n\tv_rcp_f32 %5, %5\n\tv_mfma_f32_16x16x32_bf16 %6, %16, %10, %6\n\tv_fma_f32 %7, -%17, %7, %17\n\tv_fma_f32 %8, -%18, %8, %18"
      : "+v"(acc[2][1]), "=&v"(p[4]), "+v"(acc[2][2]), "+v"(gz[2]), "+v"(acc[2][3]), "+v"(gz[3]), "+v"(acc[2][0]), "+v"(gz[0]), "+v"(gz[1]) : "v"(fw1[15][1]), "v"(p[5]), "v"(vHc), "s"(rsW), "s"(sh[12]), "v"(fw1[15][2]), "v"(fw1[15][3]), "v"(fw1[15][0]), "v"(gm[0]), "v"(gm[1]) : "memory"); SB();
  WAITVB(12);
  SB();
  asm volatile("v_mfma_f32_16x16x32_bf16 %0, %9, %10, 0\n\tbuffer_load_dwordx4 %1, %11, %12, %13 offen offset:512 sc1\n\tv_mfma_f32_16x16x32_bf16 %2, %14, %10, 0\n\tv_fma_f32 %3, -%15, %3, %15\n\tv_fma_f32 %4, -%16, %4, %16\n\tv_mfma_f32_16x16x32_bf16 %5, %17, %10, 0\n\tv_cvt_pk_bf16_f32 %6, %18, %19\n\tv_cvt_pk_bf16_f32 %7, %3, %4\n\tv_mfma_f32_16x16x32_bf16 %8, %20, %10, %21"
      : "=&v"(acc[3][1]), "=&v"(p[5]), "=&v"(acc[3][2]), "+v"(gz[2]), "+v"(gz[3]), "=&v"(acc[3][3]), "=&v"(uv[1][0]), "=&v"(uv[1][1]), "=&v"(acc[3][0]) : "a"(fw1[0][1]), "v"(p[6]), "v"(vHc), "s"(rsW), "s"(sh[13]), "a"(fw1[0][2]), "v"(gm[2]), "v"(gm[3]), "a"(fw1[0][3]), "v"(gz[0]), "v"(gz[1]), "a"(fw1[0][0]), "v"(bb) : "memory"); SB();
  WAITVB(12);
  SB();
  asm volatile("v_mfma_f32_16x16x32_bf16 %0, %6, %7, %0\n\tbuffer_load_dwordx4 %1, %8, %9, %10 offen offset:512 sc1\n\tv_mfma_f32_16x16x32_bf16 %2, %11, %7, %2\n\tds_write_b128 %12, %13 offset:1024\n\tv_mfma_f32_16x16x32_bf16 %3, %14, %7, %3\n\tbuffer_load_dword %4, %15, %9, 0 offen sc1\n\tv_mfma_f32_16x16x32_bf16 %5, %16, %7, %5"
      : "+v"(acc[3][1]), "=&v"(p[6]), "+v"(acc[3][2]), "+v"(acc[3][3]), "=&v"(fq), "+v"(acc[3][0]) : "a"(fw1[1][1]), "v"(p[7]), "v"(vHc), "s"(rsW), "s"(sh[14]), "a"(fw1[1][2]), "v"(rw), "v"(acc[2][1]), "a"(fw1[1][3]), "v"(vFQ), "a"(fw1[1][0]) : "memory"); SB();
  WAITVB(13);
  SB();
  asm volatile("v_mfma_f32_16x16x32_bf16 %0, %5, %6, %0\n\tbuffer_load_dwordx4 %1, %7, %8, %9 offen offset:512 sc1\n\tv_mfma_f32_16x16x32_bf16 %2, %10, %6, %2\n\tds_write_b128 %11, %12 offset:2048\n\tv_mfma_f32_16x16x32_bf16 %3, %13, %6, %3\n\tv_mfma_f32_16x16x32_bf16 %4, %14, %6, %4"
      : "+v"(acc[3][1]), "=&v"(p[7]), "+v"(acc[3][2]), "+v"(acc[3][3]), "+v"(acc[3][0]) : "a"(fw1[2][1]), "v"(p[8]), "v"(vHc), "s"(rsW), "s"(sh[15]), "a"(fw1[2][2]), "v"(rw), "v"(acc[2][2]), "a"(fw1[2][3]), "a"(fw1[2][0]) : "memory"); SB();
  WAITVB(13);
  SB();
  asm volatile("v_mfma_f32_16x16x32_bf16 %0, %5, %6, %0\n\tbuffer_load_dwordx4 %1, %7, %8, %9 offen offset:256 sc1\n\tv_mfma_f32_16x16x32_bf16 %2, %10, %6, %2\n\tds_write_b128 %11, %12 offset:3072\n\tv_mfma_f32_16x16x32_bf16 %3, %13, %6, %3\n\tv_mfma_f32_16x16x32_bf16 %4, %14, %6, %4"
      : "+v"(acc[3][1]), "=&v"(p[8]), "+v"(acc[3][2]), "+v"(acc[3][3]), "+v"(acc[3][0]) : "a"(fw1[3][1]), "v"(p[9]), "v"(vHa), "s"(rsW), "s"(sh[0]), "a"(fw1[3][2]), "v"(rw), "v"(acc[2][3]), "a"(fw1[3][3]), "a"(fw1[3][0]) : "memory"); SB();
  WAITVB(13);
  SB();
  asm volatile("v_mfma_f32_16x16x32_bf16 %0, %3, %4, %0\n\tbuffer_load_dwordx4 %1, %5, %6, %7 offen offset:256 sc1\n\tv_mfma_f32_16x16x32_bf16 %2, %8, %4, %2"
      : "+v"(acc[3][1]), "=&v"(p[9]), "+v"(acc[3][2]) : "a"(fw1[4][1]), "v"(p[10]), "v"(vHa), "s"(rsW), "s"(sh[1]), "a"(fw1[4][2]) : "memory"); SB();
  BAR();
  SB();
  asm volatile("v_mfma_f32_16x16x32_bf16 %0, %5, %6, %0\n\tds_read_b128 %1, %7 offset:1024\n\tds_read_b128 %2, %8 offset:2048\n\tv_mfma_f32_16x16x32_bf16 %3, %9, %6, %3\n\tds_read_b128 %4, %10 offset:3072"
      : "+v"(acc[3][3]), "=&v"(pm[2][0]), "=&v"(pm[2][1]), "+v"(acc[3][0]), "=&v"(pm[2][2]) : "a"(fw1[4][3]), "v"(p[10]), "v"(rr1), "v"(rr2), "a"(fw1[4][0]), "v"(rr3) : "memory"); SB();
  WAITVB(13);
  SB();
  asm volatile("v_mfma_f32_16x16x32_bf16 %0, %5, %6, %0\n\tbuffer_load_dwordx4 %1, %7, %8, %9 offen offset:256 sc1\n\tv_mfma_f32_16x16x32_bf16 %2, %10, %6, %2\n\tv_mfma_f32_16x16x32_bf16 %3, %11, %6, %3\n\tv_mfma_f32_16x16x32_bf16 %4, %12, %6, %4"
      : "+v"(acc[3][1]), "=&v"(p[10]), "+v"(acc[3][2]), "+v"(acc[3][3]), "+v"(acc[3][0]) : "a"(fw1[5][1]), "v"(p[11]), "v"(vHa), "s"(rsW), "s"(sh[2]), "a"(fw1[5][2]), "a"(fw1[5][3]), "a"(fw1[5][0]) : "memory"); SB();
  WAITVB(13);
  SB();
  asm volatile("v_mfma_f32_16x16x32_bf16 %0, %3, %4, %0\n\tbuffer_load_dwordx4 %1, %5, %6, %7 offen offset:256 sc1\n\tv_mfma_f32_16x16x32_bf16 %2, %8, %4, %2"
      : "+v"(acc[3][1]), "=&v"(p[11]), "+v"(acc[3][2]) : "a"(fw1[6][1]), "v"(p[12]), "v"(vHa), "s"(rsW), "s"(sh[3]), "a"(fw1[6][2]) : "memory"); SB();
  WAITL(0);
  asm volatile("v_add_f32 %0, %6, %7\n\tv_mfma_f32_16x16x32_bf16 %1, %8, %9, %1\n\tv_add_f32 %2, %10, %11\n\tv_add_f32 %3, %12, %13\n\tv_mfma_f32_16x16x32_bf16 %4, %14, %9, %4\n\tv_add_f32 %5, %15, %16"
      : "=&v"(gm[0]), "+v"(acc[3][3]), "=&v"(gm[1]), "=&v"(gm[2]), "+v"(acc[3][0]), "=&v"(gm[3]) : "v"(acc[2][0][0]), "v"(pm[2][0][0]), "a"(fw1[6][3]), "v"(p[12]), "v"(acc[2][0][1]), "v"(pm[2][0][1]), "v"(acc[2][0][2]), "v"(pm[2][0][2]), "a"(fw1[6][0]), "v"(acc[2][0][3]), "v"(pm[2][0][3])); SB();
  WAITVB(13);
  SB();
  asm volatile("v_mfma_f32_16x16x32_bf16 %0, %9, %10, %0\n\tbuffer_load_dwordx4 %1, %11, %12, %13 offen offset:256 sc1\n\tv_mfma_f32_16x16x32_bf16 %2, %14, %10, %2\n\tv_add_f32 %3, %15, %16\n\tv_add_f32 %4, %17, %18\n\tv_mfma_f32_16x16x32_bf16 %5, %19, %10, %5\n\tv_add_f32 %6, %20, %21\n\tv_add_f32 %7, %22, %23\n\tv_mfma_f32_16x16x32_bf16 %8, %24, %10, %8"
      : "+v"(acc[3][1]), "=&v"(p[12]), "+v"(acc[3][2]), "=&v"(gz[0]), "=&v"(gz[1]), "+v"(acc[3][3]), "=&v"(gz[2]), "=&v"(gz[3]), "+v"(acc[3][0]) : "a"(fw1[7][1]), "v"(p[13]), "v"(vHa), "s"(rsW), "s"(sh[4]), "a"(fw1[7][2]), "v"(pm[2][1][0]), "v"(pm[2][2][0]), "v"(pm[2][1][1]), "v"(pm[2][2][1]), "a"(fw1[7][3]), "v"(pm[2][1][2]), "v"(pm[2][2][2]), "v"(pm[2][1][3]), "v"(pm[2][2][3]), "a"(fw1[7][0]) : "memory"); SB();
  asm volatile("v_add_f32 %0, %0, %1"
      : "+v"(gm[0]) : "v"(gz[0])); SB();
  WAITVB(13);
  SB();
  asm volatile("v_mfma_f32_16x16x32_bf16 %0, %10, %11, %0\n\tbuffer_load_dwordx4 %1, %12, %13, %14 offen offset:256 sc1\n\tv_mfma_f32_16x16x32_bf16 %2, %15, %11, %2\n\tv_add_f32 %3, %3, %4\n\tv_add_f32 %5, %5, %16\n\tv_mfma_f32_16x16x32_bf16 %6, %17, %11, %6\n\tv_add_f32 %7, %7, %18\n\tv_mul_f32 %8, %19, %19\n\tv_mfma_f32_16x16x32_bf16 %9, %20, %11, %9\n\tv_mul_f32 %4, %3, %3"
      : "+v"(acc[3][1]), "=&v"(p[13]), "+v"(acc[3][2]), "+v"(gm[1]), "+v"(gz[1]), "+v"(gm[2]), "+v"(acc[3][3]), "+v"(gm[3]), "=&v"(gz[0]), "+v"(acc[3][0]) : "a"(fw1[8][1]), "v"(p[0]), "v"(vHa), "s"(rsW), "s"(sh[5]), "a"(fw1[8][2]), "v"(gz[2]), "a"(fw1[8][3]), "v"(gz[3]), "v"(gm[0]), "a"(fw1[8][0]) : "memory"); SB();
  WAITVB(13);
  SB();
  asm volatile("v_mfma_f32_16x16x32_bf16 %0, %9, %10, %0\n\tbuffer_load_dwordx4 %1, %11, %12, %13 offen offset:256 sc1\n\tv_mfma_f32_16x16x32_bf16 %2, %14, %10, %2\n\tv_mul_f32 %3, %15, %15\n\tv_mul_f32 %4, %16, %16\n\tv_mfma_f32_16x16x32_bf16 %5, %17, %10, %5\n\tv_fmaak_f32 %6, %18, %6, 0x40135761\n\tv_fmaak_f32 %7, %18, %7, 0x40135761\n\tv_mfma_f32_16x16x32_bf16 %8, %19, %10, %8\n\tv_fmaak_f32 %3, %18, %3, 0x40135761"
      : "+v"(acc[3][1]), "=&v"(p[0]), "+v"(acc[3][2]), "=&v"(gz[2]), "=&v"(gz[3]), "+v"(acc[3][3]), "+v"(gz[0]), "+v"(gz[1]), "+v"(acc[3][0]) : "a"(fw1[9][1]), "v"(p[1]), "v"(vHa), "s"(rsW), "s"(sh[6]), "a"(fw1[9][2]), "v"(gm[2]), "v"(gm[3]), "a"(fw1[9][3]), "v"(gc), "a"(fw1[9][0]) : "memory"); SB();
  WAITVB(13);
  SB();
  asm volatile("v_mfma_f32_16x16x32_bf16 %0, %9, %10, %0\n\tbuffer_load_dwordx4 %1, %11, %12, %13 offen offset:256 sc1\n\tv_mfma_f32_16x16x32_bf16 %2, %14, %10, %2\n\tv_fmaak_f32 %3, %15, %3, 0x40135761\n\tv_mul_f32 %4, %4, %16\n\tv_mfma_f32_16x16x32_bf16 %5, %17, %10, %5\n\tv_mul_f32 %6, %6, %18\n\tv_mul_f32 %7, %7, %19\n\tv_mfma_f32_16x16x32_bf16 %8, %20, %10, %8\n\tv_mul_f32 %3, %3, %21"
      : "+v"(acc[3][1]), "=&v"(p[1]), "+v"(acc[3][2]), "+v"(gz[3]), "+v"(gz[0]), "+v"(acc[3][3]), "+v"(gz[1]), "+v"(gz[2]), "+v"(acc[3][0]) : "a"(fw1[10][1]), "v"(p[2]), "v"(vHa), "s"(rsW), "s"(sh[7]), "a"(fw1[10][2]), "v"(gc), "v"(gm[0]), "a"(fw1[10][3]), "v"(gm[1]), "v"(gm[2]), "a"(fw1[10][0]), "v"(gm[3]) : "memory"); SB();
  WAITVB(13);
  SB();
  asm volatile("v_mfma_f32_16x16x32_bf16 %0, %7, %8, %0\n\tbuffer_load_dwordx4 %1, %9, %10, %11 offen offset:256 sc1\n\tv_mfma_f32_16x16x32_bf16 %2, %12, %8, %2\n\tv_exp_f32 %3, %3\n\tv_mfma_f32_16x16x32_bf16 %4, %13, %8, %4\n\tv_exp_f32 %5, %5\n\tv_mfma_f32_16x16x32_bf16 %6, %14, %8, %6"
      : "+v"(acc[3][1]), "=&v"(p[2]), "+v"(acc[3][2]), "+v"(gz[0]), "+v"(acc[3][3]), "+v"(gz[1]), "+v"(acc[3][0]) : "a"(fw1[11][1]), "v"(p[3]), "v"(vHa), "s"(rsW), "s"(sh[8]), "a"(fw1[11][2]), "a"(fw1[11][3]), "a"(fw1[11][0]) : "memory"); SB();
  WAITVB(13);
  SB();
  asm volatile("v_mfma_f32_16x16x32_bf16 %0, %8, %9, %0\n\tbuffer_load_dwordx4 %1, %10, %11, %12 offen offset:256 sc1\n\tv_mfma_f32_16x16x32_bf16 %2, %13, %9, %2\n\tv_exp_f32 %3, %3\n\tv_mfma_f32_16x16x32_bf16 %4, %14, %9, %4\n\tv_exp_f32 %5, %5\n\tv_mfma_f32_16x16x32_bf16 %6, %15, %9, %6\n\tv_add_f32 %7, 1.0, %7"
      : "+v"(acc[3][1]), "=&v"(p[3]), "+v"(acc[3][2]), "+v"(gz[2]), "+v"(acc[3][3]), "+v"(gz[3]), "+v"(acc[3][0]), "+v"(gz[0]) : "a"(fw1[12][1]), "v"(p[4]), "v"(vHa), "s"(rsW), "s"(sh[9]), "a"(fw1[12][2]), "a"(fw1[12][3]), "a"(fw1[12][0]) : "memory"); SB();
  WAITVB(13);
  SB();
  asm volatile("v_mfma_f32_16x16x32_bf16 %0, %8, %9, %0\n\tbuffer_load_dwordx4 %1, %10, %11, %12 offen offset:256 sc1\n\tv_mfma_f32_16x16x32_bf16 %2, %13, %9, %2\n\tv_add_f32 %3, 1.0, %3\n\tv_add_f32 %4, 1.0, %4\n\tv_mfma_f32_16x16x32_bf16 %5, %14, %9, %5\n\tv_add_f32 %6, 1.0, %6\n\tv_mfma_f32_16x16x32_bf16 %7, %15, %9, %7"
      : "+v"(acc[3][1]), "=&v"(p[4]), "+v"(acc[3][2]), "+v"(gz[1]), "+v"(gz[2]), "+v"(acc[3][3]), "+v"(gz[3]), "+v"(acc[3][0]) : "a"(fw1[13][1]), "v"(p[5]), "v"(vHa), "s"(rsW), "s"(sh[10]), "a"(fw1[13][2]), "a"(fw1[13][3]), "a"(fw1[13][0]) : "memory"); SB();
  WAITVB(13);
  SB();
  asm volatile("v_mfma_f32_16x16x32_bf16 %0, %7, %8, %0\n\tbuffer_load_dwordx4 %1, %9, %10, %11 offen offset:256 sc1\n\tv_mfma_f32_16x16x32_bf16 %2, %12, %8, %2\n\tv_rcp_f32 %3, %3\n\tv_mfma_f32_16x16x32_bf16 %4, %13, %8, %4\n\tv_rcp_f32 %5, %5\n\tv_mfma_f32_16x16x32_bf16 %6, %14, %8, %6"
      : "+v"(acc[3][1]), "=&v"(p[5]), "+v"(acc[3][2]), "+v"(gz[0]), "+v"(acc[3][3]), "+v"(gz[1]), "+v"(acc[3][0]) : "a"(fw1[14][1]), "v"(p[6]), "v"(vHa), "s"(rsW), "s"(sh[11]), "a"(fw1[14][2]), "a"(fw1[14][3]), "a"(fw1[14][0]) : "memory"); SB();
  WAITVB(12);
  SB();
  asm volatile("v_mfma_f32_16x16x32_bf16 %0, %9, %10, %0\n\tbuffer_load_dwordx4 %1, %11, %12, %13 offen offset:256 sc1\n\tv_mfma_f32_16x16x32_bf16 %2, %14, %10, %2\n\tv_rcp_f32 %3, %3\n\tv_mfma_f32_16x16x32_bf16 %4, %15, %10, %4\n\tv_rcp_f32 %5, %5\n\tv_mfma_f32_16x16x32_bf16 %6, %16, %10, %6\n\tv_fma_f32 %7, -%17, %7, %17\n\tv_fma_f32 %8, -%18, %8, %18"
      : "+v"(acc[3][1]), "=&v"(p[6]), "+v"(acc[3][2]), "+v"(gz[2]), "+v"(acc[3][3]), "+v"(gz[3]), "+v"(acc[3][0]), "+v"(gz[0]), "+v"(gz[1]) : "v"(fw1[15][1]), "v"(p[7]), "v"(vHa), "s"(rsW), "s"(sh[12]), "v"(fw1[15][2]), "v"(fw1[15][3]), "v"(fw1[15][0]), "v"(gm[0]), "v"(gm[1]) : "memory"); SB();
  STAMP(9);
  WAITVB(12);
  SB();
  asm volatile("v_mfma_f32_16x16x32_bf16 %0, %9, %10, 0\n\tbuffer_load_dwordx4 %1, %11, %12, %13 offen offset:256 sc1\n\tv_mfma_f32_16x16x32_bf16 %2, %14, %10, 0\n\tv_fma_f32 %3, -%15, %3, %15\n\tv_fma_f32 %4, -%16, %4, %16\n\tv_mfma_f32_16x16x32_bf16 %5, %17, %10, 0\n\tv_cvt_pk_bf16_f32 %6, %18, %19\n\tv_cvt_pk_bf16_f32 %7, %3, %4\n\tv_mfma_f32_16x16x32_bf16 %8, %20, %10, %21"
      : "=&v"(acc[4][1]), "=&v"(p[7]), "=&v"(acc[4][2]), "+v"(gz[2]), "+v"(gz[3]), "=&v"(acc[4][3]), "=&v"(uv[2][0]), "=&v"(uv[2][1]), "=&v"(acc[4][0]) : "a"(fw1[0][1]), "v"(p[8]), "v"(vHa), "s"(rsW), "s"(sh[13]), "a"(fw1[0][2]), "v"(gm[2]), "v"(gm[3]), "a"(fw1[0][3]), "v"(gz[0]), "v"(gz[1]), "a"(fw1[0][0]), "v"(bb) : "memory"); SB();
  WAITVB(12);
  SB();
  asm volatile("v_mfma_f32_16x16x32_bf16 %0, %5, %6, %0\n\tbuffer_load_dwordx4 %1, %7, %8, %9 offen offset:256 sc1\n\tv_mfma_f32_16x16x32_bf16 %2, %10, %6, %2\n\tds_write_b128 %11, %12 offset:5120\n\tv_mfma_f32_16x16x32_bf16 %3, %13, %6, %3\n\tv_mfma_f32_16x16x32_bf16 %4, %14, %6, %4"
      : "+v"(acc[4][1]), "=&v"(p[8]), "+v"(acc[4][2]), "+v"(acc[4][3]), "+v"(acc[4][0]) : "a"(fw1[1][1]), "v"(p[9]), "v"(vHa), "s"(rsW), "s"(sh[14]), "a"(fw1[1][2]), "v"(rw), "v"(acc[3][1]), "a"(fw1[1][3]), "a"(fw1[1][0]) : "memory"); SB();
  WAITVB(12);
  SB();
  asm volatile("v_mfma_f32_16x16x32_bf16 %0, %5, %6, %0\n\tbuffer_load_dwordx4 %1, %7, %8, %9 offen offset:256 sc1\n\tv_mfma_f32_16x16x32_bf16 %2, %10, %6, %2\n\tds_write_b128 %11, %12 offset:6144\n\tv_mfma_f32_16x16x32_bf16 %3, %13, %6, %3\n\tv_mfma_f32_16x16x32_bf16 %4, %14, %6, %4"
      : "+v"(acc[4][1]), "=&v"(p[9]), "+v"(acc[4][2]), "+v"(acc[4][3]), "+v"(acc[4][0]) : "a"(fw1[2][1]), "v"(p[10]), "v"(vHa), "s"(rsW), "s"(sh[15]), "a"(fw1[2][2]), "v"(rw), "v"(acc[3][2]), "a"(fw1[2][3]), "a"(fw1[2][0]) : "memory"); SB();
  WAITVB(12);
  SB();
  asm volatile("v_mfma_f32_16x16x32_bf16 %0, %5, %6, %0\n\tbuffer_load_dwordx4 %1, %7, %8, %9 offen offset:256 sc1\n\tv_mfma_f32_16x16x32_bf16 %2, %10, %6, %2\n\tds_write_b128 %11, %12 offset:7168\n\tv_mfma_f32_16x16x32_bf16 %3, %13, %6, %3\n\tv_mfma_f32_16x16x32_bf16 %4, %14, %6, %4"
      : "+v"(acc[4][1]), "=&v"(p[10]), "+v"(acc[4][2]), "+v"(acc[4][3]), "+v"(acc[4][0]) : "a"(fw1[3][1]), "v"(p[11]), "v"(vHc), "s"(rsW), "s"(sh[0]), "a"(fw1[3][2]), "v"(rw), "v"(acc[3][3]), "a"(fw1[3][3]), "a"(fw1[3][0]) : "memory"); SB();
  WAITVB(12);
  SB();
  asm volatile("v_mfma_f32_16x16x32_bf16 %0, %3, %4, %0\n\tbuffer_load_dwordx4 %1, %5, %6, %7 offen offset:256 sc1\n\tv_mfma_f32_16x16x32_bf16 %2, %8, %4, %2"
      : "+v"(acc[4][1]), "=&v"(p[11]), "+v"(acc[4][2]) : "a"(fw1[4][1]), "v"(p[12]), "v"(vHc), "s"(rsW), "s"(sh[1]), "a"(fw1[4][2]) : "memory"); SB();
  BAR();
  SB();
  asm volatile("v_mfma_f32_16x16x32_bf16 %0, %5, %6, %0\n\tds_read_b128 %1, %7 offset:5120\n\tds_read_b128 %2, %8 offset:6144\n\tv_mfma_f32_16x16x32_bf16 %3, %9, %6, %3\n\tds_read_b128 %4, %10 offset:7168"
      : "+v"(acc[4][3]), "=&v"(pm[3][0]), "=&v"(pm[3][1]), "+v"(acc[4][0]), "=&v"(pm[3][2]) : "a"(fw1[4][3]), "v"(p[12]), "v"(rr1), "v"(rr2), "a"(fw1[4][0]), "v"(rr3) : "memory"); SB();
  WAITVB(12);
  SB();
  asm volatile("v_mfma_f32_16x16x32_bf16 %0, %5, %6, %0\n\tbuffer_load_dwordx4 %1, %7, %8, %9 offen offset:256 sc1\n\tv_mfma_f32_16x16x32_bf16 %2, %10, %6, %2\n\tv_mfma_f32_16x16x32_bf16 %3, %11, %6, %3\n\tv_mfma_f32_16x16x32_bf16 %4, %12, %6, %4"
      : "+v"(acc[4][1]), "=&v"(p[12]), "+v"(acc[4][2]), "+v"(acc[4][3]), "+v"(acc[4][0]) : "a"(fw1[5][1]), "v"(p[13]), "v"(vHc), "s"(rsW), "s"(sh[2]), "a"(fw1[5][2]), "a"(fw1[5][3]), "a"(fw1[5][0]) : "memory"); SB();
  WAITVB(12);
  SB();
  asm volatile("v_mfma_f32_16x16x32_bf16 %0, %3, %4, %0\n\tbuffer_load_dwordx4 %1, %5, %6, %7 offen offset:256 sc1\n\tv_mfma_f32_16x16x32_bf16 %2, %8, %4, %2"
      : "+v"(acc[4][1]), "=&v"(p[13]), "+v"(acc[4][2]) : "a"(fw1[6][1]), "v"(p[0]), "v"(vHc), "s"(rsW), "s"(sh[3]), "a"(fw1[6][2]) : "memory"); SB();
  WAITL(0);
  asm volatile("v_add_f32 %0, %6, %7\n\tv_mfma_f32_16x16x32_bf16 %1, %8, %9, %1\n\tv_add_f32 %2, %10, %11\n\tv_add_f32 %3, %12, %13\n\tv_mfma_f32_16x16x32_bf16 %4, %14, %9, %4\n\tv_add_f32 %5, %15, %16"
      : "=&v"(gm[0]), "+v"(acc[4][3]), "=&v"(gm[1]), "=&v"(gm[2]), "+v"(acc[4][0]), "=&v"(gm[3]) : "v"(acc[3][0][0]), "v"(pm[3][0][0]), "a"(fw1[6][3]), "v"(p[0]), "v"(acc[3][0][1]), "v"(pm[3][0][1]), "v"(acc[3][0][2]), "v"(pm[3][0][2]), "a"(fw1[6][0]), "v"(acc[3][0][3]), "v"(pm[3][0][3])); SB();
  WAITVB(12);
  SB();
  asm volatile("v_mfma_f32_16x16x32_bf16 %0, %9, %10, %0\n\tbuffer_load_dwordx4 %1, %11, %12, %13 offen offset:256 sc1\n\tv_mfma_f32_16x16x32_bf16 %2, %14, %10, %2\n\tv_add_f32 %3, %15, %16\n\tv_add_f32 %4, %17, %18\n\tv_mfma_f32_16x16x32_bf16 %5, %19, %10, %5\n\tv_add_f32 %6, %20, %21\n\tv_add_f32 %7, %22, %23\n\tv_mfma_f32_16x16x32_bf16 %8, %24, %10, %8"
      : "+v"(acc[4][1]), "=&v"(p[0]), "+v"(acc[4][2]), "=&v"(gz[0]), "=&v"(gz[1]), "+v"(acc[4][3]), "=&v"(gz[2]), "=&v"(gz[3]), "+v"(acc[4][0]) : "a"(fw1[7][1]), "v"(p[1]), "v"(vHc), "s"(rsW), "s"(sh[4]), "a"(fw1[7][2]), "v"(pm[3][1][0]), "v"(pm[3][2][0]), "v"(pm[3][1][1]), "v"(pm[3][2][1]), "a"(fw1[7][3]), "v"(pm[3][1][2]), "v"(pm[3][2][2]), "v"(pm[3][1][3]), "v"(pm[3][2][3]), "a"(fw1[7][0]) : "memory"); SB();
  asm volatile("v_add_f32 %0, %0, %1"
      : "+v"(gm[0]) : "v"(gz[0])); SB();
  WAITVB(12);
  SB();
  asm volatile("v_mfma_f32_16x16x32_bf16 %0, %10, %11, %0\n\tbuffer_load_dwordx4 %1, %12, %13, %14 offen offset:256 sc1\n\tv_mfma_f32_16x16x32_bf16 %2, %15, %11, %2\n\tv_add_f32 %3, %3, %4\n\tv_add_f32 %5, %5, %16\n\tv_mfma_f32_16x16x32_bf16 %6, %17, %11, %6\n\tv_add_f32 %7, %7, %18\n\tv_mul_f32 %8, %19, %19\n\tv_mfma_f32_16x16x32_bf16 %9, %20, %11, %9\n\tv_mul_f32 %4, %3, %3"
      : "+v"(acc[4][1]), "=&v"(p[1]), "+v"(acc[4][2]), "+v"(gm[1]), "+v"(gz[1]), "+v"(gm[2]), "+v"(acc[4][3]), "+v"(gm[3]), "=&v"(gz[0]), "+v"(acc[4][0]) : "a"(fw1[8][1]), "v"(p[2]), "v"(vHc), "s"(rsW), "s"(sh[5]), "a"(fw1[8][2]), "v"(gz[2]), "a"(fw1[8][3]), "v"(gz[3]), "v"(gm[0]), "a"(fw1[8][0]) : "memory"); SB();
  WAITVB(12);
  SB();
  asm volatile("v_mfma_f32_16x16x32_bf16 %0, %9, %10, %0\n\tbuffer_load_dwordx4 %1, %11, %12, %13 offen offset:256 sc1\n\tv_mfma_f32_16x16x32_bf16 %2, %14, %10, %2\n\tv_mul_f32 %3, %15, %15\n\tv_mul_f32 %4, %16, %16\n\tv_mfma_f32_16x16x32_bf16 %5, %17, %10, %5\n\tv_fmaak_f32 %6, %18, %6, 0x40135761\n\tv_fmaak_f32 %7, %18, %7, 0x40135761\n\tv_mfma_f32_16x16x32_bf16 %8, %19, %10, %8\n\tv_fmaak_f32 %3, %18, %3, 0x40135761"
      : "+v"(acc[4][1]), "=&v"(p[2]), "+v"(acc[4][2]), "=&v"(gz[2]), "=&v"(gz[3]), "+v"(acc[4][3]), "+v"(gz[0]), "+v"(gz[1]), "+v"(acc[4][0]) : "a"(fw1[9][1]), "v"(p[3]), "v"(vHc), "s"(rsW), "s"(sh[6]), "a"(fw1[9][2]), "v"(gm[2]), "v"(gm[3]), "a"(fw1[9][3]), "v"(gc), "a"(fw1[9][0]) : "memory"); SB();
  WAITVB(12);
  SB();
  asm volatile("v_mfma_f32_16x16x32_bf16 %0, %9, %10, %0\n\tbuffer_load_dwordx4 %1, %11, %12, %13 offen offset:256 sc1\n\tv_mfma_f32_16x16x32_bf16 %2, %14, %10, %2\n\tv_fmaak_f32 %3, %15, %3, 0x40135761\n\tv_mul_f32 %4, %4, %16\n\tv_mfma_f32_16x16x32_bf16 %5, %17, %10, %5\n\tv_mul_f32 %6, %6, %18\n\tv_mul_f32 %7, %7, %19\n\tv_mfma_f32_16x16x32_bf16 %8, %20, %10, %8\n\tv_mul_f32 %3, %3, %21"
      : "+v"(acc[4][1]), "=&v"(p[3]), "+v"(acc[4][2]), "+v"(gz[3]), "+v"(gz[0]), "+v"(acc[4][3]), "+v"(gz[1]), "+v"(gz[2]), "+v"(acc[4][0]) : "a"(fw1[10][1]), "v"(p[4]), "v"(vHc), "s"(rsW), "s"(sh[7]), "a"(fw1[10][2]), "v"(gc), "v"(gm[0]), "a"(fw1[10][3]), "v"(gm[1]), "v"(gm[2]), "a"(fw1[10][0]), "v"(gm[3]) : "memory"); SB();
  WAITVB(12);
  SB();
  asm volatile("v_mfma_f32_16x16x32_bf16 %0, %7, %8, %0\n\tbuffer_load_dwordx4 %1, %9, %10, %11 offen offset:256 sc1\n\tv_mfma_f32_16x16x32_bf16 %2, %12, %8, %2\n\tv_exp_f32 %3, %3\n\tv_mfma_f32_16x16x32_bf16 %4, %13, %8, %4\n\tv_exp_f32 %5, %5\n\tv_mfma_f32_16x16x32_bf16 %6, %14, %8, %6"
      : "+v"(acc[4][1]), "=&v"(p[4]), "+v"(acc[4][2]), "+v"(gz[0]), "+v"(acc[4][3]), "+v"(gz[1]), "+v"(acc[4][0]) : "a"(fw1[11][1]), "v"(p[5]), "v"(vHc), "s"(rsW), "s"(sh[8]), "a"(fw1[11][2]), "a"(fw1[11][3]), "a"(fw1[11][0]) : "memory"); SB();
  WAITVB(12);
  SB();
  asm volatile("v_mfma_f32_16x16x32_bf16 %0, %8, %9, %0\n\tbuffer_load_dwordx4 %1, %10, %11, %12 offen offset:256 sc1\n\tv_mfma_f32_16x16x32_bf16 %2, %13, %9, %2\n\tv_exp_f32 %3, %3\n\tv_mfma_f32_16x16x32_bf16 %4, %14, %9, %4\n\tv_exp_f32 %5, %5\n\tv_mfma_f32_16x16x32_bf16 %6, %15, %9, %6\n\tv_add_f32 %7, 1.0, %7"
      : "+v"(acc[4][1]), "=&v"(p[5]), "+v"(acc[4][2]), "+v"(gz[2]), "+v"(acc[4][3]), "+v"(gz[3]), "+v"(acc[4][0]), "+v"(gz[0]) : "a"(fw1[12][1]), "v"(p[6]), "v"(vHc), "s"(rsW), "s"(sh[9]), "a"(fw1[12][2]), "a"(fw1[12][3]), "a"(fw1[12][0]) : "memory"); SB();
  WAITVB(12);
  SB();
  asm volatile("v_mfma_f32_16x16x32_bf16 %0, %8, %9, %0\n\tbuffer_load_dwordx4 %1, %10, %11, %12 offen offset:256 sc1\n\tv_mfma_f32_16x16x32_bf16 %2, %13, %9, %2\n\tv_add_f32 %3, 1.0, %3\n\tv_add_f32 %4, 1.0, %4\n\tv_mfma_f32_16x16x32_bf16 %5, %14, %9, %5\n\tv_add_f32 %6, 1.0, %6\n\tv_mfma_f32_16x16x32_bf16 %7, %15, %9, %7"
      : "+v"(acc[4][1]), "=&v"(p[6]), "+v"(acc[4][2]), "+v"(gz[1]), "+v"(gz[2]), "+v"(acc[4][3]), "+v"(gz[3]), "+v"(acc[4][0]) : "a"(fw1[13][1]), "v"(p[7]), "v"(vHc), "s"(rsW), "s"(sh[10]), "a"(fw1[13][2]), "a"(fw1[13][3]), "a"(fw1[13][0]) : "memory"); SB();
  WAITVB(12);
  SB();
  asm volatile("v_mfma_f32_16x16x32_bf16 %0, %7, %8, %0\n\tbuffer_load_dwordx4 %1, %9, %10, %11 offen offset:256 sc1\n\tv_mfma_f32_16x16x32_bf16 %2, %12, %8, %2\n\tv_rcp_f32 %3, %3\n\tv_mfma_f32_16x16x32_bf16 %4, %13, %8, %4\n\tv_rcp_f32 %5, %5\n\tv_mfma_f32_16x16x32_bf16 %6, %14, %8, %6"
      : "+v"(acc[4][1]), "=&v"(p[7]), "+v"(acc[4][2]), "+v"(gz[0]), "+v"(acc[4][3]), "+v"(gz[1]), "+v"(acc[4][0]) : "a"(fw1[14][1]), "v"(p[8]), "v"(vHc), "s"(rsW), "s"(sh[11]), "a"(fw1[14][2]), "a"(fw1[14][3]), "a"(fw1[14][0]) : "memory"); SB();
  WAITVB(12);
  SB();
  asm volatile("v_mfma_f32_16x16x32_bf16 %0, %9, %10, %0\n\tbuffer_load_dwordx4 %1, %11, %12, %13 offen offset:256 sc1\n\tv_mfma_f32_16x16x32_bf16 %2, %14, %10, %2\n\tv_rcp_f32 %3, %3\n\tv_mfma_f32_16x16x32_bf16 %4, %15, %10, %4\n\tv_rcp_f32 %5, %5\n\tv_mfma_f32_16x16x32_bf16 %6, %16, %10, %6\n\tv_fma_f32 %7, -%17, %7, %17\n\tv_fma_f32 %8, -%18, %8, %18"
      : "+v"(acc[4][1]), "=&v"(p[8]), "+v"(acc[4][2]), "+v"(gz[2]), "+v"(acc[4][3]), "+v"(gz[3]), "+v"(acc[4][0]), "+v"(gz[0]), "+v"(gz[1]) : "v"(fw1[15][1]), "v"(p[9]), "v"(vHc), "s"(rsW), "s"(sh[12]), "v"(fw1[15][2]), "v"(fw1[15][3]), "v"(fw1[15][0]), "v"(gm[0]), "v"(gm[1]) : "memory"); SB();
  WAITVB(12);
  SB();
  asm volatile("v_mfma_f32_16x16x32_bf16 %0, %9, %10, 0\n\tbuffer_load_dwordx4 %1, %11, %12, %13 offen offset:256 sc1\n\tv_mfma_f32_16x16x32_bf16 %2, %14, %10, 0\n\tv_fma_f32 %3, -%15, %3, %15\n\tv_fma_f32 %4, -%16, %4, %16\n\tv_mfma_f32_16x16x32_bf16 %5, %17, %10, 0\n\tv_cvt_pk_bf16_f32 %6, %18, %19\n\tv_cvt_pk_bf16_f32 %7, %3, %4\n\tv_mfma_f32_16x16x32_bf16 %8, %20, %10, %21"
      : "=&v"(acc[5][1]), "=&v"(p[9]), "=&v"(acc[5][2]), "+v"(gz[2]), "+v"(gz[3]), "=&v"(acc[5][3]), "=&v"(uv[3][0]), "=&v"(uv[3][1]), "=&v"(acc[5][0]) : "a"(fw1[0][1]), "v"(p[10]), "v"(vHc), "s"(rsW), "s"(sh[13]), "a"(fw1[0][2]), "v"(gm[2]), "v"(gm[3]), "a"(fw1[0][3]), "v"(gz[0]), "v"(gz[1]), "a"(fw1[0][0]), "v"(bb) : "memory"); SB();
  WAITVB(12);
  SB();
  asm volatile("v_mfma_f32_16x16x32_bf16 %0, %5, %6, %0\n\tbuffer_load_dwordx4 %1, %7, %8, %9 offen offset:256 sc1\n\tv_mfma_f32_16x16x32_bf16 %2, %10, %6, %2\n\tds_write_b128 %11, %12 offset:1024\n\tv_mfma_f32_16x16x32_bf16 %3, %13, %6, %3\n\tv_mfma_f32_16x16x32_bf16 %4, %14, %6, %4"
      : "+v"(acc[5][1]), "=&v"(p[10]), "+v"(acc[5][2]), "+v"(acc[5][3]), "+v"(acc[5][0]) : "a"(fw1[1][1]), "v"(p[11]), "v"(vHc), "s"(rsW), "s"(sh[14]), "a"(fw1[1][2]), "v"(rw), "v"(acc[4][1]), "a"(fw1[1][3]), "a"(fw1[1][0]) : "memory"); SB();
  WAITVB(12);
  SB();
  asm volatile("v_mfma_f32_16x16x32_bf16 %0, %5, %6, %0\n\tbuffer_load_dwordx4 %1, %7, %8, %9 offen offset:256 sc1\n\tv_mfma_f32_16x16x32_bf16 %2, %10, %6, %2\n\tds_write_b128 %11, %12 offset:2048\n\tv_mfma_f32_16x16x32_bf16 %3, %13, %6, %3\n\tv_mfma_f32_16x16x32_bf16 %4, %14, %6, %4"
      : "+v"(acc[5][1]), "=&v"(p[11]), "+v"(acc[5][2]), "+v"(acc[5][3]), "+v"(acc[5][0]) : "a"(fw1[2][1]), "v"(p[12]), "v"(vHc), "s"(rsW), "s"(sh[15]), "a"(fw1[2][2]), "v"(rw), "v"(acc[4][2]), "a"(fw1[2][3]), "a"(fw1[2][0]) : "memory"); SB();
  WAITVB(12);
  SB();
  asm volatile("v_mfma_f32_16x16x32_bf16 %0, %5, %6, %0\n\tbuffer_load_dwordx4 %1, %7, %8, %9 offen offset:0 sc1\n\tv_mfma_f32_16x16x32_bf16 %2, %10, %6, %2\n\tds_write_b128 %11, %12 offset:3072\n\tv_mfma_f32_16x16x32_bf16 %3, %13, %6, %3\n\tv_mfma_f32_16x16x32_bf16 %4, %14, %6, %4"
      : "+v"(acc[5][1]), "=&v"(p[12]), "+v"(acc[5][2]), "+v"(acc[5][3]), "+v"(acc[5][0]) : "a"(fw1[3][1]), "v"(p[13]), "v"(vHb), "s"(rsW), "s"(sh[0]), "a"(fw1[3][2]), "v"(rw), "v"(acc[4][3]), "a"(fw1[3][3]), "a"(fw1[3][0]) : "memory"); SB();
  WAITVB(12);
  SB();
  asm volatile("v_mfma_f32_16x16x32_bf16 %0, %3, %4, %0\n\tbuffer_load_dwordx4 %1, %5, %6, %7 offen offset:0 sc1\n\tv_mfma_f32_16x16x32_bf16 %2, %8, %4, %2"
      : "+v"(acc[5][1]), "=&v"(p[13]), "+v"(acc[5][2]) : "a"(fw1[4][1]), "v"(p[0]), "v"(vHb), "s"(rsW), "s"(sh[1]), "a"(fw1[4][2]) : "memory"); SB();
  BAR();
  SB();
  asm volatile("v_mfma_f32_16x16x32_bf16 %0, %5, %6, %0\n\tds_read_b128 %1, %7 offset:1024\n\tds_read_b128 %2, %8 offset:2048\n\tv_mfma_f32_16x16x32_bf16 %3, %9, %6, %3\n\tds_read_b128 %4, %10 offset:3072"
      : "+v"(acc[5][3]), "=&v"(pm[4][0]), "=&v"(pm[4][1]), "+v"(acc[5][0]), "=&v"(pm[4][2]) : "a"(fw1[4][3]), "v"(p[0]), "v"(rr1), "v"(rr2), "a"(fw1[4][0]), "v"(rr3) : "memory"); SB();
  WAITVB(12);
  SB();
  asm volatile("v_mfma_f32_16x16x32_bf16 %0, %5, %6, %0\n\tbuffer_load_dwordx4 %1, %7, %8, %9 offen offset:0 sc1\n\tv_mfma_f32_16x16x32_bf16 %2, %10, %6, %2\n\tv_mfma_f32_16x16x32_bf16 %3, %11, %6, %3\n\tv_mfma_f32_16x16x32_bf16 %4, %12, %6, %4"
      : "+v"(acc[5][1]), "=&v"(p[0]), "+v"(acc[5][2]), "+v"(acc[5][3]), "+v"(acc[5][0]) : "a"(fw1[5][1]), "v"(p[1]), "v"(vHb), "s"(rsW), "s"(sh[2]), "a"(fw1[5][2]), "a"(fw1[5][3]), "a"(fw1[5][0]) : "memory"); SB();
  WAITVB(12);
  SB();
  asm volatile("v_mfma_f32_16x16x32_bf16 %0, %3, %4, %0\n\tbuffer_load_dwordx4 %1, %5, %6, %7 offen offset:0 sc1\n\tv_mfma_f32_16x16x32_bf16 %2, %8, %4, %2"
      : "+v"(acc[5][1]), "=&v"(p[1]), "+v"(acc[5][2]) : "a"(fw1[6][1]), "v"(p[2]), "v"(vHb), "s"(rsW), "s"(sh[3]), "a"(fw1[6][2]) : "memory"); SB();
  WAITL(0);
  asm volatile("v_add_f32 %0, %6, %7\n\tv_mfma_f32_16x16x32_bf16 %1, %8, %9, %1\n\tv_add_f32 %2, %10, %11\n\tv_add_f32 %3, %12, %13\n\tv_mfma_f32_16x16x32_bf16 %4, %14, %9, %4\n\tv_add_f32 %5, %15, %16"
      : "=&v"(gm[0]), "+v"(acc[5][3]), "=&v"(gm[1]), "=&v"(gm[2]), "+v"(acc[5][0]), "=&v"(gm[3]) : "v"(acc[4][0][0]), "v"(pm[4][0][0]), "a"(fw1[6][3]), "v"(p[2]), "v"(acc[4][0][1]), "v"(pm[4][0][1]), "v"(acc[4][0][2]), "v"(pm[4][0][2]), "a"(fw1[6][0]), "v"(acc[4][0][3]), "v"(pm[4][0][3])); SB();
  WAITVB(12);
  SB();
  asm volatile("v_mfma_f32_16x16x32_bf16 %0, %9, %10, %0\n\tbuffer_load_dwordx4 %1, %11, %12, %13 offen offset:0 sc1\n\tv_mfma_f32_16x16x32_bf16 %2, %14, %10, %2\n\tv_add_f32 %3, %15, %16\n\tv_add_f32 %4, %17, %18\n\tv_mfma_f32_16x16x32_bf16 %5, %19, %10, %5\n\tv_add_f32 %6, %20, %21\n\tv_add_f32 %7, %22, %23\n\tv_mfma_f32_16x16x32_bf16 %8, %24, %10, %8"
      : "+v"(acc[5][1]), "=&v"(p[2]), "+v"(acc[5][2]), "=&v"(gz[0]), "=&v"(gz[1]), "+v"(acc[5][3]), "=&v"(gz[2]), "=&v"(gz[3]), "+v"(acc[5][0]) : "a"(fw1[7][1]), "v"(p[3]), "v"(vHb), "s"(rsW), "s"(sh[4]), "a"(fw1[7][2]), "v"(pm[4][1][0]), "v"(pm[4][2][0]), "v"(pm[4][1][1]), "v"(pm[4][2][1]), "a"(fw1[7][3]), "v"(pm[4][1][2]), "v"(pm[4][2][2]), "v"(pm[4][1][3]), "v"(pm[4][2][3]), "a"(fw1[7][0]) : "memory"); SB();
  asm volatile("v_add_f32 %0, %0, %1"
      : "+v"(gm[0]) : "v"(gz[0])); SB();
  WAITVB(12);
  SB();
  asm volatile("v_mfma_f32_16x16x32_bf16 %0, %10, %11, %0\n\tbuffer_load_dwordx4 %1, %12, %13, %14 offen offset:0 sc1\n\tv_mfma_f32_16x16x32_bf16 %2, %15, %11, %2\n\tv_add_f32 %3, %3, %4\n\tv_add_f32 %5, %5, %16\n\tv_mfma_f32_16x16x32_bf16 %6, %17, %11, %6\n\tv_add_f32 %7, %7, %18\n\tv_mul_f32 %8, %19, %19\n\tv_mfma_f32_16x16x32_bf16 %9, %20, %11, %9\n\tv_mul_f32 %4, %3, %3"
      : "+v"(acc[5][1]), "=&v"(p[3]), "+v"(acc[5][2]), "+v"(gm[1]), "+v"(gz[1]), "+v"(gm[2]), "+v"(acc[5][3]), "+v"(gm[3]), "=&v"(gz[0]), "+v"(acc[5][0]) : "a"(fw1[8][1]), "v"(p[4]), "v"(vHb), "s"(rsW), "s"(sh[5]), "a"(fw1[8][2]), "v"(gz[2]), "a"(fw1[8][3]), "v"(gz[3]), "v"(gm[0]), "a"(fw1[8][0]) : "memory"); SB();
  WAITVB(12);
  SB();
  asm volatile("v_mfma_f32_16x16x32_bf16 %0, %9, %10, %0\n\tbuffer_load_dwordx4 %1, %11, %12, %13 offen offset:0 sc1\n\tv_mfma_f32_16x16x32_bf16 %2, %14, %10, %2\n\tv_mul_f32 %3, %15, %15\n\tv_mul_f32 %4, %16, %16\n\tv_mfma_f32_16x16x32_bf16 %5, %17, %10, %5\n\tv_fmaak_f32 %6, %18, %6, 0x40135761\n\tv_fmaak_f32 %7, %18, %7, 0x40135761\n\tv_mfma_f32_16x16x32_bf16 %8, %19, %10, %8\n\tv_fmaak_f32 %3, %18, %3, 0x40135761"
      : "+v"(acc[5][1]), "=&v"(p[4]), "+v"(acc[5][2]), "=&v"(gz[2]), "=&v"(gz[3]), "+v"(acc[5][3]), "+v"(gz[0]), "+v"(gz[1]), "+v"(acc[5][0]) : "a"(fw1[9][1]), "v"(p[5]), "v"(vHb), "s"(rsW), "s"(sh[6]), "a"(fw1[9][2]), "v"(gm[2]), "v"(gm[3]), "a"(fw1[9][3]), "v"(gc), "a"(fw1[9][0]) : "memory"); SB();
  WAITVB(12);
  SB();
  asm volatile("v_mfma_f32_16x16x32_bf16 %0, %9, %10, %0\n\tbuffer_load_dwordx4 %1, %11, %12, %13 offen offset:0 sc1\n\tv_mfma_f32_16x16x32_bf16 %2, %14, %10, %2\n\tv_fmaak_f32 %3, %15, %3, 0x40135761\n\tv_mul_f32 %4, %4, %16\n\tv_mfma_f32_16x16x32_bf16 %5, %17, %10, %5\n\tv_mul_f32 %6, %6, %18\n\tv_mul_f32 %7, %7, %19\n\tv_mfma_f32_16x16x32_bf16 %8, %20, %10, %8\n\tv_mul_f32 %3, %3, %21"
      : "+v"(acc[5][1]), "=&v"(p[5]), "+v"(acc[5][2]), "+v"(gz[3]), "+v"(gz[0]), "+v"(acc[5][3]), "+v"(gz[1]), "+v"(gz[2]), "+v"(acc[5][0]) : "a"(fw1[10][1]), "v"(p[6]), "v"(vHb), "s"(rsW), "s"(sh[7]), "a"(fw1[10][2]), "v"(gc), "v"(gm[0]), "a"(fw1[10][3]), "v"(gm[1]), "v"(gm[2]), "a"(fw1[10][0]), "v"(gm[3]) : "memory"); SB();
  WAITVB(12);
  SB();
  asm volatile("v_mfma_f32_16x16x32_bf16 %0, %7, %8, %0\n\tbuffer_load_dwordx4 %1, %9, %10, %11 offen offset:0 sc1\n\tv_mfma_f32_16x16x32_bf16 %2, %12, %8, %2\n\tv_exp_f32 %3, %3\n\tv_mfma_f32_16x16x32_bf16 %4, %13, %8, %4\n\tv_exp_f32 %5, %5\n\tv_mfma_f32_16x16x32_bf16 %6, %14, %8, %6"
      : "+v"(acc[5][1]), "=&v"(p[6]), "+v"(acc[5][2]), "+v"(gz[0]), "+v"(acc[5][3]), "+v"(gz[1]), "+v"(acc[5][0]) : "a"(fw1[11][1]), "v"(p[7]), "v"(vHb), "s"(rsW), "s"(sh[8]), "a"(fw1[11][2]), "a"(fw1[11][3]), "a"(fw1[11][0]) : "memory"); SB();
  WAITVB(12);
  SB();
  asm volatile("v_mfma_f32_16x16x32_bf16 %0, %8, %9, %0\n\tbuffer_load_dwordx4 %1, %10, %11, %12 offen offset:0 sc1\n\tv_mfma_f32_16x16x32_bf16 %2, %13, %9, %2\n\tv_exp_f32 %3, %3\n\tv_mfma_f32_16x16x32_bf16 %4, %14, %9, %4\n\tv_exp_f32 %5, %5\n\tv_mfma_f32_16x16x32_bf16 %6, %15, %9, %6\n\tv_add_f32 %7, 1.0, %7"
      : "+v"(acc[5][1]), "=&v"(p[7]), "+v"(acc[5][2]), "+v"(gz[2]), "+v"(acc[5][3]), "+v"(gz[3]), "+v"(acc[5][0]), "+v"(gz[0]) : "a"(fw1[12][1]), "v"(p[8]), "v"(vHb), "s"(rsW), "s"(sh[9]), "a"(fw1[12][2]), "a"(fw1[12][3]), "a"(fw1[12][0]) : "memory"); SB();
  WAITVB(12);
  SB();
  asm volatile("v_mfma_f32_16x16x32_bf16 %0, %8, %9, %0\n\tbuffer_load_dwordx4 %1, %10, %11, %12 offen offset:0 sc1\n\tv_mfma_f32_16x16x32_bf16 %2, %13, %9, %2\n\tv_add_f32 %3, 1.0, %3\n\tv_add_f32 %4, 1.0, %4\n\tv_mfma_f32_16x16x32_bf16 %5, %14, %9, %5\n\tv_add_f32 %6, 1.0, %6\n\tv_mfma_f32_16x16x32_bf16 %7, %15, %9, %7"
      : "+v"(acc[5][1]), "=&v"(p[8]), "+v"(acc[5][2]), "+v"(gz[1]), "+v"(gz[2]), "+v"(acc[5][3]), "+v"(gz[3]), "+v"(acc[5][0]) : "a"(fw1[13][1]), "v"(p[9]), "v"(vHb), "s"(rsW), "s"(sh[10]), "a"(fw1[13][2]), "a"(fw1[13][3]), "a"(fw1[13][0]) : "memory"); SB();
  WAITVB(12);
  SB();
  asm volatile("v_mfma_f32_16x16x32_bf16 %0, %7, %8, %0\n\tbuffer_load_dwordx4 %1, %9, %10, %11 offen offset:0 sc1\n\tv_mfma_f32_16x16x32_bf16 %2, %12, %8, %2\n\tv_rcp_f32 %3, %3\n\tv_mfma_f32_16x16x32_bf16 %4, %13, %8, %4\n\tv_rcp_f32 %5, %5\n\tv_mfma_f32_16x16x32_bf16 %6, %14, %8, %6"
      : "+v"(acc[5][1]), "=&v"(p[9]), "+v"(acc[5][2]), "+v"(gz[0]), "+v"(acc[5][3]), "+v"(gz[1]), "+v"(acc[5][0]) : "a"(fw1[14][1]), "v"(p[10]), "v"(vHb), "s"(rsW), "s"(sh[11]), "a"(fw1[14][2]), "a"(fw1[14][3]), "a"(fw1[14][0]) : "memory"); SB();
  WAITVB(12);
  SB();
  asm volatile("v_mfma_f32_16x16x32_bf16 %0, %9, %10, %0\n\tbuffer_load_dwordx4 %1, %11, %12, %13 offen offset:0 sc1\n\tv_mfma_f32_16x16x32_bf16 %2, %14, %10, %2\n\tv_rcp_f32 %3, %3\n\tv_mfma_f32_16x16x32_bf16 %4, %15, %10, %4\n\tv_rcp_f32 %5, %5\n\tv_mfma_f32_16x16x32_bf16 %6, %16, %10, %6\n\tv_fma_f32 %7, -%17, %7, %17\n\tv_fma_f32 %8, -%18, %8, %18"
      : "+v"(acc[5][1]), "=&v"(p[10]), "+v"(acc[5][2]), "+v"(gz[2]), "+v"(acc[5][3]), "+v"(gz[3]), "+v"(acc[5][0]), "+v"(gz[0]), "+v"(gz[1]) : "v"(fw1[15][1]), "v"(p[11]), "v"(vHb), "s"(rsW), "s"(sh[12]), "v"(fw1[15][2]), "v"(fw1[15][3]), "v"(fw1[15][0]), "v"(gm[0]), "v"(gm[1]) : "memory"); SB();
  WAITVB(12);
  SB();
  asm volatile("v_mfma_f32_16x16x32_bf16 %0, %9, %10, 0\n\tbuffer_load_dwordx4 %1, %11, %12, %13 offen offset:0 sc1\n\tv_mfma_f32_16x16x32_bf16 %2, %14, %10, 0\n\tv_fma_f32 %3, -%15, %3, %15\n\tv_fma_f32 %4, -%16, %4, %16\n\tv_mfma_f32_16x16x32_bf16 %5, %17, %10, 0\n\tv_cvt_pk_bf16_f32 %6, %18, %19\n\tv_cvt_pk_bf16_f32 %7, %3, %4\n\tv_mfma_f32_16x16x32_bf16 %8, %20, %10, %21"
      : "=&v"(acc[6][1]), "=&v"(p[11]), "=&v"(acc[6][2]), "+v"(gz[2]), "+v"(gz[3]), "=&v"(acc[6][3]), "=&v"(uv[4][0]), "=&v"(uv[4][1]), "=&v"(acc[6][0]) : "a"(fw1[0][1]), "v"(p[12]), "v"(vHb), "s"(rsW), "s"(sh[13]), "a"(fw1[0][2]), "v"(gm[2]), "v"(gm[3]), "a"(fw1[0][3]), "v"(gz[0]), "v"(gz[1]), "a"(fw1[0][0]), "v"(bb) : "memory"); SB();
  WAITVB(12);
  SB();
  asm volatile("v_mfma_f32_16x16x32_bf16 %0, %5, %6, %0\n\tbuffer_load_dwordx4 %1, %7, %8, %9 offen offset:0 sc1\n\tv_mfma_f32_16x16x32_bf16 %2, %10, %6, %2\n\tds_write_b128 %11, %12 offset:5120\n\tv_mfma_f32_16x16x32_bf16 %3, %13, %6, %3\n\tv_mfma_f32_16x16x32_bf16 %4, %14, %6, %4"
      : "+v"(acc[6][1]), "=&v"(p[12]), "+v"(acc[6][2]), "+v"(acc[6][3]), "+v"(acc[6][0]) : "a"(fw1[1][1]), "v"(p[13]), "v"(vHb), "s"(rsW), "s"(sh[14]), "a"(fw1[1][2]), "v"(rw), "v"(acc[5][1]), "a"(fw1[1][3]), "a"(fw1[1][0]) : "memory"); SB();
  WAITVB(12);
  SB();
  asm volatile("v_mfma_f32_16x16x32_bf16 %0, %5, %6, %0\n\tbuffer_load_dwordx4 %1, %7, %8, %9 offen offset:0 sc1\n\tv_mfma_f32_16x16x32_bf16 %2, %10, %6, %2\n\tds_write_b128 %11, %12 offset:6144\n\tv_mfma_f32_16x16x32_bf16 %3, %13, %6, %3\n\tv_mfma_f32_16x16x32_bf16 %4, %14, %6, %4"
      : "+v"(acc[6][1]), "=&v"(p[13]), "+v"(acc[6][2]), "+v"(acc[6][3]), "+v"(acc[6][0]) : "a"(fw1[2][1]), "v"(p[0]), "v"(vHb), "s"(rsW), "s"(sh[15]), "a"(fw1[2][2]), "v"(rw), "v"(acc[5][2]), "a"(fw1[2][3]), "a"(fw1[2][0]) : "memory"); SB();
  WAITVB(12);
  SB();
  asm volatile("v_mfma_f32_16x16x32_bf16 %0, %5, %6, %0\n\tv_permlane16_swap_b32 %1, %2\n\tv_permlane16_swap_b32 %3, %4"
      : "+v"(acc[6][1]), "+v"(uv[0][0]), "+v"(uv[1][0]), "+v"(uv[0][1]), "+v"(uv[1][1]) : "a"(fw1[3][1]), "v"(p[1])); SB();
  HS16(vU, sU, 0, ((u32x4){uv[0][0], uv[0][1], uv[1][0], uv[1][1]}));
  SB();
  asm volatile("v_mfma_f32_16x16x32_bf16 %0, %6, %7, %0\n\tds_write_b128 %8, %9 offset:7168\n\tv_mfma_f32_16x16x32_bf16 %1, %10, %7, %1\n\tv_permlane16_swap_b32 %2, %3\n\tv_permlane16_swap_b32 %4, %5"
      : "+v"(acc[6][2]), "+v"(acc[6][3]), "+v"(uv[2][0]), "+v"(uv[3][0]), "+v"(uv[2][1]), "+v"(uv[3][1]) : "a"(fw1[3][2]), "v"(p[1]), "v"(rw), "v"(acc[5][3]), "a"(fw1[3][3]) : "memory"); SB();
  HS16(vU, sU, 512, ((u32x4){uv[2][0], uv[2][1], uv[3][0], uv[3][1]}));
  SB();
  asm volatile("v_mfma_f32_16x16x32_bf16 %0, %1, %2, %0"
      : "+v"(acc[6][0]) : "a"(fw1[3][0]), "v"(p[1])); SB();
  WAITVB(13);
  SB();
  asm volatile("v_mfma_f32_16x16x32_bf16 %0, %2, %3, %0\n\ts_add_u32 m0, %4, 0\n\ts_nop 0\n\tv_mfma_f32_16x16x32_bf16 %1, %5, %3, %1"
      : "+v"(acc[6][1]), "+v"(acc[6][2]) : "a"(fw1[4][1]), "v"(p[2]), "s"(ldsO), "a"(fw1[4][2]) : "memory", "scc"); SB();
  BAR();
  SB();
  asm volatile("v_mfma_f32_16x16x32_bf16 %0, %5, %6, %0\n\tds_read_b128 %1, %7 offset:5120\n\tds_read_b128 %2, %8 offset:6144\n\tv_mfma_f32_16x16x32_bf16 %3, %9, %6, %3\n\tds_read_b128 %4, %10 offset:7168"
      : "+v"(acc[6][3]), "=&v"(pm[5][0]), "=&v"(pm[5][1]), "+v"(acc[6][0]), "=&v"(pm[5][2]) : "a"(fw1[4][3]), "v"(p[2]), "v"(rr1), "v"(rr2), "a"(fw1[4][0]), "v"(rr3) : "memory"); SB();
  WAITVB(12);
  SB();
  asm volatile("v_mfma_f32_16x16x32_bf16 %0, %4, %5, %0\n\tbuffer_load_dwordx4 %6, %7, %8 offen offset:0  lds\n\tv_mfma_f32_16x16x32_bf16 %1, %9, %5, %1\n\tv_mfma_f32_16x16x32_bf16 %2, %10, %5, %2\n\tbuffer_load_dwordx4 %11, %7, %8 offen offset:1024  lds\n\tv_mfma_f32_16x16x32_bf16 %3, %12, %5, %3"
      : "+v"(acc[6][1]), "+v"(acc[6][2]), "+v"(acc[6][3]), "+v"(acc[6][0]) : "a"(fw1[5][1]), "v"(p[3]), "v"(vW2[0]), "s"(rs2), "s"(sw[0]), "a"(fw1[5][2]), "a"(fw1[5][3]), "v"(vW2[1]), "a"(fw1[5][0]) : "memory"); SB();
  WAITVB(13);
  SB();
  asm volatile("v_mfma_f32_16x16x32_bf16 %0, %1, %2, %0"
      : "+v"(acc[6][1]) : "a"(fw1[6][1]), "v"(p[4])); SB();
  WAITL(0);
  asm volatile("v_add_f32 %0, %9, %10\n\tv_mfma_f32_16x16x32_bf16 %1, %11, %12, %1\n\tv_add_f32 %2, %13, %14\n\tv_add_f32 %3, %15, %16\n\tv_mfma_f32_16x16x32_bf16 %4, %17, %12, %4\n\tv_add_f32 %5, %18, %19\n\tv_add_f32 %6, %20, %21\n\tv_mfma_f32_16x16x32_bf16 %7, %22, %12, %7\n\tv_add_f32 %8, %23, %24"
      : "=&v"(gm[0]), "+v"(acc[6][2]), "=&v"(gm[1]), "=&v"(gm[2]), "+v"(acc[6][3]), "=&v"(gm[3]), "=&v"(gz[0]), "+v"(acc[6][0]), "=&v"(gz[1]) : "v"(acc[5][0][0]), "v"(pm[5][0][0]), "a"(fw1[6][2]), "v"(p[4]), "v"(acc[5][0][1]), "v"(pm[5][0][1]), "v"(acc[5][0][2]), "v"(pm[5][0][2]), "a"(fw1[6][3]), "v"(acc[5][0][3]), "v"(pm[5][0][3]), "v"(pm[5][1][0]), "v"(pm[5][2][0]), "a"(fw1[6][0]), "v"(pm[5][1][1]), "v"(pm[5][2][1])); SB();
  WAITVB(12);
  SB();
  asm volatile("v_mfma_f32_16x16x32_bf16 %0, %11, %12, %0\n\tv_add_f32 %1, %13, %14\n\tv_add_f32 %2, %15, %16\n\tv_mfma_f32_16x16x32_bf16 %3, %17, %12, %3\n\tv_add_f32 %4, %4, %5\n\tv_add_f32 %6, %6, %18\n\tv_mfma_f32_16x16x32_bf16 %7, %19, %12, %7\n\tv_add_f32 %8, %8, %1\n\tv_add_f32 %9, %9, %2\n\tv_mfma_f32_16x16x32_bf16 %10, %20, %12, %10\n\tv_mul_f32 %5, %4, %4"
      : "+v"(acc[6][1]), "=&v"(gz[2]), "=&v"(gz[3]), "+v"(acc[6][2]), "+v"(gm[0]), "+v"(gz[0]), "+v"(gm[1]), "+v"(acc[6][3]), "+v"(gm[2]), "+v"(gm[3]), "+v"(acc[6][0]) : "a"(fw1[7][1]), "v"(p[5]), "v"(pm[5][1][2]), "v"(pm[5][2][2]), "v"(pm[5][1][3]), "v"(pm[5][2][3]), "a"(fw1[7][2]), "v"(gz[1]), "a"(fw1[7][3]), "a"(fw1[7][0])); SB();
  WAITVB(11);
  SB();
  asm volatile("v_mfma_f32_16x16x32_bf16 %0, %8, %9, %0\n\tv_mul_f32 %1, %10, %10\n\tv_mul_f32 %2, %11, %11\n\tv_mfma_f32_16x16x32_bf16 %3, %12, %9, %3\n\tv_mul_f32 %4, %13, %13\n\tv_fmaak_f32 %5, %14, %5, 0x40135761\n\tv_mfma_f32_16x16x32_bf16 %6, %15, %9, %6\n\tv_fmaak_f32 %1, %14, %1, 0x40135761\n\tv_fmaak_f32 %2, %14, %2, 0x40135761\n\tv_mfma_f32_16x16x32_bf16 %7, %16, %9, %7\n\tv_fmaak_f32 %4, %14, %4, 0x40135761"
      : "+v"(acc[6][1]), "=&v"(gz[1]), "=&v"(gz[2]), "+v"(acc[6][2]), "=&v"(gz[3]), "+v"(gz[0]), "+v"(acc[6][3]), "+v"(acc[6][0]) : "a"(fw1[8][1]), "v"(p[6]), "v"(gm[1]), "v"(gm[2]), "a"(fw1[8][2]), "v"(gm[3]), "v"(gc), "a"(fw1[8][3]), "a"(fw1[8][0])); SB();
  WAITVB(10);
  SB();
  asm volatile("v_mfma_f32_16x16x32_bf16 %0, %8, %9, %0\n\tv_mul_f32 %1, %1, %10\n\tv_mul_f32 %2, %2, %11\n\tv_mfma_f32_16x16x32_bf16 %3, %12, %9, %3\n\tv_mul_f32 %4, %4, %13\n\tv_mul_f32 %5, %5, %14\n\tv_mfma_f32_16x16x32_bf16 %6, %15, %9, %6\n\tv_exp_f32 %1, %1\n\tv_mfma_f32_16x16x32_bf16 %7, %16, %9, %7"
      : "+v"(acc[6][1]), "+v"(gz[0]), "+v"(gz[1]), "+v"(acc[6][2]), "+v"(gz[2]), "+v"(gz[3]), "+v"(acc[6][3]), "+v"(acc[6][0]) : "a"(fw1[9][1]), "v"(p[7]), "v"(gm[0]), "v"(gm[1]), "a"(fw1[9][2]), "v"(gm[2]), "v"(gm[3]), "a"(fw1[9][3]), "a"(fw1[9][0])); SB();
  WAITVB(9);
  SB();
  asm volatile("v_mfma_f32_16x16x32_bf16 %0, %8, %9, %0\n\tv_exp_f32 %1, %1\n\tv_mfma_f32_16x16x32_bf16 %2, %10, %9, %2\n\tv_exp_f32 %3, %3\n\tv_mfma_f32_16x16x32_bf16 %4, %11, %9, %4\n\tv_exp_f32 %5, %5\n\tv_mfma_f32_16x16x32_bf16 %6, %12, %9, %6\n\tv_add_f32 %7, 1.0, %7"
      : "+v"(acc[6][1]), "+v"(gz[1]), "+v"(acc[6][2]), "+v"(gz[2]), "+v"(acc[6][3]), "+v"(gz[3]), "+v"(acc[6][0]), "+v"(gz[0]) : "a"(fw1[10][1]), "v"(p[8]), "a"(fw1[10][2]), "a"(fw1[10][3]), "a"(fw1[10][0])); SB();
  WAITVB(8);
  SB();
  asm volatile("v_mfma_f32_16x16x32_bf16 %0, %8, %9, %0\n\tv_add_f32 %1, 1.0, %1\n\tv_add_f32 %2, 1.0, %2\n\tv_mfma_f32_16x16x32_bf16 %3, %10, %9, %3\n\tv_add_f32 %4, 1.0, %4\n\tv_mfma_f32_16x16x32_bf16 %5, %11, %9, %5\n\tv_rcp_f32 %6, %6\n\tv_mfma_f32_16x16x32_bf16 %7, %12, %9, %7"
      : "+v"(acc[6][1]), "+v"(gz[1]), "+v"(gz[2]), "+v"(acc[6][2]), "+v"(gz[3]), "+v"(acc[6][3]), "+v"(gz[0]), "+v"(acc[6][0]) : "a"(fw1[11][1]), "v"(p[9]), "a"(fw1[11][2]), "a"(fw1[11][3]), "a"(fw1[11][0])); SB();
  WAITVB(7);
  SB();
  asm volatile("v_mfma_f32_16x16x32_bf16 %0, %8, %9, %0\n\tv_rcp_f32 %1, %1\n\tv_mfma_f32_16x16x32_bf16 %2, %10, %9, %2\n\tv_rcp_f32 %3, %3\n\tv_mfma_f32_16x16x32_bf16 %4, %11, %9, %4\n\tv_rcp_f32 %5, %5\n\tv_mfma_f32_16x16x32_bf16 %6, %12, %9, %6\n\tv_fma_f32 %7, -%13, %7, %13"
      : "+v"(acc[6][1]), "+v"(gz[1]), "+v"(acc[6][2]), "+v"(gz[2]), "+v"(acc[6][3]), "+v"(gz[3]), "+v"(acc[6][0]), "+v"(gz[0]) : "a"(fw1[12][1]), "v"(p[10]), "a"(fw1[12][2]), "a"(fw1[12][3]), "a"(fw1[12][0]), "v"(gm[0])); SB();
  WAITVB(6);
  SB();
  asm volatile("v_mfma_f32_16x16x32_bf16 %0, %9, %10, %0\n\tv_fma_f32 %1, -%11, %1, %11\n\tv_fma_f32 %2, -%12, %2, %12\n\tv_mfma_f32_16x16x32_bf16 %3, %13, %10, %3\n\tv_fma_f32 %4, -%14, %4, %14\n\tv_cvt_pk_bf16_f32 %5, %15, %1\n\tv_mfma_f32_16x16x32_bf16 %6, %16, %10, %6\n\tv_cvt_pk_bf16_f32 %7, %2, %4\n\tv_mfma_f32_16x16x32_bf16 %8, %17, %10, %8"
      : "+v"(acc[6][1]), "+v"(gz[1]), "+v"(gz[2]), "+v"(acc[6][2]), "+v"(gz[3]), "=&v"(uv[5][0]), "+v"(acc[6][3]), "=&v"(uv[5][1]), "+v"(acc[6][0]) : "a"(fw1[13][1]), "v"(p[11]), "v"(gm[1]), "v"(gm[2]), "a"(fw1[13][2]), "v"(gm[3]), "v"(gz[0]), "a"(fw1[13][3]), "a"(fw1[13][0])); SB();
  WAITVB(5);
  SB();
  asm volatile("v_mfma_f32_16x16x32_bf16 %0, %5, %6, %0\n\tv_permlane16_swap_b32 %1, %2\n\tv_permlane16_swap_b32 %3, %4"
      : "+v"(acc[6][1]), "+v"(uv[4][0]), "+v"(uv[5][0]), "+v"(uv[4][1]), "+v"(uv[5][1]) : "a"(fw1[14][1]), "v"(p[12])); SB();
  HS16(vU, sU, 256, ((u32x4){uv[4][0], uv[4][1], uv[5][0], uv[5][1]}));
  SB();
  asm volatile("v_mfma_f32_16x16x32_bf16 %0, %3, %4, %0\n\tbuffer_load_dwordx4 %5, %6, %7 offen offset:2048  lds\n\tv_mfma_f32_16x16x32_bf16 %1, %8, %4, %1\n\tv_mfma_f32_16x16x32_bf16 %2, %9, %4, %2"
      : "+v"(acc[6][2]), "+v"(acc[6][3]), "+v"(acc[6][0]) : "a"(fw1[14][2]), "v"(p[12]), "v"(vW2[2]), "s"(rs2), "s"(sw[0]), "a"(fw1[14][3]), "a"(fw1[14][0]) : "memory"); SB();
  WAITVB(6);
  SB();
  asm volatile("v_mfma_f32_16x16x32_bf16 %0, %4, %5, %0\n\tbuffer_load_dwordx4 %6, %7, %8 offen offset:3072  lds\n\tv_mfma_f32_16x16x32_bf16 %1, %9, %5, %1\n\tv_mfma_f32_16x16x32_bf16 %2, %10, %5, %2\n\ts_add_u32 m0, %11, 4096\n\ts_nop 0\n\tv_mfma_f32_16x16x32_bf16 %3, %12, %5, %3"
      : "+v"(acc[6][1]), "+v"(acc[6][2]), "+v"(acc[6][3]), "+v"(acc[6][0]) : "v"(fw1[15][1]), "v"(p[13]), "v"(vW2[3]), "s"(rs2), "s"(sw[0]), "v"(fw1[15][2]), "v"(fw1[15][3]), "s"(ldsO), "v"(fw1[15][0]) : "memory", "scc"); SB();
  STAMP(8);
  asm volatile("s_nop 4\n\tds_write_b128 %0, %1 offset:1024\n\tds_write_b128 %0, %2 offset:2048\n\tds_write_b128 %0, %3 offset:3072\n\tbuffer_load_dwordx4 %4, %5, %6 offen offset:0  lds\n\tbuffer_load_dwordx4 %7, %5, %6 offen offset:1024  lds\n\tbuffer_load_dwordx4 %8, %5, %6 offen offset:2048  lds\n\tbuffer_load_dwordx4 %9, %5, %6 offen offset:3072  lds\n\ts_add_u32 m0, %10, 18432\n\ts_nop 0\n\tbuffer_load_dwordx4 %4, %5, %11 offen offset:0  lds\n\tbuffer_load_dwordx4 %7, %5, %11 offen offset:1024  lds\n\tbuffer_load_dwordx4 %8, %5, %11 offen offset:2048  lds\n\tbuffer_load_dwordx4 %9, %5, %11 offen offset:3072  lds\n\ts_waitcnt vmcnt(13)"
      :  : "v"(rw), "v"(acc[6][1]), "v"(acc[6][2]), "v"(acc[6][3]), "v"(vW2[0]), "s"(rs2), "s"(sw[1]), "v"(vW2[1]), "v"(vW2[2]), "v"(vW2[3]), "s"(ldsS), "s"(sw[2]) : "memory", "scc"); SB();
  STFU(sFU + 2048 * 4);
  BAR();
  asm volatile("ds_read_b128 %0, %3 offset:1024\n\tds_read_b128 %1, %4 offset:2048\n\tds_read_b128 %2, %5 offset:3072"
      : "=&v"(pm[6][0]), "=&v"(pm[6][1]), "=&v"(pm[6][2]) : "v"(rr1), "v"(rr2), "v"(rr3) : "memory"); SB();
  WAITL(0);
  asm volatile("v_add_f32 %0, %12, %13\n\tv_add_f32 %1, %14, %15\n\tv_add_f32 %2, %16, %17\n\tv_add_f32 %3, %18, %19\n\tv_add_f32 %4, %20, %21\n\tv_add_f32 %5, %22, %23\n\tv_add_f32 %6, %24, %25\n\tv_add_f32 %7, %26, %27\n\tv_add_f32 %0, %0, %4\n\tv_add_f32 %1, %1, %5\n\tv_add_f32 %2, %2, %6\n\tv_add_f32 %3, %3, %7\n\tv_mul_f32 %4, %0, %0\n\tv_mul_f32 %5, %1, %1\n\tv_mul_f32 %6, %2, %2\n\tv_mul_f32 %7, %3, %3\n\tv_fmaak_f32 %4, %28, %4, 0x40135761\n\tv_fmaak_f32 %5, %28, %5, 0x40135761\n\tv_fmaak_f32 %6, %28, %6, 0x40135761\n\tv_fmaak_f32 %7, %28, %7, 0x40135761\n\tv_mul_f32 %4, %4, %0\n\tv_mul_f32 %5, %5, %1\n\tv_mul_f32 %6, %6, %2\n\tv_mul_f32 %7, %7, %3\n\tv_exp_f32 %4, %4\n\tv_exp_f32 %5, %5\n\tv_exp_f32 %6, %6\n\tv_exp_f32 %7, %7\n\tv_add_f32 %4, 1.0, %4\n\tv_add_f32 %5, 1.0, %5\n\tv_add_f32 %6, 1.0, %6\n\tv_add_f32 %7, 1.0, %7\n\tv_rcp_f32 %4, %4\n\tv_rcp_f32 %5, %5\n\tv_rcp_f32 %6, %6\n\tv_rcp_f32 %7, %7\n\tv_fma_f32 %4, -%0, %4, %0\n\tv_fma_f32 %5, -%1, %5, %1\n\tv_fma_f32 %6, -%2, %6, %2\n\tv_fma_f32 %7, -%3, %7, %3\n\tv_cvt_pk_bf16_f32 %8, %4, %5\n\tv_cvt_pk_bf16_f32 %9, %6, %7\n\tv_cvt_pk_bf16_f32 %10, %4, %5\n\tv_cvt_pk_bf16_f32 %11, %6, %7\n\ts_nop 0\n\tv_permlane16_swap_b32 %8, %10\n\tv_permlane16_swap_b32 %9, %11"
      : "=&v"(gm[0]), "=&v"(gm[1]), "=&v"(gm[2]), "=&v"(gm[3]), "=&v"(gz[0]), "=&v"(gz[1]), "=&v"(gz[2]), "=&v"(gz[3]), "=&v"(uv[6][0]), "=&v"(uv[6][1]), "=&v"(uv[7][0]), "=&v"(uv[7][1]) : "v"(acc[6][0][0]), "v"(pm[6][0][0]), "v"(acc[6][0][1]), "v"(pm[6][0][1]), "v"(acc[6][0][2]), "v"(pm[6][0][2]), "v"(acc[6][0][3]), "v"(pm[6][0][3]), "v"(pm[6][1][0]), "v"(pm[6][2][0]), "v"(pm[6][1][1]), "v"(pm[6][2][1]), "v"(pm[6][1][2]), "v"(pm[6][2][2]), "v"(pm[6][1][3]), "v"(pm[6][2][3]), "v"(gc)); SB();
  HS16M(0xffff, 0xffff, vU6, sU, 0, ((u32x4){uv[6][0], uv[6][1], uv[7][0], uv[7][1]}));
  STAMP(10);
  asm volatile("s_add_u32 m0, %0, 34816\n\ts_nop 0\n\tbuffer_load_dwordx4 %1, %2, %3 offen offset:0  lds\n\tbuffer_load_dwordx4 %4, %2, %3 offen offset:1024  lds\n\tbuffer_load_dwordx4 %5, %2, %3 offen offset:2048  lds\n\tbuffer_load_dwordx4 %6, %2, %3 offen offset:3072  lds\n\ts_add_u32 m0, %0, 0\n\ts_nop 0\n\tbuffer_load_dwordx4 %1, %2, %7 offen offset:0  lds\n\tbuffer_load_dwordx4 %4, %2, %7 offen offset:1024  lds\n\tbuffer_load_dwordx4 %5, %2, %7 offen offset:2048  lds\n\tbuffer_load_dwordx4 %6, %2, %7 offen offset:3072  lds\n\ts_waitcnt vmcnt(18)"
      :  : "s"(ldsS), "v"(vW2[0]), "s"(rs2), "s"(sw[3]), "v"(vW2[1]), "v"(vW2[2]), "v"(vW2[3]), "s"(sw[4]) : "memory", "scc"); SB();
  fw2[0][0] = tr8u(tO[0] + 0, 512);
  fw2[0][1] = tr8u(tO[1] + 0, 512);
  fw2[0][2] = tr8u(tO[2] + 0, 512);
  fw2[0][3] = tr8u(tO[3] + 0, 512);
  WAITL(0);
  asm volatile("s_add_u32 m0, %0, 0\n\ts_nop 0\n\tbuffer_load_dwordx4 %1, %2, %3 offen offset:0  lds\n\tbuffer_load_dwordx4 %4, %2, %3 offen offset:1024  lds\n\tbuffer_load_dwordx4 %5, %2, %3 offen offset:2048  lds\n\tbuffer_load_dwordx4 %6, %2, %3 offen offset:3072  lds\n\ts_waitcnt vmcnt(18)"
      :  : "s"(ldsO), "v"(vW2[0]), "s"(rs2), "s"(sw[5]), "v"(vW2[1]), "v"(vW2[2]), "v"(vW2[3]) : "memory", "scc"); SB();
  fw2[1][0] = tr8u(tO[0] + 4096, 512);
  fw2[1][1] = tr8u(tO[1] + 4096, 512);
  fw2[1][2] = tr8u(tO[2] + 4096, 512);
  fw2[1][3] = tr8u(tO[3] + 4096, 512);
  asm volatile("s_waitcnt vmcnt(14)"
      :  :  : "memory"); SB();
  fw2[2][0] = tr8u(tS[0] + 18432, 512);
  fw2[2][1] = tr8u(tS[1] + 18432, 512);
  fw2[2][2] = tr8u(tS[2] + 18432, 512);
  fw2[2][3] = tr8u(tS[3] + 18432, 512);
  WAITV1(12, fq);
  STFU(sFU);
  STAMP(11);
  {
  if (__builtin_expect(__ballot(fq != nonce) != 0ull, 0)) poll1<4>(rsW, vFQ, nonce);
  unsigned fu;
  WAITL(8);
  asm volatile("s_add_u32 m0, %0, 4096\n\ts_nop 0\n\tbuffer_load_dwordx4 %1, %2, %3 offen offset:0  lds\n\tbuffer_load_dwordx4 %4, %2, %3 offen offset:1024  lds\n\tbuffer_load_dwordx4 %5, %2, %3 offen offset:2048  lds\n\tbuffer_load_dwordx4 %6, %2, %3 offen offset:3072  lds\n\ts_waitcnt vmcnt(13)"
      :  : "s"(ldsO), "v"(vW2[0]), "s"(rs2), "s"(sw[6]), "v"(vW2[1]), "v"(vW2[2]), "v"(vW2[3]) : "memory", "scc"); SB();
  fw2[3][0] = tr8u(tS[0] + 34816, 512);
  fw2[3][1] = tr8u(tS[1] + 34816, 512);
  fw2[3][2] = tr8u(tS[2] + 34816, 512);
  fw2[3][3] = tr8u(tS[3] + 34816, 512);
  WAITL(8);
  asm volatile("s_add_u32 m0, %0, 18432\n\ts_nop 0\n\tbuffer_load_dwordx4 %1, %2, %3 offen offset:0  lds\n\tbuffer_load_dwordx4 %4, %2, %3 offen offset:1024  lds\n\tbuffer_load_dwordx4 %5, %2, %3 offen offset:2048  lds\n\tbuffer_load_dwordx4 %6, %2, %3 offen offset:3072  lds\n\ts_waitcnt vmcnt(13)"
      :  : "s"(ldsS), "v"(vW2[0]), "s"(rs2), "s"(sw[7]), "v"(vW2[1]), "v"(vW2[2]), "v"(vW2[3]) : "memory", "scc"); SB();
  fw2[4][0] = tr8u(tS[0] + 0, 512);
  fw2[4][1] = tr8u(tS[1] + 0, 512);
  fw2[4][2] = tr8u(tS[2] + 0, 512);
  fw2[4][3] = tr8u(tS[3] + 0, 512);
  WAITL(8);
  asm volatile("s_add_u32 m0, %0, 34816\n\ts_nop 0\n\tbuffer_load_dwordx4 %1, %2, %3 offen offset:0  lds\n\tbuffer_load_dwordx4 %4, %2, %3 offen offset:1024  lds\n\tbuffer_load_dwordx4 %5, %2, %3 offen offset:2048  lds\n\tbuffer_load_dwordx4 %6, %2, %3 offen offset:3072  lds\n\ts_waitcnt vmcnt(13)"
      :  : "s"(ldsS), "v"(vW2[0]), "s"(rs2), "s"(sw[8]), "v"(vW2[1]), "v"(vW2[2]), "v"(vW2[3]) : "memory", "scc"); SB();
  fw2[5][0] = tr8u(tO[0] + 0, 512);
  fw2[5][1] = tr8u(tO[1] + 0, 512);
  fw2[5][2] = tr8u(tO[2] + 0, 512);
  fw2[5][3] = tr8u(tO[3] + 0, 512);
  WAITL(8);
  asm volatile("s_add_u32 m0, %0, 0\n\ts_nop 0\n\tbuffer_load_dwordx4 %1, %2, %3 offen offset:0  lds\n\tbuffer_load_dwordx4 %4, %2, %3 offen offset:1024  lds\n\tbuffer_load_dwordx4 %5, %2, %3 offen offset:2048  lds\n\tbuffer_load_dwordx4 %6, %2, %3 offen offset:3072  lds\n\ts_waitcnt vmcnt(12)"
      :  : "s"(ldsS), "v"(vW2[0]), "s"(rs2), "s"(sw[9]), "v"(vW2[1]), "v"(vW2[2]), "v"(vW2[3]) : "memory", "scc"); SB();
  fw2[6][0] = tr8u(tO[0] + 4096, 512);
  fw2[6][1] = tr8u(tO[1] + 4096, 512);
  fw2[6][2] = tr8u(tO[2] + 4096, 512);
  fw2[6][3] = tr8u(tO[3] + 4096, 512);
  WAITL(8);
  asm volatile("s_add_u32 m0, %0, 0\n\ts_nop 0\n\tbuffer_load_dwordx4 %1, %2, %3 offen offset:0  lds\n\tbuffer_load_dwordx4 %4, %2, %3 offen offset:1024  lds\n\tbuffer_load_dwordx4 %5, %2, %3 offen offset:2048  lds\n\tbuffer_load_dwordx4 %6, %2, %3 offen offset:3072  lds\n\ts_waitcnt vmcnt(12)"
      :  : "s"(ldsO), "v"(vW2[0]), "s"(rs2), "s"(sw[10]), "v"(vW2[1]), "v"(vW2[2]), "v"(vW2[3]) : "memory", "scc"); SB();
  fw2[7][0] = tr8u(tS[0] + 18432, 512);
  fw2[7][1] = tr8u(tS[1] + 18432, 512);
  fw2[7][2] = tr8u(tS[2] + 18432, 512);
  fw2[7][3] = tr8u(tS[3] + 18432, 512);
  WAITL(0);
  asm volatile("s_add_u32 m0, %0, 18432\n\ts_nop 0\n\tbuffer_load_dwordx4 %1, %2, %3 offen offset:0  lds\n\tbuffer_load_dwordx4 %4, %2, %3 offen offset:1024  lds\n\tbuffer_load_dwordx4 %5, %2, %3 offen offset:2048  lds\n\tbuffer_load_dwordx4 %6, %2, %3 offen offset:3072  lds\n\ts_waitcnt vmcnt(12)"
      :  : "s"(ldsS), "v"(vW2[0]), "s"(rs2), "s"(sw[11]), "v"(vW2[1]), "v"(vW2[2]), "v"(vW2[3]) : "memory", "scc"); SB();
  fw2[8][0] = tr8u(tS[0] + 34816, 512);
  fw2[8][1] = tr8u(tS[1] + 34816, 512);
  fw2[8][2] = tr8u(tS[2] + 34816, 512);
  fw2[8][3] = tr8u(tS[3] + 34816, 512);
  WAITL(0);
  asm volatile("s_add_u32 m0, %0, 34816\n\ts_nop 0\n\tbuffer_load_dwordx4 %1, %2, %3 offen offset:0  lds\n\tbuffer_load_dwordx4 %4, %2, %3 offen offset:1024  lds\n\tbuffer_load_dwordx4 %5, %2, %3 offen offset:2048  lds\n\tbuffer_load_dwordx4 %6, %2, %3 offen offset:3072  lds\n\ts_waitcnt vmcnt(12)"
      :  : "s"(ldsS), "v"(vW2[0]), "s"(rs2), "s"(sw[12]), "v"(vW2[1]), "v"(vW2[2]), "v"(vW2[3]) : "memory", "scc"); SB();
  fw2[9][0] = tr8u(tS[0] + 0, 512);
  fw2[9][1] = tr8u(tS[1] + 0, 512);
  fw2[9][2] = tr8u(tS[2] + 0, 512);
  fw2[9][3] = tr8u(tS[3] + 0, 512);
  WAITL(0);
  asm volatile("s_add_u32 m0, %1, 0\n\ts_nop 0\n\tbuffer_load_dwordx4 %2, %3, %4 offen offset:0  lds\n\tbuffer_load_dwordx4 %5, %3, %4 offen offset:1024  lds\n\tbuffer_load_dwordx4 %6, %3, %4 offen offset:2048  lds\n\tbuffer_load_dwordx4 %7, %3, %4 offen offset:3072  lds\n\ts_add_u32 m0, %8, 8192\n\ts_add_u32 %0, %9, 8192"
      : "=&s"(sWt) : "s"(ldsS), "v"(vW2[0]), "s"(rs2), "s"(sw[13]), "v"(vW2[1]), "v"(vW2[2]), "v"(vW2[3]), "s"(ldsP), "s"(sWo) : "memory", "scc"); SB();
  DMAW4(vWo, rsW, sWt);
  asm volatile("s_waitcnt vmcnt(16)"
      :  :  : "memory"); SB();
  fw2[10][0] = tr8u(tO[0] + 0, 512);
  fw2[10][1] = tr8u(tO[1] + 0, 512);
  fw2[10][2] = tr8u(tO[2] + 0, 512);
  fw2[10][3] = tr8u(tO[3] + 0, 512);
  asm volatile("buffer_load_dword %0, %2, %3, 0 offen sc1\n\ts_add_u32 m0, %4, 12288\n\ts_add_u32 %1, %5, 12288"
      : "=&v"(fu), "=&s"(sWt) : "v"(vFU), "s"(rsW), "s"(ldsP), "s"(sWo) : "memory", "scc"); SB();
  DMAW2(vWo, rsW, sWt);
  asm volatile("s_waitcnt vmcnt(15)"
      :  :  : "memory"); SB();
  fw2[11][0] = tr8u(tS[0] + 18432, 512);
  fw2[11][1] = tr8u(tS[1] + 18432, 512);
  fw2[11][2] = tr8u(tS[2] + 18432, 512);
  fw2[11][3] = tr8u(tS[3] + 18432, 512);
  WAITL(8);
  asm volatile("s_add_u32 m0, %1, 0\n\ts_add_u32 %0, %2, 0"
      : "=&s"(sWt) : "s"(ldsO), "s"(sWo) : "memory", "scc"); SB();
  DMAW4(vWo, rsW, sWt);
  asm volatile("s_waitcnt vmcnt(15)"
      :  :  : "memory"); SB();
  fw2[12][0] = tr8u(tS[0] + 34816, 512);
  fw2[12][1] = tr8u(tS[1] + 34816, 512);
  fw2[12][2] = tr8u(tS[2] + 34816, 512);
  fw2[12][3] = tr8u(tS[3] + 34816, 512);
  asm volatile("s_add_u32 m0, %1, 4096\n\ts_add_u32 %0, %2, 4096"
      : "=&s"(sWt) : "s"(ldsO), "s"(sWo) : "memory", "scc"); SB();
  DMAW4(vWo, rsW, sWt);
  u32x4 rsX = srd(x, 2048u * 896u); OPQ(rsX);
  WAITL(0);
  asm volatile("s_add_u32 m0, %1, 34816\n\ts_nop 0\n\tbuffer_load_dwordx4 %2, %3, %4 offen offset:0  lds\n\tbuffer_load_dwordx4 %5, %3, %4 offen offset:1024  lds\n\tglobal_load_ushort %0, %6, %7 offset:0"
      : "=&v"(braw) : "s"(ldsS), "v"(vXa), "s"(rsX), "s"(0u), "v"(vXb), "v"(vB2), "s"(b2) : "memory", "scc"); SB();
  STAMP(16);
  bf16x8 pu[14], fwo[56];
  f32x4 ac[2][4], as[4], py[4][4];
  float ye[4], ze[4], ge[16], xe[16], bias;
  u32x2 ov[4]; unsigned fo;
  u32x2 xv[4]; unsigned xta[4];
  WAITV1(13, fu);
  if (__ballot(fu != nonce) != 0ull) poll1<4>(rsW, vFU, nonce);
  STAMP(17);
  asm volatile("buffer_load_dwordx4 %0, %10, %11, %12 offen offset:0 sc1\n\tbuffer_load_dwordx4 %1, %10, %11, %13 offen offset:0 sc1\n\tbuffer_load_dwordx4 %2, %10, %11, %14 offen offset:0 sc1\n\tbuffer_load_dwordx4 %3, %10, %11, %15 offen offset:0 sc1\n\tbuffer_load_dwordx4 %4, %10, %11, %16 offen offset:0 sc1\n\tbuffer_load_dwordx4 %5, %10, %11, %17 offen offset:0 sc1\n\tbuffer_load_dwordx4 %6, %10, %11, %18 offen offset:0 sc1\n\tbuffer_load_dwordx4 %7, %10, %11, %19 offen offset:0 sc1\n\tbuffer_load_dwordx4 %8, %10, %11, %20 offen offset:0 sc1\n\tbuffer_load_dwordx4 %9, %10, %11, %21 offen offset:0 sc1"
      : "=&v"(pu[0]), "=&v"(pu[1]), "=&v"(pu[2]), "=&v"(pu[3]), "=&v"(pu[4]), "=&v"(pu[5]), "=&v"(pu[6]), "=&v"(pu[7]), "=&v"(pu[8]), "=&v"(pu[9]) : "v"(vFj[0]), "s"(rsW), "s"(su[0]), "s"(su[1]), "s"(su[2]), "s"(su[3]), "s"(su[4]), "s"(su[5]), "s"(su[6]), "s"(su[7]), "s"(su[8]), "s"(su[9]) : "memory"); SB();
  STAMP(39);
  WAITVB(9);
  SB();
  asm volatile("v_mfma_f32_16x16x32_bf16 %0, %6, %7, 0\n\tbuffer_load_dwordx4 %1, %8, %9, %10 offen offset:0 sc1\n\tv_mfma_f32_16x16x32_bf16 %2, %6, %11, 0\n\ts_waitcnt vmcnt(29)\n\tv_mfma_f32_16x16x32_bf16 %3, %6, %12, 0\n\tbuffer_load_dwordx4 %4, %8, %9, %13 offen offset:0 sc1\n\tv_mfma_f32_16x16x32_bf16 %5, %6, %14, 0"
      : "=&v"(ac[0][0]), "=&v"(pu[10]), "=&v"(ac[0][1]), "=&v"(ac[0][2]), "=&v"(pu[11]), "=&v"(ac[0][3]) : "v"(pu[0]), "a"(fw2[0][0]), "v"(vFj[0]), "s"(rsW), "s"(su[10]), "a"(fw2[0][1]), "a"(fw2[0][2]), "s"(su[11]), "a"(fw2[0][3]) : "memory"); SB();
  WAITVB(10);
  SB();
  asm volatile("v_mfma_f32_16x16x32_bf16 %0, %3, %4, %0\n\tbuffer_load_dwordx4 %1, %5, %6, %7 offen offset:0 sc1\n\tv_mfma_f32_16x16x32_bf16 %2, %3, %8, %2"
      : "+v"(ac[0][0]), "=&v"(pu[12]), "+v"(ac[0][1]) : "v"(pu[1]), "a"(fw2[1][0]), "v"(vFj[0]), "s"(rsW), "s"(su[12]), "a"(fw2[1][1]) : "memory"); SB();
  fw2[13][0] = tr8u(tS[0] + 0, 512);
  SB();
  asm volatile("v_mfma_f32_16x16x32_bf16 %0, %3, %4, %0\n\tbuffer_load_dwordx4 %1, %5, %6, %7 offen offset:0 sc1\n\tv_mfma_f32_16x16x32_bf16 %2, %3, %8, %2"
      : "+v"(ac[0][2]), "=&v"(pu[13]), "+v"(ac[0][3]) : "v"(pu[1]), "a"(fw2[1][2]), "v"(vFj[0]), "s"(rsW), "s"(su[13]), "a"(fw2[1][3]) : "memory"); SB();
  WAITVB(11);
  SB();
  asm volatile("v_mfma_f32_16x16x32_bf16 %0, %3, %4, %0\n\tbuffer_load_dwordx4 %1, %5, %6, %7 offen offset:0 sc1\n\tv_mfma_f32_16x16x32_bf16 %2, %3, %8, %2"
      : "+v"(ac[0][0]), "=&v"(pu[0]), "+v"(ac[0][1]) : "v"(pu[2]), "a"(fw2[2][0]), "v"(vFj[1]), "s"(rsW), "s"(su[0]), "a"(fw2[2][1]) : "memory"); SB();
  fw2[13][1] = tr8u(tS[1] + 0, 512);
  SB();
  asm volatile("v_mfma_f32_16x16x32_bf16 %0, %3, %4, %0\n\tbuffer_load_dwordx4 %1, %5, %6, %7 offen offset:0 sc1\n\tv_mfma_f32_16x16x32_bf16 %2, %3, %8, %2"
      : "+v"(ac[0][2]), "=&v"(pu[1]), "+v"(ac[0][3]) : "v"(pu[2]), "a"(fw2[2][2]), "v"(vFj[1]), "s"(rsW), "s"(su[1]), "a"(fw2[2][3]) : "memory"); SB();
  fw2[13][2] = tr8u(tS[2] + 0, 512);
  fw2[13][3] = tr8u(tS[3] + 0, 512);
  WAITVB(16);
  SB();
  WAITVB(12);
  SB();
  asm volatile("v_mfma_f32_16x16x32_bf16 %0, %10, %11, %0\n\tbuffer_load_dwordx4 %1, %12, %13, %14 offen offset:0 sc1\n\tv_mfma_f32_16x16x32_bf16 %2, %10, %15, %2\n\tv_lshlrev_b32 %3, 16, %16\n\tv_xor_b32 %4, 0, %17\n\tv_mfma_f32_16x16x32_bf16 %5, %10, %18, %5\n\tv_xor_b32 %6, 32, %17\n\tv_xor_b32 %7, 64, %19\n\tv_mfma_f32_16x16x32_bf16 %8, %10, %20, %8\n\tds_read_b64 %9, %4 offset:0"
      : "+v"(ac[0][0]), "=&v"(pu[2]), "+v"(ac[0][1]), "=&v"(bias), "=&v"(xta[0]), "+v"(ac[0][2]), "=&v"(xta[1]), "=&v"(xta[2]), "+v"(ac[0][3]), "=&v"(xv[0]) : "v"(pu[3]), "a"(fw2[3][0]), "v"(vFj[1]), "s"(rsW), "s"(su[2]), "a"(fw2[3][1]), "v"(braw), "v"(xs), "a"(fw2[3][2]), "v"(xt), "a"(fw2[3][3]) : "memory"); SB();
  WAITVB(12);
  SB();
  asm volatile("v_mfma_f32_16x16x32_bf16 %0, %7, %8, %0\n\tbuffer_load_dwordx4 %1, %9, %10, %11 offen offset:0 sc1\n\tv_mfma_f32_16x16x32_bf16 %2, %7, %12, %2\n\tds_read_b64 %3, %13 offset:0\n\tds_read_b64 %4, %14 offset:0\n\tv_mfma_f32_16x16x32_bf16 %5, %7, %15, %5\n\tv_mfma_f32_16x16x32_bf16 %6, %7, %16, %6"
      : "+v"(ac[0][0]), "=&v"(pu[3]), "+v"(ac[0][1]), "=&v"(xv[1]), "=&v"(xv[2]), "+v"(ac[0][2]), "+v"(ac[0][3]) : "v"(pu[4]), "a"(fw2[4][0]), "v"(vFj[1]), "s"(rsW), "s"(su[3]), "a"(fw2[4][1]), "v"(xta[1]), "v"(xta[2]), "a"(fw2[4][2]), "a"(fw2[4][3]) : "memory"); SB();
  WAITVB(12);
  SB();
  asm volatile("v_mfma_f32_16x16x32_bf16 %0, %4, %5, %0\n\tbuffer_load_dwordx4 %1, %6, %7, %8 offen offset:0 sc1\n\tv_mfma_f32_16x16x32_bf16 %2, %4, %9, %2\n\tv_mfma_f32_16x16x32_bf16 %3, %4, %10, %3"
      : "+v"(ac[0][0]), "=&v"(pu[4]), "+v"(ac[0][1]), "+v"(ac[0][2]) : "v"(pu[5]), "a"(fw2[5][0]), "v"(vFj[1]), "s"(rsW), "s"(su[4]), "a"(fw2[5][1]), "a"(fw2[5][2]) : "memory"); SB();
  WAITL(0);
  asm volatile("v_lshlrev_b32 %0, 16, %3\n\tv_mfma_f32_16x16x32_bf16 %1, %4, %5, %1\n\tv_and_b32 %2, 0xffff0000, %3"
      : "=&v"(xe[0]), "+v"(ac[0][3]), "=&v"(xe[1]) : "v"(xv[0][0]), "v"(pu[5]), "a"(fw2[5][3])); SB();
  WAITVB(12);
  SB();
  asm volatile("v_mfma_f32_16x16x32_bf16 %0, %10, %11, %0\n\tbuffer_load_dwordx4 %1, %12, %13, %14 offen offset:0 sc1\n\tv_mfma_f32_16x16x32_bf16 %2, %10, %15, %2\n\tv_lshlrev_b32 %3, 16, %16\n\tv_and_b32 %4, 0xffff0000, %16\n\tv_mfma_f32_16x16x32_bf16 %5, %10, %17, %5\n\tv_lshlrev_b32 %6, 16, %18\n\tv_and_b32 %7, 0xffff0000, %18\n\tv_mfma_f32_16x16x32_bf16 %8, %10, %19, %8\n\tv_lshlrev_b32 %9, 16, %20"
      : "+v"(ac[0][0]), "=&v"(pu[5]), "+v"(ac[0][1]), "=&v"(xe[2]), "=&v"(xe[3]), "+v"(ac[0][2]), "=&v"(xe[4]), "=&v"(xe[5]), "+v"(ac[0][3]), "=&v"(xe[6]) : "v"(pu[6]), "a"(fw2[6][0]), "v"(vFj[1]), "s"(rsW), "s"(su[5]), "a"(fw2[6][1]), "v"(xv[0][1]), "a"(fw2[6][2]), "v"(xv[1][0]), "a"(fw2[6][3]), "v"(xv[1][1]) : "memory"); SB();
  WAITVB(12);
  SB();
  asm volatile("v_mfma_f32_16x16x32_bf16 %0, %10, %11, %0\n\tbuffer_load_dwordx4 %1, %12, %13, %14 offen offset:0 sc1\n\tv_mfma_f32_16x16x32_bf16 %2, %10, %15, %2\n\tv_and_b32 %3, 0xffff0000, %16\n\tv_lshlrev_b32 %4, 16, %17\n\tv_mfma_f32_16x16x32_bf16 %5, %10, %18, %5\n\tv_and_b32 %6, 0xffff0000, %17\n\tv_lshlrev_b32 %7, 16, %19\n\tv_mfma_f32_16x16x32_bf16 %8, %10, %20, %8\n\tv_and_b32 %9, 0xffff0000, %19"
      : "+v"(ac[0][0]), "=&v"(pu[6]), "+v"(ac[0][1]), "=&v"(xe[7]), "=&v"(xe[8]), "+v"(ac[0][2]), "=&v"(xe[9]), "=&v"(xe[10]), "+v"(ac[0][3]), "=&v"(xe[11]) : "v"(pu[7]), "a"(fw2[7][0]), "v"(vFj[1]), "s"(rsW), "s"(su[6]), "a"(fw2[7][1]), "v"(xv[1][1]), "v"(xv[2][0]), "a"(fw2[7][2]), "v"(xv[2][1]), "a"(fw2[7][3]) : "memory"); SB();
  WAITVB(12);
  SB();
  asm volatile("v_mfma_f32_16x16x32_bf16 %0, %5, %6, %0\n\tbuffer_load_dwordx4 %1, %7, %8, %9 offen offset:0 sc1\n\tv_mfma_f32_16x16x32_bf16 %2, %5, %10, %2\n\tv_mfma_f32_16x16x32_bf16 %3, %5, %11, %3\n\tv_mfma_f32_16x16x32_bf16 %4, %5, %12, %4"
      : "+v"(ac[0][0]), "=&v"(pu[7]), "+v"(ac[0][1]), "+v"(ac[0][2]), "+v"(ac[0][3]) : "v"(pu[8]), "a"(fw2[8][0]), "v"(vFj[1]), "s"(rsW), "s"(su[7]), "a"(fw2[8][1]), "a"(fw2[8][2]), "a"(fw2[8][3]) : "memory"); SB();
  WAITVB(12);
  SB();
  asm volatile("v_mfma_f32_16x16x32_bf16 %0, %5, %6, %0\n\tbuffer_load_dwordx4 %1, %7, %8, %9 offen offset:0 sc1\n\tv_mfma_f32_16x16x32_bf16 %2, %5, %10, %2\n\tv_mfma_f32_16x16x32_bf16 %3, %5, %11, %3\n\tv_mfma_f32_16x16x32_bf16 %4, %5, %12, %4"
      : "+v"(ac[0][0]), "=&v"(pu[8]), "+v"(ac[0][1]), "+v"(ac[0][2]), "+v"(ac[0][3]) : "v"(pu[9]), "v"(fw2[9][0]), "v"(vFj[1]), "s"(rsW), "s"(su[8]), "v"(fw2[9][1]), "v"(fw2[9][2]), "v"(fw2[9][3]) : "memory"); SB();
  WAITVB(12);
  SB();
  asm volatile("v_mfma_f32_16x16x32_bf16 %0, %5, %6, %0\n\tbuffer_load_dwordx4 %1, %7, %8, %9 offen offset:0 sc1\n\tv_mfma_f32_16x16x32_bf16 %2, %5, %10, %2\n\tv_mfma_f32_16x16x32_bf16 %3, %5, %11, %3\n\tv_mfma_f32_16x16x32_bf16 %4, %5, %12, %4"
      : "+v"(ac[0][0]), "=&v"(pu[9]), "+v"(ac[0][1]), "+v"(ac[0][2]), "+v"(ac[0][3]) : "v"(pu[10]), "v"(fw2[10][0]), "v"(vFj[1]), "s"(rsW), "s"(su[9]), "v"(fw2[10][1]), "v"(fw2[10][2]), "v"(fw2[10][3]) : "memory"); SB();
  WAITVB(12);
  SB();
  asm volatile("v_mfma_f32_16x16x32_bf16 %0, %5, %6, %0\n\tbuffer_load_dwordx4 %1, %7, %8, %9 offen offset:0 sc1\n\tv_mfma_f32_16x16x32_bf16 %2, %5, %10, %2\n\tv_mfma_f32_16x16x32_bf16 %3, %5, %11, %3\n\tv_mfma_f32_16x16x32_bf16 %4, %5, %12, %4"
      : "+v"(ac[0][0]), "=&v"(pu[10]), "+v"(ac[0][1]), "+v"(ac[0][2]), "+v"(ac[0][3]) : "v"(pu[11]), "v"(fw2[11][0]), "v"(vFj[1]), "s"(rsW), "s"(su[10]), "v"(fw2[11][1]), "v"(fw2[11][2]), "v"(fw2[11][3]) : "memory"); SB();
  WAITVB(12);
  SB();
  asm volatile("v_mfma_f32_16x16x32_bf16 %0, %4, %5, %0\n\tbuffer_load_dwordx4 %1, %6, %7, %8 offen offset:0 sc1\n\tv_mfma_f32_16x16x32_bf16 %2, %4, %9, %2\n\tv_mfma_f32_16x16x32_bf16 %3, %4, %10, %3"
      : "+v"(ac[0][0]), "=&v"(pu[11]), "+v"(ac[0][1]), "+v"(ac[0][2]) : "v"(pu[12]), "v"(fw2[12][0]), "v"(vFj[1]), "s"(rsW), "s"(su[11]), "v"(fw2[12][1]), "v"(fw2[12][2]) : "memory"); SB();
  u32x4 rsO_ = srd(out, 2048u * 896u); OPQ(rsO_);
  SB();
  asm volatile("v_mfma_f32_16x16x32_bf16 %0, %1, %2, %0"
      : "+v"(ac[0][3]) : "v"(pu[12]), "v"(fw2[12][3])); SB();
  WAITVB(12);
  SB();
  asm volatile("v_mfma_f32_16x16x32_bf16 %0, %5, %6, %0\n\tbuffer_load_dwordx4 %1, %7, %8, %9 offen offset:0 sc1\n\tv_mfma_f32_16x16x32_bf16 %2, %5, %10, %2\n\tv_mfma_f32_16x16x32_bf16 %3, %5, %11, %3\n\tv_mfma_f32_16x16x32_bf16 %4, %5, %12, %4"
      : "+v"(ac[0][0]), "=&v"(pu[12]), "+v"(ac[0][1]), "+v"(ac[0][2]), "+v"(ac[0][3]) : "v"(pu[13]), "v"(fw2[13][0]), "v"(vFj[1]), "s"(rsW), "s"(su[12]), "v"(fw2[13][1]), "v"(fw2[13][2]), "v"(fw2[13][3]) : "memory"); SB();
  STAMP(40);
  WAITVB(12);
  SB();
  asm volatile("v_mfma_f32_16x16x32_bf16 %0, %6, %7, 0\n\tbuffer_load_dwordx4 %1, %8, %9, %10 offen offset:0 sc1\n\tv_mfma_f32_16x16x32_bf16 %2, %6, %11, 0\n\tbuffer_load_dword %3, %12, %9, 0 offen sc1\n\tv_mfma_f32_16x16x32_bf16 %4, %6, %13, 0\n\tv_mfma_f32_16x16x32_bf16 %5, %6, %14, 0"
      : "=&v"(ac[1][0]), "=&v"(pu[13]), "=&v"(ac[1][1]), "=&v"(fo), "=&v"(ac[1][2]), "=&v"(ac[1][3]) : "v"(pu[0]), "a"(fw2[0][0]), "v"(vFj[1]), "s"(rsW), "s"(su[13]), "a"(fw2[0][1]), "v"(vFO), "a"(fw2[0][2]), "a"(fw2[0][3]) : "memory"); SB();
  WAITVB(13);
  SB();
  asm volatile("v_mfma_f32_16x16x32_bf16 %0, %5, %6, %0\n\tbuffer_load_dwordx4 %1, %7, %8, %9 offen offset:0 sc1\n\tv_mfma_f32_16x16x32_bf16 %2, %5, %10, %2\n\tds_write_b128 %11, %12 offset:0\n\tv_mfma_f32_16x16x32_bf16 %3, %5, %13, %3\n\tv_mfma_f32_16x16x32_bf16 %4, %5, %14, %4"
      : "+v"(ac[1][0]), "=&v"(pu[0]), "+v"(ac[1][1]), "+v"(ac[1][2]), "+v"(ac[1][3]) : "v"(pu[1]), "a"(fw2[1][0]), "v"(vFj[2]), "s"(rsW), "s"(su[0]), "a"(fw2[1][1]), "v"(rwd), "v"(ac[0][0]), "a"(fw2[1][2]), "a"(fw2[1][3]) : "memory"); SB();
  WAITVB(13);
  SB();
  asm volatile("v_mfma_f32_16x16x32_bf16 %0, %5, %6, %0\n\tbuffer_load_dwordx4 %1, %7, %8, %9 offen offset:0 sc1\n\tv_mfma_f32_16x16x32_bf16 %2, %5, %10, %2\n\tds_write_b128 %11, %12 offset:1024\n\tv_mfma_f32_16x16x32_bf16 %3, %5, %13, %3\n\tv_mfma_f32_16x16x32_bf16 %4, %5, %14, %4"
      : "+v"(ac[1][0]), "=&v"(pu[1]), "+v"(ac[1][1]), "+v"(ac[1][2]), "+v"(ac[1][3]) : "v"(pu[2]), "a"(fw2[2][0]), "v"(vFj[2]), "s"(rsW), "s"(su[1]), "a"(fw2[2][1]), "v"(rwd), "v"(ac[0][1]), "a"(fw2[2][2]), "a"(fw2[2][3]) : "memory"); SB();
  WAITVB(13);
  SB();
  asm volatile("v_mfma_f32_16x16x32_bf16 %0, %5, %6, %0\n\tbuffer_load_dwordx4 %1, %7, %8, %9 offen offset:0 sc1\n\tv_mfma_f32_16x16x32_bf16 %2, %5, %10, %2\n\tds_write_b128 %11, %12 offset:2048\n\tv_mfma_f32_16x16x32_bf16 %3, %5, %13, %3\n\tv_mfma_f32_16x16x32_bf16 %4, %5, %14, %4"
      : "+v"(ac[1][0]), "=&v"(pu[2]), "+v"(ac[1][1]), "+v"(ac[1][2]), "+v"(ac[1][3]) : "v"(pu[3]), "a"(fw2[3][0]), "v"(vFj[2]), "s"(rsW), "s"(su[2]), "a"(fw2[3][1]), "v"(rwd), "v"(ac[0][2]), "a"(fw2[3][2]), "a"(fw2[3][3]) : "memory"); SB();
  WAITVB(13);
  SB();
  asm volatile("v_mfma_f32_16x16x32_bf16 %0, %5, %6, %0\n\tbuffer_load_dwordx4 %1, %7, %8, %9 offen offset:0 sc1\n\tv_mfma_f32_16x16x32_bf16 %2, %5, %10, %2\n\tds_write_b128 %11, %12 offset:3072\n\tv_mfma_f32_16x16x32_bf16 %3, %5, %13, %3\n\tv_mfma_f32_16x16x32_bf16 %4, %5, %14, %4"
      : "+v"(ac[1][0]), "=&v"(pu[3]), "+v"(ac[1][1]), "+v"(ac[1][2]), "+v"(ac[1][3]) : "v"(pu[4]), "a"(fw2[4][0]), "v"(vFj[2]), "s"(rsW), "s"(su[3]), "a"(fw2[4][1]), "v"(rwd), "v"(ac[0][3]), "a"(fw2[4][2]), "a"(fw2[4][3]) : "memory"); SB();
  WAITVB(13);
  SB();
  asm volatile("v_mfma_f32_16x16x32_bf16 %0, %3, %4, %0\n\tbuffer_load_dwordx4 %1, %5, %6, %7 offen offset:0 sc1\n\tv_mfma_f32_16x16x32_bf16 %2, %3, %8, %2\n\ts_waitcnt vmcnt(37)"
      : "+v"(ac[1][0]), "=&v"(pu[4]), "+v"(ac[1][1]) : "v"(pu[5]), "a"(fw2[5][0]), "v"(vFj[2]), "s"(rsW), "s"(su[4]), "a"(fw2[5][1]) : "memory"); SB();
  WAITL(0);
  BAR0();
  SB();
  asm volatile("v_mfma_f32_16x16x32_bf16 %0, %5, %6, %0\n\tds_read_b128 %1, %7 offset:0\n\tds_read_b128 %2, %7 offset:4096\n\tv_mfma_f32_16x16x32_bf16 %3, %5, %8, %3\n\tds_read_b128 %4, %7 offset:8192"
      : "+v"(ac[1][2]), "=&v"(py[0][0]), "=&v"(py[0][1]), "+v"(ac[1][3]), "=&v"(py[0][2]) : "v"(pu[5]), "a"(fw2[5][2]), "v"(rr), "a"(fw2[5][3]) : "memory"); SB();
  WAITVB(13);
  SB();
  asm volatile("v_mfma_f32_16x16x32_bf16 %0, %8, %9, %0\n\tbuffer_load_dwordx4 %1, %10, %11, %12 offen offset:0 sc1\n\tv_mfma_f32_16x16x32_bf16 %2, %8, %13, %2\n\tds_read_b128 %3, %14 offset:12288\n\tds_read_b128 %4, %15 offset:0\n\tv_mfma_f32_16x16x32_bf16 %5, %8, %16, %5\n\tv_mfma_f32_16x16x32_bf16 %6, %8, %17, %6\n\tds_read_b128 %7, %15 offset:8192"
      : "+v"(ac[1][0]), "=&v"(pu[5]), "+v"(ac[1][1]), "=&v"(py[0][3]), "=&a"(fwo[0]), "+v"(ac[1][2]), "+v"(ac[1][3]), "=&a"(fwo[1]) : "v"(pu[6]), "a"(fw2[6][0]), "v"(vFj[2]), "s"(rsW), "s"(su[5]), "a"(fw2[6][1]), "v"(rr), "v"(ro), "a"(fw2[6][2]), "a"(fw2[6][3]) : "memory"); SB();
  WAITVB(13);
  SB();
  asm volatile("v_mfma_f32_16x16x32_bf16 %0, %5, %6, %0\n\tbuffer_load_dwordx4 %1, %7, %8, %9 offen offset:0 sc1\n\tv_mfma_f32_16x16x32_bf16 %2, %5, %10, %2\n\tds_read_b128 %3, %11 offset:16384\n\tv_mfma_f32_16x16x32_bf16 %4, %5, %12, %4"
      : "+v"(ac[1][0]), "=&v"(pu[6]), "+v"(ac[1][1]), "=&a"(fwo[2]), "+v"(ac[1][2]) : "v"(pu[7]), "a"(fw2[7][0]), "v"(vFj[2]), "s"(rsW), "s"(su[6]), "a"(fw2[7][1]), "v"(ro), "a"(fw2[7][2]) : "memory"); SB();
  WAITL(3);
  asm volatile("v_add_f32 %0, %3, %4\n\tv_mfma_f32_16x16x32_bf16 %1, %5, %6, %1\n\tv_add_f32 %2, %7, %4"
      : "=&v"(ye[0]), "+v"(ac[1][3]), "=&v"(ye[1]) : "v"(py[0][0][0]), "v"(bias), "v"(pu[7]), "a"(fw2[7][3]), "v"(py[0][0][1])); SB();
  WAITVB(13);
  SB();
  asm volatile("v_mfma_f32_16x16x32_bf16 %0, %9, %10, %0\n\tbuffer_load_dwordx4 %1, %11, %12, %13 offen offset:0 sc1\n\tv_mfma_f32_16x16x32_bf16 %2, %9, %14, %2\n\tv_add_f32 %3, %15, %16\n\tv_add_f32 %4, %17, %16\n\tv_mfma_f32_16x16x32_bf16 %5, %9, %18, %5\n\tv_add_f32 %6, %19, %20\n\tv_add_f32 %7, %21, %22\n\tv_mfma_f32_16x16x32_bf16 %8, %9, %23, %8"
      : "+v"(ac[1][0]), "=&v"(pu[7]), "+v"(ac[1][1]), "=&v"(ye[2]), "=&v"(ye[3]), "+v"(ac[1][2]), "=&v"(ze[0]), "=&v"(ze[1]), "+v"(ac[1][3]) : "v"(pu[8]), "a"(fw2[8][0]), "v"(vFj[2]), "s"(rsW), "s"(su[7]), "a"(fw2[8][1]), "v"(py[0][0][2]), "v"(bias), "v"(py[0][0][3]), "a"(fw2[8][2]), "v"(py[0][1][0]), "v"(py[0][2][0]), "v"(py[0][1][1]), "v"(py[0][2][1]), "a"(fw2[8][3]) : "memory"); SB();
  asm volatile("v_add_f32 %0, %1, %2"
      : "=&v"(ze[2]) : "v"(py[0][1][2]), "v"(py[0][2][2])); SB();
  WAITVB(13);
  SB();
  asm volatile("v_mfma_f32_16x16x32_bf16 %0, %9, %10, %0\n\tbuffer_load_dwordx4 %1, %11, %12, %13 offen offset:0 sc1\n\tv_mfma_f32_16x16x32_bf16 %2, %9, %14, %2\n\tv_add_f32 %3, %15, %16\n\tv_add_f32 %4, %4, %17\n\tv_mfma_f32_16x16x32_bf16 %5, %9, %18, %5\n\tv_add_f32 %6, %6, %19\n\tv_add_f32 %7, %7, %20\n\tv_mfma_f32_16x16x32_bf16 %8, %9, %21, %8"
      : "+v"(ac[1][0]), "=&v"(pu[8]), "+v"(ac[1][1]), "=&v"(ze[3]), "+v"(ye[0]), "+v"(ac[1][2]), "+v"(ye[1]), "+v"(ye[2]), "+v"(ac[1][3]) : "v"(pu[9]), "v"(fw2[9][0]), "v"(vFj[2]), "s"(rsW), "s"(su[8]), "v"(fw2[9][1]), "v"(py[0][1][3]), "v"(py[0][2][3]), "v"(py[0][3][0]), "v"(fw2[9][2]), "v"(py[0][3][1]), "v"(py[0][3][2]), "v"(fw2[9][3]) : "memory"); SB();
  asm volatile("v_add_f32 %0, %0, %1"
      : "+v"(ye[3]) : "v"(py[0][3][3])); SB();
  WAITVB(13);
  SB();
  asm volatile("v_mfma_f32_16x16x32_bf16 %0, %10, %11, %0\n\tbuffer_load_dwordx4 %1, %12, %13, %14 offen offset:0 sc1\n\tv_mfma_f32_16x16x32_bf16 %2, %10, %15, %2\n\tv_add_f32 %3, %3, %4\n\tv_add_f32 %5, %5, %16\n\tv_mfma_f32_16x16x32_bf16 %6, %10, %17, %6\n\tv_add_f32 %7, %7, %18\n\tv_add_f32 %8, %8, %19\n\tv_mfma_f32_16x16x32_bf16 %9, %10, %20, %9\n\tv_mul_f32 %4, 0xbfb8aa3b, %3"
      : "+v"(ac[1][0]), "=&v"(pu[9]), "+v"(ac[1][1]), "+v"(ye[0]), "+v"(ze[0]), "+v"(ye[1]), "+v"(ac[1][2]), "+v"(ye[2]), "+v"(ye[3]), "+v"(ac[1][3]) : "v"(pu[10]), "v"(fw2[10][0]), "v"(vFj[2]), "s"(rsW), "s"(su[9]), "v"(fw2[10][1]), "v"(ze[1]), "v"(fw2[10][2]), "v"(ze[2]), "v"(ze[3]), "v"(fw2[10][3]) : "memory"); SB();
  WAITVB(13);
  SB();
  asm volatile("v_mfma_f32_16x16x32_bf16 %0, %9, %10, %0\n\tbuffer_load_dwordx4 %1, %11, %12, %13 offen offset:0 sc1\n\tv_mfma_f32_16x16x32_bf16 %2, %9, %14, %2\n\tv_mul_f32 %3, 0xbfb8aa3b, %15\n\tv_mul_f32 %4, 0xbfb8aa3b, %16\n\tv_mfma_f32_16x16x32_bf16 %5, %9, %17, %5\n\tds_read_b128 %6, %18 offset:1024\n\tv_mul_f32 %7, 0xbfb8aa3b, %19\n\tv_mfma_f32_16x16x32_bf16 %8, %9, %20, %8"
      : "+v"(ac[1][0]), "=&v"(pu[10]), "+v"(ac[1][1]), "=&v"(ze[1]), "=&v"(ze[2]), "+v"(ac[1][2]), "=&a"(fwo[3]), "=&v"(ze[3]), "+v"(ac[1][3]) : "v"(pu[11]), "v"(fw2[11][0]), "v"(vFj[2]), "s"(rsW), "s"(su[10]), "v"(fw2[11][1]), "v"(ye[1]), "v"(ye[2]), "v"(fw2[11][2]), "v"(ro), "v"(ye[3]), "v"(fw2[11][3]) : "memory"); SB();
  WAITVB(13);
  SB();
  asm volatile("v_mfma_f32_16x16x32_bf16 %0, %8, %9, %0\n\tbuffer_load_dwordx4 %1, %10, %11, %12 offen offset:0 sc1\n\tv_mfma_f32_16x16x32_bf16 %2, %8, %13, %2\n\tv_exp_f32 %3, %3\n\tv_mfma_f32_16x16x32_bf16 %4, %8, %14, %4\n\tv_exp_f32 %5, %5\n\tv_mfma_f32_16x16x32_bf16 %6, %8, %15, %6\n\tds_read_b128 %7, %16 offset:9216"
      : "+v"(ac[1][0]), "=&v"(pu[11]), "+v"(ac[1][1]), "+v"(ze[0]), "+v"(ac[1][2]), "+v"(ze[1]), "+v"(ac[1][3]), "=&a"(fwo[4]) : "v"(pu[12]), "v"(fw2[12][0]), "v"(vFj[2]), "s"(rsW), "s"(su[11]), "v"(fw2[12][1]), "v"(fw2[12][2]), "v"(fw2[12][3]), "v"(ro) : "memory"); SB();
  WAITVB(13);
  SB();
  asm volatile("v_mfma_f32_16x16x32_bf16 %0, %9, %10, %0\n\tbuffer_load_dwordx4 %1, %11, %12, %13 offen offset:0 sc1\n\tv_mfma_f32_16x16x32_bf16 %2, %9, %14, %2\n\tv_exp_f32 %3, %3\n\tv_mfma_f32_16x16x32_bf16 %4, %9, %15, %4\n\tv_exp_f32 %5, %5\n\tv_mfma_f32_16x16x32_bf16 %6, %9, %16, %6\n\tv_add_f32 %7, 1.0, %7\n\tv_add_f32 %8, 1.0, %8"
      : "+v"(ac[1][0]), "=&v"(pu[12]), "+v"(ac[1][1]), "+v"(ze[2]), "+v"(ac[1][2]), "+v"(ze[3]), "+v"(ac[1][3]), "+v"(ze[0]), "+v"(ze[1]) : "v"(pu[13]), "v"(fw2[13][0]), "v"(vFj[2]), "s"(rsW), "s"(su[12]), "v"(fw2[13][1]), "v"(fw2[13][2]), "v"(fw2[13][3]) : "memory"); SB();
  STAMP(41);
  WAITVB(12);
  SB();
  asm volatile("v_mfma_f32_16x16x32_bf16 %0, %9, %10, 0\n\tbuffer_load_dwordx4 %1, %11, %12, %13 offen offset:0 sc1\n\tv_mfma_f32_16x16x32_bf16 %2, %9, %14, 0\n\tv_add_f32 %3, 1.0, %3\n\tv_add_f32 %4, 1.0, %4\n\tv_mfma_f32_16x16x32_bf16 %5, %9, %15, 0\n\tv_rcp_f32 %6, %6\n\tv_mfma_f32_16x16x32_bf16 %7, %9, %16, 0\n\tds_read_b128 %8, %17 offset:17408"
      : "=&v"(ac[0][0]), "=&v"(pu[13]), "=&v"(ac[0][1]), "+v"(ze[2]), "+v"(ze[3]), "=&v"(ac[0][2]), "+v"(ze[0]), "=&v"(ac[0][3]), "=&a"(fwo[5]) : "v"(pu[0]), "a"(fw2[0][0]), "v"(vFj[2]), "s"(rsW), "s"(su[13]), "a"(fw2[0][1]), "a"(fw2[0][2]), "a"(fw2[0][3]), "v"(ro) : "memory"); SB();
  WAITVB(12);
  SB();
  asm volatile("v_mfma_f32_16x16x32_bf16 %0, %1, %2, %0"
      : "+v"(ac[0][0]) : "v"(pu[1]), "a"(fw2[1][0])); SB();
  WAITV1(14, fo);
  if (__builtin_expect(__ballot(fo != nonce) != 0ull, 0)) poll1<4>(rsW, vFO, nonce);
  STAMP(18);
  asm volatile("buffer_load_dwordx4 %0, %6, %7, %8 offen offset:0 sc1\n\tv_mfma_f32_16x16x32_bf16 %1, %9, %10, %1\n\tds_write_b128 %11, %12 offset:18432\n\tv_mfma_f32_16x16x32_bf16 %2, %9, %13, %2\n\tv_rcp_f32 %3, %3\n\tv_mfma_f32_16x16x32_bf16 %4, %9, %14, %4\n\tds_read_b128 %5, %15 offset:2048"
      : "=&v"(pu[0]), "+v"(ac[0][1]), "+v"(ac[0][2]), "+v"(ze[1]), "+v"(ac[0][3]), "=&a"(fwo[6]) : "v"(vF), "s"(rsW), "s"(so0), "v"(pu[1]), "a"(fw2[1][1]), "v"(rwd), "v"(ac[1][0]), "a"(fw2[1][2]), "a"(fw2[1][3]), "v"(ro) : "memory"); SB();
  WAITVB(12);
  SB();
  asm volatile("v_mfma_f32_16x16x32_bf16 %0, %7, %8, %0\n\tbuffer_load_dwordx4 %1, %9, %10, %11 offen offset:128 sc1\n\tv_mfma_f32_16x16x32_bf16 %2, %7, %12, %2\n\tds_write_b128 %13, %14 offset:19456\n\tv_mfma_f32_16x16x32_bf16 %3, %7, %15, %3\n\tv_rcp_f32 %4, %4\n\tv_mfma_f32_16x16x32_bf16 %5, %7, %16, %5\n\tds_read_b128 %6, %17 offset:10240"
      : "+v"(ac[0][0]), "=&v"(pu[1]), "+v"(ac[0][1]), "+v"(ac[0][2]), "+v"(ze[2]), "+v"(ac[0][3]), "=&a"(fwo[7]) : "v"(pu[2]), "a"(fw2[2][0]), "v"(vF), "s"(rsW), "s"(so0), "a"(fw2[2][1]), "v"(rwd), "v"(ac[1][1]), "a"(fw2[2][2]), "a"(fw2[2][3]), "v"(ro) : "memory"); SB();
  WAITVB(12);
  SB();
  asm volatile("v_mfma_f32_16x16x32_bf16 %0, %7, %8, %0\n\tbuffer_load_dwordx4 %1, %9, %10, %11 offen offset:256 sc1\n\tv_mfma_f32_16x16x32_bf16 %2, %7, %12, %2\n\tds_write_b128 %13, %14 offset:20480\n\tv_mfma_f32_16x16x32_bf16 %3, %7, %15, %3\n\tv_rcp_f32 %4, %4\n\tv_mfma_f32_16x16x32_bf16 %5, %7, %16, %5\n\tv_mul_f32 %6, %17, %18"
      : "+v"(ac[0][0]), "=&v"(pu[2]), "+v"(ac[0][1]), "+v"(ac[0][2]), "+v"(ze[3]), "+v"(ac[0][3]), "=&v"(ge[0]) : "v"(pu[3]), "a"(fw2[3][0]), "v"(vF), "s"(rsW), "s"(so0), "a"(fw2[3][1]), "v"(rwd), "v"(ac[1][2]), "a"(fw2[3][2]), "a"(fw2[3][3]), "v"(ye[0]), "v"(ze[0]) : "memory"); SB();
  WAITVB(12);
  SB();
  asm volatile("v_mfma_f32_16x16x32_bf16 %0, %8, %9, %0\n\tbuffer_load_dwordx4 %1, %10, %11, %12 offen offset:384 sc1\n\tv_mfma_f32_16x16x32_bf16 %2, %8, %13, %2\n\tds_write_b128 %14, %15 offset:21504\n\tv_mfma_f32_16x16x32_bf16 %3, %8, %16, %3\n\tv_mul_f32 %4, %17, %18\n\tv_mul_f32 %5, %19, %20\n\tv_mfma_f32_16x16x32_bf16 %6, %8, %21, %6\n\tv_mul_f32 %7, %22, %23"
      : "+v"(ac[0][0]), "=&v"(pu[3]), "+v"(ac[0][1]), "+v"(ac[0][2]), "=&v"(ge[1]), "=&v"(ge[2]), "+v"(ac[0][3]), "=&v"(ge[3]) : "v"(pu[4]), "a"(fw2[4][0]), "v"(vF), "s"(rsW), "s"(so0), "a"(fw2[4][1]), "v"(rwd), "v"(ac[1][3]), "a"(fw2[4][2]), "v"(ye[1]), "v"(ze[1]), "v"(ye[2]), "v"(ze[2]), "a"(fw2[4][3]), "v"(ye[3]), "v"(ze[3]) : "memory"); SB();
  WAITVB(12);
  SB();
  asm volatile("v_mfma_f32_16x16x32_bf16 %0, %3, %4, %0\n\tbuffer_load_dwordx4 %1, %5, %6, %7 offen offset:512 sc1\n\tv_mfma_f32_16x16x32_bf16 %2, %3, %8, %2"
      : "+v"(ac[0][0]), "=&v"(pu[4]), "+v"(ac[0][1]) : "v"(pu[5]), "a"(fw2[5][0]), "v"(vF), "s"(rsW), "s"(so0), "a"(fw2[5][1]) : "memory"); SB();
  WAITL(0);
  BAR0();
  SB();
  asm volatile("v_mfma_f32_16x16x32_bf16 %0, %5, %6, %0\n\tds_read_b128 %1, %7 offset:18432\n\tds_read_b128 %2, %7 offset:22528\n\tv_mfma_f32_16x16x32_bf16 %3, %5, %8, %3\n\tds_read_b128 %4, %7 offset:26624"
      : "+v"(ac[0][2]), "=&v"(py[1][0]), "=&v"(py[1][1]), "+v"(ac[0][3]), "=&v"(py[1][2]) : "v"(pu[5]), "a"(fw2[5][2]), "v"(rr), "a"(fw2[5][3]) : "memory"); SB();
  WAITVB(12);
  SB();
  asm volatile("v_mfma_f32_16x16x32_bf16 %0, %8, %9, %0\n\tbuffer_load_dwordx4 %1, %10, %11, %12 offen offset:640 sc1\n\tv_mfma_f32_16x16x32_bf16 %2, %8, %13, %2\n\tds_read_b128 %3, %14 offset:30720\n\tds_read_b128 %4, %15 offset:18432\n\tv_mfma_f32_16x16x32_bf16 %5, %8, %16, %5\n\tv_mfma_f32_16x16x32_bf16 %6, %8, %17, %6\n\tds_read_b128 %7, %15 offset:3072"
      : "+v"(ac[0][0]), "=&v"(pu[5]), "+v"(ac[0][1]), "=&v"(py[1][3]), "=&a"(fwo[8]), "+v"(ac[0][2]), "+v"(ac[0][3]), "=&a"(fwo[9]) : "v"(pu[6]), "a"(fw2[6][0]), "v"(vF), "s"(rsW), "s"(so0), "a"(fw2[6][1]), "v"(rr), "v"(ro), "a"(fw2[6][2]), "a"(fw2[6][3]) : "memory"); SB();
  WAITVB(12);
  SB();
  asm volatile("v_mfma_f32_16x16x32_bf16 %0, %5, %6, %0\n\tbuffer_load_dwordx4 %1, %7, %8, %9 offen offset:768 sc1\n\tv_mfma_f32_16x16x32_bf16 %2, %5, %10, %2\n\tds_read_b128 %3, %11 offset:11264\n\tv_mfma_f32_16x16x32_bf16 %4, %5, %12, %4"
      : "+v"(ac[0][0]), "=&v"(pu[6]), "+v"(ac[0][1]), "=&a"(fwo[10]), "+v"(ac[0][2]) : "v"(pu[7]), "a"(fw2[7][0]), "v"(vF), "s"(rsW), "s"(so0), "a"(fw2[7][1]), "v"(ro), "a"(fw2[7][2]) : "memory"); SB();
  WAITL(3);
  asm volatile("v_add_f32 %0, %3, %4\n\tv_mfma_f32_16x16x32_bf16 %1, %5, %6, %1\n\tv_add_f32 %2, %7, %4"
      : "=&v"(ye[0]), "+v"(ac[0][3]), "=&v"(ye[1]) : "v"(py[1][0][0]), "v"(bias), "v"(pu[7]), "a"(fw2[7][3]), "v"(py[1][0][1])); SB();
  WAITVB(12);
  SB();
  asm volatile("v_mfma_f32_16x16x32_bf16 %0, %9, %10, %0\n\tbuffer_load_dwordx4 %1, %11, %12, %13 offen offset:0 sc1\n\tv_mfma_f32_16x16x32_bf16 %2, %9, %14, %2\n\tv_add_f32 %3, %15, %16\n\tv_add_f32 %4, %17, %16\n\tv_mfma_f32_16x16x32_bf16 %5, %9, %18, %5\n\tv_add_f32 %6, %19, %20\n\tv_add_f32 %7, %21, %22\n\tv_mfma_f32_16x16x32_bf16 %8, %9, %23, %8"
      : "+v"(ac[0][0]), "=&v"(pu[7]), "+v"(ac[0][1]), "=&v"(ye[2]), "=&v"(ye[3]), "+v"(ac[0][2]), "=&v"(ze[0]), "=&v"(ze[1]), "+v"(ac[0][3]) : "v"(pu[8]), "a"(fw2[8][0]), "v"(vF), "s"(rsW), "s"(so1), "a"(fw2[8][1]), "v"(py[1][0][2]), "v"(bias), "v"(py[1][0][3]), "a"(fw2[8][2]), "v"(py[1][1][0]), "v"(py[1][2][0]), "v"(py[1][1][1]), "v"(py[1][2][1]), "a"(fw2[8][3]) : "memory"); SB();
  asm volatile("v_add_f32 %0, %1, %2"
      : "=&v"(ze[2]) : "v"(py[1][1][2]), "v"(py[1][2][2])); SB();
  WAITVB(12);
  SB();
  asm volatile("v_mfma_f32_16x16x32_bf16 %0, %9, %10, %0\n\tbuffer_load_dwordx4 %1, %11, %12, %13 offen offset:128 sc1\n\tv_mfma_f32_16x16x32_bf16 %2, %9, %14, %2\n\tv_add_f32 %3, %15, %16\n\tv_add_f32 %4, %4, %17\n\tv_mfma_f32_16x16x32_bf16 %5, %9, %18, %5\n\tv_add_f32 %6, %6, %19\n\tv_add_f32 %7, %7, %20\n\tv_mfma_f32_16x16x32_bf16 %8, %9, %21, %8"
      : "+v"(ac[0][0]), "=&v"(pu[8]), "+v"(ac[0][1]), "=&v"(ze[3]), "+v"(ye[0]), "+v"(ac[0][2]), "+v"(ye[1]), "+v"(ye[2]), "+v"(ac[0][3]) : "v"(pu[9]), "v"(fw2[9][0]), "v"(vF), "s"(rsW), "s"(so1), "v"(fw2[9][1]), "v"(py[1][1][3]), "v"(py[1][2][3]), "v"(py[1][3][0]), "v"(fw2[9][2]), "v"(py[1][3][1]), "v"(py[1][3][2]), "v"(fw2[9][3]) : "memory"); SB();
  asm volatile("v_add_f32 %0, %0, %1"
      : "+v"(ye[3]) : "v"(py[1][3][3])); SB();
  WAITVB(12);
  SB();
  asm volatile("v_mfma_f32_16x16x32_bf16 %0, %10, %11, %0\n\tbuffer_load_dwordx4 %1, %12, %13, %14 offen offset:256 sc1\n\tv_mfma_f32_16x16x32_bf16 %2, %10, %15, %2\n\tv_add_f32 %3, %3, %4\n\tv_add_f32 %5, %5, %16\n\tv_mfma_f32_16x16x32_bf16 %6, %10, %17, %6\n\tv_add_f32 %7, %7, %18\n\tv_add_f32 %8, %8, %19\n\tv_mfma_f32_16x16x32_bf16 %9, %10, %20, %9\n\tv_mul_f32 %4, 0xbfb8aa3b, %3"
      : "+v"(ac[0][0]), "=&v"(pu[9]), "+v"(ac[0][1]), "+v"(ye[0]), "+v"(ze[0]), "+v"(ye[1]), "+v"(ac[0][2]), "+v"(ye[2]), "+v"(ye[3]), "+v"(ac[0][3]) : "v"(pu[10]), "v"(fw2[10][0]), "v"(vF), "s"(rsW), "s"(so1), "v"(fw2[10][1]), "v"(ze[1]), "v"(fw2[10][2]), "v"(ze[2]), "v"(ze[3]), "v"(fw2[10][3]) : "memory"); SB();
  WAITVB(12);
  SB();
  asm volatile("v_mfma_f32_16x16x32_bf16 %0, %9, %10, %0\n\tbuffer_load_dwordx4 %1, %11, %12, %13 offen offset:384 sc1\n\tv_mfma_f32_16x16x32_bf16 %2, %9, %14, %2\n\tv_mul_f32 %3, 0xbfb8aa3b, %15\n\tv_mul_f32 %4, 0xbfb8aa3b, %16\n\tv_mfma_f32_16x16x32_bf16 %5, %9, %17, %5\n\tds_read_b128 %6, %18 offset:19456\n\tv_mul_f32 %7, 0xbfb8aa3b, %19\n\tv_mfma_f32_16x16x32_bf16 %8, %9, %20, %8"
      : "+v"(ac[0][0]), "=&v"(pu[10]), "+v"(ac[0][1]), "=&v"(ze[1]), "=&v"(ze[2]), "+v"(ac[0][2]), "=&a"(fwo[11]), "=&v"(ze[3]), "+v"(ac[0][3]) : "v"(pu[11]), "v"(fw2[11][0]), "v"(vF), "s"(rsW), "s"(so1), "v"(fw2[11][1]), "v"(ye[1]), "v"(ye[2]), "v"(fw2[11][2]), "v"(ro), "v"(ye[3]), "v"(fw2[11][3]) : "memory"); SB();
  WAITVB(12);
  SB();
  asm volatile("v_mfma_f32_16x16x32_bf16 %0, %8, %9, %0\n\tbuffer_load_dwordx4 %1, %10, %11, %12 offen offset:512 sc1\n\tv_mfma_f32_16x16x32_bf16 %2, %8, %13, %2\n\tv_exp_f32 %3, %3\n\tv_mfma_f32_16x16x32_bf16 %4, %8, %14, %4\n\tv_exp_f32 %5, %5\n\tv_mfma_f32_16x16x32_bf16 %6, %8, %15, %6\n\tds_read_b128 %7, %16 offset:4096"
      : "+v"(ac[0][0]), "=&v"(pu[11]), "+v"(ac[0][1]), "+v"(ze[0]), "+v"(ac[0][2]), "+v"(ze[1]), "+v"(ac[0][3]), "=&a"(fwo[12]) : "v"(pu[12]), "v"(fw2[12][0]), "v"(vF), "s"(rsW), "s"(so1), "v"(fw2[12][1]), "v"(fw2[12][2]), "v"(fw2[12][3]), "v"(ro) : "memory"); SB();
  WAITVB(12);
  SB();
  asm volatile("v_mfma_f32_16x16x32_bf16 %0, %9, %10, %0\n\tbuffer_load_dwordx4 %1, %11, %12, %13 offen offset:640 sc1\n\tv_mfma_f32_16x16x32_bf16 %2, %9, %14, %2\n\tv_exp_f32 %3, %3\n\tv_mfma_f32_16x16x32_bf16 %4, %9, %15, %4\n\tv_exp_f32 %5, %5\n\tv_mfma_f32_16x16x32_bf16 %6, %9, %16, %6\n\tv_add_f32 %7, 1.0, %7\n\tv_add_f32 %8, 1.0, %8"
      : "+v"(ac[0][0]), "=&v"(pu[12]), "+v"(ac[0][1]), "+v"(ze[2]), "+v"(ac[0][2]), "+v"(ze[3]), "+v"(ac[0][3]), "+v"(ze[0]), "+v"(ze[1]) : "v"(pu[13]), "v"(fw2[13][0]), "v"(vF), "s"(rsW), "s"(so1), "v"(fw2[13][1]), "v"(fw2[13][2]), "v"(fw2[13][3]) : "memory"); SB();
  STAMP(42);
  WAITVB(12);
  asm volatile("ds_read_b128 %0, %8 offset:12288\n\tv_mfma_f32_16x16x32_bf16 %1, %9, %10, 0\n\tbuffer_load_dwordx4 %2, %11, %12, %13 offen offset:768 sc1\n\tv_mfma_f32_16x16x32_bf16 %3, %14, %10, 0\n\tv_add_f32 %4, 1.0, %4\n\tv_add_f32 %5, 1.0, %5\n\tv_mfma_f32_16x16x32_bf16 %6, %15, %10, 0\n\tds_read_b128 %7, %8 offset:20480"
      : "=&a"(fwo[13]), "=&v"(as[0]), "=&v"(pu[13]), "=&v"(as[1]), "+v"(ze[2]), "+v"(ze[3]), "=&v"(as[2]), "=&a"(fwo[14]) : "v"(ro), "a"(fwo[0]), "v"(pu[0]), "v"(vF), "s"(rsW), "s"(so1), "a"(fwo[1]), "a"(fwo[2]) : "memory"); SB();
  WAITVB(12);
  SB();
  asm volatile("v_mfma_f32_16x16x32_bf16 %0, %5, %6, %0\n\tds_write_b128 %7, %8 offset:0\n\tv_mfma_f32_16x16x32_bf16 %1, %9, %6, %1\n\tv_rcp_f32 %2, %2\n\tv_mfma_f32_16x16x32_bf16 %3, %10, %6, %3\n\tds_read_b128 %4, %11 offset:5120"
      : "+v"(as[0]), "+v"(as[1]), "+v"(ze[0]), "+v"(as[2]), "=&a"(fwo[15]) : "a"(fwo[3]), "v"(pu[1]), "v"(rwd), "v"(ac[0][0]), "a"(fwo[4]), "a"(fwo[5]), "v"(ro) : "memory"); SB();
  WAITVB(11);
  asm volatile("ds_read_b128 %0, %4 offset:13312\n\tds_read_b128 %1, %4 offset:21504\n\tds_read_b128 %2, %4 offset:6144\n\tds_read_b128 %3, %4 offset:14336"
      : "=&a"(fwo[16]), "=&a"(fwo[17]), "=&a"(fwo[18]), "=&a"(fwo[19]) : "v"(ro) : "memory"); SB();
  WAITL(9);
  SB();
  asm volatile("v_mfma_f32_16x16x32_bf16 %0, %5, %6, %0\n\tds_write_b128 %7, %8 offset:1024\n\tv_mfma_f32_16x16x32_bf16 %1, %9, %6, %1\n\tv_rcp_f32 %2, %2\n\tv_mfma_f32_16x16x32_bf16 %3, %10, %6, %3\n\tds_read_b128 %4, %11 offset:22528"
      : "+v"(as[0]), "+v"(as[1]), "+v"(ze[1]), "+v"(as[2]), "=&a"(fwo[20]) : "a"(fwo[6]), "v"(pu[2]), "v"(rwd), "v"(ac[0][1]), "a"(fwo[7]), "a"(fwo[8]), "v"(ro) : "memory"); SB();
  WAITVB(10);
  SB();
  asm volatile("v_mfma_f32_16x16x32_bf16 %0, %5, %6, %0\n\tds_write_b128 %7, %8 offset:2048\n\tv_mfma_f32_16x16x32_bf16 %1, %9, %6, %1\n\tv_rcp_f32 %2, %2\n\tv_mfma_f32_16x16x32_bf16 %3, %10, %6, %3\n\tds_read_b128 %4, %11 offset:7168"
      : "+v"(as[0]), "+v"(as[1]), "+v"(ze[2]), "+v"(as[2]), "=&a"(fwo[21]) : "a"(fwo[9]), "v"(pu[3]), "v"(rwd), "v"(ac[0][2]), "a"(fwo[10]), "a"(fwo[11]), "v"(ro) : "memory"); SB();
  WAITVB(9);
  asm volatile("ds_read_b128 %0, %4 offset:15360\n\tds_read_b128 %1, %4 offset:23552\n\tds_read_b128 %2, %4 offset:32768\n\tds_read_b128 %3, %4 offset:38912"
      : "=&a"(fwo[22]), "=&a"(fwo[23]), "=&a"(fwo[24]), "=&a"(fwo[25]) : "v"(ro) : "memory"); SB();
  WAITL(10);
  SB();
  asm volatile("v_mfma_f32_16x16x32_bf16 %0, %5, %6, %0\n\tds_write_b128 %7, %8 offset:3072\n\tv_mfma_f32_16x16x32_bf16 %1, %9, %6, %1\n\tv_rcp_f32 %2, %2\n\tv_mfma_f32_16x16x32_bf16 %3, %10, %6, %3\n\tv_mul_f32 %4, %11, %12"
      : "+v"(as[0]), "+v"(as[1]), "+v"(ze[3]), "+v"(as[2]), "=&v"(ge[4]) : "a"(fwo[12]), "v"(pu[4]), "v"(rwd), "v"(ac[0][3]), "a"(fwo[13]), "a"(fwo[14]), "v"(ye[0]), "v"(ze[0]) : "memory"); SB();
  WAITVB(8);
  SB();
  asm volatile("v_mfma_f32_16x16x32_bf16 %0, %4, %5, %0\n\tv_mul_f32 %1, %6, %7\n\tv_mul_f32 %2, %8, %9\n\tv_mfma_f32_16x16x32_bf16 %3, %10, %5, %3"
      : "+v"(as[0]), "=&v"(ge[5]), "=&v"(ge[6]), "+v"(as[1]) : "a"(fwo[15]), "v"(pu[5]), "v"(ye[1]), "v"(ze[1]), "v"(ye[2]), "v"(ze[2]), "a"(fwo[16])); SB();
  WAITL(0);
  BAR0();
  SB();
  asm volatile("v_mfma_f32_16x16x32_bf16 %0, %1, %2, %0"
      : "+v"(as[2]) : "a"(fwo[17]), "v"(pu[5])); SB();
  WAITVB(7);
  asm volatile("ds_read_b128 %0, %14 offset:45056\n\tds_read_b128 %1, %14 offset:33792\n\tds_read_b128 %2, %14 offset:39936\n\tds_read_b128 %3, %14 offset:46080\n\tds_read_b128 %4, %14 offset:34816\n\tds_read_b128 %5, %14 offset:40960\n\tv_mfma_f32_16x16x32_bf16 %6, %15, %16, %6\n\tds_read_b128 %7, %17 offset:0\n\tds_read_b128 %8, %17 offset:4096\n\tv_mfma_f32_16x16x32_bf16 %9, %18, %16, %9\n\tds_read_b128 %10, %17 offset:8192\n\tds_read_b128 %11, %17 offset:12288\n\tv_mfma_f32_16x16x32_bf16 %12, %19, %16, %12\n\tv_mul_f32 %13, %20, %21"
      : "=&a"(fwo[26]), "=&a"(fwo[27]), "=&a"(fwo[28]), "=&a"(fwo[29]), "=&a"(fwo[30]), "=&a"(fwo[31]), "+v"(as[0]), "=&v"(py[2][0]), "=&v"(py[2][1]), "+v"(as[1]), "=&v"(py[2][2]), "=&v"(py[2][3]), "+v"(as[2]), "=&v"(ge[7]) : "v"(ro), "a"(fwo[18]), "v"(pu[6]), "v"(rr), "a"(fwo[19]), "a"(fwo[20]), "v"(ye[3]), "v"(ze[3]) : "memory"); SB();
  WAITVB(6);
  SB();
  asm volatile("v_mfma_f32_16x16x32_bf16 %0, %4, %5, %0\n\tds_read_b128 %1, %6 offset:47104\n\tv_mfma_f32_16x16x32_bf16 %2, %7, %5, %2\n\tv_mfma_f32_16x16x32_bf16 %3, %8, %5, %3"
      : "+v"(as[0]), "=&a"(fwo[32]), "+v"(as[1]), "+v"(as[2]) : "a"(fwo[21]), "v"(pu[7]), "v"(ro), "a"(fwo[22]), "a"(fwo[23]) : "memory"); SB();
  WAITVB(5);
  asm volatile("ds_read_b128 %0, %5 offset:35840\n\tds_read_b128 %1, %5 offset:41984\n\tds_read_b128 %2, %5 offset:48128\n\tds_read_b128 %3, %5 offset:36864\n\tds_read_b128 %4, %5 offset:43008"
      : "=&a"(fwo[33]), "=&a"(fwo[34]), "=&a"(fwo[35]), "=&a"(fwo[36]), "=&a"(fwo[37]) : "v"(ro) : "memory"); SB();
  WAITL(12);
  SB();
  asm volatile("v_mfma_f32_16x16x32_bf16 %0, %1, %2, %0"
      : "+v"(as[0]) : "a"(fwo[24]), "v"(pu[8])); SB();
  WAITL(6);
  asm volatile("v_add_f32 %0, %6, %7\n\tv_mfma_f32_16x16x32_bf16 %1, %8, %9, %1\n\tv_add_f32 %2, %10, %7\n\tv_add_f32 %3, %11, %7\n\tv_mfma_f32_16x16x32_bf16 %4, %12, %9, %4\n\tv_add_f32 %5, %13, %7"
      : "=&v"(ye[0]), "+v"(as[1]), "=&v"(ye[1]), "=&v"(ye[2]), "+v"(as[2]), "=&v"(ye[3]) : "v"(py[2][0][0]), "v"(bias), "a"(fwo[25]), "v"(pu[8]), "v"(py[2][0][1]), "v"(py[2][0][2]), "a"(fwo[26]), "v"(py[2][0][3])); SB();
  WAITVB(4);
  SB();
  asm volatile("v_mfma_f32_16x16x32_bf16 %0, %7, %8, %0\n\tv_add_f32 %1, %9, %10\n\tv_add_f32 %2, %11, %12\n\tv_mfma_f32_16x16x32_bf16 %3, %13, %8, %3\n\tv_add_f32 %4, %14, %15\n\tv_add_f32 %5, %16, %17\n\tv_mfma_f32_16x16x32_bf16 %6, %18, %8, %6"
      : "+v"(as[0]), "=&v"(ze[0]), "=&v"(ze[1]), "+v"(as[1]), "=&v"(ze[2]), "=&v"(ze[3]), "+v"(as[2]) : "a"(fwo[27]), "v"(pu[9]), "v"(py[2][1][0]), "v"(py[2][2][0]), "v"(py[2][1][1]), "v"(py[2][2][1]), "a"(fwo[28]), "v"(py[2][1][2]), "v"(py[2][2][2]), "v"(py[2][1][3]), "v"(py[2][2][3]), "a"(fwo[29])); SB();
  WAITVB(3);
  asm volatile("ds_read_b128 %0, %4 offset:49152\n\tds_read_b128 %1, %4 offset:37888\n\tds_read_b128 %2, %4 offset:44032\n\tds_read_b128 %3, %4 offset:50176"
      : "=&a"(fwo[38]), "=&a"(fwo[39]), "=&a"(fwo[40]), "=&a"(fwo[41]) : "v"(ro) : "memory"); SB();
  WAITL(6);
  SB();
  asm volatile("v_mfma_f32_16x16x32_bf16 %0, %7, %8, %0\n\tv_add_f32 %1, %1, %9\n\tv_add_f32 %2, %2, %10\n\tv_mfma_f32_16x16x32_bf16 %3, %11, %8, %3\n\tv_add_f32 %4, %4, %12\n\tv_add_f32 %5, %5, %13\n\tv_mfma_f32_16x16x32_bf16 %6, %14, %8, %6"
      : "+v"(as[0]), "+v"(ye[0]), "+v"(ye[1]), "+v"(as[1]), "+v"(ye[2]), "+v"(ye[3]), "+v"(as[2]) : "a"(fwo[30]), "v"(pu[10]), "v"(py[2][3][0]), "v"(py[2][3][1]), "a"(fwo[31]), "v"(py[2][3][2]), "v"(py[2][3][3]), "a"(fwo[32])); SB();
  WAITL(0);
  WAITVB(0);
  SB();
  asm volatile("v_mfma_f32_16x16x32_bf16 %0, %10, %11, %0\n\tv_add_f32 %1, %1, %2\n\tv_add_f32 %3, %3, %4\n\tv_mfma_f32_16x16x32_bf16 %0, %12, %13, %0\n\tv_add_f32 %5, %5, %6\n\tv_add_f32 %7, %7, %8\n\tv_mfma_f32_16x16x32_bf16 %0, %14, %15, %0\n\tv_mul_f32 %2, 0xbfb8aa3b, %1\n\tv_mul_f32 %4, 0xbfb8aa3b, %3\n\tv_mfma_f32_16x16x32_bf16 %9, %16, %11, %9\n\tv_mul_f32 %6, 0xbfb8aa3b, %5\n\tv_mul_f32 %8, 0xbfb8aa3b, %7\n\tv_mfma_f32_16x16x32_bf16 %9, %17, %13, %9\n\tv_exp_f32 %2, %2\n\tv_mfma_f32_16x16x32_bf16 %9, %18, %15, %9\n\tv_exp_f32 %4, %4"
      : "+v"(as[0]), "+v"(ye[0]), "+v"(ze[0]), "+v"(ye[1]), "+v"(ze[1]), "+v"(ye[2]), "+v"(ze[2]), "+v"(ye[3]), "+v"(ze[3]), "+v"(as[1]) : "a"(fwo[33]), "v"(pu[11]), "a"(fwo[36]), "v"(pu[12]), "a"(fwo[39]), "v"(pu[13]), "a"(fwo[34]), "a"(fwo[37]), "a"(fwo[40])); SB();
  asm volatile("v_mfma_f32_16x16x32_bf16 %0, %5, %6, %0\n\tv_exp_f32 %1, %1\n\tv_mfma_f32_16x16x32_bf16 %0, %7, %8, %0\n\tv_exp_f32 %2, %2\n\tv_mfma_f32_16x16x32_bf16 %0, %9, %10, %0\n\tv_add_f32 %3, 1.0, %3\n\tv_add_f32 %4, 1.0, %4"
      : "+v"(as[2]), "+v"(ze[2]), "+v"(ze[3]), "+v"(ze[0]), "+v"(ze[1]) : "a"(fwo[35]), "v"(pu[11]), "a"(fwo[38]), "v"(pu[12]), "a"(fwo[41]), "v"(pu[13])); SB();
  STAMP(44);
  asm volatile("v_add_f32 %0, 1.0, %0\n\tv_add_f32 %1, 1.0, %1\n\tv_rcp_f32 %2, %2\n\tv_rcp_f32 %3, %3\n\tv_rcp_f32 %0, %0\n\tv_rcp_f32 %1, %1\n\tv_mul_f32 %4, %11, %2\n\tv_mul_f32 %5, %12, %3\n\tv_mul_f32 %6, %13, %0\n\tv_mul_f32 %7, %14, %1\n\tv_fma_f32 %8, %15, %16, %8\n\tv_fma_f32 %9, %17, %18, %9\n\tv_fma_f32 %10, %19, %20, %10"
      : "+v"(ze[2]), "+v"(ze[3]), "+v"(ze[0]), "+v"(ze[1]), "=&v"(ge[8]), "=&v"(ge[9]), "=&v"(ge[10]), "=&v"(ge[11]), "+v"(xe[0]), "+v"(xe[1]), "+v"(xe[2]) : "v"(ye[0]), "v"(ye[1]), "v"(ye[2]), "v"(ye[3]), "v"(as[0][0]), "v"(ge[0]), "v"(as[0][1]), "v"(ge[1]), "v"(as[0][2]), "v"(ge[2])); SB();
  asm volatile("v_fma_f32 %0, %9, %10, %0\n\tv_cvt_pk_bf16_f32 %1, %11, %12\n\tv_cvt_pk_bf16_f32 %2, %13, %0\n\tv_fma_f32 %3, %14, %15, %3\n\tv_fma_f32 %4, %16, %17, %4\n\tv_fma_f32 %5, %18, %19, %5\n\tv_fma_f32 %6, %20, %21, %6\n\tv_cvt_pk_bf16_f32 %7, %3, %4\n\tv_cvt_pk_bf16_f32 %8, %5, %6"
      : "+v"(xe[3]), "=&v"(ov[0][0]), "=&v"(ov[0][1]), "+v"(xe[4]), "+v"(xe[5]), "+v"(xe[6]), "+v"(xe[7]), "=&v"(ov[1][0]), "=&v"(ov[1][1]) : "v"(as[0][3]), "v"(ge[3]), "v"(xe[0]), "v"(xe[1]), "v"(xe[2]), "v"(as[1][0]), "v"(ge[4]), "v"(as[1][1]), "v"(ge[5]), "v"(as[1][2]), "v"(ge[6]), "v"(as[1][3]), "v"(ge[7])); SB();
  asm volatile("ds_write_b64 %6, %7 offset:0\n\tv_fma_f32 %0, %8, %9, %0\n\tv_fma_f32 %1, %10, %11, %1\n\tv_fma_f32 %2, %12, %13, %2\n\tv_fma_f32 %3, %14, %15, %3\n\tv_cvt_pk_bf16_f32 %4, %0, %1\n\tv_cvt_pk_bf16_f32 %5, %2, %3\n\tds_write_b64 %16, %17 offset:0"
      : "+v"(xe[8]), "+v"(xe[9]), "+v"(xe[10]), "+v"(xe[11]), "=&v"(ov[2][0]), "=&v"(ov[2][1]) : "v"(xta[0]), "v"(ov[0]), "v"(as[2][0]), "v"(ge[8]), "v"(as[2][1]), "v"(ge[9]), "v"(as[2][2]), "v"(ge[10]), "v"(as[2][3]), "v"(ge[11]), "v"(xta[1]), "v"(ov[1]) : "memory"); SB();
  asm volatile("ds_write_b64 %0, %1 offset:0"
      :  : "v"(xta[2]), "v"(ov[2]) : "memory"); SB();
  { u32x4 t0_, t1_; DSR16(t0_, xo, 0); DSR16(t1_, xo, 1024); WAITLB(1);
    BS16(vXa, rsO_, 0, t0_, ""); WAITLB(0); BS16(vXb, rsO_, 1024, t1_, ""); }
  STAMP(23);

  STAMP(12);
  STAMP_DUMP();
  }
  } else {
  asm volatile("; NEED B");
  unsigned nonce0; asm volatile("s_load_dword %0, %1, 0x0" : "=&s"(nonce0) : "s"(ws) : "memory");
  const int iw = (4 * xq + jb - 28) * 4 + w, ih = (iw * 37) >> 8;
  const unsigned ldsI = lds0 + w * SCW;
  { u32x4 rsO = srd(wo, 448u * 896u);
    const unsigned vO_ = (28 * (r8 >> 2) + (r8 & 3)) * 896 + ((lane & 7) ^ ((r8 & 2) << 1)) * 16, sO_ = (4 * iw + 28 * ih) * 896;
    DMA1(vO_, rsO, sO_, ldsI, 0, 0, "");
    DMA1(vO_, rsO, sO_, ldsI, 896, 128, "");
    DMA1(vO_, rsO, sO_, ldsI, 1792, 256, "");
    DMA1(vO_, rsO, sO_, ldsI, 2688, 384, "");
    DMA1(vO_, rsO, sO_, ldsI, 3584, 512, "");
    DMA1(vO_, rsO, sO_, ldsI, 4480, 640, "");
    DMA1(vO_, rsO, sO_, ldsI, 5376, 768, "");
  }
  u32x4 rsW = srd((char*)(((size_t)ws + 8191) & ~(size_t)4095) + 512, WS_END); OPQ(rsW);
  const unsigned trb = (8 * g4 + q4) * 128 + pq * 8, trf = ((q4 >> 1) | ((g4 & 1) << 1)) * 32;
  const unsigned vF = r8 * 1024 + (lane & 7) * 16;
  const int r2 = (3 - (int)((size_t)w2 >> 7)) & 7;
  const int st = 8 * xq + r2, dt = jb - 28, db0 = 16 + 3 * dt;
#undef BLK
#define BLK(j) (db0 + ((j) == 2 ? 2 - 2 * (dt & 1) : ((j) ^ 0) + (dt & 1)))
  unsigned to = ldsI + q4 * 128 + ((g4 ^ (q4 & 2)) * 32) + pq * 8, vo[7], so_ = iw < 56 ? WS_WO + (iw >> 2) * 1024 + (iw & 3) * 32768 : 0x80000000u; OPQ(so_);
#pragma unroll
  for (int t = 0; t < 7; ++t) { const int k_ = 4 * t + g4, k7 = (k_ * 37) >> 8; vo[t] = k7 * (131072 - 896) + k_ * 128 + (n16 >> 3) * 16384 + (lane & 7) * 16; }
  u32x4 rs2 = srd(w2, 1792u * 4096u); OPQ(rs2);
  unsigned vW2[4];
#pragma unroll
  for (int i = 0; i < 4; ++i) vW2[i] = (8 * (r8 & 3) + 4 * (r8 >> 2)) * 4096 + st * 128 + ((lane & 7) ^ (2 * i)) * 16 + i * (4096 - 1024);
  int rot2 = 4 * dt + 2 * (st & 7) + 2; rot2 -= 14 * ((rot2 * 37) >> 9);
  bf16x8 fw2[14][4];
  unsigned ldsS = lds0 + S3B + w * 4096, ldsO = lds0 + WOB + w * 8192, ldsP = lds0 + WOB + 24576 + w * 6144, sWo, vWo = r8 * 16384 + (lane & 7) * 16, sWt;
  { const int k_ = BLK(w), k7 = (k_ * 37) >> 8; sWo = WS_WO + k7 * (131072 - 896) + k_ * 128; }
  unsigned fq, vFQ = WS_FQ + (lane < 56 ? lane : 55) * (4 * 4);
  asm volatile("" : "+v"(vW2[0]), "+v"(vW2[1]), "+v"(vW2[2]), "+v"(vW2[3]), "+v"(vFQ), "+v"(vWo)); asm volatile("" : "+s"(rot2), "+s"(ldsS), "+s"(ldsO), "+s"(ldsP), "+s"(sWo));
  if (w == 3) vWo = 0x40000000u; OPV(vWo);
  unsigned tS[4];
#pragma unroll
  for (int i = 0; i < 4; ++i) { tS[i] = ldsS + q4 * 1024 + g4 * 128 + pq * 8 + ((i ^ q4) * 32); OPV(tS[i]); }
  unsigned tO[4];
#pragma unroll
  for (int i = 0; i < 4; ++i) { tO[i] = ldsO + q4 * 1024 + g4 * 128 + pq * 8 + ((i ^ q4) * 32); OPV(tO[i]); }
  unsigned su[14], sw[14], sc_ = 14 * w + rot2; OPQ(sc_);
  { unsigned k_ = sc_ + (rot2 >= 14 ? -14 : 0); OPQ(k_); su[0] = k_ << 13; sw[0] = k_ << 17; OPQ(su[0]); OPQ(sw[0]); }
  { unsigned k_ = sc_ + (rot2 >= 13 ? -13 : 1); OPQ(k_); su[1] = k_ << 13; sw[1] = k_ << 17; OPQ(su[1]); OPQ(sw[1]); }
  { unsigned k_ = sc_ + (rot2 >= 12 ? -12 : 2); OPQ(k_); su[2] = k_ << 13; sw[2] = k_ << 17; OPQ(su[2]); OPQ(sw[2]); }
  { unsigned k_ = sc_ + (rot2 >= 11 ? -11 : 3); OPQ(k_); su[3] = k_ << 13; sw[3] = k_ << 17; OPQ(su[3]); OPQ(sw[3]); }
  { unsigned k_ = sc_ + (rot2 >= 10 ? -10 : 4); OPQ(k_); su[4] = k_ << 13; sw[4] = k_ << 17; OPQ(su[4]); OPQ(sw[4]); }
  { unsigned k_ = sc_ + (rot2 >= 9 ? -9 : 5); OPQ(k_); su[5] = k_ << 13; sw[5] = k_ << 17; OPQ(su[5]); OPQ(sw[5]); }
  { unsigned k_ = sc_ + (rot2 >= 8 ? -8 : 6); OPQ(k_); su[6] = k_ << 13; sw[6] = k_ << 17; OPQ(su[6]); OPQ(sw[6]); }
  { unsigned k_ = sc_ + (rot2 >= 7 ? -7 : 7); OPQ(k_); su[7] = k_ << 13; sw[7] = k_ << 17; OPQ(su[7]); OPQ(sw[7]); }
  { unsigned k_ = sc_ + (rot2 >= 6 ? -6 : 8); OPQ(k_); su[8] = k_ << 13; sw[8] = k_ << 17; OPQ(su[8]); OPQ(sw[8]); }
  { unsigned k_ = sc_ + (rot2 >= 5 ? -5 : 9); OPQ(k_); su[9] = k_ << 13; sw[9] = k_ << 17; OPQ(su[9]); OPQ(sw[9]); }
  { unsigned k_ = sc_ + (rot2 >= 4 ? -4 : 10); OPQ(k_); su[10] = k_ << 13; sw[10] = k_ << 17; OPQ(su[10]); OPQ(sw[10]); }
  { unsigned k_ = sc_ + (rot2 >= 3 ? -3 : 11); OPQ(k_); su[11] = k_ << 13; sw[11] = k_ << 17; OPQ(su[11]); OPQ(sw[11]); }
  { unsigned k_ = sc_ + (rot2 >= 2 ? -2 : 12); OPQ(k_); su[12] = k_ << 13; sw[12] = k_ << 17; OPQ(su[12]); OPQ(sw[12]); }
  { unsigned k_ = sc_ + (rot2 >= 1 ? -1 : 13); OPQ(k_); su[13] = k_ << 13; sw[13] = k_ << 17; OPQ(su[13]); OPQ(sw[13]); }
  unsigned vFU, vFO;
  { const int da = (db0 * 37) >> 8, db = ((db0 + 2) * 37) >> 8, l_ = lane < 56 ? lane : 55;
    vFU = WS_FU + ((l_ < 28 ? da * 128 + l_ : db * 128 + l_ - 28) + w * 28) * (4 * 4);
    vFO = WS_FO + ((2 * st + (w >> 1)) * 8 + (lane & 7)) * (4 * 16);
    asm volatile("" : "+v"(vFU), "+v"(vFO)); }
  unsigned vFU1 = WS_FU + (512 + ((db0 * 37) >> 8) * 128 + w * 28 + (lane < 28 ? lane : 27)) * (4 * 4), fu2; OPV(vFU1);
  unsigned vFj[4], so0 = WS_OT + (4 * st + w) * 16384, so1 = so0 + 8192;
#pragma unroll
  for (int j = 0; j < 3; ++j) { const int k_ = BLK(j), k7 = (k_ * 37) >> 8; vFj[j] = vF + (WS_UT + k7 * (56 * 8192 - 896) + k_ * 128); OPV(vFj[j]); }
  asm volatile("" : "+s"(so0), "+s"(so1));
  const int tau = 2 * (n16 & 7) + (n16 >> 3);
  unsigned rwd = ldsS + vL16, rr = lds0 + S3B + w * 1024 + (16 * g4 + tau) * 16, ro = lds0 + WOB + vL16;
  unsigned xo = ldsS + 34816 + vL16, braw, xt = ldsS + 34816 + tau * 128 + (((g4 >> 1) ^ (n16 & 7)) * 16) + (g4 & 1) * 8, vB2 = (64 * st + 16 * w + tau) * 2;
  unsigned xs = xt ^ (32 * 0);
  asm volatile("" : "+v"(rwd), "+v"(rr), "+v"(ro), "+v"(xt), "+v"(xs), "+v"(xo), "+v"(vB2));
  unsigned vXa, vXb;
  { const int ga = (lane & 7) ^ (r8 >> 1), gb = ga ^ 4, r_ = (64 * st + 16 * w + r8) * 896;
    vXa = ga < 4 ? r_ + (db0 + (dt & 1)) * 32 + ga * 16 : ga < 6 ? r_ + BLK(2) * 32 + (ga - 4) * 16 : 0x40000000;
    vXb = (gb < 4 ? r_ + (db0 + (dt & 1)) * 32 + gb * 16 : gb < 6 ? r_ + BLK(2) * 32 + (gb - 4) * 16 : 0x40000000) + (8 * 896 - 1024); OPV(vXa); OPV(vXb); }
  unsigned nonce; asm volatile("s_waitcnt lgkmcnt(0)\n\ts_add_u32 %0, %1, 1" : "=s"(nonce) : "s"(nonce0) : "memory", "scc");
  asm volatile("s_waitcnt vmcnt(6)"
      :  :  : "memory"); SB();
  { const bf16x8 q_ = tr8u(to + 0, 512);
    HS16(vo[0], so_, 0, __builtin_bit_cast(u32x4, q_)); }
  asm volatile("s_waitcnt vmcnt(6)"
      :  :  : "memory"); SB();
  { const bf16x8 q_ = tr8u(to + 1024, 512);
    HS16(vo[1], so_, 0, __builtin_bit_cast(u32x4, q_)); }
  asm volatile("s_waitcnt vmcnt(6)"
      :  :  : "memory"); SB();
  { const bf16x8 q_ = tr8u(to + 2048, 512);
    HS16(vo[2], so_, 0, __builtin_bit_cast(u32x4, q_)); }
  asm volatile("s_waitcnt vmcnt(6)"
      :  :  : "memory"); SB();
  { const bf16x8 q_ = tr8u(to + 3072, 512);
    HS16(vo[3], so_, 0, __builtin_bit_cast(u32x4, q_)); }
  asm volatile("s_waitcnt vmcnt(6)"
      :  :  : "memory"); SB();
  { const bf16x8 q_ = tr8u(to + 4096, 512);
    HS16(vo[4], so_, 0, __builtin_bit_cast(u32x4, q_)); }
  asm volatile("s_waitcnt vmcnt(6)"
      :  :  : "memory"); SB();
  { const bf16x8 q_ = tr8u(to + 5120, 512);
    HS16(vo[5], so_, 0, __builtin_bit_cast(u32x4, q_)); }
  asm volatile("s_waitcnt vmcnt(6)"
      :  :  : "memory"); SB();
  { const bf16x8 q_ = tr8u(to + 6144, 512);
    HS16(vo[6], so_, 0, __builtin_bit_cast(u32x4, q_)); }
  for (int k = 0; k < 0; ++k) __builtin_amdgcn_s_sleep(4);
  const unsigned vT = (lane & 7) == r2 ? 0x40000000u : (448 * w + 8 * dt + r8) * 4096 + (8 * xq + (lane & 7)) * 128; const int k0 = (2 * (lane & 7)) % 14;
  { const unsigned v_ = vT + (k0 + 0 >= 14 ? k0 + 0 - 14 : k0 + 0) * 131072; TOUCH(v_, rs2, 0u, 0); }
  { const unsigned v_ = vT + (k0 + 4 >= 14 ? k0 + 4 - 14 : k0 + 4) * 131072; TOUCH(v_, rs2, 0u, 0); }
  asm volatile("s_waitcnt vmcnt(1)"
      :  :  : "memory"); SB();
  { const unsigned v_ = vT + (k0 + 8 >= 14 ? k0 + 8 - 14 : k0 + 8) * 131072; TOUCH(v_, rs2, 0u, 0); }
  asm volatile("s_waitcnt vmcnt(3)"
      :  :  : "memory"); SB();
  STFQ(WS_FQ + iw * (4 * 4));
  STAMP(4);
  asm volatile("s_waitcnt vmcnt(1)"
      :  :  : "memory"); SB();
  { const unsigned v_ = vT + (k0 + 12 >= 14 ? k0 + 12 - 14 : k0 + 12) * 131072; TOUCH(v_, rs2, 0u, 0); }
  asm volatile("s_waitcnt vmcnt(1)"
      :  :  : "memory"); SB();
  { const unsigned v_ = vT + (k0 + 1 >= 14 ? k0 + 1 - 14 : k0 + 1) * 131072; TOUCH(v_, rs2, 0u, 0); }
  asm volatile("s_waitcnt vmcnt(1)"
      :  :  : "memory"); SB();
  { const unsigned v_ = vT + (k0 + 5 >= 14 ? k0 + 5 - 14 : k0 + 5) * 131072; TOUCH(v_, rs2, 0u, 0); }
  asm volatile("s_waitcnt vmcnt(1)"
      :  :  : "memory"); SB();
  { const unsigned v_ = vT + (k0 + 9 >= 14 ? k0 + 9 - 14 : k0 + 9) * 131072; TOUCH(v_, rs2, 0u, 0); }
  asm volatile("s_waitcnt vmcnt(1)"
      :  :  : "memory"); SB();
  { const unsigned v_ = vT + (k0 + 13 >= 14 ? k0 + 13 - 14 : k0 + 13) * 131072; TOUCH(v_, rs2, 0u, 0); }
  asm volatile("s_waitcnt vmcnt(1)"
      :  :  : "memory"); SB();
  { const unsigned v_ = vT + (k0 + 2 >= 14 ? k0 + 2 - 14 : k0 + 2) * 131072; TOUCH(v_, rs2, 0u, 0); }
  STAMP(3);
  asm volatile("s_add_u32 m0, %0, 0\n\ts_nop 0\n\tbuffer_load_dwordx4 %1, %2, %3 offen offset:0  lds\n\tbuffer_load_dwordx4 %4, %2, %3 offen offset:1024  lds\n\tbuffer_load_dwordx4 %5, %2, %3 offen offset:2048  lds\n\tbuffer_load_dwordx4 %6, %2, %3 offen offset:3072  lds\n\ts_add_u32 m0, %0, 4096\n\ts_nop 0\n\tbuffer_load_dwordx4 %1, %2, %7 offen offset:0  lds\n\tbuffer_load_dwordx4 %4, %2, %7 offen offset:1024  lds\n\tbuffer_load_dwordx4 %5, %2, %7 offen offset:2048  lds\n\tbuffer_load_dwordx4 %6, %2, %7 offen offset:3072  lds\n\ts_add_u32 m0, %8, 18432\n\ts_nop 0\n\tbuffer_load_dwordx4 %1, %2, %9 offen offset:0  lds\n\tbuffer_load_dwordx4 %4, %2, %9 offen offset:1024  lds\n\tbuffer_load_dwordx4 %5, %2, %9 offen offset:2048  lds\n\tbuffer_load_dwordx4 %6, %2, %9 offen offset:3072  lds\n\ts_add_u32 m0, %8, 34816\n\ts_nop 0\n\tbuffer_load_dwordx4 %1, %2, %10 offen offset:0  lds\n\tbuffer_load_dwordx4 %4, %2, %10 offen offset:1024  lds\n\tbuffer_load_dwordx4 %5, %2, %10 offen offset:2048  lds\n\tbuffer_load_dwordx4 %6, %2, %10 offen offset:3072  lds\n\ts_waitcnt vmcnt(12)"
      :  : "s"(ldsO), "v"(vW2[0]), "s"(rs2), "s"(sw[0]), "v"(vW2[1]), "v"(vW2[2]), "v"(vW2[3]), "s"(sw[1]), "s"(ldsS), "s"(sw[2]), "s"(sw[3]) : "memory", "scc"); SB();
  fw2[0][0] = tr8u(tO[0] + 0, 512);
  fw2[0][1] = tr8u(tO[1] + 0, 512);
  fw2[0][2] = tr8u(tO[2] + 0, 512);
  fw2[0][3] = tr8u(tO[3] + 0, 512);
  asm volatile("s_add_u32 m0, %0, 0\n\ts_nop 0\n\tbuffer_load_dwordx4 %1, %2, %3 offen offset:0  lds\n\tbuffer_load_dwordx4 %4, %2, %3 offen offset:1024  lds\n\tbuffer_load_dwordx4 %5, %2, %3 offen offset:2048  lds\n\tbuffer_load_dwordx4 %6, %2, %3 offen offset:3072  lds\n\ts_waitcnt vmcnt(12)"
      :  : "s"(ldsS), "v"(vW2[0]), "s"(rs2), "s"(sw[4]), "v"(vW2[1]), "v"(vW2[2]), "v"(vW2[3]) : "memory", "scc"); SB();
  fw2[1][0] = tr8u(tO[0] + 4096, 512);
  fw2[1][1] = tr8u(tO[1] + 4096, 512);
  fw2[1][2] = tr8u(tO[2] + 4096, 512);
  fw2[1][3] = tr8u(tO[3] + 4096, 512);
  WAITL(8);
  asm volatile("s_add_u32 m0, %0, 0\n\ts_nop 0\n\tbuffer_load_dwordx4 %1, %2, %3 offen offset:0  lds\n\tbuffer_load_dwordx4 %4, %2, %3 offen offset:1024  lds\n\tbuffer_load_dwordx4 %5, %2, %3 offen offset:2048  lds\n\tbuffer_load_dwordx4 %6, %2, %3 offen offset:3072  lds\n\ts_waitcnt vmcnt(12)"
      :  : "s"(ldsO), "v"(vW2[0]), "s"(rs2), "s"(sw[5]), "v"(vW2[1]), "v"(vW2[2]), "v"(vW2[3]) : "memory", "scc"); SB();
  fw2[2][0] = tr8u(tS[0] + 18432, 512);
  fw2[2][1] = tr8u(tS[1] + 18432, 512);
  fw2[2][2] = tr8u(tS[2] + 18432, 512);
  fw2[2][3] = tr8u(tS[3] + 18432, 512);
  WAITL(8);
  asm volatile("s_add_u32 m0, %0, 4096\n\ts_nop 0\n\tbuffer_load_dwordx4 %1, %2, %3 offen offset:0  lds\n\tbuffer_load_dwordx4 %4, %2, %3 offen offset:1024  lds\n\tbuffer_load_dwordx4 %5, %2, %3 offen offset:2048  lds\n\tbuffer_load_dwordx4 %6, %2, %3 offen offset:3072  lds\n\ts_waitcnt vmcnt(12)"
      :  : "s"(ldsO), "v"(vW2[0]), "s"(rs2), "s"(sw[6]), "v"(vW2[1]), "v"(vW2[2]), "v"(vW2[3]) : "memory", "scc"); SB();
  fw2[3][0] = tr8u(tS[0] + 34816, 512);
  fw2[3][1] = tr8u(tS[1] + 34816, 512);
  fw2[3][2] = tr8u(tS[2] + 34816, 512);
  fw2[3][3] = tr8u(tS[3] + 34816, 512);
  WAITL(8);
  asm volatile("s_add_u32 m0, %0, 18432\n\ts_nop 0\n\tbuffer_load_dwordx4 %1, %2, %3 offen offset:0  lds\n\tbuffer_load_dwordx4 %4, %2, %3 offen offset:1024  lds\n\tbuffer_load_dwordx4 %5, %2, %3 offen offset:2048  lds\n\tbuffer_load_dwordx4 %6, %2, %3 offen offset:3072  lds\n\ts_waitcnt vmcnt(12)"
      :  : "s"(ldsS), "v"(vW2[0]), "s"(rs2), "s"(sw[7]), "v"(vW2[1]), "v"(vW2[2]), "v"(vW2[3]) : "memory", "scc"); SB();
  fw2[4][0] = tr8u(tS[0] + 0, 512);
  fw2[4][1] = tr8u(tS[1] + 0, 512);
  fw2[4][2] = tr8u(tS[2] + 0, 512);
  fw2[4][3] = tr8u(tS[3] + 0, 512);
  WAITL(8);
  asm volatile("s_add_u32 m0, %0, 34816\n\ts_nop 0\n\tbuffer_load_dwordx4 %1, %2, %3 offen offset:0  lds\n\tbuffer_load_dwordx4 %4, %2, %3 offen offset:1024  lds\n\tbuffer_load_dwordx4 %5, %2, %3 offen offset:2048  lds\n\tbuffer_load_dwordx4 %6, %2, %3 offen offset:3072  lds\n\ts_waitcnt vmcnt(12)"
      :  : "s"(ldsS), "v"(vW2[0]), "s"(rs2), "s"(sw[8]), "v"(vW2[1]), "v"(vW2[2]), "v"(vW2[3]) : "memory", "scc"); SB();
  fw2[5][0] = tr8u(tO[0] + 0, 512);
  fw2[5][1] = tr8u(tO[1] + 0, 512);
  fw2[5][2] = tr8u(tO[2] + 0, 512);
  fw2[5][3] = tr8u(tO[3] + 0, 512);
  WAITL(8);
  asm volatile("s_add_u32 m0, %0, 0\n\ts_nop 0\n\tbuffer_load_dwordx4 %1, %2, %3 offen offset:0  lds\n\tbuffer_load_dwordx4 %4, %2, %3 offen offset:1024  lds\n\tbuffer_load_dwordx4 %5, %2, %3 offen offset:2048  lds\n\tbuffer_load_dwordx4 %6, %2, %3 offen offset:3072  lds\n\ts_waitcnt vmcnt(12)"
      :  : "s"(ldsS), "v"(vW2[0]), "s"(rs2), "s"(sw[9]), "v"(vW2[1]), "v"(vW2[2]), "v"(vW2[3]) : "memory", "scc"); SB();
  fw2[6][0] = tr8u(tO[0] + 4096, 512);
  fw2[6][1] = tr8u(tO[1] + 4096, 512);
  fw2[6][2] = tr8u(tO[2] + 4096, 512);
  fw2[6][3] = tr8u(tO[3] + 4096, 512);
  WAITL(8);
  asm volatile("s_add_u32 m0, %0, 0\n\ts_nop 0\n\tbuffer_load_dwordx4 %1, %2, %3 offen offset:0  lds\n\tbuffer_load_dwordx4 %4, %2, %3 offen offset:1024  lds\n\tbuffer_load_dwordx4 %5, %2, %3 offen offset:2048  lds\n\tbuffer_load_dwordx4 %6, %2, %3 offen offset:3072  lds\n\ts_waitcnt vmcnt(12)"
      :  : "s"(ldsO), "v"(vW2[0]), "s"(rs2), "s"(sw[10]), "v"(vW2[1]), "v"(vW2[2]), "v"(vW2[3]) : "memory", "scc"); SB();
  fw2[7][0] = tr8u(tS[0] + 18432, 512);
  fw2[7][1] = tr8u(tS[1] + 18432, 512);
  fw2[7][2] = tr8u(tS[2] + 18432, 512);
  fw2[7][3] = tr8u(tS[3] + 18432, 512);
  WAITL(0);
  asm volatile("s_add_u32 m0, %0, 18432\n\ts_nop 0\n\tbuffer_load_dwordx4 %1, %2, %3 offen offset:0  lds\n\tbuffer_load_dwordx4 %4, %2, %3 offen offset:1024  lds\n\tbuffer_load_dwordx4 %5, %2, %3 offen offset:2048  lds\n\tbuffer_load_dwordx4 %6, %2, %3 offen offset:3072  lds\n\ts_waitcnt vmcnt(12)"
      :  : "s"(ldsS), "v"(vW2[0]), "s"(rs2), "s"(sw[11]), "v"(vW2[1]), "v"(vW2[2]), "v"(vW2[3]) : "memory", "scc"); SB();
  fw2[8][0] = tr8u(tS[0] + 34816, 512);
  fw2[8][1] = tr8u(tS[1] + 34816, 512);
  fw2[8][2] = tr8u(tS[2] + 34816, 512);
  fw2[8][3] = tr8u(tS[3] + 34816, 512);
  WAITL(0);
  asm volatile("s_add_u32 m0, %0, 34816\n\ts_nop 0\n\tbuffer_load_dwordx4 %1, %2, %3 offen offset:0  lds\n\tbuffer_load_dwordx4 %4, %2, %3 offen offset:1024  lds\n\tbuffer_load_dwordx4 %5, %2, %3 offen offset:2048  lds\n\tbuffer_load_dwordx4 %6, %2, %3 offen offset:3072  lds\n\ts_waitcnt vmcnt(12)"
      :  : "s"(ldsS), "v"(vW2[0]), "s"(rs2), "s"(sw[12]), "v"(vW2[1]), "v"(vW2[2]), "v"(vW2[3]) : "memory", "scc"); SB();
  fw2[9][0] = tr8u(tS[0] + 0, 512);
  fw2[9][1] = tr8u(tS[1] + 0, 512);
  fw2[9][2] = tr8u(tS[2] + 0, 512);
  fw2[9][3] = tr8u(tS[3] + 0, 512);
  WAITL(0);
  asm volatile("s_add_u32 m0, %0, 0\n\ts_nop 0\n\tbuffer_load_dwordx4 %1, %2, %3 offen offset:0  lds\n\tbuffer_load_dwordx4 %4, %2, %3 offen offset:1024  lds\n\tbuffer_load_dwordx4 %5, %2, %3 offen offset:2048  lds\n\tbuffer_load_dwordx4 %6, %2, %3 offen offset:3072  lds\n\ts_waitcnt vmcnt(12)"
      :  : "s"(ldsS), "v"(vW2[0]), "s"(rs2), "s"(sw[13]), "v"(vW2[1]), "v"(vW2[2]), "v"(vW2[3]) : "memory", "scc"); SB();
  fw2[10][0] = tr8u(tO[0] + 0, 512);
  fw2[10][1] = tr8u(tO[1] + 0, 512);
  fw2[10][2] = tr8u(tO[2] + 0, 512);
  fw2[10][3] = tr8u(tO[3] + 0, 512);
  asm volatile("s_waitcnt vmcnt(8)"
      :  :  : "memory"); SB();
  fw2[11][0] = tr8u(tS[0] + 18432, 512);
  fw2[11][1] = tr8u(tS[1] + 18432, 512);
  fw2[11][2] = tr8u(tS[2] + 18432, 512);
  fw2[11][3] = tr8u(tS[3] + 18432, 512);
  asm volatile("s_waitcnt vmcnt(4)"
      :  :  : "memory"); SB();
  fw2[12][0] = tr8u(tS[0] + 34816, 512);
  fw2[12][1] = tr8u(tS[1] + 34816, 512);
  fw2[12][2] = tr8u(tS[2] + 34816, 512);
  fw2[12][3] = tr8u(tS[3] + 34816, 512);
  asm volatile("s_waitcnt vmcnt(0)"
      :  :  : "memory"); SB();
  fw2[13][0] = tr8u(tS[0] + 0, 512);
  fw2[13][1] = tr8u(tS[1] + 0, 512);
  fw2[13][2] = tr8u(tS[2] + 0, 512);
  fw2[13][3] = tr8u(tS[3] + 0, 512);
  poll0<16>(rsW, vFQ, nonce);
  asm volatile("s_add_u32 m0, %1, 8192\n\ts_add_u32 %0, %2, 8192"
      : "=&s"(sWt) : "s"(ldsP), "s"(sWo) : "memory", "scc"); SB();
  DMAW4(vWo, rsW, sWt);
  u32x4 rsX = srd(x, 2048u * 896u); OPQ(rsX);
  WAITL(8);
  asm volatile("s_add_u32 m0, %2, 34816\n\ts_nop 0\n\tbuffer_load_dwordx4 %3, %4, %5 offen offset:0  lds\n\tbuffer_load_dwordx4 %6, %4, %5 offen offset:1024  lds\n\tglobal_load_ushort %0, %7, %8 offset:0\n\ts_add_u32 m0, %9, 12288\n\ts_add_u32 %1, %10, 12288"
      : "=&v"(braw), "=&s"(sWt) : "s"(ldsS), "v"(vXa), "s"(rsX), "s"(0u), "v"(vXb), "v"(vB2), "s"(b2), "s"(ldsP), "s"(sWo) : "memory", "scc"); SB();
  DMAW2(vWo, rsW, sWt);
  asm volatile("s_add_u32 m0, %1, 0\n\ts_add_u32 %0, %2, 0"
      : "=&s"(sWt) : "s"(ldsO), "s"(sWo) : "memory", "scc"); SB();
  DMAW4(vWo, rsW, sWt);
  asm volatile("s_add_u32 m0, %1, 4096\n\ts_add_u32 %0, %2, 4096"
      : "=&s"(sWt) : "s"(ldsO), "s"(sWo) : "memory", "scc"); SB();
  DMAW4(vWo, rsW, sWt);
  STAMP(16);
  asm volatile("s_waitcnt vmcnt(0)"
      :  :  : "memory"); SB();
  BAR();
  STAMP(5);
  for (int k = 0; k < 0; ++k) __builtin_amdgcn_s_sleep(4);
  {
  unsigned fz[3], vZ0 = WS_FU + (w * 128 + (lane < 56 ? lane : 55)) * (4 * 4), vZ1 = vZ0 + 56 * (4 * 4), vZ2 = WS_FQ + lane * (4 * 4); OPV(vZ0); OPV(vZ1); OPV(vZ2);
  bf16x8 pu[14], fwo[56];
  f32x4 ac[2][4], as[4], py[4][4];
  float ye[4], ze[4], ge[16], xe[16], bias;
  u32x2 ov[4]; unsigned fo;
  u32x2 xv[4]; unsigned xta[4];
  if (w != xq) poll0<16>(rsW, vFU, nonce);
  BAR0();
  if (w == xq) poll0<16>(rsW, vFU, nonce);
  STAMP(17);
  asm volatile("buffer_load_dwordx4 %0, %10, %11, %12 offen offset:0 sc1\n\tbuffer_load_dwordx4 %1, %10, %11, %13 offen offset:0 sc1\n\tbuffer_load_dwordx4 %2, %10, %11, %14 offen offset:0 sc1\n\tbuffer_load_dwordx4 %3, %10, %11, %15 offen offset:0 sc1\n\tbuffer_load_dwordx4 %4, %10, %11, %16 offen offset:0 sc1\n\tbuffer_load_dwordx4 %5, %10, %11, %17 offen offset:0 sc1\n\tbuffer_load_dwordx4 %6, %10, %11, %18 offen offset:0 sc1\n\tbuffer_load_dwordx4 %7, %10, %11, %19 offen offset:0 sc1\n\tbuffer_load_dwordx4 %8, %10, %11, %20 offen offset:0 sc1\n\tbuffer_load_dwordx4 %9, %10, %11, %21 offen offset:0 sc1"
      : "=&v"(pu[0]), "=&v"(pu[1]), "=&v"(pu[2]), "=&v"(pu[3]), "=&v"(pu[4]), "=&v"(pu[5]), "=&v"(pu[6]), "=&v"(pu[7]), "=&v"(pu[8]), "=&v"(pu[9]) : "v"(vFj[0]), "s"(rsW), "s"(su[0]), "s"(su[1]), "s"(su[2]), "s"(su[3]), "s"(su[4]), "s"(su[5]), "s"(su[6]), "s"(su[7]), "s"(su[8]), "s"(su[9]) : "memory"); SB();
  STAMP(39);
  WAITVB(9);
  SB();
  asm volatile("v_mfma_f32_16x16x32_bf16 %0, %8, %9, 0\n\tbuffer_load_dwordx4 %1, %10, %11, %12 offen offset:0 sc1\n\tv_mfma_f32_16x16x32_bf16 %2, %8, %13, 0\n\tds_read_b128 %3, %14 offset:0\n\tv_mfma_f32_16x16x32_bf16 %4, %8, %15, 0\n\tbuffer_load_dwordx4 %5, %10, %11, %16 offen offset:0 sc1\n\tv_mfma_f32_16x16x32_bf16 %6, %8, %17, 0\n\tds_read_b128 %7, %14 offset:8192"
      : "=&v"(ac[0][0]), "=&v"(pu[10]), "=&v"(ac[0][1]), "=&a"(fwo[0]), "=&v"(ac[0][2]), "=&v"(pu[11]), "=&v"(ac[0][3]), "=&a"(fwo[1]) : "v"(pu[0]), "a"(fw2[0][0]), "v"(vFj[0]), "s"(rsW), "s"(su[10]), "a"(fw2[0][1]), "v"(ro), "a"(fw2[0][2]), "s"(su[11]), "a"(fw2[0][3]) : "memory"); SB();
  WAITVB(10);
  SB();
  asm volatile("v_mfma_f32_16x16x32_bf16 %0, %8, %9, %0\n\tbuffer_load_dwordx4 %1, %10, %11, %12 offen offset:0 sc1\n\tv_mfma_f32_16x16x32_bf16 %2, %8, %13, %2\n\tds_read_b128 %3, %14 offset:16384\n\tv_mfma_f32_16x16x32_bf16 %4, %8, %15, %4\n\tbuffer_load_dwordx4 %5, %10, %11, %16 offen offset:0 sc1\n\tv_mfma_f32_16x16x32_bf16 %6, %8, %17, %6\n\tds_read_b128 %7, %14 offset:1024"
      : "+v"(ac[0][0]), "=&v"(pu[12]), "+v"(ac[0][1]), "=&a"(fwo[2]), "+v"(ac[0][2]), "=&v"(pu[13]), "+v"(ac[0][3]), "=&a"(fwo[3]) : "v"(pu[1]), "a"(fw2[1][0]), "v"(vFj[0]), "s"(rsW), "s"(su[12]), "a"(fw2[1][1]), "v"(ro), "a"(fw2[1][2]), "s"(su[13]), "a"(fw2[1][3]) : "memory"); SB();
  WAITVB(11);
  SB();
  asm volatile("v_mfma_f32_16x16x32_bf16 %0, %3, %4, %0\n\tbuffer_load_dwordx4 %1, %5, %6, %7 offen offset:0 sc1\n\tv_mfma_f32_16x16x32_bf16 %2, %3, %8, %2"
      : "+v"(ac[0][0]), "=&v"(pu[0]), "+v"(ac[0][1]) : "v"(pu[2]), "a"(fw2[2][0]), "v"(vFj[1]), "s"(rsW), "s"(su[0]), "a"(fw2[2][1]) : "memory"); SB();
  WAITVB(25);
  asm volatile("v_lshlrev_b32 %0, 16, %5\n\tv_mfma_f32_16x16x32_bf16 %1, %6, %7, %1\n\tbuffer_load_dwordx4 %2, %8, %9, %10 offen offset:0 sc1\n\tv_mfma_f32_16x16x32_bf16 %3, %6, %11, %3\n\tv_xor_b32 %4, 0, %12"
      : "=&v"(bias), "+v"(ac[0][2]), "=&v"(pu[1]), "+v"(ac[0][3]), "=&v"(xta[0]) : "v"(braw), "v"(pu[2]), "a"(fw2[2][2]), "v"(vFj[1]), "s"(rsW), "s"(su[1]), "a"(fw2[2][3]), "v"(xs) : "memory"); SB();
  WAITVB(12);
  SB();
  asm volatile("v_mfma_f32_16x16x32_bf16 %0, %10, %11, %0\n\tbuffer_load_dwordx4 %1, %12, %13, %14 offen offset:0 sc1\n\tv_mfma_f32_16x16x32_bf16 %2, %10, %15, %2\n\tv_xor_b32 %3, 32, %16\n\tv_xor_b32 %4, 64, %17\n\tv_mfma_f32_16x16x32_bf16 %5, %10, %18, %5\n\tds_read_b64 %6, %19 offset:0\n\tds_read_b64 %7, %3 offset:0\n\tv_mfma_f32_16x16x32_bf16 %8, %10, %20, %8\n\tds_read_b64 %9, %4 offset:0"
      : "+v"(ac[0][0]), "=&v"(pu[2]), "+v"(ac[0][1]), "=&v"(xta[1]), "=&v"(xta[2]), "+v"(ac[0][2]), "=&v"(xv[0]), "=&v"(xv[1]), "+v"(ac[0][3]), "=&v"(xv[2]) : "v"(pu[3]), "a"(fw2[3][0]), "v"(vFj[1]), "s"(rsW), "s"(su[2]), "a"(fw2[3][1]), "v"(xs), "v"(xt), "a"(fw2[3][2]), "v"(xta[0]), "a"(fw2[3][3]) : "memory"); SB();
  WAITVB(12);
  SB();
  asm volatile("v_mfma_f32_16x16x32_bf16 %0, %7, %8, %0\n\tbuffer_load_dwordx4 %1, %9, %10, %11 offen offset:0 sc1\n\tv_mfma_f32_16x16x32_bf16 %2, %7, %12, %2\n\tds_read_b128 %3, %13 offset:9216\n\tv_mfma_f32_16x16x32_bf16 %4, %7, %14, %4\n\tv_mfma_f32_16x16x32_bf16 %5, %7, %15, %5\n\tds_read_b128 %6, %13 offset:17408"
      : "+v"(ac[0][0]), "=&v"(pu[3]), "+v"(ac[0][1]), "=&a"(fwo[4]), "+v"(ac[0][2]), "+v"(ac[0][3]), "=&a"(fwo[5]) : "v"(pu[4]), "a"(fw2[4][0]), "v"(vFj[1]), "s"(rsW), "s"(su[3]), "a"(fw2[4][1]), "v"(ro), "a"(fw2[4][2]), "a"(fw2[4][3]) : "memory"); SB();
  WAITVB(12);
  SB();
  asm volatile("v_mfma_f32_16x16x32_bf16 %0, %3, %4, %0\n\tbuffer_load_dwordx4 %1, %5, %6, %7 offen offset:0 sc1\n\tv_mfma_f32_16x16x32_bf16 %2, %3, %8, %2"
      : "+v"(ac[0][0]), "=&v"(pu[4]), "+v"(ac[0][1]) : "v"(pu[5]), "a"(fw2[5][0]), "v"(vFj[1]), "s"(rsW), "s"(su[4]), "a"(fw2[5][1]) : "memory"); SB();
  WAITL(2);
  asm volatile("v_lshlrev_b32 %0, 16, %6\n\tv_mfma_f32_16x16x32_bf16 %1, %7, %8, %1\n\tv_and_b32 %2, 0xffff0000, %6\n\tv_lshlrev_b32 %3, 16, %9\n\tv_mfma_f32_16x16x32_bf16 %4, %7, %10, %4\n\tv_and_b32 %5, 0xffff0000, %9"
      : "=&v"(xe[0]), "+v"(ac[0][2]), "=&v"(xe[1]), "=&v"(xe[2]), "+v"(ac[0][3]), "=&v"(xe[3]) : "v"(xv[0][0]), "v"(pu[5]), "a"(fw2[5][2]), "v"(xv[0][1]), "a"(fw2[5][3])); SB();
  WAITVB(12);
  SB();
  asm volatile("v_mfma_f32_16x16x32_bf16 %0, %10, %11, %0\n\tbuffer_load_dwordx4 %1, %12, %13, %14 offen offset:0 sc1\n\tv_mfma_f32_16x16x32_bf16 %2, %10, %15, %2\n\tv_lshlrev_b32 %3, 16, %16\n\tv_and_b32 %4, 0xffff0000, %16\n\tv_mfma_f32_16x16x32_bf16 %5, %10, %17, %5\n\tv_lshlrev_b32 %6, 16, %18\n\tv_and_b32 %7, 0xffff0000, %18\n\tv_mfma_f32_16x16x32_bf16 %8, %10, %19, %8\n\tv_lshlrev_b32 %9, 16, %20"
      : "+v"(ac[0][0]), "=&v"(pu[5]), "+v"(ac[0][1]), "=&v"(xe[4]), "=&v"(xe[5]), "+v"(ac[0][2]), "=&v"(xe[6]), "=&v"(xe[7]), "+v"(ac[0][3]), "=&v"(xe[8]) : "v"(pu[6]), "a"(fw2[6][0]), "v"(vFj[1]), "s"(rsW), "s"(su[5]), "a"(fw2[6][1]), "v"(xv[1][0]), "a"(fw2[6][2]), "v"(xv[1][1]), "a"(fw2[6][3]), "v"(xv[2][0]) : "memory"); SB();
  WAITVB(12);
  SB();
  asm volatile("v_mfma_f32_16x16x32_bf16 %0, %9, %10, %0\n\tbuffer_load_dwordx4 %1, %11, %12, %13 offen offset:0 sc1\n\tv_mfma_f32_16x16x32_bf16 %2, %9, %14, %2\n\tv_and_b32 %3, 0xffff0000, %15\n\tv_lshlrev_b32 %4, 16, %16\n\tv_mfma_f32_16x16x32_bf16 %5, %9, %17, %5\n\tds_read_b128 %6, %18 offset:2048\n\tv_and_b32 %7, 0xffff0000, %16\n\tv_mfma_f32_16x16x32_bf16 %8, %9, %19, %8"
      : "+v"(ac[0][0]), "=&v"(pu[6]), "+v"(ac[0][1]), "=&v"(xe[9]), "=&v"(xe[10]), "+v"(ac[0][2]), "=&a"(fwo[6]), "=&v"(xe[11]), "+v"(ac[0][3]) : "v"(pu[7]), "a"(fw2[7][0]), "v"(vFj[1]), "s"(rsW), "s"(su[6]), "a"(fw2[7][1]), "v"(xv[2][0]), "v"(xv[2][1]), "a"(fw2[7][2]), "v"(ro), "a"(fw2[7][3]) : "memory"); SB();
  WAITVB(12);
  SB();
  asm volatile("v_mfma_f32_16x16x32_bf16 %0, %7, %8, %0\n\tbuffer_load_dwordx4 %1, %9, %10, %11 offen offset:0 sc1\n\tv_mfma_f32_16x16x32_bf16 %2, %7, %12, %2\n\tds_read_b128 %3, %13 offset:10240\n\tv_mfma_f32_16x16x32_bf16 %4, %7, %14, %4\n\tv_mfma_f32_16x16x32_bf16 %5, %7, %15, %5\n\tds_read_b128 %6, %13 offset:18432"
      : "+v"(ac[0][0]), "=&v"(pu[7]), "+v"(ac[0][1]), "=&a"(fwo[7]), "+v"(ac[0][2]), "+v"(ac[0][3]), "=&a"(fwo[8]) : "v"(pu[8]), "a"(fw2[8][0]), "v"(vFj[1]), "s"(rsW), "s"(su[7]), "a"(fw2[8][1]), "v"(ro), "a"(fw2[8][2]), "a"(fw2[8][3]) : "memory"); SB();
  WAITVB(12);
  SB();
  asm volatile("v_mfma_f32_16x16x32_bf16 %0, %7, %8, %0\n\tbuffer_load_dwordx4 %1, %9, %10, %11 offen offset:0 sc1\n\tv_mfma_f32_16x16x32_bf16 %2, %7, %12, %2\n\tds_read_b128 %3, %13 offset:3072\n\tv_mfma_f32_16x16x32_bf16 %4, %7, %14, %4\n\tv_mfma_f32_16x16x32_bf16 %5, %7, %15, %5\n\tds_read_b128 %6, %13 offset:11264"
      : "+v"(ac[0][0]), "=&v"(pu[8]), "+v"(ac[0][1]), "=&a"(fwo[9]), "+v"(ac[0][2]), "+v"(ac[0][3]), "=&a"(fwo[10]) : "v"(pu[9]), "v"(fw2[9][0]), "v"(vFj[1]), "s"(rsW), "s"(su[8]), "v"(fw2[9][1]), "v"(ro), "v"(fw2[9][2]), "v"(fw2[9][3]) : "memory"); SB();
  WAITVB(12);
  SB();
  asm volatile("v_mfma_f32_16x16x32_bf16 %0, %7, %8, %0\n\tbuffer_load_dwordx4 %1, %9, %10, %11 offen offset:0 sc1\n\tv_mfma_f32_16x16x32_bf16 %2, %7, %12, %2\n\tds_read_b128 %3, %13 offset:19456\n\tv_mfma_f32_16x16x32_bf16 %4, %7, %14, %4\n\tv_mfma_f32_16x16x32_bf16 %5, %7, %15, %5\n\tds_read_b128 %6, %13 offset:4096"
      : "+v"(ac[0][0]), "=&v"(pu[9]), "+v"(ac[0][1]), "=&a"(fwo[11]), "+v"(ac[0][2]), "+v"(ac[0][3]), "=&a"(fwo[12]) : "v"(pu[10]), "v"(fw2[10][0]), "v"(vFj[1]), "s"(rsW), "s"(su[9]), "v"(fw2[10][1]), "v"(ro), "v"(fw2[10][2]), "v"(fw2[10][3]) : "memory"); SB();
  WAITVB(12);
  SB();
  asm volatile("v_mfma_f32_16x16x32_bf16 %0, %7, %8, %0\n\tbuffer_load_dwordx4 %1, %9, %10, %11 offen offset:0 sc1\n\tv_mfma_f32_16x16x32_bf16 %2, %7, %12, %2\n\tds_read_b128 %3, %13 offset:12288\n\tv_mfma_f32_16x16x32_bf16 %4, %7, %14, %4\n\tv_mfma_f32_16x16x32_bf16 %5, %7, %15, %5\n\tds_read_b128 %6, %13 offset:20480"
      : "+v"(ac[0][0]), "=&v"(pu[10]), "+v"(ac[0][1]), "=&a"(fwo[13]), "+v"(ac[0][2]), "+v"(ac[0][3]), "=&a"(fwo[14]) : "v"(pu[11]), "v"(fw2[11][0]), "v"(vFj[1]), "s"(rsW), "s"(su[10]), "v"(fw2[11][1]), "v"(ro), "v"(fw2[11][2]), "v"(fw2[11][3]) : "memory"); SB();
  WAITVB(12);
  SB();
  asm volatile("v_mfma_f32_16x16x32_bf16 %0, %5, %6, %0\n\tbuffer_load_dwordx4 %1, %7, %8, %9 offen offset:0 sc1\n\tv_mfma_f32_16x16x32_bf16 %2, %5, %10, %2\n\tds_read_b128 %3, %11 offset:5120\n\tv_mfma_f32_16x16x32_bf16 %4, %5, %12, %4"
      : "+v"(ac[0][0]), "=&v"(pu[11]), "+v"(ac[0][1]), "=&a"(fwo[15]), "+v"(ac[0][2]) : "v"(pu[12]), "v"(fw2[12][0]), "v"(vFj[1]), "s"(rsW), "s"(su[11]), "v"(fw2[12][1]), "v"(ro), "v"(fw2[12][2]) : "memory"); SB();
  u32x4 rsO_ = srd(out, 2048u * 896u); OPQ(rsO_);
  SB();
  asm volatile("v_mfma_f32_16x16x32_bf16 %0, %2, %3, %0\n\tds_read_b128 %1, %4 offset:13312"
      : "+v"(ac[0][3]), "=&a"(fwo[16]) : "v"(pu[12]), "v"(fw2[12][3]), "v"(ro) : "memory"); SB();
  WAITVB(12);
  SB();
  asm volatile("v_mfma_f32_16x16x32_bf16 %0, %7, %8, %0\n\tbuffer_load_dwordx4 %1, %9, %10, %11 offen offset:0 sc1\n\tv_mfma_f32_16x16x32_bf16 %2, %7, %12, %2\n\tds_read_b128 %3, %13 offset:21504\n\tv_mfma_f32_16x16x32_bf16 %4, %7, %14, %4\n\tv_mfma_f32_16x16x32_bf16 %5, %7, %15, %5\n\tds_read_b128 %6, %13 offset:6144"
      : "+v"(ac[0][0]), "=&v"(pu[12]), "+v"(ac[0][1]), "=&a"(fwo[17]), "+v"(ac[0][2]), "+v"(ac[0][3]), "=&a"(fwo[18]) : "v"(pu[13]), "v"(fw2[13][0]), "v"(vFj[1]), "s"(rsW), "s"(su[12]), "v"(fw2[13][1]), "v"(ro), "v"(fw2[13][2]), "v"(fw2[13][3]) : "memory"); SB();
  STAMP(40);
  WAITVB(12);
  SB();
  asm volatile("v_mfma_f32_16x16x32_bf16 %0, %7, %8, 0\n\tbuffer_load_dwordx4 %1, %9, %10, %11 offen offset:0 sc1\n\tv_mfma_f32_16x16x32_bf16 %2, %7, %12, 0\n\tbuffer_load_dword %3, %13, %10, 0 offen sc1\n\tv_mfma_f32_16x16x32_bf16 %4, %7, %14, 0\n\tds_read_b128 %5, %15 offset:14336\n\tv_mfma_f32_16x16x32_bf16 %6, %7, %16, 0"
      : "=&v"(ac[1][0]), "=&v"(pu[13]), "=&v"(ac[1][1]), "=&v"(fo), "=&v"(ac[1][2]), "=&a"(fwo[19]), "=&v"(ac[1][3]) : "v"(pu[0]), "a"(fw2[0][0]), "v"(vFj[1]), "s"(rsW), "s"(su[13]), "a"(fw2[0][1]), "v"(vFO), "a"(fw2[0][2]), "v"(ro), "a"(fw2[0][3]) : "memory"); SB();
  WAITVB(13);
  SB();
  asm volatile("v_mfma_f32_16x16x32_bf16 %0, %5, %6, %0\n\tbuffer_load_dwordx4 %1, %7, %8, %9 offen offset:0 sc1\n\tv_mfma_f32_16x16x32_bf16 %2, %5, %10, %2\n\tds_write_b128 %11, %12 offset:0\n\tv_mfma_f32_16x16x32_bf16 %3, %5, %13, %3\n\tv_mfma_f32_16x16x32_bf16 %4, %5, %14, %4"
      : "+v"(ac[1][0]), "=&v"(pu[0]), "+v"(ac[1][1]), "+v"(ac[1][2]), "+v"(ac[1][3]) : "v"(pu[1]), "a"(fw2[1][0]), "v"(vFj[2]), "s"(rsW), "s"(su[0]), "a"(fw2[1][1]), "v"(rwd), "v"(ac[0][0]), "a"(fw2[1][2]), "a"(fw2[1][3]) : "memory"); SB();
  WAITVB(13);
  SB();
  asm volatile("v_mfma_f32_16x16x32_bf16 %0, %5, %6, %0\n\tbuffer_load_dwordx4 %1, %7, %8, %9 offen offset:0 sc1\n\tv_mfma_f32_16x16x32_bf16 %2, %5, %10, %2\n\tds_write_b128 %11, %12 offset:1024\n\tv_mfma_f32_16x16x32_bf16 %3, %5, %13, %3\n\tv_mfma_f32_16x16x32_bf16 %4, %5, %14, %4"
      : "+v"(ac[1][0]), "=&v"(pu[1]), "+v"(ac[1][1]), "+v"(ac[1][2]), "+v"(ac[1][3]) : "v"(pu[2]), "a"(fw2[2][0]), "v"(vFj[2]), "s"(rsW), "s"(su[1]), "a"(fw2[2][1]), "v"(rwd), "v"(ac[0][1]), "a"(fw2[2][2]), "a"(fw2[2][3]) : "memory"); SB();
  WAITVB(13);
  SB();
  asm volatile("v_mfma_f32_16x16x32_bf16 %0, %5, %6, %0\n\tbuffer_load_dwordx4 %1, %7, %8, %9 offen offset:0 sc1\n\tv_mfma_f32_16x16x32_bf16 %2, %5, %10, %2\n\tds_write_b128 %11, %12 offset:2048\n\tv_mfma_f32_16x16x32_bf16 %3, %5, %13, %3\n\tv_mfma_f32_16x16x32_bf16 %4, %5, %14, %4"
      : "+v"(ac[1][0]), "=&v"(pu[2]), "+v"(ac[1][1]), "+v"(ac[1][2]), "+v"(ac[1][3]) : "v"(pu[3]), "a"(fw2[3][0]), "v"(vFj[2]), "s"(rsW), "s"(su[2]), "a"(fw2[3][1]), "v"(rwd), "v"(ac[0][2]), "a"(fw2[3][2]), "a"(fw2[3][3]) : "memory"); SB();
  WAITVB(13);
  SB();
  asm volatile("v_mfma_f32_16x16x32_bf16 %0, %5, %6, %0\n\tbuffer_load_dwordx4 %1, %7, %8, %9 offen offset:0 sc1\n\tv_mfma_f32_16x16x32_bf16 %2, %5, %10, %2\n\tds_write_b128 %11, %12 offset:3072\n\tv_mfma_f32_16x16x32_bf16 %3, %5, %13, %3\n\tv_mfma_f32_16x16x32_bf16 %4, %5, %14, %4"
      : "+v"(ac[1][0]), "=&v"(pu[3]), "+v"(ac[1][1]), "+v"(ac[1][2]), "+v"(ac[1][3]) : "v"(pu[4]), "a"(fw2[4][0]), "v"(vFj[2]), "s"(rsW), "s"(su[3]), "a"(fw2[4][1]), "v"(rwd), "v"(ac[0][3]), "a"(fw2[4][2]), "a"(fw2[4][3]) : "memory"); SB();
  WAITVB(13);
  SB();
  asm volatile("v_mfma_f32_16x16x32_bf16 %0, %3, %4, %0\n\tbuffer_load_dwordx4 %1, %5, %6, %7 offen offset:0 sc1\n\tv_mfma_f32_16x16x32_bf16 %2, %3, %8, %2"
      : "+v"(ac[1][0]), "=&v"(pu[4]), "+v"(ac[1][1]) : "v"(pu[5]), "a"(fw2[5][0]), "v"(vFj[2]), "s"(rsW), "s"(su[4]), "a"(fw2[5][1]) : "memory"); SB();
  WAITL(0);
  BAR0();
  SB();
  asm volatile("v_mfma_f32_16x16x32_bf16 %0, %5, %6, %0\n\tds_read_b128 %1, %7 offset:0\n\tds_read_b128 %2, %7 offset:4096\n\tv_mfma_f32_16x16x32_bf16 %3, %5, %8, %3\n\tds_read_b128 %4, %7 offset:8192"
      : "+v"(ac[1][2]), "=&v"(py[0][0]), "=&v"(py[0][1]), "+v"(ac[1][3]), "=&v"(py[0][2]) : "v"(pu[5]), "a"(fw2[5][2]), "v"(rr), "a"(fw2[5][3]) : "memory"); SB();
  WAITVB(13);
  SB();
  asm volatile("v_mfma_f32_16x16x32_bf16 %0, %9, %10, %0\n\tbuffer_load_dwordx4 %1, %11, %12, %13 offen offset:0 sc1\n\tv_mfma_f32_16x16x32_bf16 %2, %9, %14, %2\n\tbuffer_load_dword %3, %15, %12, 0 offen sc1\n\tbuffer_load_dword %4, %16, %12, 0 offen sc1\n\tbuffer_load_dword %5, %17, %12, 0 offen sc1\n\tv_mfma_f32_16x16x32_bf16 %6, %9, %18, %6\n\tds_read_b128 %7, %19 offset:12288\n\tv_mfma_f32_16x16x32_bf16 %8, %9, %20, %8"
      : "+v"(ac[1][0]), "=&v"(pu[5]), "+v"(ac[1][1]), "=&v"(fz[0]), "=&v"(fz[1]), "=&v"(fz[2]), "+v"(ac[1][2]), "=&v"(py[0][3]), "+v"(ac[1][3]) : "v"(pu[6]), "a"(fw2[6][0]), "v"(vFj[2]), "s"(rsW), "s"(su[5]), "a"(fw2[6][1]), "v"(vZ0), "v"(vZ1), "v"(vZ2), "a"(fw2[6][2]), "v"(rr), "a"(fw2[6][3]) : "memory"); SB();
  WAITVB(16);
  SB();
  asm volatile("v_mfma_f32_16x16x32_bf16 %0, %5, %6, %0\n\tbuffer_load_dwordx4 %1, %7, %8, %9 offen offset:0 sc1\n\tv_mfma_f32_16x16x32_bf16 %2, %5, %10, %2\n\tv_mfma_f32_16x16x32_bf16 %3, %5, %11, %3\n\tv_mfma_f32_16x16x32_bf16 %4, %5, %12, %4"
      : "+v"(ac[1][0]), "=&v"(pu[6]), "+v"(ac[1][1]), "+v"(ac[1][2]), "+v"(ac[1][3]) : "v"(pu[7]), "a"(fw2[7][0]), "v"(vFj[2]), "s"(rsW), "s"(su[6]), "a"(fw2[7][1]), "a"(fw2[7][2]), "a"(fw2[7][3]) : "memory"); SB();
  WAITL(0);
  SB();
  WAITVB(16);
  SB();
  asm volatile("v_mfma_f32_16x16x32_bf16 %0, %10, %11, %0\n\tbuffer_load_dwordx4 %1, %12, %13, %14 offen offset:0 sc1\n\tv_mfma_f32_16x16x32_bf16 %2, %10, %15, %2\n\tv_add_f32 %3, %16, %17\n\tv_add_f32 %4, %18, %17\n\tv_mfma_f32_16x16x32_bf16 %5, %10, %19, %5\n\tv_add_f32 %6, %20, %17\n\tv_add_f32 %7, %21, %17\n\tv_mfma_f32_16x16x32_bf16 %8, %10, %22, %8\n\tv_add_f32 %9, %23, %24"
      : "+v"(ac[1][0]), "=&v"(pu[7]), "+v"(ac[1][1]), "=&v"(ye[0]), "=&v"(ye[1]), "+v"(ac[1][2]), "=&v"(ye[2]), "=&v"(ye[3]), "+v"(ac[1][3]), "=&v"(ze[0]) : "v"(pu[8]), "a"(fw2[8][0]), "v"(vFj[2]), "s"(rsW), "s"(su[7]), "a"(fw2[8][1]), "v"(py[0][0][0]), "v"(bias), "v"(py[0][0][1]), "a"(fw2[8][2]), "v"(py[0][0][2]), "v"(py[0][0][3]), "a"(fw2[8][3]), "v"(py[0][1][0]), "v"(py[0][2][0]) : "memory"); SB();
  WAITVB(16);
  SB();
  asm volatile("v_mfma_f32_16x16x32_bf16 %0, %9, %10, %0\n\tbuffer_load_dwordx4 %1, %11, %12, %13 offen offset:0 sc1\n\tv_mfma_f32_16x16x32_bf16 %2, %9, %14, %2\n\tv_add_f32 %3, %15, %16\n\tv_add_f32 %4, %17, %18\n\tv_mfma_f32_16x16x32_bf16 %5, %9, %19, %5\n\tv_add_f32 %6, %20, %21\n\tv_add_f32 %7, %7, %22\n\tv_mfma_f32_16x16x32_bf16 %8, %9, %23, %8"
      : "+v"(ac[1][0]), "=&v"(pu[8]), "+v"(ac[1][1]), "=&v"(ze[1]), "=&v"(ze[2]), "+v"(ac[1][2]), "=&v"(ze[3]), "+v"(ye[0]), "+v"(ac[1][3]) : "v"(pu[9]), "v"(fw2[9][0]), "v"(vFj[2]), "s"(rsW), "s"(su[8]), "v"(fw2[9][1]), "v"(py[0][1][1]), "v"(py[0][2][1]), "v"(py[0][1][2]), "v"(py[0][2][2]), "v"(fw2[9][2]), "v"(py[0][1][3]), "v"(py[0][2][3]), "v"(py[0][3][0]), "v"(fw2[9][3]) : "memory"); SB();
  asm volatile("v_add_f32 %0, %0, %1"
      : "+v"(ye[1]) : "v"(py[0][3][1])); SB();
  WAITVB(16);
  SB();
  asm volatile("v_mfma_f32_16x16x32_bf16 %0, %9, %10, %0\n\tbuffer_load_dwordx4 %1, %11, %12, %13 offen offset:0 sc1\n\tv_mfma_f32_16x16x32_bf16 %2, %9, %14, %2\n\tv_add_f32 %3, %3, %15\n\tv_add_f32 %4, %4, %16\n\tv_mfma_f32_16x16x32_bf16 %5, %9, %17, %5\n\tv_add_f32 %6, %6, %18\n\tv_add_f32 %7, %7, %19\n\tv_mfma_f32_16x16x32_bf16 %8, %9, %20, %8\n\tv_add_f32 %3, %3, %21"
      : "+v"(ac[1][0]), "=&v"(pu[9]), "+v"(ac[1][1]), "+v"(ye[2]), "+v"(ye[3]), "+v"(ac[1][2]), "+v"(ye[0]), "+v"(ye[1]), "+v"(ac[1][3]) : "v"(pu[10]), "v"(fw2[10][0]), "v"(vFj[2]), "s"(rsW), "s"(su[9]), "v"(fw2[10][1]), "v"(py[0][3][2]), "v"(py[0][3][3]), "v"(fw2[10][2]), "v"(ze[0]), "v"(ze[1]), "v"(fw2[10][3]), "v"(ze[2]) : "memory"); SB();
  WAITVB(16);
  SB();
  asm volatile("v_mfma_f32_16x16x32_bf16 %0, %10, %11, %0\n\tbuffer_load_dwordx4 %1, %12, %13, %14 offen offset:0 sc1\n\tv_mfma_f32_16x16x32_bf16 %2, %10, %15, %2\n\tv_add_f32 %3, %3, %4\n\tv_mul_f32 %5, 0xbfb8aa3b, %16\n\tv_mfma_f32_16x16x32_bf16 %6, %10, %17, %6\n\tv_mul_f32 %7, 0xbfb8aa3b, %18\n\tv_mul_f32 %8, 0xbfb8aa3b, %19\n\tv_mfma_f32_16x16x32_bf16 %9, %10, %20, %9\n\tv_mul_f32 %4, 0xbfb8aa3b, %3"
      : "+v"(ac[1][0]), "=&v"(pu[10]), "+v"(ac[1][1]), "+v"(ye[3]), "+v"(ze[3]), "=&v"(ze[0]), "+v"(ac[1][2]), "=&v"(ze[1]), "=&v"(ze[2]), "+v"(ac[1][3]) : "v"(pu[11]), "v"(fw2[11][0]), "v"(vFj[2]), "s"(rsW), "s"(su[10]), "v"(fw2[11][1]), "v"(ye[0]), "v"(fw2[11][2]), "v"(ye[1]), "v"(ye[2]), "v"(fw2[11][3]) : "memory"); SB();
  WAITVB(16);
  SB();
  asm volatile("v_mfma_f32_16x16x32_bf16 %0, %7, %8, %0\n\tbuffer_load_dwordx4 %1, %9, %10, %11 offen offset:0 sc1\n\tv_mfma_f32_16x16x32_bf16 %2, %7, %12, %2\n\tv_exp_f32 %3, %3\n\tv_mfma_f32_16x16x32_bf16 %4, %7, %13, %4\n\tv_exp_f32 %5, %5\n\tv_mfma_f32_16x16x32_bf16 %6, %7, %14, %6"
      : "+v"(ac[1][0]), "=&v"(pu[11]), "+v"(ac[1][1]), "+v"(ze[0]), "+v"(ac[1][2]), "+v"(ze[1]), "+v"(ac[1][3]) : "v"(pu[12]), "v"(fw2[12][0]), "v"(vFj[2]), "s"(rsW), "s"(su[11]), "v"(fw2[12][1]), "v"(fw2[12][2]), "v"(fw2[12][3]) : "memory"); SB();
  WAITVB(16);
  SB();
  asm volatile("v_mfma_f32_16x16x32_bf16 %0, %9, %10, %0\n\tbuffer_load_dwordx4 %1, %11, %12, %13 offen offset:0 sc1\n\tv_mfma_f32_16x16x32_bf16 %2, %9, %14, %2\n\tv_exp_f32 %3, %3\n\tv_mfma_f32_16x16x32_bf16 %4, %9, %15, %4\n\tv_exp_f32 %5, %5\n\tv_mfma_f32_16x16x32_bf16 %6, %9, %16, %6\n\tv_add_f32 %7, 1.0, %7\n\tv_add_f32 %8, 1.0, %8"
      : "+v"(ac[1][0]), "=&v"(pu[12]), "+v"(ac[1][1]), "+v"(ze[2]), "+v"(ac[1][2]), "+v"(ze[3]), "+v"(ac[1][3]), "+v"(ze[0]), "+v"(ze[1]) : "v"(pu[13]), "v"(fw2[13][0]), "v"(vFj[2]), "s"(rsW), "s"(su[12]), "v"(fw2[13][1]), "v"(fw2[13][2]), "v"(fw2[13][3]) : "memory"); SB();
  STAMP(41);
  WAITVB(15);
  SB();
  asm volatile("v_mfma_f32_16x16x32_bf16 %0, %8, %9, 0\n\tbuffer_load_dwordx4 %1, %10, %11, %12 offen offset:0 sc1\n\tv_mfma_f32_16x16x32_bf16 %2, %8, %13, 0\n\tv_add_f32 %3, 1.0, %3\n\tv_add_f32 %4, 1.0, %4\n\tv_mfma_f32_16x16x32_bf16 %5, %8, %14, 0\n\tv_rcp_f32 %6, %6\n\tv_mfma_f32_16x16x32_bf16 %7, %8, %15, 0"
      : "=&v"(ac[0][0]), "=&v"(pu[13]), "=&v"(ac[0][1]), "+v"(ze[2]), "+v"(ze[3]), "=&v"(ac[0][2]), "+v"(ze[0]), "=&v"(ac[0][3]) : "v"(pu[0]), "a"(fw2[0][0]), "v"(vFj[2]), "s"(rsW), "s"(su[13]), "a"(fw2[0][1]), "a"(fw2[0][2]), "a"(fw2[0][3]) : "memory"); SB();
  WAITVB(15);
  SB();
  asm volatile("v_mfma_f32_16x16x32_bf16 %0, %1, %2, %0"
      : "+v"(ac[0][0]) : "v"(pu[1]), "a"(fw2[1][0])); SB();
  WAITV1(17, fo);
  if (__builtin_expect(__ballot(fo != nonce) != 0ull, 0)) poll1<4>(rsW, vFO, nonce);
  STAMP(18);
  asm volatile("buffer_load_dwordx4 %0, %6, %7, %8 offen offset:0 sc1\n\tv_mfma_f32_16x16x32_bf16 %1, %9, %10, %1\n\tds_write_b128 %11, %12 offset:18432\n\tv_mfma_f32_16x16x32_bf16 %2, %9, %13, %2\n\tv_rcp_f32 %3, %3\n\tv_mfma_f32_16x16x32_bf16 %4, %9, %14, %4\n\tds_read_b128 %5, %15 offset:22528"
      : "=&v"(pu[0]), "+v"(ac[0][1]), "+v"(ac[0][2]), "+v"(ze[1]), "+v"(ac[0][3]), "=&a"(fwo[20]) : "v"(vF), "s"(rsW), "s"(so0), "v"(pu[1]), "a"(fw2[1][1]), "v"(rwd), "v"(ac[1][0]), "a"(fw2[1][2]), "a"(fw2[1][3]), "v"(ro) : "memory"); SB();
  WAITVB(15);
  SB();
  asm volatile("v_mfma_f32_16x16x32_bf16 %0, %7, %8, %0\n\tbuffer_load_dwordx4 %1, %9, %10, %11 offen offset:128 sc1\n\tv_mfma_f32_16x16x32_bf16 %2, %7, %12, %2\n\tds_write_b128 %13, %14 offset:19456\n\tv_mfma_f32_16x16x32_bf16 %3, %7, %15, %3\n\tv_rcp_f32 %4, %4\n\tv_mfma_f32_16x16x32_bf16 %5, %7, %16, %5\n\tds_read_b128 %6, %17 offset:7168"
      : "+v"(ac[0][0]), "=&v"(pu[1]), "+v"(ac[0][1]), "+v"(ac[0][2]), "+v"(ze[2]), "+v"(ac[0][3]), "=&a"(fwo[21]) : "v"(pu[2]), "a"(fw2[2][0]), "v"(vF), "s"(rsW), "s"(so0), "a"(fw2[2][1]), "v"(rwd), "v"(ac[1][1]), "a"(fw2[2][2]), "a"(fw2[2][3]), "v"(ro) : "memory"); SB();
  WAITVB(15);
  SB();
  asm volatile("v_mfma_f32_16x16x32_bf16 %0, %7, %8, %0\n\tbuffer_load_dwordx4 %1, %9, %10, %11 offen offset:256 sc1\n\tv_mfma_f32_16x16x32_bf16 %2, %7, %12, %2\n\tds_write_b128 %13, %14 offset:20480\n\tv_mfma_f32_16x16x32_bf16 %3, %7, %15, %3\n\tv_rcp_f32 %4, %4\n\tv_mfma_f32_16x16x32_bf16 %5, %7, %16, %5\n\tv_mul_f32 %6, %17, %18"
      : "+v"(ac[0][0]), "=&v"(pu[2]), "+v"(ac[0][1]), "+v"(ac[0][2]), "+v"(ze[3]), "+v"(ac[0][3]), "=&v"(ge[0]) : "v"(pu[3]), "a"(fw2[3][0]), "v"(vF), "s"(rsW), "s"(so0), "a"(fw2[3][1]), "v"(rwd), "v"(ac[1][2]), "a"(fw2[3][2]), "a"(fw2[3][3]), "v"(ye[0]), "v"(ze[0]) : "memory"); SB();
  WAITVB(15);
  SB();
  asm volatile("v_mfma_f32_16x16x32_bf16 %0, %8, %9, %0\n\tbuffer_load_dwordx4 %1, %10, %11, %12 offen offset:384 sc1\n\tv_mfma_f32_16x16x32_bf16 %2, %8, %13, %2\n\tds_write_b128 %14, %15 offset:21504\n\tv_mfma_f32_16x16x32_bf16 %3, %8, %16, %3\n\tv_mul_f32 %4, %17, %18\n\tv_mul_f32 %5, %19, %20\n\tv_mfma_f32_16x16x32_bf16 %6, %8, %21, %6\n\tv_mul_f32 %7, %22, %23"
      : "+v"(ac[0][0]), "=&v"(pu[3]), "+v"(ac[0][1]), "+v"(ac[0][2]), "=&v"(ge[1]), "=&v"(ge[2]), "+v"(ac[0][3]), "=&v"(ge[3]) : "v"(pu[4]), "a"(fw2[4][0]), "v"(vF), "s"(rsW), "s"(so0), "a"(fw2[4][1]), "v"(rwd), "v"(ac[1][3]), "a"(fw2[4][2]), "v"(ye[1]), "v"(ze[1]), "v"(ye[2]), "v"(ze[2]), "a"(fw2[4][3]), "v"(ye[3]), "v"(ze[3]) : "memory"); SB();
  WAITVB(15);
  SB();
  asm volatile("v_mfma_f32_16x16x32_bf16 %0, %3, %4, %0\n\tbuffer_load_dwordx4 %1, %5, %6, %7 offen offset:512 sc1\n\tv_mfma_f32_16x16x32_bf16 %2, %3, %8, %2"
      : "+v"(ac[0][0]), "=&v"(pu[4]), "+v"(ac[0][1]) : "v"(pu[5]), "a"(fw2[5][0]), "v"(vF), "s"(rsW), "s"(so0), "a"(fw2[5][1]) : "memory"); SB();
  WAITL(0);
  BAR0();
  SB();
  asm volatile("v_mfma_f32_16x16x32_bf16 %0, %5, %6, %0\n\tds_read_b128 %1, %7 offset:18432\n\tds_read_b128 %2, %7 offset:22528\n\tv_mfma_f32_16x16x32_bf16 %3, %5, %8, %3\n\tds_read_b128 %4, %7 offset:26624"
      : "+v"(ac[0][2]), "=&v"(py[1][0]), "=&v"(py[1][1]), "+v"(ac[0][3]), "=&v"(py[1][2]) : "v"(pu[5]), "a"(fw2[5][2]), "v"(rr), "a"(fw2[5][3]) : "memory"); SB();
  WAITVB(12);
  SB();
  asm volatile("v_mfma_f32_16x16x32_bf16 %0, %8, %9, %0\n\tbuffer_load_dwordx4 %1, %10, %11, %12 offen offset:640 sc1\n\tv_mfma_f32_16x16x32_bf16 %2, %8, %13, %2\n\tds_read_b128 %3, %14 offset:30720\n\tds_read_b128 %4, %15 offset:15360\n\tv_mfma_f32_16x16x32_bf16 %5, %8, %16, %5\n\tv_mfma_f32_16x16x32_bf16 %6, %8, %17, %6\n\tds_read_b128 %7, %15 offset:23552"
      : "+v"(ac[0][0]), "=&v"(pu[5]), "+v"(ac[0][1]), "=&v"(py[1][3]), "=&a"(fwo[22]), "+v"(ac[0][2]), "+v"(ac[0][3]), "=&a"(fwo[23]) : "v"(pu[6]), "a"(fw2[6][0]), "v"(vF), "s"(rsW), "s"(so0), "a"(fw2[6][1]), "v"(rr), "v"(ro), "a"(fw2[6][2]), "a"(fw2[6][3]) : "memory"); SB();
  WAITVB(12);
  SB();
  asm volatile("v_mfma_f32_16x16x32_bf16 %0, %5, %6, %0\n\tbuffer_load_dwordx4 %1, %7, %8, %9 offen offset:768 sc1\n\tv_mfma_f32_16x16x32_bf16 %2, %5, %10, %2\n\tds_read_b128 %3, %11 offset:32768\n\tv_mfma_f32_16x16x32_bf16 %4, %5, %12, %4"
      : "+v"(ac[0][0]), "=&v"(pu[6]), "+v"(ac[0][1]), "=&a"(fwo[24]), "+v"(ac[0][2]) : "v"(pu[7]), "a"(fw2[7][0]), "v"(vF), "s"(rsW), "s"(so0), "a"(fw2[7][1]), "v"(ro), "a"(fw2[7][2]) : "memory"); SB();
  WAITL(3);
  asm volatile("v_add_f32 %0, %3, %4\n\tv_mfma_f32_16x16x32_bf16 %1, %5, %6, %1\n\tv_add_f32 %2, %7, %4"
      : "=&v"(ye[0]), "+v"(ac[0][3]), "=&v"(ye[1]) : "v"(py[1][0][0]), "v"(bias), "v"(pu[7]), "a"(fw2[7][3]), "v"(py[1][0][1])); SB();
  WAITVB(12);
  SB();
  asm volatile("v_mfma_f32_16x16x32_bf16 %0, %9, %10, %0\n\tbuffer_load_dwordx4 %1, %11, %12, %13 offen offset:0 sc1\n\tv_mfma_f32_16x16x32_bf16 %2, %9, %14, %2\n\tv_add_f32 %3, %15, %16\n\tv_add_f32 %4, %17, %16\n\tv_mfma_f32_16x16x32_bf16 %5, %9, %18, %5\n\tv_add_f32 %6, %19, %20\n\tv_add_f32 %7, %21, %22\n\tv_mfma_f32_16x16x32_bf16 %8, %9, %23, %8"
      : "+v"(ac[0][0]), "=&v"(pu[7]), "+v"(ac[0][1]), "=&v"(ye[2]), "=&v"(ye[3]), "+v"(ac[0][2]), "=&v"(ze[0]), "=&v"(ze[1]), "+v"(ac[0][3]) : "v"(pu[8]), "a"(fw2[8][0]), "v"(vF), "s"(rsW), "s"(so1), "a"(fw2[8][1]), "v"(py[1][0][2]), "v"(bias), "v"(py[1][0][3]), "a"(fw2[8][2]), "v"(py[1][1][0]), "v"(py[1][2][0]), "v"(py[1][1][1]), "v"(py[1][2][1]), "a"(fw2[8][3]) : "memory"); SB();
  asm volatile("v_add_f32 %0, %1, %2"
      : "=&v"(ze[2]) : "v"(py[1][1][2]), "v"(py[1][2][2])); SB();
  WAITVB(12);
  SB();
  asm volatile("v_mfma_f32_16x16x32_bf16 %0, %9, %10, %0\n\tbuffer_load_dwordx4 %1, %11, %12, %13 offen offset:128 sc1\n\tv_mfma_f32_16x16x32_bf16 %2, %9, %14, %2\n\tv_add_f32 %3, %15, %16\n\tv_add_f32 %4, %4, %17\n\tv_mfma_f32_16x16x32_bf16 %5, %9, %18, %5\n\tv_add_f32 %6, %6, %19\n\tv_add_f32 %7, %7, %20\n\tv_mfma_f32_16x16x32_bf16 %8, %9, %21, %8"
      : "+v"(ac[0][0]), "=&v"(pu[8]), "+v"(ac[0][1]), "=&v"(ze[3]), "+v"(ye[0]), "+v"(ac[0][2]), "+v"(ye[1]), "+v"(ye[2]), "+v"(ac[0][3]) : "v"(pu[9]), "v"(fw2[9][0]), "v"(vF), "s"(rsW), "s"(so1), "v"(fw2[9][1]), "v"(py[1][1][3]), "v"(py[1][2][3]), "v"(py[1][3][0]), "v"(fw2[9][2]), "v"(py[1][3][1]), "v"(py[1][3][2]), "v"(fw2[9][3]) : "memory"); SB();
  asm volatile("v_add_f32 %0, %0, %1"
      : "+v"(ye[3]) : "v"(py[1][3][3])); SB();
  WAITVB(12);
  SB();
  asm volatile("v_mfma_f32_16x16x32_bf16 %0, %10, %11, %0\n\tbuffer_load_dwordx4 %1, %12, %13, %14 offen offset:256 sc1\n\tv_mfma_f32_16x16x32_bf16 %2, %10, %15, %2\n\tv_add_f32 %3, %3, %4\n\tv_add_f32 %5, %5, %16\n\tv_mfma_f32_16x16x32_bf16 %6, %10, %17, %6\n\tv_add_f32 %7, %7, %18\n\tv_add_f32 %8, %8, %19\n\tv_mfma_f32_16x16x32_bf16 %9, %10, %20, %9\n\tv_mul_f32 %4, 0xbfb8aa3b, %3"
      : "+v"(ac[0][0]), "=&v"(pu[9]), "+v"(ac[0][1]), "+v"(ye[0]), "+v"(ze[0]), "+v"(ye[1]), "+v"(ac[0][2]), "+v"(ye[2]), "+v"(ye[3]), "+v"(ac[0][3]) : "v"(pu[10]), "v"(fw2[10][0]), "v"(vF), "s"(rsW), "s"(so1), "v"(fw2[10][1]), "v"(ze[1]), "v"(fw2[10][2]), "v"(ze[2]), "v"(ze[3]), "v"(fw2[10][3]) : "memory"); SB();
  WAITVB(12);
  SB();
  asm volatile("v_mfma_f32_16x16x32_bf16 %0, %9, %10, %0\n\tbuffer_load_dwordx4 %1, %11, %12, %13 offen offset:384 sc1\n\tv_mfma_f32_16x16x32_bf16 %2, %9, %14, %2\n\tv_mul_f32 %3, 0xbfb8aa3b, %15\n\tv_mul_f32 %4, 0xbfb8aa3b, %16\n\tv_mfma_f32_16x16x32_bf16 %5, %9, %17, %5\n\tds_read_b128 %6, %18 offset:38912\n\tv_mul_f32 %7, 0xbfb8aa3b, %19\n\tv_mfma_f32_16x16x32_bf16 %8, %9, %20, %8"
      : "+v"(ac[0][0]), "=&v"(pu[10]), "+v"(ac[0][1]), "=&v"(ze[1]), "=&v"(ze[2]), "+v"(ac[0][2]), "=&a"(fwo[25]), "=&v"(ze[3]), "+v"(ac[0][3]) : "v"(pu[11]), "v"(fw2[11][0]), "v"(vF), "s"(rsW), "s"(so1), "v"(fw2[11][1]), "v"(ye[1]), "v"(ye[2]), "v"(fw2[11][2]), "v"(ro), "v"(ye[3]), "v"(fw2[11][3]) : "memory"); SB();
  WAITVB(12);
  SB();
  asm volatile("v_mfma_f32_16x16x32_bf16 %0, %3, %4, %0\n\tbuffer_load_dwordx4 %1, %5, %6, %7 offen offset:512 sc1\n\tv_mfma_f32_16x16x32_bf16 %2, %3, %8, %2"
      : "+v"(ac[0][0]), "=&v"(pu[11]), "+v"(ac[0][1]) : "v"(pu[12]), "v"(fw2[12][0]), "v"(vF), "s"(rsW), "s"(so1), "v"(fw2[12][1]) : "memory"); SB();
  WAITV3(20, fz[0], fz[1], fz[2]);
  if (__builtin_expect(__ballot(((fz[0] ^ nonce) | (fz[1] ^ nonce) | (fz[2] ^ nonce)) != 0u) != 0ull, 0)) { poll0<16>(rsW, vZ0, nonce); poll0<16>(rsW, vZ1, nonce); poll0<16>(rsW, vZ2, nonce); }
  SB();
  asm volatile("v_mfma_f32_16x16x32_bf16 %0, %4, %5, %0\n\tv_exp_f32 %1, %1\n\tv_mfma_f32_16x16x32_bf16 %2, %4, %6, %2\n\tds_read_b128 %3, %7 offset:45056"
      : "+v"(ac[0][2]), "+v"(ze[0]), "+v"(ac[0][3]), "=&a"(fwo[26]) : "v"(pu[12]), "v"(fw2[12][2]), "v"(fw2[12][3]), "v"(ro) : "memory"); SB();
  WAITVB(12);
  SB();
  asm volatile("v_mfma_f32_16x16x32_bf16 %0, %8, %9, %0\n\tbuffer_load_dwordx4 %1, %10, %11, %12 offen offset:640 sc1\n\tv_mfma_f32_16x16x32_bf16 %2, %8, %13, %2\n\tv_exp_f32 %3, %3\n\tv_mfma_f32_16x16x32_bf16 %4, %8, %14, %4\n\tv_exp_f32 %5, %5\n\tv_mfma_f32_16x16x32_bf16 %6, %8, %15, %6\n\tv_exp_f32 %7, %7"
      : "+v"(ac[0][0]), "=&v"(pu[12]), "+v"(ac[0][1]), "+v"(ze[1]), "+v"(ac[0][2]), "+v"(ze[2]), "+v"(ac[0][3]), "+v"(ze[3]) : "v"(pu[13]), "v"(fw2[13][0]), "v"(vF), "s"(rsW), "s"(so1), "v"(fw2[13][1]), "v"(fw2[13][2]), "v"(fw2[13][3]) : "memory"); SB();
  STAMP(42);
  WAITVB(12);
  SB();
  asm volatile("v_mfma_f32_16x16x32_bf16 %0, %7, %8, 0\n\tbuffer_load_dwordx4 %1, %9, %10, %11 offen offset:768 sc1\n\tv_mfma_f32_16x16x32_bf16 %2, %12, %8, 0\n\tv_add_f32 %3, 1.0, %3\n\tv_add_f32 %4, 1.0, %4\n\tv_mfma_f32_16x16x32_bf16 %5, %13, %8, 0\n\tv_add_f32 %6, 1.0, %6"
      : "=&v"(as[0]), "=&v"(pu[13]), "=&v"(as[1]), "+v"(ze[0]), "+v"(ze[1]), "=&v"(as[2]), "+v"(ze[2]) : "a"(fwo[0]), "v"(pu[0]), "v"(vF), "s"(rsW), "s"(so1), "a"(fwo[1]), "a"(fwo[2]) : "memory"); SB();
  WAITVB(12);
  SB();
  asm volatile("v_mfma_f32_16x16x32_bf16 %0, %5, %6, %0\n\tds_write_b128 %7, %8 offset:0\n\tv_mfma_f32_16x16x32_bf16 %1, %9, %6, %1\n\tds_read_b128 %2, %10 offset:33792\n\tv_add_f32 %3, 1.0, %3\n\tv_mfma_f32_16x16x32_bf16 %4, %11, %6, %4"
      : "+v"(as[0]), "+v"(as[1]), "=&a"(fwo[27]), "+v"(ze[3]), "+v"(as[2]) : "a"(fwo[3]), "v"(pu[1]), "v"(rwd), "v"(ac[0][0]), "a"(fwo[4]), "v"(ro), "a"(fwo[5]) : "memory"); SB();
  WAITVB(11);
  SB();
  asm volatile("v_mfma_f32_16x16x32_bf16 %0, %5, %6, %0\n\tds_write_b128 %7, %8 offset:1024\n\tv_mfma_f32_16x16x32_bf16 %1, %9, %6, %1\n\tv_rcp_f32 %2, %2\n\tv_mfma_f32_16x16x32_bf16 %3, %10, %6, %3\n\tds_read_b128 %4, %11 offset:39936"
      : "+v"(as[0]), "+v"(as[1]), "+v"(ze[0]), "+v"(as[2]), "=&a"(fwo[28]) : "a"(fwo[6]), "v"(pu[2]), "v"(rwd), "v"(ac[0][1]), "a"(fwo[7]), "a"(fwo[8]), "v"(ro) : "memory"); SB();
  WAITVB(10);
  SB();
  asm volatile("v_mfma_f32_16x16x32_bf16 %0, %5, %6, %0\n\tds_write_b128 %7, %8 offset:2048\n\tv_mfma_f32_16x16x32_bf16 %1, %9, %6, %1\n\tv_rcp_f32 %2, %2\n\tv_mfma_f32_16x16x32_bf16 %3, %10, %6, %3\n\tds_read_b128 %4, %11 offset:46080"
      : "+v"(as[0]), "+v"(as[1]), "+v"(ze[1]), "+v"(as[2]), "=&a"(fwo[29]) : "a"(fwo[9]), "v"(pu[3]), "v"(rwd), "v"(ac[0][2]), "a"(fwo[10]), "a"(fwo[11]), "v"(ro) : "memory"); SB();
  WAITVB(9);
  SB();
  asm volatile("v_mfma_f32_16x16x32_bf16 %0, %5, %6, %0\n\tds_write_b128 %7, %8 offset:3072\n\tv_mfma_f32_16x16x32_bf16 %1, %9, %6, %1\n\tv_rcp_f32 %2, %2\n\tv_mfma_f32_16x16x32_bf16 %3, %10, %6, %3\n\tds_read_b128 %4, %11 offset:34816"
      : "+v"(as[0]), "+v"(as[1]), "+v"(ze[2]), "+v"(as[2]), "=&a"(fwo[30]) : "a"(fwo[12]), "v"(pu[4]), "v"(rwd), "v"(ac[0][3]), "a"(fwo[13]), "a"(fwo[14]), "v"(ro) : "memory"); SB();
  WAITVB(8);
  SB();
  asm volatile("v_mfma_f32_16x16x32_bf16 %0, %3, %4, %0\n\tv_rcp_f32 %1, %1\n\tv_mfma_f32_16x16x32_bf16 %2, %5, %4, %2"
      : "+v"(as[0]), "+v"(ze[3]), "+v"(as[1]) : "a"(fwo[15]), "v"(pu[5]), "a"(fwo[16])); SB();
  WAITL(1);
  BAR0();
  SB();
  asm volatile("v_mfma_f32_16x16x32_bf16 %0, %1, %2, %0"
      : "+v"(as[2]) : "a"(fwo[17]), "v"(pu[5])); SB();
  WAITVB(7);
  asm volatile("ds_read_b128 %0, %9 offset:40960\n\tv_mfma_f32_16x16x32_bf16 %1, %10, %11, %1\n\tds_read_b128 %2, %12 offset:0\n\tds_read_b128 %3, %12 offset:4096\n\tv_mfma_f32_16x16x32_bf16 %4, %13, %11, %4\n\tds_read_b128 %5, %12 offset:8192\n\tds_read_b128 %6, %12 offset:12288\n\tv_mfma_f32_16x16x32_bf16 %7, %14, %11, %7\n\tv_mul_f32 %8, %15, %16"
      : "=&a"(fwo[31]), "+v"(as[0]), "=&v"(py[2][0]), "=&v"(py[2][1]), "+v"(as[1]), "=&v"(py[2][2]), "=&v"(py[2][3]), "+v"(as[2]), "=&v"(ge[4]) : "v"(ro), "a"(fwo[18]), "v"(pu[6]), "v"(rr), "a"(fwo[19]), "a"(fwo[20]), "v"(ye[0]), "v"(ze[0]) : "memory"); SB();
  WAITVB(6);
  SB();
  asm volatile("v_mfma_f32_16x16x32_bf16 %0, %7, %8, %0\n\tv_mul_f32 %1, %9, %10\n\tv_mul_f32 %2, %11, %12\n\tv_mfma_f32_16x16x32_bf16 %3, %13, %8, %3\n\tds_read_b128 %4, %14 offset:47104\n\tv_mul_f32 %5, %15, %16\n\tv_mfma_f32_16x16x32_bf16 %6, %17, %8, %6"
      : "+v"(as[0]), "=&v"(ge[5]), "=&v"(ge[6]), "+v"(as[1]), "=&a"(fwo[32]), "=&v"(ge[7]), "+v"(as[2]) : "a"(fwo[21]), "v"(pu[7]), "v"(ye[1]), "v"(ze[1]), "v"(ye[2]), "v"(ze[2]), "a"(fwo[22]), "v"(ro), "v"(ye[3]), "v"(ze[3]), "a"(fwo[23]) : "memory"); SB();
  WAITVB(5);
  asm volatile("ds_read_b128 %0, %6 offset:35840\n\tds_read_b128 %1, %6 offset:41984\n\tds_read_b128 %2, %6 offset:48128\n\tds_read_b128 %3, %6 offset:36864\n\tds_read_b128 %4, %6 offset:43008\n\tv_mfma_f32_16x16x32_bf16 %5, %7, %8, %5"
      : "=&a"(fwo[33]), "=&a"(fwo[34]), "=&a"(fwo[35]), "=&a"(fwo[36]), "=&a"(fwo[37]), "+v"(as[0]) : "v"(ro), "a"(fwo[24]), "v"(pu[8]) : "memory"); SB();
  WAITL(6);
  asm volatile("v_add_f32 %0, %6, %7\n\tv_mfma_f32_16x16x32_bf16 %1, %8, %9, %1\n\tv_add_f32 %2, %10, %7\n\tv_add_f32 %3, %11, %7\n\tv_mfma_f32_16x16x32_bf16 %4, %12, %9, %4\n\tv_add_f32 %5, %13, %7"
      : "=&v"(ye[0]), "+v"(as[1]), "=&v"(ye[1]), "=&v"(ye[2]), "+v"(as[2]), "=&v"(ye[3]) : "v"(py[2][0][0]), "v"(bias), "a"(fwo[25]), "v"(pu[8]), "v"(py[2][0][1]), "v"(py[2][0][2]), "a"(fwo[26]), "v"(py[2][0][3])); SB();
  WAITVB(4);
  SB();
  asm volatile("v_mfma_f32_16x16x32_bf16 %0, %7, %8, %0\n\tv_add_f32 %1, %9, %10\n\tv_add_f32 %2, %11, %12\n\tv_mfma_f32_16x16x32_bf16 %3, %13, %8, %3\n\tv_add_f32 %4, %14, %15\n\tv_add_f32 %5, %16, %17\n\tv_mfma_f32_16x16x32_bf16 %6, %18, %8, %6"
      : "+v"(as[0]), "=&v"(ze[0]), "=&v"(ze[1]), "+v"(as[1]), "=&v"(ze[2]), "=&v"(ze[3]), "+v"(as[2]) : "a"(fwo[27]), "v"(pu[9]), "v"(py[2][1][0]), "v"(py[2][2][0]), "v"(py[2][1][1]), "v"(py[2][2][1]), "a"(fwo[28]), "v"(py[2][1][2]), "v"(py[2][2][2]), "v"(py[2][1][3]), "v"(py[2][2][3]), "a"(fwo[29])); SB();
  WAITVB(3);
  asm volatile("ds_read_b128 %0, %4 offset:49152\n\tds_read_b128 %1, %4 offset:37888\n\tds_read_b128 %2, %4 offset:44032\n\tds_read_b128 %3, %4 offset:50176"
      : "=&a"(fwo[38]), "=&a"(fwo[39]), "=&a"(fwo[40]), "=&a"(fwo[41]) : "v"(ro) : "memory"); SB();
  WAITL(6);
  SB();
  asm volatile("v_mfma_f32_16x16x32_bf16 %0, %7, %8, %0\n\tv_add_f32 %1, %1, %9\n\tv_add_f32 %2, %2, %10\n\tv_mfma_f32_16x16x32_bf16 %3, %11, %8, %3\n\tv_add_f32 %4, %4, %12\n\tv_add_f32 %5, %5, %13\n\tv_mfma_f32_16x16x32_bf16 %6, %14, %8, %6"
      : "+v"(as[0]), "+v"(ye[0]), "+v"(ye[1]), "+v"(as[1]), "+v"(ye[2]), "+v"(ye[3]), "+v"(as[2]) : "a"(fwo[30]), "v"(pu[10]), "v"(py[2][3][0]), "v"(py[2][3][1]), "a"(fwo[31]), "v"(py[2][3][2]), "v"(py[2][3][3]), "a"(fwo[32])); SB();
  WAITL(0);
  WAITVB(0);
  SB();
  asm volatile("v_mfma_f32_16x16x32_bf16 %0, %10, %11, %0\n\tv_add_f32 %1, %1, %2\n\tv_add_f32 %3, %3, %4\n\tv_mfma_f32_16x16x32_bf16 %0, %12, %13, %0\n\tv_add_f32 %5, %5, %6\n\tv_add_f32 %7, %7, %8\n\tv_mfma_f32_16x16x32_bf16 %0, %14, %15, %0\n\tv_mul_f32 %2, 0xbfb8aa3b, %1\n\tv_mul_f32 %4, 0xbfb8aa3b, %3\n\tv_mfma_f32_16x16x32_bf16 %9, %16, %11, %9\n\tv_mul_f32 %6, 0xbfb8aa3b, %5\n\tv_mul_f32 %8, 0xbfb8aa3b, %7\n\tv_mfma_f32_16x16x32_bf16 %9, %17, %13, %9\n\tv_exp_f32 %2, %2\n\tv_mfma_f32_16x16x32_bf16 %9, %18, %15, %9\n\tv_exp_f32 %4, %4"
      : "+v"(as[0]), "+v"(ye[0]), "+v"(ze[0]), "+v"(ye[1]), "+v"(ze[1]), "+v"(ye[2]), "+v"(ze[2]), "+v"(ye[3]), "+v"(ze[3]), "+v"(as[1]) : "a"(fwo[33]), "v"(pu[11]), "a"(fwo[36]), "v"(pu[12]), "a"(fwo[39]), "v"(pu[13]), "a"(fwo[34]), "a"(fwo[37]), "a"(fwo[40])); SB();
  asm volatile("v_mfma_f32_16x16x32_bf16 %0, %5, %6, %0\n\tv_exp_f32 %1, %1\n\tv_mfma_f32_16x16x32_bf16 %0, %7, %8, %0\n\tv_exp_f32 %2, %2\n\tv_mfma_f32_16x16x32_bf16 %0, %9, %10, %0\n\tv_add_f32 %3, 1.0, %3\n\tv_add_f32 %4, 1.0, %4"
      : "+v"(as[2]), "+v"(ze[2]), "+v"(ze[3]), "+v"(ze[0]), "+v"(ze[1]) : "a"(fwo[35]), "v"(pu[11]), "a"(fwo[38]), "v"(pu[12]), "a"(fwo[41]), "v"(pu[13])); SB();
  STAMP(44);
  asm volatile("v_add_f32 %0, 1.0, %0\n\tv_add_f32 %1, 1.0, %1\n\tv_rcp_f32 %2, %2\n\tv_rcp_f32 %3, %3\n\tv_rcp_f32 %0, %0\n\tv_rcp_f32 %1, %1\n\tv_mul_f32 %4, %11, %2\n\tv_mul_f32 %5, %12, %3\n\tv_mul_f32 %6, %13, %0\n\tv_mul_f32 %7, %14, %1\n\tv_fma_f32 %8, %15, %16, %8\n\tv_fma_f32 %9, %17, %18, %9\n\tv_fma_f32 %10, %19, %20, %10"
      : "+v"(ze[2]), "+v"(ze[3]), "+v"(ze[0]), "+v"(ze[1]), "=&v"(ge[8]), "=&v"(ge[9]), "=&v"(ge[10]), "=&v"(ge[11]), "+v"(xe[0]), "+v"(xe[1]), "+v"(xe[2]) : "v"(ye[0]), "v"(ye[1]), "v"(ye[2]), "v"(ye[3]), "v"(as[0][0]), "v"(ge[0]), "v"(as[0][1]), "v"(ge[1]), "v"(as[0][2]), "v"(ge[2])); SB();
  asm volatile("v_fma_f32 %0, %9, %10, %0\n\tv_cvt_pk_bf16_f32 %1, %11, %12\n\tv_cvt_pk_bf16_f32 %2, %13, %0\n\tv_fma_f32 %3, %14, %15, %3\n\tv_fma_f32 %4, %16, %17, %4\n\tv_fma_f32 %5, %18, %19, %5\n\tv_fma_f32 %6, %20, %21, %6\n\tv_cvt_pk_bf16_f32 %7, %3, %4\n\tv_cvt_pk_bf16_f32 %8, %5, %6"
      : "+v"(xe[3]), "=&v"(ov[0][0]), "=&v"(ov[0][1]), "+v"(xe[4]), "+v"(xe[5]), "+v"(xe[6]), "+v"(xe[7]), "=&v"(ov[1][0]), "=&v"(ov[1][1]) : "v"(as[0][3]), "v"(ge[3]), "v"(xe[0]), "v"(xe[1]), "v"(xe[2]), "v"(as[1][0]), "v"(ge[4]), "v"(as[1][1]), "v"(ge[5]), "v"(as[1][2]), "v"(ge[6]), "v"(as[1][3]), "v"(ge[7])); SB();
  asm volatile("ds_write_b64 %6, %7 offset:0\n\tv_fma_f32 %0, %8, %9, %0\n\tv_fma_f32 %1, %10, %11, %1\n\tv_fma_f32 %2, %12, %13, %2\n\tv_fma_f32 %3, %14, %15, %3\n\tv_cvt_pk_bf16_f32 %4, %0, %1\n\tv_cvt_pk_bf16_f32 %5, %2, %3\n\tds_write_b64 %16, %17 offset:0"
      : "+v"(xe[8]), "+v"(xe[9]), "+v"(xe[10]), "+v"(xe[11]), "=&v"(ov[2][0]), "=&v"(ov[2][1]) : "v"(xta[0]), "v"(ov[0]), "v"(as[2][0]), "v"(ge[8]), "v"(as[2][1]), "v"(ge[9]), "v"(as[2][2]), "v"(ge[10]), "v"(as[2][3]), "v"(ge[11]), "v"(xta[1]), "v"(ov[1]) : "memory"); SB();
  asm volatile("ds_write_b64 %0, %1 offset:0"
      :  : "v"(xta[2]), "v"(ov[2]) : "memory"); SB();
  { u32x4 t0_, t1_; DSR16(t0_, xo, 0); DSR16(t1_, xo, 1024); WAITLB(1);
    BS16(vXa, rsO_, 0, t0_, ""); WAITLB(0); BS16(vXb, rsO_, 1024, t1_, ""); }
  STAMP(23);
  if (b == 8 * 28 && w == 0) { const unsigned vZ = 0u; ST4(vZ, ws, nonce); }

  STAMP(12);
  STAMP_DUMP();
  }
  }
  asm volatile(".Lcpf_e:");
}

extern "C" void kernel_launch(void* const* d_in, const int* in_sizes, int n_in,
                              void* d_out, int out_size, void* d_ws, size_t ws_size,
                              hipStream_t stream) {
  fused_kernel<<<256, 256, 0, stream>>>((const char*)d_in[0], (const char*)d_in[1], (const char*)d_in[2], (const char*)d_in[4],
                                        (const char*)d_in[5], (const char*)d_in[6], (const char*)d_in[7], (char*)d_ws, (char*)d_out);
}
```
